```python
import torch

def _softmax(x, axis=-1):
    return torch.softmax(x, dim=axis)
def _gelu(x):
    return torch.nn.functional.gelu(x, approximate="tanh")
def _silu(x):
    return torch.nn.functional.silu(x)
def _sigmoid(x):
    return torch.sigmoid(x)
def _layernorm(x, axis=-1, eps=1e-5):
    m = x.mean(dim=axis, keepdim=True)
    v = x.var(dim=axis, keepdim=True, unbiased=False)
    return (x - m) / torch.sqrt(v + eps)
def _scatter_add(tgt, idx, src, axis=0):
    return tgt.clone().index_add_(axis, idx, src)
def _segment_sum(src, seg, n):
    out = torch.zeros((n,) + src.shape[1:], dtype=src.dtype)
    return out.index_add_(0, seg, src)
def _pad(x, pw):
    flat = tuple(v for pair in reversed(pw) for v in pair)
    return torch.nn.functional.pad(x, flat)

kernel_name = 'interleave_layers__sliding_local_dilated__ssm_conv_s4d__prompt'

def setup_inputs():

    _w = (lambda a: (lambda t: t.to(torch.bfloat16) if t.is_floating_point() else t)(torch.as_tensor(a)))
    def _A_setup():
        B, S, D = 1, 2048, 640
        return {
            "x": _w((lambda *s: torch.randn(*s, dtype=torch.bfloat16) * 0.05)(B, S, D)),
            "wqkv": _w((lambda *s: torch.randn(*s, dtype=torch.bfloat16) * 0.05)(D, 3 * D)),
            "wo": _w((lambda *s: torch.randn(*s, dtype=torch.bfloat16) * 0.05)(D, D)),
        }
    _A_in = _A_setup()

    def _B_setup():
        Bsz, L, D, N = 1, 2048, 640, 192
        return {
            "u": _w((lambda *s: torch.randn(*s, dtype=torch.bfloat16) * 0.05)(Bsz, L, D)),
            "log_A_real": _w((lambda *s: torch.randn(*s, dtype=torch.bfloat16) * 3.2)(D, N) * 0.5),
            "A_imag": _w((lambda *s: torch.randn(*s, dtype=torch.bfloat16) * 0.1)(D, N) * 3.14),
            "C_real": _w((lambda *s: torch.randn(*s, dtype=torch.bfloat16) * 0.4)(D, N) / N ** 0.5),
            "C_imag": _w((lambda *s: torch.randn(*s, dtype=torch.bfloat16) * 0.4)(D, N) / N ** 0.5),
            "log_dt": _w((lambda *s: torch.randn(*s, dtype=torch.bfloat16) * 0.1)(D) * 0.5 - 4.0),
            "D_skip": _w((lambda *s: torch.randn(*s, dtype=torch.bfloat16) * 0.1)(D)),
        }
    _B_in = _B_setup()

    return {
        "A_x": _A_in["x"],
        "A_wqkv": _A_in["wqkv"],
        "A_wo": _A_in["wo"],
        "B_u": _B_in["u"],
        "B_log_A_real": _B_in["log_A_real"],
        "B_A_imag": _B_in["A_imag"],
        "B_C_real": _B_in["C_real"],
        "B_C_imag": _B_in["C_imag"],
        "B_log_dt": _B_in["log_dt"],
        "B_D_skip": _B_in["D_skip"],
    }

def reference(A_x, A_wqkv, A_wo, B_u, B_log_A_real, B_A_imag, B_C_real, B_C_imag, B_log_dt, B_D_skip):
    def _A_ref(x, wqkv, wo):
        B, S, D = x.shape
        H = 32; Dh = D // H
        W, dil = 8, 2
        qkv = (lambda x, *d: x.permute(*d))((x @ wqkv).reshape(B, S, 3, H, Dh), 2, 0, 3, 1, 4)
        q, k, v = qkv[0], qkv[1], qkv[2]
        scale = Dh ** -0.5
        pos = torch.arange(S)
        off = torch.arange(W) * dil
        idx = pos[:, None] - off[None, :]
        mask = idx >= 0
        idx_c = torch.where(mask, idx, 0).reshape(-1)
        kg = k[:, :, idx_c, :].reshape(B, H, S, W, Dh)
        vg = v[:, :, idx_c, :].reshape(B, H, S, W, Dh)
        logits = torch.einsum("bhsd,bhswd->bhsw", q, kg) * scale
        logits = torch.where(mask[None, None, :, :], logits, -1e9)
        attn = _softmax(logits, axis=-1)
        o = torch.einsum("bhsw,bhswd->bhsd", attn, vg)
        return (lambda x, *d: x.permute(*d))(o, 0, 2, 1, 3).reshape(B, S, D) @ wo

    def _B_ref(u, log_A_real, A_imag, C_real, C_imag, log_dt, D_skip):
        L = u.shape[1]
        dt = torch.exp(log_dt)
        A = -torch.exp(log_A_real) + 1j * A_imag
        C = (C_real + 1j * C_imag) * dt[:, None]
        pos = (lambda x, dt: x.to(dt))(torch.arange(L), log_dt.dtype)
        P = torch.exp((A * dt[:, None])[:, :, None] * pos[None, None, :])
        K = 2.0 * torch.einsum("dn,dnl->dl", C, P).real
        L2 = 2 * L
        u_t = (lambda x, *d: x.permute(*d))(u, 0, 2, 1)
        Kf = (lambda x, *a, **k: torch.fft.rfft(x.float(), *a, **k))(K, L2)
        Uf = (lambda x, *a, **k: torch.fft.rfft(x.float(), *a, **k))(u_t, L2)
        y = (lambda x, *a, **k: torch.fft.irfft(x, *a, **k).to(torch.bfloat16))(Uf * Kf[None, :, :], L2)[:, :, :L]
        y = (lambda x, *d: x.permute(*d))(y, 0, 2, 1)
        return y + u * D_skip[None, None, :]

    x = A_x
    x = x + _A_ref(_layernorm(x, axis=-1), A_wqkv, A_wo)
    x = x + _B_ref(_layernorm(x, axis=-1), B_log_A_real, B_A_imag, B_C_real, B_C_imag, B_log_dt, B_D_skip)
    x = x + _A_ref(_layernorm(x, axis=-1), A_wqkv, A_wo)
    x = x + _B_ref(_layernorm(x, axis=-1), B_log_A_real, B_A_imag, B_C_real, B_C_imag, B_log_dt, B_D_skip)
    return x
```

```cpp
#include <hip/hip_runtime.h>
typedef unsigned short u16;

__global__ __launch_bounds__(512, 2) void mega_kernel(
    const u16* __restrict__ x_, const u16* __restrict__ wqkv_, const u16* __restrict__ wo_,
    const u16* __restrict__ log_A_real_, const u16* __restrict__ A_imag_,
    const u16* __restrict__ C_real_, const u16* __restrict__ C_imag_, const u16* __restrict__ log_dt_,
    const u16* __restrict__ D_skip, u16* __restrict__ out, unsigned char* __restrict__ ws_) {
  __shared__ __attribute__((aligned(16))) unsigned char smem[163840];
  const int tid = (int)__builtin_amdgcn_workitem_id_x();
  asm volatile(
"s_cmp_lt_u32 s3, 4\n"
"s_cbranch_scc1 .Lxs_1\n"
"s_load_dwordx8 s[8:15], s[0:1], 0x18\n"
"s_load_dwordx4 s[16:19], s[0:1], 0x38\n"
"s_load_dwordx2 s[22:23], s[0:1], 0x0\n"
"s_load_dwordx4 s[24:27], s[0:1], 0x48\n"
"s_getpc_b64 s[4:5]\n"
".Lmk_a:\n"
"v_lshrrev_b32 v79, 6, v0\n"
"v_and_b32 v78, 7, v0\n"
"v_lshl_add_u32 v79, v79, 3, v78\n"
"s_lshr_b32 s79, s2, 3\n"
"s_lshl_b32 s79, s79, 7\n"
"s_sub_u32 s79, s79, 0x20000\n"
"v_lshl_add_u32 v2, v79, 12, s79\n"
"v_min_u32 v2, .L_yend-.Lmk_a-4, v2\n"
"s_mov_b32 m0, 163328\n"
"v_mov_b32 v4, 0\n"
"global_load_lds_dword v2, s[4:5]\n"
"v_lshlrev_b32 v79, 2, v0\n"
"v_add_u32 v2, 0x26000, v79\n"
"ds_write_b32 v2, v4\n"
"v_lshlrev_b32 v79, 4, v0\n"
"v_add_u32 v2, 0x25840, v79\n"
"v_mov_b32 v8, -1\n"
"v_mov_b32 v9, -1\n"
"v_mov_b32 v10, -1\n"
"v_mov_b32 v11, -1\n"
"ds_write_b128 v2, v[8:11]\n"
"ds_write_b128 v2, v[8:11] offset:4096\n"
"s_and_b32 s79, s2, 7\n"
"s_lshl_b32 s79, s79, 4\n"
"s_lshr_b32 s78, s2, 3\n"
"s_add_u32 s79, s79, s78\n"
"s_and_b32 s78, s2, 3\n"
"s_lshl_b32 s78, s78, 3\n"
"s_and_b32 s77, s2, 4\n"
"s_cmp_eq_i32 s77, 4\n"
"s_cselect_b32 s78, 32, s78\n"
"s_add_u32 s79, s79, s78\n"
"s_lshl_b32 s79, s79, 2\n"
"s_and_b32 s78, s3, 3\n"
"s_add_u32 s6, s79, s78\n"
"v_and_b32 v79, 63, v0\n"
"s_mul_i32 s79, s6, 0x180\n"
"v_lshl_add_u32 v2, v79, 1, s79\n"
"s_lshl_b32 s79, s6, 1\n"
"s_and_b32 s20, s79, 0xffc\n"
"s_and_b32 s79, s2, 4\n"
"s_lshl_b32 s79, s79, 4\n"
"s_add_u32 s7, s2, s79\n"
"s_waitcnt lgkmcnt(0)\n"
"s_barrier\n"
"s_cmp_ge_u32 s7, 192\n"
"s_cbranch_scc1 .Lnl_2\n"
"s_load_dword s40, s[16:17], s20\n"
"global_load_ushort v40, v2, s[8:9] offset:0\n"
"global_load_ushort v41, v2, s[10:11] offset:0\n"
"global_load_ushort v42, v2, s[12:13] offset:0\n"
"global_load_ushort v43, v2, s[14:15] offset:0\n"
"global_load_ushort v44, v2, s[8:9] offset:128\n"
"global_load_ushort v45, v2, s[10:11] offset:128\n"
"global_load_ushort v46, v2, s[12:13] offset:128\n"
"global_load_ushort v47, v2, s[14:15] offset:128\n"
"global_load_ushort v48, v2, s[8:9] offset:256\n"
"global_load_ushort v49, v2, s[10:11] offset:256\n"
"global_load_ushort v50, v2, s[12:13] offset:256\n"
"global_load_ushort v51, v2, s[14:15] offset:256\n"
"s_load_dword s41, s[18:19], s20\n"
".Lnl_2:\n"
"s_load_dword s16, s[26:27], 0x0\n"
"s_branch .L_xend\n"
".Lxs_1:\n"
"s_load_dwordx4 s[20:23], s[0:1], 0x0\n"
"s_load_dwordx2 s[56:57], s[0:1], 0x10\n"
"s_load_dwordx4 s[24:27], s[0:1], 0x48\n"
"v_and_b32 v0, 63, v0\n"
"s_barrier\n"
"s_lshl_b32 s79, s2, 2\n"
"s_add_u32 s31, s79, s3\n"
"s_mul_i32 s79, s31, 0xcd\n"
"s_lshr_b32 s46, s79, 11\n"
"s_mul_i32 s79, s46, 10\n"
"s_sub_u32 s47, s31, s79\n"
"s_lshl_b32 s79, s47, 6\n"
"v_add_u32 v68, s79, v0\n"
"v_lshlrev_b32 v80, 1, v68\n"
"s_mul_i32 s48, s46, 0x7800\n"
"s_mov_b32 s14, 0x258000\n"
"s_mov_b32 s15, 0x20000\n"
"s_waitcnt lgkmcnt(0)\n"
"s_mov_b64 s[12:13], s[22:23]\n"
"s_cmp_ge_u32 s2, 200\n"
"s_cbranch_scc1 .Lnorep_3\n"
"buffer_load_ushort v28, v80, s[12:15], s48 offen offset:0\n"
"buffer_load_ushort v36, v80, s[12:15], s48 offen offset:1280\n"
"buffer_load_ushort v44, v80, s[12:15], s48 offen offset:2560\n"
"s_add_u32 s48, s48, 3840\n"
"buffer_load_ushort v29, v80, s[12:15], s48 offen offset:0\n"
"buffer_load_ushort v37, v80, s[12:15], s48 offen offset:1280\n"
"buffer_load_ushort v45, v80, s[12:15], s48 offen offset:2560\n"
"s_add_u32 s48, s48, 3840\n"
"buffer_load_ushort v30, v80, s[12:15], s48 offen offset:0\n"
"buffer_load_ushort v38, v80, s[12:15], s48 offen offset:1280\n"
"buffer_load_ushort v46, v80, s[12:15], s48 offen offset:2560\n"
"s_add_u32 s48, s48, 3840\n"
"buffer_load_ushort v31, v80, s[12:15], s48 offen offset:0\n"
"buffer_load_ushort v39, v80, s[12:15], s48 offen offset:1280\n"
"buffer_load_ushort v47, v80, s[12:15], s48 offen offset:2560\n"
"s_add_u32 s48, s48, 3840\n"
"buffer_load_ushort v32, v80, s[12:15], s48 offen offset:0\n"
"buffer_load_ushort v40, v80, s[12:15], s48 offen offset:1280\n"
"buffer_load_ushort v48, v80, s[12:15], s48 offen offset:2560\n"
"s_add_u32 s48, s48, 3840\n"
"buffer_load_ushort v33, v80, s[12:15], s48 offen offset:0\n"
"buffer_load_ushort v41, v80, s[12:15], s48 offen offset:1280\n"
"buffer_load_ushort v49, v80, s[12:15], s48 offen offset:2560\n"
"s_add_u32 s48, s48, 3840\n"
"buffer_load_ushort v34, v80, s[12:15], s48 offen offset:0\n"
"buffer_load_ushort v42, v80, s[12:15], s48 offen offset:1280\n"
"buffer_load_ushort v50, v80, s[12:15], s48 offen offset:2560\n"
"s_add_u32 s48, s48, 3840\n"
"buffer_load_ushort v35, v80, s[12:15], s48 offen offset:0\n"
"buffer_load_ushort v43, v80, s[12:15], s48 offen offset:1280\n"
"buffer_load_ushort v51, v80, s[12:15], s48 offen offset:2560\n"
"s_branch .Lrep_4\n"
".Lnorep_3:\n"
"s_mov_b64 s[12:13], s[56:57]\n"
"s_cmp_lt_i32 s31, 0x3e8\n"
"s_cselect_b32 s14, 0xc8000, 0\n"
"v_lshlrev_b32 v80, 1, v0\n"
"s_lshl_b32 s79, s31, 2\n"
"s_add_u32 s79, s79, 0xfffff380\n"
"s_mul_i32 s79, s79, 0xcd\n"
"s_lshr_b32 s80, s79, 11\n"
"s_lshl_b32 s79, s31, 2\n"
"s_add_u32 s79, s79, 0xfffff380\n"
"s_mul_i32 s78, s80, 10\n"
"s_sub_u32 s84, s79, s78\n"
"s_mul_i32 s79, s80, 0x67\n"
"s_lshr_b32 s79, s79, 9\n"
"s_mul_i32 s79, s79, 20\n"
"s_lshl_b32 s78, s80, 2\n"
"s_add_u32 s79, s79, s78\n"
"s_mul_i32 s79, s79, 0x500\n"
"s_lshl_b32 s78, s84, 7\n"
"s_add_u32 s48, s79, s78\n"
"s_add_u32 s49, s48, 0x6400\n"
"buffer_load_ushort v100, v80, s[12:15], s48 offen offset:0\n"
"buffer_load_ushort v101, v80, s[12:15], s48 offen offset:1280\n"
"buffer_load_ushort v102, v80, s[12:15], s48 offen offset:2560\n"
"buffer_load_ushort v103, v80, s[12:15], s48 offen offset:3840\n"
"buffer_load_ushort v104, v80, s[12:15], s49 offen offset:0\n"
"buffer_load_ushort v105, v80, s[12:15], s49 offen offset:1280\n"
"buffer_load_ushort v106, v80, s[12:15], s49 offen offset:2560\n"
"buffer_load_ushort v107, v80, s[12:15], s49 offen offset:3840\n"
"s_lshl_b32 s79, s31, 2\n"
"s_add_u32 s79, s79, 0xfffff381\n"
"s_mul_i32 s79, s79, 0xcd\n"
"s_lshr_b32 s81, s79, 11\n"
"s_lshl_b32 s79, s31, 2\n"
"s_add_u32 s79, s79, 0xfffff381\n"
"s_mul_i32 s78, s81, 10\n"
"s_sub_u32 s85, s79, s78\n"
"s_mul_i32 s79, s81, 0x67\n"
"s_lshr_b32 s79, s79, 9\n"
"s_mul_i32 s79, s79, 20\n"
"s_lshl_b32 s78, s81, 2\n"
"s_add_u32 s79, s79, s78\n"
"s_mul_i32 s79, s79, 0x500\n"
"s_lshl_b32 s78, s85, 7\n"
"s_add_u32 s48, s79, s78\n"
"s_add_u32 s49, s48, 0x6400\n"
"buffer_load_ushort v108, v80, s[12:15], s48 offen offset:0\n"
"buffer_load_ushort v109, v80, s[12:15], s48 offen offset:1280\n"
"buffer_load_ushort v110, v80, s[12:15], s48 offen offset:2560\n"
"buffer_load_ushort v111, v80, s[12:15], s48 offen offset:3840\n"
"buffer_load_ushort v112, v80, s[12:15], s49 offen offset:0\n"
"buffer_load_ushort v113, v80, s[12:15], s49 offen offset:1280\n"
"buffer_load_ushort v114, v80, s[12:15], s49 offen offset:2560\n"
"buffer_load_ushort v115, v80, s[12:15], s49 offen offset:3840\n"
"s_lshl_b32 s79, s31, 2\n"
"s_add_u32 s79, s79, 0xfffff382\n"
"s_mul_i32 s79, s79, 0xcd\n"
"s_lshr_b32 s82, s79, 11\n"
"s_lshl_b32 s79, s31, 2\n"
"s_add_u32 s79, s79, 0xfffff382\n"
"s_mul_i32 s78, s82, 10\n"
"s_sub_u32 s86, s79, s78\n"
"s_mul_i32 s79, s82, 0x67\n"
"s_lshr_b32 s79, s79, 9\n"
"s_mul_i32 s79, s79, 20\n"
"s_lshl_b32 s78, s82, 2\n"
"s_add_u32 s79, s79, s78\n"
"s_mul_i32 s79, s79, 0x500\n"
"s_lshl_b32 s78, s86, 7\n"
"s_add_u32 s48, s79, s78\n"
"s_add_u32 s49, s48, 0x6400\n"
"buffer_load_ushort v116, v80, s[12:15], s48 offen offset:0\n"
"buffer_load_ushort v117, v80, s[12:15], s48 offen offset:1280\n"
"buffer_load_ushort v118, v80, s[12:15], s48 offen offset:2560\n"
"buffer_load_ushort v119, v80, s[12:15], s48 offen offset:3840\n"
"buffer_load_ushort v120, v80, s[12:15], s49 offen offset:0\n"
"buffer_load_ushort v121, v80, s[12:15], s49 offen offset:1280\n"
"buffer_load_ushort v122, v80, s[12:15], s49 offen offset:2560\n"
"buffer_load_ushort v123, v80, s[12:15], s49 offen offset:3840\n"
"s_lshl_b32 s79, s31, 2\n"
"s_add_u32 s79, s79, 0xfffff383\n"
"s_mul_i32 s79, s79, 0xcd\n"
"s_lshr_b32 s83, s79, 11\n"
"s_lshl_b32 s79, s31, 2\n"
"s_add_u32 s79, s79, 0xfffff383\n"
"s_mul_i32 s78, s83, 10\n"
"s_sub_u32 s87, s79, s78\n"
"s_mul_i32 s79, s83, 0x67\n"
"s_lshr_b32 s79, s79, 9\n"
"s_mul_i32 s79, s79, 20\n"
"s_lshl_b32 s78, s83, 2\n"
"s_add_u32 s79, s79, s78\n"
"s_mul_i32 s79, s79, 0x500\n"
"s_lshl_b32 s78, s87, 7\n"
"s_add_u32 s48, s79, s78\n"
"s_add_u32 s49, s48, 0x6400\n"
"buffer_load_ushort v124, v80, s[12:15], s48 offen offset:0\n"
"buffer_load_ushort v125, v80, s[12:15], s48 offen offset:1280\n"
"buffer_load_ushort v126, v80, s[12:15], s48 offen offset:2560\n"
"buffer_load_ushort v127, v80, s[12:15], s48 offen offset:3840\n"
"buffer_load_ushort v128, v80, s[12:15], s49 offen offset:0\n"
"buffer_load_ushort v129, v80, s[12:15], s49 offen offset:1280\n"
"buffer_load_ushort v130, v80, s[12:15], s49 offen offset:2560\n"
"buffer_load_ushort v131, v80, s[12:15], s49 offen offset:3840\n"
".Lrep_4:\n"
"s_mov_b32 s4, s26\n"
"s_mov_b32 s5, s27\n"
"s_mov_b32 s6, 0x15a8000\n"
"s_mov_b32 s7, 0x20000\n"
"s_load_dword s16, s[26:27], 0x0\n"
"v_lshlrev_b32 v1, 4, v0\n"
"s_and_b32 s79, s2, 7\n"
"s_lshl_b32 s79, s79, 4\n"
"s_lshr_b32 s78, s2, 3\n"
"s_add_u32 s79, s79, s78\n"
"s_and_b32 s78, s2, 3\n"
"s_lshl_b32 s78, s78, 3\n"
"s_and_b32 s77, s2, 4\n"
"s_cmp_eq_i32 s77, 4\n"
"s_cselect_b32 s78, 32, s78\n"
"s_add_u32 s79, s79, s78\n"
"s_and_b32 s78, s2, 4\n"
"s_lshl_b32 s78, s78, 4\n"
"s_add_u32 s78, s2, s78\n"
"s_cmp_lt_i32 s78, 0xc0\n"
"s_cselect_b32 s45, s79, -1\n"
"s_mov_b32 s8, s20\n"
"s_mov_b32 s9, s21\n"
"s_mov_b32 s10, 0x280000\n"
"s_mov_b32 s11, 0x20000\n"
"s_mov_b32 s12, s24\n"
"s_mov_b32 s13, s25\n"
"s_mov_b32 s14, 0x280000\n"
"s_mov_b32 s15, 0x20000\n"
"s_and_b32 s79, s2, 1\n"
"s_lshl_b32 s79, s79, 3\n"
"s_lshr_b32 s78, s2, 3\n"
"s_and_b32 s78, s78, 7\n"
"s_add_u32 s18, s79, s78\n"
"s_lshr_b32 s79, s2, 1\n"
"s_and_b32 s79, s79, 3\n"
"s_lshl_b32 s79, s79, 2\n"
"s_lshr_b32 s78, s2, 3\n"
"s_lshr_b32 s78, s78, 3\n"
"s_add_u32 s19, s79, s78\n"
"s_mul_i32 s79, s19, 0\n"
"s_add_u32 s79, s3, s79\n"
"s_and_b32 s43, s79, 3\n"
"v_mul_i32_i24 v79, 0xcd, v68\n"
"v_lshrrev_b32 v84, 13, v79\n"
"v_mul_i32_i24 v79, 40, v84\n"
"v_sub_u32 v85, v68, v79\n"
"v_mul_i32_i24 v79, 0xa0, v84\n"
"v_lshlrev_b32 v84, 10, v79\n"
"v_subrev_u32 v79, 24, v85\n"
"v_and_b32 v78, 15, v85\n"
"v_cmp_le_i32 vcc, 32, v85\n"
"s_nop 1\n"
"v_cndmask_b32 v79, v78, v79, vcc\n"
"v_lshrrev_b32 v78, 4, v85\n"
"s_mov_b32 s79, 0xa000\n"
"v_mad_i32_i24 v78, v78, s79, v84\n"
"v_lshl_add_u32 v64, v79, 4, v78\n"
"v_mov_b32 v86, v85\n"
"v_and_b32 v79, 15, v86\n"
"v_lshrrev_b32 v78, 4, v86\n"
"v_min_i32 v78, 3, v78\n"
"s_mov_b32 s79, 0xa000\n"
"v_mad_i32_i24 v78, v78, s79, v84\n"
"v_mov_b32 v77, 0\n"
"v_mov_b32 v76, 0x400\n"
"v_lshrrev_b32 v75, 4, v86\n"
"v_cmp_eq_i32 vcc, 3, v75\n"
"s_nop 1\n"
"v_cndmask_b32 v77, v76, v77, vcc\n"
"v_add_u32 v78, v78, v77\n"
"v_lshl_add_u32 v65, v79, 4, v78\n"
"v_add_u32 v86, 40, v85\n"
"v_and_b32 v79, 15, v86\n"
"v_lshrrev_b32 v78, 4, v86\n"
"v_min_i32 v78, 3, v78\n"
"s_mov_b32 s79, 0xa000\n"
"v_mad_i32_i24 v78, v78, s79, v84\n"
"v_mov_b32 v77, 0\n"
"v_mov_b32 v76, 0x400\n"
"v_lshrrev_b32 v75, 4, v86\n"
"v_cmp_eq_i32 vcc, 3, v75\n"
"s_nop 1\n"
"v_cndmask_b32 v77, v76, v77, vcc\n"
"v_add_u32 v78, v78, v77\n"
"v_lshl_add_u32 v66, v79, 4, v78\n"
"v_subrev_u32 v79, 0x80, v64\n"
"v_mov_b32 v78, 0x40000000\n"
"v_cmp_le_i32 vcc, 32, v85\n"
"s_nop 1\n"
"v_cndmask_b32 v67, v78, v79, vcc\n"
"s_lshr_b32 s79, s46, 2\n"
"s_lshl_b32 s79, s79, 11\n"
"s_and_b32 s78, s46, 3\n"
"s_lshl_b32 s78, s78, 8\n"
"s_add_u32 s79, s79, s78\n"
"s_add_u32 s48, s79, 0x20000\n"
"s_lshl_b32 s79, s47, 9\n"
"s_lshl_b32 s78, s46, 2\n"
"s_add_u32 s79, s79, s78\n"
"s_lshl_b32 s78, s31, 2\n"
"s_add_u32 s78, s78, 0x780\n"
"s_cmp_lt_i32 s31, 0x320\n"
"s_cselect_b32 s79, s79, s78\n"
"s_add_u32 s32, s79, 0x1000\n"
"v_mov_b32 v20, 0x3f803f80\n"
"v_mov_b32 v21, 0x3f803f80\n"
"v_mov_b32 v22, 0x3f803f80\n"
"v_mov_b32 v23, 0x3f803f80\n"
"s_mov_b32 s79, 0x55\n"
"s_cmp_lt_i32 s47, 5\n"
"s_cselect_b32 s79, s79, 0xaa\n"
"s_cmp_lt_i32 s31, 0x320\n"
"s_cselect_b32 s58, s79, 0xff\n"
"s_mov_b32 s59, 0\n"
"s_mul_i32 s79, s19, 0x14000\n"
"s_mul_i32 s78, s43, 0x140\n"
"s_add_u32 s28, s79, s78\n"
"s_mul_i32 s29, s43, 0x1400\n"
"s_lshr_b32 s79, s2, 3\n"
"s_and_b32 s31, s79, 7\n"
"s_mul_i32 s33, s43, 0x140\n"
"s_sub_u32 s79, s31, 5\n"
"s_cmp_ge_i32 s31, 5\n"
"s_cselect_b32 s31, s79, s31\n"
"s_mul_i32 s79, s31, 0x5000\n"
"s_add_u32 s79, s79, s28\n"
"s_sub_u32 s79, s79, 0x5000\n"
"s_max_i32 s60, s79, s33\n"
"s_mul_i32 s79, s31, 0x5000\n"
"s_mov_b32 s78, 0x12400\n"
"s_cmp_ge_i32 s31, 2\n"
"s_cselect_b32 s78, s78, 0xa000\n"
"s_add_u32 s79, s79, s78\n"
"s_cmp_eq_i32 s31, 4\n"
"s_cselect_b32 s79, 0, s79\n"
"s_add_u32 s61, s29, s79\n"
"s_sub_u32 s79, s31, 4\n"
"s_add_u32 s78, s31, 1\n"
"s_cmp_ge_i32 s31, 4\n"
"s_cselect_b32 s31, s79, s78\n"
"s_mul_i32 s79, s31, 0x5000\n"
"s_add_u32 s79, s79, s28\n"
"s_sub_u32 s79, s79, 0x5000\n"
"s_max_i32 s62, s79, s33\n"
"s_mul_i32 s79, s31, 0x5000\n"
"s_mov_b32 s78, 0x12400\n"
"s_cmp_ge_i32 s31, 2\n"
"s_cselect_b32 s78, s78, 0xa000\n"
"s_add_u32 s79, s79, s78\n"
"s_cmp_eq_i32 s31, 4\n"
"s_cselect_b32 s79, 0, s79\n"
"s_add_u32 s63, s29, s79\n"
"s_sub_u32 s79, s31, 4\n"
"s_add_u32 s78, s31, 1\n"
"s_cmp_ge_i32 s31, 4\n"
"s_cselect_b32 s31, s79, s78\n"
"s_mul_i32 s79, s31, 0x5000\n"
"s_add_u32 s79, s79, s28\n"
"s_sub_u32 s79, s79, 0x5000\n"
"s_max_i32 s64, s79, s33\n"
"s_mul_i32 s79, s31, 0x5000\n"
"s_mov_b32 s78, 0x12400\n"
"s_cmp_ge_i32 s31, 2\n"
"s_cselect_b32 s78, s78, 0xa000\n"
"s_add_u32 s79, s79, s78\n"
"s_cmp_eq_i32 s31, 4\n"
"s_cselect_b32 s79, 0, s79\n"
"s_add_u32 s65, s29, s79\n"
"s_sub_u32 s79, s31, 4\n"
"s_add_u32 s78, s31, 1\n"
"s_cmp_ge_i32 s31, 4\n"
"s_cselect_b32 s31, s79, s78\n"
"s_mul_i32 s79, s31, 0x5000\n"
"s_add_u32 s79, s79, s28\n"
"s_sub_u32 s79, s79, 0x5000\n"
"s_max_i32 s66, s79, s33\n"
"s_mul_i32 s79, s31, 0x5000\n"
"s_mov_b32 s78, 0x12400\n"
"s_cmp_ge_i32 s31, 2\n"
"s_cselect_b32 s78, s78, 0xa000\n"
"s_add_u32 s79, s79, s78\n"
"s_cmp_eq_i32 s31, 4\n"
"s_cselect_b32 s79, 0, s79\n"
"s_add_u32 s67, s29, s79\n"
"s_sub_u32 s79, s31, 4\n"
"s_add_u32 s78, s31, 1\n"
"s_cmp_ge_i32 s31, 4\n"
"s_cselect_b32 s31, s79, s78\n"
"s_mul_i32 s79, s31, 0x5000\n"
"s_add_u32 s79, s79, s28\n"
"s_sub_u32 s79, s79, 0x5000\n"
"s_max_i32 s68, s79, s33\n"
"s_mul_i32 s79, s31, 0x5000\n"
"s_mov_b32 s78, 0x12400\n"
"s_cmp_ge_i32 s31, 2\n"
"s_cselect_b32 s78, s78, 0xa000\n"
"s_add_u32 s79, s79, s78\n"
"s_cmp_eq_i32 s31, 4\n"
"s_cselect_b32 s79, 0, s79\n"
"s_add_u32 s69, s29, s79\n"
"v_and_b32 v79, 3, v0\n"
"v_lshrrev_b32 v78, 5, v0\n"
"v_lshlrev_b32 v78, 1, v78\n"
"v_xor_b32 v79, v79, v78\n"
"v_lshrrev_b32 v78, 2, v0\n"
"v_mul_i32_i24 v78, 0x500, v78\n"
"v_lshl_add_u32 v2, v79, 4, v78\n"
"v_min_i32 v79, 24, v0\n"
"s_lshr_b32 s79, s2, 3\n"
"s_and_b32 s79, s79, 7\n"
"s_lshl_b32 s79, s79, 2\n"
"s_add_u32 s79, s79, s3\n"
"s_mul_i32 s79, s79, 0xc80\n"
"s_lshl_b32 s78, s19, 6\n"
"s_add_u32 s78, s78, 0x3f0\n"
"s_mul_i32 s78, s78, 0x500\n"
"s_add_u32 s79, s79, s78\n"
"v_lshl_add_u32 v89, v79, 7, s79\n"
"s_mov_b32 s31, 0x05040100\n"
"s_cmp_ge_u32 s2, 200\n"
"s_cbranch_scc1 .Lnorep_5\n"
"buffer_store_dwordx4 v[20:23], v67, s[4:7], s48 offen sc1\n"
"s_waitcnt vmcnt(1)\n"
"v_perm_b32 v52, v29, v28, s31\n"
"v_perm_b32 v53, v31, v30, s31\n"
"v_perm_b32 v54, v33, v32, s31\n"
"v_perm_b32 v55, v35, v34, s31\n"
"buffer_store_dwordx4 v[52:55], v64, s[4:7], s48 offen sc1\n"
"v_perm_b32 v56, v37, v36, s31\n"
"v_perm_b32 v57, v39, v38, s31\n"
"v_perm_b32 v58, v41, v40, s31\n"
"v_perm_b32 v59, v43, v42, s31\n"
"buffer_store_dwordx4 v[56:59], v65, s[4:7], s48 offen sc1\n"
"v_perm_b32 v60, v45, v44, s31\n"
"v_perm_b32 v61, v47, v46, s31\n"
"v_perm_b32 v62, v49, v48, s31\n"
"v_perm_b32 v63, v51, v50, s31\n"
"buffer_store_dwordx4 v[60:63], v66, s[4:7], s48 offen sc1\n"
"s_branch .Lrep_6\n"
".Lnorep_5:\n"
"s_lshl_b32 s79, s84, 6\n"
"v_add_u32 v68, s79, v0\n"
"v_mul_i32_i24 v79, 0xcd, v68\n"
"v_lshrrev_b32 v81, 14, v79\n"
"s_lshr_b32 s79, s80, 2\n"
"s_mul_i32 s79, s79, 13\n"
"s_lshr_b32 s79, s79, 6\n"
"s_and_b32 s79, s79, 1\n"
"s_lshl_b32 s79, s79, 1\n"
"v_mul_i32_i24 v79, 0x50, v81\n"
"v_sub_u32 v79, v68, v79\n"
"v_lshrrev_b32 v79, 4, v79\n"
"v_add_u32 v82, s79, v79\n"
"v_subrev_u32 v79, 5, v82\n"
"v_cmp_le_i32 vcc, 5, v82\n"
"s_nop 1\n"
"v_cndmask_b32 v79, v82, v79, vcc\n"
"s_mov_b32 s79, 0x64\n"
"v_mad_i32_i24 v79, v81, s79, v79\n"
"v_and_b32 v78, 15, v68\n"
"v_lshlrev_b32 v78, 4, v78\n"
"v_lshl_add_u32 v64, v79, 10, v78\n"
"s_lshr_b32 s79, s80, 2\n"
"s_mul_i32 s79, s79, 0x1400\n"
"s_and_b32 s78, s80, 3\n"
"s_lshl_b32 s78, s78, 8\n"
"s_add_u32 s79, s79, s78\n"
"s_add_u32 s79, s79, 0x2a0000\n"
"s_cmp_lt_i32 s80, 0x50\n"
"s_cselect_b32 s80, s79, 0x40000000\n"
"s_lshl_b32 s79, s85, 6\n"
"v_add_u32 v68, s79, v0\n"
"v_mul_i32_i24 v79, 0xcd, v68\n"
"v_lshrrev_b32 v81, 14, v79\n"
"s_lshr_b32 s79, s81, 2\n"
"s_mul_i32 s79, s79, 13\n"
"s_lshr_b32 s79, s79, 6\n"
"s_and_b32 s79, s79, 1\n"
"s_lshl_b32 s79, s79, 1\n"
"v_mul_i32_i24 v79, 0x50, v81\n"
"v_sub_u32 v79, v68, v79\n"
"v_lshrrev_b32 v79, 4, v79\n"
"v_add_u32 v82, s79, v79\n"
"v_subrev_u32 v79, 5, v82\n"
"v_cmp_le_i32 vcc, 5, v82\n"
"s_nop 1\n"
"v_cndmask_b32 v79, v82, v79, vcc\n"
"s_mov_b32 s79, 0x64\n"
"v_mad_i32_i24 v79, v81, s79, v79\n"
"v_and_b32 v78, 15, v68\n"
"v_lshlrev_b32 v78, 4, v78\n"
"v_lshl_add_u32 v65, v79, 10, v78\n"
"s_lshr_b32 s79, s81, 2\n"
"s_mul_i32 s79, s79, 0x1400\n"
"s_and_b32 s78, s81, 3\n"
"s_lshl_b32 s78, s78, 8\n"
"s_add_u32 s79, s79, s78\n"
"s_add_u32 s79, s79, 0x2a0000\n"
"s_cmp_lt_i32 s81, 0x50\n"
"s_cselect_b32 s81, s79, 0x40000000\n"
"s_lshl_b32 s79, s86, 6\n"
"v_add_u32 v68, s79, v0\n"
"v_mul_i32_i24 v79, 0xcd, v68\n"
"v_lshrrev_b32 v81, 14, v79\n"
"s_lshr_b32 s79, s82, 2\n"
"s_mul_i32 s79, s79, 13\n"
"s_lshr_b32 s79, s79, 6\n"
"s_and_b32 s79, s79, 1\n"
"s_lshl_b32 s79, s79, 1\n"
"v_mul_i32_i24 v79, 0x50, v81\n"
"v_sub_u32 v79, v68, v79\n"
"v_lshrrev_b32 v79, 4, v79\n"
"v_add_u32 v82, s79, v79\n"
"v_subrev_u32 v79, 5, v82\n"
"v_cmp_le_i32 vcc, 5, v82\n"
"s_nop 1\n"
"v_cndmask_b32 v79, v82, v79, vcc\n"
"s_mov_b32 s79, 0x64\n"
"v_mad_i32_i24 v79, v81, s79, v79\n"
"v_and_b32 v78, 15, v68\n"
"v_lshlrev_b32 v78, 4, v78\n"
"v_lshl_add_u32 v66, v79, 10, v78\n"
"s_lshr_b32 s79, s82, 2\n"
"s_mul_i32 s79, s79, 0x1400\n"
"s_and_b32 s78, s82, 3\n"
"s_lshl_b32 s78, s78, 8\n"
"s_add_u32 s79, s79, s78\n"
"s_add_u32 s79, s79, 0x2a0000\n"
"s_cmp_lt_i32 s82, 0x50\n"
"s_cselect_b32 s82, s79, 0x40000000\n"
"s_lshl_b32 s79, s87, 6\n"
"v_add_u32 v68, s79, v0\n"
"v_mul_i32_i24 v79, 0xcd, v68\n"
"v_lshrrev_b32 v81, 14, v79\n"
"s_lshr_b32 s79, s83, 2\n"
"s_mul_i32 s79, s79, 13\n"
"s_lshr_b32 s79, s79, 6\n"
"s_and_b32 s79, s79, 1\n"
"s_lshl_b32 s79, s79, 1\n"
"v_mul_i32_i24 v79, 0x50, v81\n"
"v_sub_u32 v79, v68, v79\n"
"v_lshrrev_b32 v79, 4, v79\n"
"v_add_u32 v82, s79, v79\n"
"v_subrev_u32 v79, 5, v82\n"
"v_cmp_le_i32 vcc, 5, v82\n"
"s_nop 1\n"
"v_cndmask_b32 v79, v82, v79, vcc\n"
"s_mov_b32 s79, 0x64\n"
"v_mad_i32_i24 v79, v81, s79, v79\n"
"v_and_b32 v78, 15, v68\n"
"v_lshlrev_b32 v78, 4, v78\n"
"v_lshl_add_u32 v67, v79, 10, v78\n"
"s_lshr_b32 s79, s83, 2\n"
"s_mul_i32 s79, s79, 0x1400\n"
"s_and_b32 s78, s83, 3\n"
"s_lshl_b32 s78, s78, 8\n"
"s_add_u32 s79, s79, s78\n"
"s_add_u32 s79, s79, 0x2a0000\n"
"s_cmp_lt_i32 s83, 0x50\n"
"s_cselect_b32 s83, s79, 0x40000000\n"
"s_waitcnt vmcnt(0)\n"
"v_perm_b32 v28, v101, v100, s31\n"
"v_perm_b32 v29, v103, v102, s31\n"
"v_perm_b32 v30, v105, v104, s31\n"
"v_perm_b32 v31, v107, v106, s31\n"
"buffer_store_dwordx4 v[28:31], v64, s[4:7], s80 offen sc1\n"
"v_perm_b32 v32, v109, v108, s31\n"
"v_perm_b32 v33, v111, v110, s31\n"
"v_perm_b32 v34, v113, v112, s31\n"
"v_perm_b32 v35, v115, v114, s31\n"
"buffer_store_dwordx4 v[32:35], v65, s[4:7], s81 offen sc1\n"
"v_perm_b32 v36, v117, v116, s31\n"
"v_perm_b32 v37, v119, v118, s31\n"
"v_perm_b32 v38, v121, v120, s31\n"
"v_perm_b32 v39, v123, v122, s31\n"
"buffer_store_dwordx4 v[36:39], v66, s[4:7], s82 offen sc1\n"
"v_perm_b32 v40, v125, v124, s31\n"
"v_perm_b32 v41, v127, v126, s31\n"
"v_perm_b32 v42, v129, v128, s31\n"
"v_perm_b32 v43, v131, v130, s31\n"
"buffer_store_dwordx4 v[40:43], v67, s[4:7], s83 offen sc1\n"
".Lrep_6:\n"
"v_lshlrev_b32 v80, 13, v0\n"
"s_mov_b32 s34, 0xaaaaaaaa\n"
"s_mov_b32 s35, 0xaaaaaaaa\n"
"s_mov_b32 s40, 0xffff\n"
"s_mov_b32 s41, 0\n"
"s_mov_b32 s36, 0xfff0ffff\n"
"s_mov_b32 s37, 0xf000ff00\n"
"s_mov_b32 s38, 0xffc0fffc\n"
"s_mov_b32 s39, 0xc000fc00\n"
"s_lshl_b32 s79, s19, 2\n"
"s_add_u32 s79, s79, s3\n"
"s_mul_i32 s79, s79, 0x5000\n"
"s_mul_i32 s78, s18, 0x500\n"
"s_add_u32 s79, s79, s78\n"
"s_add_u32 s30, s79, 0x368000\n"
"s_mul_i32 s79, s18, 40\n"
"s_lshr_b32 s79, s79, 6\n"
"s_lshl_b32 s79, s79, 9\n"
"v_lshl_add_u32 v79, v0, 4, s79\n"
"v_min_i32 v78, 39, v0\n"
"v_subrev_u32 v78, 20, v78\n"
"s_mul_i32 s79, s18, 40\n"
"s_add_u32 s79, s79, 39\n"
"s_lshr_b32 s79, s79, 6\n"
"s_lshl_b32 s79, s79, 9\n"
"v_lshl_add_u32 v78, v78, 4, s79\n"
"v_cmp_gt_i32 vcc, 20, v0\n"
"s_nop 1\n"
"v_cndmask_b32 v19, v78, v79, vcc\n"
"s_and_b32 s79, s2, 7\n"
"s_lshl_b32 s79, s79, 13\n"
"s_add_u32 s50, s79, 0x1000\n"
"s_mul_i32 s79, s19, 0x14000\n"
"s_mul_i32 s78, s43, 0x1400\n"
"s_add_u32 s79, s79, s78\n"
"s_add_u32 s44, s79, 0xb48000\n"
"s_add_u32 s79, s19, s3\n"
"s_cmp_eq_i32 s79, 0\n"
"s_cselect_b32 s42, 0xfe967699, 0\n"
"s_lshl_b32 s79, s18, 2\n"
"s_add_u32 s79, s79, s3\n"
"s_mul_i32 s79, s79, 0xa000\n"
"s_add_u32 s31, s79, 0x20000\n"
"s_lshl_b32 s79, s43, 11\n"
"s_add_u32 s52, s31, s79\n"
"s_mov_b32 s79, 0xffffe800\n"
"s_cmp_ge_i32 s43, 3\n"
"s_cselect_b32 s79, s79, 0x8800\n"
"s_add_u32 s53, s52, s79\n"
"s_mov_b32 s79, 0xfffff000\n"
"s_cmp_ge_i32 s43, 2\n"
"s_cselect_b32 s79, s79, 0x9000\n"
"s_add_u32 s54, s52, s79\n"
"s_mov_b32 s79, 0xfffff800\n"
"s_cmp_ge_i32 s43, 1\n"
"s_cselect_b32 s79, s79, 0x9800\n"
"s_add_u32 s55, s52, s79\n"
"s_mov_b32 s20, 0xcccccccc\n"
"s_mov_b32 s21, 0xcccccccc\n"
"s_mov_b32 s22, 0xf0000f\n"
"s_mov_b32 s23, 0xf0000f00\n"
"v_and_b32 v169, 3, v0\n"
"v_and_b32 v168, 15, v0\n"
"v_lshrrev_b32 v167, 4, v0\n"
"v_and_b32 v166, 3, v168\n"
"v_lshlrev_b32 v165, 2, v166\n"
"v_lshlrev_b32 v164, 3, v167\n"
"v_lshlrev_b32 v163, 8, v167\n"
"v_lshlrev_b32 v162, 2, v168\n"
"v_lshlrev_b32 v161, 4, v168\n"
"v_lshlrev_b32 v160, 6, v168\n"
"v_lshrrev_b32 v159, 2, v168\n"
"v_lshrrev_b32 v158, 3, v168\n"
"v_lshlrev_b32 v157, 1, v158\n"
"v_mul_i32_i24 v156, 0xf8, v168\n"
"v_add_u32 v155, v161, v165\n"
"v_add_u32 v154, v155, v163\n"
"v_xor_b32 v153, v167, v157\n"
"v_lshlrev_b32 v152, 4, v153\n"
"v_add_u32 v151, v160, v152\n"
"v_add_u32 v17, 0x5000, v154\n"
"v_add_u32 v12, 0x26940, v162\n"
"s_lshl_b32 s79, s3, 4\n"
"v_add_u32 v150, s79, v168\n"
"v_mul_i32_i24 v79, 0xf8, v150\n"
"v_add_u32 v24, 0x5000, v79\n"
"s_lshl_b32 s79, s3, 11\n"
"s_add_u32 s79, s79, 0x5000\n"
"v_add_u32 v79, s79, v161\n"
"v_mov_b32 v78, 0x40000\n"
"v_cmp_eq_i32 vcc, v167, v159\n"
"s_nop 1\n"
"v_cndmask_b32 v92, v78, v79, vcc\n"
"v_mov_b32 v79, 0xf0\n"
"v_add_u32 v78, 48, v164\n"
"v_cmp_gt_i32 vcc, 2, v167\n"
"s_nop 1\n"
"v_cndmask_b32 v79, v78, v79, vcc\n"
"s_lshl_b32 s79, s3, 5\n"
"s_cmp_eq_i32 s3, 3\n"
"s_cselect_b32 s79, 0xb0, s79\n"
"v_add_u32 v78, s79, v164\n"
"v_mov_b32 v77, s3\n"
"v_cmp_eq_i32 vcc, 2, v77\n"
"s_nop 1\n"
"v_cndmask_b32 v79, v78, v79, vcc\n"
"s_mov_b32 s79, 0x5000\n"
"v_add3_u32 v7, v156, v79, s79\n"
"s_lshl_b32 s79, s3, 5\n"
"s_add_u32 s79, s79, 0x50\n"
"s_cmp_eq_i32 s3, 3\n"
"s_cselect_b32 s79, 0xd0, s79\n"
"s_add_u32 s79, s79, 0x5000\n"
"v_add3_u32 v8, v156, v164, s79\n"
"s_lshl_b32 s79, s3, 2\n"
"s_add_u32 s79, s79, 0x26840\n"
"v_add_u32 v79, s79, v161\n"
"v_mov_b32 v78, 0x40000\n"
"s_xor_b32 s79, s3, 2\n"
"s_lshl_b32 s79, s79, 2\n"
"v_add_u32 v77, s79, v167\n"
"v_mov_b32 v76, 1\n"
"v_cmp_gt_i32 vcc, 4, v168\n"
"s_nop 1\n"
"v_cndmask_b32 v76, 0, v76, vcc\n"
"v_cmp_eq_i32 vcc, v77, v76\n"
"s_nop 1\n"
"v_cndmask_b32 v78, v78, v12, vcc\n"
"v_cmp_eq_i32 vcc, v167, v159\n"
"s_nop 1\n"
"v_cndmask_b32 v9, v78, v79, vcc\n"
"v_add_u32 v11, 0x26840, v161\n"
"s_lshl_b32 s79, s3, 2\n"
"s_add_u32 s79, s79, 0x26840\n"
"v_add_u32 v79, s79, v161\n"
"v_mov_b32 v78, 0x40000\n"
"v_cmp_eq_i32 vcc, 0, v167\n"
"s_nop 1\n"
"v_cndmask_b32 v10, v78, v79, vcc\n"
"s_lshl_b32 s79, s3, 2\n"
"s_add_u32 s79, s79, 0x26400\n"
"v_mov_b32 v79, s79\n"
"v_mov_b32 v78, 0x40000\n"
"v_cmp_eq_i32 vcc, 0, v0\n"
"s_nop 1\n"
"v_cndmask_b32 v13, v78, v79, vcc\n"
"v_lshlrev_b32 v79, 2, v169\n"
"v_add_u32 v14, 0x26400, v79\n"
"v_add_u32 v25, v24, v164\n"
"s_lshl_b32 s79, s3, 4\n"
"v_lshl_add_u32 v79, v167, 2, s79\n"
"v_add_u32 v79, v79, v159\n"
"v_mul_i32_i24 v79, 0xf8, v79\n"
"v_lshl_add_u32 v79, v166, 3, v79\n"
"v_add_u32 v26, 0x50a0, v79\n"
"v_mov_b32 v27, 0x3727c5ac\n"
"v_xor_b32 v18, 4, v17\n"
"v_xor_b32 v79, 1, v166\n"
"v_lshl_add_u32 v91, v79, 2, v92\n"
"s_mul_i32 s79, s43, 0x1400\n"
"v_lshrrev_b32 v79, 2, v0\n"
"v_lshlrev_b32 v78, 4, v169\n"
"v_lshl_add_u32 v79, v79, 10, v78\n"
"v_add_u32 v79, s79, v79\n"
"v_mov_b32 v78, 0x40000\n"
"v_cmp_gt_i32 vcc, 20, v0\n"
"s_nop 1\n"
"v_cndmask_b32 v93, v78, v79, vcc\n"
"v_lshlrev_b32 v79, 3, v168\n"
"v_add_u32 v94, 0x9d80, v79\n"
"v_lshlrev_b32 v79, 3, v0\n"
"v_add_u32 v16, 0x9d80, v79\n"
"s_mul_i32 s79, s43, 0x1400\n"
"v_add_u32 v88, s79, v151\n"
"s_lshl_b32 s79, s43, 10\n"
"v_add_u32 v3, s79, v151\n"
"s_lshl_b32 s79, s43, 10\n"
"s_add_u32 s79, s79, 0xfffff400\n"
"s_lshl_b32 s78, s43, 10\n"
"s_add_u32 s78, s78, 0x4400\n"
"s_cmp_ge_i32 s43, 3\n"
"s_cselect_b32 s79, s79, s78\n"
"v_add_u32 v4, s79, v151\n"
"s_lshl_b32 s79, s43, 10\n"
"s_add_u32 s79, s79, 0xfffff800\n"
"s_lshl_b32 s78, s43, 10\n"
"s_add_u32 s78, s78, 0x4800\n"
"s_cmp_ge_i32 s43, 2\n"
"s_cselect_b32 s79, s79, s78\n"
"v_add_u32 v5, s79, v151\n"
"s_lshl_b32 s79, s43, 10\n"
"s_add_u32 s79, s79, 0xfffffc00\n"
"s_lshl_b32 s78, s43, 10\n"
"s_add_u32 s78, s78, 0x4c00\n"
"s_cmp_ge_i32 s43, 1\n"
"s_cselect_b32 s79, s79, s78\n"
"v_add_u32 v6, s79, v151\n"
"s_waitcnt lgkmcnt(0)\n"
"s_mul_i32 s16, s16, 0x2c9277b5\n"
"s_add_u32 s16, s16, 0xac564b05\n"
"v_mov_b32 v82, s16\n"
"s_waitcnt vmcnt(0)\n"
"s_mov_b64 exec, s[58:59]\n"
"buffer_store_dword v82, v80, s[4:7], s32 offen sc1\n"
"s_mov_b64 exec, -1\n"
"s_setprio 1\n"
"s_add_u32 m0, s61, 0\n"
"s_nop 0\n"
"buffer_load_dwordx4 v2, s[8:11], s60 offen offset:0 lds\n"
"s_add_u32 m0, s61, 0x3c0\n"
"s_nop 0\n"
"buffer_load_dwordx4 v2, s[8:11], s60 offen offset:64 lds\n"
"s_add_u32 m0, s61, 0x780\n"
"s_nop 0\n"
"buffer_load_dwordx4 v2, s[8:11], s60 offen offset:128 lds\n"
"s_add_u32 m0, s61, 0xb40\n"
"s_nop 0\n"
"buffer_load_dwordx4 v2, s[8:11], s60 offen offset:192 lds\n"
"s_add_u32 m0, s61, 0xf00\n"
"s_nop 0\n"
"buffer_load_dwordx4 v2, s[8:11], s60 offen offset:256 lds\n"
"s_add_u32 m0, s63, 0\n"
"s_nop 0\n"
"buffer_load_dwordx4 v2, s[8:11], s62 offen offset:0 lds\n"
"s_add_u32 m0, s63, 0x3c0\n"
"s_nop 0\n"
"buffer_load_dwordx4 v2, s[8:11], s62 offen offset:64 lds\n"
"s_add_u32 m0, s63, 0x780\n"
"s_nop 0\n"
"buffer_load_dwordx4 v2, s[8:11], s62 offen offset:128 lds\n"
"s_add_u32 m0, s63, 0xb40\n"
"s_nop 0\n"
"buffer_load_dwordx4 v2, s[8:11], s62 offen offset:192 lds\n"
"s_add_u32 m0, s63, 0xf00\n"
"s_nop 0\n"
"buffer_load_dwordx4 v2, s[8:11], s62 offen offset:256 lds\n"
"s_add_u32 m0, s65, 0\n"
"s_nop 0\n"
"buffer_load_dwordx4 v2, s[8:11], s64 offen offset:0 lds\n"
"s_add_u32 m0, s65, 0x3c0\n"
"s_nop 0\n"
"buffer_load_dwordx4 v2, s[8:11], s64 offen offset:64 lds\n"
"s_add_u32 m0, s65, 0x780\n"
"s_nop 0\n"
"buffer_load_dwordx4 v2, s[8:11], s64 offen offset:128 lds\n"
"s_add_u32 m0, s65, 0xb40\n"
"s_nop 0\n"
"buffer_load_dwordx4 v2, s[8:11], s64 offen offset:192 lds\n"
"s_add_u32 m0, s65, 0xf00\n"
"s_nop 0\n"
"buffer_load_dwordx4 v2, s[8:11], s64 offen offset:256 lds\n"
"s_add_u32 m0, s67, 0\n"
"s_nop 0\n"
"buffer_load_dwordx4 v2, s[8:11], s66 offen offset:0 lds\n"
"s_add_u32 m0, s67, 0x3c0\n"
"s_nop 0\n"
"buffer_load_dwordx4 v2, s[8:11], s66 offen offset:64 lds\n"
"s_add_u32 m0, s67, 0x780\n"
"s_nop 0\n"
"buffer_load_dwordx4 v2, s[8:11], s66 offen offset:128 lds\n"
"s_add_u32 m0, s67, 0xb40\n"
"s_nop 0\n"
"buffer_load_dwordx4 v2, s[8:11], s66 offen offset:192 lds\n"
"s_add_u32 m0, s67, 0xf00\n"
"s_nop 0\n"
"buffer_load_dwordx4 v2, s[8:11], s66 offen offset:256 lds\n"
"s_add_u32 m0, s69, 0\n"
"s_nop 0\n"
"buffer_load_dwordx4 v2, s[8:11], s68 offen offset:0 lds\n"
"s_add_u32 m0, s69, 0x3c0\n"
"s_nop 0\n"
"buffer_load_dwordx4 v2, s[8:11], s68 offen offset:64 lds\n"
"s_add_u32 m0, s69, 0x780\n"
"s_nop 0\n"
"buffer_load_dwordx4 v2, s[8:11], s68 offen offset:128 lds\n"
"s_add_u32 m0, s69, 0xb40\n"
"s_nop 0\n"
"buffer_load_dwordx4 v2, s[8:11], s68 offen offset:192 lds\n"
"s_add_u32 m0, s69, 0xf00\n"
"s_nop 0\n"
"buffer_load_dwordx4 v2, s[8:11], s68 offen offset:256 lds\n"
"buffer_load_dwordx4 v[96:99], v19, s[4:7], s50 offen sc1\n"
"s_waitcnt vmcnt(1)\n"
"ds_write_b128 v93, v[20:23] offset:40960\n"
"ds_write_b32 v13, v13 offset:0\n"
"v_add_u32 v88, 0xa000, v88\n"
"ds_read_b128 v[68:71], v88 offset:0\n"
"ds_read_b128 v[72:75], v88 offset:1024\n"
"ds_read_b128 v[76:79], v88 offset:2048\n"
"ds_read_b128 v[80:83], v88 offset:3072\n"
"s_waitcnt vmcnt(0)\n"
"v_xor_b32 v96, s16, v96\n"
"v_xor_b32 v97, s16, v97\n"
"v_xor_b32 v98, s16, v98\n"
"v_xor_b32 v99, s16, v99\n"
"v_or_b32 v96, v96, v97\n"
"v_or3_b32 v96, v96, v98, v99\n"
"v_cmp_ne_u32 vcc, 0, v96\n"
"s_cbranch_vccnz .Lwt_11\n"
"buffer_load_dwordx4 v[96:99], v1, s[4:7], s52 offen offset:0 sc1\n"
"s_waitcnt lgkmcnt(3)\n"
"v_mfma_f32_16x16x32_bf16 v[28:31], v[68:71], v[68:71], 0\n"
"ds_read_b128 v[84:87], v88 offset:4096\n"
"v_mfma_f32_16x16x32_bf16 v[32:35], v[20:23], v[68:71], 0\n"
"s_waitcnt lgkmcnt(3)\n"
"v_mfma_f32_16x16x32_bf16 v[28:31], v[72:75], v[72:75], v[28:31]\n"
"buffer_load_dwordx4 v[100:103], v1, s[4:7], s52 offen offset:1024 sc1\n"
"ds_read_b128 v[68:71], v88 offset:20480\n"
"v_mfma_f32_16x16x32_bf16 v[32:35], v[20:23], v[72:75], v[32:35]\n"
"s_waitcnt lgkmcnt(3)\n"
"v_mfma_f32_16x16x32_bf16 v[28:31], v[76:79], v[76:79], v[28:31]\n"
"ds_read_b128 v[72:75], v88 offset:21504\n"
"v_mfma_f32_16x16x32_bf16 v[32:35], v[20:23], v[76:79], v[32:35]\n"
"buffer_load_dwordx4 v[104:107], v1, s[4:7], s52 offen offset:2048 sc1\n"
"s_waitcnt lgkmcnt(3)\n"
"v_mfma_f32_16x16x32_bf16 v[28:31], v[80:83], v[80:83], v[28:31]\n"
"ds_read_b128 v[76:79], v88 offset:22528\n"
"v_mfma_f32_16x16x32_bf16 v[32:35], v[20:23], v[80:83], v[32:35]\n"
"s_waitcnt lgkmcnt(3)\n"
"v_mfma_f32_16x16x32_bf16 v[28:31], v[84:87], v[84:87], v[28:31]\n"
"ds_read_b128 v[80:83], v88 offset:23552\n"
"buffer_load_dwordx4 v[108:111], v1, s[4:7], s52 offen offset:3072 sc1\n"
"v_mfma_f32_16x16x32_bf16 v[32:35], v[20:23], v[84:87], v[32:35]\n"
"s_waitcnt lgkmcnt(3)\n"
"v_mfma_f32_16x16x32_bf16 v[36:39], v[68:71], v[68:71], 0\n"
"ds_read_b128 v[84:87], v88 offset:24576\n"
"v_mfma_f32_16x16x32_bf16 v[40:43], v[20:23], v[68:71], 0\n"
"s_waitcnt lgkmcnt(3)\n"
"v_mfma_f32_16x16x32_bf16 v[36:39], v[72:75], v[72:75], v[36:39]\n"
"s_add_u32 s52, s52, 4096\n"
"buffer_load_dwordx4 v[112:115], v1, s[4:7], s52 offen offset:0 sc1\n"
"v_add_u32 v88, 0x12400, v88\n"
"ds_read_b128 v[68:71], v88 offset:0\n"
"v_mfma_f32_16x16x32_bf16 v[40:43], v[20:23], v[72:75], v[40:43]\n"
"ds_write_b128 v92, v[28:31] offset:0\n"
"buffer_load_dwordx4 v[116:119], v1, s[4:7], s52 offen offset:1024 sc1\n"
"s_waitcnt lgkmcnt(4)\n"
"v_mfma_f32_16x16x32_bf16 v[36:39], v[76:79], v[76:79], v[36:39]\n"
"ds_read_b128 v[72:75], v88 offset:1024\n"
"v_mfma_f32_16x16x32_bf16 v[40:43], v[20:23], v[76:79], v[40:43]\n"
"ds_write_b32 v91, v32 offset:0\n"
"s_waitcnt lgkmcnt(5)\n"
"v_mfma_f32_16x16x32_bf16 v[36:39], v[80:83], v[80:83], v[36:39]\n"
"buffer_load_dwordx4 v[120:123], v1, s[4:7], s52 offen offset:2048 sc1\n"
"ds_read_b128 v[76:79], v88 offset:2048\n"
"v_mfma_f32_16x16x32_bf16 v[40:43], v[20:23], v[80:83], v[40:43]\n"
"s_waitcnt lgkmcnt(5)\n"
"v_mfma_f32_16x16x32_bf16 v[36:39], v[84:87], v[84:87], v[36:39]\n"
"ds_read_b128 v[80:83], v88 offset:3072\n"
"v_mfma_f32_16x16x32_bf16 v[40:43], v[20:23], v[84:87], v[40:43]\n"
"buffer_load_dwordx4 v[124:127], v1, s[4:7], s52 offen offset:3072 sc1\n"
"s_waitcnt lgkmcnt(5)\n"
"v_mfma_f32_16x16x32_bf16 v[44:47], v[68:71], v[68:71], 0\n"
"ds_read_b128 v[84:87], v88 offset:4096\n"
"v_mfma_f32_16x16x32_bf16 v[48:51], v[20:23], v[68:71], 0\n"
"s_waitcnt lgkmcnt(4)\n"
"v_mfma_f32_16x16x32_bf16 v[44:47], v[72:75], v[72:75], v[44:47]\n"
"ds_read_b128 v[68:71], v88 offset:20480\n"
"s_add_u32 s52, s52, 4096\n"
"buffer_load_dwordx4 v[128:131], v1, s[4:7], s52 offen offset:0 sc1\n"
"v_mfma_f32_16x16x32_bf16 v[48:51], v[20:23], v[72:75], v[48:51]\n"
"ds_write_b128 v92, v[36:39] offset:256\n"
"s_waitcnt lgkmcnt(4)\n"
"v_mfma_f32_16x16x32_bf16 v[44:47], v[76:79], v[76:79], v[44:47]\n"
"ds_read_b128 v[72:75], v88 offset:21504\n"
"buffer_load_dwordx4 v[132:135], v1, s[4:7], s52 offen offset:1024 sc1\n"
"v_mfma_f32_16x16x32_bf16 v[48:51], v[20:23], v[76:79], v[48:51]\n"
"ds_write_b32 v91, v40 offset:256\n"
"s_waitcnt lgkmcnt(5)\n"
"v_mfma_f32_16x16x32_bf16 v[44:47], v[80:83], v[80:83], v[44:47]\n"
"ds_read_b128 v[76:79], v88 offset:22528\n"
"v_mfma_f32_16x16x32_bf16 v[48:51], v[20:23], v[80:83], v[48:51]\n"
"buffer_load_dwordx4 v[136:139], v1, s[4:7], s52 offen offset:2048 sc1\n"
"s_waitcnt lgkmcnt(5)\n"
"v_mfma_f32_16x16x32_bf16 v[44:47], v[84:87], v[84:87], v[44:47]\n"
"ds_read_b128 v[80:83], v88 offset:23552\n"
"v_mfma_f32_16x16x32_bf16 v[48:51], v[20:23], v[84:87], v[48:51]\n"
"s_waitcnt lgkmcnt(5)\n"
"v_mfma_f32_16x16x32_bf16 v[52:55], v[68:71], v[68:71], 0\n"
"ds_read_b128 v[84:87], v88 offset:24576\n"
"buffer_load_dwordx4 v[140:143], v1, s[4:7], s52 offen offset:3072 sc1\n"
"v_mfma_f32_16x16x32_bf16 v[56:59], v[20:23], v[68:71], 0\n"
"s_waitcnt lgkmcnt(4)\n"
"v_mfma_f32_16x16x32_bf16 v[52:55], v[72:75], v[72:75], v[52:55]\n"
"v_add_u32 v88, 0xfffe3c00, v88\n"
"ds_read_b128 v[68:71], v88 offset:0\n"
"v_mfma_f32_16x16x32_bf16 v[56:59], v[20:23], v[72:75], v[56:59]\n"
"s_add_u32 s52, s52, 4096\n"
"buffer_load_dwordx4 v[144:147], v1, s[4:7], s52 offen offset:0 sc1\n"
"ds_write_b128 v92, v[44:47] offset:512\n"
"s_waitcnt lgkmcnt(4)\n"
"v_mfma_f32_16x16x32_bf16 v[52:55], v[76:79], v[76:79], v[52:55]\n"
"ds_read_b128 v[72:75], v88 offset:1024\n"
"v_mfma_f32_16x16x32_bf16 v[56:59], v[20:23], v[76:79], v[56:59]\n"
"buffer_load_dwordx4 v[148:151], v1, s[4:7], s52 offen offset:1024 sc1\n"
"ds_write_b32 v91, v48 offset:512\n"
"ds_read_b32 v90, v14 offset:0\n"
"s_waitcnt lgkmcnt(6)\n"
"v_mfma_f32_16x16x32_bf16 v[52:55], v[80:83], v[80:83], v[52:55]\n"
"ds_read_b128 v[76:79], v88 offset:2048\n"
"v_mfma_f32_16x16x32_bf16 v[56:59], v[20:23], v[80:83], v[56:59]\n"
"buffer_load_dwordx4 v[152:155], v1, s[4:7], s52 offen offset:2048 sc1\n"
"s_waitcnt lgkmcnt(6)\n"
"v_mfma_f32_16x16x32_bf16 v[52:55], v[84:87], v[84:87], v[52:55]\n"
"ds_read_b128 v[80:83], v88 offset:3072\n"
"v_mfma_f32_16x16x32_bf16 v[56:59], v[20:23], v[84:87], v[56:59]\n"
"s_waitcnt lgkmcnt(6)\n"
"v_mfma_f32_16x16x32_bf16 v[60:63], v[68:71], v[68:71], 0\n"
"ds_read_b128 v[84:87], v88 offset:4096\n"
"buffer_load_dwordx4 v[156:159], v1, s[4:7], s52 offen offset:3072 sc1\n"
"v_mfma_f32_16x16x32_bf16 v[64:67], v[20:23], v[68:71], 0\n"
"s_waitcnt lgkmcnt(3)\n"
"v_cmp_eq_u32 vcc, 0, v90\n"
"v_mfma_f32_16x16x32_bf16 v[60:63], v[72:75], v[72:75], v[60:63]\n"
"v_mfma_f32_16x16x32_bf16 v[64:67], v[20:23], v[72:75], v[64:67]\n"
"ds_write_b128 v92, v[52:55] offset:768\n"
"s_cbranch_vccnz .Lsl_7\n"
".Lbk_8:\n"
"s_waitcnt lgkmcnt(3)\n"
"v_mfma_f32_16x16x32_bf16 v[60:63], v[76:79], v[76:79], v[60:63]\n"
"v_add_u32 v3, 0x5000, v3\n"
"v_mfma_f32_16x16x32_bf16 v[64:67], v[20:23], v[76:79], v[64:67]\n"
"ds_read_b128 v[68:71], v3 offset:20480\n"
"ds_write_b32 v91, v56 offset:768\n"
"ds_read_b128 v[72:75], v3 offset:40960\n"
"s_waitcnt lgkmcnt(5)\n"
"v_mfma_f32_16x16x32_bf16 v[60:63], v[80:83], v[80:83], v[60:63]\n"
"v_add_u32 v95, 0x17400, v3\n"
"v_mfma_f32_16x16x32_bf16 v[64:67], v[20:23], v[80:83], v[64:67]\n"
"ds_read_b128 v[76:79], v95 offset:0\n"
"s_waitcnt lgkmcnt(5)\n"
"v_mfma_f32_16x16x32_bf16 v[60:63], v[84:87], v[84:87], v[60:63]\n"
"ds_read_b128 v[80:83], v95 offset:20480\n"
"v_mfma_f32_16x16x32_bf16 v[64:67], v[20:23], v[84:87], v[64:67]\n"
"s_nop 4\n"
"ds_write_b128 v92, v[60:63] offset:1024\n"
"s_nop 1\n"
"ds_write_b32 v91, v64 offset:1024\n"
"s_branch .Ljn_12\n"
".Lwt_11:\n"
"s_waitcnt lgkmcnt(0)\n"
"v_mfma_f32_16x16x32_bf16 v[28:31], v[68:71], v[68:71], 0\n"
"ds_read_b128 v[84:87], v88 offset:4096\n"
"v_mfma_f32_16x16x32_bf16 v[32:35], v[20:23], v[68:71], 0\n"
"v_mfma_f32_16x16x32_bf16 v[28:31], v[72:75], v[72:75], v[28:31]\n"
"ds_read_b128 v[68:71], v88 offset:20480\n"
"v_mfma_f32_16x16x32_bf16 v[32:35], v[20:23], v[72:75], v[32:35]\n"
"v_mfma_f32_16x16x32_bf16 v[28:31], v[76:79], v[76:79], v[28:31]\n"
"ds_read_b128 v[72:75], v88 offset:21504\n"
"v_mfma_f32_16x16x32_bf16 v[32:35], v[20:23], v[76:79], v[32:35]\n"
"v_mfma_f32_16x16x32_bf16 v[28:31], v[80:83], v[80:83], v[28:31]\n"
"ds_read_b128 v[76:79], v88 offset:22528\n"
"v_mfma_f32_16x16x32_bf16 v[32:35], v[20:23], v[80:83], v[32:35]\n"
"s_waitcnt lgkmcnt(3)\n"
"v_mfma_f32_16x16x32_bf16 v[28:31], v[84:87], v[84:87], v[28:31]\n"
"ds_read_b128 v[80:83], v88 offset:23552\n"
"v_mfma_f32_16x16x32_bf16 v[32:35], v[20:23], v[84:87], v[32:35]\n"
"s_waitcnt lgkmcnt(3)\n"
"v_mfma_f32_16x16x32_bf16 v[36:39], v[68:71], v[68:71], 0\n"
"ds_read_b128 v[84:87], v88 offset:24576\n"
"v_mfma_f32_16x16x32_bf16 v[40:43], v[20:23], v[68:71], 0\n"
"s_waitcnt lgkmcnt(3)\n"
"v_mfma_f32_16x16x32_bf16 v[36:39], v[72:75], v[72:75], v[36:39]\n"
"v_add_u32 v88, 0x12400, v88\n"
"ds_read_b128 v[68:71], v88 offset:0\n"
"v_mfma_f32_16x16x32_bf16 v[40:43], v[20:23], v[72:75], v[40:43]\n"
"ds_write_b128 v92, v[28:31] offset:0\n"
"s_waitcnt lgkmcnt(4)\n"
"v_mfma_f32_16x16x32_bf16 v[36:39], v[76:79], v[76:79], v[36:39]\n"
"ds_read_b128 v[72:75], v88 offset:1024\n"
"v_mfma_f32_16x16x32_bf16 v[40:43], v[20:23], v[76:79], v[40:43]\n"
"ds_write_b32 v91, v32 offset:0\n"
"s_waitcnt lgkmcnt(5)\n"
"v_mfma_f32_16x16x32_bf16 v[36:39], v[80:83], v[80:83], v[36:39]\n"
"ds_read_b128 v[76:79], v88 offset:2048\n"
"v_mfma_f32_16x16x32_bf16 v[40:43], v[20:23], v[80:83], v[40:43]\n"
"s_waitcnt lgkmcnt(5)\n"
"v_mfma_f32_16x16x32_bf16 v[36:39], v[84:87], v[84:87], v[36:39]\n"
"ds_read_b128 v[80:83], v88 offset:3072\n"
"v_mfma_f32_16x16x32_bf16 v[40:43], v[20:23], v[84:87], v[40:43]\n"
"s_waitcnt lgkmcnt(5)\n"
"v_mfma_f32_16x16x32_bf16 v[44:47], v[68:71], v[68:71], 0\n"
"ds_read_b128 v[84:87], v88 offset:4096\n"
"v_mfma_f32_16x16x32_bf16 v[48:51], v[20:23], v[68:71], 0\n"
"s_waitcnt lgkmcnt(4)\n"
"v_mfma_f32_16x16x32_bf16 v[44:47], v[72:75], v[72:75], v[44:47]\n"
"ds_read_b128 v[68:71], v88 offset:20480\n"
"v_mfma_f32_16x16x32_bf16 v[48:51], v[20:23], v[72:75], v[48:51]\n"
"ds_write_b128 v92, v[36:39] offset:256\n"
"s_waitcnt lgkmcnt(4)\n"
"v_mfma_f32_16x16x32_bf16 v[44:47], v[76:79], v[76:79], v[44:47]\n"
"ds_read_b128 v[72:75], v88 offset:21504\n"
"v_mfma_f32_16x16x32_bf16 v[48:51], v[20:23], v[76:79], v[48:51]\n"
"ds_write_b32 v91, v40 offset:256\n"
"s_waitcnt lgkmcnt(5)\n"
"v_mfma_f32_16x16x32_bf16 v[44:47], v[80:83], v[80:83], v[44:47]\n"
"ds_read_b128 v[76:79], v88 offset:22528\n"
"v_mfma_f32_16x16x32_bf16 v[48:51], v[20:23], v[80:83], v[48:51]\n"
"s_waitcnt lgkmcnt(5)\n"
"v_mfma_f32_16x16x32_bf16 v[44:47], v[84:87], v[84:87], v[44:47]\n"
"ds_read_b128 v[80:83], v88 offset:23552\n"
"v_mfma_f32_16x16x32_bf16 v[48:51], v[20:23], v[84:87], v[48:51]\n"
"s_waitcnt lgkmcnt(5)\n"
"v_mfma_f32_16x16x32_bf16 v[52:55], v[68:71], v[68:71], 0\n"
"ds_read_b128 v[84:87], v88 offset:24576\n"
"v_mfma_f32_16x16x32_bf16 v[56:59], v[20:23], v[68:71], 0\n"
"s_waitcnt lgkmcnt(4)\n"
"v_mfma_f32_16x16x32_bf16 v[52:55], v[72:75], v[72:75], v[52:55]\n"
"v_add_u32 v88, 0xfffe3c00, v88\n"
"ds_read_b128 v[68:71], v88 offset:0\n"
"v_mfma_f32_16x16x32_bf16 v[56:59], v[20:23], v[72:75], v[56:59]\n"
"ds_write_b128 v92, v[44:47] offset:512\n"
"s_waitcnt lgkmcnt(4)\n"
"v_mfma_f32_16x16x32_bf16 v[52:55], v[76:79], v[76:79], v[52:55]\n"
"ds_read_b128 v[72:75], v88 offset:1024\n"
"v_mfma_f32_16x16x32_bf16 v[56:59], v[20:23], v[76:79], v[56:59]\n"
"ds_write_b32 v91, v48 offset:512\n"
"ds_read_b32 v90, v14 offset:0\n"
"s_waitcnt lgkmcnt(6)\n"
"v_mfma_f32_16x16x32_bf16 v[52:55], v[80:83], v[80:83], v[52:55]\n"
"ds_read_b128 v[76:79], v88 offset:2048\n"
"v_mfma_f32_16x16x32_bf16 v[56:59], v[20:23], v[80:83], v[56:59]\n"
"s_waitcnt lgkmcnt(6)\n"
"v_mfma_f32_16x16x32_bf16 v[52:55], v[84:87], v[84:87], v[52:55]\n"
"ds_read_b128 v[80:83], v88 offset:3072\n"
"v_mfma_f32_16x16x32_bf16 v[56:59], v[20:23], v[84:87], v[56:59]\n"
"s_waitcnt lgkmcnt(6)\n"
"v_mfma_f32_16x16x32_bf16 v[60:63], v[68:71], v[68:71], 0\n"
"ds_read_b128 v[84:87], v88 offset:4096\n"
"v_mfma_f32_16x16x32_bf16 v[64:67], v[20:23], v[68:71], 0\n"
"s_waitcnt lgkmcnt(3)\n"
"v_cmp_eq_u32 vcc, 0, v90\n"
"v_mfma_f32_16x16x32_bf16 v[60:63], v[72:75], v[72:75], v[60:63]\n"
"v_mfma_f32_16x16x32_bf16 v[64:67], v[20:23], v[72:75], v[64:67]\n"
"ds_write_b128 v92, v[52:55] offset:768\n"
"s_waitcnt lgkmcnt(3)\n"
"v_mfma_f32_16x16x32_bf16 v[60:63], v[76:79], v[76:79], v[60:63]\n"
"v_mfma_f32_16x16x32_bf16 v[64:67], v[20:23], v[76:79], v[64:67]\n"
"ds_write_b32 v91, v56 offset:768\n"
"s_waitcnt lgkmcnt(3)\n"
"v_mfma_f32_16x16x32_bf16 v[60:63], v[80:83], v[80:83], v[60:63]\n"
"v_mfma_f32_16x16x32_bf16 v[64:67], v[20:23], v[80:83], v[64:67]\n"
"s_waitcnt lgkmcnt(2)\n"
"v_mfma_f32_16x16x32_bf16 v[60:63], v[84:87], v[84:87], v[60:63]\n"
"v_mfma_f32_16x16x32_bf16 v[64:67], v[20:23], v[84:87], v[64:67]\n"
"buffer_load_dwordx4 v[96:99], v19, s[4:7], s50 offen sc1\n"
"s_mov_b32 s17, 0x100000\n"
".Ltp_13:\n"
"ds_read_b32 v90, v14 offset:0\n"
"s_sub_u32 s17, s17, 1\n"
"s_cbranch_scc1 .Lou_14\n"
"s_waitcnt lgkmcnt(0)\n"
"v_cmp_eq_u32 vcc, 0, v90\n"
"s_cbranch_vccnz .Ltp_13\n"
".Lou_14:\n"
"s_waitcnt lgkmcnt(0)\n"
"v_add_u32 v3, 0x5000, v3\n"
"ds_read_b128 v[68:71], v3 offset:20480\n"
"ds_read_b128 v[72:75], v3 offset:40960\n"
"v_add_u32 v95, 0x17400, v3\n"
"ds_read_b128 v[76:79], v95 offset:0\n"
"ds_read_b128 v[80:83], v95 offset:20480\n"
"ds_write_b128 v92, v[60:63] offset:1024\n"
"ds_write_b32 v91, v64 offset:1024\n"
"s_waitcnt vmcnt(0) lgkmcnt(2)\n"
"s_mov_b32 s17, 32768\n"
"s_branch .Lmd_17\n"
".Ltp_15:\n"
"buffer_load_dwordx4 v[96:99], v19, s[4:7], s50 offen sc1\n"
".Lmd_17:\n"
"s_sub_u32 s17, s17, 1\n"
"s_cbranch_scc1 .Lou_16\n"
"s_waitcnt vmcnt(0)\n"
"v_xor_b32 v96, s16, v96\n"
"v_xor_b32 v97, s16, v97\n"
"v_xor_b32 v98, s16, v98\n"
"v_xor_b32 v99, s16, v99\n"
"v_or_b32 v96, v96, v97\n"
"v_or3_b32 v96, v96, v98, v99\n"
"v_cmp_ne_u32 vcc, 0, v96\n"
"s_cbranch_vccz .Lou_16\n"
"s_sleep 1\n"
"s_branch .Ltp_15\n"
".Lou_16:\n"
"s_waitcnt vmcnt(0)\n"
"buffer_load_dwordx4 v[96:99], v1, s[4:7], s52 offen offset:0 sc1\n"
"buffer_load_dwordx4 v[100:103], v1, s[4:7], s52 offen offset:1024 sc1\n"
"buffer_load_dwordx4 v[104:107], v1, s[4:7], s52 offen offset:2048 sc1\n"
"buffer_load_dwordx4 v[108:111], v1, s[4:7], s52 offen offset:3072 sc1\n"
"s_add_u32 s52, s52, 4096\n"
"buffer_load_dwordx4 v[112:115], v1, s[4:7], s52 offen offset:0 sc1\n"
"buffer_load_dwordx4 v[116:119], v1, s[4:7], s52 offen offset:1024 sc1\n"
"buffer_load_dwordx4 v[120:123], v1, s[4:7], s52 offen offset:2048 sc1\n"
"buffer_load_dwordx4 v[124:127], v1, s[4:7], s52 offen offset:3072 sc1\n"
"s_add_u32 s52, s52, 4096\n"
"buffer_load_dwordx4 v[128:131], v1, s[4:7], s52 offen offset:0 sc1\n"
"buffer_load_dwordx4 v[132:135], v1, s[4:7], s52 offen offset:1024 sc1\n"
"buffer_load_dwordx4 v[136:139], v1, s[4:7], s52 offen offset:2048 sc1\n"
"buffer_load_dwordx4 v[140:143], v1, s[4:7], s52 offen offset:3072 sc1\n"
"s_add_u32 s52, s52, 4096\n"
"buffer_load_dwordx4 v[144:147], v1, s[4:7], s52 offen offset:0 sc1\n"
"buffer_load_dwordx4 v[148:151], v1, s[4:7], s52 offen offset:1024 sc1\n"
"buffer_load_dwordx4 v[152:155], v1, s[4:7], s52 offen offset:2048 sc1\n"
"buffer_load_dwordx4 v[156:159], v1, s[4:7], s52 offen offset:3072 sc1\n"
".Ljn_12:\n"
"s_waitcnt lgkmcnt(2)\n"
"ds_write_b32 v13, v13 offset:16\n"
"s_waitcnt vmcnt(14)\n"
"v_mfma_f32_16x16x32_bf16 v[28:31], v[96:99], v[68:71], 0\n"
"ds_read_b128 v[84:87], v3 offset:21504\n"
"s_add_u32 s52, s52, 4096\n"
"v_mfma_f32_16x16x32_bf16 v[32:35], v[100:103], v[68:71], 0\n"
"v_mfma_f32_16x16x32_bf16 v[36:39], v[96:99], v[72:75], 0\n"
"ds_read_b128 v[68:71], v3 offset:41984\n"
"buffer_load_dwordx4 v[160:163], v1, s[4:7], s52 offen offset:0 sc1\n"
"v_mfma_f32_16x16x32_bf16 v[40:43], v[100:103], v[72:75], 0\n"
"v_mfma_f32_16x16x32_bf16 v[44:47], v[96:99], v[76:79], 0\n"
"ds_read_b128 v[72:75], v95 offset:1024\n"
"v_mfma_f32_16x16x32_bf16 v[48:51], v[100:103], v[76:79], 0\n"
"buffer_load_dwordx4 v[164:167], v1, s[4:7], s52 offen offset:1024 sc1\n"
"v_mfma_f32_16x16x32_bf16 v[52:55], v[96:99], v[80:83], 0\n"
"ds_read_b128 v[76:79], v95 offset:21504\n"
"v_mfma_f32_16x16x32_bf16 v[56:59], v[100:103], v[80:83], 0\n"
"s_waitcnt vmcnt(14) lgkmcnt(2)\n"
"v_mfma_f32_16x16x32_bf16 v[28:31], v[104:107], v[84:87], v[28:31]\n"
"ds_read_b128 v[80:83], v3 offset:22528\n"
"buffer_load_dwordx4 v[168:171], v1, s[4:7], s52 offen offset:2048 sc1\n"
"v_mfma_f32_16x16x32_bf16 v[32:35], v[108:111], v[84:87], v[32:35]\n"
"v_mfma_f32_16x16x32_bf16 v[36:39], v[104:107], v[68:71], v[36:39]\n"
"ds_read_b128 v[84:87], v3 offset:43008\n"
"v_mfma_f32_16x16x32_bf16 v[40:43], v[108:111], v[68:71], v[40:43]\n"
"buffer_load_dwordx4 v[172:175], v1, s[4:7], s52 offen offset:3072 sc1\n"
"s_waitcnt lgkmcnt(2)\n"
"v_mfma_f32_16x16x32_bf16 v[44:47], v[104:107], v[72:75], v[44:47]\n"
"ds_read_b128 v[68:71], v95 offset:2048\n"
"s_add_u32 s52, s52, 4096\n"
"v_mfma_f32_16x16x32_bf16 v[48:51], v[108:111], v[72:75], v[48:51]\n"
"v_mfma_f32_16x16x32_bf16 v[52:55], v[104:107], v[76:79], v[52:55]\n"
"ds_read_b128 v[72:75], v95 offset:22528\n"
"buffer_load_dwordx4 v[176:179], v1, s[4:7], s52 offen offset:0 sc1\n"
"v_mfma_f32_16x16x32_bf16 v[56:59], v[108:111], v[76:79], v[56:59]\n"
"s_waitcnt vmcnt(15) lgkmcnt(2)\n"
"v_mfma_f32_16x16x32_bf16 v[28:31], v[112:115], v[80:83], v[28:31]\n"
"ds_read_b128 v[76:79], v3 offset:23552\n"
"v_mfma_f32_16x16x32_bf16 v[32:35], v[116:119], v[80:83], v[32:35]\n"
"buffer_load_dwordx4 v[180:183], v1, s[4:7], s52 offen offset:1024 sc1\n"
"v_mfma_f32_16x16x32_bf16 v[36:39], v[112:115], v[84:87], v[36:39]\n"
"ds_read_b128 v[80:83], v3 offset:44032\n"
"v_mfma_f32_16x16x32_bf16 v[40:43], v[116:119], v[84:87], v[40:43]\n"
"s_waitcnt lgkmcnt(2)\n"
"v_mfma_f32_16x16x32_bf16 v[44:47], v[112:115], v[68:71], v[44:47]\n"
"ds_read_b128 v[84:87], v95 offset:3072\n"
"buffer_load_dwordx4 v[184:187], v1, s[4:7], s52 offen offset:2048 sc1\n"
"v_mfma_f32_16x16x32_bf16 v[48:51], v[116:119], v[68:71], v[48:51]\n"
"ds_read_b32 v90, v14 offset:16\n"
"v_mfma_f32_16x16x32_bf16 v[52:55], v[112:115], v[72:75], v[52:55]\n"
"ds_read_b128 v[68:71], v95 offset:23552\n"
"v_mfma_f32_16x16x32_bf16 v[56:59], v[116:119], v[72:75], v[56:59]\n"
"buffer_load_dwordx4 v[188:191], v1, s[4:7], s52 offen offset:3072 sc1\n"
"s_waitcnt vmcnt(16) lgkmcnt(3)\n"
"v_mfma_f32_16x16x32_bf16 v[28:31], v[120:123], v[76:79], v[28:31]\n"
"ds_read_b128 v[72:75], v3 offset:24576\n"
"s_add_u32 s52, s52, 4096\n"
"v_mfma_f32_16x16x32_bf16 v[32:35], v[124:127], v[76:79], v[32:35]\n"
"v_mfma_f32_16x16x32_bf16 v[36:39], v[120:123], v[80:83], v[36:39]\n"
"ds_read_b128 v[76:79], v3 offset:45056\n"
"buffer_load_dwordx4 v[192:195], v1, s[4:7], s52 offen offset:0 sc1\n"
"v_mfma_f32_16x16x32_bf16 v[40:43], v[124:127], v[80:83], v[40:43]\n"
"s_waitcnt lgkmcnt(2)\n"
"v_mfma_f32_16x16x32_bf16 v[44:47], v[120:123], v[84:87], v[44:47]\n"
"ds_read_b128 v[80:83], v95 offset:4096\n"
"v_mfma_f32_16x16x32_bf16 v[48:51], v[124:127], v[84:87], v[48:51]\n"
"buffer_load_dwordx4 v[196:199], v1, s[4:7], s52 offen offset:1024 sc1\n"
"v_mfma_f32_16x16x32_bf16 v[52:55], v[120:123], v[68:71], v[52:55]\n"
"ds_read_b128 v[84:87], v95 offset:24576\n"
"v_mfma_f32_16x16x32_bf16 v[56:59], v[124:127], v[68:71], v[56:59]\n"
"v_cmp_eq_u32 vcc, 0, v90\n"
"s_waitcnt vmcnt(16) lgkmcnt(2)\n"
"v_mfma_f32_16x16x32_bf16 v[28:31], v[128:131], v[72:75], v[28:31]\n"
"ds_read_b128 v[68:71], v3 offset:25600\n"
"buffer_load_dwordx4 v[200:203], v1, s[4:7], s52 offen offset:2048 sc1\n"
"v_mfma_f32_16x16x32_bf16 v[32:35], v[132:135], v[72:75], v[32:35]\n"
"s_cbranch_vccnz .Lsl_22\n"
".Lbk_23:\n"
"v_mfma_f32_16x16x32_bf16 v[36:39], v[128:131], v[76:79], v[36:39]\n"
"ds_read_b128 v[72:75], v3 offset:46080\n"
"v_mfma_f32_16x16x32_bf16 v[40:43], v[132:135], v[76:79], v[40:43]\n"
"buffer_load_dwordx4 v[204:207], v1, s[4:7], s52 offen offset:3072 sc1\n"
"ds_read2st64_b32 v[60:61], v17 offset0:0 offset1:8\n"
"s_waitcnt lgkmcnt(3)\n"
"v_mfma_f32_16x16x32_bf16 v[44:47], v[128:131], v[80:83], v[44:47]\n"
"ds_read_b128 v[76:79], v95 offset:5120\n"
"s_add_u32 s52, s52, 4096\n"
"v_mfma_f32_16x16x32_bf16 v[48:51], v[132:135], v[80:83], v[48:51]\n"
"ds_read2st64_b32 v[62:63], v17 offset0:16 offset1:24\n"
"v_mfma_f32_16x16x32_bf16 v[52:55], v[128:131], v[84:87], v[52:55]\n"
"ds_read_b128 v[80:83], v95 offset:25600\n"
"buffer_load_dwordx4 v[208:211], v1, s[4:7], s52 offen offset:0 sc1\n"
"v_mfma_f32_16x16x32_bf16 v[56:59], v[132:135], v[84:87], v[56:59]\n"
"ds_read2st64_b32 v[64:65], v18 offset0:0 offset1:8\n"
"s_waitcnt vmcnt(17) lgkmcnt(5)\n"
"v_mfma_f32_16x16x32_bf16 v[28:31], v[136:139], v[68:71], v[28:31]\n"
"ds_read_b128 v[84:87], v3 offset:26624\n"
"v_mfma_f32_16x16x32_bf16 v[32:35], v[140:143], v[68:71], v[32:35]\n"
"buffer_load_dwordx4 v[212:215], v1, s[4:7], s52 offen offset:1024 sc1\n"
"v_mov_b32 v20, -1\n"
"v_mov_b32 v21, -1\n"
"ds_read2st64_b32 v[66:67], v18 offset0:16 offset1:24\n"
"v_mfma_f32_16x16x32_bf16 v[36:39], v[136:139], v[72:75], v[36:39]\n"
"ds_read_b128 v[68:71], v3 offset:47104\n"
"v_mov_b32 v22, -1\n"
"v_mov_b32 v23, -1\n"
"v_mfma_f32_16x16x32_bf16 v[40:43], v[140:143], v[72:75], v[40:43]\n"
"v_cndmask_b32 v91, 2.0, v1, s[40:41]\n"
"s_mov_b32 s33, s30\n"
"s_waitcnt lgkmcnt(4)\n"
"v_mfma_f32_16x16x32_bf16 v[44:47], v[136:139], v[76:79], v[44:47]\n"
"ds_read_b128 v[72:75], v95 offset:6144\n"
"buffer_load_dwordx4 v[216:219], v1, s[4:7], s52 offen offset:2048 sc1\n"
"v_mfma_f32_16x16x32_bf16 v[48:51], v[140:143], v[76:79], v[48:51]\n"
"buffer_store_dwordx4 v[20:23], v1, s[4:7], s33 offen offset:0 sc1\n"
"v_mfma_f32_16x16x32_bf16 v[52:55], v[136:139], v[80:83], v[52:55]\n"
"ds_read_b128 v[76:79], v95 offset:26624\n"
"v_mfma_f32_16x16x32_bf16 v[56:59], v[140:143], v[80:83], v[56:59]\n"
"buffer_load_dwordx4 v[220:223], v1, s[4:7], s52 offen offset:3072 sc1\n"
"s_waitcnt vmcnt(19) lgkmcnt(2)\n"
"v_mfma_f32_16x16x32_bf16 v[28:31], v[144:147], v[84:87], v[28:31]\n"
"ds_read_b128 v[80:83], v3 offset:27648\n"
"s_add_u32 s52, s52, 4096\n"
"v_mfma_f32_16x16x32_bf16 v[32:35], v[148:151], v[84:87], v[32:35]\n"
"v_mfma_f32_16x16x32_bf16 v[36:39], v[144:147], v[68:71], v[36:39]\n"
"ds_read_b128 v[84:87], v3 offset:48128\n"
"buffer_load_dwordx4 v[224:227], v1, s[4:7], s52 offen offset:0 sc1\n"
"buffer_store_dwordx4 v[20:23], v91, s[4:7], s33 offen offset:1024 sc1\n"
"v_mfma_f32_16x16x32_bf16 v[40:43], v[148:151], v[68:71], v[40:43]\n"
"s_add_u32 s33, s30, 0x140000\n"
"v_add_f32 v60, v60, v61\n"
"s_waitcnt lgkmcnt(2)\n"
"v_mfma_f32_16x16x32_bf16 v[44:47], v[144:147], v[72:75], v[44:47]\n"
"ds_read_b128 v[68:71], v95 offset:7168\n"
"v_add_f32 v62, v62, v63\n"
"v_add_f32 v64, v64, v65\n"
"v_mfma_f32_16x16x32_bf16 v[48:51], v[148:151], v[72:75], v[48:51]\n"
"buffer_load_dwordx4 v[228:231], v1, s[4:7], s52 offen offset:1024 sc1\n"
"v_add_f32 v66, v66, v67\n"
"v_add_f32 v64, v64, v66\n"
"v_mfma_f32_16x16x32_bf16 v[52:55], v[144:147], v[76:79], v[52:55]\n"
"ds_read_b128 v[72:75], v95 offset:27648\n"
"v_add_f32 v60, v60, v62\n"
"v_mul_f32 v63, 0x3acccccd, v64\n"
"v_mfma_f32_16x16x32_bf16 v[56:59], v[148:151], v[76:79], v[56:59]\n"
"buffer_store_dwordx4 v[20:23], v1, s[4:7], s33 offen offset:0 sc1\n"
"v_fma_f32 v60, -v64, v63, v60\n"
"s_waitcnt vmcnt(21) lgkmcnt(2)\n"
"v_mfma_f32_16x16x32_bf16 v[28:31], v[152:155], v[80:83], v[28:31]\n"
"ds_read_b128 v[76:79], v3 offset:28672\n"
"buffer_load_dwordx4 v[232:235], v1, s[4:7], s53 offen offset:0 sc1\n"
"v_fmamk_f32 v60, v60, 0x3acccccd, v27\n"
"v_mfma_f32_16x16x32_bf16 v[32:35], v[156:159], v[80:83], v[32:35]\n"
"v_max_f32 v60, v60, v27\n"
"v_rsq_f32 v62, v60\n"
"v_mfma_f32_16x16x32_bf16 v[36:39], v[152:155], v[84:87], v[36:39]\n"
"ds_read_b128 v[80:83], v3 offset:49152\n"
"v_mul_f32 v63, -v63, v62\n"
"v_mfma_f32_16x16x32_bf16 v[40:43], v[156:159], v[84:87], v[40:43]\n"
"buffer_load_dwordx4 v[236:239], v1, s[4:7], s53 offen offset:1024 sc1\n"
"s_waitcnt lgkmcnt(2)\n"
"v_mfma_f32_16x16x32_bf16 v[44:47], v[152:155], v[68:71], v[44:47]\n"
"ds_read_b128 v[84:87], v95 offset:8192\n"
"buffer_store_dwordx4 v[20:23], v91, s[4:7], s33 offen offset:1024 sc1\n"
"v_mfma_f32_16x16x32_bf16 v[48:51], v[156:159], v[68:71], v[48:51]\n"
"s_lshl_b32 s79, s2, 2\n"
"s_add_u32 s79, s79, s3\n"
"s_lshl_b32 s79, s79, 10\n"
"v_mfma_f32_16x16x32_bf16 v[52:55], v[152:155], v[72:75], v[52:55]\n"
"ds_read_b128 v[68:71], v95 offset:28672\n"
"buffer_load_dwordx4 v[240:243], v1, s[4:7], s54 offen offset:0 sc1\n"
"s_add_u32 s33, s79, 0xae8000\n"
"v_mfma_f32_16x16x32_bf16 v[56:59], v[156:159], v[72:75], v[56:59]\n"
"s_sub_u32 s79, 0xb47fff, s33\n"
"s_and_b32 s79, s79, 0x80000000\n"
"s_waitcnt vmcnt(23) lgkmcnt(2)\n"
"v_mfma_f32_16x16x32_bf16 v[28:31], v[160:163], v[76:79], v[28:31]\n"
"ds_read_b128 v[72:75], v3 offset:29696\n"
"s_or_b32 s33, s33, s79\n"
"v_mfma_f32_16x16x32_bf16 v[32:35], v[164:167], v[76:79], v[32:35]\n"
"buffer_load_dwordx4 v[244:247], v1, s[4:7], s54 offen offset:1024 sc1\n"
"buffer_store_dwordx4 v[20:23], v1, s[4:7], s33 offen sc1\n"
"v_mfma_f32_16x16x32_bf16 v[36:39], v[160:163], v[80:83], v[36:39]\n"
"ds_read_b128 v[76:79], v3 offset:50176\n"
"v_mfma_f32_16x16x32_bf16 v[40:43], v[164:167], v[80:83], v[40:43]\n"
"ds_write_b64 v16, v[62:63] offset:0\n"
"s_waitcnt lgkmcnt(3)\n"
"v_mfma_f32_16x16x32_bf16 v[44:47], v[160:163], v[84:87], v[44:47]\n"
"ds_read_b128 v[80:83], v95 offset:9216\n"
"buffer_load_dwordx4 v[248:251], v1, s[4:7], s55 offen offset:0 sc1\n"
"v_mfma_f32_16x16x32_bf16 v[48:51], v[164:167], v[84:87], v[48:51]\n"
"v_mfma_f32_16x16x32_bf16 v[52:55], v[160:163], v[68:71], v[52:55]\n"
"ds_read_b128 v[84:87], v95 offset:29696\n"
"v_mfma_f32_16x16x32_bf16 v[56:59], v[164:167], v[68:71], v[56:59]\n"
"buffer_load_dwordx4 v[252:255], v1, s[4:7], s55 offen offset:1024 sc1\n"
"ds_read2st64_b32 v[60:61], v17 offset0:4 offset1:12\n"
"s_waitcnt vmcnt(25) lgkmcnt(4)\n"
"v_mfma_f32_16x16x32_bf16 v[28:31], v[168:171], v[72:75], v[28:31]\n"
"ds_read_b128 v[68:71], v3 offset:30720\n"
"s_mov_b32 m0, 163328\n"
"v_mfma_f32_16x16x32_bf16 v[32:35], v[172:175], v[72:75], v[32:35]\n"
"ds_read2st64_b32 v[62:63], v17 offset0:20 offset1:28\n"
"v_mfma_f32_16x16x32_bf16 v[36:39], v[168:171], v[76:79], v[36:39]\n"
"ds_read_b128 v[72:75], v3 offset:51200\n"
"buffer_load_dword v89, s[8:11], 0 offen lds\n"
"v_mfma_f32_16x16x32_bf16 v[40:43], v[172:175], v[76:79], v[40:43]\n"
"ds_read2st64_b32 v[64:65], v18 offset0:4 offset1:12\n"
"s_waitcnt lgkmcnt(5)\n"
"v_mfma_f32_16x16x32_bf16 v[44:47], v[168:171], v[80:83], v[44:47]\n"
"ds_read_b128 v[76:79], v95 offset:10240\n"
"ds_write_b32 v13, v13 offset:32\n"
"v_mfma_f32_16x16x32_bf16 v[48:51], v[172:175], v[80:83], v[48:51]\n"
"ds_read2st64_b32 v[66:67], v18 offset0:20 offset1:28\n"
"v_mfma_f32_16x16x32_bf16 v[52:55], v[168:171], v[84:87], v[52:55]\n"
"ds_read_b128 v[80:83], v95 offset:30720\n"
"v_mfma_f32_16x16x32_bf16 v[56:59], v[172:175], v[84:87], v[56:59]\n"
"s_waitcnt vmcnt(24) lgkmcnt(5)\n"
"v_mfma_f32_16x16x32_bf16 v[28:31], v[176:179], v[68:71], v[28:31]\n"
"ds_read_b128 v[84:87], v3 offset:31744\n"
"v_mfma_f32_16x16x32_bf16 v[32:35], v[180:183], v[68:71], v[32:35]\n"
"v_mfma_f32_16x16x32_bf16 v[36:39], v[176:179], v[72:75], v[36:39]\n"
"ds_read_b128 v[68:71], v3 offset:52224\n"
"v_mfma_f32_16x16x32_bf16 v[40:43], v[180:183], v[72:75], v[40:43]\n"
"s_waitcnt lgkmcnt(2)\n"
"v_mfma_f32_16x16x32_bf16 v[44:47], v[176:179], v[76:79], v[44:47]\n"
"ds_read_b128 v[72:75], v95 offset:11264\n"
"v_mfma_f32_16x16x32_bf16 v[48:51], v[180:183], v[76:79], v[48:51]\n"
"v_mfma_f32_16x16x32_bf16 v[52:55], v[176:179], v[80:83], v[52:55]\n"
"ds_read_b128 v[76:79], v95 offset:31744\n"
"v_mfma_f32_16x16x32_bf16 v[56:59], v[180:183], v[80:83], v[56:59]\n"
"v_add_f32 v60, v60, v61\n"
"s_waitcnt vmcnt(22) lgkmcnt(2)\n"
"v_mfma_f32_16x16x32_bf16 v[28:31], v[184:187], v[84:87], v[28:31]\n"
"ds_read_b128 v[80:83], v3 offset:32768\n"
"v_add_f32 v62, v62, v63\n"
"v_mfma_f32_16x16x32_bf16 v[32:35], v[188:191], v[84:87], v[32:35]\n"
"v_add_f32 v64, v64, v65\n"
"v_add_f32 v66, v66, v67\n"
"v_mfma_f32_16x16x32_bf16 v[36:39], v[184:187], v[68:71], v[36:39]\n"
"ds_read_b128 v[84:87], v3 offset:53248\n"
"v_add_f32 v64, v64, v66\n"
"v_mfma_f32_16x16x32_bf16 v[40:43], v[188:191], v[68:71], v[40:43]\n"
"v_add_f32 v60, v60, v62\n"
"s_waitcnt lgkmcnt(2)\n"
"v_mfma_f32_16x16x32_bf16 v[44:47], v[184:187], v[72:75], v[44:47]\n"
"ds_read_b128 v[68:71], v95 offset:12288\n"
"v_mul_f32 v63, 0x3acccccd, v64\n"
"v_mfma_f32_16x16x32_bf16 v[48:51], v[188:191], v[72:75], v[48:51]\n"
"v_fma_f32 v60, -v64, v63, v60\n"
"v_fmamk_f32 v60, v60, 0x3acccccd, v27\n"
"v_mfma_f32_16x16x32_bf16 v[52:55], v[184:187], v[76:79], v[52:55]\n"
"ds_read_b128 v[72:75], v95 offset:32768\n"
"v_max_f32 v60, v60, v27\n"
"v_mfma_f32_16x16x32_bf16 v[56:59], v[188:191], v[76:79], v[56:59]\n"
"v_rsq_f32 v62, v60\n"
"s_waitcnt vmcnt(20) lgkmcnt(2)\n"
"v_mfma_f32_16x16x32_bf16 v[28:31], v[192:195], v[80:83], v[28:31]\n"
"ds_read_b128 v[76:79], v3 offset:33792\n"
"v_mul_f32 v63, -v63, v62\n"
"v_mfma_f32_16x16x32_bf16 v[32:35], v[196:199], v[80:83], v[32:35]\n"
"v_cndmask_b32 v16, 2.0, v16, s[40:41]\n"
"v_mfma_f32_16x16x32_bf16 v[36:39], v[192:195], v[84:87], v[36:39]\n"
"ds_read_b128 v[80:83], v3 offset:54272\n"
"v_mfma_f32_16x16x32_bf16 v[40:43], v[196:199], v[84:87], v[40:43]\n"
"s_waitcnt lgkmcnt(2)\n"
"v_mfma_f32_16x16x32_bf16 v[44:47], v[192:195], v[68:71], v[44:47]\n"
"ds_read_b128 v[84:87], v95 offset:13312\n"
"v_mfma_f32_16x16x32_bf16 v[48:51], v[196:199], v[68:71], v[48:51]\n"
"v_mfma_f32_16x16x32_bf16 v[52:55], v[192:195], v[72:75], v[52:55]\n"
"ds_read_b128 v[68:71], v95 offset:33792\n"
"v_mfma_f32_16x16x32_bf16 v[56:59], v[196:199], v[72:75], v[56:59]\n"
"ds_write_b64 v16, v[62:63] offset:512\n"
"s_waitcnt vmcnt(18) lgkmcnt(3)\n"
"v_mfma_f32_16x16x32_bf16 v[28:31], v[200:203], v[76:79], v[28:31]\n"
"ds_read_b128 v[72:75], v3 offset:34816\n"
"v_mfma_f32_16x16x32_bf16 v[32:35], v[204:207], v[76:79], v[32:35]\n"
"ds_write_b32 v13, v13 offset:64\n"
"v_mfma_f32_16x16x32_bf16 v[36:39], v[200:203], v[80:83], v[36:39]\n"
"ds_read_b128 v[76:79], v3 offset:55296\n"
"v_mfma_f32_16x16x32_bf16 v[40:43], v[204:207], v[80:83], v[40:43]\n"
"s_waitcnt lgkmcnt(4)\n"
"v_mfma_f32_16x16x32_bf16 v[44:47], v[200:203], v[84:87], v[44:47]\n"
"ds_read_b128 v[80:83], v95 offset:14336\n"
"v_mfma_f32_16x16x32_bf16 v[48:51], v[204:207], v[84:87], v[48:51]\n"
"v_mfma_f32_16x16x32_bf16 v[52:55], v[200:203], v[68:71], v[52:55]\n"
"ds_read_b128 v[84:87], v95 offset:34816\n"
"v_mfma_f32_16x16x32_bf16 v[56:59], v[204:207], v[68:71], v[56:59]\n"
"s_waitcnt vmcnt(16) lgkmcnt(2)\n"
"v_mfma_f32_16x16x32_bf16 v[28:31], v[208:211], v[72:75], v[28:31]\n"
"ds_read_b128 v[68:71], v3 offset:35840\n"
"v_mfma_f32_16x16x32_bf16 v[32:35], v[212:215], v[72:75], v[32:35]\n"
"v_mfma_f32_16x16x32_bf16 v[36:39], v[208:211], v[76:79], v[36:39]\n"
"ds_read_b128 v[72:75], v3 offset:56320\n"
"v_mfma_f32_16x16x32_bf16 v[40:43], v[212:215], v[76:79], v[40:43]\n"
"s_waitcnt lgkmcnt(2)\n"
"v_mfma_f32_16x16x32_bf16 v[44:47], v[208:211], v[80:83], v[44:47]\n"
"ds_read_b128 v[76:79], v95 offset:15360\n"
"v_mfma_f32_16x16x32_bf16 v[48:51], v[212:215], v[80:83], v[48:51]\n"
"v_mfma_f32_16x16x32_bf16 v[52:55], v[208:211], v[84:87], v[52:55]\n"
"ds_read_b128 v[80:83], v95 offset:35840\n"
"v_mfma_f32_16x16x32_bf16 v[56:59], v[212:215], v[84:87], v[56:59]\n"
"s_waitcnt vmcnt(13) lgkmcnt(2)\n"
"v_mfma_f32_16x16x32_bf16 v[28:31], v[216:219], v[68:71], v[28:31]\n"
"ds_read_b128 v[84:87], v3 offset:36864\n"
"v_mfma_f32_16x16x32_bf16 v[32:35], v[220:223], v[68:71], v[32:35]\n"
"ds_read_b32 v90, v14 offset:64\n"
"v_mfma_f32_16x16x32_bf16 v[36:39], v[216:219], v[72:75], v[36:39]\n"
"ds_read_b128 v[68:71], v4 offset:40960\n"
"v_mfma_f32_16x16x32_bf16 v[40:43], v[220:223], v[72:75], v[40:43]\n"
"s_waitcnt lgkmcnt(3)\n"
"v_mfma_f32_16x16x32_bf16 v[44:47], v[216:219], v[76:79], v[44:47]\n"
"ds_read_b128 v[72:75], v5 offset:40960\n"
"v_mfma_f32_16x16x32_bf16 v[48:51], v[220:223], v[76:79], v[48:51]\n"
"ds_read_b64 v[88:89], v94 offset:0\n"
"v_mfma_f32_16x16x32_bf16 v[52:55], v[216:219], v[80:83], v[52:55]\n"
"ds_read_b128 v[76:79], v6 offset:40960\n"
"v_mfma_f32_16x16x32_bf16 v[56:59], v[220:223], v[80:83], v[56:59]\n"
"s_waitcnt vmcnt(7) lgkmcnt(3)\n"
"v_mfma_f32_16x16x32_bf16 v[28:31], v[224:227], v[84:87], v[28:31]\n"
"ds_read_b128 v[80:83], v3 offset:57344\n"
"v_mfma_f32_16x16x32_bf16 v[32:35], v[228:231], v[84:87], v[32:35]\n"
"ds_read_b64 v[92:93], v94 offset:128\n"
"v_mfma_f32_16x16x32_bf16 v[28:31], v[232:235], v[68:71], v[28:31]\n"
"ds_read_b128 v[84:87], v4 offset:61440\n"
"v_mfma_f32_16x16x32_bf16 v[32:35], v[236:239], v[68:71], v[32:35]\n"
"v_cmp_eq_u32 vcc, 0, v90\n"
"s_waitcnt vmcnt(1) lgkmcnt(3)\n"
"v_mfma_f32_16x16x32_bf16 v[28:31], v[240:243], v[72:75], v[28:31]\n"
"ds_read_b128 v[68:71], v5 offset:61440\n"
"v_mfma_f32_16x16x32_bf16 v[32:35], v[244:247], v[72:75], v[32:35]\n"
"s_cbranch_vccnz .Lsl_26\n"
".Lbk_27:\n"
"v_mfma_f32_16x16x32_bf16 v[28:31], v[248:251], v[76:79], v[28:31]\n"
"ds_read_b128 v[72:75], v6 offset:61440\n"
"v_mfma_f32_16x16x32_bf16 v[32:35], v[252:255], v[76:79], v[32:35]\n"
"s_waitcnt lgkmcnt(2)\n"
"v_mfma_f32_16x16x32_bf16 v[36:39], v[224:227], v[80:83], v[36:39]\n"
"ds_read_b128 v[76:79], v95 offset:16384\n"
"v_mfma_f32_16x16x32_bf16 v[40:43], v[228:231], v[80:83], v[40:43]\n"
"v_mov_b32_dpp v16, v28 row_newbcast:0 row_mask:0xf bank_mask:0xf\n"
"v_mov_b32_dpp v17, v29 row_newbcast:0 row_mask:0xf bank_mask:0xf\n"
"v_mov_b32_dpp v18, v30 row_newbcast:0 row_mask:0xf bank_mask:0xf\n"
"v_mfma_f32_16x16x32_bf16 v[36:39], v[232:235], v[84:87], v[36:39]\n"
"v_add_u32 v4, 0x1c400, v4\n"
"ds_read_b128 v[80:83], v4 offset:0\n"
"v_mov_b32_dpp v19, v31 row_newbcast:0 row_mask:0xf bank_mask:0xf\n"
"v_mfma_f32_16x16x32_bf16 v[40:43], v[236:239], v[84:87], v[40:43]\n"
"v_mov_b32_dpp v20, v32 row_newbcast:0 row_mask:0xf bank_mask:0xf\n"
"v_mov_b32_dpp v21, v33 row_newbcast:0 row_mask:0xf bank_mask:0xf\n"
"s_waitcnt lgkmcnt(2)\n"
"v_mfma_f32_16x16x32_bf16 v[36:39], v[240:243], v[68:71], v[36:39]\n"
"v_add_u32 v5, 0x1c400, v5\n"
"ds_read_b128 v[84:87], v5 offset:0\n"
"v_mfma_f32_16x16x32_bf16 v[40:43], v[244:247], v[68:71], v[40:43]\n"
"v_mov_b32_dpp v22, v34 row_newbcast:0 row_mask:0xf bank_mask:0xf\n"
"v_mov_b32_dpp v23, v35 row_newbcast:0 row_mask:0xf bank_mask:0xf\n"
"v_mul_f32 v28, v88, v28\n"
"v_mfma_f32_16x16x32_bf16 v[36:39], v[248:251], v[72:75], v[36:39]\n"
"v_add_u32 v6, 0x1c400, v6\n"
"ds_read_b128 v[68:71], v6 offset:0\n"
"v_mul_f32 v29, v88, v29\n"
"v_mfma_f32_16x16x32_bf16 v[40:43], v[252:255], v[72:75], v[40:43]\n"
"v_mul_f32 v30, v88, v30\n"
"v_mul_f32 v31, v88, v31\n"
"s_waitcnt lgkmcnt(2)\n"
"v_mfma_f32_16x16x32_bf16 v[44:47], v[224:227], v[76:79], v[44:47]\n"
"ds_read_b128 v[72:75], v95 offset:36864\n"
"v_fmac_f32 v28, v89, v16\n"
"v_fmac_f32 v29, v89, v17\n"
"v_mfma_f32_16x16x32_bf16 v[48:51], v[228:231], v[76:79], v[48:51]\n"
"ds_write_b32 v13, v13 offset:48\n"
"v_mfma_f32_16x16x32_bf16 v[44:47], v[232:235], v[80:83], v[44:47]\n"
"ds_read_b128 v[76:79], v4 offset:20480\n"
"v_fmac_f32 v30, v89, v18\n"
"v_fmac_f32 v31, v89, v19\n"
"v_mfma_f32_16x16x32_bf16 v[48:51], v[236:239], v[80:83], v[48:51]\n"
"v_cvt_pk_bf16_f32 v28, v28, v29\n"
"v_cvt_pk_bf16_f32 v29, v30, v31\n"
"s_waitcnt lgkmcnt(3)\n"
"v_mfma_f32_16x16x32_bf16 v[44:47], v[240:243], v[84:87], v[44:47]\n"
"ds_read_b128 v[80:83], v5 offset:20480\n"
"ds_write_b64 v7, v[28:29] offset:0\n"
"v_mfma_f32_16x16x32_bf16 v[48:51], v[244:247], v[84:87], v[48:51]\n"
"v_mul_f32 v32, v88, v32\n"
"v_mul_f32 v33, v88, v33\n"
"v_mul_f32 v34, v88, v34\n"
"v_mfma_f32_16x16x32_bf16 v[44:47], v[248:251], v[68:71], v[44:47]\n"
"ds_read_b128 v[84:87], v6 offset:20480\n"
"v_mul_f32 v35, v88, v35\n"
"v_fmac_f32 v32, v89, v20\n"
"v_mfma_f32_16x16x32_bf16 v[48:51], v[252:255], v[68:71], v[48:51]\n"
"v_fmac_f32 v33, v89, v21\n"
"v_fmac_f32 v34, v89, v22\n"
"s_waitcnt lgkmcnt(3)\n"
"v_mfma_f32_16x16x32_bf16 v[52:55], v[224:227], v[72:75], v[52:55]\n"
"v_add_u32 v3, 0xffffb000, v3\n"
"ds_read_b128 v[68:71], v3 offset:0\n"
"v_fmac_f32 v35, v89, v23\n"
"v_mfma_f32_16x16x32_bf16 v[56:59], v[228:231], v[72:75], v[56:59]\n"
"v_cvt_pk_bf16_f32 v32, v32, v33\n"
"v_cvt_pk_bf16_f32 v33, v34, v35\n"
"ds_write_b64 v8, v[32:33] offset:0\n"
"v_mfma_f32_16x16x32_bf16 v[52:55], v[232:235], v[76:79], v[52:55]\n"
"ds_read_b128 v[72:75], v3 offset:1024\n"
"ds_read_b64 v[88:89], v94 offset:256\n"
"v_mul_f32 v36, v92, v36\n"
"v_mfma_f32_16x16x32_bf16 v[56:59], v[236:239], v[76:79], v[56:59]\n"
"ds_read_b32 v90, v14 offset:48\n"
"v_mul_f32 v37, v92, v37\n"
"s_waitcnt lgkmcnt(5)\n"
"v_mfma_f32_16x16x32_bf16 v[52:55], v[240:243], v[80:83], v[52:55]\n"
"ds_read_b128 v[76:79], v3 offset:2048\n"
"v_mul_f32 v38, v92, v38\n"
"v_mul_f32 v39, v92, v39\n"
"v_mfma_f32_16x16x32_bf16 v[56:59], v[244:247], v[80:83], v[56:59]\n"
"v_fmac_f32 v36, v93, v16\n"
"v_fmac_f32 v37, v93, v17\n"
"v_fmac_f32 v38, v93, v18\n"
"v_mfma_f32_16x16x32_bf16 v[52:55], v[248:251], v[84:87], v[52:55]\n"
"ds_read_b128 v[80:83], v3 offset:3072\n"
"v_fmac_f32 v39, v93, v19\n"
"v_cvt_pk_bf16_f32 v36, v36, v37\n"
"v_mfma_f32_16x16x32_bf16 v[56:59], v[252:255], v[84:87], v[56:59]\n"
"v_cvt_pk_bf16_f32 v37, v38, v39\n"
"ds_write_b64 v7, v[36:37] offset:3968\n"
"s_waitcnt lgkmcnt(5)\n"
"v_mfma_f32_16x16x32_bf16 v[60:63], v[96:99], v[68:71], 0\n"
"ds_read_b128 v[84:87], v3 offset:4096\n"
"v_mul_f32 v40, v92, v40\n"
"v_mul_f32 v41, v92, v41\n"
"v_mfma_f32_16x16x32_bf16 v[64:67], v[100:103], v[68:71], 0\n"
"v_mul_f32 v42, v92, v42\n"
"v_mul_f32 v43, v92, v43\n"
"v_fmac_f32 v40, v93, v20\n"
"v_mfma_f32_16x16x32_bf16 v[60:63], v[104:107], v[72:75], v[60:63]\n"
"ds_read_b128 v[68:71], v3 offset:5120\n"
"v_fmac_f32 v41, v93, v21\n"
"v_fmac_f32 v42, v93, v22\n"
"v_mfma_f32_16x16x32_bf16 v[64:67], v[108:111], v[72:75], v[64:67]\n"
"s_waitcnt lgkmcnt(5)\n"
"v_cmp_eq_u32 vcc, 0, v90\n"
"v_fmac_f32 v43, v93, v23\n"
"s_waitcnt lgkmcnt(3)\n"
"v_mfma_f32_16x16x32_bf16 v[60:63], v[112:115], v[76:79], v[60:63]\n"
"ds_read_b128 v[72:75], v3 offset:6144\n"
"v_cvt_pk_bf16_f32 v40, v40, v41\n"
"v_cvt_pk_bf16_f32 v41, v42, v43\n"
"v_mfma_f32_16x16x32_bf16 v[64:67], v[116:119], v[76:79], v[64:67]\n"
"s_cbranch_vccnz .Lsl_18\n"
".Lbk_19:\n"
"ds_write_b64 v8, v[40:41] offset:3968\n"
"v_mfma_f32_16x16x32_bf16 v[60:63], v[120:123], v[80:83], v[60:63]\n"
"ds_read_b128 v[76:79], v3 offset:7168\n"
"v_lshrrev_b32 v95, 3, v0\n"
"ds_read_b64 v[92:93], v94 offset:384\n"
"v_mfma_f32_16x16x32_bf16 v[64:67], v[124:127], v[80:83], v[64:67]\n"
"v_add_u32 v95, s3, v95\n"
"v_mul_f32 v44, v88, v44\n"
"s_waitcnt lgkmcnt(4)\n"
"v_mfma_f32_16x16x32_bf16 v[60:63], v[128:131], v[84:87], v[60:63]\n"
"ds_read_b128 v[80:83], v3 offset:8192\n"
"v_lshlrev_b32 v91, 27, v95\n"
"v_mul_f32 v45, v88, v45\n"
"v_mfma_f32_16x16x32_bf16 v[64:67], v[132:135], v[84:87], v[64:67]\n"
"v_lshl_add_u32 v91, v0, 12, v91\n"
"v_mov_b32 v95, s16\n"
"v_mul_f32 v46, v88, v46\n"
"v_mfma_f32_16x16x32_bf16 v[60:63], v[136:139], v[68:71], v[60:63]\n"
"ds_read_b128 v[84:87], v3 offset:9216\n"
"s_lshl_b32 s79, s2, 2\n"
"v_mul_f32 v47, v88, v47\n"
"v_mfma_f32_16x16x32_bf16 v[64:67], v[140:143], v[68:71], v[64:67]\n"
"s_add_u32 s32, s79, 0x11000\n"
"v_fmac_f32 v44, v89, v16\n"
"s_waitcnt lgkmcnt(3)\n"
"v_mfma_f32_16x16x32_bf16 v[60:63], v[144:147], v[72:75], v[60:63]\n"
"ds_read_b128 v[68:71], v3 offset:10240\n"
"buffer_store_dword v95, v91, s[4:7], s32 offen sc1\n"
"v_fmac_f32 v45, v89, v17\n"
"v_mfma_f32_16x16x32_bf16 v[64:67], v[148:151], v[72:75], v[64:67]\n"
"v_fmac_f32 v46, v89, v18\n"
"v_fmac_f32 v47, v89, v19\n"
"v_cvt_pk_bf16_f32 v44, v44, v45\n"
"v_mfma_f32_16x16x32_bf16 v[60:63], v[152:155], v[76:79], v[60:63]\n"
"ds_read_b128 v[72:75], v3 offset:11264\n"
"v_cvt_pk_bf16_f32 v45, v46, v47\n"
"ds_write_b64 v7, v[44:45] offset:7936\n"
"v_mfma_f32_16x16x32_bf16 v[64:67], v[156:159], v[76:79], v[64:67]\n"
"v_mul_f32 v48, v88, v48\n"
"v_mul_f32 v49, v88, v49\n"
"s_waitcnt lgkmcnt(3)\n"
"v_mfma_f32_16x16x32_bf16 v[60:63], v[160:163], v[80:83], v[60:63]\n"
"ds_read_b128 v[76:79], v3 offset:12288\n"
"v_mul_f32 v50, v88, v50\n"
"v_mul_f32 v51, v88, v51\n"
"v_mfma_f32_16x16x32_bf16 v[64:67], v[164:167], v[80:83], v[64:67]\n"
"v_fmac_f32 v48, v89, v20\n"
"v_fmac_f32 v49, v89, v21\n"
"v_fmac_f32 v50, v89, v22\n"
"v_mfma_f32_16x16x32_bf16 v[60:63], v[168:171], v[84:87], v[60:63]\n"
"ds_read_b128 v[80:83], v3 offset:13312\n"
"v_fmac_f32 v51, v89, v23\n"
"v_cvt_pk_bf16_f32 v48, v48, v49\n"
"v_mfma_f32_16x16x32_bf16 v[64:67], v[172:175], v[84:87], v[64:67]\n"
"v_cvt_pk_bf16_f32 v49, v50, v51\n"
"s_waitcnt lgkmcnt(3)\n"
"v_mfma_f32_16x16x32_bf16 v[60:63], v[176:179], v[68:71], v[60:63]\n"
"ds_read_b128 v[84:87], v3 offset:14336\n"
"ds_write_b64 v8, v[48:49] offset:7936\n"
"v_mfma_f32_16x16x32_bf16 v[64:67], v[180:183], v[68:71], v[64:67]\n"
"ds_read_b64 v[88:89], v94 offset:512\n"
"v_mul_f32 v52, v92, v52\n"
"v_mul_f32 v53, v92, v53\n"
"v_mfma_f32_16x16x32_bf16 v[60:63], v[184:187], v[72:75], v[60:63]\n"
"ds_read_b128 v[68:71], v3 offset:15360\n"
"v_mul_f32 v54, v92, v54\n"
"v_mfma_f32_16x16x32_bf16 v[64:67], v[188:191], v[72:75], v[64:67]\n"
"v_mul_f32 v55, v92, v55\n"
"s_waitcnt lgkmcnt(4)\n"
"v_mfma_f32_16x16x32_bf16 v[60:63], v[192:195], v[76:79], v[60:63]\n"
"ds_read_b128 v[72:75], v3 offset:16384\n"
"v_fmac_f32 v52, v93, v16\n"
"v_mfma_f32_16x16x32_bf16 v[64:67], v[196:199], v[76:79], v[64:67]\n"
"v_fmac_f32 v53, v93, v17\n"
"v_fmac_f32 v54, v93, v18\n"
"v_mfma_f32_16x16x32_bf16 v[60:63], v[200:203], v[80:83], v[60:63]\n"
"v_add_u32 v4, 0xfffe3c00, v4\n"
"ds_read_b128 v[76:79], v4 offset:0\n"
"v_mfma_f32_16x16x32_bf16 v[64:67], v[204:207], v[80:83], v[64:67]\n"
"v_fmac_f32 v55, v93, v19\n"
"s_waitcnt lgkmcnt(2)\n"
"v_mfma_f32_16x16x32_bf16 v[60:63], v[208:211], v[84:87], v[60:63]\n"
"v_add_u32 v5, 0xfffe3c00, v5\n"
"ds_read_b128 v[80:83], v5 offset:0\n"
"v_mfma_f32_16x16x32_bf16 v[64:67], v[212:215], v[84:87], v[64:67]\n"
"v_cvt_pk_bf16_f32 v52, v52, v53\n"
"v_cvt_pk_bf16_f32 v53, v54, v55\n"
"v_mfma_f32_16x16x32_bf16 v[60:63], v[216:219], v[68:71], v[60:63]\n"
"v_add_u32 v6, 0xfffe3c00, v6\n"
"ds_read_b128 v[84:87], v6 offset:0\n"
"v_mfma_f32_16x16x32_bf16 v[64:67], v[220:223], v[68:71], v[64:67]\n"
"ds_write_b64 v7, v[52:53] offset:11904\n"
"s_waitcnt lgkmcnt(3)\n"
"v_mfma_f32_16x16x32_bf16 v[60:63], v[224:227], v[72:75], v[60:63]\n"
"v_mul_f32 v56, v92, v56\n"
"v_mul_f32 v57, v92, v57\n"
"v_mfma_f32_16x16x32_bf16 v[64:67], v[228:231], v[72:75], v[64:67]\n"
"v_mul_f32 v58, v92, v58\n"
"v_mul_f32 v59, v92, v59\n"
"v_mfma_f32_16x16x32_bf16 v[60:63], v[232:235], v[76:79], v[60:63]\n"
"v_fmac_f32 v56, v93, v20\n"
"v_fmac_f32 v57, v93, v21\n"
"v_mfma_f32_16x16x32_bf16 v[64:67], v[236:239], v[76:79], v[64:67]\n"
"v_fmac_f32 v58, v93, v22\n"
"s_waitcnt lgkmcnt(1)\n"
"v_mfma_f32_16x16x32_bf16 v[60:63], v[240:243], v[80:83], v[60:63]\n"
"v_fmac_f32 v59, v93, v23\n"
"v_cvt_pk_bf16_f32 v56, v56, v57\n"
"v_mfma_f32_16x16x32_bf16 v[64:67], v[244:247], v[80:83], v[64:67]\n"
"v_cvt_pk_bf16_f32 v57, v58, v59\n"
"ds_write_b64 v8, v[56:57] offset:11904\n"
"v_mfma_f32_16x16x32_bf16 v[60:63], v[248:251], v[84:87], v[60:63]\n"
"v_mfma_f32_16x16x32_bf16 v[64:67], v[252:255], v[84:87], v[64:67]\n"
"s_nop 4\n"
"v_mul_f32 v60, v88, v60\n"
"v_mul_f32 v61, v88, v61\n"
"v_mul_f32 v62, v88, v62\n"
"v_mul_f32 v63, v88, v63\n"
"v_fmac_f32 v60, v89, v16\n"
"v_fmac_f32 v61, v89, v17\n"
"v_fmac_f32 v62, v89, v18\n"
"v_fmac_f32 v63, v89, v19\n"
"v_cvt_pk_bf16_f32 v60, v60, v61\n"
"v_cvt_pk_bf16_f32 v61, v62, v63\n"
"ds_write_b64 v7, v[60:61] offset:15872\n"
"v_mul_f32 v64, v88, v64\n"
"v_mul_f32 v65, v88, v65\n"
"v_mul_f32 v66, v88, v66\n"
"v_mul_f32 v67, v88, v67\n"
"v_fmac_f32 v64, v89, v20\n"
"v_fmac_f32 v65, v89, v21\n"
"v_fmac_f32 v66, v89, v22\n"
"v_fmac_f32 v67, v89, v23\n"
"v_cvt_pk_bf16_f32 v64, v64, v65\n"
"v_cvt_pk_bf16_f32 v65, v66, v67\n"
"ds_write_b64 v8, v[64:65] offset:15872\n"
"ds_write_b32 v13, v13 offset:80\n"
"v_mov_b32 v84, s42\n"
"v_mov_b32 v85, s42\n"
"v_mov_b32 v86, s42\n"
"v_mov_b32 v87, s42\n"
"v_mov_b32 v30, 0\n"
"v_mov_b32 v31, 0\n"
"v_mov_b32 v34, 0\n"
"v_mov_b32 v35, 0\n"
"s_mov_b32 s17, 0x100000\n"
".Ltp_30:\n"
"ds_read_b32 v90, v14 offset:80\n"
"s_sub_u32 s17, s17, 1\n"
"s_cbranch_scc1 .Lou_31\n"
"s_waitcnt lgkmcnt(0)\n"
"v_cmp_eq_u32 vcc, 0, v90\n"
"s_cbranch_vccnz .Ltp_30\n"
".Lou_31:\n"
"s_waitcnt lgkmcnt(0)\n"
"s_mov_b64 exec, 0xffff\n"
"ds_read_b64 v[30:31], v24 offset:4000\n"
"ds_read_b64 v[34:35], v24 offset:4040\n"
"s_mov_b64 exec, -1\n"
"ds_read_b64 v[28:29], v25 offset:3968\n"
"ds_read_b64 v[68:69], v25 offset:80\n"
"ds_read_b64 v[70:71], v24 offset:112\n"
"ds_read_b64 v[76:77], v25 offset:4048\n"
"ds_read_b64 v[78:79], v24 offset:4080\n"
"ds_read_b64 v[32:33], v25 offset:4008\n"
"ds_read_b64 v[72:73], v25 offset:120\n"
"ds_read_b64 v[74:75], v24 offset:152\n"
"s_add_u32 s31, s28, 0x140000\n"
"s_add_u32 m0, s29, 0xa000\n"
"s_nop 0\n"
"buffer_load_dwordx4 v2, s[8:11], s31 offen offset:0 lds\n"
"ds_read_b64 v[80:81], v25 offset:4088\n"
"ds_read_b64 v[82:83], v24 offset:4120\n"
"ds_read_b64_tr_b16 v[44:45], v26 offset:0\n"
"ds_read_b64_tr_b16 v[46:47], v26 offset:3968\n"
"ds_read_b64_tr_b16 v[48:49], v26 offset:40\n"
"v_mov_b32 v64, 0x0c0c0c0c\n"
"v_mov_b32 v65, 0xc0c0100\n"
"v_mov_b32 v66, 0x1000c0c\n"
"s_add_u32 m0, s29, 0xa3c0\n"
"s_nop 0\n"
"buffer_load_dwordx4 v2, s[8:11], s31 offen offset:64 lds\n"
"v_cndmask_b32 v65, v65, v66, s[34:35]\n"
"v_cndmask_b32 v36, v65, v64, s[36:37]\n"
"v_cndmask_b32 v38, v64, v65, s[36:37]\n"
"v_mov_b32 v65, 0xc0c0302\n"
"v_mov_b32 v66, 0x3020c0c\n"
"v_cndmask_b32 v65, v65, v66, s[34:35]\n"
"v_cndmask_b32 v37, v65, v64, s[38:39]\n"
"v_cndmask_b32 v39, v64, v65, s[38:39]\n"
"s_add_u32 m0, s29, 0xa780\n"
"s_nop 0\n"
"buffer_load_dwordx4 v2, s[8:11], s31 offen offset:128 lds\n"
"s_waitcnt lgkmcnt(10)\n"
"v_mfma_f32_16x16x32_bf16 v[68:71], v[68:71], v[28:31], v[84:87]\n"
"s_waitcnt lgkmcnt(8)\n"
"v_mfma_f32_16x16x32_bf16 v[76:79], v[76:79], v[28:31], 0\n"
"ds_read_b64_tr_b16 v[50:51], v26 offset:4008\n"
"ds_read_b64_tr_b16 v[52:53], v26 offset:32\n"
"ds_read_b64_tr_b16 v[54:55], v26 offset:4000\n"
"s_waitcnt lgkmcnt(8)\n"
"v_mfma_f32_16x16x32_bf16 v[72:75], v[72:75], v[32:35], v[84:87]\n"
"s_waitcnt lgkmcnt(6)\n"
"v_mfma_f32_16x16x32_bf16 v[80:83], v[80:83], v[32:35], 0\n"
"ds_read_b64_tr_b16 v[56:57], v26 offset:72\n"
"ds_read_b64_tr_b16 v[58:59], v26 offset:4040\n"
"s_add_u32 m0, s29, 0xab40\n"
"s_nop 0\n"
"buffer_load_dwordx4 v2, s[8:11], s31 offen offset:192 lds\n"
"v_mov_b32 v40, 0x3f803f80\n"
"v_mov_b32 v41, 0x3f803f80\n"
"v_mov_b32 v42, 0x3f803f80\n"
"v_mov_b32 v43, 0x3f803f80\n"
"v_cndmask_b32 v60, v68, v69, s[34:35]\n"
"v_cndmask_b32 v84, v76, v77, s[34:35]\n"
"v_cndmask_b32 v60, v60, v84, s[36:37]\n"
"v_cndmask_b32 v61, v70, v71, s[34:35]\n"
"s_add_u32 m0, s29, 0xaf00\n"
"s_nop 0\n"
"buffer_load_dwordx4 v2, s[8:11], s31 offen offset:256 lds\n"
"v_cndmask_b32 v84, v78, v79, s[34:35]\n"
"v_cndmask_b32 v61, v61, v84, s[38:39]\n"
"v_max_f32 v64, v60, v61\n"
"v_max_f32 v66, v60, v61\n"
"v_cndmask_b32 v62, v72, v73, s[34:35]\n"
"v_cndmask_b32 v84, v80, v81, s[34:35]\n"
"v_cndmask_b32 v62, v62, v84, s[36:37]\n"
"v_cndmask_b32 v63, v74, v75, s[34:35]\n"
"v_cndmask_b32 v84, v82, v83, s[34:35]\n"
"s_add_u32 s31, s28, 0x145000\n"
"s_add_u32 m0, s29, 0xf000\n"
"s_nop 0\n"
"buffer_load_dwordx4 v2, s[8:11], s31 offen offset:0 lds\n"
"v_cndmask_b32 v63, v63, v84, s[38:39]\n"
"v_max_f32 v65, v62, v63\n"
"v_max_f32 v67, v62, v63\n"
"v_permlane16_swap_b32 v64, v66\n"
"s_nop 0\n"
"v_permlane16_swap_b32 v65, v67\n"
"v_max_f32 v84, v64, v66\n"
"v_max_f32 v64, v64, v66\n"
"v_max_f32 v85, v65, v67\n"
"s_add_u32 m0, s29, 0xf3c0\n"
"s_nop 0\n"
"buffer_load_dwordx4 v2, s[8:11], s31 offen offset:64 lds\n"
"v_max_f32 v65, v65, v67\n"
"v_permlane32_swap_b32 v64, v84\n"
"s_nop 0\n"
"v_permlane32_swap_b32 v65, v85\n"
"v_max_f32 v64, v64, v84\n"
"v_max_f32 v65, v65, v85\n"
"v_mul_f32 v64, 0xbea52b5b, v64\n"
"v_mul_f32 v65, 0xbea52b5b, v65\n"
"v_fmamk_f32 v60, v60, 0x3ea52b5b, v64\n"
"s_add_u32 m0, s29, 0xf780\n"
"s_nop 0\n"
"buffer_load_dwordx4 v2, s[8:11], s31 offen offset:128 lds\n"
"v_fmamk_f32 v61, v61, 0x3ea52b5b, v64\n"
"v_fmamk_f32 v62, v62, 0x3ea52b5b, v65\n"
"v_fmamk_f32 v63, v63, 0x3ea52b5b, v65\n"
"v_exp_f32 v60, v60\n"
"v_exp_f32 v61, v61\n"
"v_exp_f32 v62, v62\n"
"v_exp_f32 v63, v63\n"
"v_cvt_pk_bf16_f32 v64, v60, v61\n"
"v_cvt_pk_bf16_f32 v65, v62, v63\n"
"s_add_u32 m0, s29, 0xfb40\n"
"s_nop 0\n"
"buffer_load_dwordx4 v2, s[8:11], s31 offen offset:192 lds\n"
"v_perm_b32 v28, v64, v64, v36\n"
"v_perm_b32 v29, v64, v64, v37\n"
"v_perm_b32 v30, v64, v64, v38\n"
"v_perm_b32 v31, v64, v64, v39\n"
"v_perm_b32 v32, v65, v65, v36\n"
"v_perm_b32 v33, v65, v65, v37\n"
"v_perm_b32 v34, v65, v65, v38\n"
"v_perm_b32 v35, v65, v65, v39\n"
"s_add_u32 m0, s29, 0xff00\n"
"s_nop 0\n"
"buffer_load_dwordx4 v2, s[8:11], s31 offen offset:256 lds\n"
"v_mfma_f32_16x16x32_bf16 v[68:71], v[40:43], v[28:31], 0\n"
"v_mfma_f32_16x16x32_bf16 v[72:75], v[40:43], v[32:35], 0\n"
"s_waitcnt lgkmcnt(6)\n"
"v_mfma_f32_16x16x32_bf16 v[44:47], v[44:47], v[28:31], 0\n"
"s_waitcnt lgkmcnt(4)\n"
"v_mfma_f32_16x16x32_bf16 v[48:51], v[48:51], v[32:35], 0\n"
"s_waitcnt lgkmcnt(2)\n"
"v_mfma_f32_16x16x32_bf16 v[52:55], v[52:55], v[28:31], 0\n"
"s_waitcnt lgkmcnt(0)\n"
"v_mfma_f32_16x16x32_bf16 v[56:59], v[56:59], v[32:35], 0\n"
"v_cndmask_b32 v88, 2.0, v1, s[40:41]\n"
"v_rcp_f32 v64, v68\n"
"v_rcp_f32 v65, v72\n"
"s_mov_b32 s31, s30\n"
"v_mul_f32 v44, v64, v44\n"
"v_mul_f32 v45, v64, v45\n"
"v_mul_f32 v46, v64, v46\n"
"v_mul_f32 v47, v64, v47\n"
"v_mul_f32 v48, v65, v48\n"
"v_mul_f32 v49, v65, v49\n"
"v_mul_f32 v50, v65, v50\n"
"v_mul_f32 v51, v65, v51\n"
"v_cvt_pk_bf16_f32 v44, v44, v45\n"
"v_cvt_pk_bf16_f32 v45, v46, v47\n"
"v_cvt_pk_bf16_f32 v46, v48, v49\n"
"v_cvt_pk_bf16_f32 v47, v50, v51\n"
"v_min_u32 v44, -2, v44\n"
"v_min_u32 v45, -2, v45\n"
"v_min_u32 v46, -2, v46\n"
"v_min_u32 v47, -2, v47\n"
"buffer_store_dwordx4 v[44:47], v1, s[4:7], s31 offen offset:0 sc1\n"
"v_mul_f32 v52, v64, v52\n"
"v_mul_f32 v53, v64, v53\n"
"v_mul_f32 v54, v64, v54\n"
"v_mul_f32 v55, v64, v55\n"
"v_mul_f32 v56, v65, v56\n"
"v_mul_f32 v57, v65, v57\n"
"v_mul_f32 v58, v65, v58\n"
"v_mul_f32 v59, v65, v59\n"
"v_cvt_pk_bf16_f32 v52, v52, v53\n"
"v_cvt_pk_bf16_f32 v53, v54, v55\n"
"v_cvt_pk_bf16_f32 v54, v56, v57\n"
"v_cvt_pk_bf16_f32 v55, v58, v59\n"
"v_min_u32 v52, -2, v52\n"
"v_min_u32 v53, -2, v53\n"
"v_min_u32 v54, -2, v54\n"
"v_min_u32 v55, -2, v55\n"
"buffer_store_dwordx4 v[52:55], v88, s[4:7], s31 offen offset:1024 sc1\n"
"ds_write_b32 v13, v13 offset:96\n"
"s_waitcnt vmcnt(2)\n"
"ds_write_b32 v13, v13 offset:112\n"
"s_mul_i32 s79, s43, 0x1400\n"
"v_lshrrev_b32 v79, 2, v0\n"
"v_and_b32 v78, 3, v0\n"
"v_lshlrev_b32 v78, 4, v78\n"
"v_lshl_add_u32 v79, v79, 10, v78\n"
"v_add_u32 v79, s79, v79\n"
"v_mov_b32 v78, 0x40000\n"
"v_cmp_gt_i32 vcc, 20, v0\n"
"s_nop 1\n"
"v_cndmask_b32 v93, v78, v79, vcc\n"
"s_mov_b32 s17, 0x100000\n"
".Ltp_32:\n"
"ds_read_b32 v90, v14 offset:112\n"
"s_sub_u32 s17, s17, 1\n"
"s_cbranch_scc1 .Lou_33\n"
"s_waitcnt lgkmcnt(0)\n"
"v_cmp_eq_u32 vcc, 0, v90\n"
"s_cbranch_vccnz .Ltp_32\n"
".Lou_33:\n"
"s_waitcnt lgkmcnt(0)\n"
"ds_read_b128 v[56:59], v3 offset:40960\n"
"ds_read_b128 v[60:63], v3 offset:41984\n"
"ds_read_b128 v[64:67], v3 offset:43008\n"
"ds_read_b128 v[68:71], v3 offset:44032\n"
"ds_read_b128 v[72:75], v3 offset:45056\n"
"s_waitcnt lgkmcnt(3)\n"
"v_mfma_f32_16x16x32_bf16 v[28:31], v[96:99], v[56:59], 0\n"
"ds_read_b128 v[76:79], v3 offset:46080\n"
"s_add_u32 s31, s28, 0x14a000\n"
"v_mfma_f32_16x16x32_bf16 v[32:35], v[100:103], v[56:59], 0\n"
"s_add_u32 m0, s29, 0\n"
"v_mfma_f32_16x16x32_bf16 v[52:55], v[56:59], v[56:59], 0\n"
"v_mfma_f32_16x16x32_bf16 v[28:31], v[104:107], v[60:63], v[28:31]\n"
"ds_read_b128 v[56:59], v3 offset:47104\n"
"buffer_load_dwordx4 v2, s[8:11], s31 offen offset:0 lds\n"
"v_mfma_f32_16x16x32_bf16 v[32:35], v[108:111], v[60:63], v[32:35]\n"
"s_add_u32 m0, s29, 0x3c0\n"
"s_waitcnt lgkmcnt(3)\n"
"v_mfma_f32_16x16x32_bf16 v[28:31], v[112:115], v[64:67], v[28:31]\n"
"ds_read_b128 v[60:63], v3 offset:48128\n"
"v_mfma_f32_16x16x32_bf16 v[32:35], v[116:119], v[64:67], v[32:35]\n"
"v_mfma_f32_16x16x32_bf16 v[28:31], v[120:123], v[68:71], v[28:31]\n"
"ds_read_b128 v[64:67], v3 offset:49152\n"
"buffer_load_dwordx4 v2, s[8:11], s31 offen offset:64 lds\n"
"v_mfma_f32_16x16x32_bf16 v[32:35], v[124:127], v[68:71], v[32:35]\n"
"s_add_u32 m0, s29, 0x780\n"
"s_waitcnt lgkmcnt(3)\n"
"v_mfma_f32_16x16x32_bf16 v[28:31], v[128:131], v[72:75], v[28:31]\n"
"ds_read_b128 v[68:71], v3 offset:50176\n"
"v_mfma_f32_16x16x32_bf16 v[32:35], v[132:135], v[72:75], v[32:35]\n"
"v_mfma_f32_16x16x32_bf16 v[52:55], v[72:75], v[72:75], v[52:55]\n"
"buffer_load_dwordx4 v2, s[8:11], s31 offen offset:128 lds\n"
"v_mfma_f32_16x16x32_bf16 v[28:31], v[136:139], v[76:79], v[28:31]\n"
"ds_read_b128 v[72:75], v3 offset:51200\n"
"s_add_u32 m0, s29, 0xb40\n"
"v_mfma_f32_16x16x32_bf16 v[32:35], v[140:143], v[76:79], v[32:35]\n"
"s_waitcnt lgkmcnt(3)\n"
"v_mfma_f32_16x16x32_bf16 v[28:31], v[144:147], v[56:59], v[28:31]\n"
"ds_read_b128 v[76:79], v3 offset:52224\n"
"v_mfma_f32_16x16x32_bf16 v[32:35], v[148:151], v[56:59], v[32:35]\n"
"buffer_load_dwordx4 v2, s[8:11], s31 offen offset:192 lds\n"
"v_mfma_f32_16x16x32_bf16 v[28:31], v[152:155], v[60:63], v[28:31]\n"
"ds_read_b128 v[56:59], v3 offset:53248\n"
"s_add_u32 m0, s29, 0xf00\n"
"v_mfma_f32_16x16x32_bf16 v[32:35], v[156:159], v[60:63], v[32:35]\n"
"s_waitcnt lgkmcnt(3)\n"
"v_mfma_f32_16x16x32_bf16 v[28:31], v[160:163], v[64:67], v[28:31]\n"
"ds_read_b128 v[60:63], v3 offset:54272\n"
"v_mfma_f32_16x16x32_bf16 v[32:35], v[164:167], v[64:67], v[32:35]\n"
"buffer_load_dwordx4 v2, s[8:11], s31 offen offset:256 lds\n"
"v_mfma_f32_16x16x32_bf16 v[52:55], v[64:67], v[64:67], v[52:55]\n"
"s_add_u32 s31, s28, 0x14f000\n"
"s_add_u32 m0, s29, 0x5000\n"
"v_mfma_f32_16x16x32_bf16 v[28:31], v[168:171], v[68:71], v[28:31]\n"
"ds_read_b128 v[64:67], v3 offset:55296\n"
"v_mfma_f32_16x16x32_bf16 v[32:35], v[172:175], v[68:71], v[32:35]\n"
"s_waitcnt lgkmcnt(3)\n"
"v_mfma_f32_16x16x32_bf16 v[28:31], v[176:179], v[72:75], v[28:31]\n"
"ds_read_b128 v[68:71], v3 offset:56320\n"
"buffer_load_dwordx4 v2, s[8:11], s31 offen offset:0 lds\n"
"v_mfma_f32_16x16x32_bf16 v[32:35], v[180:183], v[72:75], v[32:35]\n"
"s_add_u32 m0, s29, 0x53c0\n"
"v_mfma_f32_16x16x32_bf16 v[28:31], v[184:187], v[76:79], v[28:31]\n"
"ds_read_b128 v[72:75], v3 offset:57344\n"
"v_mfma_f32_16x16x32_bf16 v[32:35], v[188:191], v[76:79], v[32:35]\n"
"s_waitcnt lgkmcnt(3)\n"
"v_mfma_f32_16x16x32_bf16 v[28:31], v[192:195], v[56:59], v[28:31]\n"
"ds_read_b128 v[76:79], v4 offset:40960\n"
"buffer_load_dwordx4 v2, s[8:11], s31 offen offset:64 lds\n"
"v_mfma_f32_16x16x32_bf16 v[32:35], v[196:199], v[56:59], v[32:35]\n"
"s_add_u32 m0, s29, 0x5780\n"
"v_mfma_f32_16x16x32_bf16 v[52:55], v[56:59], v[56:59], v[52:55]\n"
"v_mfma_f32_16x16x32_bf16 v[28:31], v[200:203], v[60:63], v[28:31]\n"
"ds_read_b128 v[56:59], v5 offset:40960\n"
"v_mfma_f32_16x16x32_bf16 v[32:35], v[204:207], v[60:63], v[32:35]\n"
"buffer_load_dwordx4 v2, s[8:11], s31 offen offset:128 lds\n"
"s_waitcnt lgkmcnt(3)\n"
"v_mfma_f32_16x16x32_bf16 v[28:31], v[208:211], v[64:67], v[28:31]\n"
"ds_read_b128 v[60:63], v6 offset:40960\n"
"s_add_u32 m0, s29, 0x5b40\n"
"v_mfma_f32_16x16x32_bf16 v[32:35], v[212:215], v[64:67], v[32:35]\n"
"v_mfma_f32_16x16x32_bf16 v[28:31], v[216:219], v[68:71], v[28:31]\n"
"v_add_u32 v3, 0x5000, v3\n"
"ds_read_b128 v[64:67], v3 offset:40960\n"
"v_mfma_f32_16x16x32_bf16 v[32:35], v[220:223], v[68:71], v[32:35]\n"
"buffer_load_dwordx4 v2, s[8:11], s31 offen offset:192 lds\n"
"s_waitcnt lgkmcnt(3)\n"
"v_mfma_f32_16x16x32_bf16 v[28:31], v[224:227], v[72:75], v[28:31]\n"
"ds_read_b128 v[68:71], v3 offset:41984\n"
"s_add_u32 m0, s29, 0x5f00\n"
"v_mfma_f32_16x16x32_bf16 v[32:35], v[228:231], v[72:75], v[32:35]\n"
"v_mfma_f32_16x16x32_bf16 v[52:55], v[72:75], v[72:75], v[52:55]\n"
"v_mfma_f32_16x16x32_bf16 v[28:31], v[232:235], v[76:79], v[28:31]\n"
"ds_read_b128 v[72:75], v3 offset:43008\n"
"buffer_load_dwordx4 v2, s[8:11], s31 offen offset:256 lds\n"
"v_mfma_f32_16x16x32_bf16 v[32:35], v[236:239], v[76:79], v[32:35]\n"
"s_waitcnt lgkmcnt(3)\n"
"v_mfma_f32_16x16x32_bf16 v[28:31], v[240:243], v[56:59], v[28:31]\n"
"ds_read_b128 v[76:79], v3 offset:44032\n"
"v_cndmask_b32 v94, v52, v53, s[34:35]\n"
"v_mfma_f32_16x16x32_bf16 v[32:35], v[244:247], v[56:59], v[32:35]\n"
"v_cndmask_b32 v54, v54, v55, s[34:35]\n"
"v_cndmask_b32 v94, v94, v54, s[20:21]\n"
"v_mfma_f32_16x16x32_bf16 v[28:31], v[248:251], v[60:63], v[28:31]\n"
"ds_read_b128 v[56:59], v3 offset:45056\n"
"v_mfma_f32_16x16x32_bf16 v[32:35], v[252:255], v[60:63], v[32:35]\n"
"s_waitcnt lgkmcnt(3)\n"
"v_mfma_f32_16x16x32_bf16 v[36:39], v[96:99], v[64:67], 0\n"
"ds_read_b128 v[60:63], v3 offset:46080\n"
"v_mfma_f32_16x16x32_bf16 v[40:43], v[100:103], v[64:67], 0\n"
"v_cndmask_b32 v94, v28, v94, s[22:23]\n"
"s_waitcnt vmcnt(5)\n"
"v_mfma_f32_16x16x32_bf16 v[52:55], v[64:67], v[64:67], 0\n"
"ds_write_b32 v9, v94 offset:0\n"
"v_mfma_f32_16x16x32_bf16 v[36:39], v[104:107], v[68:71], v[36:39]\n"
"ds_read_b128 v[64:67], v3 offset:47104\n"
"v_mfma_f32_16x16x32_bf16 v[40:43], v[108:111], v[68:71], v[40:43]\n"
"s_waitcnt lgkmcnt(4)\n"
"v_mfma_f32_16x16x32_bf16 v[36:39], v[112:115], v[72:75], v[36:39]\n"
"ds_read_b128 v[68:71], v3 offset:48128\n"
"v_mfma_f32_16x16x32_bf16 v[40:43], v[116:119], v[72:75], v[40:43]\n"
"v_mfma_f32_16x16x32_bf16 v[36:39], v[120:123], v[76:79], v[36:39]\n"
"ds_read_b128 v[72:75], v3 offset:49152\n"
"v_mfma_f32_16x16x32_bf16 v[40:43], v[124:127], v[76:79], v[40:43]\n"
"s_waitcnt lgkmcnt(4)\n"
"v_mfma_f32_16x16x32_bf16 v[36:39], v[128:131], v[56:59], v[36:39]\n"
"ds_read_b128 v[76:79], v3 offset:50176\n"
"v_mfma_f32_16x16x32_bf16 v[40:43], v[132:135], v[56:59], v[40:43]\n"
"v_mfma_f32_16x16x32_bf16 v[52:55], v[56:59], v[56:59], v[52:55]\n"
"ds_read_b128 v[84:87], v11 offset:0\n"
"ds_read_b32 v91, v12 offset:0\n"
"v_mfma_f32_16x16x32_bf16 v[36:39], v[136:139], v[60:63], v[36:39]\n"
"ds_read_b128 v[56:59], v3 offset:51200\n"
"v_mfma_f32_16x16x32_bf16 v[40:43], v[140:143], v[60:63], v[40:43]\n"
"s_waitcnt lgkmcnt(5)\n"
"v_mfma_f32_16x16x32_bf16 v[36:39], v[144:147], v[64:67], v[36:39]\n"
"ds_read_b128 v[60:63], v3 offset:52224\n"
"v_mfma_f32_16x16x32_bf16 v[40:43], v[148:151], v[64:67], v[40:43]\n"
"v_mfma_f32_16x16x32_bf16 v[36:39], v[152:155], v[68:71], v[36:39]\n"
"ds_read_b128 v[64:67], v3 offset:53248\n"
"v_mfma_f32_16x16x32_bf16 v[40:43], v[156:159], v[68:71], v[40:43]\n"
"s_waitcnt lgkmcnt(5)\n"
"v_mfma_f32_16x16x32_bf16 v[36:39], v[160:163], v[72:75], v[36:39]\n"
"ds_read_b128 v[68:71], v3 offset:54272\n"
"s_waitcnt lgkmcnt(5)\n"
"v_add_f32 v84, v84, v85\n"
"v_mfma_f32_16x16x32_bf16 v[40:43], v[164:167], v[72:75], v[40:43]\n"
"v_add_f32 v86, v86, v87\n"
"v_add_f32 v84, v84, v86\n"
"v_mfma_f32_16x16x32_bf16 v[52:55], v[72:75], v[72:75], v[52:55]\n"
"s_waitcnt lgkmcnt(4)\n"
"v_mul_f32 v89, 0x3acccccd, v91\n"
"v_fma_f32 v84, -v91, v89, v84\n"
"v_mfma_f32_16x16x32_bf16 v[36:39], v[168:171], v[76:79], v[36:39]\n"
"ds_read_b128 v[72:75], v3 offset:55296\n"
"v_fmamk_f32 v84, v84, 0x3acccccd, v27\n"
"v_mfma_f32_16x16x32_bf16 v[40:43], v[172:175], v[76:79], v[40:43]\n"
"v_cmp_u_f32 vcc, v84, v84\n"
"s_waitcnt lgkmcnt(3)\n"
"v_mfma_f32_16x16x32_bf16 v[36:39], v[176:179], v[56:59], v[36:39]\n"
"ds_read_b128 v[76:79], v3 offset:56320\n"
"v_max_f32 v84, v84, v27\n"
"v_mfma_f32_16x16x32_bf16 v[40:43], v[180:183], v[56:59], v[40:43]\n"
"v_rsq_f32 v88, v84\n"
"v_mfma_f32_16x16x32_bf16 v[36:39], v[184:187], v[60:63], v[36:39]\n"
"ds_read_b128 v[56:59], v3 offset:57344\n"
"v_mul_f32 v89, -v89, v88\n"
"v_mfma_f32_16x16x32_bf16 v[40:43], v[188:191], v[60:63], v[40:43]\n"
"s_cbranch_vccnz .Lsl_34\n"
".Lbk_35:\n"
"s_waitcnt lgkmcnt(3)\n"
"v_mfma_f32_16x16x32_bf16 v[36:39], v[192:195], v[64:67], v[36:39]\n"
"ds_read_b128 v[60:63], v4 offset:61440\n"
"s_add_u32 s31, s28, 0x13b000\n"
"v_mfma_f32_16x16x32_bf16 v[40:43], v[196:199], v[64:67], v[40:43]\n"
"s_add_u32 m0, s29, 0xa000\n"
"v_mul_f32 v28, v88, v28\n"
"v_mfma_f32_16x16x32_bf16 v[52:55], v[64:67], v[64:67], v[52:55]\n"
"v_mul_f32 v29, v88, v29\n"
"v_mul_f32 v30, v88, v30\n"
"v_mfma_f32_16x16x32_bf16 v[36:39], v[200:203], v[68:71], v[36:39]\n"
"ds_read_b128 v[64:67], v5 offset:61440\n"
"buffer_load_dwordx4 v2, s[8:11], s31 offen offset:0 lds\n"
"v_mfma_f32_16x16x32_bf16 v[40:43], v[204:207], v[68:71], v[40:43]\n"
"s_add_u32 m0, s29, 0xa3c0\n"
"s_waitcnt lgkmcnt(3)\n"
"v_mfma_f32_16x16x32_bf16 v[36:39], v[208:211], v[72:75], v[36:39]\n"
"ds_read_b128 v[68:71], v6 offset:61440\n"
"v_mul_f32 v31, v88, v31\n"
"v_mfma_f32_16x16x32_bf16 v[40:43], v[212:215], v[72:75], v[40:43]\n"
"v_fmac_f32 v28, v89, v16\n"
"v_fmac_f32 v29, v89, v17\n"
"v_mfma_f32_16x16x32_bf16 v[36:39], v[216:219], v[76:79], v[36:39]\n"
"v_add_u32 v3, 0xffffb000, v3\n"
"ds_read_b128 v[72:75], v3 offset:0\n"
"buffer_load_dwordx4 v2, s[8:11], s31 offen offset:64 lds\n"
"v_mfma_f32_16x16x32_bf16 v[40:43], v[220:223], v[76:79], v[40:43]\n"
"s_add_u32 m0, s29, 0xa780\n"
"s_waitcnt lgkmcnt(3)\n"
"v_mfma_f32_16x16x32_bf16 v[36:39], v[224:227], v[56:59], v[36:39]\n"
"ds_read_b128 v[76:79], v3 offset:1024\n"
"v_fmac_f32 v30, v89, v18\n"
"v_mfma_f32_16x16x32_bf16 v[40:43], v[228:231], v[56:59], v[40:43]\n"
"v_fmac_f32 v31, v89, v19\n"
"v_cvt_pk_bf16_f32 v28, v28, v29\n"
"v_mfma_f32_16x16x32_bf16 v[52:55], v[56:59], v[56:59], v[52:55]\n"
"buffer_load_dwordx4 v2, s[8:11], s31 offen offset:128 lds\n"
"v_cvt_pk_bf16_f32 v29, v30, v31\n"
"v_mfma_f32_16x16x32_bf16 v[36:39], v[232:235], v[60:63], v[36:39]\n"
"ds_read_b128 v[56:59], v3 offset:2048\n"
"s_add_u32 m0, s29, 0xab40\n"
"v_mfma_f32_16x16x32_bf16 v[40:43], v[236:239], v[60:63], v[40:43]\n"
"v_mul_f32 v32, v88, v32\n"
"s_waitcnt lgkmcnt(3)\n"
"v_mfma_f32_16x16x32_bf16 v[36:39], v[240:243], v[64:67], v[36:39]\n"
"ds_read_b128 v[60:63], v3 offset:3072\n"
"v_cndmask_b32 v94, v52, v53, s[34:35]\n"
"v_mfma_f32_16x16x32_bf16 v[40:43], v[244:247], v[64:67], v[40:43]\n"
"v_cndmask_b32 v54, v54, v55, s[34:35]\n"
"v_cndmask_b32 v94, v94, v54, s[20:21]\n"
"buffer_load_dwordx4 v2, s[8:11], s31 offen offset:192 lds\n"
"v_mfma_f32_16x16x32_bf16 v[36:39], v[248:251], v[68:71], v[36:39]\n"
"ds_read_b128 v[64:67], v3 offset:4096\n"
"s_add_u32 m0, s29, 0xaf00\n"
"v_mfma_f32_16x16x32_bf16 v[40:43], v[252:255], v[68:71], v[40:43]\n"
"v_mul_f32 v33, v88, v33\n"
"s_waitcnt lgkmcnt(3)\n"
"v_mfma_f32_16x16x32_bf16 v[44:47], v[96:99], v[72:75], 0\n"
"ds_read_b128 v[68:71], v3 offset:5120\n"
"v_mul_f32 v34, v88, v34\n"
"v_mfma_f32_16x16x32_bf16 v[48:51], v[100:103], v[72:75], 0\n"
"v_cndmask_b32 v94, v36, v94, s[22:23]\n"
"s_waitcnt vmcnt(4)\n"
"buffer_load_dwordx4 v2, s[8:11], s31 offen offset:256 lds\n"
"v_mfma_f32_16x16x32_bf16 v[52:55], v[72:75], v[72:75], 0\n"
"ds_write_b32 v9, v94 offset:320\n"
"v_mfma_f32_16x16x32_bf16 v[44:47], v[104:107], v[76:79], v[44:47]\n"
"ds_read_b128 v[72:75], v3 offset:6144\n"
"v_mul_f32 v35, v88, v35\n"
"v_mfma_f32_16x16x32_bf16 v[48:51], v[108:111], v[76:79], v[48:51]\n"
"v_fmac_f32 v32, v89, v20\n"
"s_waitcnt lgkmcnt(4)\n"
"v_mfma_f32_16x16x32_bf16 v[44:47], v[112:115], v[56:59], v[44:47]\n"
"ds_read_b128 v[76:79], v3 offset:7168\n"
"v_fmac_f32 v33, v89, v21\n"
"v_mfma_f32_16x16x32_bf16 v[48:51], v[116:119], v[56:59], v[48:51]\n"
"v_fmac_f32 v34, v89, v22\n"
"v_fmac_f32 v35, v89, v23\n"
"v_mfma_f32_16x16x32_bf16 v[44:47], v[120:123], v[60:63], v[44:47]\n"
"ds_read_b128 v[56:59], v3 offset:8192\n"
"v_cvt_pk_bf16_f32 v32, v32, v33\n"
"v_mfma_f32_16x16x32_bf16 v[48:51], v[124:127], v[60:63], v[48:51]\n"
"v_cvt_pk_bf16_f32 v33, v34, v35\n"
"s_waitcnt lgkmcnt(4)\n"
"v_mfma_f32_16x16x32_bf16 v[44:47], v[128:131], v[64:67], v[44:47]\n"
"ds_read_b128 v[60:63], v3 offset:9216\n"
"v_mfma_f32_16x16x32_bf16 v[48:51], v[132:135], v[64:67], v[48:51]\n"
"v_mfma_f32_16x16x32_bf16 v[52:55], v[64:67], v[64:67], v[52:55]\n"
"ds_read_b128 v[84:87], v11 offset:320\n"
"ds_read_b32 v91, v12 offset:320\n"
"v_mfma_f32_16x16x32_bf16 v[44:47], v[136:139], v[68:71], v[44:47]\n"
"ds_read_b128 v[64:67], v3 offset:10240\n"
"v_mfma_f32_16x16x32_bf16 v[48:51], v[140:143], v[68:71], v[48:51]\n"
"s_waitcnt lgkmcnt(5)\n"
"v_mfma_f32_16x16x32_bf16 v[44:47], v[144:147], v[72:75], v[44:47]\n"
"ds_read_b128 v[68:71], v3 offset:11264\n"
"v_mfma_f32_16x16x32_bf16 v[48:51], v[148:151], v[72:75], v[48:51]\n"
"v_mfma_f32_16x16x32_bf16 v[44:47], v[152:155], v[76:79], v[44:47]\n"
"ds_read_b128 v[72:75], v3 offset:12288\n"
"v_mfma_f32_16x16x32_bf16 v[48:51], v[156:159], v[76:79], v[48:51]\n"
"s_waitcnt lgkmcnt(5)\n"
"v_mfma_f32_16x16x32_bf16 v[44:47], v[160:163], v[56:59], v[44:47]\n"
"ds_read_b128 v[76:79], v3 offset:13312\n"
"s_waitcnt lgkmcnt(5)\n"
"v_add_f32 v84, v84, v85\n"
"v_mfma_f32_16x16x32_bf16 v[48:51], v[164:167], v[56:59], v[48:51]\n"
"v_add_f32 v86, v86, v87\n"
"v_add_f32 v84, v84, v86\n"
"v_mfma_f32_16x16x32_bf16 v[52:55], v[56:59], v[56:59], v[52:55]\n"
"s_waitcnt lgkmcnt(4)\n"
"v_mul_f32 v89, 0x3acccccd, v91\n"
"v_fma_f32 v84, -v91, v89, v84\n"
"v_mfma_f32_16x16x32_bf16 v[44:47], v[168:171], v[60:63], v[44:47]\n"
"ds_read_b128 v[56:59], v3 offset:14336\n"
"v_fmamk_f32 v84, v84, 0x3acccccd, v27\n"
"v_mfma_f32_16x16x32_bf16 v[48:51], v[172:175], v[60:63], v[48:51]\n"
"v_cmp_u_f32 vcc, v84, v84\n"
"s_waitcnt lgkmcnt(3)\n"
"v_mfma_f32_16x16x32_bf16 v[44:47], v[176:179], v[64:67], v[44:47]\n"
"ds_read_b128 v[60:63], v3 offset:15360\n"
"v_max_f32 v84, v84, v27\n"
"v_mfma_f32_16x16x32_bf16 v[48:51], v[180:183], v[64:67], v[48:51]\n"
"v_rsq_f32 v88, v84\n"
"v_mfma_f32_16x16x32_bf16 v[44:47], v[184:187], v[68:71], v[44:47]\n"
"ds_read_b128 v[64:67], v3 offset:16384\n"
"v_mul_f32 v89, -v89, v88\n"
"v_mfma_f32_16x16x32_bf16 v[48:51], v[188:191], v[68:71], v[48:51]\n"
"s_cbranch_vccnz .Lsl_38\n"
".Lbk_39:\n"
"s_waitcnt lgkmcnt(3)\n"
"v_mfma_f32_16x16x32_bf16 v[44:47], v[192:195], v[72:75], v[44:47]\n"
"ds_read_b128 v[68:71], v4 offset:0\n"
"v_mul_f32 v36, v88, v36\n"
"v_mfma_f32_16x16x32_bf16 v[48:51], v[196:199], v[72:75], v[48:51]\n"
"v_mul_f32 v37, v88, v37\n"
"v_mul_f32 v38, v88, v38\n"
"v_mfma_f32_16x16x32_bf16 v[52:55], v[72:75], v[72:75], v[52:55]\n"
"ds_write_b64 v7, v[28:29] offset:44928\n"
"v_mfma_f32_16x16x32_bf16 v[44:47], v[200:203], v[76:79], v[44:47]\n"
"ds_read_b128 v[72:75], v5 offset:0\n"
"v_mul_f32 v39, v88, v39\n"
"v_mfma_f32_16x16x32_bf16 v[48:51], v[204:207], v[76:79], v[48:51]\n"
"v_fmac_f32 v36, v89, v16\n"
"s_waitcnt lgkmcnt(4)\n"
"v_mfma_f32_16x16x32_bf16 v[44:47], v[208:211], v[56:59], v[44:47]\n"
"ds_read_b128 v[76:79], v6 offset:0\n"
"v_fmac_f32 v37, v89, v17\n"
"v_mfma_f32_16x16x32_bf16 v[48:51], v[212:215], v[56:59], v[48:51]\n"
"v_fmac_f32 v38, v89, v18\n"
"v_fmac_f32 v39, v89, v19\n"
"v_mfma_f32_16x16x32_bf16 v[44:47], v[216:219], v[60:63], v[44:47]\n"
"ds_read_b128 v[56:59], v3 offset:20480\n"
"v_cvt_pk_bf16_f32 v36, v36, v37\n"
"v_mfma_f32_16x16x32_bf16 v[48:51], v[220:223], v[60:63], v[48:51]\n"
"v_cvt_pk_bf16_f32 v37, v38, v39\n"
"s_waitcnt lgkmcnt(4)\n"
"v_mfma_f32_16x16x32_bf16 v[44:47], v[224:227], v[64:67], v[44:47]\n"
"ds_read_b128 v[60:63], v3 offset:21504\n"
"v_mul_f32 v40, v88, v40\n"
"v_mfma_f32_16x16x32_bf16 v[48:51], v[228:231], v[64:67], v[48:51]\n"
"v_mov_b32 v84, 0x3f803f80\n"
"v_mov_b32 v85, 0x3f803f80\n"
"v_mfma_f32_16x16x32_bf16 v[52:55], v[64:67], v[64:67], v[52:55]\n"
"v_mov_b32 v86, 0x3f803f80\n"
"v_mov_b32 v87, 0x3f803f80\n"
"ds_write_b64 v8, v[32:33] offset:44928\n"
"v_mfma_f32_16x16x32_bf16 v[44:47], v[232:235], v[68:71], v[44:47]\n"
"ds_read_b128 v[64:67], v3 offset:22528\n"
"v_mul_f32 v41, v88, v41\n"
"v_mfma_f32_16x16x32_bf16 v[48:51], v[236:239], v[68:71], v[48:51]\n"
"v_mul_f32 v42, v88, v42\n"
"s_waitcnt lgkmcnt(4)\n"
"v_mfma_f32_16x16x32_bf16 v[44:47], v[240:243], v[72:75], v[44:47]\n"
"ds_read_b128 v[68:71], v3 offset:23552\n"
"v_cndmask_b32 v94, v52, v53, s[34:35]\n"
"v_mfma_f32_16x16x32_bf16 v[48:51], v[244:247], v[72:75], v[48:51]\n"
"v_cndmask_b32 v54, v54, v55, s[34:35]\n"
"v_cndmask_b32 v94, v94, v54, s[20:21]\n"
"v_mfma_f32_16x16x32_bf16 v[44:47], v[248:251], v[76:79], v[44:47]\n"
"ds_read_b128 v[72:75], v3 offset:24576\n"
"v_mul_f32 v43, v88, v43\n"
"v_mfma_f32_16x16x32_bf16 v[48:51], v[252:255], v[76:79], v[48:51]\n"
"v_fmac_f32 v40, v89, v20\n"
"s_waitcnt lgkmcnt(4)\n"
"v_mfma_f32_16x16x32_bf16 v[28:31], v[96:99], v[56:59], 0\n"
"ds_read_b128 v[76:79], v3 offset:25600\n"
"v_fmac_f32 v41, v89, v21\n"
"v_mfma_f32_16x16x32_bf16 v[32:35], v[100:103], v[56:59], 0\n"
"v_cndmask_b32 v94, v44, v94, s[22:23]\n"
"s_waitcnt vmcnt(0)\n"
"ds_write_b128 v93, v[84:87] offset:40960\n"
"v_mfma_f32_16x16x32_bf16 v[52:55], v[56:59], v[56:59], 0\n"
"ds_write_b32 v9, v94 offset:640\n"
"v_mfma_f32_16x16x32_bf16 v[28:31], v[104:107], v[60:63], v[28:31]\n"
"ds_read_b128 v[56:59], v3 offset:26624\n"
"v_fmac_f32 v42, v89, v22\n"
"v_mfma_f32_16x16x32_bf16 v[32:35], v[108:111], v[60:63], v[32:35]\n"
"v_fmac_f32 v43, v89, v23\n"
"s_waitcnt lgkmcnt(5)\n"
"v_mfma_f32_16x16x32_bf16 v[28:31], v[112:115], v[64:67], v[28:31]\n"
"ds_read_b128 v[60:63], v3 offset:27648\n"
"v_cvt_pk_bf16_f32 v40, v40, v41\n"
"v_mfma_f32_16x16x32_bf16 v[32:35], v[116:119], v[64:67], v[32:35]\n"
"v_cvt_pk_bf16_f32 v41, v42, v43\n"
"v_mfma_f32_16x16x32_bf16 v[28:31], v[120:123], v[68:71], v[28:31]\n"
"ds_read_b128 v[64:67], v3 offset:28672\n"
"v_mfma_f32_16x16x32_bf16 v[32:35], v[124:127], v[68:71], v[32:35]\n"
"s_waitcnt lgkmcnt(5)\n"
"v_mfma_f32_16x16x32_bf16 v[28:31], v[128:131], v[72:75], v[28:31]\n"
"ds_read_b128 v[68:71], v3 offset:29696\n"
"v_mfma_f32_16x16x32_bf16 v[32:35], v[132:135], v[72:75], v[32:35]\n"
"v_mfma_f32_16x16x32_bf16 v[52:55], v[72:75], v[72:75], v[52:55]\n"
"ds_read_b128 v[84:87], v11 offset:640\n"
"ds_read_b32 v91, v12 offset:640\n"
"v_mfma_f32_16x16x32_bf16 v[28:31], v[136:139], v[76:79], v[28:31]\n"
"ds_read_b128 v[72:75], v3 offset:30720\n"
"v_mfma_f32_16x16x32_bf16 v[32:35], v[140:143], v[76:79], v[32:35]\n"
"s_waitcnt lgkmcnt(5)\n"
"v_mfma_f32_16x16x32_bf16 v[28:31], v[144:147], v[56:59], v[28:31]\n"
"ds_read_b128 v[76:79], v3 offset:31744\n"
"v_mfma_f32_16x16x32_bf16 v[32:35], v[148:151], v[56:59], v[32:35]\n"
"v_mfma_f32_16x16x32_bf16 v[28:31], v[152:155], v[60:63], v[28:31]\n"
"ds_read_b128 v[56:59], v3 offset:32768\n"
"v_mfma_f32_16x16x32_bf16 v[32:35], v[156:159], v[60:63], v[32:35]\n"
"s_waitcnt lgkmcnt(5)\n"
"v_mfma_f32_16x16x32_bf16 v[28:31], v[160:163], v[64:67], v[28:31]\n"
"ds_read_b128 v[60:63], v3 offset:33792\n"
"s_waitcnt lgkmcnt(5)\n"
"v_add_f32 v84, v84, v85\n"
"v_mfma_f32_16x16x32_bf16 v[32:35], v[164:167], v[64:67], v[32:35]\n"
"v_add_f32 v86, v86, v87\n"
"v_add_f32 v84, v84, v86\n"
"v_mfma_f32_16x16x32_bf16 v[52:55], v[64:67], v[64:67], v[52:55]\n"
"s_waitcnt lgkmcnt(4)\n"
"v_mul_f32 v89, 0x3acccccd, v91\n"
"v_fma_f32 v84, -v91, v89, v84\n"
"v_mfma_f32_16x16x32_bf16 v[28:31], v[168:171], v[68:71], v[28:31]\n"
"ds_read_b128 v[64:67], v3 offset:34816\n"
"v_fmamk_f32 v84, v84, 0x3acccccd, v27\n"
"v_mfma_f32_16x16x32_bf16 v[32:35], v[172:175], v[68:71], v[32:35]\n"
"v_cmp_u_f32 vcc, v84, v84\n"
"s_waitcnt lgkmcnt(3)\n"
"v_mfma_f32_16x16x32_bf16 v[28:31], v[176:179], v[72:75], v[28:31]\n"
"ds_read_b128 v[68:71], v3 offset:35840\n"
"v_max_f32 v84, v84, v27\n"
"v_mfma_f32_16x16x32_bf16 v[32:35], v[180:183], v[72:75], v[32:35]\n"
"v_rsq_f32 v88, v84\n"
"v_mfma_f32_16x16x32_bf16 v[28:31], v[184:187], v[76:79], v[28:31]\n"
"ds_read_b128 v[72:75], v3 offset:36864\n"
"v_mul_f32 v89, -v89, v88\n"
"v_mfma_f32_16x16x32_bf16 v[32:35], v[188:191], v[76:79], v[32:35]\n"
"s_cbranch_vccnz .Lsl_42\n"
".Lbk_43:\n"
"s_waitcnt lgkmcnt(3)\n"
"v_mfma_f32_16x16x32_bf16 v[28:31], v[192:195], v[56:59], v[28:31]\n"
"ds_read_b128 v[76:79], v4 offset:20480\n"
"v_mul_f32 v44, v88, v44\n"
"v_mfma_f32_16x16x32_bf16 v[32:35], v[196:199], v[56:59], v[32:35]\n"
"v_mul_f32 v45, v88, v45\n"
"v_mul_f32 v46, v88, v46\n"
"v_mfma_f32_16x16x32_bf16 v[52:55], v[56:59], v[56:59], v[52:55]\n"
"ds_write_b64 v7, v[36:37] offset:48896\n"
"v_mfma_f32_16x16x32_bf16 v[28:31], v[200:203], v[60:63], v[28:31]\n"
"ds_read_b128 v[56:59], v5 offset:20480\n"
"v_mul_f32 v47, v88, v47\n"
"v_mfma_f32_16x16x32_bf16 v[32:35], v[204:207], v[60:63], v[32:35]\n"
"v_fmac_f32 v44, v89, v16\n"
"s_waitcnt lgkmcnt(4)\n"
"v_mfma_f32_16x16x32_bf16 v[28:31], v[208:211], v[64:67], v[28:31]\n"
"ds_read_b128 v[60:63], v6 offset:20480\n"
"v_fmac_f32 v45, v89, v17\n"
"v_mfma_f32_16x16x32_bf16 v[32:35], v[212:215], v[64:67], v[32:35]\n"
"v_fmac_f32 v46, v89, v18\n"
"v_fmac_f32 v47, v89, v19\n"
"v_mfma_f32_16x16x32_bf16 v[28:31], v[216:219], v[68:71], v[28:31]\n"
"ds_read_b128 v[64:67], v3 offset:40960\n"
"v_cvt_pk_bf16_f32 v44, v44, v45\n"
"v_mfma_f32_16x16x32_bf16 v[32:35], v[220:223], v[68:71], v[32:35]\n"
"v_cvt_pk_bf16_f32 v45, v46, v47\n"
"s_waitcnt lgkmcnt(4)\n"
"v_mfma_f32_16x16x32_bf16 v[28:31], v[224:227], v[72:75], v[28:31]\n"
"ds_read_b128 v[68:71], v3 offset:45056\n"
"v_mul_f32 v48, v88, v48\n"
"v_mfma_f32_16x16x32_bf16 v[32:35], v[228:231], v[72:75], v[32:35]\n"
"v_mul_f32 v49, v88, v49\n"
"v_mul_f32 v50, v88, v50\n"
"v_mfma_f32_16x16x32_bf16 v[52:55], v[72:75], v[72:75], v[52:55]\n"
"ds_write_b64 v8, v[40:41] offset:48896\n"
"v_mfma_f32_16x16x32_bf16 v[28:31], v[232:235], v[76:79], v[28:31]\n"
"ds_read_b128 v[72:75], v3 offset:49152\n"
"v_mul_f32 v51, v88, v51\n"
"v_mfma_f32_16x16x32_bf16 v[32:35], v[236:239], v[76:79], v[32:35]\n"
"v_fmac_f32 v48, v89, v20\n"
"s_waitcnt lgkmcnt(4)\n"
"v_mfma_f32_16x16x32_bf16 v[28:31], v[240:243], v[56:59], v[28:31]\n"
"ds_read_b128 v[76:79], v3 offset:53248\n"
"v_cndmask_b32 v94, v52, v53, s[34:35]\n"
"v_mfma_f32_16x16x32_bf16 v[32:35], v[244:247], v[56:59], v[32:35]\n"
"v_cndmask_b32 v54, v54, v55, s[34:35]\n"
"v_cndmask_b32 v94, v94, v54, s[20:21]\n"
"v_mfma_f32_16x16x32_bf16 v[28:31], v[248:251], v[60:63], v[28:31]\n"
"ds_read_b128 v[56:59], v3 offset:57344\n"
"v_fmac_f32 v49, v89, v21\n"
"v_mfma_f32_16x16x32_bf16 v[32:35], v[252:255], v[60:63], v[32:35]\n"
"v_fmac_f32 v50, v89, v22\n"
"s_waitcnt lgkmcnt(4)\n"
"v_mfma_f32_16x16x32_bf16 v[36:39], v[96:99], v[64:67], 0\n"
"ds_read_b128 v[60:63], v3 offset:41984\n"
"v_fmac_f32 v51, v89, v23\n"
"v_mfma_f32_16x16x32_bf16 v[40:43], v[100:103], v[64:67], 0\n"
"v_cndmask_b32 v94, v28, v94, s[22:23]\n"
"v_cvt_pk_bf16_f32 v48, v48, v49\n"
"v_mfma_f32_16x16x32_bf16 v[52:55], v[64:67], v[64:67], 0\n"
"ds_write_b32 v9, v94 offset:960\n"
"v_mfma_f32_16x16x32_bf16 v[36:39], v[128:131], v[68:71], v[36:39]\n"
"ds_read_b128 v[64:67], v3 offset:43008\n"
"v_cvt_pk_bf16_f32 v49, v50, v51\n"
"v_mfma_f32_16x16x32_bf16 v[40:43], v[132:135], v[68:71], v[40:43]\n"
"v_mfma_f32_16x16x32_bf16 v[52:55], v[68:71], v[68:71], v[52:55]\n"
"ds_write_b64 v7, v[44:45] offset:52864\n"
"s_waitcnt lgkmcnt(5)\n"
"v_mfma_f32_16x16x32_bf16 v[36:39], v[160:163], v[72:75], v[36:39]\n"
"ds_read_b128 v[68:71], v3 offset:44032\n"
"v_mfma_f32_16x16x32_bf16 v[40:43], v[164:167], v[72:75], v[40:43]\n"
"v_mfma_f32_16x16x32_bf16 v[52:55], v[72:75], v[72:75], v[52:55]\n"
"ds_write_b64 v8, v[48:49] offset:52864\n"
"v_mfma_f32_16x16x32_bf16 v[36:39], v[192:195], v[76:79], v[36:39]\n"
"ds_read_b128 v[72:75], v3 offset:46080\n"
"v_mfma_f32_16x16x32_bf16 v[40:43], v[196:199], v[76:79], v[40:43]\n"
"v_mfma_f32_16x16x32_bf16 v[52:55], v[76:79], v[76:79], v[52:55]\n"
"ds_read_b128 v[84:87], v11 offset:960\n"
"ds_read_b32 v91, v12 offset:960\n"
"s_waitcnt lgkmcnt(8)\n"
"v_mfma_f32_16x16x32_bf16 v[36:39], v[224:227], v[56:59], v[36:39]\n"
"ds_read_b128 v[76:79], v3 offset:47104\n"
"v_mfma_f32_16x16x32_bf16 v[40:43], v[228:231], v[56:59], v[40:43]\n"
"v_mfma_f32_16x16x32_bf16 v[52:55], v[56:59], v[56:59], v[52:55]\n"
"v_mfma_f32_16x16x32_bf16 v[36:39], v[104:107], v[60:63], v[36:39]\n"
"ds_read_b128 v[56:59], v3 offset:48128\n"
"v_mfma_f32_16x16x32_bf16 v[40:43], v[108:111], v[60:63], v[40:43]\n"
"s_waitcnt lgkmcnt(6)\n"
"v_mfma_f32_16x16x32_bf16 v[36:39], v[112:115], v[64:67], v[36:39]\n"
"ds_read_b128 v[60:63], v3 offset:50176\n"
"ds_write_b32 v10, v52 offset:1600\n"
"v_cndmask_b32 v94, v52, v53, s[34:35]\n"
"v_cndmask_b32 v54, v54, v55, s[34:35]\n"
"v_mfma_f32_16x16x32_bf16 v[40:43], v[116:119], v[64:67], v[40:43]\n"
"s_waitcnt lgkmcnt(5)\n"
"v_add_f32 v84, v84, v85\n"
"v_add_f32 v86, v86, v87\n"
"v_add_f32 v84, v84, v86\n"
"v_cndmask_b32 v94, v94, v54, s[20:21]\n"
"ds_write_b32 v9, v94 offset:1280\n"
"v_mfma_f32_16x16x32_bf16 v[36:39], v[120:123], v[68:71], v[36:39]\n"
"ds_read_b128 v[64:67], v3 offset:51200\n"
"s_waitcnt lgkmcnt(6)\n"
"v_mul_f32 v89, 0x3acccccd, v91\n"
"v_fma_f32 v84, -v91, v89, v84\n"
"v_mfma_f32_16x16x32_bf16 v[40:43], v[124:127], v[68:71], v[40:43]\n"
"v_fmamk_f32 v84, v84, 0x3acccccd, v27\n"
"s_waitcnt lgkmcnt(5)\n"
"v_mfma_f32_16x16x32_bf16 v[36:39], v[136:139], v[72:75], v[36:39]\n"
"ds_read_b128 v[68:71], v3 offset:52224\n"
"v_cmp_u_f32 vcc, v84, v84\n"
"v_mfma_f32_16x16x32_bf16 v[40:43], v[140:143], v[72:75], v[40:43]\n"
"v_max_f32 v84, v84, v27\n"
"v_rsq_f32 v88, v84\n"
"v_mfma_f32_16x16x32_bf16 v[36:39], v[144:147], v[76:79], v[36:39]\n"
"ds_read_b128 v[72:75], v3 offset:54272\n"
"v_mul_f32 v89, -v89, v88\n"
"v_mfma_f32_16x16x32_bf16 v[40:43], v[148:151], v[76:79], v[40:43]\n"
"s_cbranch_vccnz .Lsl_46\n"
".Lbk_47:\n"
"s_waitcnt lgkmcnt(5)\n"
"v_mfma_f32_16x16x32_bf16 v[36:39], v[152:155], v[56:59], v[36:39]\n"
"ds_read_b128 v[76:79], v3 offset:55296\n"
"v_mul_f32 v28, v88, v28\n"
"v_mul_f32 v29, v88, v29\n"
"v_mfma_f32_16x16x32_bf16 v[40:43], v[156:159], v[56:59], v[40:43]\n"
"ds_read_b128 v[44:47], v11 offset:1280\n"
"ds_read_b128 v[48:51], v11 offset:1600\n"
"v_mul_f32 v30, v88, v30\n"
"v_mfma_f32_16x16x32_bf16 v[36:39], v[168:171], v[60:63], v[36:39]\n"
"ds_read_b128 v[56:59], v3 offset:56320\n"
"v_mul_f32 v31, v88, v31\n"
"v_fmac_f32 v28, v89, v16\n"
"v_mfma_f32_16x16x32_bf16 v[40:43], v[172:175], v[60:63], v[40:43]\n"
"v_fmac_f32 v29, v89, v17\n"
"v_fmac_f32 v30, v89, v18\n"
"s_waitcnt lgkmcnt(5)\n"
"v_mfma_f32_16x16x32_bf16 v[36:39], v[176:179], v[64:67], v[36:39]\n"
"ds_read_b128 v[60:63], v4 offset:40960\n"
"v_fmac_f32 v31, v89, v19\n"
"v_cvt_pk_bf16_f32 v28, v28, v29\n"
"v_mfma_f32_16x16x32_bf16 v[40:43], v[180:183], v[64:67], v[40:43]\n"
"v_cvt_pk_bf16_f32 v29, v30, v31\n"
"ds_write_b64 v7, v[28:29] offset:56832\n"
"v_mfma_f32_16x16x32_bf16 v[36:39], v[184:187], v[68:71], v[36:39]\n"
"ds_read_b128 v[64:67], v5 offset:40960\n"
"v_mul_f32 v32, v88, v32\n"
"v_mul_f32 v33, v88, v33\n"
"v_mfma_f32_16x16x32_bf16 v[40:43], v[188:191], v[68:71], v[40:43]\n"
"v_mul_f32 v34, v88, v34\n"
"v_mul_f32 v35, v88, v35\n"
"s_waitcnt lgkmcnt(6)\n"
"v_mfma_f32_16x16x32_bf16 v[36:39], v[200:203], v[72:75], v[36:39]\n"
"ds_read_b128 v[68:71], v6 offset:40960\n"
"s_waitcnt lgkmcnt(6)\n"
"v_add_f32 v44, v44, v45\n"
"v_fmac_f32 v32, v89, v20\n"
"v_mfma_f32_16x16x32_bf16 v[40:43], v[204:207], v[72:75], v[40:43]\n"
"v_add_f32 v46, v46, v47\n"
"s_waitcnt lgkmcnt(5)\n"
"v_add_f32 v48, v48, v49\n"
"v_fmac_f32 v33, v89, v21\n"
"v_mfma_f32_16x16x32_bf16 v[36:39], v[208:211], v[76:79], v[36:39]\n"
"v_add_f32 v50, v50, v51\n"
"v_add_f32 v48, v48, v50\n"
"v_fmac_f32 v34, v89, v22\n"
"v_mfma_f32_16x16x32_bf16 v[40:43], v[212:215], v[76:79], v[40:43]\n"
"v_add_f32 v44, v44, v46\n"
"v_fmac_f32 v35, v89, v23\n"
"s_waitcnt lgkmcnt(3)\n"
"v_mfma_f32_16x16x32_bf16 v[36:39], v[216:219], v[56:59], v[36:39]\n"
"v_mul_f32 v53, 0x3acccccd, v48\n"
"v_fma_f32 v44, -v48, v53, v44\n"
"v_cvt_pk_bf16_f32 v32, v32, v33\n"
"v_mfma_f32_16x16x32_bf16 v[40:43], v[220:223], v[56:59], v[40:43]\n"
"v_fmamk_f32 v44, v44, 0x3acccccd, v27\n"
"v_cmp_u_f32 vcc, v44, v44\n"
"v_mfma_f32_16x16x32_bf16 v[36:39], v[232:235], v[60:63], v[36:39]\n"
"v_max_f32 v44, v44, v27\n"
"v_rsq_f32 v52, v44\n"
"v_mfma_f32_16x16x32_bf16 v[40:43], v[236:239], v[60:63], v[40:43]\n"
"v_mul_f32 v53, -v53, v52\n"
"s_waitcnt lgkmcnt(0)\n"
"v_mfma_f32_16x16x32_bf16 v[36:39], v[240:243], v[64:67], v[36:39]\n"
"s_cbranch_vccnz .Lsl_50\n"
".Lbk_51:\n"
"v_mfma_f32_16x16x32_bf16 v[40:43], v[244:247], v[64:67], v[40:43]\n"
"v_cvt_pk_bf16_f32 v33, v34, v35\n"
"ds_write_b64 v8, v[32:33] offset:56832\n"
"v_mfma_f32_16x16x32_bf16 v[36:39], v[248:251], v[68:71], v[36:39]\n"
"v_mfma_f32_16x16x32_bf16 v[40:43], v[252:255], v[68:71], v[40:43]\n"
"s_nop 4\n"
"v_mul_f32 v36, v52, v36\n"
"v_mul_f32 v37, v52, v37\n"
"v_mul_f32 v38, v52, v38\n"
"v_mul_f32 v39, v52, v39\n"
"v_fmac_f32 v36, v53, v16\n"
"v_fmac_f32 v37, v53, v17\n"
"v_fmac_f32 v38, v53, v18\n"
"v_fmac_f32 v39, v53, v19\n"
"v_cvt_pk_bf16_f32 v36, v36, v37\n"
"v_cvt_pk_bf16_f32 v37, v38, v39\n"
"ds_write_b64 v7, v[36:37] offset:40960\n"
"v_mul_f32 v40, v52, v40\n"
"v_mul_f32 v41, v52, v41\n"
"v_mul_f32 v42, v52, v42\n"
"v_mul_f32 v43, v52, v43\n"
"v_fmac_f32 v40, v53, v20\n"
"v_fmac_f32 v41, v53, v21\n"
"v_fmac_f32 v42, v53, v22\n"
"v_fmac_f32 v43, v53, v23\n"
"v_cvt_pk_bf16_f32 v40, v40, v41\n"
"v_cvt_pk_bf16_f32 v41, v42, v43\n"
"ds_write_b64 v8, v[40:41] offset:40960\n"
"ds_write_b32 v13, v13 offset:128\n"
"v_mov_b32 v84, 0\n"
"v_mov_b32 v85, 0\n"
"v_mov_b32 v86, 0\n"
"v_mov_b32 v87, 0\n"
"v_mov_b32 v30, 0\n"
"v_mov_b32 v31, 0\n"
"v_mov_b32 v34, 0\n"
"v_mov_b32 v35, 0\n"
"s_mov_b32 s17, 0x100000\n"
".Ltp_54:\n"
"ds_read_b32 v90, v14 offset:128\n"
"s_sub_u32 s17, s17, 1\n"
"s_cbranch_scc1 .Lou_55\n"
"s_waitcnt lgkmcnt(0)\n"
"v_cmp_eq_u32 vcc, 0, v90\n"
"s_cbranch_vccnz .Ltp_54\n"
".Lou_55:\n"
"s_waitcnt lgkmcnt(0)\n"
"s_mov_b64 exec, 0xffff\n"
"ds_read_b64 v[30:31], v24 offset:44960\n"
"ds_read_b64 v[34:35], v24 offset:45000\n"
"s_mov_b64 exec, -1\n"
"ds_read_b64 v[28:29], v25 offset:44928\n"
"ds_read_b64 v[68:69], v25 offset:41040\n"
"ds_read_b64 v[70:71], v24 offset:41072\n"
"ds_read_b64 v[76:77], v25 offset:45008\n"
"ds_read_b64 v[78:79], v24 offset:45040\n"
"ds_read_b64 v[32:33], v25 offset:44968\n"
"ds_read_b64 v[72:73], v25 offset:41080\n"
"ds_read_b64 v[74:75], v24 offset:41112\n"
"ds_read_b64 v[80:81], v25 offset:45048\n"
"ds_read_b64 v[82:83], v24 offset:45080\n"
"ds_read_b64_tr_b16 v[44:45], v26 offset:40960\n"
"ds_read_b64_tr_b16 v[46:47], v26 offset:44928\n"
"ds_read_b64_tr_b16 v[48:49], v26 offset:41000\n"
"v_mov_b32 v64, 0x0c0c0c0c\n"
"v_mov_b32 v65, 0xc0c0100\n"
"v_mov_b32 v66, 0x1000c0c\n"
"v_cndmask_b32 v65, v65, v66, s[34:35]\n"
"v_cndmask_b32 v36, v65, v64, s[36:37]\n"
"v_cndmask_b32 v38, v64, v65, s[36:37]\n"
"v_mov_b32 v65, 0xc0c0302\n"
"v_mov_b32 v66, 0x3020c0c\n"
"v_cndmask_b32 v65, v65, v66, s[34:35]\n"
"v_cndmask_b32 v37, v65, v64, s[38:39]\n"
"v_cndmask_b32 v39, v64, v65, s[38:39]\n"
"s_waitcnt lgkmcnt(10)\n"
"v_mfma_f32_16x16x32_bf16 v[68:71], v[68:71], v[28:31], v[84:87]\n"
"s_waitcnt lgkmcnt(8)\n"
"v_mfma_f32_16x16x32_bf16 v[76:79], v[76:79], v[28:31], 0\n"
"ds_read_b64_tr_b16 v[50:51], v26 offset:44968\n"
"ds_read_b64_tr_b16 v[52:53], v26 offset:40992\n"
"ds_read_b64_tr_b16 v[54:55], v26 offset:44960\n"
"s_waitcnt lgkmcnt(8)\n"
"v_mfma_f32_16x16x32_bf16 v[72:75], v[72:75], v[32:35], v[84:87]\n"
"s_waitcnt lgkmcnt(6)\n"
"v_mfma_f32_16x16x32_bf16 v[80:83], v[80:83], v[32:35], 0\n"
"ds_read_b64_tr_b16 v[56:57], v26 offset:41032\n"
"ds_read_b64_tr_b16 v[58:59], v26 offset:45000\n"
"v_mov_b32 v40, 0x3f803f80\n"
"v_mov_b32 v41, 0x3f803f80\n"
"v_mov_b32 v42, 0x3f803f80\n"
"v_mov_b32 v43, 0x3f803f80\n"
"v_cndmask_b32 v60, v68, v69, s[34:35]\n"
"v_cndmask_b32 v84, v76, v77, s[34:35]\n"
"v_cndmask_b32 v60, v60, v84, s[36:37]\n"
"v_cndmask_b32 v61, v70, v71, s[34:35]\n"
"v_cndmask_b32 v84, v78, v79, s[34:35]\n"
"v_cndmask_b32 v61, v61, v84, s[38:39]\n"
"v_max_f32 v64, v60, v61\n"
"v_max_f32 v66, v60, v61\n"
"v_cndmask_b32 v62, v72, v73, s[34:35]\n"
"v_cndmask_b32 v84, v80, v81, s[34:35]\n"
"v_cndmask_b32 v62, v62, v84, s[36:37]\n"
"v_cndmask_b32 v63, v74, v75, s[34:35]\n"
"v_cndmask_b32 v84, v82, v83, s[34:35]\n"
"v_cndmask_b32 v63, v63, v84, s[38:39]\n"
"v_max_f32 v65, v62, v63\n"
"v_max_f32 v67, v62, v63\n"
"v_permlane16_swap_b32 v64, v66\n"
"s_nop 0\n"
"v_permlane16_swap_b32 v65, v67\n"
"v_max_f32 v84, v64, v66\n"
"v_max_f32 v64, v64, v66\n"
"v_max_f32 v85, v65, v67\n"
"v_max_f32 v65, v65, v67\n"
"v_permlane32_swap_b32 v64, v84\n"
"s_nop 0\n"
"v_permlane32_swap_b32 v65, v85\n"
"v_max_f32 v64, v64, v84\n"
"v_max_f32 v65, v65, v85\n"
"v_mul_f32 v64, 0xbea52b5b, v64\n"
"v_mul_f32 v65, 0xbea52b5b, v65\n"
"v_fmamk_f32 v60, v60, 0x3ea52b5b, v64\n"
"v_fmamk_f32 v61, v61, 0x3ea52b5b, v64\n"
"v_fmamk_f32 v62, v62, 0x3ea52b5b, v65\n"
"v_fmamk_f32 v63, v63, 0x3ea52b5b, v65\n"
"v_exp_f32 v60, v60\n"
"v_exp_f32 v61, v61\n"
"v_exp_f32 v62, v62\n"
"v_exp_f32 v63, v63\n"
"v_cvt_pk_bf16_f32 v64, v60, v61\n"
"v_cvt_pk_bf16_f32 v65, v62, v63\n"
"v_perm_b32 v28, v64, v64, v36\n"
"v_perm_b32 v29, v64, v64, v37\n"
"v_perm_b32 v30, v64, v64, v38\n"
"v_perm_b32 v31, v64, v64, v39\n"
"v_perm_b32 v32, v65, v65, v36\n"
"v_perm_b32 v33, v65, v65, v37\n"
"v_perm_b32 v34, v65, v65, v38\n"
"v_perm_b32 v35, v65, v65, v39\n"
"v_mfma_f32_16x16x32_bf16 v[68:71], v[40:43], v[28:31], 0\n"
"s_nop 0\n"
"v_mfma_f32_16x16x32_bf16 v[72:75], v[40:43], v[32:35], 0\n"
"s_waitcnt lgkmcnt(6)\n"
"v_mfma_f32_16x16x32_bf16 v[44:47], v[44:47], v[28:31], 0\n"
"s_waitcnt lgkmcnt(4)\n"
"v_mfma_f32_16x16x32_bf16 v[48:51], v[48:51], v[32:35], 0\n"
"s_waitcnt lgkmcnt(2)\n"
"v_mfma_f32_16x16x32_bf16 v[52:55], v[52:55], v[28:31], 0\n"
"s_waitcnt lgkmcnt(0)\n"
"v_mfma_f32_16x16x32_bf16 v[56:59], v[56:59], v[32:35], 0\n"
"v_cndmask_b32 v88, 2.0, v1, s[40:41]\n"
"v_rcp_f32 v64, v68\n"
"v_rcp_f32 v65, v72\n"
"s_add_u32 s31, s30, 0x140000\n"
"v_mul_f32 v44, v64, v44\n"
"v_mul_f32 v45, v64, v45\n"
"v_mul_f32 v46, v64, v46\n"
"v_mul_f32 v47, v64, v47\n"
"v_mul_f32 v48, v65, v48\n"
"v_mul_f32 v49, v65, v49\n"
"v_mul_f32 v50, v65, v50\n"
"v_mul_f32 v51, v65, v51\n"
"v_cvt_pk_bf16_f32 v44, v44, v45\n"
"v_cvt_pk_bf16_f32 v45, v46, v47\n"
"v_cvt_pk_bf16_f32 v46, v48, v49\n"
"v_cvt_pk_bf16_f32 v47, v50, v51\n"
"v_min_u32 v44, -2, v44\n"
"v_min_u32 v45, -2, v45\n"
"v_min_u32 v46, -2, v46\n"
"v_min_u32 v47, -2, v47\n"
"buffer_store_dwordx4 v[44:47], v1, s[4:7], s31 offen offset:0 sc1\n"
"v_mul_f32 v52, v64, v52\n"
"v_mul_f32 v53, v64, v53\n"
"v_mul_f32 v54, v64, v54\n"
"v_mul_f32 v55, v64, v55\n"
"v_mul_f32 v56, v65, v56\n"
"v_mul_f32 v57, v65, v57\n"
"v_mul_f32 v58, v65, v58\n"
"v_mul_f32 v59, v65, v59\n"
"v_cvt_pk_bf16_f32 v52, v52, v53\n"
"v_cvt_pk_bf16_f32 v53, v54, v55\n"
"v_cvt_pk_bf16_f32 v54, v56, v57\n"
"v_cvt_pk_bf16_f32 v55, v58, v59\n"
"v_min_u32 v52, -2, v52\n"
"v_min_u32 v53, -2, v53\n"
"v_min_u32 v54, -2, v54\n"
"v_min_u32 v55, -2, v55\n"
"buffer_store_dwordx4 v[52:55], v88, s[4:7], s31 offen offset:1024 sc1\n"
"ds_write_b32 v13, v13 offset:144\n"
"s_nop 2\n"
"s_cmp_lt_i32 s45, 0\n"
"s_cbranch_scc1 .Lnc_56\n"
"s_setprio 2\n"
"s_lshl_b32 s79, s3, 10\n"
"v_lshl_add_u32 v34, v0, 4, s79\n"
"v_lshrrev_b32 v59, 7, v34\n"
"v_and_b32 v58, 0x70, v34\n"
"s_mov_b32 s79, 0x5000\n"
"v_mad_i32_i24 v33, v59, s79, v58\n"
"s_mov_b32 s17, 0x100000\n"
".Ltp_57:\n"
"s_sleep 1\n"
"ds_read_b32 v42, v14 offset:160\n"
"s_sub_u32 s17, s17, 1\n"
"s_cbranch_scc1 .Lou_58\n"
"s_waitcnt lgkmcnt(0)\n"
"v_cmp_eq_u32 vcc, 0, v42\n"
"s_cbranch_vccnz .Ltp_57\n"
".Lou_58:\n"
"s_waitcnt lgkmcnt(0)\n"
"s_lshl_b32 s79, s45, 8\n"
"s_add_u32 s46, s79, 0x5e8000\n"
"s_add_u32 s47, s46, 0x140000\n"
"s_and_b32 s79, s2, 7\n"
"s_lshl_b32 s79, s79, 14\n"
"s_add_u32 s48, s79, 0xb28000\n"
"s_add_u32 s49, s48, 0x1000\n"
"v_and_b32 v94, 0xffffff80, v33\n"
"v_add_u32 v94, v94, v33\n"
"buffer_load_dwordx4 v[78:81], v94, s[4:7], s46 offen offset:0 sc1\n"
"buffer_load_dwordx4 v[82:85], v94, s[4:7], s46 offen offset:128 sc1\n"
"buffer_load_dwordx4 v[86:89], v94, s[4:7], s47 offen offset:0 sc1\n"
"buffer_load_dwordx4 v[90:93], v94, s[4:7], s47 offen offset:128 sc1\n"
"buffer_load_dwordx4 v[70:73], v34, s[4:7], s48 offen sc1\n"
"buffer_load_dwordx4 v[74:77], v34, s[4:7], s49 offen sc1\n"
"s_mul_i32 s79, s3, 0x1200\n"
"s_add_u32 s79, s79, 0x1c400\n"
"v_lshl_add_u32 v44, v0, 3, s79\n"
"v_mov_b32 v46, 0\n"
"v_mov_b32 v47, 0\n"
"ds_write_b64 v44, v[46:47]\n"
"v_and_b32 v51, 15, v0\n"
"v_and_b32 v50, 1, v51\n"
"v_and_b32 v49, 2, v51\n"
"v_lshlrev_b32 v48, 4, v0\n"
"v_lshrrev_b32 v47, 4, v0\n"
"v_xor_b32 v46, s3, v51\n"
"v_and_b32 v45, 1, v46\n"
"v_lshlrev_b32 v44, 1, v45\n"
"s_lshl_b32 s79, s3, 10\n"
"v_add_u32 v59, s79, v48\n"
"v_lshrrev_b32 v59, 2, v59\n"
"s_lshl_b32 s79, s3, 10\n"
"v_add_u32 v58, s79, v48\n"
"v_lshrrev_b32 v58, 6, v58\n"
"v_and_b32 v58, 24, v58\n"
"v_xor_b32 v59, v59, v58\n"
"v_add_u32 v36, 0xa000, v59\n"
"s_lshl_b32 s79, s3, 8\n"
"s_add_u32 s79, s79, 0xa000\n"
"v_lshrrev_b32 v59, 2, v51\n"
"v_lshrrev_b32 v58, 1, v47\n"
"v_xor_b32 v59, v59, v58\n"
"v_and_b32 v59, 1, v59\n"
"v_add_u32 v59, v44, v59\n"
"v_lshrrev_b32 v58, 2, v51\n"
"v_lshrrev_b32 v57, 3, v51\n"
"v_lshlrev_b32 v57, 7, v57\n"
"v_lshl_add_u32 v58, v58, 11, v57\n"
"v_lshl_add_u32 v58, v47, 6, v58\n"
"v_lshl_add_u32 v58, v49, 4, v58\n"
"v_lshl_add_u32 v59, v59, 3, v58\n"
"v_add_u32 v31, s79, v59\n"
"s_mul_i32 s79, s3, 0x2100\n"
"s_add_u32 s79, s79, 0x13840\n"
"v_lshlrev_b32 v59, 3, v47\n"
"v_sub_u32 v59, v59, v51\n"
"v_add_u32 v59, 0x3ef, v59\n"
"v_lshrrev_b32 v59, 2, v59\n"
"v_lshlrev_b32 v58, 3, v47\n"
"v_sub_u32 v58, v58, v51\n"
"v_add_u32 v58, 0x3ef, v58\n"
"v_and_b32 v58, 3, v58\n"
"v_mul_i32_i24 v58, 0x840, v58\n"
"v_lshl_add_u32 v59, v59, 3, v58\n"
"v_add_u32 v28, s79, v59\n"
"s_mul_i32 s79, s3, 0x1200\n"
"s_add_u32 s79, s79, 0x1c420\n"
"v_lshlrev_b32 v59, 4, v47\n"
"v_lshl_add_u32 v59, v51, 5, v59\n"
"v_add_u32 v29, s79, v59\n"
"s_lshl_b32 s79, s3, 11\n"
"s_lshr_b32 s78, s3, 1\n"
"s_lshl_b32 s78, s78, 7\n"
"s_add_u32 s79, s79, s78\n"
"s_add_u32 s79, s79, 0xa000\n"
"s_and_b32 s78, s3, 1\n"
"v_lshrrev_b32 v59, 2, v51\n"
"v_xor_b32 v59, v47, v59\n"
"v_xor_b32 v59, s78, v59\n"
"v_and_b32 v59, 3, v59\n"
"v_lshlrev_b32 v59, 3, v59\n"
"v_lshl_add_u32 v59, v51, 5, v59\n"
"v_add_u32 v30, s79, v59\n"
"v_xor_b32 v32, 8, v31\n"
"s_lshl_b32 s79, s3, 10\n"
"v_add_u32 v59, s79, v48\n"
"v_lshrrev_b32 v59, 2, v59\n"
"v_add_u32 v35, 0x1c600, v59\n"
"v_xor_b32 v37, 8, v36\n"
"v_mov_b32 v59, 0xffff0000\n"
"v_mov_b32 v58, 0xffff\n"
"v_cmp_eq_i32 vcc, 1, v50\n"
"s_nop 1\n"
"v_cndmask_b32 v59, v58, v59, vcc\n"
"v_lshrrev_b32 v58, 1, v51\n"
"v_lshlrev_b32 v57, 1, v47\n"
"v_cmp_eq_i32 vcc, v58, v57\n"
"s_nop 1\n"
"v_cndmask_b32 v38, 0, v59, vcc\n"
"v_mov_b32 v59, 0xffff0000\n"
"v_mov_b32 v58, 0xffff\n"
"v_cmp_eq_i32 vcc, 1, v50\n"
"s_nop 1\n"
"v_cndmask_b32 v59, v58, v59, vcc\n"
"v_lshrrev_b32 v58, 1, v51\n"
"v_lshl_add_u32 v57, v47, 1, 1\n"
"v_cmp_eq_i32 vcc, v58, v57\n"
"s_nop 1\n"
"v_cndmask_b32 v39, 0, v59, vcc\n"
"s_lshl_b32 s79, s3, 2\n"
"s_add_u32 s79, s79, 0x26800\n"
"v_mov_b32 v40, s79\n"
"s_lshl_b32 s79, s3, 10\n"
"v_add_u32 v59, s79, v48\n"
"v_lshrrev_b32 v59, 6, v59\n"
"s_lshl_b32 s79, s3, 10\n"
"v_add_u32 v58, s79, v48\n"
"v_and_b32 v58, 48, v58\n"
"v_mul_i32_i24 v58, 0x50, v58\n"
"s_mov_b32 s79, 0x2800\n"
"v_mad_i32_i24 v41, v59, s79, v58\n"
"s_mov_b32 s17, 0x100000\n"
".Ltp_59:\n"
"s_sleep 1\n"
"ds_read_b32 v42, v14 offset:176\n"
"s_sub_u32 s17, s17, 1\n"
"s_cbranch_scc1 .Lou_60\n"
"s_waitcnt lgkmcnt(0)\n"
"v_cmp_eq_u32 vcc, 0, v42\n"
"s_cbranch_vccnz .Ltp_59\n"
".Lou_60:\n"
"s_waitcnt lgkmcnt(0)\n"
"ds_read_b32 v40, v40\n"
"s_waitcnt lgkmcnt(0)\n"
"v_sub_u32 v44, 0x7f000000, v40\n"
"v_lshrrev_b32 v45, 16, v44\n"
"v_or_b32 v44, v44, v45\n"
"v_and_b32 v38, v44, v38\n"
"v_and_b32 v39, v44, v39\n"
"s_mov_b32 s52, 0x05040100\n"
"s_mov_b32 s53, 0x07060302\n"
"s_mov_b32 s17, 32768\n"
".Ltp_61:\n"
"s_waitcnt vmcnt(0)\n"
"s_sub_u32 s17, s17, 1\n"
"s_cbranch_scc1 .Lou_62\n"
"v_bitop3_b32 v54, v79, v81, s16 bitop3:0x7e\n"
"v_bitop3_b32 v55, v83, v85, s16 bitop3:0x7e\n"
"v_bitop3_b32 v56, v87, v89, s16 bitop3:0x7e\n"
"v_bitop3_b32 v57, v91, v93, s16 bitop3:0x7e\n"
"v_or3_b32 v58, v54, v55, v56\n"
"v_or_b32 v58, v58, v57\n"
"v_cmp_ne_u32 vcc, 0, v58\n"
"s_cbranch_vccz .Lou_62\n"
"s_sleep 2\n"
"v_cmp_ne_u32 vcc, 0, v54\n"
"s_cbranch_vccz .Lsk_63\n"
"s_mov_b64 exec, vcc\n"
"buffer_load_dwordx4 v[78:81], v94, s[4:7], s46 offen offset:0 sc1\n"
"s_mov_b64 exec, -1\n"
".Lsk_63:\n"
"v_cmp_ne_u32 vcc, 0, v55\n"
"s_cbranch_vccz .Lsk_64\n"
"s_mov_b64 exec, vcc\n"
"buffer_load_dwordx4 v[82:85], v94, s[4:7], s46 offen offset:128 sc1\n"
"s_mov_b64 exec, -1\n"
".Lsk_64:\n"
"v_cmp_ne_u32 vcc, 0, v56\n"
"s_cbranch_vccz .Lsk_65\n"
"s_mov_b64 exec, vcc\n"
"buffer_load_dwordx4 v[86:89], v94, s[4:7], s47 offen offset:0 sc1\n"
"s_mov_b64 exec, -1\n"
".Lsk_65:\n"
"v_cmp_ne_u32 vcc, 0, v57\n"
"s_cbranch_vccz .Lsk_66\n"
"s_mov_b64 exec, vcc\n"
"buffer_load_dwordx4 v[90:93], v94, s[4:7], s47 offen offset:128 sc1\n"
"s_mov_b64 exec, -1\n"
".Lsk_66:\n"
"s_branch .Ltp_61\n"
".Lou_62:\n"
"s_waitcnt vmcnt(0)\n"
"v_perm_b32 v54, v82, v78, s52\n"
"v_perm_b32 v55, v82, v78, s53\n"
"v_perm_b32 v56, v84, v80, s52\n"
"v_perm_b32 v57, v84, v80, s53\n"
"ds_write_b32 v36, v54 offset:0\n"
"ds_write_b32 v37, v55 offset:2048\n"
"ds_write_b32 v36, v56 offset:4224\n"
"ds_write_b32 v37, v57 offset:6272\n"
"v_perm_b32 v54, v90, v86, s52\n"
"v_perm_b32 v55, v90, v86, s53\n"
"v_perm_b32 v56, v92, v88, s52\n"
"v_perm_b32 v57, v92, v88, s53\n"
"ds_write_b32 v36, v54 offset:1024\n"
"ds_write_b32 v37, v55 offset:3072\n"
"ds_write_b32 v36, v56 offset:5248\n"
"ds_write_b32 v37, v57 offset:7296\n"
"v_lshlrev_b32 v54, 16, v78\n"
"v_lshlrev_b32 v55, 16, v82\n"
"v_and_b32 v56, 0xffff0000, v78\n"
"v_and_b32 v57, 0xffff0000, v82\n"
"v_lshlrev_b32 v58, 16, v80\n"
"v_lshlrev_b32 v59, 16, v84\n"
"v_and_b32 v60, 0xffff0000, v80\n"
"v_and_b32 v61, 0xffff0000, v84\n"
"v_lshlrev_b32 v62, 16, v86\n"
"v_lshlrev_b32 v63, 16, v90\n"
"v_and_b32 v64, 0xffff0000, v86\n"
"v_and_b32 v65, 0xffff0000, v90\n"
"v_lshlrev_b32 v66, 16, v88\n"
"v_lshlrev_b32 v67, 16, v92\n"
"v_and_b32 v68, 0xffff0000, v88\n"
"v_and_b32 v69, 0xffff0000, v92\n"
"s_mov_b32 s17, 32768\n"
".Ltp_67:\n"
"s_waitcnt vmcnt(0)\n"
"s_sub_u32 s17, s17, 1\n"
"s_cbranch_scc1 .Lou_68\n"
"v_max_u32 v78, v70, v71\n"
"v_max3_u32 v78, v72, v73, v78\n"
"v_max3_u32 v78, v74, v75, v78\n"
"v_max3_u32 v78, v76, v77, v78\n"
"v_cmp_eq_u32 vcc, -1, v78\n"
"s_cbranch_vccz .Lou_68\n"
"v_max_u32 v78, v72, v73\n"
"v_max3_u32 v78, v70, v71, v78\n"
"v_cmp_eq_u32 vcc, -1, v78\n"
"s_cbranch_vccz .Lsk_69\n"
"s_mov_b64 exec, vcc\n"
"buffer_load_dwordx4 v[70:73], v34, s[4:7], s48 offen sc1\n"
"s_mov_b64 exec, -1\n"
".Lsk_69:\n"
"v_max_u32 v78, v76, v77\n"
"v_max3_u32 v78, v74, v75, v78\n"
"v_cmp_eq_u32 vcc, -1, v78\n"
"s_cbranch_vccz .Lsk_70\n"
"s_mov_b64 exec, vcc\n"
"buffer_load_dwordx4 v[74:77], v34, s[4:7], s49 offen sc1\n"
"s_mov_b64 exec, -1\n"
".Lsk_70:\n"
"s_branch .Ltp_67\n"
".Lou_68:\n"
"s_waitcnt vmcnt(0)\n"
"v_fma_f32 v54, v54, v71, v70\n"
"v_fma_f32 v55, v55, v73, v72\n"
"v_cvt_pk_f16_f32 v54, v54, v55\n"
"v_fma_f32 v56, v56, v71, v70\n"
"v_fma_f32 v57, v57, v73, v72\n"
"v_cvt_pk_f16_f32 v56, v56, v57\n"
"v_fma_f32 v58, v58, v71, v70\n"
"v_fma_f32 v59, v59, v73, v72\n"
"v_cvt_pk_f16_f32 v58, v58, v59\n"
"v_fma_f32 v60, v60, v71, v70\n"
"v_fma_f32 v61, v61, v73, v72\n"
"v_cvt_pk_f16_f32 v60, v60, v61\n"
"ds_write2st64_b32 v35, v54, v56 offset0:0 offset1:18\n"
"ds_write2st64_b32 v35, v58, v60 offset0:36 offset1:54\n"
"v_fma_f32 v62, v62, v75, v74\n"
"v_fma_f32 v63, v63, v77, v76\n"
"v_cvt_pk_f16_f32 v62, v62, v63\n"
"v_fma_f32 v64, v64, v75, v74\n"
"v_fma_f32 v65, v65, v77, v76\n"
"v_cvt_pk_f16_f32 v64, v64, v65\n"
"v_fma_f32 v66, v66, v75, v74\n"
"v_fma_f32 v67, v67, v77, v76\n"
"v_cvt_pk_f16_f32 v66, v66, v67\n"
"v_fma_f32 v68, v68, v75, v74\n"
"v_fma_f32 v69, v69, v77, v76\n"
"v_cvt_pk_f16_f32 v68, v68, v69\n"
"ds_write2st64_b32 v35, v62, v64 offset0:4 offset1:22\n"
"ds_write2st64_b32 v35, v66, v68 offset0:40 offset1:58\n"
"ds_write_b32 v13, v13 offset:192\n"
"s_mov_b32 s17, 0x100000\n"
".Ltp_71:\n"
"ds_read_b32 v42, v14 offset:192\n"
"s_sub_u32 s17, s17, 1\n"
"s_cbranch_scc1 .Lou_72\n"
"s_waitcnt lgkmcnt(0)\n"
"v_cmp_eq_u32 vcc, 0, v42\n"
"s_cbranch_vccnz .Ltp_71\n"
".Lou_72:\n"
"s_waitcnt lgkmcnt(0)\n"
"s_mov_b32 s50, 0xfffefffe\n"
"ds_read_b64 v[60:61], v28 offset:1984\n"
"ds_read_b64 v[62:63], v28 offset:1992\n"
"ds_read_b128 v[64:67], v29 offset:1984\n"
"ds_read_b128 v[68:71], v29 offset:1472\n"
"ds_read_b64 v[72:73], v28 offset:1472\n"
"ds_read_b64 v[74:75], v28 offset:1480\n"
"ds_read_b128 v[76:79], v29 offset:960\n"
"s_waitcnt lgkmcnt(1)\n"
"v_mfma_f32_16x16x32_f16 v[56:59], v[60:63], v[64:67], 0\n"
"ds_read_b64 v[80:81], v28 offset:960\n"
"ds_read_b64 v[82:83], v28 offset:968\n"
"v_mfma_f32_16x16x32_f16 v[52:55], v[60:63], v[68:71], 0\n"
"ds_read_b128 v[84:87], v29 offset:448\n"
"ds_read_b64 v[88:89], v28 offset:448\n"
"v_mfma_f32_16x16x32_f16 v[56:59], v[72:75], v[68:71], v[56:59]\n"
"ds_read_b64 v[90:91], v28 offset:456\n"
"s_waitcnt lgkmcnt(3)\n"
"v_mfma_f32_16x16x32_f16 v[48:51], v[60:63], v[76:79], 0\n"
"ds_read_b64 v[92:93], v28 offset:1920\n"
"ds_read_b64 v[94:95], v28 offset:1928\n"
"v_mfma_f32_16x16x32_f16 v[52:55], v[72:75], v[76:79], v[52:55]\n"
"ds_read_b128 v[2:5], v29 offset:1920\n"
"ds_read_b128 v[34:37], v29 offset:1408\n"
"v_mfma_f32_16x16x32_f16 v[56:59], v[80:83], v[76:79], v[56:59]\n"
"ds_read_b64 v[64:65], v28 offset:1408\n"
"s_waitcnt lgkmcnt(7)\n"
"v_mfma_f32_16x16x32_f16 v[44:47], v[60:63], v[84:87], 0\n"
"ds_read_b64 v[66:67], v28 offset:1416\n"
"ds_read_b128 v[68:71], v29 offset:896\n"
"v_mfma_f32_16x16x32_f16 v[48:51], v[72:75], v[84:87], v[48:51]\n"
"ds_read_b64 v[76:77], v28 offset:896\n"
"ds_read_b64 v[78:79], v28 offset:904\n"
"v_mfma_f32_16x16x32_f16 v[52:55], v[80:83], v[84:87], v[52:55]\n"
"ds_read_b128 v[60:63], v29 offset:384\n"
"s_waitcnt lgkmcnt(6)\n"
"v_mfma_f32_16x16x32_f16 v[56:59], v[88:91], v[84:87], v[56:59]\n"
"ds_read_b64 v[72:73], v28 offset:384\n"
"v_mfma_f32_16x16x32_f16 v[56:59], v[92:95], v[2:5], v[56:59]\n"
"ds_read_b64 v[74:75], v28 offset:392\n"
"ds_read_b64 v[80:81], v28 offset:1856\n"
"v_mfma_f32_16x16x32_f16 v[52:55], v[92:95], v[34:37], v[52:55]\n"
"ds_read_b64 v[82:83], v28 offset:1864\n"
"s_waitcnt lgkmcnt(7)\n"
"v_mfma_f32_16x16x32_f16 v[56:59], v[64:67], v[34:37], v[56:59]\n"
"ds_read_b128 v[84:87], v29 offset:1856\n"
"ds_read_b128 v[88:91], v29 offset:1344\n"
"v_mfma_f32_16x16x32_f16 v[48:51], v[92:95], v[68:71], v[48:51]\n"
"ds_read_b64 v[2:3], v30 offset:0\n"
"ds_read_b64 v[34:35], v28 offset:1344\n"
"v_mfma_f32_16x16x32_f16 v[52:55], v[64:67], v[68:71], v[52:55]\n"
"ds_read_b64 v[36:37], v28 offset:1352\n"
"s_waitcnt lgkmcnt(9)\n"
"v_mfma_f32_16x16x32_f16 v[56:59], v[76:79], v[68:71], v[56:59]\n"
"ds_read_b128 v[68:71], v29 offset:832\n"
"v_mfma_f32_16x16x32_f16 v[44:47], v[92:95], v[60:63], v[44:47]\n"
"ds_read_b64 v[92:93], v28 offset:832\n"
"v_mfma_f32_16x16x32_f16 v[48:51], v[64:67], v[60:63], v[48:51]\n"
"ds_read_b64 v[94:95], v28 offset:840\n"
"s_waitcnt lgkmcnt(7)\n"
"v_mfma_f32_16x16x32_f16 v[52:55], v[76:79], v[60:63], v[52:55]\n"
"ds_read_b128 v[64:67], v29 offset:320\n"
"v_mfma_f32_16x16x32_f16 v[56:59], v[72:75], v[60:63], v[56:59]\n"
"v_mfma_f32_16x16x32_f16 v[56:59], v[80:83], v[84:87], v[56:59]\n"
"ds_read_b64 v[76:77], v28 offset:320\n"
"s_waitcnt lgkmcnt(5)\n"
"v_mfma_f32_16x16x32_f16 v[52:55], v[80:83], v[88:91], v[52:55]\n"
"ds_read_b64 v[78:79], v28 offset:328\n"
"s_and_b32 s79, s45, 0xfc\n"
"v_mfma_f32_16x16x16_bf16 v[44:47], v[38:39], v[2:3], v[44:47]\n"
"ds_read_b64 v[60:61], v30 offset:512\n"
"ds_read_b64 v[72:73], v28 offset:1792\n"
"v_mfma_f32_16x16x32_f16 v[56:59], v[34:37], v[88:91], v[56:59]\n"
"ds_read_b64 v[74:75], v28 offset:1800\n"
"s_waitcnt lgkmcnt(6)\n"
"v_mfma_f32_16x16x32_f16 v[48:51], v[80:83], v[68:71], v[48:51]\n"
"ds_read_b128 v[84:87], v29 offset:1792\n"
"ds_read_b128 v[2:5], v29 offset:1280\n"
"v_mfma_f32_16x16x32_f16 v[52:55], v[34:37], v[68:71], v[52:55]\n"
"ds_read_b64 v[88:89], v28 offset:1280\n"
"s_and_b32 s78, s45, 1\n"
"v_mfma_f32_16x16x32_f16 v[56:59], v[92:95], v[68:71], v[56:59]\n"
"ds_read_b64 v[90:91], v28 offset:1288\n"
"s_waitcnt lgkmcnt(9)\n"
"v_mfma_f32_16x16x32_f16 v[44:47], v[80:83], v[64:67], v[44:47]\n"
"ds_read_b128 v[68:71], v29 offset:768\n"
"ds_read_b64 v[80:81], v28 offset:768\n"
"v_mfma_f32_16x16x32_f16 v[48:51], v[34:37], v[64:67], v[48:51]\n"
"ds_read_b64 v[82:83], v28 offset:776\n"
"s_lshl_b32 s78, s78, 1\n"
"v_mfma_f32_16x16x32_f16 v[52:55], v[92:95], v[64:67], v[52:55]\n"
"ds_read_b128 v[34:37], v29 offset:256\n"
"s_waitcnt lgkmcnt(7)\n"
"v_mfma_f32_16x16x32_f16 v[56:59], v[76:79], v[64:67], v[56:59]\n"
"s_add_u32 s79, s79, s78\n"
"s_lshr_b32 s78, s45, 1\n"
"v_mfma_f32_16x16x16_bf16 v[48:51], v[38:39], v[60:61], v[48:51]\n"
"ds_read_b64 v[92:93], v28 offset:256\n"
"s_and_b32 s78, s78, 1\n"
"v_mfma_f32_16x16x32_f16 v[56:59], v[72:75], v[84:87], v[56:59]\n"
"ds_read_b64 v[94:95], v28 offset:264\n"
"s_waitcnt lgkmcnt(5)\n"
"v_mfma_f32_16x16x32_f16 v[52:55], v[72:75], v[2:5], v[52:55]\n"
"ds_read_b64 v[64:65], v28 offset:1728\n"
"ds_read_b64 v[66:67], v28 offset:1736\n"
"v_mfma_f32_16x16x32_f16 v[56:59], v[88:91], v[2:5], v[56:59]\n"
"ds_read_b128 v[76:79], v29 offset:1728\n"
"ds_read_b128 v[60:63], v29 offset:1216\n"
"v_mfma_f32_16x16x32_f16 v[48:51], v[72:75], v[68:71], v[48:51]\n"
"ds_read_b64 v[84:85], v28 offset:1216\n"
"s_waitcnt lgkmcnt(7)\n"
"v_mfma_f32_16x16x32_f16 v[52:55], v[88:91], v[68:71], v[52:55]\n"
"ds_read_b64 v[86:87], v28 offset:1224\n"
"s_add_u32 s79, s79, s78\n"
"v_mfma_f32_16x16x32_f16 v[56:59], v[80:83], v[68:71], v[56:59]\n"
"ds_read_b128 v[2:5], v29 offset:704\n"
"s_lshl_b32 s79, s79, 7\n"
"v_mfma_f32_16x16x32_f16 v[44:47], v[72:75], v[34:37], v[44:47]\n"
"ds_read_b64 v[68:69], v30 offset:1024\n"
"s_waitcnt lgkmcnt(8)\n"
"v_mfma_f32_16x16x32_f16 v[48:51], v[88:91], v[34:37], v[48:51]\n"
"ds_read_b64 v[72:73], v28 offset:704\n"
"ds_read_b64 v[74:75], v28 offset:712\n"
"v_mfma_f32_16x16x32_f16 v[52:55], v[80:83], v[34:37], v[52:55]\n"
"ds_read_b128 v[88:91], v29 offset:192\n"
"s_add_u32 s46, s79, 0xb48000\n"
"v_mfma_f32_16x16x32_f16 v[56:59], v[92:95], v[34:37], v[56:59]\n"
"s_waitcnt lgkmcnt(5)\n"
"v_mfma_f32_16x16x32_f16 v[56:59], v[64:67], v[76:79], v[56:59]\n"
"ds_read_b64 v[80:81], v28 offset:192\n"
"v_mfma_f32_16x16x32_f16 v[52:55], v[64:67], v[60:63], v[52:55]\n"
"ds_read_b64 v[82:83], v28 offset:200\n"
"ds_read_b64 v[34:35], v28 offset:1664\n"
"v_mfma_f32_16x16x32_f16 v[56:59], v[84:87], v[60:63], v[56:59]\n"
"ds_read_b64 v[36:37], v28 offset:1672\n"
"s_waitcnt lgkmcnt(7)\n"
"v_mfma_f32_16x16x32_f16 v[48:51], v[64:67], v[2:5], v[48:51]\n"
"ds_read_b128 v[92:95], v29 offset:1664\n"
"ds_read_b128 v[76:79], v29 offset:1152\n"
"v_mfma_f32_16x16x16_bf16 v[52:55], v[38:39], v[68:69], v[52:55]\n"
"ds_read_b64 v[60:61], v28 offset:1152\n"
"ds_read_b64 v[62:63], v28 offset:1160\n"
"s_nop 3\n"
"v_mfma_f32_16x16x32_f16 v[52:55], v[84:87], v[2:5], v[52:55]\n"
"ds_read_b128 v[68:71], v29 offset:640\n"
"s_waitcnt lgkmcnt(9)\n"
"v_mfma_f32_16x16x32_f16 v[56:59], v[72:75], v[2:5], v[56:59]\n"
"ds_read_b64 v[2:3], v28 offset:640\n"
"v_mfma_f32_16x16x32_f16 v[44:47], v[64:67], v[88:91], v[44:47]\n"
"ds_read_b64 v[4:5], v28 offset:648\n"
"v_mfma_f32_16x16x32_f16 v[48:51], v[84:87], v[88:91], v[48:51]\n"
"ds_read_b128 v[64:67], v29 offset:128\n"
"s_waitcnt lgkmcnt(7)\n"
"v_mfma_f32_16x16x32_f16 v[52:55], v[72:75], v[88:91], v[52:55]\n"
"ds_read_b64 v[84:85], v30 offset:1536\n"
"v_mfma_f32_16x16x32_f16 v[56:59], v[80:83], v[88:91], v[56:59]\n"
"v_mfma_f32_16x16x32_f16 v[56:59], v[34:37], v[92:95], v[56:59]\n"
"ds_read_b64 v[72:73], v28 offset:128\n"
"s_waitcnt lgkmcnt(5)\n"
"v_mfma_f32_16x16x32_f16 v[52:55], v[34:37], v[76:79], v[52:55]\n"
"ds_read_b64 v[74:75], v28 offset:136\n"
"ds_read_b64 v[88:89], v28 offset:1600\n"
"v_mfma_f32_16x16x32_f16 v[56:59], v[60:63], v[76:79], v[56:59]\n"
"ds_read_b64 v[90:91], v28 offset:1608\n"
"ds_read_b128 v[80:83], v29 offset:1600\n"
"v_mfma_f32_16x16x32_f16 v[48:51], v[34:37], v[68:71], v[48:51]\n"
"ds_read_b128 v[92:95], v29 offset:1088\n"
"s_waitcnt lgkmcnt(7)\n"
"v_mfma_f32_16x16x32_f16 v[52:55], v[60:63], v[68:71], v[52:55]\n"
"ds_read_b64 v[76:77], v28 offset:1088\n"
"v_mfma_f32_16x16x32_f16 v[56:59], v[2:5], v[68:71], v[56:59]\n"
"ds_read_b64 v[78:79], v28 offset:1096\n"
"v_mfma_f32_16x16x32_f16 v[44:47], v[34:37], v[64:67], v[44:47]\n"
"ds_read_b128 v[68:71], v29 offset:576\n"
"s_waitcnt lgkmcnt(9)\n"
"s_nop 0\n"
"v_mfma_f32_16x16x16_bf16 v[56:59], v[38:39], v[84:85], v[56:59]\n"
"ds_read_b64 v[34:35], v28 offset:576\n"
"ds_read_b64 v[36:37], v28 offset:584\n"
"v_mfma_f32_16x16x32_f16 v[48:51], v[60:63], v[64:67], v[48:51]\n"
"ds_read_b128 v[84:87], v29 offset:64\n"
"v_mfma_f32_16x16x32_f16 v[52:55], v[2:5], v[64:67], v[52:55]\n"
"s_waitcnt lgkmcnt(6)\n"
"v_mfma_f32_16x16x32_f16 v[56:59], v[72:75], v[64:67], v[56:59]\n"
"ds_read_b64 v[60:61], v28 offset:64\n"
"v_mfma_f32_16x16x32_f16 v[56:59], v[88:91], v[80:83], v[56:59]\n"
"ds_read_b64 v[62:63], v28 offset:72\n"
"ds_read_b64 v[2:3], v28 offset:1536\n"
"v_mfma_f32_16x16x32_f16 v[52:55], v[88:91], v[92:95], v[52:55]\n"
"ds_read_b64 v[4:5], v28 offset:1544\n"
"s_waitcnt lgkmcnt(7)\n"
"v_mfma_f32_16x16x32_f16 v[56:59], v[76:79], v[92:95], v[56:59]\n"
"ds_read_b128 v[64:67], v29 offset:0\n"
"ds_read_b128 v[72:75], v29 offset:512\n"
"v_mfma_f32_16x16x32_f16 v[48:51], v[88:91], v[68:71], v[48:51]\n"
"ds_read_b64 v[80:81], v28 offset:1024\n"
"ds_read_b64 v[82:83], v28 offset:1032\n"
"v_mfma_f32_16x16x32_f16 v[52:55], v[76:79], v[68:71], v[52:55]\n"
"ds_read_b128 v[92:95], v29 offset:1024\n"
"s_waitcnt lgkmcnt(9)\n"
"v_mfma_f32_16x16x32_f16 v[56:59], v[34:37], v[68:71], v[56:59]\n"
"ds_read_b64 v[68:69], v28 offset:512\n"
"v_mfma_f32_16x16x32_f16 v[44:47], v[88:91], v[84:87], v[44:47]\n"
"ds_read_b64 v[70:71], v28 offset:520\n"
"v_mfma_f32_16x16x32_f16 v[48:51], v[76:79], v[84:87], v[48:51]\n"
"ds_read_b128 v[88:91], v29 offset:1536\n"
"s_waitcnt lgkmcnt(7)\n"
"v_mfma_f32_16x16x32_f16 v[52:55], v[34:37], v[84:87], v[52:55]\n"
"v_mfma_f32_16x16x32_f16 v[56:59], v[60:63], v[84:87], v[56:59]\n"
"ds_read_b64 v[76:77], v28 offset:0\n"
"v_mfma_f32_16x16x32_f16 v[44:47], v[2:5], v[64:67], v[44:47]\n"
"ds_read_b64 v[78:79], v28 offset:8\n"
"s_waitcnt lgkmcnt(5)\n"
"v_mfma_f32_16x16x32_f16 v[48:51], v[2:5], v[72:75], v[48:51]\n"
"v_mfma_f32_16x16x32_f16 v[48:51], v[80:83], v[64:67], v[48:51]\n"
"v_mfma_f32_16x16x32_f16 v[52:55], v[2:5], v[92:95], v[52:55]\n"
"v_mul_f32 v44, v40, v44\n"
"v_mul_f32 v45, v40, v45\n"
"v_mul_f32 v46, v40, v46\n"
"s_waitcnt lgkmcnt(2)\n"
"v_mfma_f32_16x16x32_f16 v[52:55], v[80:83], v[72:75], v[52:55]\n"
"v_mul_f32 v47, v40, v47\n"
"v_cvt_pk_bf16_f32 v44, v44, v45\n"
"v_cvt_pk_bf16_f32 v45, v46, v47\n"
"v_mfma_f32_16x16x32_f16 v[52:55], v[68:71], v[64:67], v[52:55]\n"
"v_pk_min_u16 v44, v44, s50\n"
"v_pk_min_u16 v45, v45, s50\n"
"ds_write_b64 v30, v[44:45] offset:0\n"
"v_mfma_f32_16x16x32_f16 v[56:59], v[2:5], v[88:91], v[56:59]\n"
"s_waitcnt lgkmcnt(1)\n"
"v_mfma_f32_16x16x32_f16 v[56:59], v[80:83], v[92:95], v[56:59]\n"
"v_mul_f32 v48, v40, v48\n"
"v_mul_f32 v49, v40, v49\n"
"v_mfma_f32_16x16x32_f16 v[56:59], v[68:71], v[72:75], v[56:59]\n"
"v_mul_f32 v50, v40, v50\n"
"v_mul_f32 v51, v40, v51\n"
"v_mfma_f32_16x16x32_f16 v[56:59], v[76:79], v[64:67], v[56:59]\n"
"v_cvt_pk_bf16_f32 v48, v48, v49\n"
"v_cvt_pk_bf16_f32 v49, v50, v51\n"
"v_pk_min_u16 v48, v48, s50\n"
"v_pk_min_u16 v49, v49, s50\n"
"ds_write_b64 v30, v[48:49] offset:512\n"
"v_mul_f32 v52, v40, v52\n"
"v_mul_f32 v53, v40, v53\n"
"v_mul_f32 v54, v40, v54\n"
"v_mul_f32 v55, v40, v55\n"
"v_cvt_pk_bf16_f32 v52, v52, v53\n"
"v_cvt_pk_bf16_f32 v53, v54, v55\n"
"v_pk_min_u16 v52, v52, s50\n"
"v_pk_min_u16 v53, v53, s50\n"
"ds_write_b64 v30, v[52:53] offset:1024\n"
"v_mul_f32 v56, v40, v56\n"
"v_mul_f32 v57, v40, v57\n"
"v_mul_f32 v58, v40, v58\n"
"v_mul_f32 v59, v40, v59\n"
"v_cvt_pk_bf16_f32 v56, v56, v57\n"
"v_cvt_pk_bf16_f32 v57, v58, v59\n"
"v_pk_min_u16 v56, v56, s50\n"
"v_pk_min_u16 v57, v57, s50\n"
"ds_write_b64 v30, v[56:57] offset:1536\n"
"ds_write_b32 v13, v13 offset:208\n"
"s_mov_b32 s17, 0x100000\n"
".Ltp_73:\n"
"ds_read_b32 v42, v14 offset:208\n"
"s_sub_u32 s17, s17, 1\n"
"s_cbranch_scc1 .Lou_74\n"
"s_waitcnt lgkmcnt(0)\n"
"v_cmp_eq_u32 vcc, 0, v42\n"
"s_cbranch_vccnz .Ltp_73\n"
".Lou_74:\n"
"s_waitcnt lgkmcnt(0)\n"
"ds_read_b64_tr_b16 v[60:61], v31 offset:0\n"
"ds_read_b64_tr_b16 v[62:63], v32 offset:0\n"
"ds_read_b64_tr_b16 v[64:65], v31 offset:1024\n"
"ds_read_b64_tr_b16 v[66:67], v32 offset:1024\n"
"s_add_u32 s47, s46, 0xa0000\n"
"s_waitcnt lgkmcnt(2)\n"
"buffer_store_dwordx4 v[60:63], v33, s[4:7], s46 offen sc1\n"
"s_waitcnt lgkmcnt(0)\n"
"buffer_store_dwordx4 v[64:67], v33, s[4:7], s47 offen sc1\n"
"ds_write_b32 v13, v13 offset:224\n"
"s_setprio 1\n"
"s_mov_b32 s17, 0x100000\n"
".Ltp_75:\n"
"ds_read_b32 v42, v14 offset:224\n"
"s_sub_u32 s17, s17, 1\n"
"s_cbranch_scc1 .Lou_76\n"
"s_waitcnt lgkmcnt(0)\n"
"v_cmp_eq_u32 vcc, 0, v42\n"
"s_cbranch_vccnz .Ltp_75\n"
".Lou_76:\n"
"s_waitcnt lgkmcnt(0)\n"
"s_nop 2\n"
".Lnc_56:\n"
"v_and_b32 v35, 15, v0\n"
"v_and_b32 v34, 3, v35\n"
"v_and_b32 v33, 4, v35\n"
"v_lshrrev_b32 v32, 4, v0\n"
"v_and_b32 v31, 1, v32\n"
"v_lshlrev_b32 v30, 7, v31\n"
"v_lshlrev_b32 v29, 1, v33\n"
"v_lshlrev_b32 v28, 4, v34\n"
"v_lshrrev_b32 v79, 3, v35\n"
"v_lshrrev_b32 v78, 1, v32\n"
"v_lshl_add_u32 v78, v78, 9, v30\n"
"v_add_u32 v78, v78, v28\n"
"v_lshl_add_u32 v79, v79, 6, v78\n"
"s_lshl_b32 s79, s43, 10\n"
"v_add3_u32 v3, v79, v29, s79\n"
"v_lshrrev_b32 v79, 3, v35\n"
"v_lshrrev_b32 v78, 1, v32\n"
"v_lshl_add_u32 v78, v78, 9, v30\n"
"v_add_u32 v78, v78, v28\n"
"v_lshl_add_u32 v79, v79, 6, v78\n"
"s_lshl_b32 s79, s43, 10\n"
"s_add_u32 s79, s79, 0xfffff400\n"
"s_lshl_b32 s78, s43, 10\n"
"s_add_u32 s78, s78, 0x4400\n"
"s_cmp_ge_i32 s43, 3\n"
"s_cselect_b32 s79, s79, s78\n"
"v_add3_u32 v4, v79, v29, s79\n"
"v_lshrrev_b32 v79, 3, v35\n"
"v_lshrrev_b32 v78, 1, v32\n"
"v_lshl_add_u32 v78, v78, 9, v30\n"
"v_add_u32 v78, v78, v28\n"
"v_lshl_add_u32 v79, v79, 6, v78\n"
"s_lshl_b32 s79, s43, 10\n"
"s_add_u32 s79, s79, 0xfffff800\n"
"s_lshl_b32 s78, s43, 10\n"
"s_add_u32 s78, s78, 0x4800\n"
"s_cmp_ge_i32 s43, 2\n"
"s_cselect_b32 s79, s79, s78\n"
"v_add3_u32 v5, v79, v29, s79\n"
"v_lshrrev_b32 v79, 3, v35\n"
"v_lshrrev_b32 v78, 1, v32\n"
"v_lshl_add_u32 v78, v78, 9, v30\n"
"v_add_u32 v78, v78, v28\n"
"v_lshl_add_u32 v79, v79, 6, v78\n"
"s_lshl_b32 s79, s43, 10\n"
"s_add_u32 s79, s79, 0xfffffc00\n"
"s_lshl_b32 s78, s43, 10\n"
"s_add_u32 s78, s78, 0x4c00\n"
"s_cmp_ge_i32 s43, 1\n"
"s_cselect_b32 s79, s79, s78\n"
"v_add3_u32 v6, v79, v29, s79\n"
"s_mul_i32 s79, s43, 0x1400\n"
"v_lshl_add_u32 v79, v0, 7, s79\n"
"v_mov_b32 v78, 0x40000\n"
"v_cmp_gt_i32 vcc, 40, v0\n"
"s_nop 1\n"
"v_cndmask_b32 v93, v78, v79, vcc\n"
"s_mov_b32 s17, 0x100000\n"
".Ltp_77:\n"
"ds_read_b32 v90, v14 offset:144\n"
"s_sub_u32 s17, s17, 1\n"
"s_cbranch_scc1 .Lou_78\n"
"s_waitcnt lgkmcnt(0)\n"
"v_cmp_eq_u32 vcc, 0, v90\n"
"s_cbranch_vccnz .Ltp_77\n"
".Lou_78:\n"
"s_waitcnt lgkmcnt(0)\n"
"s_cmp_ge_i32 s45, 0\n"
"s_cbranch_scc1 .Lnw_79\n"
"s_mov_b32 s17, 0x100000\n"
".Ltp_80:\n"
"s_sleep 1\n"
"ds_read_b32 v90, v14 offset:160\n"
"s_sub_u32 s17, s17, 1\n"
"s_cbranch_scc1 .Lou_81\n"
"s_waitcnt lgkmcnt(0)\n"
"v_cmp_eq_u32 vcc, 0, v90\n"
"s_cbranch_vccnz .Ltp_80\n"
".Lou_81:\n"
"s_waitcnt lgkmcnt(0)\n"
"s_sleep 20\n"
"s_and_b32 s79, s2, 7\n"
"s_lshl_b32 s79, s79, 14\n"
"s_add_u32 s46, s79, 0xb28000\n"
"s_add_u32 s47, s46, 0x1000\n"
"buffer_load_dwordx4 v[28:31], v1, s[4:7], s46 offen offset:0 sc1\n"
"buffer_load_dwordx4 v[32:35], v1, s[4:7], s46 offen offset:1024 sc1\n"
"buffer_load_dwordx4 v[36:39], v1, s[4:7], s46 offen offset:2048 sc1\n"
"buffer_load_dwordx4 v[40:43], v1, s[4:7], s46 offen offset:3072 sc1\n"
"buffer_load_dwordx4 v[44:47], v1, s[4:7], s47 offen offset:0 sc1\n"
"buffer_load_dwordx4 v[48:51], v1, s[4:7], s47 offen offset:1024 sc1\n"
"buffer_load_dwordx4 v[52:55], v1, s[4:7], s47 offen offset:2048 sc1\n"
"buffer_load_dwordx4 v[56:59], v1, s[4:7], s47 offen offset:3072 sc1\n"
"s_mov_b32 s17, 32768\n"
".Ltp_82:\n"
"s_waitcnt vmcnt(0)\n"
"s_sub_u32 s17, s17, 1\n"
"s_cbranch_scc1 .Lou_83\n"
"v_max_u32 v60, v28, v29\n"
"v_max3_u32 v60, v30, v31, v60\n"
"v_max3_u32 v60, v32, v33, v60\n"
"v_max3_u32 v60, v34, v35, v60\n"
"v_max3_u32 v60, v36, v37, v60\n"
"v_max3_u32 v60, v38, v39, v60\n"
"v_max3_u32 v60, v40, v41, v60\n"
"v_max3_u32 v60, v42, v43, v60\n"
"v_max3_u32 v60, v44, v45, v60\n"
"v_max3_u32 v60, v46, v47, v60\n"
"v_max3_u32 v60, v48, v49, v60\n"
"v_max3_u32 v60, v50, v51, v60\n"
"v_max3_u32 v60, v52, v53, v60\n"
"v_max3_u32 v60, v54, v55, v60\n"
"v_max3_u32 v60, v56, v57, v60\n"
"v_max3_u32 v60, v58, v59, v60\n"
"v_cmp_eq_u32 vcc, -1, v60\n"
"s_cbranch_vccz .Lou_83\n"
"s_sleep 4\n"
"v_max_u32 v60, v30, v31\n"
"v_max3_u32 v60, v28, v29, v60\n"
"v_cmp_eq_u32 vcc, -1, v60\n"
"s_cbranch_vccz .Lsk_84\n"
"s_mov_b64 exec, vcc\n"
"buffer_load_dwordx4 v[28:31], v1, s[4:7], s46 offen offset:0 sc1\n"
"s_mov_b64 exec, -1\n"
".Lsk_84:\n"
"v_max_u32 v60, v34, v35\n"
"v_max3_u32 v60, v32, v33, v60\n"
"v_cmp_eq_u32 vcc, -1, v60\n"
"s_cbranch_vccz .Lsk_85\n"
"s_mov_b64 exec, vcc\n"
"buffer_load_dwordx4 v[32:35], v1, s[4:7], s46 offen offset:1024 sc1\n"
"s_mov_b64 exec, -1\n"
".Lsk_85:\n"
"v_max_u32 v60, v38, v39\n"
"v_max3_u32 v60, v36, v37, v60\n"
"v_cmp_eq_u32 vcc, -1, v60\n"
"s_cbranch_vccz .Lsk_86\n"
"s_mov_b64 exec, vcc\n"
"buffer_load_dwordx4 v[36:39], v1, s[4:7], s46 offen offset:2048 sc1\n"
"s_mov_b64 exec, -1\n"
".Lsk_86:\n"
"v_max_u32 v60, v42, v43\n"
"v_max3_u32 v60, v40, v41, v60\n"
"v_cmp_eq_u32 vcc, -1, v60\n"
"s_cbranch_vccz .Lsk_87\n"
"s_mov_b64 exec, vcc\n"
"buffer_load_dwordx4 v[40:43], v1, s[4:7], s46 offen offset:3072 sc1\n"
"s_mov_b64 exec, -1\n"
".Lsk_87:\n"
"v_max_u32 v60, v46, v47\n"
"v_max3_u32 v60, v44, v45, v60\n"
"v_cmp_eq_u32 vcc, -1, v60\n"
"s_cbranch_vccz .Lsk_88\n"
"s_mov_b64 exec, vcc\n"
"buffer_load_dwordx4 v[44:47], v1, s[4:7], s47 offen offset:0 sc1\n"
"s_mov_b64 exec, -1\n"
".Lsk_88:\n"
"v_max_u32 v60, v50, v51\n"
"v_max3_u32 v60, v48, v49, v60\n"
"v_cmp_eq_u32 vcc, -1, v60\n"
"s_cbranch_vccz .Lsk_89\n"
"s_mov_b64 exec, vcc\n"
"buffer_load_dwordx4 v[48:51], v1, s[4:7], s47 offen offset:1024 sc1\n"
"s_mov_b64 exec, -1\n"
".Lsk_89:\n"
"v_max_u32 v60, v54, v55\n"
"v_max3_u32 v60, v52, v53, v60\n"
"v_cmp_eq_u32 vcc, -1, v60\n"
"s_cbranch_vccz .Lsk_90\n"
"s_mov_b64 exec, vcc\n"
"buffer_load_dwordx4 v[52:55], v1, s[4:7], s47 offen offset:2048 sc1\n"
"s_mov_b64 exec, -1\n"
".Lsk_90:\n"
"v_max_u32 v60, v58, v59\n"
"v_max3_u32 v60, v56, v57, v60\n"
"v_cmp_eq_u32 vcc, -1, v60\n"
"s_cbranch_vccz .Lsk_91\n"
"s_mov_b64 exec, vcc\n"
"buffer_load_dwordx4 v[56:59], v1, s[4:7], s47 offen offset:3072 sc1\n"
"s_mov_b64 exec, -1\n"
".Lsk_91:\n"
"s_branch .Ltp_82\n"
".Lou_83:\n"
"s_waitcnt vmcnt(0)\n"
"s_sleep 20\n"
".Lnw_79:\n"
"s_sleep 20\n"
"v_min_i32 v79, 7, v0\n"
"v_mul_i32_i24 v28, 0x280, v79\n"
"s_and_b32 s79, s2, 7\n"
"s_lshl_b32 s79, s79, 3\n"
"s_add_u32 s79, s79, 7\n"
"s_mul_i32 s79, s79, 0x5000\n"
"s_and_b32 s78, s2, 7\n"
"s_lshr_b32 s78, s78, 1\n"
"s_lshl_b32 s78, s78, 7\n"
"s_add_u32 s79, s79, s78\n"
"s_add_u32 s79, s79, s29\n"
"s_add_u32 s46, s79, 0xb48000\n"
"s_mov_b32 s17, 32768\n"
".Ltp_92:\n"
"buffer_load_dword v29, v28, s[4:7], s46 offen sc1\n"
"s_sub_u32 s17, s17, 1\n"
"s_cbranch_scc1 .Lou_93\n"
"s_waitcnt vmcnt(0)\n"
"v_cmp_eq_u32 vcc, -1, v29\n"
"s_cbranch_vccz .Lou_93\n"
"s_sleep 2\n"
"s_branch .Ltp_92\n"
".Lou_93:\n"
"s_waitcnt vmcnt(0)\n"
"s_mov_b32 s54, 0\n"
"s_nop 0\n"
".Lag_94:\n"
"v_mov_b32 v92, 0\n"
"s_mov_b32 s46, s44\n"
"s_add_u32 s47, s46, 0x1000\n"
"v_add_u32 v2, s29, v1\n"
"buffer_load_dwordx4 v[28:31], v1, s[4:7], s46 offen offset:0 sc1\n"
"buffer_load_dwordx4 v[32:35], v1, s[4:7], s46 offen offset:1024 sc1\n"
"buffer_load_dwordx4 v[36:39], v1, s[4:7], s46 offen offset:2048 sc1\n"
"buffer_load_dwordx4 v[40:43], v1, s[4:7], s46 offen offset:3072 sc1\n"
"buffer_load_dwordx4 v[44:47], v1, s[4:7], s47 offen offset:0 sc1\n"
"s_mov_b32 s17, 32768\n"
".Ltp_96:\n"
"s_waitcnt vmcnt(0)\n"
"s_sub_u32 s17, s17, 1\n"
"s_cbranch_scc1 .Lou_97\n"
"v_max_u32 v48, v28, v29\n"
"v_max3_u32 v48, v30, v31, v48\n"
"v_max3_u32 v48, v32, v33, v48\n"
"v_max3_u32 v48, v34, v35, v48\n"
"v_max3_u32 v48, v36, v37, v48\n"
"v_max3_u32 v48, v38, v39, v48\n"
"v_max3_u32 v48, v40, v41, v48\n"
"v_max3_u32 v48, v42, v43, v48\n"
"v_max3_u32 v48, v44, v45, v48\n"
"v_max3_u32 v48, v46, v47, v48\n"
"v_cmp_eq_u32 vcc, -1, v48\n"
"s_cbranch_vccz .Lou_97\n"
"s_sleep 1\n"
"v_max_u32 v48, v30, v31\n"
"v_max3_u32 v48, v28, v29, v48\n"
"v_cmp_eq_u32 vcc, -1, v48\n"
"s_cbranch_vccz .Lsk_98\n"
"s_mov_b64 exec, vcc\n"
"buffer_load_dwordx4 v[28:31], v1, s[4:7], s46 offen offset:0 sc1\n"
"s_mov_b64 exec, -1\n"
".Lsk_98:\n"
"v_max_u32 v48, v34, v35\n"
"v_max3_u32 v48, v32, v33, v48\n"
"v_cmp_eq_u32 vcc, -1, v48\n"
"s_cbranch_vccz .Lsk_99\n"
"s_mov_b64 exec, vcc\n"
"buffer_load_dwordx4 v[32:35], v1, s[4:7], s46 offen offset:1024 sc1\n"
"s_mov_b64 exec, -1\n"
".Lsk_99:\n"
"v_max_u32 v48, v38, v39\n"
"v_max3_u32 v48, v36, v37, v48\n"
"v_cmp_eq_u32 vcc, -1, v48\n"
"s_cbranch_vccz .Lsk_100\n"
"s_mov_b64 exec, vcc\n"
"buffer_load_dwordx4 v[36:39], v1, s[4:7], s46 offen offset:2048 sc1\n"
"s_mov_b64 exec, -1\n"
".Lsk_100:\n"
"v_max_u32 v48, v42, v43\n"
"v_max3_u32 v48, v40, v41, v48\n"
"v_cmp_eq_u32 vcc, -1, v48\n"
"s_cbranch_vccz .Lsk_101\n"
"s_mov_b64 exec, vcc\n"
"buffer_load_dwordx4 v[40:43], v1, s[4:7], s46 offen offset:3072 sc1\n"
"s_mov_b64 exec, -1\n"
".Lsk_101:\n"
"v_max_u32 v48, v46, v47\n"
"v_max3_u32 v48, v44, v45, v48\n"
"v_cmp_eq_u32 vcc, -1, v48\n"
"s_cbranch_vccz .Lsk_102\n"
"s_mov_b64 exec, vcc\n"
"buffer_load_dwordx4 v[44:47], v1, s[4:7], s47 offen offset:0 sc1\n"
"s_mov_b64 exec, -1\n"
".Lsk_102:\n"
"s_branch .Ltp_96\n"
".Lou_97:\n"
"s_waitcnt vmcnt(0)\n"
"s_add_u32 m0, s29, 0x5000\n"
"s_add_u32 s31, s44, 0x5000\n"
"buffer_load_dwordx4 v1, s[4:7], s31 offen offset:0 sc1 lds\n"
"ds_write_b128 v2, v[28:31] offset:0\n"
"buffer_load_dwordx4 v1, s[4:7], s31 offen offset:1024 sc1 lds\n"
"ds_write_b128 v2, v[32:35] offset:1024\n"
"buffer_load_dwordx4 v1, s[4:7], s31 offen offset:2048 sc1 lds\n"
"ds_write_b128 v2, v[36:39] offset:2048\n"
"buffer_load_dwordx4 v1, s[4:7], s31 offen offset:3072 sc1 lds\n"
"ds_write_b128 v2, v[40:43] offset:3072\n"
"s_add_u32 m0, m0, 1024\n"
"s_add_u32 s31, s31, 1024\n"
"buffer_load_dwordx4 v1, s[4:7], s31 offen offset:3072 sc1 lds\n"
"ds_write_b128 v2, v[44:47] offset:4096\n"
"ds_write_b32 v13, v13 offset:240\n"
"s_mov_b32 s17, 0x100000\n"
".Ltp_103:\n"
"ds_read_b32 v90, v14 offset:240\n"
"s_sub_u32 s17, s17, 1\n"
"s_cbranch_scc1 .Lou_104\n"
"s_waitcnt lgkmcnt(0)\n"
"v_cmp_eq_u32 vcc, 0, v90\n"
"s_cbranch_vccnz .Ltp_103\n"
".Lou_104:\n"
"s_waitcnt lgkmcnt(0)\n"
"ds_read_b64 v[56:57], v3 offset:0\n"
"ds_read_b64 v[58:59], v3 offset:256\n"
"ds_read_b64 v[60:61], v3 offset:1024\n"
"ds_read_b64 v[62:63], v3 offset:1280\n"
"ds_read_b64 v[64:65], v3 offset:2048\n"
"ds_read_b64 v[66:67], v3 offset:2304\n"
"ds_read_b64 v[68:69], v3 offset:3072\n"
"ds_read_b64 v[70:71], v3 offset:3328\n"
"ds_read_b64 v[72:73], v3 offset:4096\n"
"ds_read_b64 v[74:75], v3 offset:4352\n"
"s_waitcnt lgkmcnt(6)\n"
"v_mfma_f32_16x16x32_bf16 v[28:31], v[96:99], v[56:59], 0\n"
"ds_read_b64 v[76:77], v3 offset:5120\n"
"s_add_u32 m0, s29, 0xa000\n"
"v_mfma_f32_16x16x32_bf16 v[32:35], v[100:103], v[56:59], 0\n"
"ds_read_b64 v[78:79], v3 offset:5376\n"
"s_add_u32 s31, s44, 0xa000\n"
"v_mfma_f32_16x16x32_bf16 v[52:55], v[56:59], v[56:59], 0\n"
"v_mfma_f32_16x16x32_bf16 v[28:31], v[104:107], v[60:63], v[28:31]\n"
"ds_read_b64 v[56:57], v3 offset:6144\n"
"buffer_load_dwordx4 v1, s[4:7], s31 offen offset:0 sc1 lds\n"
"v_mfma_f32_16x16x32_bf16 v[32:35], v[108:111], v[60:63], v[32:35]\n"
"ds_read_b64 v[58:59], v3 offset:6400\n"
"s_waitcnt lgkmcnt(6)\n"
"v_mfma_f32_16x16x32_bf16 v[28:31], v[112:115], v[64:67], v[28:31]\n"
"ds_read_b64 v[60:61], v3 offset:7168\n"
"v_mfma_f32_16x16x32_bf16 v[32:35], v[116:119], v[64:67], v[32:35]\n"
"ds_read_b64 v[62:63], v3 offset:7424\n"
"v_mfma_f32_16x16x32_bf16 v[28:31], v[120:123], v[68:71], v[28:31]\n"
"ds_read_b64 v[64:65], v3 offset:8192\n"
"buffer_load_dwordx4 v1, s[4:7], s31 offen offset:1024 sc1 lds\n"
"v_mfma_f32_16x16x32_bf16 v[32:35], v[124:127], v[68:71], v[32:35]\n"
"ds_read_b64 v[66:67], v3 offset:8448\n"
"s_waitcnt lgkmcnt(6)\n"
"v_mfma_f32_16x16x32_bf16 v[28:31], v[128:131], v[72:75], v[28:31]\n"
"ds_read_b64 v[68:69], v3 offset:9216\n"
"v_mfma_f32_16x16x32_bf16 v[32:35], v[132:135], v[72:75], v[32:35]\n"
"ds_read_b64 v[70:71], v3 offset:9472\n"
"v_mfma_f32_16x16x32_bf16 v[52:55], v[72:75], v[72:75], v[52:55]\n"
"buffer_load_dwordx4 v1, s[4:7], s31 offen offset:2048 sc1 lds\n"
"v_mfma_f32_16x16x32_bf16 v[28:31], v[136:139], v[76:79], v[28:31]\n"
"ds_read_b64 v[72:73], v3 offset:10240\n"
"v_mfma_f32_16x16x32_bf16 v[32:35], v[140:143], v[76:79], v[32:35]\n"
"ds_read_b64 v[74:75], v3 offset:10496\n"
"s_waitcnt lgkmcnt(6)\n"
"v_mfma_f32_16x16x32_bf16 v[28:31], v[144:147], v[56:59], v[28:31]\n"
"ds_read_b64 v[76:77], v3 offset:11264\n"
"v_mfma_f32_16x16x32_bf16 v[32:35], v[148:151], v[56:59], v[32:35]\n"
"ds_read_b64 v[78:79], v3 offset:11520\n"
"buffer_load_dwordx4 v1, s[4:7], s31 offen offset:3072 sc1 lds\n"
"v_mfma_f32_16x16x32_bf16 v[28:31], v[152:155], v[60:63], v[28:31]\n"
"ds_read_b64 v[56:57], v3 offset:12288\n"
"s_add_u32 m0, m0, 1024\n"
"v_mfma_f32_16x16x32_bf16 v[32:35], v[156:159], v[60:63], v[32:35]\n"
"ds_read_b64 v[58:59], v3 offset:12544\n"
"s_waitcnt lgkmcnt(6)\n"
"v_mfma_f32_16x16x32_bf16 v[28:31], v[160:163], v[64:67], v[28:31]\n"
"ds_read_b64 v[60:61], v3 offset:13312\n"
"s_add_u32 s31, s31, 1024\n"
"v_mfma_f32_16x16x32_bf16 v[32:35], v[164:167], v[64:67], v[32:35]\n"
"ds_read_b64 v[62:63], v3 offset:13568\n"
"buffer_load_dwordx4 v1, s[4:7], s31 offen offset:3072 sc1 lds\n"
"v_mfma_f32_16x16x32_bf16 v[52:55], v[64:67], v[64:67], v[52:55]\n"
"s_waitcnt vmcnt(5)\n"
"ds_write_b32 v13, v13 offset:256\n"
"v_mfma_f32_16x16x32_bf16 v[28:31], v[168:171], v[68:71], v[28:31]\n"
"ds_read_b64 v[64:65], v3 offset:14336\n"
"s_add_u32 m0, s29, 0xf000\n"
"s_add_u32 s31, s44, 0xf000\n"
"v_mfma_f32_16x16x32_bf16 v[32:35], v[172:175], v[68:71], v[32:35]\n"
"ds_read_b64 v[66:67], v3 offset:14592\n"
"s_waitcnt lgkmcnt(7)\n"
"v_mfma_f32_16x16x32_bf16 v[28:31], v[176:179], v[72:75], v[28:31]\n"
"ds_read_b64 v[68:69], v3 offset:15360\n"
"buffer_load_dwordx4 v1, s[4:7], s31 offen offset:0 sc1 lds\n"
"v_mfma_f32_16x16x32_bf16 v[32:35], v[180:183], v[72:75], v[32:35]\n"
"ds_read_b64 v[70:71], v3 offset:15616\n"
"v_mfma_f32_16x16x32_bf16 v[28:31], v[184:187], v[76:79], v[28:31]\n"
"ds_read_b64 v[72:73], v3 offset:16384\n"
"v_mfma_f32_16x16x32_bf16 v[32:35], v[188:191], v[76:79], v[32:35]\n"
"ds_read_b64 v[74:75], v3 offset:16640\n"
"s_waitcnt lgkmcnt(7)\n"
"v_mfma_f32_16x16x32_bf16 v[28:31], v[192:195], v[56:59], v[28:31]\n"
"ds_read_b64 v[76:77], v4 offset:0\n"
"buffer_load_dwordx4 v1, s[4:7], s31 offen offset:1024 sc1 lds\n"
"v_mfma_f32_16x16x32_bf16 v[32:35], v[196:199], v[56:59], v[32:35]\n"
"ds_read_b64 v[78:79], v4 offset:256\n"
"ds_read_b32 v90, v14 offset:256\n"
"v_mfma_f32_16x16x32_bf16 v[52:55], v[56:59], v[56:59], v[52:55]\n"
"v_mfma_f32_16x16x32_bf16 v[28:31], v[200:203], v[60:63], v[28:31]\n"
"ds_read_b64 v[56:57], v5 offset:0\n"
"v_mfma_f32_16x16x32_bf16 v[32:35], v[204:207], v[60:63], v[32:35]\n"
"ds_read_b64 v[58:59], v5 offset:256\n"
"buffer_load_dwordx4 v1, s[4:7], s31 offen offset:2048 sc1 lds\n"
"s_waitcnt lgkmcnt(7)\n"
"v_mfma_f32_16x16x32_bf16 v[28:31], v[208:211], v[64:67], v[28:31]\n"
"ds_read_b64 v[60:61], v6 offset:0\n"
"s_waitcnt lgkmcnt(3)\n"
"v_cmp_eq_u32 vcc, 0, v90\n"
"v_mfma_f32_16x16x32_bf16 v[32:35], v[212:215], v[64:67], v[32:35]\n"
"ds_read_b64 v[62:63], v6 offset:256\n"
"v_mfma_f32_16x16x32_bf16 v[28:31], v[216:219], v[68:71], v[28:31]\n"
"s_cbranch_vccnz .Lsl_105\n"
".Lbk_106:\n"
"ds_read_b64 v[64:65], v3 offset:20480\n"
"v_mfma_f32_16x16x32_bf16 v[32:35], v[220:223], v[68:71], v[32:35]\n"
"ds_read_b64 v[66:67], v3 offset:20736\n"
"buffer_load_dwordx4 v1, s[4:7], s31 offen offset:3072 sc1 lds\n"
"v_mfma_f32_16x16x32_bf16 v[28:31], v[224:227], v[72:75], v[28:31]\n"
"ds_read_b64 v[68:69], v3 offset:21504\n"
"s_add_u32 m0, m0, 1024\n"
"v_mfma_f32_16x16x32_bf16 v[32:35], v[228:231], v[72:75], v[32:35]\n"
"ds_read_b64 v[70:71], v3 offset:21760\n"
"s_add_u32 s31, s31, 1024\n"
"v_mfma_f32_16x16x32_bf16 v[52:55], v[72:75], v[72:75], v[52:55]\n"
"v_mfma_f32_16x16x32_bf16 v[28:31], v[232:235], v[76:79], v[28:31]\n"
"ds_read_b64 v[72:73], v3 offset:22528\n"
"buffer_load_dwordx4 v1, s[4:7], s31 offen offset:3072 sc1 lds\n"
"v_mfma_f32_16x16x32_bf16 v[32:35], v[236:239], v[76:79], v[32:35]\n"
"ds_read_b64 v[74:75], v3 offset:22784\n"
"s_waitcnt lgkmcnt(6)\n"
"v_mfma_f32_16x16x32_bf16 v[28:31], v[240:243], v[56:59], v[28:31]\n"
"ds_read_b64 v[76:77], v3 offset:23552\n"
"v_cndmask_b32 v94, v52, v53, s[34:35]\n"
"v_mfma_f32_16x16x32_bf16 v[32:35], v[244:247], v[56:59], v[32:35]\n"
"ds_read_b64 v[78:79], v3 offset:23808\n"
"v_cndmask_b32 v54, v54, v55, s[34:35]\n"
"v_mfma_f32_16x16x32_bf16 v[28:31], v[248:251], v[60:63], v[28:31]\n"
"ds_read_b64 v[56:57], v3 offset:24576\n"
"v_cndmask_b32 v94, v94, v54, s[20:21]\n"
"v_mfma_f32_16x16x32_bf16 v[32:35], v[252:255], v[60:63], v[32:35]\n"
"ds_read_b64 v[58:59], v3 offset:24832\n"
"s_waitcnt lgkmcnt(6)\n"
"v_mfma_f32_16x16x32_bf16 v[36:39], v[96:99], v[64:67], 0\n"
"ds_read_b64 v[60:61], v3 offset:25600\n"
"v_mfma_f32_16x16x32_bf16 v[40:43], v[100:103], v[64:67], 0\n"
"ds_read_b64 v[62:63], v3 offset:25856\n"
"v_cndmask_b32 v94, v28, v94, s[22:23]\n"
"s_waitcnt vmcnt(5)\n"
"v_mfma_f32_16x16x32_bf16 v[52:55], v[64:67], v[64:67], 0\n"
"ds_write_b32 v9, v94 offset:1856\n"
"v_mfma_f32_16x16x32_bf16 v[36:39], v[104:107], v[68:71], v[36:39]\n"
"ds_read_b64 v[64:65], v3 offset:26624\n"
"v_mfma_f32_16x16x32_bf16 v[40:43], v[108:111], v[68:71], v[40:43]\n"
"ds_read_b64 v[66:67], v3 offset:26880\n"
"s_waitcnt lgkmcnt(7)\n"
"v_mfma_f32_16x16x32_bf16 v[36:39], v[112:115], v[72:75], v[36:39]\n"
"ds_read_b64 v[68:69], v3 offset:27648\n"
"v_mfma_f32_16x16x32_bf16 v[40:43], v[116:119], v[72:75], v[40:43]\n"
"ds_read_b64 v[70:71], v3 offset:27904\n"
"v_mfma_f32_16x16x32_bf16 v[36:39], v[120:123], v[76:79], v[36:39]\n"
"ds_read_b64 v[72:73], v3 offset:28672\n"
"v_mfma_f32_16x16x32_bf16 v[40:43], v[124:127], v[76:79], v[40:43]\n"
"ds_read_b64 v[74:75], v3 offset:28928\n"
"s_waitcnt lgkmcnt(7)\n"
"v_mfma_f32_16x16x32_bf16 v[36:39], v[128:131], v[56:59], v[36:39]\n"
"ds_read_b64 v[76:77], v3 offset:29696\n"
"v_mfma_f32_16x16x32_bf16 v[40:43], v[132:135], v[56:59], v[40:43]\n"
"ds_read_b64 v[78:79], v3 offset:29952\n"
"v_mfma_f32_16x16x32_bf16 v[52:55], v[56:59], v[56:59], v[52:55]\n"
"ds_read_b128 v[84:87], v11 offset:1856\n"
"ds_read_b32 v91, v12 offset:1856\n"
"v_mfma_f32_16x16x32_bf16 v[36:39], v[136:139], v[60:63], v[36:39]\n"
"ds_read_b64 v[56:57], v3 offset:30720\n"
"v_mfma_f32_16x16x32_bf16 v[40:43], v[140:143], v[60:63], v[40:43]\n"
"ds_read_b64 v[58:59], v3 offset:30976\n"
"s_waitcnt lgkmcnt(8)\n"
"v_mfma_f32_16x16x32_bf16 v[36:39], v[144:147], v[64:67], v[36:39]\n"
"ds_read_b64 v[60:61], v3 offset:31744\n"
"v_mfma_f32_16x16x32_bf16 v[40:43], v[148:151], v[64:67], v[40:43]\n"
"ds_read_b64 v[62:63], v3 offset:32000\n"
"v_mfma_f32_16x16x32_bf16 v[36:39], v[152:155], v[68:71], v[36:39]\n"
"ds_read_b64 v[64:65], v3 offset:32768\n"
"v_mfma_f32_16x16x32_bf16 v[40:43], v[156:159], v[68:71], v[40:43]\n"
"ds_read_b64 v[66:67], v3 offset:33024\n"
"s_waitcnt lgkmcnt(8)\n"
"v_mfma_f32_16x16x32_bf16 v[36:39], v[160:163], v[72:75], v[36:39]\n"
"ds_read_b64 v[68:69], v3 offset:33792\n"
"s_waitcnt lgkmcnt(8)\n"
"v_add_f32 v84, v84, v85\n"
"v_add_f32 v86, v86, v87\n"
"v_mfma_f32_16x16x32_bf16 v[40:43], v[164:167], v[72:75], v[40:43]\n"
"ds_read_b64 v[70:71], v3 offset:34048\n"
"v_add_f32 v84, v84, v86\n"
"s_waitcnt lgkmcnt(8)\n"
"v_mul_f32 v89, 0x3acccccd, v91\n"
"v_mfma_f32_16x16x32_bf16 v[52:55], v[72:75], v[72:75], v[52:55]\n"
"v_fma_f32 v84, -v91, v89, v84\n"
"v_fmamk_f32 v84, v84, 0x3acccccd, v27\n"
"v_mfma_f32_16x16x32_bf16 v[36:39], v[168:171], v[76:79], v[36:39]\n"
"ds_read_b64 v[72:73], v3 offset:34816\n"
"v_cmp_u_f32 vcc, v84, v84\n"
"v_mfma_f32_16x16x32_bf16 v[40:43], v[172:175], v[76:79], v[40:43]\n"
"ds_read_b64 v[74:75], v3 offset:35072\n"
"s_waitcnt lgkmcnt(6)\n"
"v_mfma_f32_16x16x32_bf16 v[36:39], v[176:179], v[56:59], v[36:39]\n"
"ds_read_b64 v[76:77], v3 offset:35840\n"
"v_max_f32 v84, v84, v27\n"
"v_mfma_f32_16x16x32_bf16 v[40:43], v[180:183], v[56:59], v[40:43]\n"
"ds_read_b64 v[78:79], v3 offset:36096\n"
"v_rsq_f32 v88, v84\n"
"v_mfma_f32_16x16x32_bf16 v[36:39], v[184:187], v[60:63], v[36:39]\n"
"ds_read_b64 v[56:57], v3 offset:36864\n"
"v_mul_f32 v89, -v89, v88\n"
"v_mfma_f32_16x16x32_bf16 v[40:43], v[188:191], v[60:63], v[40:43]\n"
"ds_read_b64 v[58:59], v3 offset:37120\n"
"s_cbranch_vccnz .Lsl_109\n"
".Lbk_110:\n"
"s_waitcnt lgkmcnt(6)\n"
"v_mfma_f32_16x16x32_bf16 v[36:39], v[192:195], v[64:67], v[36:39]\n"
"ds_read_b64 v[60:61], v4 offset:20480\n"
"s_add_u32 m0, s29, 0\n"
"v_mul_f32 v28, v88, v28\n"
"v_mfma_f32_16x16x32_bf16 v[40:43], v[196:199], v[64:67], v[40:43]\n"
"ds_read_b64 v[62:63], v4 offset:20736\n"
"s_sub_u32 s79, s44, 0x5000\n"
"v_mul_f32 v29, v88, v29\n"
"v_mfma_f32_16x16x32_bf16 v[52:55], v[64:67], v[64:67], v[52:55]\n"
"s_add_u32 s78, s29, 0xb48000\n"
"s_max_i32 s31, s79, s78\n"
"v_mul_f32 v30, v88, v30\n"
"v_mfma_f32_16x16x32_bf16 v[36:39], v[200:203], v[68:71], v[36:39]\n"
"ds_read_b64 v[64:65], v5 offset:20480\n"
"buffer_load_dwordx4 v1, s[4:7], s31 offen offset:0 sc1 lds\n"
"v_mul_f32 v31, v88, v31\n"
"v_mfma_f32_16x16x32_bf16 v[40:43], v[204:207], v[68:71], v[40:43]\n"
"ds_read_b64 v[66:67], v5 offset:20736\n"
"s_waitcnt lgkmcnt(6)\n"
"v_mfma_f32_16x16x32_bf16 v[36:39], v[208:211], v[72:75], v[36:39]\n"
"ds_read_b64 v[68:69], v6 offset:20480\n"
"v_fmac_f32 v28, v89, v16\n"
"v_mfma_f32_16x16x32_bf16 v[40:43], v[212:215], v[72:75], v[40:43]\n"
"ds_read_b64 v[70:71], v6 offset:20736\n"
"v_fmac_f32 v29, v89, v17\n"
"v_mfma_f32_16x16x32_bf16 v[36:39], v[216:219], v[76:79], v[36:39]\n"
"ds_read_b64 v[72:73], v3 offset:40960\n"
"buffer_load_dwordx4 v1, s[4:7], s31 offen offset:1024 sc1 lds\n"
"v_mfma_f32_16x16x32_bf16 v[40:43], v[220:223], v[76:79], v[40:43]\n"
"ds_read_b64 v[74:75], v3 offset:41216\n"
"s_waitcnt lgkmcnt(6)\n"
"v_mfma_f32_16x16x32_bf16 v[36:39], v[224:227], v[56:59], v[36:39]\n"
"ds_read_b64 v[76:77], v3 offset:41984\n"
"v_fmac_f32 v30, v89, v18\n"
"v_mfma_f32_16x16x32_bf16 v[40:43], v[228:231], v[56:59], v[40:43]\n"
"ds_read_b64 v[78:79], v3 offset:42240\n"
"v_fmac_f32 v31, v89, v19\n"
"v_mfma_f32_16x16x32_bf16 v[52:55], v[56:59], v[56:59], v[52:55]\n"
"buffer_load_dwordx4 v1, s[4:7], s31 offen offset:2048 sc1 lds\n"
"v_cvt_pk_bf16_f32 v28, v28, v29\n"
"v_mfma_f32_16x16x32_bf16 v[36:39], v[232:235], v[60:63], v[36:39]\n"
"ds_read_b64 v[56:57], v3 offset:43008\n"
"v_cvt_pk_bf16_f32 v29, v30, v31\n"
"v_mfma_f32_16x16x32_bf16 v[40:43], v[236:239], v[60:63], v[40:43]\n"
"ds_read_b64 v[58:59], v3 offset:43264\n"
"s_waitcnt lgkmcnt(6)\n"
"v_mfma_f32_16x16x32_bf16 v[36:39], v[240:243], v[64:67], v[36:39]\n"
"ds_read_b64 v[60:61], v3 offset:44032\n"
"v_cndmask_b32 v94, v52, v53, s[34:35]\n"
"v_mfma_f32_16x16x32_bf16 v[40:43], v[244:247], v[64:67], v[40:43]\n"
"ds_read_b64 v[62:63], v3 offset:44288\n"
"v_cndmask_b32 v54, v54, v55, s[34:35]\n"
"buffer_load_dwordx4 v1, s[4:7], s31 offen offset:3072 sc1 lds\n"
"v_mfma_f32_16x16x32_bf16 v[36:39], v[248:251], v[68:71], v[36:39]\n"
"ds_read_b64 v[64:65], v3 offset:45056\n"
"v_cndmask_b32 v94, v94, v54, s[20:21]\n"
"s_add_u32 m0, m0, 1024\n"
"v_mfma_f32_16x16x32_bf16 v[40:43], v[252:255], v[68:71], v[40:43]\n"
"ds_read_b64 v[66:67], v3 offset:45312\n"
"s_waitcnt lgkmcnt(6)\n"
"v_mfma_f32_16x16x32_bf16 v[44:47], v[96:99], v[72:75], 0\n"
"ds_read_b64 v[68:69], v3 offset:46080\n"
"s_add_u32 s31, s31, 1024\n"
"v_mfma_f32_16x16x32_bf16 v[48:51], v[100:103], v[72:75], 0\n"
"ds_read_b64 v[70:71], v3 offset:46336\n"
"v_cndmask_b32 v94, v36, v94, s[22:23]\n"
"s_waitcnt vmcnt(4)\n"
"buffer_load_dwordx4 v1, s[4:7], s31 offen offset:3072 sc1 lds\n"
"v_mfma_f32_16x16x32_bf16 v[52:55], v[72:75], v[72:75], 0\n"
"ds_write_b32 v9, v94 offset:2176\n"
"v_mfma_f32_16x16x32_bf16 v[44:47], v[104:107], v[76:79], v[44:47]\n"
"ds_read_b64 v[72:73], v3 offset:47104\n"
"v_mul_f32 v32, v88, v32\n"
"v_mfma_f32_16x16x32_bf16 v[48:51], v[108:111], v[76:79], v[48:51]\n"
"ds_read_b64 v[74:75], v3 offset:47360\n"
"s_waitcnt lgkmcnt(7)\n"
"v_mfma_f32_16x16x32_bf16 v[44:47], v[112:115], v[56:59], v[44:47]\n"
"ds_read_b64 v[76:77], v3 offset:48128\n"
"v_mul_f32 v33, v88, v33\n"
"v_mfma_f32_16x16x32_bf16 v[48:51], v[116:119], v[56:59], v[48:51]\n"
"ds_read_b64 v[78:79], v3 offset:48384\n"
"v_mul_f32 v34, v88, v34\n"
"v_mfma_f32_16x16x32_bf16 v[44:47], v[120:123], v[60:63], v[44:47]\n"
"ds_read_b64 v[56:57], v3 offset:49152\n"
"v_mul_f32 v35, v88, v35\n"
"v_mfma_f32_16x16x32_bf16 v[48:51], v[124:127], v[60:63], v[48:51]\n"
"ds_read_b64 v[58:59], v3 offset:49408\n"
"s_waitcnt lgkmcnt(7)\n"
"v_mfma_f32_16x16x32_bf16 v[44:47], v[128:131], v[64:67], v[44:47]\n"
"ds_read_b64 v[60:61], v3 offset:50176\n"
"v_fmac_f32 v32, v89, v20\n"
"v_mfma_f32_16x16x32_bf16 v[48:51], v[132:135], v[64:67], v[48:51]\n"
"ds_read_b64 v[62:63], v3 offset:50432\n"
"v_fmac_f32 v33, v89, v21\n"
"v_mfma_f32_16x16x32_bf16 v[52:55], v[64:67], v[64:67], v[52:55]\n"
"ds_read_b128 v[84:87], v11 offset:2176\n"
"ds_read_b32 v91, v12 offset:2176\n"
"v_mfma_f32_16x16x32_bf16 v[44:47], v[136:139], v[68:71], v[44:47]\n"
"ds_read_b64 v[64:65], v3 offset:51200\n"
"v_fmac_f32 v34, v89, v22\n"
"v_mfma_f32_16x16x32_bf16 v[48:51], v[140:143], v[68:71], v[48:51]\n"
"ds_read_b64 v[66:67], v3 offset:51456\n"
"s_waitcnt lgkmcnt(8)\n"
"v_mfma_f32_16x16x32_bf16 v[44:47], v[144:147], v[72:75], v[44:47]\n"
"ds_read_b64 v[68:69], v3 offset:52224\n"
"v_fmac_f32 v35, v89, v23\n"
"v_mfma_f32_16x16x32_bf16 v[48:51], v[148:151], v[72:75], v[48:51]\n"
"ds_read_b64 v[70:71], v3 offset:52480\n"
"v_cvt_pk_bf16_f32 v32, v32, v33\n"
"v_mfma_f32_16x16x32_bf16 v[44:47], v[152:155], v[76:79], v[44:47]\n"
"ds_read_b64 v[72:73], v3 offset:53248\n"
"v_cvt_pk_bf16_f32 v33, v34, v35\n"
"v_mfma_f32_16x16x32_bf16 v[48:51], v[156:159], v[76:79], v[48:51]\n"
"ds_read_b64 v[74:75], v3 offset:53504\n"
"s_waitcnt lgkmcnt(8)\n"
"v_mfma_f32_16x16x32_bf16 v[44:47], v[160:163], v[56:59], v[44:47]\n"
"ds_read_b64 v[76:77], v3 offset:54272\n"
"s_waitcnt lgkmcnt(8)\n"
"v_add_f32 v84, v84, v85\n"
"v_add_f32 v86, v86, v87\n"
"v_mfma_f32_16x16x32_bf16 v[48:51], v[164:167], v[56:59], v[48:51]\n"
"ds_read_b64 v[78:79], v3 offset:54528\n"
"v_add_f32 v84, v84, v86\n"
"s_waitcnt lgkmcnt(8)\n"
"v_mul_f32 v89, 0x3acccccd, v91\n"
"v_mfma_f32_16x16x32_bf16 v[52:55], v[56:59], v[56:59], v[52:55]\n"
"v_fma_f32 v84, -v91, v89, v84\n"
"v_fmamk_f32 v84, v84, 0x3acccccd, v27\n"
"v_mfma_f32_16x16x32_bf16 v[44:47], v[168:171], v[60:63], v[44:47]\n"
"ds_read_b64 v[56:57], v3 offset:55296\n"
"v_cmp_u_f32 vcc, v84, v84\n"
"v_mfma_f32_16x16x32_bf16 v[48:51], v[172:175], v[60:63], v[48:51]\n"
"ds_read_b64 v[58:59], v3 offset:55552\n"
"s_waitcnt lgkmcnt(6)\n"
"v_mfma_f32_16x16x32_bf16 v[44:47], v[176:179], v[64:67], v[44:47]\n"
"ds_read_b64 v[60:61], v3 offset:56320\n"
"v_max_f32 v84, v84, v27\n"
"v_mfma_f32_16x16x32_bf16 v[48:51], v[180:183], v[64:67], v[48:51]\n"
"ds_read_b64 v[62:63], v3 offset:56576\n"
"v_rsq_f32 v88, v84\n"
"v_mfma_f32_16x16x32_bf16 v[44:47], v[184:187], v[68:71], v[44:47]\n"
"ds_read_b64 v[64:65], v3 offset:57344\n"
"v_mul_f32 v89, -v89, v88\n"
"v_mfma_f32_16x16x32_bf16 v[48:51], v[188:191], v[68:71], v[48:51]\n"
"ds_read_b64 v[66:67], v3 offset:57600\n"
"s_cbranch_vccnz .Lsl_113\n"
".Lbk_114:\n"
"s_waitcnt lgkmcnt(6)\n"
"v_mfma_f32_16x16x32_bf16 v[44:47], v[192:195], v[72:75], v[44:47]\n"
"ds_read_b64 v[68:69], v4 offset:40960\n"
"v_mul_f32 v36, v88, v36\n"
"v_mul_f32 v37, v88, v37\n"
"v_mfma_f32_16x16x32_bf16 v[48:51], v[196:199], v[72:75], v[48:51]\n"
"ds_read_b64 v[70:71], v4 offset:41216\n"
"v_mul_f32 v38, v88, v38\n"
"v_mul_f32 v39, v88, v39\n"
"v_mfma_f32_16x16x32_bf16 v[52:55], v[72:75], v[72:75], v[52:55]\n"
"ds_write_b64 v7, v[28:29] offset:3968\n"
"v_mfma_f32_16x16x32_bf16 v[44:47], v[200:203], v[76:79], v[44:47]\n"
"ds_read_b64 v[72:73], v5 offset:40960\n"
"v_fmac_f32 v36, v89, v16\n"
"v_mfma_f32_16x16x32_bf16 v[48:51], v[204:207], v[76:79], v[48:51]\n"
"ds_read_b64 v[74:75], v5 offset:41216\n"
"s_waitcnt lgkmcnt(7)\n"
"v_mfma_f32_16x16x32_bf16 v[44:47], v[208:211], v[56:59], v[44:47]\n"
"ds_read_b64 v[76:77], v6 offset:40960\n"
"v_fmac_f32 v37, v89, v17\n"
"v_mfma_f32_16x16x32_bf16 v[48:51], v[212:215], v[56:59], v[48:51]\n"
"ds_read_b64 v[78:79], v6 offset:41216\n"
"v_fmac_f32 v38, v89, v18\n"
"v_mfma_f32_16x16x32_bf16 v[44:47], v[216:219], v[60:63], v[44:47]\n"
"v_add_u32 v3, 0x5000, v3\n"
"ds_read_b64 v[56:57], v3 offset:40960\n"
"v_mfma_f32_16x16x32_bf16 v[48:51], v[220:223], v[60:63], v[48:51]\n"
"ds_read_b64 v[58:59], v3 offset:41216\n"
"s_waitcnt lgkmcnt(7)\n"
"v_mfma_f32_16x16x32_bf16 v[44:47], v[224:227], v[64:67], v[44:47]\n"
"ds_read_b64 v[60:61], v3 offset:41984\n"
"v_fmac_f32 v39, v89, v19\n"
"v_mfma_f32_16x16x32_bf16 v[48:51], v[228:231], v[64:67], v[48:51]\n"
"ds_read_b64 v[62:63], v3 offset:42240\n"
"v_mov_b32 v84, 0x3f803f80\n"
"v_mfma_f32_16x16x32_bf16 v[52:55], v[64:67], v[64:67], v[52:55]\n"
"v_mov_b32 v85, 0x3f803f80\n"
"ds_write_b64 v8, v[32:33] offset:3968\n"
"v_mfma_f32_16x16x32_bf16 v[44:47], v[232:235], v[68:71], v[44:47]\n"
"ds_read_b64 v[64:65], v3 offset:43008\n"
"v_cvt_pk_bf16_f32 v36, v36, v37\n"
"v_mfma_f32_16x16x32_bf16 v[48:51], v[236:239], v[68:71], v[48:51]\n"
"ds_read_b64 v[66:67], v3 offset:43264\n"
"s_waitcnt lgkmcnt(7)\n"
"v_mfma_f32_16x16x32_bf16 v[44:47], v[240:243], v[72:75], v[44:47]\n"
"ds_read_b64 v[68:69], v3 offset:44032\n"
"v_cndmask_b32 v94, v52, v53, s[34:35]\n"
"v_mfma_f32_16x16x32_bf16 v[48:51], v[244:247], v[72:75], v[48:51]\n"
"ds_read_b64 v[70:71], v3 offset:44288\n"
"v_cndmask_b32 v54, v54, v55, s[34:35]\n"
"v_mfma_f32_16x16x32_bf16 v[44:47], v[248:251], v[76:79], v[44:47]\n"
"ds_read_b64 v[72:73], v3 offset:45056\n"
"v_cndmask_b32 v94, v94, v54, s[20:21]\n"
"v_mfma_f32_16x16x32_bf16 v[48:51], v[252:255], v[76:79], v[48:51]\n"
"ds_read_b64 v[74:75], v3 offset:45312\n"
"s_waitcnt lgkmcnt(7)\n"
"v_mfma_f32_16x16x32_bf16 v[28:31], v[96:99], v[56:59], 0\n"
"ds_read_b64 v[76:77], v3 offset:46080\n"
"v_cvt_pk_bf16_f32 v37, v38, v39\n"
"v_mfma_f32_16x16x32_bf16 v[32:35], v[100:103], v[56:59], 0\n"
"ds_read_b64 v[78:79], v3 offset:46336\n"
"v_cndmask_b32 v94, v44, v94, s[22:23]\n"
"s_waitcnt vmcnt(0)\n"
"ds_write_b64 v93, v[84:85] offset:0\n"
"v_mfma_f32_16x16x32_bf16 v[52:55], v[56:59], v[56:59], 0\n"
"ds_write_b32 v9, v94 offset:2496\n"
"v_mfma_f32_16x16x32_bf16 v[28:31], v[104:107], v[60:63], v[28:31]\n"
"ds_read_b64 v[56:57], v3 offset:47104\n"
"v_mul_f32 v40, v88, v40\n"
"v_mfma_f32_16x16x32_bf16 v[32:35], v[108:111], v[60:63], v[32:35]\n"
"ds_read_b64 v[58:59], v3 offset:47360\n"
"s_waitcnt lgkmcnt(8)\n"
"v_mfma_f32_16x16x32_bf16 v[28:31], v[112:115], v[64:67], v[28:31]\n"
"ds_read_b64 v[60:61], v3 offset:48128\n"
"v_mul_f32 v41, v88, v41\n"
"v_mfma_f32_16x16x32_bf16 v[32:35], v[116:119], v[64:67], v[32:35]\n"
"ds_read_b64 v[62:63], v3 offset:48384\n"
"v_mul_f32 v42, v88, v42\n"
"v_mfma_f32_16x16x32_bf16 v[28:31], v[120:123], v[68:71], v[28:31]\n"
"ds_read_b64 v[64:65], v3 offset:49152\n"
"v_mul_f32 v43, v88, v43\n"
"v_mfma_f32_16x16x32_bf16 v[32:35], v[124:127], v[68:71], v[32:35]\n"
"ds_read_b64 v[66:67], v3 offset:49408\n"
"s_waitcnt lgkmcnt(8)\n"
"v_mfma_f32_16x16x32_bf16 v[28:31], v[128:131], v[72:75], v[28:31]\n"
"ds_read_b64 v[68:69], v3 offset:50176\n"
"v_fmac_f32 v40, v89, v20\n"
"v_mfma_f32_16x16x32_bf16 v[32:35], v[132:135], v[72:75], v[32:35]\n"
"ds_read_b64 v[70:71], v3 offset:50432\n"
"v_fmac_f32 v41, v89, v21\n"
"v_mfma_f32_16x16x32_bf16 v[52:55], v[72:75], v[72:75], v[52:55]\n"
"ds_read_b128 v[84:87], v11 offset:2496\n"
"ds_read_b32 v91, v12 offset:2496\n"
"v_mfma_f32_16x16x32_bf16 v[28:31], v[136:139], v[76:79], v[28:31]\n"
"ds_read_b64 v[72:73], v3 offset:51200\n"
"v_fmac_f32 v42, v89, v22\n"
"v_mfma_f32_16x16x32_bf16 v[32:35], v[140:143], v[76:79], v[32:35]\n"
"ds_read_b64 v[74:75], v3 offset:51456\n"
"s_waitcnt lgkmcnt(8)\n"
"v_mfma_f32_16x16x32_bf16 v[28:31], v[144:147], v[56:59], v[28:31]\n"
"ds_read_b64 v[76:77], v3 offset:52224\n"
"v_fmac_f32 v43, v89, v23\n"
"v_mfma_f32_16x16x32_bf16 v[32:35], v[148:151], v[56:59], v[32:35]\n"
"ds_read_b64 v[78:79], v3 offset:52480\n"
"v_cvt_pk_bf16_f32 v40, v40, v41\n"
"v_mfma_f32_16x16x32_bf16 v[28:31], v[152:155], v[60:63], v[28:31]\n"
"ds_read_b64 v[56:57], v3 offset:53248\n"
"v_cvt_pk_bf16_f32 v41, v42, v43\n"
"v_mfma_f32_16x16x32_bf16 v[32:35], v[156:159], v[60:63], v[32:35]\n"
"ds_read_b64 v[58:59], v3 offset:53504\n"
"s_waitcnt lgkmcnt(8)\n"
"v_mfma_f32_16x16x32_bf16 v[28:31], v[160:163], v[64:67], v[28:31]\n"
"ds_read_b64 v[60:61], v3 offset:54272\n"
"s_waitcnt lgkmcnt(8)\n"
"v_add_f32 v84, v84, v85\n"
"v_add_f32 v86, v86, v87\n"
"v_mfma_f32_16x16x32_bf16 v[32:35], v[164:167], v[64:67], v[32:35]\n"
"ds_read_b64 v[62:63], v3 offset:54528\n"
"v_add_f32 v84, v84, v86\n"
"s_waitcnt lgkmcnt(8)\n"
"v_mul_f32 v89, 0x3acccccd, v91\n"
"v_mfma_f32_16x16x32_bf16 v[52:55], v[64:67], v[64:67], v[52:55]\n"
"v_fma_f32 v84, -v91, v89, v84\n"
"v_fmamk_f32 v84, v84, 0x3acccccd, v27\n"
"v_mfma_f32_16x16x32_bf16 v[28:31], v[168:171], v[68:71], v[28:31]\n"
"ds_read_b64 v[64:65], v3 offset:55296\n"
"v_cmp_u_f32 vcc, v84, v84\n"
"v_mfma_f32_16x16x32_bf16 v[32:35], v[172:175], v[68:71], v[32:35]\n"
"ds_read_b64 v[66:67], v3 offset:55552\n"
"s_waitcnt lgkmcnt(6)\n"
"v_mfma_f32_16x16x32_bf16 v[28:31], v[176:179], v[72:75], v[28:31]\n"
"ds_read_b64 v[68:69], v3 offset:56320\n"
"v_max_f32 v84, v84, v27\n"
"v_mfma_f32_16x16x32_bf16 v[32:35], v[180:183], v[72:75], v[32:35]\n"
"ds_read_b64 v[70:71], v3 offset:56576\n"
"v_rsq_f32 v88, v84\n"
"v_mfma_f32_16x16x32_bf16 v[28:31], v[184:187], v[76:79], v[28:31]\n"
"ds_read_b64 v[72:73], v3 offset:57344\n"
"v_mul_f32 v89, -v89, v88\n"
"v_mfma_f32_16x16x32_bf16 v[32:35], v[188:191], v[76:79], v[32:35]\n"
"ds_read_b64 v[74:75], v3 offset:57600\n"
"s_cbranch_vccnz .Lsl_117\n"
".Lbk_118:\n"
"s_waitcnt lgkmcnt(6)\n"
"v_mfma_f32_16x16x32_bf16 v[28:31], v[192:195], v[56:59], v[28:31]\n"
"ds_read_b64 v[76:77], v4 offset:61440\n"
"v_mul_f32 v44, v88, v44\n"
"v_mul_f32 v45, v88, v45\n"
"v_mfma_f32_16x16x32_bf16 v[32:35], v[196:199], v[56:59], v[32:35]\n"
"ds_read_b64 v[78:79], v4 offset:61696\n"
"v_mul_f32 v46, v88, v46\n"
"v_mul_f32 v47, v88, v47\n"
"v_mfma_f32_16x16x32_bf16 v[52:55], v[56:59], v[56:59], v[52:55]\n"
"ds_write_b64 v7, v[36:37] offset:7936\n"
"v_mfma_f32_16x16x32_bf16 v[28:31], v[200:203], v[60:63], v[28:31]\n"
"ds_read_b64 v[56:57], v5 offset:61440\n"
"v_fmac_f32 v44, v89, v16\n"
"v_fmac_f32 v45, v89, v17\n"
"v_mfma_f32_16x16x32_bf16 v[32:35], v[204:207], v[60:63], v[32:35]\n"
"ds_read_b64 v[58:59], v5 offset:61696\n"
"v_fmac_f32 v46, v89, v18\n"
"s_waitcnt lgkmcnt(7)\n"
"v_mfma_f32_16x16x32_bf16 v[28:31], v[208:211], v[64:67], v[28:31]\n"
"ds_read_b64 v[60:61], v6 offset:61440\n"
"v_fmac_f32 v47, v89, v19\n"
"v_cvt_pk_bf16_f32 v44, v44, v45\n"
"v_mfma_f32_16x16x32_bf16 v[32:35], v[212:215], v[64:67], v[32:35]\n"
"ds_read_b64 v[62:63], v6 offset:61696\n"
"v_cvt_pk_bf16_f32 v45, v46, v47\n"
"v_mul_f32 v48, v88, v48\n"
"v_mfma_f32_16x16x32_bf16 v[28:31], v[216:219], v[68:71], v[28:31]\n"
"v_add_u32 v3, 0xffffb000, v3\n"
"ds_read_b64 v[64:65], v3 offset:0\n"
"v_mul_f32 v49, v88, v49\n"
"v_mfma_f32_16x16x32_bf16 v[32:35], v[220:223], v[68:71], v[32:35]\n"
"ds_read_b64 v[66:67], v3 offset:256\n"
"v_mul_f32 v50, v88, v50\n"
"s_waitcnt lgkmcnt(7)\n"
"v_mfma_f32_16x16x32_bf16 v[28:31], v[224:227], v[72:75], v[28:31]\n"
"ds_read_b64 v[68:69], v3 offset:4096\n"
"v_mul_f32 v51, v88, v51\n"
"v_fmac_f32 v48, v89, v20\n"
"v_mfma_f32_16x16x32_bf16 v[32:35], v[228:231], v[72:75], v[32:35]\n"
"ds_read_b64 v[70:71], v3 offset:4352\n"
"v_fmac_f32 v49, v89, v21\n"
"v_mfma_f32_16x16x32_bf16 v[52:55], v[72:75], v[72:75], v[52:55]\n"
"ds_write_b64 v8, v[40:41] offset:7936\n"
"v_mfma_f32_16x16x32_bf16 v[28:31], v[232:235], v[76:79], v[28:31]\n"
"ds_read_b64 v[72:73], v3 offset:8192\n"
"v_fmac_f32 v50, v89, v22\n"
"v_mfma_f32_16x16x32_bf16 v[32:35], v[236:239], v[76:79], v[32:35]\n"
"ds_read_b64 v[74:75], v3 offset:8448\n"
"s_waitcnt lgkmcnt(7)\n"
"v_mfma_f32_16x16x32_bf16 v[28:31], v[240:243], v[56:59], v[28:31]\n"
"ds_read_b64 v[76:77], v3 offset:12288\n"
"v_cndmask_b32 v94, v52, v53, s[34:35]\n"
"v_mfma_f32_16x16x32_bf16 v[32:35], v[244:247], v[56:59], v[32:35]\n"
"ds_read_b64 v[78:79], v3 offset:12544\n"
"v_cndmask_b32 v54, v54, v55, s[34:35]\n"
"v_mfma_f32_16x16x32_bf16 v[28:31], v[248:251], v[60:63], v[28:31]\n"
"ds_read_b64 v[56:57], v3 offset:16384\n"
"v_cndmask_b32 v94, v94, v54, s[20:21]\n"
"v_mfma_f32_16x16x32_bf16 v[32:35], v[252:255], v[60:63], v[32:35]\n"
"ds_read_b64 v[58:59], v3 offset:16640\n"
"s_waitcnt lgkmcnt(7)\n"
"v_mfma_f32_16x16x32_bf16 v[36:39], v[96:99], v[64:67], 0\n"
"ds_read_b64 v[60:61], v3 offset:1024\n"
"v_fmac_f32 v51, v89, v23\n"
"v_mfma_f32_16x16x32_bf16 v[40:43], v[100:103], v[64:67], 0\n"
"ds_read_b64 v[62:63], v3 offset:1280\n"
"v_cndmask_b32 v94, v28, v94, s[22:23]\n"
"v_mfma_f32_16x16x32_bf16 v[52:55], v[64:67], v[64:67], 0\n"
"ds_write_b32 v9, v94 offset:2816\n"
"v_mfma_f32_16x16x32_bf16 v[36:39], v[128:131], v[68:71], v[36:39]\n"
"ds_read_b64 v[64:65], v3 offset:2048\n"
"v_cvt_pk_bf16_f32 v48, v48, v49\n"
"v_mfma_f32_16x16x32_bf16 v[40:43], v[132:135], v[68:71], v[40:43]\n"
"ds_read_b64 v[66:67], v3 offset:2304\n"
"v_cvt_pk_bf16_f32 v49, v50, v51\n"
"v_mfma_f32_16x16x32_bf16 v[52:55], v[68:71], v[68:71], v[52:55]\n"
"ds_write_b64 v7, v[44:45] offset:11904\n"
"s_waitcnt lgkmcnt(8)\n"
"v_mfma_f32_16x16x32_bf16 v[36:39], v[160:163], v[72:75], v[36:39]\n"
"ds_read_b64 v[68:69], v3 offset:3072\n"
"v_mfma_f32_16x16x32_bf16 v[40:43], v[164:167], v[72:75], v[40:43]\n"
"ds_read_b64 v[70:71], v3 offset:3328\n"
"v_mfma_f32_16x16x32_bf16 v[52:55], v[72:75], v[72:75], v[52:55]\n"
"ds_write_b64 v8, v[48:49] offset:11904\n"
"v_mfma_f32_16x16x32_bf16 v[36:39], v[192:195], v[76:79], v[36:39]\n"
"ds_read_b64 v[72:73], v3 offset:5120\n"
"v_mfma_f32_16x16x32_bf16 v[40:43], v[196:199], v[76:79], v[40:43]\n"
"ds_read_b64 v[74:75], v3 offset:5376\n"
"v_mfma_f32_16x16x32_bf16 v[52:55], v[76:79], v[76:79], v[52:55]\n"
"ds_read_b128 v[84:87], v11 offset:2816\n"
"ds_read_b32 v91, v12 offset:2816\n"
"s_waitcnt lgkmcnt(11)\n"
"v_mfma_f32_16x16x32_bf16 v[36:39], v[224:227], v[56:59], v[36:39]\n"
"ds_read_b64 v[76:77], v3 offset:6144\n"
"v_mfma_f32_16x16x32_bf16 v[40:43], v[228:231], v[56:59], v[40:43]\n"
"ds_read_b64 v[78:79], v3 offset:6400\n"
"v_mfma_f32_16x16x32_bf16 v[52:55], v[56:59], v[56:59], v[52:55]\n"
"v_mfma_f32_16x16x32_bf16 v[36:39], v[104:107], v[60:63], v[36:39]\n"
"ds_read_b64 v[56:57], v3 offset:7168\n"
"v_mfma_f32_16x16x32_bf16 v[40:43], v[108:111], v[60:63], v[40:43]\n"
"ds_read_b64 v[58:59], v3 offset:7424\n"
"s_waitcnt lgkmcnt(9)\n"
"v_mfma_f32_16x16x32_bf16 v[36:39], v[112:115], v[64:67], v[36:39]\n"
"ds_read_b64 v[60:61], v3 offset:9216\n"
"ds_write_b32 v10, v52 offset:3456\n"
"v_cndmask_b32 v94, v52, v53, s[34:35]\n"
"v_cndmask_b32 v54, v54, v55, s[34:35]\n"
"v_mfma_f32_16x16x32_bf16 v[40:43], v[116:119], v[64:67], v[40:43]\n"
"ds_read_b64 v[62:63], v3 offset:9472\n"
"s_waitcnt lgkmcnt(8)\n"
"v_add_f32 v84, v84, v85\n"
"v_add_f32 v86, v86, v87\n"
"v_cndmask_b32 v94, v94, v54, s[20:21]\n"
"ds_write_b32 v9, v94 offset:3136\n"
"v_mfma_f32_16x16x32_bf16 v[36:39], v[120:123], v[68:71], v[36:39]\n"
"ds_read_b64 v[64:65], v3 offset:10240\n"
"v_add_f32 v84, v84, v86\n"
"s_waitcnt lgkmcnt(9)\n"
"v_mul_f32 v89, 0x3acccccd, v91\n"
"v_mfma_f32_16x16x32_bf16 v[40:43], v[124:127], v[68:71], v[40:43]\n"
"ds_read_b64 v[66:67], v3 offset:10496\n"
"v_fma_f32 v84, -v91, v89, v84\n"
"s_waitcnt lgkmcnt(8)\n"
"v_mfma_f32_16x16x32_bf16 v[36:39], v[136:139], v[72:75], v[36:39]\n"
"ds_read_b64 v[68:69], v3 offset:11264\n"
"v_fmamk_f32 v84, v84, 0x3acccccd, v27\n"
"v_cmp_u_f32 vcc, v84, v84\n"
"v_mfma_f32_16x16x32_bf16 v[40:43], v[140:143], v[72:75], v[40:43]\n"
"ds_read_b64 v[70:71], v3 offset:11520\n"
"v_max_f32 v84, v84, v27\n"
"v_rsq_f32 v88, v84\n"
"v_mfma_f32_16x16x32_bf16 v[36:39], v[144:147], v[76:79], v[36:39]\n"
"ds_read_b64 v[72:73], v3 offset:13312\n"
"v_mul_f32 v89, -v89, v88\n"
"v_mfma_f32_16x16x32_bf16 v[40:43], v[148:151], v[76:79], v[40:43]\n"
"ds_read_b64 v[74:75], v3 offset:13568\n"
"s_cbranch_vccnz .Lsl_121\n"
".Lbk_122:\n"
"s_waitcnt lgkmcnt(7)\n"
"v_mfma_f32_16x16x32_bf16 v[36:39], v[152:155], v[56:59], v[36:39]\n"
"ds_read_b64 v[76:77], v3 offset:14336\n"
"v_mul_f32 v28, v88, v28\n"
"v_mul_f32 v29, v88, v29\n"
"v_mfma_f32_16x16x32_bf16 v[40:43], v[156:159], v[56:59], v[40:43]\n"
"ds_read_b64 v[78:79], v3 offset:14592\n"
"ds_read_b128 v[44:47], v11 offset:3136\n"
"ds_read_b128 v[48:51], v11 offset:3456\n"
"v_mfma_f32_16x16x32_bf16 v[36:39], v[168:171], v[60:63], v[36:39]\n"
"ds_read_b64 v[56:57], v3 offset:15360\n"
"v_mul_f32 v30, v88, v30\n"
"v_mul_f32 v31, v88, v31\n"
"v_mfma_f32_16x16x32_bf16 v[40:43], v[172:175], v[60:63], v[40:43]\n"
"ds_read_b64 v[58:59], v3 offset:15616\n"
"v_fmac_f32 v28, v89, v16\n"
"s_waitcnt lgkmcnt(8)\n"
"v_mfma_f32_16x16x32_bf16 v[36:39], v[176:179], v[64:67], v[36:39]\n"
"ds_read_b64 v[60:61], v4 offset:0\n"
"v_fmac_f32 v29, v89, v17\n"
"v_fmac_f32 v30, v89, v18\n"
"v_mfma_f32_16x16x32_bf16 v[40:43], v[180:183], v[64:67], v[40:43]\n"
"ds_read_b64 v[62:63], v4 offset:256\n"
"v_fmac_f32 v31, v89, v19\n"
"v_cvt_pk_bf16_f32 v28, v28, v29\n"
"v_mfma_f32_16x16x32_bf16 v[36:39], v[184:187], v[68:71], v[36:39]\n"
"ds_read_b64 v[64:65], v5 offset:0\n"
"v_cvt_pk_bf16_f32 v29, v30, v31\n"
"ds_write_b64 v7, v[28:29] offset:15872\n"
"v_mfma_f32_16x16x32_bf16 v[40:43], v[188:191], v[68:71], v[40:43]\n"
"ds_read_b64 v[66:67], v5 offset:256\n"
"v_mul_f32 v32, v88, v32\n"
"s_waitcnt lgkmcnt(9)\n"
"v_mfma_f32_16x16x32_bf16 v[36:39], v[200:203], v[72:75], v[36:39]\n"
"ds_read_b64 v[68:69], v6 offset:0\n"
"s_waitcnt lgkmcnt(9)\n"
"v_add_f32 v44, v44, v45\n"
"v_mul_f32 v33, v88, v33\n"
"v_mfma_f32_16x16x32_bf16 v[40:43], v[204:207], v[72:75], v[40:43]\n"
"ds_read_b64 v[70:71], v6 offset:256\n"
"v_add_f32 v46, v46, v47\n"
"v_mul_f32 v34, v88, v34\n"
"v_mfma_f32_16x16x32_bf16 v[36:39], v[208:211], v[76:79], v[36:39]\n"
"s_waitcnt lgkmcnt(9)\n"
"v_add_f32 v48, v48, v49\n"
"v_add_f32 v50, v50, v51\n"
"v_mul_f32 v35, v88, v35\n"
"v_mfma_f32_16x16x32_bf16 v[40:43], v[212:215], v[76:79], v[40:43]\n"
"v_add_f32 v48, v48, v50\n"
"v_fmac_f32 v32, v89, v20\n"
"s_waitcnt lgkmcnt(5)\n"
"v_mfma_f32_16x16x32_bf16 v[36:39], v[216:219], v[56:59], v[36:39]\n"
"v_add_f32 v44, v44, v46\n"
"v_mul_f32 v53, 0x3acccccd, v48\n"
"v_fmac_f32 v33, v89, v21\n"
"v_mfma_f32_16x16x32_bf16 v[40:43], v[220:223], v[56:59], v[40:43]\n"
"v_fma_f32 v44, -v48, v53, v44\n"
"v_fmamk_f32 v44, v44, 0x3acccccd, v27\n"
"v_fmac_f32 v34, v89, v22\n"
"v_mfma_f32_16x16x32_bf16 v[36:39], v[232:235], v[60:63], v[36:39]\n"
"v_cmp_u_f32 vcc, v44, v44\n"
"v_max_f32 v44, v44, v27\n"
"v_fmac_f32 v35, v89, v23\n"
"v_mfma_f32_16x16x32_bf16 v[40:43], v[236:239], v[60:63], v[40:43]\n"
"v_rsq_f32 v52, v44\n"
"s_waitcnt lgkmcnt(0)\n"
"v_mfma_f32_16x16x32_bf16 v[36:39], v[240:243], v[64:67], v[36:39]\n"
"v_mul_f32 v53, -v53, v52\n"
"v_cvt_pk_bf16_f32 v32, v32, v33\n"
"v_cvt_pk_bf16_f32 v33, v34, v35\n"
"v_mfma_f32_16x16x32_bf16 v[40:43], v[244:247], v[64:67], v[40:43]\n"
"s_cbranch_vccnz .Lsl_125\n"
".Lbk_126:\n"
"ds_write_b64 v8, v[32:33] offset:15872\n"
"v_mfma_f32_16x16x32_bf16 v[36:39], v[248:251], v[68:71], v[36:39]\n"
"v_mfma_f32_16x16x32_bf16 v[40:43], v[252:255], v[68:71], v[40:43]\n"
"v_cmp_u_f32 vcc, v92, v92\n"
"s_nop 3\n"
"v_mul_f32 v36, v52, v36\n"
"v_mul_f32 v37, v52, v37\n"
"v_mul_f32 v38, v52, v38\n"
"v_mul_f32 v39, v52, v39\n"
"v_fmac_f32 v36, v53, v16\n"
"v_fmac_f32 v37, v53, v17\n"
"v_fmac_f32 v38, v53, v18\n"
"v_fmac_f32 v39, v53, v19\n"
"v_cvt_pk_bf16_f32 v36, v36, v37\n"
"v_cvt_pk_bf16_f32 v37, v38, v39\n"
"ds_write_b64 v7, v[36:37] offset:0\n"
"v_mul_f32 v40, v52, v40\n"
"v_mul_f32 v41, v52, v41\n"
"v_mul_f32 v42, v52, v42\n"
"v_mul_f32 v43, v52, v43\n"
"v_fmac_f32 v40, v53, v20\n"
"v_fmac_f32 v41, v53, v21\n"
"v_fmac_f32 v42, v53, v22\n"
"v_fmac_f32 v43, v53, v23\n"
"v_cvt_pk_bf16_f32 v40, v40, v41\n"
"v_cvt_pk_bf16_f32 v41, v42, v43\n"
"ds_write_b64 v8, v[40:41] offset:0\n"
"s_cbranch_vccnz .Lrd_129\n"
".Lgo_95:\n"
"ds_write_b32 v13, v13 offset:320\n"
"v_mov_b32 v84, s42\n"
"v_mov_b32 v85, s42\n"
"v_mov_b32 v86, s42\n"
"v_mov_b32 v87, s42\n"
"v_mov_b32 v30, 0\n"
"v_mov_b32 v31, 0\n"
"v_mov_b32 v34, 0\n"
"v_mov_b32 v35, 0\n"
"s_mov_b32 s17, 0x100000\n"
".Ltp_175:\n"
"ds_read_b32 v90, v14 offset:320\n"
"s_sub_u32 s17, s17, 1\n"
"s_cbranch_scc1 .Lou_176\n"
"s_waitcnt lgkmcnt(0)\n"
"v_cmp_eq_u32 vcc, 0, v90\n"
"s_cbranch_vccnz .Ltp_175\n"
".Lou_176:\n"
"s_waitcnt lgkmcnt(0)\n"
"s_mov_b64 exec, 0xffff\n"
"ds_read_b64 v[30:31], v24 offset:4000\n"
"ds_read_b64 v[34:35], v24 offset:4040\n"
"s_mov_b64 exec, -1\n"
"ds_read_b64 v[28:29], v25 offset:3968\n"
"ds_read_b64 v[68:69], v25 offset:80\n"
"ds_read_b64 v[70:71], v24 offset:112\n"
"ds_read_b64 v[76:77], v25 offset:4048\n"
"ds_read_b64 v[78:79], v24 offset:4080\n"
"ds_read_b64 v[32:33], v25 offset:4008\n"
"ds_read_b64 v[72:73], v25 offset:120\n"
"ds_read_b64 v[74:75], v24 offset:152\n"
"ds_read_b64 v[80:81], v25 offset:4088\n"
"ds_read_b64 v[82:83], v24 offset:4120\n"
"ds_read_b64_tr_b16 v[44:45], v26 offset:0\n"
"ds_read_b64_tr_b16 v[46:47], v26 offset:3968\n"
"ds_read_b64_tr_b16 v[48:49], v26 offset:40\n"
"v_mov_b32 v64, 0x0c0c0c0c\n"
"v_mov_b32 v65, 0xc0c0100\n"
"v_mov_b32 v66, 0x1000c0c\n"
"v_cndmask_b32 v65, v65, v66, s[34:35]\n"
"v_cndmask_b32 v36, v65, v64, s[36:37]\n"
"v_cndmask_b32 v38, v64, v65, s[36:37]\n"
"v_mov_b32 v65, 0xc0c0302\n"
"v_mov_b32 v66, 0x3020c0c\n"
"v_cndmask_b32 v65, v65, v66, s[34:35]\n"
"v_cndmask_b32 v37, v65, v64, s[38:39]\n"
"v_cndmask_b32 v39, v64, v65, s[38:39]\n"
"s_waitcnt lgkmcnt(10)\n"
"v_mfma_f32_16x16x32_bf16 v[68:71], v[68:71], v[28:31], v[84:87]\n"
"s_waitcnt lgkmcnt(8)\n"
"v_mfma_f32_16x16x32_bf16 v[76:79], v[76:79], v[28:31], 0\n"
"ds_read_b64_tr_b16 v[50:51], v26 offset:4008\n"
"ds_read_b64_tr_b16 v[52:53], v26 offset:32\n"
"ds_read_b64_tr_b16 v[54:55], v26 offset:4000\n"
"s_waitcnt lgkmcnt(8)\n"
"v_mfma_f32_16x16x32_bf16 v[72:75], v[72:75], v[32:35], v[84:87]\n"
"s_waitcnt lgkmcnt(6)\n"
"v_mfma_f32_16x16x32_bf16 v[80:83], v[80:83], v[32:35], 0\n"
"ds_read_b64_tr_b16 v[56:57], v26 offset:72\n"
"ds_read_b64_tr_b16 v[58:59], v26 offset:4040\n"
"v_mov_b32 v40, 0x3f803f80\n"
"v_mov_b32 v41, 0x3f803f80\n"
"v_mov_b32 v42, 0x3f803f80\n"
"v_mov_b32 v43, 0x3f803f80\n"
"v_cndmask_b32 v60, v68, v69, s[34:35]\n"
"v_cndmask_b32 v84, v76, v77, s[34:35]\n"
"v_cndmask_b32 v60, v60, v84, s[36:37]\n"
"v_cndmask_b32 v61, v70, v71, s[34:35]\n"
"v_cndmask_b32 v84, v78, v79, s[34:35]\n"
"v_cndmask_b32 v61, v61, v84, s[38:39]\n"
"v_max_f32 v64, v60, v61\n"
"v_max_f32 v66, v60, v61\n"
"v_cndmask_b32 v62, v72, v73, s[34:35]\n"
"v_cndmask_b32 v84, v80, v81, s[34:35]\n"
"v_cndmask_b32 v62, v62, v84, s[36:37]\n"
"v_cndmask_b32 v63, v74, v75, s[34:35]\n"
"v_cndmask_b32 v84, v82, v83, s[34:35]\n"
"v_cndmask_b32 v63, v63, v84, s[38:39]\n"
"v_max_f32 v65, v62, v63\n"
"v_max_f32 v67, v62, v63\n"
"v_permlane16_swap_b32 v64, v66\n"
"s_nop 0\n"
"v_permlane16_swap_b32 v65, v67\n"
"v_max_f32 v84, v64, v66\n"
"v_max_f32 v64, v64, v66\n"
"v_max_f32 v85, v65, v67\n"
"v_max_f32 v65, v65, v67\n"
"v_permlane32_swap_b32 v64, v84\n"
"s_nop 0\n"
"v_permlane32_swap_b32 v65, v85\n"
"v_max_f32 v64, v64, v84\n"
"v_max_f32 v65, v65, v85\n"
"v_mul_f32 v64, 0xbea52b5b, v64\n"
"v_mul_f32 v65, 0xbea52b5b, v65\n"
"v_fmamk_f32 v60, v60, 0x3ea52b5b, v64\n"
"v_fmamk_f32 v61, v61, 0x3ea52b5b, v64\n"
"v_fmamk_f32 v62, v62, 0x3ea52b5b, v65\n"
"v_fmamk_f32 v63, v63, 0x3ea52b5b, v65\n"
"v_exp_f32 v60, v60\n"
"v_exp_f32 v61, v61\n"
"v_exp_f32 v62, v62\n"
"v_exp_f32 v63, v63\n"
"v_cvt_pk_bf16_f32 v64, v60, v61\n"
"v_cvt_pk_bf16_f32 v65, v62, v63\n"
"v_perm_b32 v28, v64, v64, v36\n"
"v_perm_b32 v29, v64, v64, v37\n"
"v_perm_b32 v30, v64, v64, v38\n"
"v_perm_b32 v31, v64, v64, v39\n"
"v_perm_b32 v32, v65, v65, v36\n"
"v_perm_b32 v33, v65, v65, v37\n"
"v_perm_b32 v34, v65, v65, v38\n"
"v_perm_b32 v35, v65, v65, v39\n"
"v_mfma_f32_16x16x32_bf16 v[68:71], v[40:43], v[28:31], 0\n"
"s_nop 0\n"
"v_mfma_f32_16x16x32_bf16 v[72:75], v[40:43], v[32:35], 0\n"
"s_waitcnt lgkmcnt(6)\n"
"v_mfma_f32_16x16x32_bf16 v[44:47], v[44:47], v[28:31], 0\n"
"s_waitcnt lgkmcnt(4)\n"
"v_mfma_f32_16x16x32_bf16 v[48:51], v[48:51], v[32:35], 0\n"
"s_waitcnt lgkmcnt(2)\n"
"v_mfma_f32_16x16x32_bf16 v[52:55], v[52:55], v[28:31], 0\n"
"s_waitcnt lgkmcnt(0)\n"
"v_mfma_f32_16x16x32_bf16 v[56:59], v[56:59], v[32:35], 0\n"
"v_cndmask_b32 v88, 2.0, v1, s[40:41]\n"
"v_rcp_f32 v64, v68\n"
"v_rcp_f32 v65, v72\n"
"ds_read_b32 v90, v14 offset:336\n"
"s_waitcnt lgkmcnt(0)\n"
"v_cmp_eq_u32 vcc, 0, v90\n"
"s_cbranch_vccnz .Lsl_171\n"
".Lbk_172:\n"
"s_add_u32 s31, s30, 0xa60000\n"
"v_mul_f32 v44, v64, v44\n"
"v_mul_f32 v45, v64, v45\n"
"v_mul_f32 v46, v64, v46\n"
"v_mul_f32 v47, v64, v47\n"
"v_mul_f32 v48, v65, v48\n"
"v_mul_f32 v49, v65, v49\n"
"v_mul_f32 v50, v65, v50\n"
"v_mul_f32 v51, v65, v51\n"
"v_cvt_pk_bf16_f32 v44, v44, v45\n"
"v_cvt_pk_bf16_f32 v45, v46, v47\n"
"v_cvt_pk_bf16_f32 v46, v48, v49\n"
"v_cvt_pk_bf16_f32 v47, v50, v51\n"
"v_min_u32 v44, -2, v44\n"
"v_min_u32 v45, -2, v45\n"
"v_min_u32 v46, -2, v46\n"
"v_min_u32 v47, -2, v47\n"
"buffer_store_dwordx4 v[44:47], v1, s[4:7], s31 offen offset:0 sc1\n"
"v_mul_f32 v52, v64, v52\n"
"v_mul_f32 v53, v64, v53\n"
"v_mul_f32 v54, v64, v54\n"
"v_mul_f32 v55, v64, v55\n"
"v_mul_f32 v56, v65, v56\n"
"v_mul_f32 v57, v65, v57\n"
"v_mul_f32 v58, v65, v58\n"
"v_mul_f32 v59, v65, v59\n"
"v_cvt_pk_bf16_f32 v52, v52, v53\n"
"v_cvt_pk_bf16_f32 v53, v54, v55\n"
"v_cvt_pk_bf16_f32 v54, v56, v57\n"
"v_cvt_pk_bf16_f32 v55, v58, v59\n"
"v_min_u32 v52, -2, v52\n"
"v_min_u32 v53, -2, v53\n"
"v_min_u32 v54, -2, v54\n"
"v_min_u32 v55, -2, v55\n"
"buffer_store_dwordx4 v[52:55], v88, s[4:7], s31 offen offset:1024 sc1\n"
"ds_write_b32 v13, v13 offset:352\n"
"s_cmp_lg_u32 s54, 0\n"
"s_cbranch_scc1 .Lfx_179\n"
".Lbk_180:\n"
"s_mov_b32 s54, 0\n"
"s_nop 0\n"
".Lag_177:\n"
"v_mov_b32 v92, 0\n"
"s_mov_b32 s17, 0x100000\n"
".Ltp_183:\n"
"ds_read_b32 v90, v14 offset:384\n"
"s_sub_u32 s17, s17, 1\n"
"s_cbranch_scc1 .Lou_184\n"
"s_waitcnt lgkmcnt(0)\n"
"v_cmp_eq_u32 vcc, 0, v90\n"
"s_cbranch_vccnz .Ltp_183\n"
".Lou_184:\n"
"s_waitcnt lgkmcnt(0)\n"
"ds_read_b64 v[56:57], v3 offset:40960\n"
"ds_read_b64 v[58:59], v3 offset:41216\n"
"ds_read_b64 v[60:61], v3 offset:41984\n"
"ds_read_b64 v[62:63], v3 offset:42240\n"
"ds_read_b64 v[64:65], v3 offset:43008\n"
"ds_read_b64 v[66:67], v3 offset:43264\n"
"ds_read_b64 v[68:69], v3 offset:44032\n"
"ds_read_b64 v[70:71], v3 offset:44288\n"
"ds_read_b64 v[72:73], v3 offset:45056\n"
"ds_read_b64 v[74:75], v3 offset:45312\n"
"s_waitcnt lgkmcnt(6)\n"
"v_mfma_f32_16x16x32_bf16 v[28:31], v[96:99], v[56:59], 0\n"
"ds_read_b64 v[76:77], v3 offset:46080\n"
"v_mfma_f32_16x16x32_bf16 v[32:35], v[100:103], v[56:59], 0\n"
"ds_read_b64 v[78:79], v3 offset:46336\n"
"v_mfma_f32_16x16x32_bf16 v[52:55], v[56:59], v[56:59], 0\n"
"v_mfma_f32_16x16x32_bf16 v[28:31], v[104:107], v[60:63], v[28:31]\n"
"ds_read_b64 v[56:57], v3 offset:47104\n"
"v_mfma_f32_16x16x32_bf16 v[32:35], v[108:111], v[60:63], v[32:35]\n"
"ds_read_b64 v[58:59], v3 offset:47360\n"
"s_waitcnt lgkmcnt(6)\n"
"v_mfma_f32_16x16x32_bf16 v[28:31], v[112:115], v[64:67], v[28:31]\n"
"ds_read_b64 v[60:61], v3 offset:48128\n"
"v_mfma_f32_16x16x32_bf16 v[32:35], v[116:119], v[64:67], v[32:35]\n"
"ds_read_b64 v[62:63], v3 offset:48384\n"
"v_mfma_f32_16x16x32_bf16 v[28:31], v[120:123], v[68:71], v[28:31]\n"
"ds_read_b64 v[64:65], v3 offset:49152\n"
"v_mfma_f32_16x16x32_bf16 v[32:35], v[124:127], v[68:71], v[32:35]\n"
"ds_read_b64 v[66:67], v3 offset:49408\n"
"s_waitcnt lgkmcnt(6)\n"
"v_mfma_f32_16x16x32_bf16 v[28:31], v[128:131], v[72:75], v[28:31]\n"
"ds_read_b64 v[68:69], v3 offset:50176\n"
"v_mfma_f32_16x16x32_bf16 v[32:35], v[132:135], v[72:75], v[32:35]\n"
"ds_read_b64 v[70:71], v3 offset:50432\n"
"v_mfma_f32_16x16x32_bf16 v[52:55], v[72:75], v[72:75], v[52:55]\n"
"v_mfma_f32_16x16x32_bf16 v[28:31], v[136:139], v[76:79], v[28:31]\n"
"ds_read_b64 v[72:73], v3 offset:51200\n"
"v_mfma_f32_16x16x32_bf16 v[32:35], v[140:143], v[76:79], v[32:35]\n"
"ds_read_b64 v[74:75], v3 offset:51456\n"
"s_waitcnt lgkmcnt(6)\n"
"v_mfma_f32_16x16x32_bf16 v[28:31], v[144:147], v[56:59], v[28:31]\n"
"ds_read_b64 v[76:77], v3 offset:52224\n"
"v_mfma_f32_16x16x32_bf16 v[32:35], v[148:151], v[56:59], v[32:35]\n"
"ds_read_b64 v[78:79], v3 offset:52480\n"
"v_mfma_f32_16x16x32_bf16 v[28:31], v[152:155], v[60:63], v[28:31]\n"
"ds_read_b64 v[56:57], v3 offset:53248\n"
"v_mfma_f32_16x16x32_bf16 v[32:35], v[156:159], v[60:63], v[32:35]\n"
"ds_read_b64 v[58:59], v3 offset:53504\n"
"s_waitcnt lgkmcnt(6)\n"
"v_mfma_f32_16x16x32_bf16 v[28:31], v[160:163], v[64:67], v[28:31]\n"
"ds_read_b64 v[60:61], v3 offset:54272\n"
"v_mfma_f32_16x16x32_bf16 v[32:35], v[164:167], v[64:67], v[32:35]\n"
"ds_read_b64 v[62:63], v3 offset:54528\n"
"v_mfma_f32_16x16x32_bf16 v[52:55], v[64:67], v[64:67], v[52:55]\n"
"v_mfma_f32_16x16x32_bf16 v[28:31], v[168:171], v[68:71], v[28:31]\n"
"ds_read_b64 v[64:65], v3 offset:55296\n"
"v_mfma_f32_16x16x32_bf16 v[32:35], v[172:175], v[68:71], v[32:35]\n"
"ds_read_b64 v[66:67], v3 offset:55552\n"
"s_waitcnt lgkmcnt(6)\n"
"v_mfma_f32_16x16x32_bf16 v[28:31], v[176:179], v[72:75], v[28:31]\n"
"ds_read_b64 v[68:69], v3 offset:56320\n"
"v_mfma_f32_16x16x32_bf16 v[32:35], v[180:183], v[72:75], v[32:35]\n"
"ds_read_b64 v[70:71], v3 offset:56576\n"
"v_mfma_f32_16x16x32_bf16 v[28:31], v[184:187], v[76:79], v[28:31]\n"
"ds_read_b64 v[72:73], v3 offset:57344\n"
"v_mfma_f32_16x16x32_bf16 v[32:35], v[188:191], v[76:79], v[32:35]\n"
"ds_read_b64 v[74:75], v3 offset:57600\n"
"s_waitcnt lgkmcnt(6)\n"
"v_mfma_f32_16x16x32_bf16 v[28:31], v[192:195], v[56:59], v[28:31]\n"
"ds_read_b64 v[76:77], v4 offset:40960\n"
"v_mfma_f32_16x16x32_bf16 v[32:35], v[196:199], v[56:59], v[32:35]\n"
"ds_read_b64 v[78:79], v4 offset:41216\n"
"ds_read_b32 v90, v14 offset:272\n"
"v_mfma_f32_16x16x32_bf16 v[52:55], v[56:59], v[56:59], v[52:55]\n"
"v_mfma_f32_16x16x32_bf16 v[28:31], v[200:203], v[60:63], v[28:31]\n"
"ds_read_b64 v[56:57], v5 offset:40960\n"
"v_mfma_f32_16x16x32_bf16 v[32:35], v[204:207], v[60:63], v[32:35]\n"
"ds_read_b64 v[58:59], v5 offset:41216\n"
"s_waitcnt lgkmcnt(7)\n"
"v_mfma_f32_16x16x32_bf16 v[28:31], v[208:211], v[64:67], v[28:31]\n"
"ds_read_b64 v[60:61], v6 offset:40960\n"
"s_waitcnt lgkmcnt(3)\n"
"v_cmp_eq_u32 vcc, 0, v90\n"
"v_mfma_f32_16x16x32_bf16 v[32:35], v[212:215], v[64:67], v[32:35]\n"
"ds_read_b64 v[62:63], v6 offset:41216\n"
"v_mfma_f32_16x16x32_bf16 v[28:31], v[216:219], v[68:71], v[28:31]\n"
"s_cbranch_vccnz .Lsl_185\n"
".Lbk_186:\n"
"v_add_u32 v3, 0x5000, v3\n"
"ds_read_b64 v[64:65], v3 offset:40960\n"
"v_mfma_f32_16x16x32_bf16 v[32:35], v[220:223], v[68:71], v[32:35]\n"
"ds_read_b64 v[66:67], v3 offset:41216\n"
"v_mfma_f32_16x16x32_bf16 v[28:31], v[224:227], v[72:75], v[28:31]\n"
"ds_read_b64 v[68:69], v3 offset:41984\n"
"v_mfma_f32_16x16x32_bf16 v[32:35], v[228:231], v[72:75], v[32:35]\n"
"ds_read_b64 v[70:71], v3 offset:42240\n"
"v_mfma_f32_16x16x32_bf16 v[52:55], v[72:75], v[72:75], v[52:55]\n"
"v_mfma_f32_16x16x32_bf16 v[28:31], v[232:235], v[76:79], v[28:31]\n"
"ds_read_b64 v[72:73], v3 offset:43008\n"
"v_mfma_f32_16x16x32_bf16 v[32:35], v[236:239], v[76:79], v[32:35]\n"
"ds_read_b64 v[74:75], v3 offset:43264\n"
"s_waitcnt lgkmcnt(6)\n"
"v_mfma_f32_16x16x32_bf16 v[28:31], v[240:243], v[56:59], v[28:31]\n"
"ds_read_b64 v[76:77], v3 offset:44032\n"
"v_cndmask_b32 v94, v52, v53, s[34:35]\n"
"v_mfma_f32_16x16x32_bf16 v[32:35], v[244:247], v[56:59], v[32:35]\n"
"ds_read_b64 v[78:79], v3 offset:44288\n"
"v_cndmask_b32 v54, v54, v55, s[34:35]\n"
"v_mfma_f32_16x16x32_bf16 v[28:31], v[248:251], v[60:63], v[28:31]\n"
"ds_read_b64 v[56:57], v3 offset:45056\n"
"v_cndmask_b32 v94, v94, v54, s[20:21]\n"
"v_mfma_f32_16x16x32_bf16 v[32:35], v[252:255], v[60:63], v[32:35]\n"
"ds_read_b64 v[58:59], v3 offset:45312\n"
"s_waitcnt lgkmcnt(6)\n"
"v_mfma_f32_16x16x32_bf16 v[36:39], v[96:99], v[64:67], 0\n"
"ds_read_b64 v[60:61], v3 offset:46080\n"
"v_mfma_f32_16x16x32_bf16 v[40:43], v[100:103], v[64:67], 0\n"
"ds_read_b64 v[62:63], v3 offset:46336\n"
"v_cndmask_b32 v94, v28, v94, s[22:23]\n"
"v_mfma_f32_16x16x32_bf16 v[52:55], v[64:67], v[64:67], 0\n"
"ds_write_b32 v9, v94 offset:3712\n"
"v_mfma_f32_16x16x32_bf16 v[36:39], v[104:107], v[68:71], v[36:39]\n"
"ds_read_b64 v[64:65], v3 offset:47104\n"
"v_mfma_f32_16x16x32_bf16 v[40:43], v[108:111], v[68:71], v[40:43]\n"
"ds_read_b64 v[66:67], v3 offset:47360\n"
"s_waitcnt lgkmcnt(7)\n"
"v_mfma_f32_16x16x32_bf16 v[36:39], v[112:115], v[72:75], v[36:39]\n"
"ds_read_b64 v[68:69], v3 offset:48128\n"
"v_mfma_f32_16x16x32_bf16 v[40:43], v[116:119], v[72:75], v[40:43]\n"
"ds_read_b64 v[70:71], v3 offset:48384\n"
"v_mfma_f32_16x16x32_bf16 v[36:39], v[120:123], v[76:79], v[36:39]\n"
"ds_read_b64 v[72:73], v3 offset:49152\n"
"v_mfma_f32_16x16x32_bf16 v[40:43], v[124:127], v[76:79], v[40:43]\n"
"ds_read_b64 v[74:75], v3 offset:49408\n"
"s_waitcnt lgkmcnt(7)\n"
"v_mfma_f32_16x16x32_bf16 v[36:39], v[128:131], v[56:59], v[36:39]\n"
"ds_read_b64 v[76:77], v3 offset:50176\n"
"v_mfma_f32_16x16x32_bf16 v[40:43], v[132:135], v[56:59], v[40:43]\n"
"ds_read_b64 v[78:79], v3 offset:50432\n"
"v_mfma_f32_16x16x32_bf16 v[52:55], v[56:59], v[56:59], v[52:55]\n"
"ds_read_b128 v[84:87], v11 offset:3712\n"
"ds_read_b32 v91, v12 offset:3712\n"
"v_mfma_f32_16x16x32_bf16 v[36:39], v[136:139], v[60:63], v[36:39]\n"
"ds_read_b64 v[56:57], v3 offset:51200\n"
"v_mfma_f32_16x16x32_bf16 v[40:43], v[140:143], v[60:63], v[40:43]\n"
"ds_read_b64 v[58:59], v3 offset:51456\n"
"s_waitcnt lgkmcnt(8)\n"
"v_mfma_f32_16x16x32_bf16 v[36:39], v[144:147], v[64:67], v[36:39]\n"
"ds_read_b64 v[60:61], v3 offset:52224\n"
"v_mfma_f32_16x16x32_bf16 v[40:43], v[148:151], v[64:67], v[40:43]\n"
"ds_read_b64 v[62:63], v3 offset:52480\n"
"v_mfma_f32_16x16x32_bf16 v[36:39], v[152:155], v[68:71], v[36:39]\n"
"ds_read_b64 v[64:65], v3 offset:53248\n"
"v_mfma_f32_16x16x32_bf16 v[40:43], v[156:159], v[68:71], v[40:43]\n"
"ds_read_b64 v[66:67], v3 offset:53504\n"
"s_waitcnt lgkmcnt(8)\n"
"v_mfma_f32_16x16x32_bf16 v[36:39], v[160:163], v[72:75], v[36:39]\n"
"ds_read_b64 v[68:69], v3 offset:54272\n"
"s_waitcnt lgkmcnt(8)\n"
"v_add_f32 v84, v84, v85\n"
"v_add_f32 v86, v86, v87\n"
"v_mfma_f32_16x16x32_bf16 v[40:43], v[164:167], v[72:75], v[40:43]\n"
"ds_read_b64 v[70:71], v3 offset:54528\n"
"v_add_f32 v84, v84, v86\n"
"s_waitcnt lgkmcnt(8)\n"
"v_mul_f32 v89, 0x3acccccd, v91\n"
"v_mfma_f32_16x16x32_bf16 v[52:55], v[72:75], v[72:75], v[52:55]\n"
"v_fma_f32 v84, -v91, v89, v84\n"
"v_fmamk_f32 v84, v84, 0x3acccccd, v27\n"
"v_mfma_f32_16x16x32_bf16 v[36:39], v[168:171], v[76:79], v[36:39]\n"
"ds_read_b64 v[72:73], v3 offset:55296\n"
"v_cmp_u_f32 vcc, v84, v84\n"
"v_mfma_f32_16x16x32_bf16 v[40:43], v[172:175], v[76:79], v[40:43]\n"
"ds_read_b64 v[74:75], v3 offset:55552\n"
"s_waitcnt lgkmcnt(6)\n"
"v_mfma_f32_16x16x32_bf16 v[36:39], v[176:179], v[56:59], v[36:39]\n"
"ds_read_b64 v[76:77], v3 offset:56320\n"
"v_max_f32 v84, v84, v27\n"
"v_mfma_f32_16x16x32_bf16 v[40:43], v[180:183], v[56:59], v[40:43]\n"
"ds_read_b64 v[78:79], v3 offset:56576\n"
"v_rsq_f32 v88, v84\n"
"v_mfma_f32_16x16x32_bf16 v[36:39], v[184:187], v[60:63], v[36:39]\n"
"ds_read_b64 v[56:57], v3 offset:57344\n"
"v_mul_f32 v89, -v89, v88\n"
"v_mfma_f32_16x16x32_bf16 v[40:43], v[188:191], v[60:63], v[40:43]\n"
"ds_read_b64 v[58:59], v3 offset:57600\n"
"s_cbranch_vccnz .Lsl_193\n"
".Lbk_194:\n"
"s_waitcnt lgkmcnt(6)\n"
"v_mfma_f32_16x16x32_bf16 v[36:39], v[192:195], v[64:67], v[36:39]\n"
"ds_read_b64 v[60:61], v4 offset:61440\n"
"s_add_u32 m0, s29, 0xa000\n"
"v_mul_f32 v28, v88, v28\n"
"v_mfma_f32_16x16x32_bf16 v[40:43], v[196:199], v[64:67], v[40:43]\n"
"ds_read_b64 v[62:63], v4 offset:61696\n"
"ds_read_b32 v90, v14 offset:400\n"
"v_mul_f32 v29, v88, v29\n"
"v_mfma_f32_16x16x32_bf16 v[52:55], v[64:67], v[64:67], v[52:55]\n"
"s_add_u32 s31, s44, 0x13b000\n"
"v_mul_f32 v30, v88, v30\n"
"v_mul_f32 v31, v88, v31\n"
"v_mfma_f32_16x16x32_bf16 v[36:39], v[200:203], v[68:71], v[36:39]\n"
"ds_read_b64 v[64:65], v5 offset:61440\n"
"buffer_load_dwordx4 v1, s[4:7], s31 offen offset:0 sc1 lds\n"
"v_fmac_f32 v28, v89, v16\n"
"v_mfma_f32_16x16x32_bf16 v[40:43], v[204:207], v[68:71], v[40:43]\n"
"ds_read_b64 v[66:67], v5 offset:61696\n"
"s_waitcnt lgkmcnt(7)\n"
"v_mfma_f32_16x16x32_bf16 v[36:39], v[208:211], v[72:75], v[36:39]\n"
"ds_read_b64 v[68:69], v6 offset:61440\n"
"s_waitcnt lgkmcnt(3)\n"
"v_cmp_eq_u32 vcc, 0, v90\n"
"v_mfma_f32_16x16x32_bf16 v[40:43], v[212:215], v[72:75], v[40:43]\n"
"ds_read_b64 v[70:71], v6 offset:61696\n"
"v_fmac_f32 v29, v89, v17\n"
"v_mfma_f32_16x16x32_bf16 v[36:39], v[216:219], v[76:79], v[36:39]\n"
"s_cbranch_vccnz .Lsl_189\n"
".Lbk_190:\n"
"v_add_u32 v3, 0xffffb000, v3\n"
"ds_read_b64 v[72:73], v3 offset:0\n"
"buffer_load_dwordx4 v1, s[4:7], s31 offen offset:1024 sc1 lds\n"
"v_mfma_f32_16x16x32_bf16 v[40:43], v[220:223], v[76:79], v[40:43]\n"
"ds_read_b64 v[74:75], v3 offset:256\n"
"v_mfma_f32_16x16x32_bf16 v[36:39], v[224:227], v[56:59], v[36:39]\n"
"ds_read_b64 v[76:77], v3 offset:1024\n"
"v_fmac_f32 v30, v89, v18\n"
"v_mfma_f32_16x16x32_bf16 v[40:43], v[228:231], v[56:59], v[40:43]\n"
"ds_read_b64 v[78:79], v3 offset:1280\n"
"v_fmac_f32 v31, v89, v19\n"
"v_mfma_f32_16x16x32_bf16 v[52:55], v[56:59], v[56:59], v[52:55]\n"
"buffer_load_dwordx4 v1, s[4:7], s31 offen offset:2048 sc1 lds\n"
"v_cvt_pk_bf16_f32 v28, v28, v29\n"
"v_mfma_f32_16x16x32_bf16 v[36:39], v[232:235], v[60:63], v[36:39]\n"
"ds_read_b64 v[56:57], v3 offset:2048\n"
"v_cvt_pk_bf16_f32 v29, v30, v31\n"
"v_mfma_f32_16x16x32_bf16 v[40:43], v[236:239], v[60:63], v[40:43]\n"
"ds_read_b64 v[58:59], v3 offset:2304\n"
"s_waitcnt lgkmcnt(6)\n"
"v_mfma_f32_16x16x32_bf16 v[36:39], v[240:243], v[64:67], v[36:39]\n"
"ds_read_b64 v[60:61], v3 offset:3072\n"
"v_cndmask_b32 v94, v52, v53, s[34:35]\n"
"v_mfma_f32_16x16x32_bf16 v[40:43], v[244:247], v[64:67], v[40:43]\n"
"ds_read_b64 v[62:63], v3 offset:3328\n"
"v_cndmask_b32 v54, v54, v55, s[34:35]\n"
"buffer_load_dwordx4 v1, s[4:7], s31 offen offset:3072 sc1 lds\n"
"v_mfma_f32_16x16x32_bf16 v[36:39], v[248:251], v[68:71], v[36:39]\n"
"ds_read_b64 v[64:65], v3 offset:4096\n"
"v_cndmask_b32 v94, v94, v54, s[20:21]\n"
"s_add_u32 m0, m0, 1024\n"
"v_mfma_f32_16x16x32_bf16 v[40:43], v[252:255], v[68:71], v[40:43]\n"
"ds_read_b64 v[66:67], v3 offset:4352\n"
"s_waitcnt lgkmcnt(6)\n"
"v_mfma_f32_16x16x32_bf16 v[44:47], v[96:99], v[72:75], 0\n"
"ds_read_b64 v[68:69], v3 offset:5120\n"
"s_add_u32 s31, s31, 1024\n"
"v_mfma_f32_16x16x32_bf16 v[48:51], v[100:103], v[72:75], 0\n"
"ds_read_b64 v[70:71], v3 offset:5376\n"
"v_cndmask_b32 v94, v36, v94, s[22:23]\n"
"buffer_load_dwordx4 v1, s[4:7], s31 offen offset:3072 sc1 lds\n"
"v_mfma_f32_16x16x32_bf16 v[52:55], v[72:75], v[72:75], 0\n"
"ds_write_b32 v9, v94 offset:4032\n"
"v_mfma_f32_16x16x32_bf16 v[44:47], v[104:107], v[76:79], v[44:47]\n"
"ds_read_b64 v[72:73], v3 offset:6144\n"
"v_mul_f32 v32, v88, v32\n"
"v_mfma_f32_16x16x32_bf16 v[48:51], v[108:111], v[76:79], v[48:51]\n"
"ds_read_b64 v[74:75], v3 offset:6400\n"
"s_waitcnt lgkmcnt(7)\n"
"v_mfma_f32_16x16x32_bf16 v[44:47], v[112:115], v[56:59], v[44:47]\n"
"ds_read_b64 v[76:77], v3 offset:7168\n"
"v_mul_f32 v33, v88, v33\n"
"v_mfma_f32_16x16x32_bf16 v[48:51], v[116:119], v[56:59], v[48:51]\n"
"ds_read_b64 v[78:79], v3 offset:7424\n"
"v_mul_f32 v34, v88, v34\n"
"v_mfma_f32_16x16x32_bf16 v[44:47], v[120:123], v[60:63], v[44:47]\n"
"ds_read_b64 v[56:57], v3 offset:8192\n"
"v_mul_f32 v35, v88, v35\n"
"v_mfma_f32_16x16x32_bf16 v[48:51], v[124:127], v[60:63], v[48:51]\n"
"ds_read_b64 v[58:59], v3 offset:8448\n"
"s_waitcnt lgkmcnt(7)\n"
"v_mfma_f32_16x16x32_bf16 v[44:47], v[128:131], v[64:67], v[44:47]\n"
"ds_read_b64 v[60:61], v3 offset:9216\n"
"v_fmac_f32 v32, v89, v20\n"
"v_mfma_f32_16x16x32_bf16 v[48:51], v[132:135], v[64:67], v[48:51]\n"
"ds_read_b64 v[62:63], v3 offset:9472\n"
"v_fmac_f32 v33, v89, v21\n"
"v_mfma_f32_16x16x32_bf16 v[52:55], v[64:67], v[64:67], v[52:55]\n"
"ds_read_b128 v[84:87], v11 offset:4032\n"
"ds_read_b32 v91, v12 offset:4032\n"
"v_mfma_f32_16x16x32_bf16 v[44:47], v[136:139], v[68:71], v[44:47]\n"
"ds_read_b64 v[64:65], v3 offset:10240\n"
"v_fmac_f32 v34, v89, v22\n"
"v_mfma_f32_16x16x32_bf16 v[48:51], v[140:143], v[68:71], v[48:51]\n"
"ds_read_b64 v[66:67], v3 offset:10496\n"
"s_waitcnt lgkmcnt(8)\n"
"v_mfma_f32_16x16x32_bf16 v[44:47], v[144:147], v[72:75], v[44:47]\n"
"ds_read_b64 v[68:69], v3 offset:11264\n"
"v_fmac_f32 v35, v89, v23\n"
"v_mfma_f32_16x16x32_bf16 v[48:51], v[148:151], v[72:75], v[48:51]\n"
"ds_read_b64 v[70:71], v3 offset:11520\n"
"v_cvt_pk_bf16_f32 v32, v32, v33\n"
"v_mfma_f32_16x16x32_bf16 v[44:47], v[152:155], v[76:79], v[44:47]\n"
"ds_read_b64 v[72:73], v3 offset:12288\n"
"v_cvt_pk_bf16_f32 v33, v34, v35\n"
"v_mfma_f32_16x16x32_bf16 v[48:51], v[156:159], v[76:79], v[48:51]\n"
"ds_read_b64 v[74:75], v3 offset:12544\n"
"s_waitcnt lgkmcnt(8)\n"
"v_mfma_f32_16x16x32_bf16 v[44:47], v[160:163], v[56:59], v[44:47]\n"
"ds_read_b64 v[76:77], v3 offset:13312\n"
"s_waitcnt lgkmcnt(8)\n"
"v_add_f32 v84, v84, v85\n"
"v_add_f32 v86, v86, v87\n"
"v_mfma_f32_16x16x32_bf16 v[48:51], v[164:167], v[56:59], v[48:51]\n"
"ds_read_b64 v[78:79], v3 offset:13568\n"
"v_add_f32 v84, v84, v86\n"
"s_waitcnt lgkmcnt(8)\n"
"v_mul_f32 v89, 0x3acccccd, v91\n"
"v_mfma_f32_16x16x32_bf16 v[52:55], v[56:59], v[56:59], v[52:55]\n"
"v_fma_f32 v84, -v91, v89, v84\n"
"v_fmamk_f32 v84, v84, 0x3acccccd, v27\n"
"v_mfma_f32_16x16x32_bf16 v[44:47], v[168:171], v[60:63], v[44:47]\n"
"ds_read_b64 v[56:57], v3 offset:14336\n"
"v_cmp_u_f32 vcc, v84, v84\n"
"v_mfma_f32_16x16x32_bf16 v[48:51], v[172:175], v[60:63], v[48:51]\n"
"ds_read_b64 v[58:59], v3 offset:14592\n"
"s_waitcnt lgkmcnt(6)\n"
"v_mfma_f32_16x16x32_bf16 v[44:47], v[176:179], v[64:67], v[44:47]\n"
"ds_read_b64 v[60:61], v3 offset:15360\n"
"v_max_f32 v84, v84, v27\n"
"v_mfma_f32_16x16x32_bf16 v[48:51], v[180:183], v[64:67], v[48:51]\n"
"ds_read_b64 v[62:63], v3 offset:15616\n"
"v_rsq_f32 v88, v84\n"
"v_mfma_f32_16x16x32_bf16 v[44:47], v[184:187], v[68:71], v[44:47]\n"
"ds_read_b64 v[64:65], v3 offset:16384\n"
"v_mul_f32 v89, -v89, v88\n"
"v_mfma_f32_16x16x32_bf16 v[48:51], v[188:191], v[68:71], v[48:51]\n"
"ds_read_b64 v[66:67], v3 offset:16640\n"
"s_cbranch_vccnz .Lsl_197\n"
".Lbk_198:\n"
"s_waitcnt lgkmcnt(6)\n"
"v_mfma_f32_16x16x32_bf16 v[44:47], v[192:195], v[72:75], v[44:47]\n"
"ds_read_b64 v[68:69], v4 offset:0\n"
"v_mul_f32 v36, v88, v36\n"
"v_mul_f32 v37, v88, v37\n"
"v_mfma_f32_16x16x32_bf16 v[48:51], v[196:199], v[72:75], v[48:51]\n"
"ds_read_b64 v[70:71], v4 offset:256\n"
"v_mul_f32 v38, v88, v38\n"
"v_mfma_f32_16x16x32_bf16 v[52:55], v[72:75], v[72:75], v[52:55]\n"
"ds_write_b64 v7, v[28:29] offset:44928\n"
"v_mfma_f32_16x16x32_bf16 v[44:47], v[200:203], v[76:79], v[44:47]\n"
"ds_read_b64 v[72:73], v5 offset:0\n"
"v_mul_f32 v39, v88, v39\n"
"v_mfma_f32_16x16x32_bf16 v[48:51], v[204:207], v[76:79], v[48:51]\n"
"ds_read_b64 v[74:75], v5 offset:256\n"
"s_waitcnt lgkmcnt(7)\n"
"v_mfma_f32_16x16x32_bf16 v[44:47], v[208:211], v[56:59], v[44:47]\n"
"ds_read_b64 v[76:77], v6 offset:0\n"
"v_fmac_f32 v36, v89, v16\n"
"v_mfma_f32_16x16x32_bf16 v[48:51], v[212:215], v[56:59], v[48:51]\n"
"ds_read_b64 v[78:79], v6 offset:256\n"
"v_fmac_f32 v37, v89, v17\n"
"v_mfma_f32_16x16x32_bf16 v[44:47], v[216:219], v[60:63], v[44:47]\n"
"ds_read_b64 v[56:57], v3 offset:20480\n"
"v_fmac_f32 v38, v89, v18\n"
"v_mfma_f32_16x16x32_bf16 v[48:51], v[220:223], v[60:63], v[48:51]\n"
"ds_read_b64 v[58:59], v3 offset:20736\n"
"s_waitcnt lgkmcnt(7)\n"
"v_mfma_f32_16x16x32_bf16 v[44:47], v[224:227], v[64:67], v[44:47]\n"
"ds_read_b64 v[60:61], v3 offset:21504\n"
"v_fmac_f32 v39, v89, v19\n"
"v_mfma_f32_16x16x32_bf16 v[48:51], v[228:231], v[64:67], v[48:51]\n"
"ds_read_b64 v[62:63], v3 offset:21760\n"
"v_mov_b32 v84, 0x3f803f80\n"
"v_mfma_f32_16x16x32_bf16 v[52:55], v[64:67], v[64:67], v[52:55]\n"
"v_mov_b32 v85, 0x3f803f80\n"
"ds_write_b64 v8, v[32:33] offset:44928\n"
"v_mfma_f32_16x16x32_bf16 v[44:47], v[232:235], v[68:71], v[44:47]\n"
"ds_read_b64 v[64:65], v3 offset:22528\n"
"v_cvt_pk_bf16_f32 v36, v36, v37\n"
"v_mfma_f32_16x16x32_bf16 v[48:51], v[236:239], v[68:71], v[48:51]\n"
"ds_read_b64 v[66:67], v3 offset:22784\n"
"s_waitcnt lgkmcnt(7)\n"
"v_mfma_f32_16x16x32_bf16 v[44:47], v[240:243], v[72:75], v[44:47]\n"
"ds_read_b64 v[68:69], v3 offset:23552\n"
"v_cndmask_b32 v94, v52, v53, s[34:35]\n"
"v_mfma_f32_16x16x32_bf16 v[48:51], v[244:247], v[72:75], v[48:51]\n"
"ds_read_b64 v[70:71], v3 offset:23808\n"
"v_cndmask_b32 v54, v54, v55, s[34:35]\n"
"v_mfma_f32_16x16x32_bf16 v[44:47], v[248:251], v[76:79], v[44:47]\n"
"ds_read_b64 v[72:73], v3 offset:24576\n"
"v_cndmask_b32 v94, v94, v54, s[20:21]\n"
"v_mfma_f32_16x16x32_bf16 v[48:51], v[252:255], v[76:79], v[48:51]\n"
"ds_read_b64 v[74:75], v3 offset:24832\n"
"s_waitcnt lgkmcnt(7)\n"
"v_mfma_f32_16x16x32_bf16 v[28:31], v[96:99], v[56:59], 0\n"
"ds_read_b64 v[76:77], v3 offset:25600\n"
"v_cvt_pk_bf16_f32 v37, v38, v39\n"
"v_mfma_f32_16x16x32_bf16 v[32:35], v[100:103], v[56:59], 0\n"
"ds_read_b64 v[78:79], v3 offset:25856\n"
"v_cndmask_b32 v94, v44, v94, s[22:23]\n"
"s_waitcnt vmcnt(0)\n"
"ds_write_b64 v93, v[84:85] offset:40960\n"
"v_mfma_f32_16x16x32_bf16 v[52:55], v[56:59], v[56:59], 0\n"
"ds_write_b32 v9, v94 offset:4352\n"
"v_mfma_f32_16x16x32_bf16 v[28:31], v[104:107], v[60:63], v[28:31]\n"
"ds_read_b64 v[56:57], v3 offset:26624\n"
"v_mul_f32 v40, v88, v40\n"
"v_mfma_f32_16x16x32_bf16 v[32:35], v[108:111], v[60:63], v[32:35]\n"
"ds_read_b64 v[58:59], v3 offset:26880\n"
"s_waitcnt lgkmcnt(8)\n"
"v_mfma_f32_16x16x32_bf16 v[28:31], v[112:115], v[64:67], v[28:31]\n"
"ds_read_b64 v[60:61], v3 offset:27648\n"
"v_mul_f32 v41, v88, v41\n"
"v_mfma_f32_16x16x32_bf16 v[32:35], v[116:119], v[64:67], v[32:35]\n"
"ds_read_b64 v[62:63], v3 offset:27904\n"
"v_mul_f32 v42, v88, v42\n"
"v_mfma_f32_16x16x32_bf16 v[28:31], v[120:123], v[68:71], v[28:31]\n"
"ds_read_b64 v[64:65], v3 offset:28672\n"
"v_mul_f32 v43, v88, v43\n"
"v_mfma_f32_16x16x32_bf16 v[32:35], v[124:127], v[68:71], v[32:35]\n"
"ds_read_b64 v[66:67], v3 offset:28928\n"
"s_waitcnt lgkmcnt(8)\n"
"v_mfma_f32_16x16x32_bf16 v[28:31], v[128:131], v[72:75], v[28:31]\n"
"ds_read_b64 v[68:69], v3 offset:29696\n"
"v_fmac_f32 v40, v89, v20\n"
"v_mfma_f32_16x16x32_bf16 v[32:35], v[132:135], v[72:75], v[32:35]\n"
"ds_read_b64 v[70:71], v3 offset:29952\n"
"v_fmac_f32 v41, v89, v21\n"
"v_mfma_f32_16x16x32_bf16 v[52:55], v[72:75], v[72:75], v[52:55]\n"
"ds_read_b128 v[84:87], v11 offset:4352\n"
"ds_read_b32 v91, v12 offset:4352\n"
"v_mfma_f32_16x16x32_bf16 v[28:31], v[136:139], v[76:79], v[28:31]\n"
"ds_read_b64 v[72:73], v3 offset:30720\n"
"v_fmac_f32 v42, v89, v22\n"
"v_mfma_f32_16x16x32_bf16 v[32:35], v[140:143], v[76:79], v[32:35]\n"
"ds_read_b64 v[74:75], v3 offset:30976\n"
"s_waitcnt lgkmcnt(8)\n"
"v_mfma_f32_16x16x32_bf16 v[28:31], v[144:147], v[56:59], v[28:31]\n"
"ds_read_b64 v[76:77], v3 offset:31744\n"
"v_fmac_f32 v43, v89, v23\n"
"v_mfma_f32_16x16x32_bf16 v[32:35], v[148:151], v[56:59], v[32:35]\n"
"ds_read_b64 v[78:79], v3 offset:32000\n"
"v_cvt_pk_bf16_f32 v40, v40, v41\n"
"v_mfma_f32_16x16x32_bf16 v[28:31], v[152:155], v[60:63], v[28:31]\n"
"ds_read_b64 v[56:57], v3 offset:32768\n"
"v_cvt_pk_bf16_f32 v41, v42, v43\n"
"v_mfma_f32_16x16x32_bf16 v[32:35], v[156:159], v[60:63], v[32:35]\n"
"ds_read_b64 v[58:59], v3 offset:33024\n"
"s_waitcnt lgkmcnt(8)\n"
"v_mfma_f32_16x16x32_bf16 v[28:31], v[160:163], v[64:67], v[28:31]\n"
"ds_read_b64 v[60:61], v3 offset:33792\n"
"s_waitcnt lgkmcnt(8)\n"
"v_add_f32 v84, v84, v85\n"
"v_add_f32 v86, v86, v87\n"
"v_mfma_f32_16x16x32_bf16 v[32:35], v[164:167], v[64:67], v[32:35]\n"
"ds_read_b64 v[62:63], v3 offset:34048\n"
"v_add_f32 v84, v84, v86\n"
"s_waitcnt lgkmcnt(8)\n"
"v_mul_f32 v89, 0x3acccccd, v91\n"
"v_mfma_f32_16x16x32_bf16 v[52:55], v[64:67], v[64:67], v[52:55]\n"
"v_fma_f32 v84, -v91, v89, v84\n"
"v_fmamk_f32 v84, v84, 0x3acccccd, v27\n"
"v_mfma_f32_16x16x32_bf16 v[28:31], v[168:171], v[68:71], v[28:31]\n"
"ds_read_b64 v[64:65], v3 offset:34816\n"
"v_cmp_u_f32 vcc, v84, v84\n"
"v_mfma_f32_16x16x32_bf16 v[32:35], v[172:175], v[68:71], v[32:35]\n"
"ds_read_b64 v[66:67], v3 offset:35072\n"
"s_waitcnt lgkmcnt(6)\n"
"v_mfma_f32_16x16x32_bf16 v[28:31], v[176:179], v[72:75], v[28:31]\n"
"ds_read_b64 v[68:69], v3 offset:35840\n"
"v_max_f32 v84, v84, v27\n"
"v_mfma_f32_16x16x32_bf16 v[32:35], v[180:183], v[72:75], v[32:35]\n"
"ds_read_b64 v[70:71], v3 offset:36096\n"
"v_rsq_f32 v88, v84\n"
"v_mfma_f32_16x16x32_bf16 v[28:31], v[184:187], v[76:79], v[28:31]\n"
"ds_read_b64 v[72:73], v3 offset:36864\n"
"v_mul_f32 v89, -v89, v88\n"
"v_mfma_f32_16x16x32_bf16 v[32:35], v[188:191], v[76:79], v[32:35]\n"
"ds_read_b64 v[74:75], v3 offset:37120\n"
"s_cbranch_vccnz .Lsl_201\n"
".Lbk_202:\n"
"s_waitcnt lgkmcnt(6)\n"
"v_mfma_f32_16x16x32_bf16 v[28:31], v[192:195], v[56:59], v[28:31]\n"
"ds_read_b64 v[76:77], v4 offset:20480\n"
"v_mul_f32 v44, v88, v44\n"
"v_mul_f32 v45, v88, v45\n"
"v_mfma_f32_16x16x32_bf16 v[32:35], v[196:199], v[56:59], v[32:35]\n"
"ds_read_b64 v[78:79], v4 offset:20736\n"
"v_mul_f32 v46, v88, v46\n"
"v_mul_f32 v47, v88, v47\n"
"v_mfma_f32_16x16x32_bf16 v[52:55], v[56:59], v[56:59], v[52:55]\n"
"ds_write_b64 v7, v[36:37] offset:48896\n"
"v_mfma_f32_16x16x32_bf16 v[28:31], v[200:203], v[60:63], v[28:31]\n"
"ds_read_b64 v[56:57], v5 offset:20480\n"
"v_fmac_f32 v44, v89, v16\n"
"v_fmac_f32 v45, v89, v17\n"
"v_mfma_f32_16x16x32_bf16 v[32:35], v[204:207], v[60:63], v[32:35]\n"
"ds_read_b64 v[58:59], v5 offset:20736\n"
"v_fmac_f32 v46, v89, v18\n"
"s_waitcnt lgkmcnt(7)\n"
"v_mfma_f32_16x16x32_bf16 v[28:31], v[208:211], v[64:67], v[28:31]\n"
"ds_read_b64 v[60:61], v6 offset:20480\n"
"v_fmac_f32 v47, v89, v19\n"
"v_cvt_pk_bf16_f32 v44, v44, v45\n"
"v_mfma_f32_16x16x32_bf16 v[32:35], v[212:215], v[64:67], v[32:35]\n"
"ds_read_b64 v[62:63], v6 offset:20736\n"
"v_cvt_pk_bf16_f32 v45, v46, v47\n"
"v_mul_f32 v48, v88, v48\n"
"v_mfma_f32_16x16x32_bf16 v[28:31], v[216:219], v[68:71], v[28:31]\n"
"ds_read_b64 v[64:65], v3 offset:40960\n"
"v_mul_f32 v49, v88, v49\n"
"v_mul_f32 v50, v88, v50\n"
"v_mfma_f32_16x16x32_bf16 v[32:35], v[220:223], v[68:71], v[32:35]\n"
"ds_read_b64 v[66:67], v3 offset:41216\n"
"v_mul_f32 v51, v88, v51\n"
"s_waitcnt lgkmcnt(7)\n"
"v_mfma_f32_16x16x32_bf16 v[28:31], v[224:227], v[72:75], v[28:31]\n"
"ds_read_b64 v[68:69], v3 offset:45056\n"
"v_fmac_f32 v48, v89, v20\n"
"v_mfma_f32_16x16x32_bf16 v[32:35], v[228:231], v[72:75], v[32:35]\n"
"ds_read_b64 v[70:71], v3 offset:45312\n"
"v_fmac_f32 v49, v89, v21\n"
"v_mfma_f32_16x16x32_bf16 v[52:55], v[72:75], v[72:75], v[52:55]\n"
"ds_write_b64 v8, v[40:41] offset:48896\n"
"v_mfma_f32_16x16x32_bf16 v[28:31], v[232:235], v[76:79], v[28:31]\n"
"ds_read_b64 v[72:73], v3 offset:49152\n"
"v_fmac_f32 v50, v89, v22\n"
"v_mfma_f32_16x16x32_bf16 v[32:35], v[236:239], v[76:79], v[32:35]\n"
"ds_read_b64 v[74:75], v3 offset:49408\n"
"s_waitcnt lgkmcnt(7)\n"
"v_mfma_f32_16x16x32_bf16 v[28:31], v[240:243], v[56:59], v[28:31]\n"
"ds_read_b64 v[76:77], v3 offset:53248\n"
"v_cndmask_b32 v94, v52, v53, s[34:35]\n"
"v_mfma_f32_16x16x32_bf16 v[32:35], v[244:247], v[56:59], v[32:35]\n"
"ds_read_b64 v[78:79], v3 offset:53504\n"
"v_cndmask_b32 v54, v54, v55, s[34:35]\n"
"v_mfma_f32_16x16x32_bf16 v[28:31], v[248:251], v[60:63], v[28:31]\n"
"ds_read_b64 v[56:57], v3 offset:57344\n"
"v_cndmask_b32 v94, v94, v54, s[20:21]\n"
"v_mfma_f32_16x16x32_bf16 v[32:35], v[252:255], v[60:63], v[32:35]\n"
"ds_read_b64 v[58:59], v3 offset:57600\n"
"s_waitcnt lgkmcnt(7)\n"
"v_mfma_f32_16x16x32_bf16 v[36:39], v[96:99], v[64:67], 0\n"
"ds_read_b64 v[60:61], v3 offset:41984\n"
"v_fmac_f32 v51, v89, v23\n"
"v_mfma_f32_16x16x32_bf16 v[40:43], v[100:103], v[64:67], 0\n"
"ds_read_b64 v[62:63], v3 offset:42240\n"
"v_cndmask_b32 v94, v28, v94, s[22:23]\n"
"v_mfma_f32_16x16x32_bf16 v[52:55], v[64:67], v[64:67], 0\n"
"ds_write_b32 v9, v94 offset:4672\n"
"v_mfma_f32_16x16x32_bf16 v[36:39], v[128:131], v[68:71], v[36:39]\n"
"ds_read_b64 v[64:65], v3 offset:43008\n"
"v_cvt_pk_bf16_f32 v48, v48, v49\n"
"v_mfma_f32_16x16x32_bf16 v[40:43], v[132:135], v[68:71], v[40:43]\n"
"ds_read_b64 v[66:67], v3 offset:43264\n"
"v_cvt_pk_bf16_f32 v49, v50, v51\n"
"v_mfma_f32_16x16x32_bf16 v[52:55], v[68:71], v[68:71], v[52:55]\n"
"ds_write_b64 v7, v[44:45] offset:52864\n"
"s_waitcnt lgkmcnt(8)\n"
"v_mfma_f32_16x16x32_bf16 v[36:39], v[160:163], v[72:75], v[36:39]\n"
"ds_read_b64 v[68:69], v3 offset:44032\n"
"v_mfma_f32_16x16x32_bf16 v[40:43], v[164:167], v[72:75], v[40:43]\n"
"ds_read_b64 v[70:71], v3 offset:44288\n"
"v_mfma_f32_16x16x32_bf16 v[52:55], v[72:75], v[72:75], v[52:55]\n"
"ds_write_b64 v8, v[48:49] offset:52864\n"
"v_mfma_f32_16x16x32_bf16 v[36:39], v[192:195], v[76:79], v[36:39]\n"
"ds_read_b64 v[72:73], v3 offset:46080\n"
"v_mfma_f32_16x16x32_bf16 v[40:43], v[196:199], v[76:79], v[40:43]\n"
"ds_read_b64 v[74:75], v3 offset:46336\n"
"v_mfma_f32_16x16x32_bf16 v[52:55], v[76:79], v[76:79], v[52:55]\n"
"ds_read_b128 v[84:87], v11 offset:4672\n"
"ds_read_b32 v91, v12 offset:4672\n"
"s_waitcnt lgkmcnt(11)\n"
"v_mfma_f32_16x16x32_bf16 v[36:39], v[224:227], v[56:59], v[36:39]\n"
"ds_read_b64 v[76:77], v3 offset:47104\n"
"v_mfma_f32_16x16x32_bf16 v[40:43], v[228:231], v[56:59], v[40:43]\n"
"ds_read_b64 v[78:79], v3 offset:47360\n"
"v_mfma_f32_16x16x32_bf16 v[52:55], v[56:59], v[56:59], v[52:55]\n"
"v_mfma_f32_16x16x32_bf16 v[36:39], v[104:107], v[60:63], v[36:39]\n"
"ds_read_b64 v[56:57], v3 offset:48128\n"
"v_mfma_f32_16x16x32_bf16 v[40:43], v[108:111], v[60:63], v[40:43]\n"
"ds_read_b64 v[58:59], v3 offset:48384\n"
"s_waitcnt lgkmcnt(9)\n"
"v_mfma_f32_16x16x32_bf16 v[36:39], v[112:115], v[64:67], v[36:39]\n"
"ds_read_b64 v[60:61], v3 offset:50176\n"
"ds_write_b32 v10, v52 offset:5312\n"
"v_cndmask_b32 v94, v52, v53, s[34:35]\n"
"v_cndmask_b32 v54, v54, v55, s[34:35]\n"
"v_mfma_f32_16x16x32_bf16 v[40:43], v[116:119], v[64:67], v[40:43]\n"
"ds_read_b64 v[62:63], v3 offset:50432\n"
"s_waitcnt lgkmcnt(8)\n"
"v_add_f32 v84, v84, v85\n"
"v_add_f32 v86, v86, v87\n"
"v_cndmask_b32 v94, v94, v54, s[20:21]\n"
"ds_write_b32 v9, v94 offset:4992\n"
"v_mfma_f32_16x16x32_bf16 v[36:39], v[120:123], v[68:71], v[36:39]\n"
"ds_read_b64 v[64:65], v3 offset:51200\n"
"v_add_f32 v84, v84, v86\n"
"s_waitcnt lgkmcnt(9)\n"
"v_mul_f32 v89, 0x3acccccd, v91\n"
"v_mfma_f32_16x16x32_bf16 v[40:43], v[124:127], v[68:71], v[40:43]\n"
"ds_read_b64 v[66:67], v3 offset:51456\n"
"v_fma_f32 v84, -v91, v89, v84\n"
"s_waitcnt lgkmcnt(8)\n"
"v_mfma_f32_16x16x32_bf16 v[36:39], v[136:139], v[72:75], v[36:39]\n"
"ds_read_b64 v[68:69], v3 offset:52224\n"
"v_fmamk_f32 v84, v84, 0x3acccccd, v27\n"
"v_cmp_u_f32 vcc, v84, v84\n"
"v_mfma_f32_16x16x32_bf16 v[40:43], v[140:143], v[72:75], v[40:43]\n"
"ds_read_b64 v[70:71], v3 offset:52480\n"
"v_max_f32 v84, v84, v27\n"
"v_rsq_f32 v88, v84\n"
"v_mfma_f32_16x16x32_bf16 v[36:39], v[144:147], v[76:79], v[36:39]\n"
"ds_read_b64 v[72:73], v3 offset:54272\n"
"v_mul_f32 v89, -v89, v88\n"
"v_mfma_f32_16x16x32_bf16 v[40:43], v[148:151], v[76:79], v[40:43]\n"
"ds_read_b64 v[74:75], v3 offset:54528\n"
"s_cbranch_vccnz .Lsl_205\n"
".Lbk_206:\n"
"s_waitcnt lgkmcnt(7)\n"
"v_mfma_f32_16x16x32_bf16 v[36:39], v[152:155], v[56:59], v[36:39]\n"
"ds_read_b64 v[76:77], v3 offset:55296\n"
"v_mul_f32 v28, v88, v28\n"
"v_mul_f32 v29, v88, v29\n"
"v_mfma_f32_16x16x32_bf16 v[40:43], v[156:159], v[56:59], v[40:43]\n"
"ds_read_b64 v[78:79], v3 offset:55552\n"
"ds_read_b128 v[44:47], v11 offset:4992\n"
"ds_read_b128 v[48:51], v11 offset:5312\n"
"v_mfma_f32_16x16x32_bf16 v[36:39], v[168:171], v[60:63], v[36:39]\n"
"ds_read_b64 v[56:57], v3 offset:56320\n"
"v_mul_f32 v30, v88, v30\n"
"v_mul_f32 v31, v88, v31\n"
"v_mfma_f32_16x16x32_bf16 v[40:43], v[172:175], v[60:63], v[40:43]\n"
"ds_read_b64 v[58:59], v3 offset:56576\n"
"v_fmac_f32 v28, v89, v16\n"
"s_waitcnt lgkmcnt(8)\n"
"v_mfma_f32_16x16x32_bf16 v[36:39], v[176:179], v[64:67], v[36:39]\n"
"ds_read_b64 v[60:61], v4 offset:40960\n"
"v_fmac_f32 v29, v89, v17\n"
"v_fmac_f32 v30, v89, v18\n"
"v_mfma_f32_16x16x32_bf16 v[40:43], v[180:183], v[64:67], v[40:43]\n"
"ds_read_b64 v[62:63], v4 offset:41216\n"
"v_fmac_f32 v31, v89, v19\n"
"v_cvt_pk_bf16_f32 v28, v28, v29\n"
"v_mfma_f32_16x16x32_bf16 v[36:39], v[184:187], v[68:71], v[36:39]\n"
"ds_read_b64 v[64:65], v5 offset:40960\n"
"v_cvt_pk_bf16_f32 v29, v30, v31\n"
"ds_write_b64 v7, v[28:29] offset:56832\n"
"v_mfma_f32_16x16x32_bf16 v[40:43], v[188:191], v[68:71], v[40:43]\n"
"ds_read_b64 v[66:67], v5 offset:41216\n"
"v_mul_f32 v32, v88, v32\n"
"s_waitcnt lgkmcnt(9)\n"
"v_mfma_f32_16x16x32_bf16 v[36:39], v[200:203], v[72:75], v[36:39]\n"
"ds_read_b64 v[68:69], v6 offset:40960\n"
"s_waitcnt lgkmcnt(9)\n"
"v_add_f32 v44, v44, v45\n"
"v_mul_f32 v33, v88, v33\n"
"v_mfma_f32_16x16x32_bf16 v[40:43], v[204:207], v[72:75], v[40:43]\n"
"ds_read_b64 v[70:71], v6 offset:41216\n"
"v_add_f32 v46, v46, v47\n"
"v_mul_f32 v34, v88, v34\n"
"v_mfma_f32_16x16x32_bf16 v[36:39], v[208:211], v[76:79], v[36:39]\n"
"s_waitcnt lgkmcnt(9)\n"
"v_add_f32 v48, v48, v49\n"
"v_add_f32 v50, v50, v51\n"
"v_mul_f32 v35, v88, v35\n"
"v_mfma_f32_16x16x32_bf16 v[40:43], v[212:215], v[76:79], v[40:43]\n"
"v_add_f32 v48, v48, v50\n"
"v_fmac_f32 v32, v89, v20\n"
"s_waitcnt lgkmcnt(5)\n"
"v_mfma_f32_16x16x32_bf16 v[36:39], v[216:219], v[56:59], v[36:39]\n"
"v_add_f32 v44, v44, v46\n"
"v_mul_f32 v53, 0x3acccccd, v48\n"
"v_fmac_f32 v33, v89, v21\n"
"v_mfma_f32_16x16x32_bf16 v[40:43], v[220:223], v[56:59], v[40:43]\n"
"v_fma_f32 v44, -v48, v53, v44\n"
"v_fmamk_f32 v44, v44, 0x3acccccd, v27\n"
"v_fmac_f32 v34, v89, v22\n"
"v_mfma_f32_16x16x32_bf16 v[36:39], v[232:235], v[60:63], v[36:39]\n"
"v_cmp_u_f32 vcc, v44, v44\n"
"v_max_f32 v44, v44, v27\n"
"v_fmac_f32 v35, v89, v23\n"
"v_mfma_f32_16x16x32_bf16 v[40:43], v[236:239], v[60:63], v[40:43]\n"
"v_rsq_f32 v52, v44\n"
"s_waitcnt lgkmcnt(0)\n"
"v_mfma_f32_16x16x32_bf16 v[36:39], v[240:243], v[64:67], v[36:39]\n"
"v_mul_f32 v53, -v53, v52\n"
"v_cvt_pk_bf16_f32 v32, v32, v33\n"
"v_cvt_pk_bf16_f32 v33, v34, v35\n"
"v_mfma_f32_16x16x32_bf16 v[40:43], v[244:247], v[64:67], v[40:43]\n"
"s_cbranch_vccnz .Lsl_209\n"
".Lbk_210:\n"
"ds_write_b64 v8, v[32:33] offset:56832\n"
"v_mfma_f32_16x16x32_bf16 v[36:39], v[248:251], v[68:71], v[36:39]\n"
"v_mfma_f32_16x16x32_bf16 v[40:43], v[252:255], v[68:71], v[40:43]\n"
"v_cmp_u_f32 vcc, v92, v92\n"
"s_nop 3\n"
"v_mul_f32 v36, v52, v36\n"
"v_mul_f32 v37, v52, v37\n"
"v_mul_f32 v38, v52, v38\n"
"v_mul_f32 v39, v52, v39\n"
"v_fmac_f32 v36, v53, v16\n"
"v_fmac_f32 v37, v53, v17\n"
"v_fmac_f32 v38, v53, v18\n"
"v_fmac_f32 v39, v53, v19\n"
"v_cvt_pk_bf16_f32 v36, v36, v37\n"
"v_cvt_pk_bf16_f32 v37, v38, v39\n"
"ds_write_b64 v7, v[36:37] offset:40960\n"
"v_mul_f32 v40, v52, v40\n"
"v_mul_f32 v41, v52, v41\n"
"v_mul_f32 v42, v52, v42\n"
"v_mul_f32 v43, v52, v43\n"
"v_fmac_f32 v40, v53, v20\n"
"v_fmac_f32 v41, v53, v21\n"
"v_fmac_f32 v42, v53, v22\n"
"v_fmac_f32 v43, v53, v23\n"
"v_cvt_pk_bf16_f32 v40, v40, v41\n"
"v_cvt_pk_bf16_f32 v41, v42, v43\n"
"ds_write_b64 v8, v[40:41] offset:40960\n"
"s_cbranch_vccnz .Lrd_213\n"
".Lgo_178:\n"
"ds_write_b32 v13, v13 offset:448\n"
"v_mov_b32 v84, 0\n"
"v_mov_b32 v85, 0\n"
"v_mov_b32 v86, 0\n"
"v_mov_b32 v87, 0\n"
"v_mov_b32 v30, 0\n"
"v_mov_b32 v31, 0\n"
"v_mov_b32 v34, 0\n"
"v_mov_b32 v35, 0\n"
"s_mov_b32 s17, 0x100000\n"
".Ltp_253:\n"
"ds_read_b32 v90, v14 offset:448\n"
"s_sub_u32 s17, s17, 1\n"
"s_cbranch_scc1 .Lou_254\n"
"s_waitcnt lgkmcnt(0)\n"
"v_cmp_eq_u32 vcc, 0, v90\n"
"s_cbranch_vccnz .Ltp_253\n"
".Lou_254:\n"
"s_waitcnt lgkmcnt(0)\n"
"s_mov_b64 exec, 0xffff\n"
"ds_read_b64 v[30:31], v24 offset:44960\n"
"ds_read_b64 v[34:35], v24 offset:45000\n"
"s_mov_b64 exec, -1\n"
"ds_read_b64 v[28:29], v25 offset:44928\n"
"ds_read_b64 v[68:69], v25 offset:41040\n"
"ds_read_b64 v[70:71], v24 offset:41072\n"
"ds_read_b64 v[76:77], v25 offset:45008\n"
"ds_read_b64 v[78:79], v24 offset:45040\n"
"ds_read_b64 v[32:33], v25 offset:44968\n"
"ds_read_b64 v[72:73], v25 offset:41080\n"
"ds_read_b64 v[74:75], v24 offset:41112\n"
"ds_read_b64 v[80:81], v25 offset:45048\n"
"ds_read_b64 v[82:83], v24 offset:45080\n"
"ds_read_b64_tr_b16 v[44:45], v26 offset:40960\n"
"ds_read_b64_tr_b16 v[46:47], v26 offset:44928\n"
"ds_read_b64_tr_b16 v[48:49], v26 offset:41000\n"
"v_mov_b32 v64, 0x0c0c0c0c\n"
"v_mov_b32 v65, 0xc0c0100\n"
"v_mov_b32 v66, 0x1000c0c\n"
"v_cndmask_b32 v65, v65, v66, s[34:35]\n"
"v_cndmask_b32 v36, v65, v64, s[36:37]\n"
"v_cndmask_b32 v38, v64, v65, s[36:37]\n"
"v_mov_b32 v65, 0xc0c0302\n"
"v_mov_b32 v66, 0x3020c0c\n"
"v_cndmask_b32 v65, v65, v66, s[34:35]\n"
"v_cndmask_b32 v37, v65, v64, s[38:39]\n"
"v_cndmask_b32 v39, v64, v65, s[38:39]\n"
"s_waitcnt lgkmcnt(10)\n"
"v_mfma_f32_16x16x32_bf16 v[68:71], v[68:71], v[28:31], v[84:87]\n"
"s_waitcnt lgkmcnt(8)\n"
"v_mfma_f32_16x16x32_bf16 v[76:79], v[76:79], v[28:31], 0\n"
"ds_read_b64_tr_b16 v[50:51], v26 offset:44968\n"
"ds_read_b64_tr_b16 v[52:53], v26 offset:40992\n"
"ds_read_b64_tr_b16 v[54:55], v26 offset:44960\n"
"s_waitcnt lgkmcnt(8)\n"
"v_mfma_f32_16x16x32_bf16 v[72:75], v[72:75], v[32:35], v[84:87]\n"
"s_waitcnt lgkmcnt(6)\n"
"v_mfma_f32_16x16x32_bf16 v[80:83], v[80:83], v[32:35], 0\n"
"ds_read_b64_tr_b16 v[56:57], v26 offset:41032\n"
"ds_read_b64_tr_b16 v[58:59], v26 offset:45000\n"
"v_mov_b32 v40, 0x3f803f80\n"
"v_mov_b32 v41, 0x3f803f80\n"
"v_mov_b32 v42, 0x3f803f80\n"
"v_mov_b32 v43, 0x3f803f80\n"
"v_cndmask_b32 v60, v68, v69, s[34:35]\n"
"v_cndmask_b32 v84, v76, v77, s[34:35]\n"
"v_cndmask_b32 v60, v60, v84, s[36:37]\n"
"v_cndmask_b32 v61, v70, v71, s[34:35]\n"
"v_cndmask_b32 v84, v78, v79, s[34:35]\n"
"v_cndmask_b32 v61, v61, v84, s[38:39]\n"
"v_max_f32 v64, v60, v61\n"
"v_max_f32 v66, v60, v61\n"
"v_cndmask_b32 v62, v72, v73, s[34:35]\n"
"v_cndmask_b32 v84, v80, v81, s[34:35]\n"
"v_cndmask_b32 v62, v62, v84, s[36:37]\n"
"v_cndmask_b32 v63, v74, v75, s[34:35]\n"
"v_cndmask_b32 v84, v82, v83, s[34:35]\n"
"v_cndmask_b32 v63, v63, v84, s[38:39]\n"
"v_max_f32 v65, v62, v63\n"
"v_max_f32 v67, v62, v63\n"
"v_permlane16_swap_b32 v64, v66\n"
"s_nop 0\n"
"v_permlane16_swap_b32 v65, v67\n"
"v_max_f32 v84, v64, v66\n"
"v_max_f32 v64, v64, v66\n"
"v_max_f32 v85, v65, v67\n"
"v_max_f32 v65, v65, v67\n"
"v_permlane32_swap_b32 v64, v84\n"
"s_nop 0\n"
"v_permlane32_swap_b32 v65, v85\n"
"v_max_f32 v64, v64, v84\n"
"v_max_f32 v65, v65, v85\n"
"v_mul_f32 v64, 0xbea52b5b, v64\n"
"v_mul_f32 v65, 0xbea52b5b, v65\n"
"v_fmamk_f32 v60, v60, 0x3ea52b5b, v64\n"
"v_fmamk_f32 v61, v61, 0x3ea52b5b, v64\n"
"v_fmamk_f32 v62, v62, 0x3ea52b5b, v65\n"
"v_fmamk_f32 v63, v63, 0x3ea52b5b, v65\n"
"v_exp_f32 v60, v60\n"
"v_exp_f32 v61, v61\n"
"v_exp_f32 v62, v62\n"
"v_exp_f32 v63, v63\n"
"v_cvt_pk_bf16_f32 v64, v60, v61\n"
"v_cvt_pk_bf16_f32 v65, v62, v63\n"
"v_perm_b32 v28, v64, v64, v36\n"
"v_perm_b32 v29, v64, v64, v37\n"
"v_perm_b32 v30, v64, v64, v38\n"
"v_perm_b32 v31, v64, v64, v39\n"
"v_perm_b32 v32, v65, v65, v36\n"
"v_perm_b32 v33, v65, v65, v37\n"
"v_perm_b32 v34, v65, v65, v38\n"
"v_perm_b32 v35, v65, v65, v39\n"
"v_mfma_f32_16x16x32_bf16 v[68:71], v[40:43], v[28:31], 0\n"
"s_nop 0\n"
"v_mfma_f32_16x16x32_bf16 v[72:75], v[40:43], v[32:35], 0\n"
"s_waitcnt lgkmcnt(6)\n"
"v_mfma_f32_16x16x32_bf16 v[44:47], v[44:47], v[28:31], 0\n"
"s_waitcnt lgkmcnt(4)\n"
"v_mfma_f32_16x16x32_bf16 v[48:51], v[48:51], v[32:35], 0\n"
"s_waitcnt lgkmcnt(2)\n"
"v_mfma_f32_16x16x32_bf16 v[52:55], v[52:55], v[28:31], 0\n"
"s_waitcnt lgkmcnt(0)\n"
"v_mfma_f32_16x16x32_bf16 v[56:59], v[56:59], v[32:35], 0\n"
"v_cndmask_b32 v88, 2.0, v1, s[40:41]\n"
"v_rcp_f32 v64, v68\n"
"v_rcp_f32 v65, v72\n"
"s_add_u32 s31, s30, 0xba0000\n"
"v_mul_f32 v44, v64, v44\n"
"v_mul_f32 v45, v64, v45\n"
"v_mul_f32 v46, v64, v46\n"
"v_mul_f32 v47, v64, v47\n"
"v_mul_f32 v48, v65, v48\n"
"v_mul_f32 v49, v65, v49\n"
"v_mul_f32 v50, v65, v50\n"
"v_mul_f32 v51, v65, v51\n"
"v_cvt_pk_bf16_f32 v44, v44, v45\n"
"v_cvt_pk_bf16_f32 v45, v46, v47\n"
"v_cvt_pk_bf16_f32 v46, v48, v49\n"
"v_cvt_pk_bf16_f32 v47, v50, v51\n"
"v_min_u32 v44, -2, v44\n"
"v_min_u32 v45, -2, v45\n"
"v_min_u32 v46, -2, v46\n"
"v_min_u32 v47, -2, v47\n"
"buffer_store_dwordx4 v[44:47], v1, s[4:7], s31 offen offset:0 sc1\n"
"v_mul_f32 v52, v64, v52\n"
"v_mul_f32 v53, v64, v53\n"
"v_mul_f32 v54, v64, v54\n"
"v_mul_f32 v55, v64, v55\n"
"v_mul_f32 v56, v65, v56\n"
"v_mul_f32 v57, v65, v57\n"
"v_mul_f32 v58, v65, v58\n"
"v_mul_f32 v59, v65, v59\n"
"v_cvt_pk_bf16_f32 v52, v52, v53\n"
"v_cvt_pk_bf16_f32 v53, v54, v55\n"
"v_cvt_pk_bf16_f32 v54, v56, v57\n"
"v_cvt_pk_bf16_f32 v55, v58, v59\n"
"v_min_u32 v52, -2, v52\n"
"v_min_u32 v53, -2, v53\n"
"v_min_u32 v54, -2, v54\n"
"v_min_u32 v55, -2, v55\n"
"buffer_store_dwordx4 v[52:55], v88, s[4:7], s31 offen offset:1024 sc1\n"
"ds_write_b32 v13, v13 offset:464\n"
"s_nop 2\n"
"s_cmp_lt_i32 s45, 0\n"
"s_cbranch_scc1 .Lnc_255\n"
"s_setprio 1\n"
"s_lshl_b32 s79, s3, 10\n"
"v_lshl_add_u32 v34, v0, 4, s79\n"
"v_lshrrev_b32 v59, 7, v34\n"
"v_and_b32 v58, 0x70, v34\n"
"s_mov_b32 s79, 0x5000\n"
"v_mad_i32_i24 v33, v59, s79, v58\n"
"s_mov_b32 s17, 0x100000\n"
".Ltp_256:\n"
"s_sleep 1\n"
"ds_read_b32 v42, v14 offset:480\n"
"s_sub_u32 s17, s17, 1\n"
"s_cbranch_scc1 .Lou_257\n"
"s_waitcnt lgkmcnt(0)\n"
"v_cmp_eq_u32 vcc, 0, v42\n"
"s_cbranch_vccnz .Ltp_256\n"
".Lou_257:\n"
"s_waitcnt lgkmcnt(0)\n"
"s_lshl_b32 s79, s45, 8\n"
"s_add_u32 s46, s79, 0x1048000\n"
"s_add_u32 s47, s46, 0x140000\n"
"s_and_b32 s79, s2, 7\n"
"s_lshl_b32 s79, s79, 14\n"
"s_add_u32 s48, s79, 0x1588000\n"
"s_add_u32 s49, s48, 0x1000\n"
"v_and_b32 v94, 0xffffff80, v33\n"
"v_add_u32 v94, v94, v33\n"
"buffer_load_dwordx4 v[78:81], v94, s[4:7], s46 offen offset:0 sc1\n"
"buffer_load_dwordx4 v[82:85], v94, s[4:7], s46 offen offset:128 sc1\n"
"buffer_load_dwordx4 v[86:89], v94, s[4:7], s47 offen offset:0 sc1\n"
"buffer_load_dwordx4 v[90:93], v94, s[4:7], s47 offen offset:128 sc1\n"
"buffer_load_dwordx4 v[70:73], v34, s[4:7], s48 offen sc1\n"
"buffer_load_dwordx4 v[74:77], v34, s[4:7], s49 offen sc1\n"
"v_and_b32 v51, 15, v0\n"
"v_and_b32 v50, 1, v51\n"
"v_and_b32 v49, 2, v51\n"
"v_lshlrev_b32 v48, 4, v0\n"
"v_lshrrev_b32 v47, 4, v0\n"
"v_xor_b32 v46, s3, v51\n"
"v_and_b32 v45, 1, v46\n"
"v_lshlrev_b32 v44, 1, v45\n"
"s_lshl_b32 s79, s3, 10\n"
"v_add_u32 v59, s79, v48\n"
"v_lshrrev_b32 v59, 2, v59\n"
"s_lshl_b32 s79, s3, 10\n"
"v_add_u32 v58, s79, v48\n"
"v_lshrrev_b32 v58, 6, v58\n"
"v_and_b32 v58, 24, v58\n"
"v_xor_b32 v59, v59, v58\n"
"v_add_u32 v36, 0xa000, v59\n"
"s_lshl_b32 s79, s3, 8\n"
"s_add_u32 s79, s79, 0xa000\n"
"v_lshrrev_b32 v59, 2, v51\n"
"v_lshrrev_b32 v58, 1, v47\n"
"v_xor_b32 v59, v59, v58\n"
"v_and_b32 v59, 1, v59\n"
"v_add_u32 v59, v44, v59\n"
"v_lshrrev_b32 v58, 2, v51\n"
"v_lshrrev_b32 v57, 3, v51\n"
"v_lshlrev_b32 v57, 7, v57\n"
"v_lshl_add_u32 v58, v58, 11, v57\n"
"v_lshl_add_u32 v58, v47, 6, v58\n"
"v_lshl_add_u32 v58, v49, 4, v58\n"
"v_lshl_add_u32 v59, v59, 3, v58\n"
"v_add_u32 v31, s79, v59\n"
"s_mul_i32 s79, s3, 0x2100\n"
"s_add_u32 s79, s79, 0x13840\n"
"v_lshlrev_b32 v59, 3, v47\n"
"v_sub_u32 v59, v59, v51\n"
"v_add_u32 v59, 0x3ef, v59\n"
"v_lshrrev_b32 v59, 2, v59\n"
"v_lshlrev_b32 v58, 3, v47\n"
"v_sub_u32 v58, v58, v51\n"
"v_add_u32 v58, 0x3ef, v58\n"
"v_and_b32 v58, 3, v58\n"
"v_mul_i32_i24 v58, 0x840, v58\n"
"v_lshl_add_u32 v59, v59, 3, v58\n"
"v_add_u32 v28, s79, v59\n"
"s_mul_i32 s79, s3, 0x1200\n"
"s_add_u32 s79, s79, 0x1c420\n"
"v_lshlrev_b32 v59, 4, v47\n"
"v_lshl_add_u32 v59, v51, 5, v59\n"
"v_add_u32 v29, s79, v59\n"
"s_lshl_b32 s79, s3, 11\n"
"s_lshr_b32 s78, s3, 1\n"
"s_lshl_b32 s78, s78, 7\n"
"s_add_u32 s79, s79, s78\n"
"s_add_u32 s79, s79, 0xa000\n"
"s_and_b32 s78, s3, 1\n"
"v_lshrrev_b32 v59, 2, v51\n"
"v_xor_b32 v59, v47, v59\n"
"v_xor_b32 v59, s78, v59\n"
"v_and_b32 v59, 3, v59\n"
"v_lshlrev_b32 v59, 3, v59\n"
"v_lshl_add_u32 v59, v51, 5, v59\n"
"v_add_u32 v30, s79, v59\n"
"v_xor_b32 v32, 8, v31\n"
"s_lshl_b32 s79, s3, 10\n"
"v_add_u32 v59, s79, v48\n"
"v_lshrrev_b32 v59, 2, v59\n"
"v_add_u32 v35, 0x1c600, v59\n"
"v_xor_b32 v37, 8, v36\n"
"v_mov_b32 v59, 0xffff0000\n"
"v_mov_b32 v58, 0xffff\n"
"v_cmp_eq_i32 vcc, 1, v50\n"
"s_nop 1\n"
"v_cndmask_b32 v59, v58, v59, vcc\n"
"v_lshrrev_b32 v58, 1, v51\n"
"v_lshlrev_b32 v57, 1, v47\n"
"v_cmp_eq_i32 vcc, v58, v57\n"
"s_nop 1\n"
"v_cndmask_b32 v38, 0, v59, vcc\n"
"v_mov_b32 v59, 0xffff0000\n"
"v_mov_b32 v58, 0xffff\n"
"v_cmp_eq_i32 vcc, 1, v50\n"
"s_nop 1\n"
"v_cndmask_b32 v59, v58, v59, vcc\n"
"v_lshrrev_b32 v58, 1, v51\n"
"v_lshl_add_u32 v57, v47, 1, 1\n"
"v_cmp_eq_i32 vcc, v58, v57\n"
"s_nop 1\n"
"v_cndmask_b32 v39, 0, v59, vcc\n"
"s_lshl_b32 s79, s3, 2\n"
"s_add_u32 s79, s79, 0x26800\n"
"v_mov_b32 v40, s79\n"
"s_lshl_b32 s79, s3, 10\n"
"v_add_u32 v59, s79, v48\n"
"v_lshrrev_b32 v59, 6, v59\n"
"s_lshl_b32 s79, s3, 10\n"
"v_add_u32 v58, s79, v48\n"
"v_and_b32 v58, 48, v58\n"
"v_mul_i32_i24 v58, 0x50, v58\n"
"s_mov_b32 s79, 0x2800\n"
"v_mad_i32_i24 v41, v59, s79, v58\n"
"ds_read_b32 v40, v40\n"
"s_waitcnt lgkmcnt(0)\n"
"v_sub_u32 v44, 0x7f000000, v40\n"
"v_lshrrev_b32 v45, 16, v44\n"
"v_or_b32 v44, v44, v45\n"
"v_and_b32 v38, v44, v38\n"
"v_and_b32 v39, v44, v39\n"
"s_mov_b32 s52, 0x05040100\n"
"s_mov_b32 s53, 0x07060302\n"
"s_mov_b32 s17, 32768\n"
".Ltp_258:\n"
"s_waitcnt vmcnt(0)\n"
"s_sub_u32 s17, s17, 1\n"
"s_cbranch_scc1 .Lou_259\n"
"v_bitop3_b32 v54, v79, v81, s16 bitop3:0x7e\n"
"v_bitop3_b32 v55, v83, v85, s16 bitop3:0x7e\n"
"v_bitop3_b32 v56, v87, v89, s16 bitop3:0x7e\n"
"v_bitop3_b32 v57, v91, v93, s16 bitop3:0x7e\n"
"v_or3_b32 v58, v54, v55, v56\n"
"v_or_b32 v58, v58, v57\n"
"v_cmp_ne_u32 vcc, 0, v58\n"
"s_cbranch_vccz .Lou_259\n"
"s_sleep 2\n"
"v_cmp_ne_u32 vcc, 0, v54\n"
"s_cbranch_vccz .Lsk_260\n"
"s_mov_b64 exec, vcc\n"
"buffer_load_dwordx4 v[78:81], v94, s[4:7], s46 offen offset:0 sc1\n"
"s_mov_b64 exec, -1\n"
".Lsk_260:\n"
"v_cmp_ne_u32 vcc, 0, v55\n"
"s_cbranch_vccz .Lsk_261\n"
"s_mov_b64 exec, vcc\n"
"buffer_load_dwordx4 v[82:85], v94, s[4:7], s46 offen offset:128 sc1\n"
"s_mov_b64 exec, -1\n"
".Lsk_261:\n"
"v_cmp_ne_u32 vcc, 0, v56\n"
"s_cbranch_vccz .Lsk_262\n"
"s_mov_b64 exec, vcc\n"
"buffer_load_dwordx4 v[86:89], v94, s[4:7], s47 offen offset:0 sc1\n"
"s_mov_b64 exec, -1\n"
".Lsk_262:\n"
"v_cmp_ne_u32 vcc, 0, v57\n"
"s_cbranch_vccz .Lsk_263\n"
"s_mov_b64 exec, vcc\n"
"buffer_load_dwordx4 v[90:93], v94, s[4:7], s47 offen offset:128 sc1\n"
"s_mov_b64 exec, -1\n"
".Lsk_263:\n"
"s_branch .Ltp_258\n"
".Lou_259:\n"
"s_waitcnt vmcnt(0)\n"
"v_perm_b32 v54, v82, v78, s52\n"
"v_perm_b32 v55, v82, v78, s53\n"
"v_perm_b32 v56, v84, v80, s52\n"
"v_perm_b32 v57, v84, v80, s53\n"
"ds_write_b32 v36, v54 offset:0\n"
"ds_write_b32 v37, v55 offset:2048\n"
"ds_write_b32 v36, v56 offset:4224\n"
"ds_write_b32 v37, v57 offset:6272\n"
"v_perm_b32 v54, v90, v86, s52\n"
"v_perm_b32 v55, v90, v86, s53\n"
"v_perm_b32 v56, v92, v88, s52\n"
"v_perm_b32 v57, v92, v88, s53\n"
"ds_write_b32 v36, v54 offset:1024\n"
"ds_write_b32 v37, v55 offset:3072\n"
"ds_write_b32 v36, v56 offset:5248\n"
"ds_write_b32 v37, v57 offset:7296\n"
"v_lshlrev_b32 v54, 16, v78\n"
"v_lshlrev_b32 v55, 16, v82\n"
"v_and_b32 v56, 0xffff0000, v78\n"
"v_and_b32 v57, 0xffff0000, v82\n"
"v_lshlrev_b32 v58, 16, v80\n"
"v_lshlrev_b32 v59, 16, v84\n"
"v_and_b32 v60, 0xffff0000, v80\n"
"v_and_b32 v61, 0xffff0000, v84\n"
"v_lshlrev_b32 v62, 16, v86\n"
"v_lshlrev_b32 v63, 16, v90\n"
"v_and_b32 v64, 0xffff0000, v86\n"
"v_and_b32 v65, 0xffff0000, v90\n"
"v_lshlrev_b32 v66, 16, v88\n"
"v_lshlrev_b32 v67, 16, v92\n"
"v_and_b32 v68, 0xffff0000, v88\n"
"v_and_b32 v69, 0xffff0000, v92\n"
"s_mov_b32 s17, 32768\n"
".Ltp_264:\n"
"s_waitcnt vmcnt(0)\n"
"s_sub_u32 s17, s17, 1\n"
"s_cbranch_scc1 .Lou_265\n"
"v_max_u32 v78, v70, v71\n"
"v_max3_u32 v78, v72, v73, v78\n"
"v_max3_u32 v78, v74, v75, v78\n"
"v_max3_u32 v78, v76, v77, v78\n"
"v_cmp_eq_u32 vcc, -1, v78\n"
"s_cbranch_vccz .Lou_265\n"
"v_max_u32 v78, v72, v73\n"
"v_max3_u32 v78, v70, v71, v78\n"
"v_cmp_eq_u32 vcc, -1, v78\n"
"s_cbranch_vccz .Lsk_266\n"
"s_mov_b64 exec, vcc\n"
"buffer_load_dwordx4 v[70:73], v34, s[4:7], s48 offen sc1\n"
"s_mov_b64 exec, -1\n"
".Lsk_266:\n"
"v_max_u32 v78, v76, v77\n"
"v_max3_u32 v78, v74, v75, v78\n"
"v_cmp_eq_u32 vcc, -1, v78\n"
"s_cbranch_vccz .Lsk_267\n"
"s_mov_b64 exec, vcc\n"
"buffer_load_dwordx4 v[74:77], v34, s[4:7], s49 offen sc1\n"
"s_mov_b64 exec, -1\n"
".Lsk_267:\n"
"s_branch .Ltp_264\n"
".Lou_265:\n"
"s_waitcnt vmcnt(0)\n"
"v_fma_f32 v54, v54, v71, v70\n"
"v_fma_f32 v55, v55, v73, v72\n"
"v_cvt_pk_f16_f32 v54, v54, v55\n"
"v_fma_f32 v56, v56, v71, v70\n"
"v_fma_f32 v57, v57, v73, v72\n"
"v_cvt_pk_f16_f32 v56, v56, v57\n"
"v_fma_f32 v58, v58, v71, v70\n"
"v_fma_f32 v59, v59, v73, v72\n"
"v_cvt_pk_f16_f32 v58, v58, v59\n"
"v_fma_f32 v60, v60, v71, v70\n"
"v_fma_f32 v61, v61, v73, v72\n"
"v_cvt_pk_f16_f32 v60, v60, v61\n"
"ds_write2st64_b32 v35, v54, v56 offset0:0 offset1:18\n"
"ds_write2st64_b32 v35, v58, v60 offset0:36 offset1:54\n"
"v_fma_f32 v62, v62, v75, v74\n"
"v_fma_f32 v63, v63, v77, v76\n"
"v_cvt_pk_f16_f32 v62, v62, v63\n"
"v_fma_f32 v64, v64, v75, v74\n"
"v_fma_f32 v65, v65, v77, v76\n"
"v_cvt_pk_f16_f32 v64, v64, v65\n"
"v_fma_f32 v66, v66, v75, v74\n"
"v_fma_f32 v67, v67, v77, v76\n"
"v_cvt_pk_f16_f32 v66, v66, v67\n"
"v_fma_f32 v68, v68, v75, v74\n"
"v_fma_f32 v69, v69, v77, v76\n"
"v_cvt_pk_f16_f32 v68, v68, v69\n"
"ds_write2st64_b32 v35, v62, v64 offset0:4 offset1:22\n"
"ds_write2st64_b32 v35, v66, v68 offset0:40 offset1:58\n"
"ds_write_b32 v13, v13 offset:496\n"
"s_mov_b32 s17, 0x100000\n"
".Ltp_268:\n"
"ds_read_b32 v42, v14 offset:496\n"
"s_sub_u32 s17, s17, 1\n"
"s_cbranch_scc1 .Lou_269\n"
"s_waitcnt lgkmcnt(0)\n"
"v_cmp_eq_u32 vcc, 0, v42\n"
"s_cbranch_vccnz .Ltp_268\n"
".Lou_269:\n"
"s_waitcnt lgkmcnt(0)\n"
"s_mov_b32 s50, 0xfffefffe\n"
"ds_read_b64 v[60:61], v28 offset:1984\n"
"ds_read_b64 v[62:63], v28 offset:1992\n"
"ds_read_b128 v[64:67], v29 offset:1984\n"
"ds_read_b128 v[68:71], v29 offset:1472\n"
"ds_read_b64 v[72:73], v28 offset:1472\n"
"ds_read_b64 v[74:75], v28 offset:1480\n"
"ds_read_b128 v[76:79], v29 offset:960\n"
"s_waitcnt lgkmcnt(1)\n"
"v_mfma_f32_16x16x32_f16 v[56:59], v[60:63], v[64:67], 0\n"
"ds_read_b64 v[80:81], v28 offset:960\n"
"ds_read_b64 v[82:83], v28 offset:968\n"
"v_mfma_f32_16x16x32_f16 v[52:55], v[60:63], v[68:71], 0\n"
"ds_read_b128 v[84:87], v29 offset:448\n"
"ds_read_b64 v[88:89], v28 offset:448\n"
"v_mfma_f32_16x16x32_f16 v[56:59], v[72:75], v[68:71], v[56:59]\n"
"ds_read_b64 v[90:91], v28 offset:456\n"
"s_waitcnt lgkmcnt(3)\n"
"v_mfma_f32_16x16x32_f16 v[48:51], v[60:63], v[76:79], 0\n"
"ds_read_b64 v[92:93], v28 offset:1920\n"
"ds_read_b64 v[94:95], v28 offset:1928\n"
"v_mfma_f32_16x16x32_f16 v[52:55], v[72:75], v[76:79], v[52:55]\n"
"ds_read_b128 v[2:5], v29 offset:1920\n"
"ds_read_b128 v[34:37], v29 offset:1408\n"
"v_mfma_f32_16x16x32_f16 v[56:59], v[80:83], v[76:79], v[56:59]\n"
"ds_read_b64 v[64:65], v28 offset:1408\n"
"s_waitcnt lgkmcnt(7)\n"
"v_mfma_f32_16x16x32_f16 v[44:47], v[60:63], v[84:87], 0\n"
"ds_read_b64 v[66:67], v28 offset:1416\n"
"ds_read_b128 v[68:71], v29 offset:896\n"
"v_mfma_f32_16x16x32_f16 v[48:51], v[72:75], v[84:87], v[48:51]\n"
"ds_read_b64 v[76:77], v28 offset:896\n"
"ds_read_b64 v[78:79], v28 offset:904\n"
"v_mfma_f32_16x16x32_f16 v[52:55], v[80:83], v[84:87], v[52:55]\n"
"ds_read_b128 v[60:63], v29 offset:384\n"
"s_waitcnt lgkmcnt(6)\n"
"v_mfma_f32_16x16x32_f16 v[56:59], v[88:91], v[84:87], v[56:59]\n"
"ds_read_b64 v[72:73], v28 offset:384\n"
"v_mfma_f32_16x16x32_f16 v[56:59], v[92:95], v[2:5], v[56:59]\n"
"ds_read_b64 v[74:75], v28 offset:392\n"
"ds_read_b64 v[80:81], v28 offset:1856\n"
"v_mfma_f32_16x16x32_f16 v[52:55], v[92:95], v[34:37], v[52:55]\n"
"ds_read_b64 v[82:83], v28 offset:1864\n"
"s_waitcnt lgkmcnt(7)\n"
"v_mfma_f32_16x16x32_f16 v[56:59], v[64:67], v[34:37], v[56:59]\n"
"ds_read_b128 v[84:87], v29 offset:1856\n"
"ds_read_b128 v[88:91], v29 offset:1344\n"
"v_mfma_f32_16x16x32_f16 v[48:51], v[92:95], v[68:71], v[48:51]\n"
"ds_read_b64 v[2:3], v30 offset:0\n"
"ds_read_b64 v[34:35], v28 offset:1344\n"
"v_mfma_f32_16x16x32_f16 v[52:55], v[64:67], v[68:71], v[52:55]\n"
"ds_read_b64 v[36:37], v28 offset:1352\n"
"s_waitcnt lgkmcnt(9)\n"
"v_mfma_f32_16x16x32_f16 v[56:59], v[76:79], v[68:71], v[56:59]\n"
"ds_read_b128 v[68:71], v29 offset:832\n"
"v_mfma_f32_16x16x32_f16 v[44:47], v[92:95], v[60:63], v[44:47]\n"
"ds_read_b64 v[92:93], v28 offset:832\n"
"v_mfma_f32_16x16x32_f16 v[48:51], v[64:67], v[60:63], v[48:51]\n"
"ds_read_b64 v[94:95], v28 offset:840\n"
"s_waitcnt lgkmcnt(7)\n"
"v_mfma_f32_16x16x32_f16 v[52:55], v[76:79], v[60:63], v[52:55]\n"
"ds_read_b128 v[64:67], v29 offset:320\n"
"v_mfma_f32_16x16x32_f16 v[56:59], v[72:75], v[60:63], v[56:59]\n"
"v_mfma_f32_16x16x32_f16 v[56:59], v[80:83], v[84:87], v[56:59]\n"
"ds_read_b64 v[76:77], v28 offset:320\n"
"s_waitcnt lgkmcnt(5)\n"
"v_mfma_f32_16x16x32_f16 v[52:55], v[80:83], v[88:91], v[52:55]\n"
"ds_read_b64 v[78:79], v28 offset:328\n"
"s_lshl_b32 s46, s45, 3\n"
"v_mfma_f32_16x16x16_bf16 v[44:47], v[38:39], v[2:3], v[44:47]\n"
"ds_read_b64 v[60:61], v30 offset:512\n"
"ds_read_b64 v[72:73], v28 offset:1792\n"
"v_mfma_f32_16x16x32_f16 v[56:59], v[34:37], v[88:91], v[56:59]\n"
"ds_read_b64 v[74:75], v28 offset:1800\n"
"s_waitcnt lgkmcnt(6)\n"
"v_mfma_f32_16x16x32_f16 v[48:51], v[80:83], v[68:71], v[48:51]\n"
"ds_read_b128 v[84:87], v29 offset:1792\n"
"ds_read_b128 v[2:5], v29 offset:1280\n"
"v_mfma_f32_16x16x32_f16 v[52:55], v[34:37], v[68:71], v[52:55]\n"
"ds_read_b64 v[88:89], v28 offset:1280\n"
"v_mfma_f32_16x16x32_f16 v[56:59], v[92:95], v[68:71], v[56:59]\n"
"ds_read_b64 v[90:91], v28 offset:1288\n"
"s_waitcnt lgkmcnt(9)\n"
"v_mfma_f32_16x16x32_f16 v[44:47], v[80:83], v[64:67], v[44:47]\n"
"ds_read_b128 v[68:71], v29 offset:768\n"
"ds_read_b64 v[80:81], v28 offset:768\n"
"v_mfma_f32_16x16x32_f16 v[48:51], v[34:37], v[64:67], v[48:51]\n"
"ds_read_b64 v[82:83], v28 offset:776\n"
"v_mfma_f32_16x16x32_f16 v[52:55], v[92:95], v[64:67], v[52:55]\n"
"ds_read_b128 v[34:37], v29 offset:256\n"
"s_waitcnt lgkmcnt(7)\n"
"v_mfma_f32_16x16x32_f16 v[56:59], v[76:79], v[64:67], v[56:59]\n"
"v_mfma_f32_16x16x16_bf16 v[48:51], v[38:39], v[60:61], v[48:51]\n"
"ds_read_b64 v[92:93], v28 offset:256\n"
"v_mfma_f32_16x16x32_f16 v[56:59], v[72:75], v[84:87], v[56:59]\n"
"ds_read_b64 v[94:95], v28 offset:264\n"
"s_waitcnt lgkmcnt(5)\n"
"v_mfma_f32_16x16x32_f16 v[52:55], v[72:75], v[2:5], v[52:55]\n"
"ds_read_b64 v[64:65], v28 offset:1728\n"
"ds_read_b64 v[66:67], v28 offset:1736\n"
"v_mfma_f32_16x16x32_f16 v[56:59], v[88:91], v[2:5], v[56:59]\n"
"ds_read_b128 v[76:79], v29 offset:1728\n"
"ds_read_b128 v[60:63], v29 offset:1216\n"
"v_mfma_f32_16x16x32_f16 v[48:51], v[72:75], v[68:71], v[48:51]\n"
"ds_read_b64 v[84:85], v28 offset:1216\n"
"s_waitcnt lgkmcnt(7)\n"
"v_mfma_f32_16x16x32_f16 v[52:55], v[88:91], v[68:71], v[52:55]\n"
"ds_read_b64 v[86:87], v28 offset:1224\n"
"v_mfma_f32_16x16x32_f16 v[56:59], v[80:83], v[68:71], v[56:59]\n"
"ds_read_b128 v[2:5], v29 offset:704\n"
"v_mfma_f32_16x16x32_f16 v[44:47], v[72:75], v[34:37], v[44:47]\n"
"ds_read_b64 v[68:69], v30 offset:1024\n"
"s_waitcnt lgkmcnt(8)\n"
"v_mfma_f32_16x16x32_f16 v[48:51], v[88:91], v[34:37], v[48:51]\n"
"ds_read_b64 v[72:73], v28 offset:704\n"
"ds_read_b64 v[74:75], v28 offset:712\n"
"v_mfma_f32_16x16x32_f16 v[52:55], v[80:83], v[34:37], v[52:55]\n"
"ds_read_b128 v[88:91], v29 offset:192\n"
"v_mfma_f32_16x16x32_f16 v[56:59], v[92:95], v[34:37], v[56:59]\n"
"s_waitcnt lgkmcnt(5)\n"
"v_mfma_f32_16x16x32_f16 v[56:59], v[64:67], v[76:79], v[56:59]\n"
"ds_read_b64 v[80:81], v28 offset:192\n"
"v_mfma_f32_16x16x32_f16 v[52:55], v[64:67], v[60:63], v[52:55]\n"
"ds_read_b64 v[82:83], v28 offset:200\n"
"ds_read_b64 v[34:35], v28 offset:1664\n"
"v_mfma_f32_16x16x32_f16 v[56:59], v[84:87], v[60:63], v[56:59]\n"
"ds_read_b64 v[36:37], v28 offset:1672\n"
"s_waitcnt lgkmcnt(7)\n"
"v_mfma_f32_16x16x32_f16 v[48:51], v[64:67], v[2:5], v[48:51]\n"
"ds_read_b128 v[92:95], v29 offset:1664\n"
"ds_read_b128 v[76:79], v29 offset:1152\n"
"v_mfma_f32_16x16x16_bf16 v[52:55], v[38:39], v[68:69], v[52:55]\n"
"ds_read_b64 v[60:61], v28 offset:1152\n"
"ds_read_b64 v[62:63], v28 offset:1160\n"
"s_nop 3\n"
"v_mfma_f32_16x16x32_f16 v[52:55], v[84:87], v[2:5], v[52:55]\n"
"ds_read_b128 v[68:71], v29 offset:640\n"
"s_waitcnt lgkmcnt(9)\n"
"v_mfma_f32_16x16x32_f16 v[56:59], v[72:75], v[2:5], v[56:59]\n"
"ds_read_b64 v[2:3], v28 offset:640\n"
"v_mfma_f32_16x16x32_f16 v[44:47], v[64:67], v[88:91], v[44:47]\n"
"ds_read_b64 v[4:5], v28 offset:648\n"
"v_mfma_f32_16x16x32_f16 v[48:51], v[84:87], v[88:91], v[48:51]\n"
"ds_read_b128 v[64:67], v29 offset:128\n"
"s_waitcnt lgkmcnt(7)\n"
"v_mfma_f32_16x16x32_f16 v[52:55], v[72:75], v[88:91], v[52:55]\n"
"ds_read_b64 v[84:85], v30 offset:1536\n"
"v_mfma_f32_16x16x32_f16 v[56:59], v[80:83], v[88:91], v[56:59]\n"
"v_mfma_f32_16x16x32_f16 v[56:59], v[34:37], v[92:95], v[56:59]\n"
"ds_read_b64 v[72:73], v28 offset:128\n"
"s_waitcnt lgkmcnt(5)\n"
"v_mfma_f32_16x16x32_f16 v[52:55], v[34:37], v[76:79], v[52:55]\n"
"ds_read_b64 v[74:75], v28 offset:136\n"
"ds_read_b64 v[88:89], v28 offset:1600\n"
"v_mfma_f32_16x16x32_f16 v[56:59], v[60:63], v[76:79], v[56:59]\n"
"ds_read_b64 v[90:91], v28 offset:1608\n"
"ds_read_b128 v[80:83], v29 offset:1600\n"
"v_mfma_f32_16x16x32_f16 v[48:51], v[34:37], v[68:71], v[48:51]\n"
"ds_read_b128 v[92:95], v29 offset:1088\n"
"s_waitcnt lgkmcnt(7)\n"
"v_mfma_f32_16x16x32_f16 v[52:55], v[60:63], v[68:71], v[52:55]\n"
"ds_read_b64 v[76:77], v28 offset:1088\n"
"v_mfma_f32_16x16x32_f16 v[56:59], v[2:5], v[68:71], v[56:59]\n"
"ds_read_b64 v[78:79], v28 offset:1096\n"
"v_mfma_f32_16x16x32_f16 v[44:47], v[34:37], v[64:67], v[44:47]\n"
"ds_read_b128 v[68:71], v29 offset:576\n"
"s_waitcnt lgkmcnt(9)\n"
"s_nop 0\n"
"v_mfma_f32_16x16x16_bf16 v[56:59], v[38:39], v[84:85], v[56:59]\n"
"ds_read_b64 v[34:35], v28 offset:576\n"
"ds_read_b64 v[36:37], v28 offset:584\n"
"v_mfma_f32_16x16x32_f16 v[48:51], v[60:63], v[64:67], v[48:51]\n"
"ds_read_b128 v[84:87], v29 offset:64\n"
"v_mfma_f32_16x16x32_f16 v[52:55], v[2:5], v[64:67], v[52:55]\n"
"s_waitcnt lgkmcnt(6)\n"
"v_mfma_f32_16x16x32_f16 v[56:59], v[72:75], v[64:67], v[56:59]\n"
"ds_read_b64 v[60:61], v28 offset:64\n"
"v_mfma_f32_16x16x32_f16 v[56:59], v[88:91], v[80:83], v[56:59]\n"
"ds_read_b64 v[62:63], v28 offset:72\n"
"ds_read_b64 v[2:3], v28 offset:1536\n"
"v_mfma_f32_16x16x32_f16 v[52:55], v[88:91], v[92:95], v[52:55]\n"
"ds_read_b64 v[4:5], v28 offset:1544\n"
"s_waitcnt lgkmcnt(7)\n"
"v_mfma_f32_16x16x32_f16 v[56:59], v[76:79], v[92:95], v[56:59]\n"
"ds_read_b128 v[64:67], v29 offset:0\n"
"ds_read_b128 v[72:75], v29 offset:512\n"
"v_mfma_f32_16x16x32_f16 v[48:51], v[88:91], v[68:71], v[48:51]\n"
"ds_read_b64 v[80:81], v28 offset:1024\n"
"ds_read_b64 v[82:83], v28 offset:1032\n"
"v_mfma_f32_16x16x32_f16 v[52:55], v[76:79], v[68:71], v[52:55]\n"
"ds_read_b128 v[92:95], v29 offset:1024\n"
"s_waitcnt lgkmcnt(9)\n"
"v_mfma_f32_16x16x32_f16 v[56:59], v[34:37], v[68:71], v[56:59]\n"
"ds_read_b64 v[68:69], v28 offset:512\n"
"v_mfma_f32_16x16x32_f16 v[44:47], v[88:91], v[84:87], v[44:47]\n"
"ds_read_b64 v[70:71], v28 offset:520\n"
"v_mfma_f32_16x16x32_f16 v[48:51], v[76:79], v[84:87], v[48:51]\n"
"ds_read_b128 v[88:91], v29 offset:1536\n"
"s_waitcnt lgkmcnt(7)\n"
"v_mfma_f32_16x16x32_f16 v[52:55], v[34:37], v[84:87], v[52:55]\n"
"v_mfma_f32_16x16x32_f16 v[56:59], v[60:63], v[84:87], v[56:59]\n"
"ds_read_b64 v[76:77], v28 offset:0\n"
"v_mfma_f32_16x16x32_f16 v[44:47], v[2:5], v[64:67], v[44:47]\n"
"ds_read_b64 v[78:79], v28 offset:8\n"
"s_waitcnt lgkmcnt(5)\n"
"v_mfma_f32_16x16x32_f16 v[48:51], v[2:5], v[72:75], v[48:51]\n"
"v_mfma_f32_16x16x32_f16 v[48:51], v[80:83], v[64:67], v[48:51]\n"
"v_mfma_f32_16x16x32_f16 v[52:55], v[2:5], v[92:95], v[52:55]\n"
"v_mul_f32 v44, v40, v44\n"
"v_mul_f32 v45, v40, v45\n"
"v_mul_f32 v46, v40, v46\n"
"s_waitcnt lgkmcnt(2)\n"
"v_mfma_f32_16x16x32_f16 v[52:55], v[80:83], v[72:75], v[52:55]\n"
"v_mul_f32 v47, v40, v47\n"
"v_cvt_pk_bf16_f32 v44, v44, v45\n"
"v_cvt_pk_bf16_f32 v45, v46, v47\n"
"v_mfma_f32_16x16x32_f16 v[52:55], v[68:71], v[64:67], v[52:55]\n"
"v_pk_min_u16 v44, v44, s50\n"
"v_pk_min_u16 v45, v45, s50\n"
"ds_write_b64 v30, v[44:45] offset:0\n"
"v_mfma_f32_16x16x32_f16 v[56:59], v[2:5], v[88:91], v[56:59]\n"
"s_waitcnt lgkmcnt(1)\n"
"v_mfma_f32_16x16x32_f16 v[56:59], v[80:83], v[92:95], v[56:59]\n"
"v_mul_f32 v48, v40, v48\n"
"v_mul_f32 v49, v40, v49\n"
"v_mfma_f32_16x16x32_f16 v[56:59], v[68:71], v[72:75], v[56:59]\n"
"v_mul_f32 v50, v40, v50\n"
"v_mul_f32 v51, v40, v51\n"
"v_mfma_f32_16x16x32_f16 v[56:59], v[76:79], v[64:67], v[56:59]\n"
"v_cvt_pk_bf16_f32 v48, v48, v49\n"
"v_cvt_pk_bf16_f32 v49, v50, v51\n"
"v_pk_min_u16 v48, v48, s50\n"
"v_pk_min_u16 v49, v49, s50\n"
"ds_write_b64 v30, v[48:49] offset:512\n"
"v_mul_f32 v52, v40, v52\n"
"v_mul_f32 v53, v40, v53\n"
"v_mul_f32 v54, v40, v54\n"
"v_mul_f32 v55, v40, v55\n"
"v_cvt_pk_bf16_f32 v52, v52, v53\n"
"v_cvt_pk_bf16_f32 v53, v54, v55\n"
"v_pk_min_u16 v52, v52, s50\n"
"v_pk_min_u16 v53, v53, s50\n"
"ds_write_b64 v30, v[52:53] offset:1024\n"
"v_mul_f32 v56, v40, v56\n"
"v_mul_f32 v57, v40, v57\n"
"v_mul_f32 v58, v40, v58\n"
"v_mul_f32 v59, v40, v59\n"
"v_cvt_pk_bf16_f32 v56, v56, v57\n"
"v_cvt_pk_bf16_f32 v57, v58, v59\n"
"v_pk_min_u16 v56, v56, s50\n"
"v_pk_min_u16 v57, v57, s50\n"
"ds_write_b64 v30, v[56:57] offset:1536\n"
"ds_write_b32 v13, v13 offset:512\n"
"s_mov_b32 s17, 0x100000\n"
".Ltp_270:\n"
"ds_read_b32 v42, v14 offset:512\n"
"s_sub_u32 s17, s17, 1\n"
"s_cbranch_scc1 .Lou_271\n"
"s_waitcnt lgkmcnt(0)\n"
"v_cmp_eq_u32 vcc, 0, v42\n"
"s_cbranch_vccnz .Ltp_270\n"
".Lou_271:\n"
"s_waitcnt lgkmcnt(0)\n"
"ds_read_b64_tr_b16 v[60:61], v31 offset:0\n"
"ds_read_b64_tr_b16 v[62:63], v32 offset:0\n"
"ds_read_b64_tr_b16 v[64:65], v31 offset:1024\n"
"ds_read_b64_tr_b16 v[66:67], v32 offset:1024\n"
"s_mov_b32 s17, 0x100000\n"
".Ltp_272:\n"
"s_sleep 1\n"
"ds_read_b32 v42, v14 offset:528\n"
"s_sub_u32 s17, s17, 1\n"
"s_cbranch_scc1 .Lou_273\n"
"s_waitcnt lgkmcnt(0)\n"
"v_cmp_eq_u32 vcc, 0, v42\n"
"s_cbranch_vccnz .Ltp_272\n"
".Lou_273:\n"
"s_waitcnt lgkmcnt(0)\n"
"s_add_u32 s47, s46, 0x1400\n"
"s_add_u32 s48, s46, 0xa0000\n"
"s_add_u32 s49, s46, 0xa1400\n"
"buffer_store_dwordx2 v[60:61], v41, s[12:15], s46 offen\n"
"buffer_store_dwordx2 v[62:63], v41, s[12:15], s47 offen\n"
"buffer_store_dwordx2 v[64:65], v41, s[12:15], s48 offen\n"
"buffer_store_dwordx2 v[66:67], v41, s[12:15], s49 offen\n"
"s_setprio 1\n"
"s_nop 0\n"
".Lnc_255:\n"
"s_add_u32 s31, s2, s3\n"
"s_cmp_lg_u32 s31, 0\n"
"s_cbranch_scc1 .Lfin_274\n"
"v_mov_b32 v32, s16\n"
"s_mov_b64 exec, 1\n"
"buffer_store_dword v32, off, s[4:7], 0 sc1\n"
"s_mov_b64 exec, -1\n"
".Lfin_274:\n"
"s_endpgm\n"
"s_branch .L_xend\n"
".Lsl_7:\n"
"s_nop 7\n"
"s_mov_b32 s17, 0x100000\n"
".Ltp_9:\n"
"ds_read_b32 v90, v14 offset:0\n"
"s_sub_u32 s17, s17, 1\n"
"s_cbranch_scc1 .Lou_10\n"
"s_waitcnt lgkmcnt(0)\n"
"v_cmp_eq_u32 vcc, 0, v90\n"
"s_cbranch_vccnz .Ltp_9\n"
".Lou_10:\n"
"s_waitcnt lgkmcnt(0)\n"
"s_branch .Lbk_8\n"
".Lsl_18:\n"
"s_nop 7\n"
"s_mov_b32 s17, 0x100000\n"
".Ltp_20:\n"
"ds_read_b32 v90, v14 offset:48\n"
"s_sub_u32 s17, s17, 1\n"
"s_cbranch_scc1 .Lou_21\n"
"s_waitcnt lgkmcnt(0)\n"
"v_cmp_eq_u32 vcc, 0, v90\n"
"s_cbranch_vccnz .Ltp_20\n"
".Lou_21:\n"
"s_waitcnt lgkmcnt(0)\n"
"s_branch .Lbk_19\n"
".Lsl_22:\n"
"s_nop 7\n"
"s_mov_b32 s17, 0x100000\n"
".Ltp_24:\n"
"ds_read_b32 v90, v14 offset:16\n"
"s_sub_u32 s17, s17, 1\n"
"s_cbranch_scc1 .Lou_25\n"
"s_waitcnt lgkmcnt(0)\n"
"v_cmp_eq_u32 vcc, 0, v90\n"
"s_cbranch_vccnz .Ltp_24\n"
".Lou_25:\n"
"s_waitcnt lgkmcnt(0)\n"
"s_branch .Lbk_23\n"
".Lsl_26:\n"
"s_nop 7\n"
"s_mov_b32 s17, 0x100000\n"
".Ltp_28:\n"
"ds_read_b32 v90, v14 offset:64\n"
"s_sub_u32 s17, s17, 1\n"
"s_cbranch_scc1 .Lou_29\n"
"s_waitcnt lgkmcnt(0)\n"
"v_cmp_eq_u32 vcc, 0, v90\n"
"s_cbranch_vccnz .Ltp_28\n"
".Lou_29:\n"
"s_waitcnt lgkmcnt(0)\n"
"s_branch .Lbk_27\n"
".Lsl_34:\n"
"s_nop 7\n"
"s_mov_b32 s17, 0x100000\n"
".Ltp_36:\n"
"ds_read_b128 v[84:87], v11 offset:0\n"
"ds_read_b32 v91, v12 offset:0\n"
"s_sub_u32 s17, s17, 1\n"
"s_cbranch_scc1 .Lou_37\n"
"s_waitcnt lgkmcnt(0)\n"
"v_max3_u32 v88, v84, v85, v91\n"
"v_max3_u32 v88, v86, v87, v88\n"
"v_cmp_eq_u32 vcc, -1, v88\n"
"s_cbranch_vccnz .Ltp_36\n"
".Lou_37:\n"
"s_waitcnt lgkmcnt(0)\n"
"v_add_f32 v84, v84, v85\n"
"v_add_f32 v86, v86, v87\n"
"v_add_f32 v84, v84, v86\n"
"v_mul_f32 v89, 0x3acccccd, v91\n"
"v_fma_f32 v84, -v91, v89, v84\n"
"v_fmamk_f32 v84, v84, 0x3acccccd, v27\n"
"v_max_f32 v84, v84, v27\n"
"v_rsq_f32 v88, v84\n"
"s_nop 0\n"
"v_mul_f32 v89, -v89, v88\n"
"s_nop 1\n"
"s_branch .Lbk_35\n"
".Lsl_38:\n"
"s_nop 7\n"
"s_mov_b32 s17, 0x100000\n"
".Ltp_40:\n"
"ds_read_b128 v[84:87], v11 offset:320\n"
"ds_read_b32 v91, v12 offset:320\n"
"s_sub_u32 s17, s17, 1\n"
"s_cbranch_scc1 .Lou_41\n"
"s_waitcnt lgkmcnt(0)\n"
"v_max3_u32 v88, v84, v85, v91\n"
"v_max3_u32 v88, v86, v87, v88\n"
"v_cmp_eq_u32 vcc, -1, v88\n"
"s_cbranch_vccnz .Ltp_40\n"
".Lou_41:\n"
"s_waitcnt lgkmcnt(0)\n"
"v_add_f32 v84, v84, v85\n"
"v_add_f32 v86, v86, v87\n"
"v_add_f32 v84, v84, v86\n"
"v_mul_f32 v89, 0x3acccccd, v91\n"
"v_fma_f32 v84, -v91, v89, v84\n"
"v_fmamk_f32 v84, v84, 0x3acccccd, v27\n"
"v_max_f32 v84, v84, v27\n"
"v_rsq_f32 v88, v84\n"
"s_nop 0\n"
"v_mul_f32 v89, -v89, v88\n"
"s_nop 1\n"
"s_branch .Lbk_39\n"
".Lsl_42:\n"
"s_nop 7\n"
"s_mov_b32 s17, 0x100000\n"
".Ltp_44:\n"
"ds_read_b128 v[84:87], v11 offset:640\n"
"ds_read_b32 v91, v12 offset:640\n"
"s_sub_u32 s17, s17, 1\n"
"s_cbranch_scc1 .Lou_45\n"
"s_waitcnt lgkmcnt(0)\n"
"v_max3_u32 v88, v84, v85, v91\n"
"v_max3_u32 v88, v86, v87, v88\n"
"v_cmp_eq_u32 vcc, -1, v88\n"
"s_cbranch_vccnz .Ltp_44\n"
".Lou_45:\n"
"s_waitcnt lgkmcnt(0)\n"
"v_add_f32 v84, v84, v85\n"
"v_add_f32 v86, v86, v87\n"
"v_add_f32 v84, v84, v86\n"
"v_mul_f32 v89, 0x3acccccd, v91\n"
"v_fma_f32 v84, -v91, v89, v84\n"
"v_fmamk_f32 v84, v84, 0x3acccccd, v27\n"
"v_max_f32 v84, v84, v27\n"
"v_rsq_f32 v88, v84\n"
"s_nop 0\n"
"v_mul_f32 v89, -v89, v88\n"
"s_nop 1\n"
"s_branch .Lbk_43\n"
".Lsl_46:\n"
"s_nop 7\n"
"s_mov_b32 s17, 0x100000\n"
".Ltp_48:\n"
"ds_read_b128 v[84:87], v11 offset:960\n"
"ds_read_b32 v91, v12 offset:960\n"
"s_sub_u32 s17, s17, 1\n"
"s_cbranch_scc1 .Lou_49\n"
"s_waitcnt lgkmcnt(0)\n"
"v_max3_u32 v88, v84, v85, v91\n"
"v_max3_u32 v88, v86, v87, v88\n"
"v_cmp_eq_u32 vcc, -1, v88\n"
"s_cbranch_vccnz .Ltp_48\n"
".Lou_49:\n"
"s_waitcnt lgkmcnt(0)\n"
"v_add_f32 v84, v84, v85\n"
"v_add_f32 v86, v86, v87\n"
"v_add_f32 v84, v84, v86\n"
"v_mul_f32 v89, 0x3acccccd, v91\n"
"v_fma_f32 v84, -v91, v89, v84\n"
"v_fmamk_f32 v84, v84, 0x3acccccd, v27\n"
"v_max_f32 v84, v84, v27\n"
"v_rsq_f32 v88, v84\n"
"s_nop 0\n"
"v_mul_f32 v89, -v89, v88\n"
"s_nop 1\n"
"s_branch .Lbk_47\n"
".Lsl_50:\n"
"s_nop 7\n"
"s_mov_b32 s17, 0x100000\n"
".Ltp_52:\n"
"ds_read_b128 v[44:47], v11 offset:1280\n"
"ds_read_b128 v[48:51], v11 offset:1600\n"
"s_sub_u32 s17, s17, 1\n"
"s_cbranch_scc1 .Lou_53\n"
"s_waitcnt lgkmcnt(0)\n"
"v_max3_u32 v52, v44, v45, v51\n"
"v_max3_u32 v52, v46, v47, v52\n"
"v_max3_u32 v52, v48, v49, v52\n"
"v_max3_u32 v52, v50, v51, v52\n"
"v_cmp_eq_u32 vcc, -1, v52\n"
"s_cbranch_vccnz .Ltp_52\n"
".Lou_53:\n"
"s_waitcnt lgkmcnt(0)\n"
"v_add_f32 v44, v44, v45\n"
"v_add_f32 v46, v46, v47\n"
"v_add_f32 v48, v48, v49\n"
"v_add_f32 v50, v50, v51\n"
"v_add_f32 v48, v48, v50\n"
"v_add_f32 v44, v44, v46\n"
"v_mul_f32 v53, 0x3acccccd, v48\n"
"v_fma_f32 v44, -v48, v53, v44\n"
"v_fmamk_f32 v44, v44, 0x3acccccd, v27\n"
"v_max_f32 v44, v44, v27\n"
"v_rsq_f32 v52, v44\n"
"s_nop 0\n"
"v_mul_f32 v53, -v53, v52\n"
"s_nop 1\n"
"s_branch .Lbk_51\n"
".Lsl_105:\n"
"s_nop 7\n"
"s_mov_b32 s17, 0x100000\n"
".Ltp_107:\n"
"ds_read_b32 v90, v14 offset:256\n"
"s_sub_u32 s17, s17, 1\n"
"s_cbranch_scc1 .Lou_108\n"
"s_waitcnt lgkmcnt(0)\n"
"v_cmp_eq_u32 vcc, 0, v90\n"
"s_cbranch_vccnz .Ltp_107\n"
".Lou_108:\n"
"s_waitcnt lgkmcnt(0)\n"
"s_branch .Lbk_106\n"
".Lsl_109:\n"
"s_nop 7\n"
"s_mov_b32 s17, 0x100000\n"
".Ltp_111:\n"
"ds_read_b128 v[84:87], v11 offset:1856\n"
"ds_read_b32 v91, v12 offset:1856\n"
"s_sub_u32 s17, s17, 1\n"
"s_cbranch_scc1 .Lou_112\n"
"s_waitcnt lgkmcnt(0)\n"
"v_max3_u32 v88, v84, v85, v91\n"
"v_max3_u32 v88, v86, v87, v88\n"
"v_cmp_eq_u32 vcc, -1, v88\n"
"s_cbranch_vccnz .Ltp_111\n"
".Lou_112:\n"
"s_waitcnt lgkmcnt(0)\n"
"v_add_f32 v84, v84, v85\n"
"v_add_f32 v86, v86, v87\n"
"v_add_f32 v84, v84, v86\n"
"v_mul_f32 v89, 0x3acccccd, v91\n"
"v_fma_f32 v84, -v91, v89, v84\n"
"v_fmamk_f32 v84, v84, 0x3acccccd, v27\n"
"v_add_f32 v92, v92, v84\n"
"v_max_f32 v84, v84, v27\n"
"v_rsq_f32 v88, v84\n"
"s_nop 0\n"
"v_mul_f32 v89, -v89, v88\n"
"s_nop 1\n"
"s_branch .Lbk_110\n"
".Lsl_113:\n"
"s_nop 7\n"
"s_mov_b32 s17, 0x100000\n"
".Ltp_115:\n"
"ds_read_b128 v[84:87], v11 offset:2176\n"
"ds_read_b32 v91, v12 offset:2176\n"
"s_sub_u32 s17, s17, 1\n"
"s_cbranch_scc1 .Lou_116\n"
"s_waitcnt lgkmcnt(0)\n"
"v_max3_u32 v88, v84, v85, v91\n"
"v_max3_u32 v88, v86, v87, v88\n"
"v_cmp_eq_u32 vcc, -1, v88\n"
"s_cbranch_vccnz .Ltp_115\n"
".Lou_116:\n"
"s_waitcnt lgkmcnt(0)\n"
"v_add_f32 v84, v84, v85\n"
"v_add_f32 v86, v86, v87\n"
"v_add_f32 v84, v84, v86\n"
"v_mul_f32 v89, 0x3acccccd, v91\n"
"v_fma_f32 v84, -v91, v89, v84\n"
"v_fmamk_f32 v84, v84, 0x3acccccd, v27\n"
"v_add_f32 v92, v92, v84\n"
"v_max_f32 v84, v84, v27\n"
"v_rsq_f32 v88, v84\n"
"s_nop 0\n"
"v_mul_f32 v89, -v89, v88\n"
"s_nop 1\n"
"s_branch .Lbk_114\n"
".Lsl_117:\n"
"s_nop 7\n"
"s_mov_b32 s17, 0x100000\n"
".Ltp_119:\n"
"ds_read_b128 v[84:87], v11 offset:2496\n"
"ds_read_b32 v91, v12 offset:2496\n"
"s_sub_u32 s17, s17, 1\n"
"s_cbranch_scc1 .Lou_120\n"
"s_waitcnt lgkmcnt(0)\n"
"v_max3_u32 v88, v84, v85, v91\n"
"v_max3_u32 v88, v86, v87, v88\n"
"v_cmp_eq_u32 vcc, -1, v88\n"
"s_cbranch_vccnz .Ltp_119\n"
".Lou_120:\n"
"s_waitcnt lgkmcnt(0)\n"
"v_add_f32 v84, v84, v85\n"
"v_add_f32 v86, v86, v87\n"
"v_add_f32 v84, v84, v86\n"
"v_mul_f32 v89, 0x3acccccd, v91\n"
"v_fma_f32 v84, -v91, v89, v84\n"
"v_fmamk_f32 v84, v84, 0x3acccccd, v27\n"
"v_add_f32 v92, v92, v84\n"
"v_max_f32 v84, v84, v27\n"
"v_rsq_f32 v88, v84\n"
"s_nop 0\n"
"v_mul_f32 v89, -v89, v88\n"
"s_nop 1\n"
"s_branch .Lbk_118\n"
".Lsl_121:\n"
"s_nop 7\n"
"s_mov_b32 s17, 0x100000\n"
".Ltp_123:\n"
"ds_read_b128 v[84:87], v11 offset:2816\n"
"ds_read_b32 v91, v12 offset:2816\n"
"s_sub_u32 s17, s17, 1\n"
"s_cbranch_scc1 .Lou_124\n"
"s_waitcnt lgkmcnt(0)\n"
"v_max3_u32 v88, v84, v85, v91\n"
"v_max3_u32 v88, v86, v87, v88\n"
"v_cmp_eq_u32 vcc, -1, v88\n"
"s_cbranch_vccnz .Ltp_123\n"
".Lou_124:\n"
"s_waitcnt lgkmcnt(0)\n"
"v_add_f32 v84, v84, v85\n"
"v_add_f32 v86, v86, v87\n"
"v_add_f32 v84, v84, v86\n"
"v_mul_f32 v89, 0x3acccccd, v91\n"
"v_fma_f32 v84, -v91, v89, v84\n"
"v_fmamk_f32 v84, v84, 0x3acccccd, v27\n"
"v_add_f32 v92, v92, v84\n"
"v_max_f32 v84, v84, v27\n"
"v_rsq_f32 v88, v84\n"
"s_nop 0\n"
"v_mul_f32 v89, -v89, v88\n"
"s_nop 1\n"
"s_branch .Lbk_122\n"
".Lsl_125:\n"
"s_nop 7\n"
"s_mov_b32 s17, 0x100000\n"
".Ltp_127:\n"
"ds_read_b128 v[44:47], v11 offset:3136\n"
"ds_read_b128 v[48:51], v11 offset:3456\n"
"s_sub_u32 s17, s17, 1\n"
"s_cbranch_scc1 .Lou_128\n"
"s_waitcnt lgkmcnt(0)\n"
"v_max3_u32 v52, v44, v45, v51\n"
"v_max3_u32 v52, v46, v47, v52\n"
"v_max3_u32 v52, v48, v49, v52\n"
"v_max3_u32 v52, v50, v51, v52\n"
"v_cmp_eq_u32 vcc, -1, v52\n"
"s_cbranch_vccnz .Ltp_127\n"
".Lou_128:\n"
"s_waitcnt lgkmcnt(0)\n"
"v_add_f32 v44, v44, v45\n"
"v_add_f32 v46, v46, v47\n"
"v_add_f32 v48, v48, v49\n"
"v_add_f32 v50, v50, v51\n"
"v_add_f32 v48, v48, v50\n"
"v_add_f32 v44, v44, v46\n"
"v_mul_f32 v53, 0x3acccccd, v48\n"
"v_fma_f32 v44, -v48, v53, v44\n"
"v_fmamk_f32 v44, v44, 0x3acccccd, v27\n"
"v_add_f32 v92, v92, v44\n"
"v_max_f32 v44, v44, v27\n"
"v_rsq_f32 v52, v44\n"
"s_nop 0\n"
"v_mul_f32 v53, -v53, v52\n"
"s_nop 1\n"
"s_branch .Lbk_126\n"
".Lrd_129:\n"
"s_nop 7\n"
"s_cmp_lg_u32 s54, 0\n"
"s_cbranch_scc1 .Lgo_95\n"
"s_mov_b32 s54, 1\n"
"s_sub_u32 s79, s44, 0x5000\n"
"s_add_u32 s78, s29, 0xb48000\n"
"s_max_i32 s46, s79, s78\n"
"s_add_u32 s47, s46, 0x1000\n"
"buffer_load_dwordx4 v[28:31], v1, s[4:7], s46 offen offset:0 sc1\n"
"buffer_load_dwordx4 v[32:35], v1, s[4:7], s46 offen offset:1024 sc1\n"
"buffer_load_dwordx4 v[36:39], v1, s[4:7], s46 offen offset:2048 sc1\n"
"buffer_load_dwordx4 v[40:43], v1, s[4:7], s46 offen offset:3072 sc1\n"
"buffer_load_dwordx4 v[44:47], v1, s[4:7], s47 offen offset:0 sc1\n"
"s_mov_b32 s17, 32768\n"
".Ltp_130:\n"
"s_waitcnt vmcnt(0)\n"
"s_sub_u32 s17, s17, 1\n"
"s_cbranch_scc1 .Lou_131\n"
"v_max_u32 v48, v28, v29\n"
"v_max3_u32 v48, v30, v31, v48\n"
"v_max3_u32 v48, v32, v33, v48\n"
"v_max3_u32 v48, v34, v35, v48\n"
"v_max3_u32 v48, v36, v37, v48\n"
"v_max3_u32 v48, v38, v39, v48\n"
"v_max3_u32 v48, v40, v41, v48\n"
"v_max3_u32 v48, v42, v43, v48\n"
"v_max3_u32 v48, v44, v45, v48\n"
"v_max3_u32 v48, v46, v47, v48\n"
"v_cmp_eq_u32 vcc, -1, v48\n"
"s_cbranch_vccz .Lou_131\n"
"v_max_u32 v48, v30, v31\n"
"v_max3_u32 v48, v28, v29, v48\n"
"v_cmp_eq_u32 vcc, -1, v48\n"
"s_cbranch_vccz .Lsk_132\n"
"s_mov_b64 exec, vcc\n"
"buffer_load_dwordx4 v[28:31], v1, s[4:7], s46 offen offset:0 sc1\n"
"s_mov_b64 exec, -1\n"
".Lsk_132:\n"
"v_max_u32 v48, v34, v35\n"
"v_max3_u32 v48, v32, v33, v48\n"
"v_cmp_eq_u32 vcc, -1, v48\n"
"s_cbranch_vccz .Lsk_133\n"
"s_mov_b64 exec, vcc\n"
"buffer_load_dwordx4 v[32:35], v1, s[4:7], s46 offen offset:1024 sc1\n"
"s_mov_b64 exec, -1\n"
".Lsk_133:\n"
"v_max_u32 v48, v38, v39\n"
"v_max3_u32 v48, v36, v37, v48\n"
"v_cmp_eq_u32 vcc, -1, v48\n"
"s_cbranch_vccz .Lsk_134\n"
"s_mov_b64 exec, vcc\n"
"buffer_load_dwordx4 v[36:39], v1, s[4:7], s46 offen offset:2048 sc1\n"
"s_mov_b64 exec, -1\n"
".Lsk_134:\n"
"v_max_u32 v48, v42, v43\n"
"v_max3_u32 v48, v40, v41, v48\n"
"v_cmp_eq_u32 vcc, -1, v48\n"
"s_cbranch_vccz .Lsk_135\n"
"s_mov_b64 exec, vcc\n"
"buffer_load_dwordx4 v[40:43], v1, s[4:7], s46 offen offset:3072 sc1\n"
"s_mov_b64 exec, -1\n"
".Lsk_135:\n"
"v_max_u32 v48, v46, v47\n"
"v_max3_u32 v48, v44, v45, v48\n"
"v_cmp_eq_u32 vcc, -1, v48\n"
"s_cbranch_vccz .Lsk_136\n"
"s_mov_b64 exec, vcc\n"
"buffer_load_dwordx4 v[44:47], v1, s[4:7], s47 offen offset:0 sc1\n"
"s_mov_b64 exec, -1\n"
".Lsk_136:\n"
"s_branch .Ltp_130\n"
".Lou_131:\n"
"s_waitcnt vmcnt(0)\n"
"s_mov_b32 s46, s44\n"
"s_add_u32 s47, s46, 0x1000\n"
"buffer_load_dwordx4 v[28:31], v1, s[4:7], s46 offen offset:0 sc1\n"
"buffer_load_dwordx4 v[32:35], v1, s[4:7], s46 offen offset:1024 sc1\n"
"buffer_load_dwordx4 v[36:39], v1, s[4:7], s46 offen offset:2048 sc1\n"
"buffer_load_dwordx4 v[40:43], v1, s[4:7], s46 offen offset:3072 sc1\n"
"buffer_load_dwordx4 v[44:47], v1, s[4:7], s47 offen offset:0 sc1\n"
"s_mov_b32 s17, 32768\n"
".Ltp_137:\n"
"s_waitcnt vmcnt(0)\n"
"s_sub_u32 s17, s17, 1\n"
"s_cbranch_scc1 .Lou_138\n"
"v_max_u32 v48, v28, v29\n"
"v_max3_u32 v48, v30, v31, v48\n"
"v_max3_u32 v48, v32, v33, v48\n"
"v_max3_u32 v48, v34, v35, v48\n"
"v_max3_u32 v48, v36, v37, v48\n"
"v_max3_u32 v48, v38, v39, v48\n"
"v_max3_u32 v48, v40, v41, v48\n"
"v_max3_u32 v48, v42, v43, v48\n"
"v_max3_u32 v48, v44, v45, v48\n"
"v_max3_u32 v48, v46, v47, v48\n"
"v_cmp_eq_u32 vcc, -1, v48\n"
"s_cbranch_vccz .Lou_138\n"
"v_max_u32 v48, v30, v31\n"
"v_max3_u32 v48, v28, v29, v48\n"
"v_cmp_eq_u32 vcc, -1, v48\n"
"s_cbranch_vccz .Lsk_139\n"
"s_mov_b64 exec, vcc\n"
"buffer_load_dwordx4 v[28:31], v1, s[4:7], s46 offen offset:0 sc1\n"
"s_mov_b64 exec, -1\n"
".Lsk_139:\n"
"v_max_u32 v48, v34, v35\n"
"v_max3_u32 v48, v32, v33, v48\n"
"v_cmp_eq_u32 vcc, -1, v48\n"
"s_cbranch_vccz .Lsk_140\n"
"s_mov_b64 exec, vcc\n"
"buffer_load_dwordx4 v[32:35], v1, s[4:7], s46 offen offset:1024 sc1\n"
"s_mov_b64 exec, -1\n"
".Lsk_140:\n"
"v_max_u32 v48, v38, v39\n"
"v_max3_u32 v48, v36, v37, v48\n"
"v_cmp_eq_u32 vcc, -1, v48\n"
"s_cbranch_vccz .Lsk_141\n"
"s_mov_b64 exec, vcc\n"
"buffer_load_dwordx4 v[36:39], v1, s[4:7], s46 offen offset:2048 sc1\n"
"s_mov_b64 exec, -1\n"
".Lsk_141:\n"
"v_max_u32 v48, v42, v43\n"
"v_max3_u32 v48, v40, v41, v48\n"
"v_cmp_eq_u32 vcc, -1, v48\n"
"s_cbranch_vccz .Lsk_142\n"
"s_mov_b64 exec, vcc\n"
"buffer_load_dwordx4 v[40:43], v1, s[4:7], s46 offen offset:3072 sc1\n"
"s_mov_b64 exec, -1\n"
".Lsk_142:\n"
"v_max_u32 v48, v46, v47\n"
"v_max3_u32 v48, v44, v45, v48\n"
"v_cmp_eq_u32 vcc, -1, v48\n"
"s_cbranch_vccz .Lsk_143\n"
"s_mov_b64 exec, vcc\n"
"buffer_load_dwordx4 v[44:47], v1, s[4:7], s47 offen offset:0 sc1\n"
"s_mov_b64 exec, -1\n"
".Lsk_143:\n"
"s_branch .Ltp_137\n"
".Lou_138:\n"
"s_waitcnt vmcnt(0)\n"
"s_add_u32 s46, s44, 0x5000\n"
"s_add_u32 s47, s46, 0x1000\n"
"buffer_load_dwordx4 v[28:31], v1, s[4:7], s46 offen offset:0 sc1\n"
"buffer_load_dwordx4 v[32:35], v1, s[4:7], s46 offen offset:1024 sc1\n"
"buffer_load_dwordx4 v[36:39], v1, s[4:7], s46 offen offset:2048 sc1\n"
"buffer_load_dwordx4 v[40:43], v1, s[4:7], s46 offen offset:3072 sc1\n"
"buffer_load_dwordx4 v[44:47], v1, s[4:7], s47 offen offset:0 sc1\n"
"s_mov_b32 s17, 32768\n"
".Ltp_144:\n"
"s_waitcnt vmcnt(0)\n"
"s_sub_u32 s17, s17, 1\n"
"s_cbranch_scc1 .Lou_145\n"
"v_max_u32 v48, v28, v29\n"
"v_max3_u32 v48, v30, v31, v48\n"
"v_max3_u32 v48, v32, v33, v48\n"
"v_max3_u32 v48, v34, v35, v48\n"
"v_max3_u32 v48, v36, v37, v48\n"
"v_max3_u32 v48, v38, v39, v48\n"
"v_max3_u32 v48, v40, v41, v48\n"
"v_max3_u32 v48, v42, v43, v48\n"
"v_max3_u32 v48, v44, v45, v48\n"
"v_max3_u32 v48, v46, v47, v48\n"
"v_cmp_eq_u32 vcc, -1, v48\n"
"s_cbranch_vccz .Lou_145\n"
"v_max_u32 v48, v30, v31\n"
"v_max3_u32 v48, v28, v29, v48\n"
"v_cmp_eq_u32 vcc, -1, v48\n"
"s_cbranch_vccz .Lsk_146\n"
"s_mov_b64 exec, vcc\n"
"buffer_load_dwordx4 v[28:31], v1, s[4:7], s46 offen offset:0 sc1\n"
"s_mov_b64 exec, -1\n"
".Lsk_146:\n"
"v_max_u32 v48, v34, v35\n"
"v_max3_u32 v48, v32, v33, v48\n"
"v_cmp_eq_u32 vcc, -1, v48\n"
"s_cbranch_vccz .Lsk_147\n"
"s_mov_b64 exec, vcc\n"
"buffer_load_dwordx4 v[32:35], v1, s[4:7], s46 offen offset:1024 sc1\n"
"s_mov_b64 exec, -1\n"
".Lsk_147:\n"
"v_max_u32 v48, v38, v39\n"
"v_max3_u32 v48, v36, v37, v48\n"
"v_cmp_eq_u32 vcc, -1, v48\n"
"s_cbranch_vccz .Lsk_148\n"
"s_mov_b64 exec, vcc\n"
"buffer_load_dwordx4 v[36:39], v1, s[4:7], s46 offen offset:2048 sc1\n"
"s_mov_b64 exec, -1\n"
".Lsk_148:\n"
"v_max_u32 v48, v42, v43\n"
"v_max3_u32 v48, v40, v41, v48\n"
"v_cmp_eq_u32 vcc, -1, v48\n"
"s_cbranch_vccz .Lsk_149\n"
"s_mov_b64 exec, vcc\n"
"buffer_load_dwordx4 v[40:43], v1, s[4:7], s46 offen offset:3072 sc1\n"
"s_mov_b64 exec, -1\n"
".Lsk_149:\n"
"v_max_u32 v48, v46, v47\n"
"v_max3_u32 v48, v44, v45, v48\n"
"v_cmp_eq_u32 vcc, -1, v48\n"
"s_cbranch_vccz .Lsk_150\n"
"s_mov_b64 exec, vcc\n"
"buffer_load_dwordx4 v[44:47], v1, s[4:7], s47 offen offset:0 sc1\n"
"s_mov_b64 exec, -1\n"
".Lsk_150:\n"
"s_branch .Ltp_144\n"
".Lou_145:\n"
"s_waitcnt vmcnt(0)\n"
"s_add_u32 s46, s44, 0xa000\n"
"s_add_u32 s47, s46, 0x1000\n"
"buffer_load_dwordx4 v[28:31], v1, s[4:7], s46 offen offset:0 sc1\n"
"buffer_load_dwordx4 v[32:35], v1, s[4:7], s46 offen offset:1024 sc1\n"
"buffer_load_dwordx4 v[36:39], v1, s[4:7], s46 offen offset:2048 sc1\n"
"buffer_load_dwordx4 v[40:43], v1, s[4:7], s46 offen offset:3072 sc1\n"
"buffer_load_dwordx4 v[44:47], v1, s[4:7], s47 offen offset:0 sc1\n"
"s_mov_b32 s17, 32768\n"
".Ltp_151:\n"
"s_waitcnt vmcnt(0)\n"
"s_sub_u32 s17, s17, 1\n"
"s_cbranch_scc1 .Lou_152\n"
"v_max_u32 v48, v28, v29\n"
"v_max3_u32 v48, v30, v31, v48\n"
"v_max3_u32 v48, v32, v33, v48\n"
"v_max3_u32 v48, v34, v35, v48\n"
"v_max3_u32 v48, v36, v37, v48\n"
"v_max3_u32 v48, v38, v39, v48\n"
"v_max3_u32 v48, v40, v41, v48\n"
"v_max3_u32 v48, v42, v43, v48\n"
"v_max3_u32 v48, v44, v45, v48\n"
"v_max3_u32 v48, v46, v47, v48\n"
"v_cmp_eq_u32 vcc, -1, v48\n"
"s_cbranch_vccz .Lou_152\n"
"v_max_u32 v48, v30, v31\n"
"v_max3_u32 v48, v28, v29, v48\n"
"v_cmp_eq_u32 vcc, -1, v48\n"
"s_cbranch_vccz .Lsk_153\n"
"s_mov_b64 exec, vcc\n"
"buffer_load_dwordx4 v[28:31], v1, s[4:7], s46 offen offset:0 sc1\n"
"s_mov_b64 exec, -1\n"
".Lsk_153:\n"
"v_max_u32 v48, v34, v35\n"
"v_max3_u32 v48, v32, v33, v48\n"
"v_cmp_eq_u32 vcc, -1, v48\n"
"s_cbranch_vccz .Lsk_154\n"
"s_mov_b64 exec, vcc\n"
"buffer_load_dwordx4 v[32:35], v1, s[4:7], s46 offen offset:1024 sc1\n"
"s_mov_b64 exec, -1\n"
".Lsk_154:\n"
"v_max_u32 v48, v38, v39\n"
"v_max3_u32 v48, v36, v37, v48\n"
"v_cmp_eq_u32 vcc, -1, v48\n"
"s_cbranch_vccz .Lsk_155\n"
"s_mov_b64 exec, vcc\n"
"buffer_load_dwordx4 v[36:39], v1, s[4:7], s46 offen offset:2048 sc1\n"
"s_mov_b64 exec, -1\n"
".Lsk_155:\n"
"v_max_u32 v48, v42, v43\n"
"v_max3_u32 v48, v40, v41, v48\n"
"v_cmp_eq_u32 vcc, -1, v48\n"
"s_cbranch_vccz .Lsk_156\n"
"s_mov_b64 exec, vcc\n"
"buffer_load_dwordx4 v[40:43], v1, s[4:7], s46 offen offset:3072 sc1\n"
"s_mov_b64 exec, -1\n"
".Lsk_156:\n"
"v_max_u32 v48, v46, v47\n"
"v_max3_u32 v48, v44, v45, v48\n"
"v_cmp_eq_u32 vcc, -1, v48\n"
"s_cbranch_vccz .Lsk_157\n"
"s_mov_b64 exec, vcc\n"
"buffer_load_dwordx4 v[44:47], v1, s[4:7], s47 offen offset:0 sc1\n"
"s_mov_b64 exec, -1\n"
".Lsk_157:\n"
"s_branch .Ltp_151\n"
".Lou_152:\n"
"s_waitcnt vmcnt(0)\n"
"s_add_u32 s46, s44, 0xf000\n"
"s_add_u32 s47, s46, 0x1000\n"
"buffer_load_dwordx4 v[28:31], v1, s[4:7], s46 offen offset:0 sc1\n"
"buffer_load_dwordx4 v[32:35], v1, s[4:7], s46 offen offset:1024 sc1\n"
"buffer_load_dwordx4 v[36:39], v1, s[4:7], s46 offen offset:2048 sc1\n"
"buffer_load_dwordx4 v[40:43], v1, s[4:7], s46 offen offset:3072 sc1\n"
"buffer_load_dwordx4 v[44:47], v1, s[4:7], s47 offen offset:0 sc1\n"
"s_mov_b32 s17, 32768\n"
".Ltp_158:\n"
"s_waitcnt vmcnt(0)\n"
"s_sub_u32 s17, s17, 1\n"
"s_cbranch_scc1 .Lou_159\n"
"v_max_u32 v48, v28, v29\n"
"v_max3_u32 v48, v30, v31, v48\n"
"v_max3_u32 v48, v32, v33, v48\n"
"v_max3_u32 v48, v34, v35, v48\n"
"v_max3_u32 v48, v36, v37, v48\n"
"v_max3_u32 v48, v38, v39, v48\n"
"v_max3_u32 v48, v40, v41, v48\n"
"v_max3_u32 v48, v42, v43, v48\n"
"v_max3_u32 v48, v44, v45, v48\n"
"v_max3_u32 v48, v46, v47, v48\n"
"v_cmp_eq_u32 vcc, -1, v48\n"
"s_cbranch_vccz .Lou_159\n"
"v_max_u32 v48, v30, v31\n"
"v_max3_u32 v48, v28, v29, v48\n"
"v_cmp_eq_u32 vcc, -1, v48\n"
"s_cbranch_vccz .Lsk_160\n"
"s_mov_b64 exec, vcc\n"
"buffer_load_dwordx4 v[28:31], v1, s[4:7], s46 offen offset:0 sc1\n"
"s_mov_b64 exec, -1\n"
".Lsk_160:\n"
"v_max_u32 v48, v34, v35\n"
"v_max3_u32 v48, v32, v33, v48\n"
"v_cmp_eq_u32 vcc, -1, v48\n"
"s_cbranch_vccz .Lsk_161\n"
"s_mov_b64 exec, vcc\n"
"buffer_load_dwordx4 v[32:35], v1, s[4:7], s46 offen offset:1024 sc1\n"
"s_mov_b64 exec, -1\n"
".Lsk_161:\n"
"v_max_u32 v48, v38, v39\n"
"v_max3_u32 v48, v36, v37, v48\n"
"v_cmp_eq_u32 vcc, -1, v48\n"
"s_cbranch_vccz .Lsk_162\n"
"s_mov_b64 exec, vcc\n"
"buffer_load_dwordx4 v[36:39], v1, s[4:7], s46 offen offset:2048 sc1\n"
"s_mov_b64 exec, -1\n"
".Lsk_162:\n"
"v_max_u32 v48, v42, v43\n"
"v_max3_u32 v48, v40, v41, v48\n"
"v_cmp_eq_u32 vcc, -1, v48\n"
"s_cbranch_vccz .Lsk_163\n"
"s_mov_b64 exec, vcc\n"
"buffer_load_dwordx4 v[40:43], v1, s[4:7], s46 offen offset:3072 sc1\n"
"s_mov_b64 exec, -1\n"
".Lsk_163:\n"
"v_max_u32 v48, v46, v47\n"
"v_max3_u32 v48, v44, v45, v48\n"
"v_cmp_eq_u32 vcc, -1, v48\n"
"s_cbranch_vccz .Lsk_164\n"
"s_mov_b64 exec, vcc\n"
"buffer_load_dwordx4 v[44:47], v1, s[4:7], s47 offen offset:0 sc1\n"
"s_mov_b64 exec, -1\n"
".Lsk_164:\n"
"s_branch .Ltp_158\n"
".Lou_159:\n"
"s_waitcnt vmcnt(0)\n"
"s_mov_b32 s17, 0x100000\n"
".Ltp_165:\n"
"ds_read_b32 v90, v14 offset:272\n"
"s_sub_u32 s17, s17, 1\n"
"s_cbranch_scc1 .Lou_166\n"
"s_waitcnt lgkmcnt(0)\n"
"v_cmp_eq_u32 vcc, 0, v90\n"
"s_cbranch_vccnz .Ltp_165\n"
".Lou_166:\n"
"s_waitcnt lgkmcnt(0)\n"
"ds_write_b32 v13, v13 offset:288\n"
"s_mov_b32 s17, 0x100000\n"
".Ltp_167:\n"
"ds_read_b32 v90, v14 offset:288\n"
"s_sub_u32 s17, s17, 1\n"
"s_cbranch_scc1 .Lou_168\n"
"s_waitcnt lgkmcnt(0)\n"
"v_cmp_eq_u32 vcc, 0, v90\n"
"s_cbranch_vccnz .Ltp_167\n"
".Lou_168:\n"
"s_waitcnt lgkmcnt(0)\n"
"v_mov_b32 v90, 0\n"
"ds_write_b32 v13, v90 offset:240\n"
"ds_write_b32 v13, v90 offset:256\n"
"v_mov_b32 v90, -1\n"
"ds_write_b32 v9, v90 offset:1856\n"
"ds_write_b32 v9, v90 offset:2176\n"
"ds_write_b32 v9, v90 offset:2496\n"
"ds_write_b32 v9, v90 offset:2816\n"
"ds_write_b32 v9, v90 offset:3136\n"
"ds_write_b32 v10, v90 offset:3456\n"
"ds_write_b32 v13, v13 offset:304\n"
"s_mov_b32 s17, 0x100000\n"
".Ltp_169:\n"
"ds_read_b32 v90, v14 offset:304\n"
"s_sub_u32 s17, s17, 1\n"
"s_cbranch_scc1 .Lou_170\n"
"s_waitcnt lgkmcnt(0)\n"
"v_cmp_eq_u32 vcc, 0, v90\n"
"s_cbranch_vccnz .Ltp_169\n"
".Lou_170:\n"
"s_waitcnt lgkmcnt(0)\n"
"s_branch .Lag_94\n"
".Lsl_171:\n"
"s_nop 7\n"
"s_mov_b32 s17, 0x100000\n"
".Ltp_173:\n"
"ds_read_b32 v90, v14 offset:336\n"
"s_sub_u32 s17, s17, 1\n"
"s_cbranch_scc1 .Lou_174\n"
"s_waitcnt lgkmcnt(0)\n"
"v_cmp_eq_u32 vcc, 0, v90\n"
"s_cbranch_vccnz .Ltp_173\n"
".Lou_174:\n"
"s_waitcnt lgkmcnt(0)\n"
"s_branch .Lbk_172\n"
".Lfx_179:\n"
"s_nop 7\n"
"s_add_u32 m0, s29, 0xa000\n"
"s_add_u32 s31, s44, 0x140000\n"
"buffer_load_dwordx4 v1, s[4:7], s31 offen offset:0 sc1 lds\n"
"buffer_load_dwordx4 v1, s[4:7], s31 offen offset:1024 sc1 lds\n"
"buffer_load_dwordx4 v1, s[4:7], s31 offen offset:2048 sc1 lds\n"
"buffer_load_dwordx4 v1, s[4:7], s31 offen offset:3072 sc1 lds\n"
"s_add_u32 m0, m0, 1024\n"
"s_add_u32 s31, s31, 1024\n"
"buffer_load_dwordx4 v1, s[4:7], s31 offen offset:3072 sc1 lds\n"
"s_add_u32 m0, s29, 0xf000\n"
"s_add_u32 s31, s44, 0x145000\n"
"buffer_load_dwordx4 v1, s[4:7], s31 offen offset:0 sc1 lds\n"
"buffer_load_dwordx4 v1, s[4:7], s31 offen offset:1024 sc1 lds\n"
"buffer_load_dwordx4 v1, s[4:7], s31 offen offset:2048 sc1 lds\n"
"buffer_load_dwordx4 v1, s[4:7], s31 offen offset:3072 sc1 lds\n"
"s_add_u32 m0, m0, 1024\n"
"s_add_u32 s31, s31, 1024\n"
"buffer_load_dwordx4 v1, s[4:7], s31 offen offset:3072 sc1 lds\n"
"s_waitcnt vmcnt(0)\n"
"ds_write_b32 v13, v13 offset:368\n"
"s_mov_b32 s17, 0x100000\n"
".Ltp_181:\n"
"ds_read_b32 v90, v14 offset:368\n"
"s_sub_u32 s17, s17, 1\n"
"s_cbranch_scc1 .Lou_182\n"
"s_waitcnt lgkmcnt(0)\n"
"v_cmp_eq_u32 vcc, 0, v90\n"
"s_cbranch_vccnz .Ltp_181\n"
".Lou_182:\n"
"s_waitcnt lgkmcnt(0)\n"
"s_branch .Lbk_180\n"
".Lsl_185:\n"
"s_nop 7\n"
"s_mov_b32 s17, 0x100000\n"
".Ltp_187:\n"
"ds_read_b32 v90, v14 offset:272\n"
"s_sub_u32 s17, s17, 1\n"
"s_cbranch_scc1 .Lou_188\n"
"s_waitcnt lgkmcnt(0)\n"
"v_cmp_eq_u32 vcc, 0, v90\n"
"s_cbranch_vccnz .Ltp_187\n"
".Lou_188:\n"
"s_waitcnt lgkmcnt(0)\n"
"s_branch .Lbk_186\n"
".Lsl_189:\n"
"s_nop 7\n"
"s_mov_b32 s17, 0x100000\n"
".Ltp_191:\n"
"ds_read_b32 v90, v14 offset:400\n"
"s_sub_u32 s17, s17, 1\n"
"s_cbranch_scc1 .Lou_192\n"
"s_waitcnt lgkmcnt(0)\n"
"v_cmp_eq_u32 vcc, 0, v90\n"
"s_cbranch_vccnz .Ltp_191\n"
".Lou_192:\n"
"s_waitcnt lgkmcnt(0)\n"
"s_branch .Lbk_190\n"
".Lsl_193:\n"
"s_nop 7\n"
"s_mov_b32 s17, 0x100000\n"
".Ltp_195:\n"
"ds_read_b128 v[84:87], v11 offset:3712\n"
"ds_read_b32 v91, v12 offset:3712\n"
"s_sub_u32 s17, s17, 1\n"
"s_cbranch_scc1 .Lou_196\n"
"s_waitcnt lgkmcnt(0)\n"
"v_max3_u32 v88, v84, v85, v91\n"
"v_max3_u32 v88, v86, v87, v88\n"
"v_cmp_eq_u32 vcc, -1, v88\n"
"s_cbranch_vccnz .Ltp_195\n"
".Lou_196:\n"
"s_waitcnt lgkmcnt(0)\n"
"v_add_f32 v84, v84, v85\n"
"v_add_f32 v86, v86, v87\n"
"v_add_f32 v84, v84, v86\n"
"v_mul_f32 v89, 0x3acccccd, v91\n"
"v_fma_f32 v84, -v91, v89, v84\n"
"v_fmamk_f32 v84, v84, 0x3acccccd, v27\n"
"v_add_f32 v92, v92, v84\n"
"v_max_f32 v84, v84, v27\n"
"v_rsq_f32 v88, v84\n"
"s_nop 0\n"
"v_mul_f32 v89, -v89, v88\n"
"s_nop 1\n"
"s_branch .Lbk_194\n"
".Lsl_197:\n"
"s_nop 7\n"
"s_mov_b32 s17, 0x100000\n"
".Ltp_199:\n"
"ds_read_b128 v[84:87], v11 offset:4032\n"
"ds_read_b32 v91, v12 offset:4032\n"
"s_sub_u32 s17, s17, 1\n"
"s_cbranch_scc1 .Lou_200\n"
"s_waitcnt lgkmcnt(0)\n"
"v_max3_u32 v88, v84, v85, v91\n"
"v_max3_u32 v88, v86, v87, v88\n"
"v_cmp_eq_u32 vcc, -1, v88\n"
"s_cbranch_vccnz .Ltp_199\n"
".Lou_200:\n"
"s_waitcnt lgkmcnt(0)\n"
"v_add_f32 v84, v84, v85\n"
"v_add_f32 v86, v86, v87\n"
"v_add_f32 v84, v84, v86\n"
"v_mul_f32 v89, 0x3acccccd, v91\n"
"v_fma_f32 v84, -v91, v89, v84\n"
"v_fmamk_f32 v84, v84, 0x3acccccd, v27\n"
"v_add_f32 v92, v92, v84\n"
"v_max_f32 v84, v84, v27\n"
"v_rsq_f32 v88, v84\n"
"s_nop 0\n"
"v_mul_f32 v89, -v89, v88\n"
"s_nop 1\n"
"s_branch .Lbk_198\n"
".Lsl_201:\n"
"s_nop 7\n"
"s_mov_b32 s17, 0x100000\n"
".Ltp_203:\n"
"ds_read_b128 v[84:87], v11 offset:4352\n"
"ds_read_b32 v91, v12 offset:4352\n"
"s_sub_u32 s17, s17, 1\n"
"s_cbranch_scc1 .Lou_204\n"
"s_waitcnt lgkmcnt(0)\n"
"v_max3_u32 v88, v84, v85, v91\n"
"v_max3_u32 v88, v86, v87, v88\n"
"v_cmp_eq_u32 vcc, -1, v88\n"
"s_cbranch_vccnz .Ltp_203\n"
".Lou_204:\n"
"s_waitcnt lgkmcnt(0)\n"
"v_add_f32 v84, v84, v85\n"
"v_add_f32 v86, v86, v87\n"
"v_add_f32 v84, v84, v86\n"
"v_mul_f32 v89, 0x3acccccd, v91\n"
"v_fma_f32 v84, -v91, v89, v84\n"
"v_fmamk_f32 v84, v84, 0x3acccccd, v27\n"
"v_add_f32 v92, v92, v84\n"
"v_max_f32 v84, v84, v27\n"
"v_rsq_f32 v88, v84\n"
"s_nop 0\n"
"v_mul_f32 v89, -v89, v88\n"
"s_nop 1\n"
"s_branch .Lbk_202\n"
".Lsl_205:\n"
"s_nop 7\n"
"s_mov_b32 s17, 0x100000\n"
".Ltp_207:\n"
"ds_read_b128 v[84:87], v11 offset:4672\n"
"ds_read_b32 v91, v12 offset:4672\n"
"s_sub_u32 s17, s17, 1\n"
"s_cbranch_scc1 .Lou_208\n"
"s_waitcnt lgkmcnt(0)\n"
"v_max3_u32 v88, v84, v85, v91\n"
"v_max3_u32 v88, v86, v87, v88\n"
"v_cmp_eq_u32 vcc, -1, v88\n"
"s_cbranch_vccnz .Ltp_207\n"
".Lou_208:\n"
"s_waitcnt lgkmcnt(0)\n"
"v_add_f32 v84, v84, v85\n"
"v_add_f32 v86, v86, v87\n"
"v_add_f32 v84, v84, v86\n"
"v_mul_f32 v89, 0x3acccccd, v91\n"
"v_fma_f32 v84, -v91, v89, v84\n"
"v_fmamk_f32 v84, v84, 0x3acccccd, v27\n"
"v_add_f32 v92, v92, v84\n"
"v_max_f32 v84, v84, v27\n"
"v_rsq_f32 v88, v84\n"
"s_nop 0\n"
"v_mul_f32 v89, -v89, v88\n"
"s_nop 1\n"
"s_branch .Lbk_206\n"
".Lsl_209:\n"
"s_nop 7\n"
"s_mov_b32 s17, 0x100000\n"
".Ltp_211:\n"
"ds_read_b128 v[44:47], v11 offset:4992\n"
"ds_read_b128 v[48:51], v11 offset:5312\n"
"s_sub_u32 s17, s17, 1\n"
"s_cbranch_scc1 .Lou_212\n"
"s_waitcnt lgkmcnt(0)\n"
"v_max3_u32 v52, v44, v45, v51\n"
"v_max3_u32 v52, v46, v47, v52\n"
"v_max3_u32 v52, v48, v49, v52\n"
"v_max3_u32 v52, v50, v51, v52\n"
"v_cmp_eq_u32 vcc, -1, v52\n"
"s_cbranch_vccnz .Ltp_211\n"
".Lou_212:\n"
"s_waitcnt lgkmcnt(0)\n"
"v_add_f32 v44, v44, v45\n"
"v_add_f32 v46, v46, v47\n"
"v_add_f32 v48, v48, v49\n"
"v_add_f32 v50, v50, v51\n"
"v_add_f32 v48, v48, v50\n"
"v_add_f32 v44, v44, v46\n"
"v_mul_f32 v53, 0x3acccccd, v48\n"
"v_fma_f32 v44, -v48, v53, v44\n"
"v_fmamk_f32 v44, v44, 0x3acccccd, v27\n"
"v_add_f32 v92, v92, v44\n"
"v_max_f32 v44, v44, v27\n"
"v_rsq_f32 v52, v44\n"
"s_nop 0\n"
"v_mul_f32 v53, -v53, v52\n"
"s_nop 1\n"
"s_branch .Lbk_210\n"
".Lrd_213:\n"
"s_nop 7\n"
"s_cmp_lg_u32 s54, 0\n"
"s_cbranch_scc1 .Lgo_178\n"
"s_mov_b32 s54, 1\n"
"s_add_u32 s46, s44, 0x13b000\n"
"s_add_u32 s47, s46, 0x1000\n"
"buffer_load_dwordx4 v[28:31], v1, s[4:7], s46 offen offset:0 sc1\n"
"buffer_load_dwordx4 v[32:35], v1, s[4:7], s46 offen offset:1024 sc1\n"
"buffer_load_dwordx4 v[36:39], v1, s[4:7], s46 offen offset:2048 sc1\n"
"buffer_load_dwordx4 v[40:43], v1, s[4:7], s46 offen offset:3072 sc1\n"
"buffer_load_dwordx4 v[44:47], v1, s[4:7], s47 offen offset:0 sc1\n"
"s_mov_b32 s17, 32768\n"
".Ltp_214:\n"
"s_waitcnt vmcnt(0)\n"
"s_sub_u32 s17, s17, 1\n"
"s_cbranch_scc1 .Lou_215\n"
"v_max_u32 v48, v28, v29\n"
"v_max3_u32 v48, v30, v31, v48\n"
"v_max3_u32 v48, v32, v33, v48\n"
"v_max3_u32 v48, v34, v35, v48\n"
"v_max3_u32 v48, v36, v37, v48\n"
"v_max3_u32 v48, v38, v39, v48\n"
"v_max3_u32 v48, v40, v41, v48\n"
"v_max3_u32 v48, v42, v43, v48\n"
"v_max3_u32 v48, v44, v45, v48\n"
"v_max3_u32 v48, v46, v47, v48\n"
"v_cmp_eq_u32 vcc, -1, v48\n"
"s_cbranch_vccz .Lou_215\n"
"v_max_u32 v48, v30, v31\n"
"v_max3_u32 v48, v28, v29, v48\n"
"v_cmp_eq_u32 vcc, -1, v48\n"
"s_cbranch_vccz .Lsk_216\n"
"s_mov_b64 exec, vcc\n"
"buffer_load_dwordx4 v[28:31], v1, s[4:7], s46 offen offset:0 sc1\n"
"s_mov_b64 exec, -1\n"
".Lsk_216:\n"
"v_max_u32 v48, v34, v35\n"
"v_max3_u32 v48, v32, v33, v48\n"
"v_cmp_eq_u32 vcc, -1, v48\n"
"s_cbranch_vccz .Lsk_217\n"
"s_mov_b64 exec, vcc\n"
"buffer_load_dwordx4 v[32:35], v1, s[4:7], s46 offen offset:1024 sc1\n"
"s_mov_b64 exec, -1\n"
".Lsk_217:\n"
"v_max_u32 v48, v38, v39\n"
"v_max3_u32 v48, v36, v37, v48\n"
"v_cmp_eq_u32 vcc, -1, v48\n"
"s_cbranch_vccz .Lsk_218\n"
"s_mov_b64 exec, vcc\n"
"buffer_load_dwordx4 v[36:39], v1, s[4:7], s46 offen offset:2048 sc1\n"
"s_mov_b64 exec, -1\n"
".Lsk_218:\n"
"v_max_u32 v48, v42, v43\n"
"v_max3_u32 v48, v40, v41, v48\n"
"v_cmp_eq_u32 vcc, -1, v48\n"
"s_cbranch_vccz .Lsk_219\n"
"s_mov_b64 exec, vcc\n"
"buffer_load_dwordx4 v[40:43], v1, s[4:7], s46 offen offset:3072 sc1\n"
"s_mov_b64 exec, -1\n"
".Lsk_219:\n"
"v_max_u32 v48, v46, v47\n"
"v_max3_u32 v48, v44, v45, v48\n"
"v_cmp_eq_u32 vcc, -1, v48\n"
"s_cbranch_vccz .Lsk_220\n"
"s_mov_b64 exec, vcc\n"
"buffer_load_dwordx4 v[44:47], v1, s[4:7], s47 offen offset:0 sc1\n"
"s_mov_b64 exec, -1\n"
".Lsk_220:\n"
"s_branch .Ltp_214\n"
".Lou_215:\n"
"s_waitcnt vmcnt(0)\n"
"s_add_u32 s46, s44, 0x140000\n"
"s_add_u32 s47, s46, 0x1000\n"
"buffer_load_dwordx4 v[28:31], v1, s[4:7], s46 offen offset:0 sc1\n"
"buffer_load_dwordx4 v[32:35], v1, s[4:7], s46 offen offset:1024 sc1\n"
"buffer_load_dwordx4 v[36:39], v1, s[4:7], s46 offen offset:2048 sc1\n"
"buffer_load_dwordx4 v[40:43], v1, s[4:7], s46 offen offset:3072 sc1\n"
"buffer_load_dwordx4 v[44:47], v1, s[4:7], s47 offen offset:0 sc1\n"
"s_mov_b32 s17, 32768\n"
".Ltp_221:\n"
"s_waitcnt vmcnt(0)\n"
"s_sub_u32 s17, s17, 1\n"
"s_cbranch_scc1 .Lou_222\n"
"v_max_u32 v48, v28, v29\n"
"v_max3_u32 v48, v30, v31, v48\n"
"v_max3_u32 v48, v32, v33, v48\n"
"v_max3_u32 v48, v34, v35, v48\n"
"v_max3_u32 v48, v36, v37, v48\n"
"v_max3_u32 v48, v38, v39, v48\n"
"v_max3_u32 v48, v40, v41, v48\n"
"v_max3_u32 v48, v42, v43, v48\n"
"v_max3_u32 v48, v44, v45, v48\n"
"v_max3_u32 v48, v46, v47, v48\n"
"v_cmp_eq_u32 vcc, -1, v48\n"
"s_cbranch_vccz .Lou_222\n"
"v_max_u32 v48, v30, v31\n"
"v_max3_u32 v48, v28, v29, v48\n"
"v_cmp_eq_u32 vcc, -1, v48\n"
"s_cbranch_vccz .Lsk_223\n"
"s_mov_b64 exec, vcc\n"
"buffer_load_dwordx4 v[28:31], v1, s[4:7], s46 offen offset:0 sc1\n"
"s_mov_b64 exec, -1\n"
".Lsk_223:\n"
"v_max_u32 v48, v34, v35\n"
"v_max3_u32 v48, v32, v33, v48\n"
"v_cmp_eq_u32 vcc, -1, v48\n"
"s_cbranch_vccz .Lsk_224\n"
"s_mov_b64 exec, vcc\n"
"buffer_load_dwordx4 v[32:35], v1, s[4:7], s46 offen offset:1024 sc1\n"
"s_mov_b64 exec, -1\n"
".Lsk_224:\n"
"v_max_u32 v48, v38, v39\n"
"v_max3_u32 v48, v36, v37, v48\n"
"v_cmp_eq_u32 vcc, -1, v48\n"
"s_cbranch_vccz .Lsk_225\n"
"s_mov_b64 exec, vcc\n"
"buffer_load_dwordx4 v[36:39], v1, s[4:7], s46 offen offset:2048 sc1\n"
"s_mov_b64 exec, -1\n"
".Lsk_225:\n"
"v_max_u32 v48, v42, v43\n"
"v_max3_u32 v48, v40, v41, v48\n"
"v_cmp_eq_u32 vcc, -1, v48\n"
"s_cbranch_vccz .Lsk_226\n"
"s_mov_b64 exec, vcc\n"
"buffer_load_dwordx4 v[40:43], v1, s[4:7], s46 offen offset:3072 sc1\n"
"s_mov_b64 exec, -1\n"
".Lsk_226:\n"
"v_max_u32 v48, v46, v47\n"
"v_max3_u32 v48, v44, v45, v48\n"
"v_cmp_eq_u32 vcc, -1, v48\n"
"s_cbranch_vccz .Lsk_227\n"
"s_mov_b64 exec, vcc\n"
"buffer_load_dwordx4 v[44:47], v1, s[4:7], s47 offen offset:0 sc1\n"
"s_mov_b64 exec, -1\n"
".Lsk_227:\n"
"s_branch .Ltp_221\n"
".Lou_222:\n"
"s_waitcnt vmcnt(0)\n"
"s_add_u32 s46, s44, 0x145000\n"
"s_add_u32 s47, s46, 0x1000\n"
"buffer_load_dwordx4 v[28:31], v1, s[4:7], s46 offen offset:0 sc1\n"
"buffer_load_dwordx4 v[32:35], v1, s[4:7], s46 offen offset:1024 sc1\n"
"buffer_load_dwordx4 v[36:39], v1, s[4:7], s46 offen offset:2048 sc1\n"
"buffer_load_dwordx4 v[40:43], v1, s[4:7], s46 offen offset:3072 sc1\n"
"buffer_load_dwordx4 v[44:47], v1, s[4:7], s47 offen offset:0 sc1\n"
"s_mov_b32 s17, 32768\n"
".Ltp_228:\n"
"s_waitcnt vmcnt(0)\n"
"s_sub_u32 s17, s17, 1\n"
"s_cbranch_scc1 .Lou_229\n"
"v_max_u32 v48, v28, v29\n"
"v_max3_u32 v48, v30, v31, v48\n"
"v_max3_u32 v48, v32, v33, v48\n"
"v_max3_u32 v48, v34, v35, v48\n"
"v_max3_u32 v48, v36, v37, v48\n"
"v_max3_u32 v48, v38, v39, v48\n"
"v_max3_u32 v48, v40, v41, v48\n"
"v_max3_u32 v48, v42, v43, v48\n"
"v_max3_u32 v48, v44, v45, v48\n"
"v_max3_u32 v48, v46, v47, v48\n"
"v_cmp_eq_u32 vcc, -1, v48\n"
"s_cbranch_vccz .Lou_229\n"
"v_max_u32 v48, v30, v31\n"
"v_max3_u32 v48, v28, v29, v48\n"
"v_cmp_eq_u32 vcc, -1, v48\n"
"s_cbranch_vccz .Lsk_230\n"
"s_mov_b64 exec, vcc\n"
"buffer_load_dwordx4 v[28:31], v1, s[4:7], s46 offen offset:0 sc1\n"
"s_mov_b64 exec, -1\n"
".Lsk_230:\n"
"v_max_u32 v48, v34, v35\n"
"v_max3_u32 v48, v32, v33, v48\n"
"v_cmp_eq_u32 vcc, -1, v48\n"
"s_cbranch_vccz .Lsk_231\n"
"s_mov_b64 exec, vcc\n"
"buffer_load_dwordx4 v[32:35], v1, s[4:7], s46 offen offset:1024 sc1\n"
"s_mov_b64 exec, -1\n"
".Lsk_231:\n"
"v_max_u32 v48, v38, v39\n"
"v_max3_u32 v48, v36, v37, v48\n"
"v_cmp_eq_u32 vcc, -1, v48\n"
"s_cbranch_vccz .Lsk_232\n"
"s_mov_b64 exec, vcc\n"
"buffer_load_dwordx4 v[36:39], v1, s[4:7], s46 offen offset:2048 sc1\n"
"s_mov_b64 exec, -1\n"
".Lsk_232:\n"
"v_max_u32 v48, v42, v43\n"
"v_max3_u32 v48, v40, v41, v48\n"
"v_cmp_eq_u32 vcc, -1, v48\n"
"s_cbranch_vccz .Lsk_233\n"
"s_mov_b64 exec, vcc\n"
"buffer_load_dwordx4 v[40:43], v1, s[4:7], s46 offen offset:3072 sc1\n"
"s_mov_b64 exec, -1\n"
".Lsk_233:\n"
"v_max_u32 v48, v46, v47\n"
"v_max3_u32 v48, v44, v45, v48\n"
"v_cmp_eq_u32 vcc, -1, v48\n"
"s_cbranch_vccz .Lsk_234\n"
"s_mov_b64 exec, vcc\n"
"buffer_load_dwordx4 v[44:47], v1, s[4:7], s47 offen offset:0 sc1\n"
"s_mov_b64 exec, -1\n"
".Lsk_234:\n"
"s_branch .Ltp_228\n"
".Lou_229:\n"
"s_waitcnt vmcnt(0)\n"
"s_add_u32 s46, s44, 0x14a000\n"
"s_add_u32 s47, s46, 0x1000\n"
"buffer_load_dwordx4 v[28:31], v1, s[4:7], s46 offen offset:0 sc1\n"
"buffer_load_dwordx4 v[32:35], v1, s[4:7], s46 offen offset:1024 sc1\n"
"buffer_load_dwordx4 v[36:39], v1, s[4:7], s46 offen offset:2048 sc1\n"
"buffer_load_dwordx4 v[40:43], v1, s[4:7], s46 offen offset:3072 sc1\n"
"buffer_load_dwordx4 v[44:47], v1, s[4:7], s47 offen offset:0 sc1\n"
"s_mov_b32 s17, 32768\n"
".Ltp_235:\n"
"s_waitcnt vmcnt(0)\n"
"s_sub_u32 s17, s17, 1\n"
"s_cbranch_scc1 .Lou_236\n"
"v_max_u32 v48, v28, v29\n"
"v_max3_u32 v48, v30, v31, v48\n"
"v_max3_u32 v48, v32, v33, v48\n"
"v_max3_u32 v48, v34, v35, v48\n"
"v_max3_u32 v48, v36, v37, v48\n"
"v_max3_u32 v48, v38, v39, v48\n"
"v_max3_u32 v48, v40, v41, v48\n"
"v_max3_u32 v48, v42, v43, v48\n"
"v_max3_u32 v48, v44, v45, v48\n"
"v_max3_u32 v48, v46, v47, v48\n"
"v_cmp_eq_u32 vcc, -1, v48\n"
"s_cbranch_vccz .Lou_236\n"
"v_max_u32 v48, v30, v31\n"
"v_max3_u32 v48, v28, v29, v48\n"
"v_cmp_eq_u32 vcc, -1, v48\n"
"s_cbranch_vccz .Lsk_237\n"
"s_mov_b64 exec, vcc\n"
"buffer_load_dwordx4 v[28:31], v1, s[4:7], s46 offen offset:0 sc1\n"
"s_mov_b64 exec, -1\n"
".Lsk_237:\n"
"v_max_u32 v48, v34, v35\n"
"v_max3_u32 v48, v32, v33, v48\n"
"v_cmp_eq_u32 vcc, -1, v48\n"
"s_cbranch_vccz .Lsk_238\n"
"s_mov_b64 exec, vcc\n"
"buffer_load_dwordx4 v[32:35], v1, s[4:7], s46 offen offset:1024 sc1\n"
"s_mov_b64 exec, -1\n"
".Lsk_238:\n"
"v_max_u32 v48, v38, v39\n"
"v_max3_u32 v48, v36, v37, v48\n"
"v_cmp_eq_u32 vcc, -1, v48\n"
"s_cbranch_vccz .Lsk_239\n"
"s_mov_b64 exec, vcc\n"
"buffer_load_dwordx4 v[36:39], v1, s[4:7], s46 offen offset:2048 sc1\n"
"s_mov_b64 exec, -1\n"
".Lsk_239:\n"
"v_max_u32 v48, v42, v43\n"
"v_max3_u32 v48, v40, v41, v48\n"
"v_cmp_eq_u32 vcc, -1, v48\n"
"s_cbranch_vccz .Lsk_240\n"
"s_mov_b64 exec, vcc\n"
"buffer_load_dwordx4 v[40:43], v1, s[4:7], s46 offen offset:3072 sc1\n"
"s_mov_b64 exec, -1\n"
".Lsk_240:\n"
"v_max_u32 v48, v46, v47\n"
"v_max3_u32 v48, v44, v45, v48\n"
"v_cmp_eq_u32 vcc, -1, v48\n"
"s_cbranch_vccz .Lsk_241\n"
"s_mov_b64 exec, vcc\n"
"buffer_load_dwordx4 v[44:47], v1, s[4:7], s47 offen offset:0 sc1\n"
"s_mov_b64 exec, -1\n"
".Lsk_241:\n"
"s_branch .Ltp_235\n"
".Lou_236:\n"
"s_waitcnt vmcnt(0)\n"
"s_add_u32 s46, s44, 0x14f000\n"
"s_add_u32 s47, s46, 0x1000\n"
"buffer_load_dwordx4 v[28:31], v1, s[4:7], s46 offen offset:0 sc1\n"
"buffer_load_dwordx4 v[32:35], v1, s[4:7], s46 offen offset:1024 sc1\n"
"buffer_load_dwordx4 v[36:39], v1, s[4:7], s46 offen offset:2048 sc1\n"
"buffer_load_dwordx4 v[40:43], v1, s[4:7], s46 offen offset:3072 sc1\n"
"buffer_load_dwordx4 v[44:47], v1, s[4:7], s47 offen offset:0 sc1\n"
"s_mov_b32 s17, 32768\n"
".Ltp_242:\n"
"s_waitcnt vmcnt(0)\n"
"s_sub_u32 s17, s17, 1\n"
"s_cbranch_scc1 .Lou_243\n"
"v_max_u32 v48, v28, v29\n"
"v_max3_u32 v48, v30, v31, v48\n"
"v_max3_u32 v48, v32, v33, v48\n"
"v_max3_u32 v48, v34, v35, v48\n"
"v_max3_u32 v48, v36, v37, v48\n"
"v_max3_u32 v48, v38, v39, v48\n"
"v_max3_u32 v48, v40, v41, v48\n"
"v_max3_u32 v48, v42, v43, v48\n"
"v_max3_u32 v48, v44, v45, v48\n"
"v_max3_u32 v48, v46, v47, v48\n"
"v_cmp_eq_u32 vcc, -1, v48\n"
"s_cbranch_vccz .Lou_243\n"
"v_max_u32 v48, v30, v31\n"
"v_max3_u32 v48, v28, v29, v48\n"
"v_cmp_eq_u32 vcc, -1, v48\n"
"s_cbranch_vccz .Lsk_244\n"
"s_mov_b64 exec, vcc\n"
"buffer_load_dwordx4 v[28:31], v1, s[4:7], s46 offen offset:0 sc1\n"
"s_mov_b64 exec, -1\n"
".Lsk_244:\n"
"v_max_u32 v48, v34, v35\n"
"v_max3_u32 v48, v32, v33, v48\n"
"v_cmp_eq_u32 vcc, -1, v48\n"
"s_cbranch_vccz .Lsk_245\n"
"s_mov_b64 exec, vcc\n"
"buffer_load_dwordx4 v[32:35], v1, s[4:7], s46 offen offset:1024 sc1\n"
"s_mov_b64 exec, -1\n"
".Lsk_245:\n"
"v_max_u32 v48, v38, v39\n"
"v_max3_u32 v48, v36, v37, v48\n"
"v_cmp_eq_u32 vcc, -1, v48\n"
"s_cbranch_vccz .Lsk_246\n"
"s_mov_b64 exec, vcc\n"
"buffer_load_dwordx4 v[36:39], v1, s[4:7], s46 offen offset:2048 sc1\n"
"s_mov_b64 exec, -1\n"
".Lsk_246:\n"
"v_max_u32 v48, v42, v43\n"
"v_max3_u32 v48, v40, v41, v48\n"
"v_cmp_eq_u32 vcc, -1, v48\n"
"s_cbranch_vccz .Lsk_247\n"
"s_mov_b64 exec, vcc\n"
"buffer_load_dwordx4 v[40:43], v1, s[4:7], s46 offen offset:3072 sc1\n"
"s_mov_b64 exec, -1\n"
".Lsk_247:\n"
"v_max_u32 v48, v46, v47\n"
"v_max3_u32 v48, v44, v45, v48\n"
"v_cmp_eq_u32 vcc, -1, v48\n"
"s_cbranch_vccz .Lsk_248\n"
"s_mov_b64 exec, vcc\n"
"buffer_load_dwordx4 v[44:47], v1, s[4:7], s47 offen offset:0 sc1\n"
"s_mov_b64 exec, -1\n"
".Lsk_248:\n"
"s_branch .Ltp_242\n"
".Lou_243:\n"
"s_waitcnt vmcnt(0)\n"
"ds_write_b32 v13, v13 offset:416\n"
"s_mov_b32 s17, 0x100000\n"
".Ltp_249:\n"
"ds_read_b32 v90, v14 offset:416\n"
"s_sub_u32 s17, s17, 1\n"
"s_cbranch_scc1 .Lou_250\n"
"s_waitcnt lgkmcnt(0)\n"
"v_cmp_eq_u32 vcc, 0, v90\n"
"s_cbranch_vccnz .Ltp_249\n"
".Lou_250:\n"
"s_waitcnt lgkmcnt(0)\n"
"v_mov_b32 v90, 0\n"
"v_mov_b32 v90, -1\n"
"ds_write_b32 v9, v90 offset:3712\n"
"ds_write_b32 v9, v90 offset:4032\n"
"ds_write_b32 v9, v90 offset:4352\n"
"ds_write_b32 v9, v90 offset:4672\n"
"ds_write_b32 v9, v90 offset:4992\n"
"ds_write_b32 v10, v90 offset:5312\n"
"s_add_u32 m0, s29, 0xa000\n"
"s_add_u32 s31, s44, 0x140000\n"
"buffer_load_dwordx4 v1, s[4:7], s31 offen offset:0 sc1 lds\n"
"buffer_load_dwordx4 v1, s[4:7], s31 offen offset:1024 sc1 lds\n"
"buffer_load_dwordx4 v1, s[4:7], s31 offen offset:2048 sc1 lds\n"
"buffer_load_dwordx4 v1, s[4:7], s31 offen offset:3072 sc1 lds\n"
"s_add_u32 m0, m0, 1024\n"
"s_add_u32 s31, s31, 1024\n"
"buffer_load_dwordx4 v1, s[4:7], s31 offen offset:3072 sc1 lds\n"
"s_add_u32 m0, s29, 0xf000\n"
"s_add_u32 s31, s44, 0x145000\n"
"buffer_load_dwordx4 v1, s[4:7], s31 offen offset:0 sc1 lds\n"
"buffer_load_dwordx4 v1, s[4:7], s31 offen offset:1024 sc1 lds\n"
"buffer_load_dwordx4 v1, s[4:7], s31 offen offset:2048 sc1 lds\n"
"buffer_load_dwordx4 v1, s[4:7], s31 offen offset:3072 sc1 lds\n"
"s_add_u32 m0, m0, 1024\n"
"s_add_u32 s31, s31, 1024\n"
"buffer_load_dwordx4 v1, s[4:7], s31 offen offset:3072 sc1 lds\n"
"s_add_u32 m0, s29, 0\n"
"s_add_u32 s31, s44, 0x14a000\n"
"buffer_load_dwordx4 v1, s[4:7], s31 offen offset:0 sc1 lds\n"
"buffer_load_dwordx4 v1, s[4:7], s31 offen offset:1024 sc1 lds\n"
"buffer_load_dwordx4 v1, s[4:7], s31 offen offset:2048 sc1 lds\n"
"buffer_load_dwordx4 v1, s[4:7], s31 offen offset:3072 sc1 lds\n"
"s_add_u32 m0, m0, 1024\n"
"s_add_u32 s31, s31, 1024\n"
"buffer_load_dwordx4 v1, s[4:7], s31 offen offset:3072 sc1 lds\n"
"s_add_u32 m0, s29, 0x5000\n"
"s_add_u32 s31, s44, 0x14f000\n"
"buffer_load_dwordx4 v1, s[4:7], s31 offen offset:0 sc1 lds\n"
"buffer_load_dwordx4 v1, s[4:7], s31 offen offset:1024 sc1 lds\n"
"buffer_load_dwordx4 v1, s[4:7], s31 offen offset:2048 sc1 lds\n"
"buffer_load_dwordx4 v1, s[4:7], s31 offen offset:3072 sc1 lds\n"
"s_add_u32 m0, m0, 1024\n"
"s_add_u32 s31, s31, 1024\n"
"buffer_load_dwordx4 v1, s[4:7], s31 offen offset:3072 sc1 lds\n"
"s_waitcnt vmcnt(0)\n"
"ds_write_b32 v13, v13 offset:432\n"
"s_mov_b32 s17, 0x100000\n"
".Ltp_251:\n"
"ds_read_b32 v90, v14 offset:432\n"
"s_sub_u32 s17, s17, 1\n"
"s_cbranch_scc1 .Lou_252\n"
"s_waitcnt lgkmcnt(0)\n"
"v_cmp_eq_u32 vcc, 0, v90\n"
"s_cbranch_vccnz .Ltp_251\n"
".Lou_252:\n"
"s_waitcnt lgkmcnt(0)\n"
"s_branch .Lag_177\n"
".L_xend:\n"
"v_and_b32 v0, 63, v0\n"
"s_and_b32 s3, s3, 3\n"
"s_and_b32 s79, s2, 7\n"
"s_lshl_b32 s79, s79, 4\n"
"s_lshr_b32 s78, s2, 3\n"
"s_add_u32 s79, s79, s78\n"
"s_and_b32 s78, s2, 3\n"
"s_lshl_b32 s78, s78, 3\n"
"s_and_b32 s77, s2, 4\n"
"s_cmp_eq_i32 s77, 4\n"
"s_cselect_b32 s78, 32, s78\n"
"s_add_u32 s79, s79, s78\n"
"s_and_b32 s78, s2, 4\n"
"s_lshl_b32 s78, s78, 4\n"
"s_add_u32 s78, s2, s78\n"
"s_cmp_lt_i32 s78, 0xc0\n"
"s_cselect_b32 s18, s79, -1\n"
"s_and_b32 s79, s2, 7\n"
"s_lshl_b32 s79, s79, 4\n"
"s_lshr_b32 s78, s2, 3\n"
"s_add_u32 s79, s79, s78\n"
"s_and_b32 s78, s2, 3\n"
"s_lshl_b32 s78, s78, 3\n"
"s_add_u32 s78, s78, 24\n"
"s_and_b32 s77, s2, 4\n"
"s_cmp_eq_i32 s77, 4\n"
"s_cselect_b32 s78, 48, s78\n"
"s_sub_u32 s79, s79, s78\n"
"s_and_b32 s78, s2, 4\n"
"s_lshl_b32 s78, s78, 4\n"
"s_add_u32 s78, s2, s78\n"
"s_cmp_lt_i32 s78, 0xc0\n"
"s_cselect_b32 s19, 0x3e8, s79\n"
"v_lshlrev_b32 v2, 4, v0\n"
"s_mov_b32 s4, s26\n"
"s_mov_b32 s5, s27\n"
"s_mov_b32 s6, 0x15a8000\n"
"s_mov_b32 s7, 0x20000\n"
"v_lshlrev_b32 v127, 2, v0\n"
"s_mul_i32 s79, s3, 0x2100\n"
"s_add_u32 s79, s79, 0x14000\n"
"v_add_u32 v152, s79, v127\n"
"v_add_u32 v153, 0x440, v152\n"
"v_add_u32 v154, 0x880, v152\n"
"v_add_u32 v155, 0xcc0, v152\n"
"s_mul_i32 s79, s3, 0x2100\n"
"s_add_u32 s79, s79, 0x14000\n"
"v_and_b32 v99, 15, v0\n"
"v_and_b32 v98, 48, v0\n"
"s_mov_b32 s78, 0x110\n"
"v_mad_i32_i24 v99, v99, s78, v98\n"
"v_add_u32 v141, s79, v99\n"
"s_lshl_b32 s79, s3, 2\n"
"s_add_u32 s79, s79, 0x26400\n"
"v_mov_b32 v99, s79\n"
"v_mov_b32 v98, 0x40000\n"
"v_cmp_eq_i32 vcc, 0, v0\n"
"s_nop 1\n"
"v_cndmask_b32 v4, v98, v99, vcc\n"
"v_and_b32 v99, 3, v0\n"
"v_lshlrev_b32 v99, 2, v99\n"
"v_add_u32 v5, 0x26400, v99\n"
"v_min_i32 v99, 49, v0\n"
"v_lshlrev_b32 v38, 4, v99\n"
"s_lshr_b32 s79, s2, 3\n"
"s_and_b32 s28, s79, 7\n"
"s_and_b32 s79, s2, 7\n"
"s_lshl_b32 s79, s79, 2\n"
"s_lshr_b32 s78, s2, 3\n"
"s_lshr_b32 s78, s78, 3\n"
"s_add_u32 s29, s79, s78\n"
"s_and_b32 s79, s2, 7\n"
"s_lshl_b32 s79, s79, 12\n"
"s_add_u32 s48, s79, 0x11000\n"
"s_mov_b32 s8, s22\n"
"s_mov_b32 s9, s23\n"
"s_mov_b32 s10, 0x280000\n"
"s_mov_b32 s11, 0x20000\n"
"s_mov_b32 s12, s24\n"
"s_mov_b32 s13, s25\n"
"s_mov_b32 s14, 0x280000\n"
"s_mov_b32 s15, 0x20000\n"
"s_mov_b32 s44, 0xcccccccc\n"
"s_mov_b32 s45, 0xcccccccc\n"
"s_mov_b32 s56, 0\n"
"s_mov_b32 s57, -2\n"
"s_mov_b32 s58, 0\n"
"s_mov_b32 s59, -1\n"
"s_lshl_b32 s79, s29, 1\n"
"s_lshr_b32 s78, s3, 1\n"
"s_add_u32 s79, s79, s78\n"
"s_mul_i32 s30, s79, 0x5000\n"
"s_and_b32 s79, s2, 7\n"
"s_lshl_b32 s79, s79, 13\n"
"s_add_u32 s33, s79, 0x2400\n"
"s_lshl_b32 s79, s28, 2\n"
"s_add_u32 s79, s79, s3\n"
"s_mul_i32 s79, s79, 0x6400\n"
"s_add_u32 s32, s79, 0x2a0000\n"
"s_lshr_b32 s79, s2, 1\n"
"s_and_b32 s79, s79, 3\n"
"s_lshl_b32 s79, s79, 2\n"
"s_lshr_b32 s78, s2, 3\n"
"s_lshr_b32 s78, s78, 3\n"
"s_add_u32 s79, s79, s78\n"
"s_lshl_b32 s79, s79, 2\n"
"s_add_u32 s79, s79, s3\n"
"s_mul_i32 s79, s79, 0x5000\n"
"s_and_b32 s78, s2, 1\n"
"s_lshl_b32 s78, s78, 3\n"
"s_lshr_b32 s77, s2, 3\n"
"s_and_b32 s77, s77, 7\n"
"s_add_u32 s78, s78, s77\n"
"s_mul_i32 s78, s78, 0x500\n"
"s_add_u32 s79, s79, s78\n"
"s_add_u32 s31, s79, 0x368000\n"
"v_mov_b32 v128, 0\n"
"v_mov_b32 v129, 0\n"
"v_mov_b32 v130, 0\n"
"v_mov_b32 v131, 0\n"
"v_mov_b32 v132, 0\n"
"v_mov_b32 v133, 0\n"
"v_mov_b32 v134, 0\n"
"v_mov_b32 v135, 0\n"
"v_mov_b32 v136, 0\n"
"v_mov_b32 v137, 0\n"
"v_mov_b32 v138, 0\n"
"v_mov_b32 v139, 0\n"
"s_waitcnt lgkmcnt(0)\n"
"s_and_b32 s79, s3, 1\n"
"s_lshl_b32 s60, s79, 4\n"
"s_lshr_b32 s40, s40, s60\n"
"s_lshr_b32 s41, s41, s60\n"
"v_mov_b32 v142, s40\n"
"v_mov_b32 v140, s41\n"
"s_mul_i32 s16, s16, 0x2c9277b5\n"
"s_add_u32 s16, s16, 0xac564b05\n"
"s_cmp_lt_i32 s18, 0\n"
"s_cbranch_scc1 .Lnk_275\n"
"s_setprio 0\n"
"s_mov_b32 s55, 3\n"
"s_mov_b32 s52, 0xc2f00000\n"
"s_mov_b32 s53, 0x3fb8aa3b\n"
"s_mov_b32 s54, 0x3f317218\n"
"v_mov_b32 v31, 0x42f00000\n"
"v_lshlrev_b32 v142, 16, v142\n"
"v_med3_f32 v142, v142, s52, v31\n"
"v_mul_f32 v28, s53, v142\n"
"v_exp_f32 v29, v28\n"
"v_fma_f32 v28, v142, s53, -v28\n"
"v_fmac_f32 v28, 0x32a57060, v142\n"
"v_fma_f32 v28, v28, s54, 1.0\n"
"v_mul_f32 v142, v29, v28\n"
"v_cvt_pk_bf16_f32 v142, v142, v142\n"
"v_lshlrev_b32 v142, 16, v142\n"
"s_waitcnt vmcnt(8)\n"
"s_nop 3\n"
".Lkg_278:\n"
"v_lshlrev_b32 v40, 16, v40\n"
"v_lshlrev_b32 v41, 16, v41\n"
"v_lshlrev_b32 v42, 16, v42\n"
"v_lshlrev_b32 v43, 16, v43\n"
"v_mul_f32 v26, v142, v42\n"
"v_mul_f32 v27, v142, v43\n"
"v_max3_f32 v30, |v26|, |v27|, 0\n"
"s_nop 1\n"
"v_max_u32_dpp v30, v30, v30 row_ror:8 row_mask:0xf bank_mask:0xf\n"
"v_med3_f32 v40, v40, s52, v31\n"
"s_nop 0\n"
"v_max_u32_dpp v30, v30, v30 row_ror:4 row_mask:0xf bank_mask:0xf\n"
"v_mul_f32 v28, s53, v40\n"
"s_nop 0\n"
"v_max_u32_dpp v30, v30, v30 row_ror:2 row_mask:0xf bank_mask:0xf\n"
"v_exp_f32 v29, v28\n"
"s_nop 0\n"
"v_max_u32_dpp v30, v30, v30 row_ror:1 row_mask:0xf bank_mask:0xf\n"
"v_fma_f32 v28, v40, s53, -v28\n"
"v_fmac_f32 v28, 0x32a57060, v40\n"
"v_mov_b32 v42, v30\n"
"v_fma_f32 v28, v28, s54, 1.0\n"
"s_nop 0\n"
"v_permlane16_swap_b32 v30, v42\n"
"v_mul_f32 v40, v29, v28\n"
"v_max_u32 v30, v30, v42\n"
"v_cvt_pk_bf16_f32 v40, v40, v40\n"
"v_lshlrev_b32 v40, 16, v40\n"
"v_mov_b32 v42, v30\n"
"v_mul_f32 v25, v142, v41\n"
"s_nop 0\n"
"v_permlane32_swap_b32 v30, v42\n"
"v_mul_f32 v24, v142, -v40\n"
"v_max_u32 v30, v30, v42\n"
"v_mul_f32 v25, 0.15915494, v25\n"
"v_mul_f32 v24, s53, v24\n"
"v_readfirstlane_b32 s51, v30\n"
"s_nop 2\n"
"s_bfe_u32 s50, s51, 0x80017\n"
"s_max_i32 s50, s50, 37\n"
"s_min_i32 s50, s50, 217\n"
"s_cmp_gt_i32 s51, 0\n"
"s_cselect_b32 s50, s50, 127\n"
"s_sub_u32 s51, 267, s50\n"
"s_lshl_b32 s51, s51, 23\n"
"s_sub_u32 s50, s50, 27\n"
"s_lshl_b32 s50, s50, 23\n"
"v_mul_f32 v26, s51, v26\n"
"v_mul_f32 v27, s51, v27\n"
"v_mul_f32 v111, 0x41800000, v25\n"
"v_mul_f32 v100, 0x41800000, v24\n"
"v_fract_f32 v111, v111\n"
"v_exp_f32 v100, v100\n"
"v_cos_f32 v110, v111\n"
"v_sin_f32 v111, -v111\n"
"v_mul_f32 v110, v110, v100\n"
"v_mul_f32 v111, v111, v100\n"
"v_fract_f32 v113, v25\n"
"v_exp_f32 v100, v24\n"
"v_cos_f32 v112, v113\n"
"v_sin_f32 v113, v113\n"
"v_mul_f32 v112, v112, v100\n"
"v_mul_f32 v113, v113, v100\n"
"v_mul_f32 v115, 0x43800000, v25\n"
"v_mul_f32 v100, 0x43800000, v24\n"
"v_fract_f32 v115, v115\n"
"v_exp_f32 v100, v100\n"
"v_cos_f32 v114, v115\n"
"v_sin_f32 v115, v115\n"
"v_mul_f32 v114, v114, v100\n"
"v_mul_f32 v115, v115, v100\n"
"v_mul_f32 v116, v111, v111\n"
"v_add_f32 v117, v110, v110\n"
"v_fma_f32 v116, v110, v110, -v116\n"
"v_mul_f32 v117, v117, v111\n"
"v_mul_f32 v120, v117, v117\n"
"v_add_f32 v121, v116, v116\n"
"v_fma_f32 v120, v116, v116, -v120\n"
"v_mul_f32 v121, v121, v117\n"
"v_mul_f32 v118, v117, v111\n"
"v_mul_f32 v119, v116, v111\n"
"v_fma_f32 v118, v116, v110, -v118\n"
"v_fmac_f32 v119, v117, v110\n"
"v_mul_f32 v122, v121, v121\n"
"v_add_f32 v123, v120, v120\n"
"v_fma_f32 v122, v120, v120, -v122\n"
"v_mul_f32 v123, v123, v121\n"
"v_mov_b32 v84, 0x7400\n"
"v_mul_f32 v124, 0x46800000, v110\n"
"v_mul_f32 v125, 0x46800000, v111\n"
"v_cvt_pk_f16_f32 v100, v124, v125\n"
"v_mul_f32 v124, 0x46800000, v116\n"
"v_mul_f32 v125, 0x46800000, v117\n"
"v_cvt_pk_f16_f32 v85, v124, v125\n"
"v_mul_f32 v124, 0x46800000, v118\n"
"v_mul_f32 v125, 0x46800000, v119\n"
"v_cvt_pk_f16_f32 v101, v124, v125\n"
"v_cvt_pk_f16_f32 v93, v120, v121\n"
"v_cvt_pk_f16_f32 v94, v122, v123\n"
"v_mul_f32 v124, v123, v121\n"
"v_mul_f32 v125, v122, v121\n"
"v_fma_f32 v124, v122, v120, -v124\n"
"v_fmac_f32 v125, v123, v120\n"
"v_cvt_pk_f16_f32 v95, v124, v125\n"
"ds_write2_b32 v152, v84, v100 offset0:0 offset1:68\n"
"v_pk_mul_f16 v86, v84, v93 op_sel:[0,0] op_sel_hi:[1,0]\n"
"v_pk_mul_f16 v102, v100, v93 op_sel:[0,0] op_sel_hi:[1,0]\n"
"v_pk_fma_f16 v86, v84, v93, v86 op_sel:[1,1,0] op_sel_hi:[0,1,1] neg_lo:[1,0,0]\n"
"v_pk_fma_f16 v102, v100, v93, v102 op_sel:[1,1,0] op_sel_hi:[0,1,1] neg_lo:[1,0,0]\n"
"ds_write2_b32 v152, v85, v101 offset0:136 offset1:204\n"
"v_pk_mul_f16 v87, v85, v93 op_sel:[0,0] op_sel_hi:[1,0]\n"
"v_pk_mul_f16 v103, v101, v93 op_sel:[0,0] op_sel_hi:[1,0]\n"
"v_pk_fma_f16 v87, v85, v93, v87 op_sel:[1,1,0] op_sel_hi:[0,1,1] neg_lo:[1,0,0]\n"
"v_pk_fma_f16 v103, v101, v93, v103 op_sel:[1,1,0] op_sel_hi:[0,1,1] neg_lo:[1,0,0]\n"
"ds_write2_b32 v153, v86, v102 offset0:0 offset1:68\n"
"v_pk_mul_f16 v88, v84, v94 op_sel:[0,0] op_sel_hi:[1,0]\n"
"v_pk_mul_f16 v102, v100, v94 op_sel:[0,0] op_sel_hi:[1,0]\n"
"v_pk_fma_f16 v88, v84, v94, v88 op_sel:[1,1,0] op_sel_hi:[0,1,1] neg_lo:[1,0,0]\n"
"v_pk_fma_f16 v102, v100, v94, v102 op_sel:[1,1,0] op_sel_hi:[0,1,1] neg_lo:[1,0,0]\n"
"ds_write2_b32 v153, v87, v103 offset0:136 offset1:204\n"
"v_pk_mul_f16 v89, v85, v94 op_sel:[0,0] op_sel_hi:[1,0]\n"
"v_pk_mul_f16 v103, v101, v94 op_sel:[0,0] op_sel_hi:[1,0]\n"
"v_pk_fma_f16 v89, v85, v94, v89 op_sel:[1,1,0] op_sel_hi:[0,1,1] neg_lo:[1,0,0]\n"
"v_pk_fma_f16 v103, v101, v94, v103 op_sel:[1,1,0] op_sel_hi:[0,1,1] neg_lo:[1,0,0]\n"
"ds_write2_b32 v154, v88, v102 offset0:0 offset1:68\n"
"v_pk_mul_f16 v90, v84, v95 op_sel:[0,0] op_sel_hi:[1,0]\n"
"v_pk_mul_f16 v102, v100, v95 op_sel:[0,0] op_sel_hi:[1,0]\n"
"v_pk_fma_f16 v90, v84, v95, v90 op_sel:[1,1,0] op_sel_hi:[0,1,1] neg_lo:[1,0,0]\n"
"v_pk_fma_f16 v102, v100, v95, v102 op_sel:[1,1,0] op_sel_hi:[0,1,1] neg_lo:[1,0,0]\n"
"ds_write2_b32 v154, v89, v103 offset0:136 offset1:204\n"
"v_pk_mul_f16 v91, v85, v95 op_sel:[0,0] op_sel_hi:[1,0]\n"
"v_pk_mul_f16 v103, v101, v95 op_sel:[0,0] op_sel_hi:[1,0]\n"
"v_pk_fma_f16 v91, v85, v95, v91 op_sel:[1,1,0] op_sel_hi:[0,1,1] neg_lo:[1,0,0]\n"
"v_pk_fma_f16 v103, v101, v95, v103 op_sel:[1,1,0] op_sel_hi:[0,1,1] neg_lo:[1,0,0]\n"
"ds_write2_b32 v155, v90, v102 offset0:0 offset1:68\n"
"ds_write2_b32 v155, v91, v103 offset0:136 offset1:204\n"
"ds_read_b128 v[52:55], v141 offset:0\n"
"ds_read_b128 v[56:59], v141 offset:64\n"
"ds_read_b128 v[60:63], v141 offset:128\n"
"ds_read_b128 v[64:67], v141 offset:192\n"
"v_mul_f32 v116, v113, v113\n"
"v_add_f32 v117, v112, v112\n"
"v_fma_f32 v116, v112, v112, -v116\n"
"v_mul_f32 v117, v117, v113\n"
"v_mul_f32 v120, v117, v117\n"
"v_add_f32 v121, v116, v116\n"
"v_fma_f32 v120, v116, v116, -v120\n"
"v_mul_f32 v121, v121, v117\n"
"v_mul_f32 v118, v117, v113\n"
"v_mul_f32 v119, v116, v113\n"
"v_fma_f32 v118, v116, v112, -v118\n"
"v_fmac_f32 v119, v117, v112\n"
"v_mul_f32 v122, v121, v121\n"
"v_add_f32 v123, v120, v120\n"
"v_fma_f32 v122, v120, v120, -v122\n"
"v_mul_f32 v123, v123, v121\n"
"v_cvt_pk_f16_f32 v84, v26, v27\n"
"v_mul_f32 v124, v27, v113\n"
"v_mul_f32 v125, v26, v113\n"
"v_fma_f32 v124, v26, v112, -v124\n"
"v_fmac_f32 v125, v27, v112\n"
"v_cvt_pk_f16_f32 v100, v124, v125\n"
"v_mul_f32 v124, v27, v117\n"
"v_mul_f32 v125, v26, v117\n"
"v_fma_f32 v124, v26, v116, -v124\n"
"v_fmac_f32 v125, v27, v116\n"
"v_cvt_pk_f16_f32 v85, v124, v125\n"
"v_mul_f32 v124, v27, v119\n"
"v_mul_f32 v125, v26, v119\n"
"v_fma_f32 v124, v26, v118, -v124\n"
"v_fmac_f32 v125, v27, v118\n"
"v_cvt_pk_f16_f32 v101, v124, v125\n"
"v_cvt_pk_f16_f32 v93, v120, v121\n"
"v_cvt_pk_f16_f32 v94, v122, v123\n"
"v_mul_f32 v124, v123, v121\n"
"v_mul_f32 v125, v122, v121\n"
"v_fma_f32 v124, v122, v120, -v124\n"
"v_fmac_f32 v125, v123, v120\n"
"v_cvt_pk_f16_f32 v95, v124, v125\n"
"ds_write2_b32 v152, v84, v100 offset0:0 offset1:68\n"
"v_pk_mul_f16 v86, v84, v93 op_sel:[0,0] op_sel_hi:[1,0]\n"
"v_pk_mul_f16 v102, v100, v93 op_sel:[0,0] op_sel_hi:[1,0]\n"
"v_pk_fma_f16 v86, v84, v93, v86 op_sel:[1,1,0] op_sel_hi:[0,1,1] neg_lo:[1,0,0]\n"
"v_pk_fma_f16 v102, v100, v93, v102 op_sel:[1,1,0] op_sel_hi:[0,1,1] neg_lo:[1,0,0]\n"
"ds_write2_b32 v152, v85, v101 offset0:136 offset1:204\n"
"v_pk_mul_f16 v87, v85, v93 op_sel:[0,0] op_sel_hi:[1,0]\n"
"v_pk_mul_f16 v103, v101, v93 op_sel:[0,0] op_sel_hi:[1,0]\n"
"v_pk_fma_f16 v87, v85, v93, v87 op_sel:[1,1,0] op_sel_hi:[0,1,1] neg_lo:[1,0,0]\n"
"v_pk_fma_f16 v103, v101, v93, v103 op_sel:[1,1,0] op_sel_hi:[0,1,1] neg_lo:[1,0,0]\n"
"ds_write2_b32 v153, v86, v102 offset0:0 offset1:68\n"
"v_pk_mul_f16 v88, v84, v94 op_sel:[0,0] op_sel_hi:[1,0]\n"
"v_pk_mul_f16 v102, v100, v94 op_sel:[0,0] op_sel_hi:[1,0]\n"
"v_pk_fma_f16 v88, v84, v94, v88 op_sel:[1,1,0] op_sel_hi:[0,1,1] neg_lo:[1,0,0]\n"
"v_pk_fma_f16 v102, v100, v94, v102 op_sel:[1,1,0] op_sel_hi:[0,1,1] neg_lo:[1,0,0]\n"
"ds_write2_b32 v153, v87, v103 offset0:136 offset1:204\n"
"v_pk_mul_f16 v89, v85, v94 op_sel:[0,0] op_sel_hi:[1,0]\n"
"v_pk_mul_f16 v103, v101, v94 op_sel:[0,0] op_sel_hi:[1,0]\n"
"v_pk_fma_f16 v89, v85, v94, v89 op_sel:[1,1,0] op_sel_hi:[0,1,1] neg_lo:[1,0,0]\n"
"v_pk_fma_f16 v103, v101, v94, v103 op_sel:[1,1,0] op_sel_hi:[0,1,1] neg_lo:[1,0,0]\n"
"ds_write2_b32 v154, v88, v102 offset0:0 offset1:68\n"
"v_pk_mul_f16 v90, v84, v95 op_sel:[0,0] op_sel_hi:[1,0]\n"
"v_pk_mul_f16 v102, v100, v95 op_sel:[0,0] op_sel_hi:[1,0]\n"
"v_pk_fma_f16 v90, v84, v95, v90 op_sel:[1,1,0] op_sel_hi:[0,1,1] neg_lo:[1,0,0]\n"
"v_pk_fma_f16 v102, v100, v95, v102 op_sel:[1,1,0] op_sel_hi:[0,1,1] neg_lo:[1,0,0]\n"
"ds_write2_b32 v154, v89, v103 offset0:136 offset1:204\n"
"v_pk_mul_f16 v91, v85, v95 op_sel:[0,0] op_sel_hi:[1,0]\n"
"v_pk_mul_f16 v103, v101, v95 op_sel:[0,0] op_sel_hi:[1,0]\n"
"v_pk_fma_f16 v91, v85, v95, v91 op_sel:[1,1,0] op_sel_hi:[0,1,1] neg_lo:[1,0,0]\n"
"v_pk_fma_f16 v103, v101, v95, v103 op_sel:[1,1,0] op_sel_hi:[0,1,1] neg_lo:[1,0,0]\n"
"ds_write2_b32 v155, v90, v102 offset0:0 offset1:68\n"
"ds_write2_b32 v155, v91, v103 offset0:136 offset1:204\n"
"ds_read_b128 v[68:71], v141 offset:0\n"
"ds_read_b128 v[72:75], v141 offset:64\n"
"ds_read_b128 v[76:79], v141 offset:128\n"
"ds_read_b128 v[80:83], v141 offset:192\n"
"v_mul_f32 v116, v115, v115\n"
"v_add_f32 v117, v114, v114\n"
"v_fma_f32 v116, v114, v114, -v116\n"
"v_mul_f32 v117, v117, v115\n"
"v_mul_f32 v120, v117, v117\n"
"v_add_f32 v121, v116, v116\n"
"v_fma_f32 v120, v116, v116, -v120\n"
"v_mul_f32 v121, v121, v117\n"
"v_mul_f32 v118, v117, v115\n"
"v_mul_f32 v119, v116, v115\n"
"v_fma_f32 v118, v116, v114, -v118\n"
"v_fmac_f32 v119, v117, v114\n"
"v_cvt_pk_f16_f32 v92, v114, v115\n"
"v_cvt_pk_f16_f32 v93, v116, v117\n"
"v_cvt_pk_f16_f32 v95, v120, v121\n"
"v_cvt_pk_f16_f32 v94, v118, v119\n"
"v_mul_f32 v124, v121, v115\n"
"v_mul_f32 v125, v120, v115\n"
"v_fma_f32 v124, v120, v114, -v124\n"
"v_fmac_f32 v125, v121, v114\n"
"v_cvt_pk_f16_f32 v96, v124, v125\n"
"v_mul_f32 v126, v121, v117\n"
"v_mul_f32 v127, v120, v117\n"
"v_fma_f32 v126, v120, v116, -v126\n"
"v_fmac_f32 v127, v121, v116\n"
"v_cvt_pk_f16_f32 v97, v126, v127\n"
"v_mul_f32 v124, v121, v119\n"
"v_mul_f32 v125, v120, v119\n"
"v_fma_f32 v124, v120, v118, -v124\n"
"v_fmac_f32 v125, v121, v118\n"
"v_cvt_pk_f16_f32 v98, v124, v125\n"
"v_mul_f32 v126, v121, v121\n"
"v_mul_f32 v127, v120, v121\n"
"v_fma_f32 v126, v120, v120, -v126\n"
"v_fmac_f32 v127, v121, v120\n"
"v_cvt_pk_f16_f32 v99, v126, v127\n"
"s_setprio 3\n"
"s_waitcnt lgkmcnt(3)\n"
"v_mfma_f32_16x16x32_f16 v[200:203], v[68:71], v[52:55], 0\n"
"v_pk_mul_f16 v100, v84, v92 op_sel:[0,0] op_sel_hi:[1,0]\n"
"v_pk_mul_f16 v101, v85, v92 op_sel:[0,0] op_sel_hi:[1,0]\n"
"s_waitcnt lgkmcnt(2)\n"
"v_mfma_f32_16x16x32_f16 v[200:203], v[72:75], v[56:59], v[200:203]\n"
"v_pk_fma_f16 v100, v84, v92, v100 op_sel:[1,1,0] op_sel_hi:[0,1,1] neg_lo:[1,0,0]\n"
"v_pk_fma_f16 v101, v85, v92, v101 op_sel:[1,1,0] op_sel_hi:[0,1,1] neg_lo:[1,0,0]\n"
"s_waitcnt lgkmcnt(1)\n"
"v_mfma_f32_16x16x32_f16 v[200:203], v[76:79], v[60:63], v[200:203]\n"
"v_pk_mul_f16 v102, v86, v92 op_sel:[0,0] op_sel_hi:[1,0]\n"
"v_pk_mul_f16 v103, v87, v92 op_sel:[0,0] op_sel_hi:[1,0]\n"
"s_waitcnt lgkmcnt(0)\n"
"v_mfma_f32_16x16x32_f16 v[200:203], v[80:83], v[64:67], v[200:203]\n"
"s_setprio 0\n"
"v_pk_fma_f16 v102, v86, v92, v102 op_sel:[1,1,0] op_sel_hi:[0,1,1] neg_lo:[1,0,0]\n"
"v_pk_fma_f16 v103, v87, v92, v103 op_sel:[1,1,0] op_sel_hi:[0,1,1] neg_lo:[1,0,0]\n"
"ds_write2_b32 v152, v100, v101 offset0:0 offset1:68\n"
"v_pk_mul_f16 v100, v88, v92 op_sel:[0,0] op_sel_hi:[1,0]\n"
"v_pk_mul_f16 v101, v89, v92 op_sel:[0,0] op_sel_hi:[1,0]\n"
"v_pk_fma_f16 v100, v88, v92, v100 op_sel:[1,1,0] op_sel_hi:[0,1,1] neg_lo:[1,0,0]\n"
"v_pk_fma_f16 v101, v89, v92, v101 op_sel:[1,1,0] op_sel_hi:[0,1,1] neg_lo:[1,0,0]\n"
"ds_write2_b32 v152, v102, v103 offset0:136 offset1:204\n"
"v_pk_mul_f16 v102, v90, v92 op_sel:[0,0] op_sel_hi:[1,0]\n"
"v_pk_mul_f16 v103, v91, v92 op_sel:[0,0] op_sel_hi:[1,0]\n"
"v_pk_fma_f16 v102, v90, v92, v102 op_sel:[1,1,0] op_sel_hi:[0,1,1] neg_lo:[1,0,0]\n"
"v_pk_fma_f16 v103, v91, v92, v103 op_sel:[1,1,0] op_sel_hi:[0,1,1] neg_lo:[1,0,0]\n"
"ds_write2_b32 v153, v100, v101 offset0:0 offset1:68\n"
"v_pk_mul_f16 v100, v84, v93 op_sel:[0,0] op_sel_hi:[1,0]\n"
"v_pk_mul_f16 v101, v86, v93 op_sel:[0,0] op_sel_hi:[1,0]\n"
"v_pk_fma_f16 v100, v84, v93, v100 op_sel:[1,1,0] op_sel_hi:[0,1,1] neg_lo:[1,0,0]\n"
"v_pk_fma_f16 v101, v86, v93, v101 op_sel:[1,1,0] op_sel_hi:[0,1,1] neg_lo:[1,0,0]\n"
"ds_write2_b32 v153, v102, v103 offset0:136 offset1:204\n"
"v_pk_mul_f16 v102, v88, v93 op_sel:[0,0] op_sel_hi:[1,0]\n"
"v_pk_mul_f16 v103, v90, v93 op_sel:[0,0] op_sel_hi:[1,0]\n"
"v_pk_fma_f16 v102, v88, v93, v102 op_sel:[1,1,0] op_sel_hi:[0,1,1] neg_lo:[1,0,0]\n"
"v_pk_fma_f16 v103, v90, v93, v103 op_sel:[1,1,0] op_sel_hi:[0,1,1] neg_lo:[1,0,0]\n"
"ds_write2_b32 v154, v100, v101 offset0:0 offset1:68\n"
"v_pk_mul_f16 v100, v84, v94 op_sel:[0,0] op_sel_hi:[1,0]\n"
"v_pk_mul_f16 v101, v86, v94 op_sel:[0,0] op_sel_hi:[1,0]\n"
"v_pk_fma_f16 v100, v84, v94, v100 op_sel:[1,1,0] op_sel_hi:[0,1,1] neg_lo:[1,0,0]\n"
"v_pk_fma_f16 v101, v86, v94, v101 op_sel:[1,1,0] op_sel_hi:[0,1,1] neg_lo:[1,0,0]\n"
"ds_write2_b32 v154, v102, v103 offset0:136 offset1:204\n"
"v_pk_mul_f16 v102, v88, v94 op_sel:[0,0] op_sel_hi:[1,0]\n"
"v_pk_mul_f16 v103, v90, v94 op_sel:[0,0] op_sel_hi:[1,0]\n"
"v_pk_fma_f16 v102, v88, v94, v102 op_sel:[1,1,0] op_sel_hi:[0,1,1] neg_lo:[1,0,0]\n"
"v_pk_fma_f16 v103, v90, v94, v103 op_sel:[1,1,0] op_sel_hi:[0,1,1] neg_lo:[1,0,0]\n"
"ds_write2_b32 v155, v100, v101 offset0:0 offset1:68\n"
"ds_write2_b32 v155, v102, v103 offset0:136 offset1:204\n"
"ds_read_b128 v[68:71], v141 offset:0\n"
"ds_read_b128 v[72:75], v141 offset:64\n"
"ds_read_b128 v[76:79], v141 offset:128\n"
"ds_read_b128 v[80:83], v141 offset:192\n"
"v_pk_mul_f16 v100, v84, v95 op_sel:[0,0] op_sel_hi:[1,0]\n"
"v_pk_mul_f16 v101, v88, v95 op_sel:[0,0] op_sel_hi:[1,0]\n"
"v_pk_fma_f16 v100, v84, v95, v100 op_sel:[1,1,0] op_sel_hi:[0,1,1] neg_lo:[1,0,0]\n"
"v_pk_fma_f16 v101, v88, v95, v101 op_sel:[1,1,0] op_sel_hi:[0,1,1] neg_lo:[1,0,0]\n"
"v_pk_mul_f16 v102, v84, v96 op_sel:[0,0] op_sel_hi:[1,0]\n"
"v_pk_mul_f16 v103, v88, v96 op_sel:[0,0] op_sel_hi:[1,0]\n"
"v_pk_fma_f16 v102, v84, v96, v102 op_sel:[1,1,0] op_sel_hi:[0,1,1] neg_lo:[1,0,0]\n"
"v_pk_fma_f16 v103, v88, v96, v103 op_sel:[1,1,0] op_sel_hi:[0,1,1] neg_lo:[1,0,0]\n"
"v_pk_mul_f16 v104, v84, v97 op_sel:[0,0] op_sel_hi:[1,0]\n"
"v_pk_mul_f16 v105, v88, v97 op_sel:[0,0] op_sel_hi:[1,0]\n"
"v_pk_fma_f16 v104, v84, v97, v104 op_sel:[1,1,0] op_sel_hi:[0,1,1] neg_lo:[1,0,0]\n"
"v_pk_fma_f16 v105, v88, v97, v105 op_sel:[1,1,0] op_sel_hi:[0,1,1] neg_lo:[1,0,0]\n"
"v_pk_mul_f16 v106, v84, v98 op_sel:[0,0] op_sel_hi:[1,0]\n"
"v_pk_mul_f16 v107, v88, v98 op_sel:[0,0] op_sel_hi:[1,0]\n"
"v_pk_fma_f16 v106, v84, v98, v106 op_sel:[1,1,0] op_sel_hi:[0,1,1] neg_lo:[1,0,0]\n"
"v_pk_fma_f16 v107, v88, v98, v107 op_sel:[1,1,0] op_sel_hi:[0,1,1] neg_lo:[1,0,0]\n"
"v_pk_mul_f16 v108, v84, v99 op_sel:[0,0] op_sel_hi:[1,0]\n"
"v_pk_mul_f16 v109, v88, v99 op_sel:[0,0] op_sel_hi:[1,0]\n"
"v_pk_fma_f16 v108, v84, v99, v108 op_sel:[1,1,0] op_sel_hi:[0,1,1] neg_lo:[1,0,0]\n"
"v_pk_fma_f16 v109, v88, v99, v109 op_sel:[1,1,0] op_sel_hi:[0,1,1] neg_lo:[1,0,0]\n"
"s_setprio 3\n"
"s_waitcnt lgkmcnt(3)\n"
"v_mfma_f32_16x16x32_f16 v[204:207], v[68:71], v[52:55], 0\n"
"ds_write2_b32 v152, v100, v101 offset0:0 offset1:68\n"
"s_waitcnt lgkmcnt(3)\n"
"v_mfma_f32_16x16x32_f16 v[204:207], v[72:75], v[56:59], v[204:207]\n"
"ds_write2_b32 v152, v102, v103 offset0:136 offset1:204\n"
"s_waitcnt lgkmcnt(3)\n"
"v_mfma_f32_16x16x32_f16 v[204:207], v[76:79], v[60:63], v[204:207]\n"
"ds_write2_b32 v153, v104, v105 offset0:0 offset1:68\n"
"s_waitcnt lgkmcnt(3)\n"
"v_mfma_f32_16x16x32_f16 v[204:207], v[80:83], v[64:67], v[204:207]\n"
"s_setprio 0\n"
"ds_write2_b32 v153, v106, v107 offset0:136 offset1:204\n"
"ds_write2_b32 v154, v108, v109 offset0:0 offset1:68\n"
"ds_read_b128 v[68:71], v141 offset:0\n"
"ds_read_b128 v[72:75], v141 offset:64\n"
"ds_read_b128 v[76:79], v141 offset:128\n"
"ds_read_b128 v[80:83], v141 offset:192\n"
"s_waitcnt vmcnt(0)\n"
"v_mov_b32 v40, v44\n"
"v_mov_b32 v41, v45\n"
"v_mov_b32 v42, v46\n"
"v_mov_b32 v43, v47\n"
"v_mov_b32 v44, v48\n"
"v_mov_b32 v45, v49\n"
"v_mov_b32 v46, v50\n"
"v_mov_b32 v47, v51\n"
"v_fmac_f32 v128, s50, v200\n"
"v_fmac_f32 v129, s50, v201\n"
"v_fmac_f32 v130, s50, v202\n"
"v_fmac_f32 v131, s50, v203\n"
"v_fmac_f32 v132, s50, v204\n"
"v_fmac_f32 v133, s50, v205\n"
"v_fmac_f32 v134, s50, v206\n"
"v_fmac_f32 v135, s50, v207\n"
"s_sub_u32 s55, s55, 1\n"
"s_setprio 3\n"
"s_waitcnt lgkmcnt(3)\n"
"v_mfma_f32_16x16x32_f16 v[208:211], v[68:71], v[52:55], 0\n"
"s_waitcnt lgkmcnt(2)\n"
"v_mfma_f32_16x16x32_f16 v[208:211], v[72:75], v[56:59], v[208:211]\n"
"s_waitcnt lgkmcnt(1)\n"
"v_mfma_f32_16x16x32_f16 v[208:211], v[76:79], v[60:63], v[208:211]\n"
"s_waitcnt lgkmcnt(0)\n"
"v_mfma_f32_16x16x32_f16 v[208:211], v[80:83], v[64:67], v[208:211]\n"
"s_setprio 0\n"
"s_nop 6\n"
"v_fmac_f32 v136, s50, v208\n"
"v_fmac_f32 v137, s50, v209\n"
"v_fmac_f32 v138, s50, v210\n"
"v_fmac_f32 v139, s50, v211\n"
"s_cmp_lg_u32 s55, 0\n"
"s_cbranch_scc1 .Lkg_278\n"
"s_setprio 3\n"
"buffer_load_dwordx4 v[40:43], v38, s[4:7], s33 offen sc1\n"
"s_setprio 0\n"
"v_and_b32 v79, 15, v0\n"
"v_and_b32 v78, 1, v79\n"
"v_lshrrev_b32 v77, 4, v0\n"
"v_lshlrev_b32 v76, 3, v77\n"
"v_lshlrev_b32 v75, 8, v77\n"
"v_lshlrev_b32 v74, 7, v78\n"
"v_add_u32 v73, v75, v74\n"
"v_lshrrev_b32 v72, 1, v79\n"
"v_lshlrev_b32 v71, 4, v72\n"
"v_add_u32 v17, v73, v71\n"
"v_mul_i32_i24 v70, 0x500, v79\n"
"v_add_u32 v14, v70, v76\n"
"s_mov_b32 s79, 0x25400\n"
"s_cmp_eq_i32 s3, 0\n"
"s_cselect_b32 s79, s79, 0x21000\n"
"s_mov_b32 s78, 0x25c00\n"
"s_cmp_eq_i32 s3, 2\n"
"s_cselect_b32 s78, s78, 0x23400\n"
"s_cmp_lt_i32 s3, 2\n"
"s_cselect_b32 s79, s79, s78\n"
"v_add_u32 v6, s79, v2\n"
"s_mov_b32 s79, 0x40000\n"
"s_cmp_eq_i32 s3, 0\n"
"s_cselect_b32 s79, s79, 0x20c00\n"
"s_mov_b32 s78, 0x40000\n"
"s_cmp_eq_i32 s3, 2\n"
"s_cselect_b32 s78, s78, 0x23000\n"
"s_cmp_lt_i32 s3, 2\n"
"s_cselect_b32 s79, s79, s78\n"
"v_add_u32 v7, s79, v2\n"
"s_mov_b32 s79, 0x23c00\n"
"s_cmp_eq_i32 s3, 0\n"
"s_cselect_b32 s79, s79, 0x1f200\n"
"s_mov_b32 s78, 0x21800\n"
"s_cmp_eq_i32 s3, 2\n"
"s_cselect_b32 s78, s78, 0x1ce00\n"
"s_cmp_lt_i32 s3, 2\n"
"s_cselect_b32 s79, s79, s78\n"
"v_add_u32 v8, s79, v2\n"
"s_mov_b32 s79, 0x20400\n"
"s_cmp_eq_i32 s3, 0\n"
"s_cselect_b32 s79, s79, 0x24c00\n"
"s_mov_b32 s78, 0x1e000\n"
"s_cmp_eq_i32 s3, 2\n"
"s_cselect_b32 s78, s78, 0x22800\n"
"s_cmp_lt_i32 s3, 2\n"
"s_cselect_b32 s79, s79, s78\n"
"v_add_u32 v9, s79, v2\n"
"s_mov_b32 s79, 0x24400\n"
"s_cmp_eq_i32 s3, 0\n"
"s_cselect_b32 s79, s79, 0x24800\n"
"s_mov_b32 s78, 0x22000\n"
"s_cmp_eq_i32 s3, 2\n"
"s_cselect_b32 s78, s78, 0x22400\n"
"s_cmp_lt_i32 s3, 2\n"
"s_cselect_b32 s79, s79, s78\n"
"v_add_u32 v10, s79, v2\n"
"s_and_b32 s79, s3, 1\n"
"s_lshl_b32 s79, s79, 30\n"
"v_add_u32 v15, s79, v14\n"
"v_lshlrev_b32 v99, 3, v79\n"
"v_mov_b32 v98, 0x40000000\n"
"v_lshrrev_b32 v97, 2, v79\n"
"v_lshl_add_u32 v97, v78, 3, v97\n"
"v_cmp_eq_i32 vcc, v97, v77\n"
"s_nop 1\n"
"v_cndmask_b32 v16, v98, v99, vcc\n"
"s_and_b32 s79, s3, 1\n"
"s_lshl_b32 s79, s79, 30\n"
"v_add_u32 v18, s79, v17\n"
"s_and_b32 s79, s3, 1\n"
"s_sub_u32 s79, s79, 1\n"
"v_mov_b32 v19, s79\n"
"v_lshrrev_b32 v99, 1, v77\n"
"v_and_b32 v98, 1, v77\n"
"v_and_b32 v97, 4, v79\n"
"v_and_b32 v96, 3, v79\n"
"v_lshrrev_b32 v95, 3, v79\n"
"v_lshlrev_b32 v95, 6, v95\n"
"v_lshl_add_u32 v96, v96, 4, v95\n"
"v_lshl_add_u32 v97, v97, 1, v96\n"
"v_lshl_add_u32 v98, v98, 8, v97\n"
"v_lshl_add_u32 v99, v99, 7, v98\n"
"v_sub_u32 v39, v99, v14\n"
"s_mov_b32 s79, 0x20c00\n"
"s_cmp_eq_i32 s3, 0\n"
"s_cselect_b32 s79, s79, 0x25400\n"
"s_mov_b32 s78, 0x23000\n"
"s_cmp_eq_i32 s3, 2\n"
"s_cselect_b32 s78, s78, 0x25c00\n"
"s_cmp_lt_i32 s3, 2\n"
"s_cselect_b32 s79, s79, s78\n"
"v_add_u32 v11, s79, v2\n"
"s_mov_b32 s79, 0x21800\n"
"s_cmp_eq_i32 s3, 0\n"
"s_cselect_b32 s79, s79, 0x1ce00\n"
"s_mov_b32 s78, 0x23c00\n"
"s_cmp_eq_i32 s3, 2\n"
"s_cselect_b32 s78, s78, 0x1f200\n"
"s_cmp_lt_i32 s3, 2\n"
"s_cselect_b32 s79, s79, s78\n"
"v_add_u32 v12, s79, v2\n"
"s_mov_b32 s79, 0x22400\n"
"s_cmp_eq_i32 s3, 0\n"
"s_cselect_b32 s79, s79, 0x1e000\n"
"s_mov_b32 s78, 0x24800\n"
"s_cmp_eq_i32 s3, 2\n"
"s_cselect_b32 s78, s78, 0x20400\n"
"s_cmp_lt_i32 s3, 2\n"
"s_cselect_b32 s79, s79, s78\n"
"v_add_u32 v13, s79, v2\n"
"v_mov_b32 v20, 0x3f803f80\n"
"v_mov_b32 v21, 0x3f803f80\n"
"v_mov_b32 v22, 0x3f803f80\n"
"v_mov_b32 v23, 0x3f803f80\n"
"s_waitcnt vmcnt(0)\n"
"s_nop 3\n"
"s_mov_b32 s17, 32768\n"
"s_branch .Lmd_281\n"
".Ltp_279:\n"
"buffer_load_dwordx4 v[40:43], v38, s[4:7], s33 offen sc1\n"
".Lmd_281:\n"
"s_sub_u32 s17, s17, 1\n"
"s_cbranch_scc1 .Lou_280\n"
"s_waitcnt vmcnt(0)\n"
"v_xor_b32 v40, s16, v40\n"
"v_xor_b32 v41, s16, v41\n"
"v_xor_b32 v42, s16, v42\n"
"v_xor_b32 v43, s16, v43\n"
"v_or_b32 v40, v40, v41\n"
"v_or3_b32 v40, v40, v42, v43\n"
"v_cmp_ne_u32 vcc, 0, v40\n"
"s_cbranch_vccz .Lou_280\n"
"s_sleep 1\n"
"s_branch .Ltp_279\n"
".Lou_280:\n"
"s_waitcnt vmcnt(0)\n"
"s_branch .Lpl_277\n"
".Lnk_275:\n"
"v_and_b32 v79, 15, v0\n"
"v_and_b32 v78, 1, v79\n"
"v_lshrrev_b32 v77, 4, v0\n"
"v_lshlrev_b32 v76, 3, v77\n"
"v_lshlrev_b32 v75, 8, v77\n"
"v_lshlrev_b32 v74, 7, v78\n"
"v_add_u32 v73, v75, v74\n"
"v_lshrrev_b32 v72, 1, v79\n"
"v_lshlrev_b32 v71, 4, v72\n"
"v_add_u32 v17, v73, v71\n"
"v_mul_i32_i24 v70, 0x500, v79\n"
"v_add_u32 v14, v70, v76\n"
"s_mov_b32 s79, 0x25400\n"
"s_cmp_eq_i32 s3, 0\n"
"s_cselect_b32 s79, s79, 0x21000\n"
"s_mov_b32 s78, 0x25c00\n"
"s_cmp_eq_i32 s3, 2\n"
"s_cselect_b32 s78, s78, 0x23400\n"
"s_cmp_lt_i32 s3, 2\n"
"s_cselect_b32 s79, s79, s78\n"
"v_add_u32 v6, s79, v2\n"
"s_mov_b32 s79, 0x40000\n"
"s_cmp_eq_i32 s3, 0\n"
"s_cselect_b32 s79, s79, 0x20c00\n"
"s_mov_b32 s78, 0x40000\n"
"s_cmp_eq_i32 s3, 2\n"
"s_cselect_b32 s78, s78, 0x23000\n"
"s_cmp_lt_i32 s3, 2\n"
"s_cselect_b32 s79, s79, s78\n"
"v_add_u32 v7, s79, v2\n"
"s_mov_b32 s79, 0x23c00\n"
"s_cmp_eq_i32 s3, 0\n"
"s_cselect_b32 s79, s79, 0x1f200\n"
"s_mov_b32 s78, 0x21800\n"
"s_cmp_eq_i32 s3, 2\n"
"s_cselect_b32 s78, s78, 0x1ce00\n"
"s_cmp_lt_i32 s3, 2\n"
"s_cselect_b32 s79, s79, s78\n"
"v_add_u32 v8, s79, v2\n"
"s_mov_b32 s79, 0x20400\n"
"s_cmp_eq_i32 s3, 0\n"
"s_cselect_b32 s79, s79, 0x24c00\n"
"s_mov_b32 s78, 0x1e000\n"
"s_cmp_eq_i32 s3, 2\n"
"s_cselect_b32 s78, s78, 0x22800\n"
"s_cmp_lt_i32 s3, 2\n"
"s_cselect_b32 s79, s79, s78\n"
"v_add_u32 v9, s79, v2\n"
"s_mov_b32 s79, 0x24400\n"
"s_cmp_eq_i32 s3, 0\n"
"s_cselect_b32 s79, s79, 0x24800\n"
"s_mov_b32 s78, 0x22000\n"
"s_cmp_eq_i32 s3, 2\n"
"s_cselect_b32 s78, s78, 0x22400\n"
"s_cmp_lt_i32 s3, 2\n"
"s_cselect_b32 s79, s79, s78\n"
"v_add_u32 v10, s79, v2\n"
"s_and_b32 s79, s3, 1\n"
"s_lshl_b32 s79, s79, 30\n"
"v_add_u32 v15, s79, v14\n"
"v_lshlrev_b32 v99, 3, v79\n"
"v_mov_b32 v98, 0x40000000\n"
"v_lshrrev_b32 v97, 2, v79\n"
"v_lshl_add_u32 v97, v78, 3, v97\n"
"v_cmp_eq_i32 vcc, v97, v77\n"
"s_nop 1\n"
"v_cndmask_b32 v16, v98, v99, vcc\n"
"s_and_b32 s79, s3, 1\n"
"s_lshl_b32 s79, s79, 30\n"
"v_add_u32 v18, s79, v17\n"
"s_and_b32 s79, s3, 1\n"
"s_sub_u32 s79, s79, 1\n"
"v_mov_b32 v19, s79\n"
"v_lshrrev_b32 v99, 1, v77\n"
"v_and_b32 v98, 1, v77\n"
"v_and_b32 v97, 4, v79\n"
"v_and_b32 v96, 3, v79\n"
"v_lshrrev_b32 v95, 3, v79\n"
"v_lshlrev_b32 v95, 6, v95\n"
"v_lshl_add_u32 v96, v96, 4, v95\n"
"v_lshl_add_u32 v97, v97, 1, v96\n"
"v_lshl_add_u32 v98, v98, 8, v97\n"
"v_lshl_add_u32 v99, v99, 7, v98\n"
"v_sub_u32 v39, v99, v14\n"
"s_mov_b32 s79, 0x20c00\n"
"s_cmp_eq_i32 s3, 0\n"
"s_cselect_b32 s79, s79, 0x25400\n"
"s_mov_b32 s78, 0x23000\n"
"s_cmp_eq_i32 s3, 2\n"
"s_cselect_b32 s78, s78, 0x25c00\n"
"s_cmp_lt_i32 s3, 2\n"
"s_cselect_b32 s79, s79, s78\n"
"v_add_u32 v11, s79, v2\n"
"s_mov_b32 s79, 0x21800\n"
"s_cmp_eq_i32 s3, 0\n"
"s_cselect_b32 s79, s79, 0x1ce00\n"
"s_mov_b32 s78, 0x23c00\n"
"s_cmp_eq_i32 s3, 2\n"
"s_cselect_b32 s78, s78, 0x1f200\n"
"s_cmp_lt_i32 s3, 2\n"
"s_cselect_b32 s79, s79, s78\n"
"v_add_u32 v12, s79, v2\n"
"s_mov_b32 s79, 0x22400\n"
"s_cmp_eq_i32 s3, 0\n"
"s_cselect_b32 s79, s79, 0x1e000\n"
"s_mov_b32 s78, 0x24800\n"
"s_cmp_eq_i32 s3, 2\n"
"s_cselect_b32 s78, s78, 0x20400\n"
"s_cmp_lt_i32 s3, 2\n"
"s_cselect_b32 s79, s79, s78\n"
"v_add_u32 v13, s79, v2\n"
"v_mov_b32 v20, 0x3f803f80\n"
"v_mov_b32 v21, 0x3f803f80\n"
"v_mov_b32 v22, 0x3f803f80\n"
"v_mov_b32 v23, 0x3f803f80\n"
"s_mov_b32 s17, 0x100000\n"
".Ltp_282:\n"
"s_sleep 2\n"
"ds_read_b32 v3, v5 offset:16\n"
"s_sub_u32 s17, s17, 1\n"
"s_cbranch_scc1 .Lou_283\n"
"s_waitcnt lgkmcnt(0)\n"
"v_cmp_eq_u32 vcc, 0, v3\n"
"s_cbranch_vccnz .Ltp_282\n"
".Lou_283:\n"
"s_waitcnt lgkmcnt(0)\n"
"s_nop 2\n"
"s_mov_b32 s17, 32768\n"
".Ltp_284:\n"
"buffer_load_dwordx4 v[40:43], v38, s[4:7], s33 offen sc1\n"
"s_sub_u32 s17, s17, 1\n"
"s_cbranch_scc1 .Lou_285\n"
"s_waitcnt vmcnt(0)\n"
"v_xor_b32 v40, s16, v40\n"
"v_xor_b32 v41, s16, v41\n"
"v_xor_b32 v42, s16, v42\n"
"v_xor_b32 v43, s16, v43\n"
"v_or_b32 v40, v40, v41\n"
"v_or3_b32 v40, v40, v42, v43\n"
"v_cmp_ne_u32 vcc, 0, v40\n"
"s_cbranch_vccz .Lou_285\n"
"s_sleep 1\n"
"s_branch .Ltp_284\n"
".Lou_285:\n"
"s_waitcnt vmcnt(0)\n"
".Lpl_277:\n"
"s_mov_b32 s17, 0x100000\n"
".Ltp_286:\n"
"s_sleep 2\n"
"ds_read_b32 v3, v5 offset:32\n"
"s_sub_u32 s17, s17, 1\n"
"s_cbranch_scc1 .Lou_287\n"
"s_waitcnt lgkmcnt(0)\n"
"v_cmp_eq_u32 vcc, 0, v3\n"
"s_cbranch_vccnz .Ltp_286\n"
".Lou_287:\n"
"s_waitcnt lgkmcnt(0)\n"
"buffer_load_dwordx4 v[156:159], v2, s[4:7], s32 offen offset:0 sc1\n"
"buffer_load_dwordx4 v[160:163], v2, s[4:7], s32 offen offset:1024 sc1\n"
"buffer_load_dwordx4 v[164:167], v2, s[4:7], s32 offen offset:2048 sc1\n"
"buffer_load_dwordx4 v[168:171], v2, s[4:7], s32 offen offset:3072 sc1\n"
"s_add_u32 s32, s32, 4096\n"
"buffer_load_dwordx4 v[172:175], v2, s[4:7], s32 offen offset:0 sc1\n"
"buffer_load_dwordx4 v[176:179], v2, s[4:7], s32 offen offset:1024 sc1\n"
"buffer_load_dwordx4 v[180:183], v2, s[4:7], s32 offen offset:2048 sc1\n"
"buffer_load_dwordx4 v[184:187], v2, s[4:7], s32 offen offset:3072 sc1\n"
"s_add_u32 s32, s32, 4096\n"
"buffer_load_dwordx4 v[188:191], v2, s[4:7], s32 offen offset:0 sc1\n"
"buffer_load_dwordx4 v[192:195], v2, s[4:7], s32 offen offset:1024 sc1\n"
"buffer_load_dwordx4 v[196:199], v2, s[4:7], s32 offen offset:2048 sc1\n"
"buffer_load_dwordx4 v[200:203], v2, s[4:7], s32 offen offset:3072 sc1\n"
"s_add_u32 s32, s32, 4096\n"
"buffer_load_dwordx4 v[204:207], v2, s[4:7], s32 offen offset:0 sc1\n"
"buffer_load_dwordx4 v[208:211], v2, s[4:7], s32 offen offset:1024 sc1\n"
"buffer_load_dwordx4 v[212:215], v2, s[4:7], s32 offen offset:2048 sc1\n"
"buffer_load_dwordx4 v[216:219], v2, s[4:7], s32 offen offset:3072 sc1\n"
"s_add_u32 s32, s32, 4096\n"
"buffer_load_dwordx4 v[220:223], v2, s[4:7], s32 offen offset:0 sc1\n"
"buffer_load_dwordx4 v[224:227], v2, s[4:7], s32 offen offset:1024 sc1\n"
"buffer_load_dwordx4 v[228:231], v2, s[4:7], s32 offen offset:2048 sc1\n"
"buffer_load_dwordx4 v[232:235], v2, s[4:7], s32 offen offset:3072 sc1\n"
"s_add_u32 s32, s32, 4096\n"
"buffer_load_dwordx4 v[236:239], v2, s[4:7], s32 offen offset:0 sc1\n"
"buffer_load_dwordx4 v[240:243], v2, s[4:7], s32 offen offset:1024 sc1\n"
"buffer_load_dwordx4 v[244:247], v2, s[4:7], s32 offen offset:2048 sc1\n"
"buffer_load_dwordx4 v[248:251], v2, s[4:7], s32 offen offset:3072 sc1\n"
"s_add_u32 s32, s32, 4096\n"
"buffer_load_dwordx4 v[252:255], v2, s[4:7], s32 offen offset:0 sc1\n"
"s_cmp_lt_i32 s18, 0\n"
"s_cselect_b32 s43, 3, 0\n"
"s_setprio 2\n"
"v_mov_b32 v40, -1\n"
"v_mov_b32 v41, -1\n"
"v_mov_b32 v42, -1\n"
"v_mov_b32 v43, -1\n"
"s_lshl_b32 s79, s2, 2\n"
"s_add_u32 s79, s79, s3\n"
"s_lshl_b32 s79, s79, 10\n"
"s_add_u32 s49, s79, 0xb48000\n"
"buffer_store_dwordx4 v[40:43], v2, s[4:7], s49 offen sc1\n"
"s_add_u32 s49, s49, 0x100000\n"
"buffer_store_dwordx4 v[40:43], v2, s[4:7], s49 offen sc1\n"
"s_add_u32 s49, s49, 0x100000\n"
"s_sub_u32 s79, 0xdc7fff, s49\n"
"s_and_b32 s79, s79, 0x80000000\n"
"s_or_b32 s49, s49, s79\n"
"buffer_store_dwordx4 v[40:43], v2, s[4:7], s49 offen sc1\n"
"s_nop 1\n"
"s_setprio 0\n"
"s_cmp_lg_u32 s43, 0\n"
"s_cbranch_scc1 .Lsk_305\n"
"ds_read_b32 v3, v5 offset:80\n"
"s_waitcnt lgkmcnt(0)\n"
"v_cmp_eq_u32 vcc, 0, v3\n"
"s_cbranch_vccz .Len_304\n"
"s_call_b64 s[46:47], .Lst_288\n"
".Lsk_305:\n"
"s_cmp_lg_u32 s43, 1\n"
"s_cbranch_scc1 .Lsk_306\n"
"ds_read_b32 v3, v5 offset:96\n"
"s_waitcnt lgkmcnt(0)\n"
"v_cmp_eq_u32 vcc, 0, v3\n"
"s_cbranch_vccz .Len_304\n"
"s_call_b64 s[46:47], .Lst_289\n"
".Lsk_306:\n"
"s_cmp_lg_u32 s43, 2\n"
"s_cbranch_scc1 .Lsk_307\n"
"ds_read_b32 v3, v5 offset:96\n"
"s_waitcnt lgkmcnt(0)\n"
"v_cmp_eq_u32 vcc, 0, v3\n"
"s_cbranch_vccz .Len_304\n"
"s_call_b64 s[46:47], .Lst_290\n"
".Lsk_307:\n"
".Len_304:\n"
"s_setprio 2\n"
"s_mov_b32 s17, 0x100000\n"
".Ltp_308:\n"
"s_sleep 2\n"
"ds_read_b32 v3, v5 offset:96\n"
"s_sub_u32 s17, s17, 1\n"
"s_cbranch_scc1 .Lou_309\n"
"s_waitcnt lgkmcnt(0)\n"
"v_cmp_eq_u32 vcc, 0, v3\n"
"s_cbranch_vccnz .Ltp_308\n"
".Lou_309:\n"
"s_waitcnt lgkmcnt(0)\n"
"s_nop 2\n"
"s_mov_b32 s17, 32768\n"
".Ltp_310:\n"
"buffer_load_dwordx4 v[40:43], v2, s[4:7], s48 offen sc1\n"
"s_sub_u32 s17, s17, 1\n"
"s_cbranch_scc1 .Lou_311\n"
"s_waitcnt vmcnt(0)\n"
"v_xor_b32 v40, s16, v40\n"
"v_xor_b32 v41, s16, v41\n"
"v_xor_b32 v42, s16, v42\n"
"v_xor_b32 v43, s16, v43\n"
"v_or_b32 v40, v40, v41\n"
"v_or3_b32 v40, v40, v42, v43\n"
"v_cmp_ne_u32 vcc, 0, v40\n"
"s_cbranch_vccz .Lou_311\n"
"s_sleep 2\n"
"s_branch .Ltp_310\n"
".Lou_311:\n"
"s_waitcnt vmcnt(0)\n"
"s_mul_i32 s79, s28, 0xa0\n"
"s_add_u32 s79, s30, s79\n"
"s_and_b32 s78, s3, 1\n"
"s_mul_i32 s78, s78, 0x60\n"
"s_add_u32 s36, s79, s78\n"
"s_mul_i32 s79, s3, 0x1400\n"
"s_add_u32 s79, s30, s79\n"
"s_add_u32 s32, s79, 0x368000\n"
"s_add_u32 s33, s32, 0x1000\n"
"s_lshl_b32 s79, s29, 1\n"
"s_lshr_b32 s78, s3, 1\n"
"s_xor_b32 s78, s78, 1\n"
"s_add_u32 s79, s79, s78\n"
"s_mul_i32 s79, s79, 0x5000\n"
"s_mul_i32 s78, s3, 0x1400\n"
"s_add_u32 s79, s79, s78\n"
"s_add_u32 s34, s79, 0x368000\n"
"s_add_u32 s35, s34, 0x1000\n"
"s_lshl_b32 s79, s30, 1\n"
"s_mul_i32 s78, s28, 0x1400\n"
"s_add_u32 s79, s79, s78\n"
"s_and_b32 s78, s3, 1\n"
"s_mul_i32 s78, s78, 0xc00\n"
"s_add_u32 s79, s79, s78\n"
"s_add_u32 s37, s79, 0x5e8000\n"
"s_lshl_b32 s79, s29, 1\n"
"s_lshr_b32 s78, s3, 1\n"
"s_add_u32 s79, s79, s78\n"
"s_lshl_b32 s79, s79, 11\n"
"s_lshl_b32 s78, s28, 8\n"
"s_add_u32 s79, s79, s78\n"
"s_and_b32 s78, s3, 1\n"
"s_lshl_b32 s78, s78, 7\n"
"s_add_u32 s79, s79, s78\n"
"s_add_u32 s38, s79, 0xae8000\n"
"buffer_load_dwordx2 v[40:41], v14, s[8:11], s36 offen offset:0\n"
"buffer_load_dwordx2 v[42:43], v14, s[8:11], s36 offen offset:32\n"
"buffer_load_dwordx2 v[44:45], v15, s[8:11], s36 offen offset:64\n"
"s_mov_b32 s17, 0x100000\n"
".Ltp_312:\n"
"s_sleep 1\n"
"ds_read_b32 v3, v5 offset:96\n"
"s_sub_u32 s17, s17, 1\n"
"s_cbranch_scc1 .Lou_313\n"
"s_waitcnt lgkmcnt(0)\n"
"v_cmp_eq_u32 vcc, 0, v3\n"
"s_cbranch_vccnz .Ltp_312\n"
".Lou_313:\n"
"s_waitcnt lgkmcnt(0)\n"
"s_sleep 8\n"
"s_cmp_eq_u32 s17, 0xfffff\n"
"s_cbranch_scc1 .Lsk_314\n"
"s_sleep 0\n"
".Lsk_314:\n"
"buffer_load_dwordx4 v[108:111], v2, s[4:7], s34 offen offset:0 sc1\n"
"s_nop 6\n"
"buffer_load_dwordx4 v[112:115], v2, s[4:7], s34 offen offset:1024 sc1\n"
"s_nop 6\n"
"buffer_load_dwordx4 v[116:119], v2, s[4:7], s34 offen offset:2048 sc1\n"
"s_nop 6\n"
"buffer_load_dwordx4 v[120:123], v2, s[4:7], s34 offen offset:3072 sc1\n"
"s_nop 6\n"
"buffer_load_dwordx4 v[124:127], v2, s[4:7], s35 offen offset:0 sc1\n"
"s_nop 6\n"
"buffer_load_dwordx4 v[88:91], v2, s[4:7], s32 offen offset:0 sc1\n"
"s_nop 6\n"
"buffer_load_dwordx4 v[92:95], v2, s[4:7], s32 offen offset:1024 sc1\n"
"s_nop 6\n"
"buffer_load_dwordx4 v[96:99], v2, s[4:7], s32 offen offset:2048 sc1\n"
"s_nop 6\n"
"buffer_load_dwordx4 v[100:103], v2, s[4:7], s32 offen offset:3072 sc1\n"
"s_nop 6\n"
"buffer_load_dwordx4 v[104:107], v2, s[4:7], s33 offen offset:0 sc1\n"
"s_waitcnt vmcnt(12)\n"
"v_lshlrev_b32 v48, 16, v40\n"
"v_and_b32 v49, 0xffff0000, v40\n"
"v_lshlrev_b32 v50, 16, v41\n"
"v_and_b32 v51, 0xffff0000, v41\n"
"s_waitcnt vmcnt(11)\n"
"v_lshlrev_b32 v52, 16, v42\n"
"v_and_b32 v53, 0xffff0000, v42\n"
"v_lshlrev_b32 v54, 16, v43\n"
"v_and_b32 v55, 0xffff0000, v43\n"
"s_waitcnt vmcnt(10)\n"
"v_lshlrev_b32 v56, 16, v44\n"
"v_and_b32 v57, 0xffff0000, v44\n"
"v_lshlrev_b32 v58, 16, v45\n"
"v_and_b32 v59, 0xffff0000, v45\n"
"v_mov_b32 v46, 0\n"
"v_mov_b32 v47, 0\n"
"s_waitcnt vmcnt(9)\n"
"v_max_u32 v38, v108, v109\n"
"v_max3_u32 v38, v110, v111, v38\n"
"v_cmp_eq_u32 vcc, -1, v38\n"
"s_cbranch_vccnz .Lsl_315\n"
".Lbk_316:\n"
"v_mfma_f32_16x16x32_bf16 v[68:71], v[156:159], v[108:111], 0\n"
"v_mfma_f32_16x16x32_bf16 v[72:75], v[160:163], v[108:111], 0\n"
"v_mfma_f32_16x16x32_bf16 v[76:79], v[164:167], v[108:111], 0\n"
"s_waitcnt vmcnt(8)\n"
"v_max_u32 v38, v112, v113\n"
"v_max3_u32 v38, v114, v115, v38\n"
"v_mfma_f32_16x16x32_bf16 v[80:83], v[168:171], v[108:111], 0\n"
"v_cmp_eq_u32 vcc, -1, v38\n"
"v_mfma_f32_16x16x32_bf16 v[84:87], v[172:175], v[108:111], 0\n"
"s_cbranch_vccnz .Lsl_317\n"
".Lbk_318:\n"
"v_mfma_f32_16x16x32_bf16 v[68:71], v[176:179], v[112:115], v[68:71]\n"
"v_mfma_f32_16x16x32_bf16 v[72:75], v[180:183], v[112:115], v[72:75]\n"
"v_mfma_f32_16x16x32_bf16 v[76:79], v[184:187], v[112:115], v[76:79]\n"
"s_waitcnt vmcnt(7)\n"
"v_max_u32 v38, v116, v117\n"
"v_max3_u32 v38, v118, v119, v38\n"
"v_mfma_f32_16x16x32_bf16 v[80:83], v[188:191], v[112:115], v[80:83]\n"
"v_cmp_eq_u32 vcc, -1, v38\n"
"v_mfma_f32_16x16x32_bf16 v[84:87], v[192:195], v[112:115], v[84:87]\n"
"s_cbranch_vccnz .Lsl_319\n"
".Lbk_320:\n"
"v_mfma_f32_16x16x32_bf16 v[68:71], v[196:199], v[116:119], v[68:71]\n"
"v_mfma_f32_16x16x32_bf16 v[72:75], v[200:203], v[116:119], v[72:75]\n"
"v_mfma_f32_16x16x32_bf16 v[76:79], v[204:207], v[116:119], v[76:79]\n"
"s_waitcnt vmcnt(6)\n"
"v_max_u32 v38, v120, v121\n"
"v_max3_u32 v38, v122, v123, v38\n"
"v_mfma_f32_16x16x32_bf16 v[80:83], v[208:211], v[116:119], v[80:83]\n"
"v_cmp_eq_u32 vcc, -1, v38\n"
"v_mfma_f32_16x16x32_bf16 v[84:87], v[212:215], v[116:119], v[84:87]\n"
"s_cbranch_vccnz .Lsl_321\n"
".Lbk_322:\n"
"v_mfma_f32_16x16x32_bf16 v[68:71], v[216:219], v[120:123], v[68:71]\n"
"v_mfma_f32_16x16x32_bf16 v[72:75], v[220:223], v[120:123], v[72:75]\n"
"v_mfma_f32_16x16x32_bf16 v[76:79], v[224:227], v[120:123], v[76:79]\n"
"s_waitcnt vmcnt(5)\n"
"v_max_u32 v38, v124, v125\n"
"v_max3_u32 v38, v126, v127, v38\n"
"v_mfma_f32_16x16x32_bf16 v[80:83], v[228:231], v[120:123], v[80:83]\n"
"v_cmp_eq_u32 vcc, -1, v38\n"
"v_mfma_f32_16x16x32_bf16 v[84:87], v[232:235], v[120:123], v[84:87]\n"
"s_cbranch_vccnz .Lsl_323\n"
".Lbk_324:\n"
"v_mfma_f32_16x16x32_bf16 v[68:71], v[236:239], v[124:127], v[68:71]\n"
"v_mfma_f32_16x16x32_bf16 v[72:75], v[240:243], v[124:127], v[72:75]\n"
"v_mfma_f32_16x16x32_bf16 v[76:79], v[244:247], v[124:127], v[76:79]\n"
"s_waitcnt vmcnt(4)\n"
"v_max_u32 v38, v88, v89\n"
"v_max3_u32 v38, v90, v91, v38\n"
"v_mfma_f32_16x16x32_bf16 v[80:83], v[248:251], v[124:127], v[80:83]\n"
"v_cmp_eq_u32 vcc, -1, v38\n"
"v_mfma_f32_16x16x32_bf16 v[84:87], v[252:255], v[124:127], v[84:87]\n"
"ds_write_b128 v8, v[68:71] offset:0\n"
"s_cbranch_vccnz .Lsl_325\n"
".Lbk_326:\n"
"v_mfma_f32_16x16x32_bf16 v[48:51], v[156:159], v[88:91], v[48:51]\n"
"ds_write_b128 v8, v[72:75] offset:1024\n"
"v_mfma_f32_16x16x32_bf16 v[52:55], v[160:163], v[88:91], v[52:55]\n"
"ds_write_b128 v10, v[76:79] offset:0\n"
"v_mfma_f32_16x16x32_bf16 v[56:59], v[164:167], v[88:91], v[56:59]\n"
"s_waitcnt vmcnt(3)\n"
"v_max_u32 v38, v92, v93\n"
"v_max3_u32 v38, v94, v95, v38\n"
"v_mfma_f32_16x16x32_bf16 v[60:63], v[168:171], v[88:91], 0\n"
"v_cmp_eq_u32 vcc, -1, v38\n"
"v_mov_b32 v117, s16\n"
"v_mfma_f32_16x16x32_bf16 v[64:67], v[172:175], v[88:91], 0\n"
"ds_write_b128 v9, v[80:83] offset:0\n"
"s_cbranch_vccnz .Lsl_327\n"
".Lbk_328:\n"
"v_mfma_f32_16x16x32_bf16 v[48:51], v[176:179], v[92:95], v[48:51]\n"
"ds_write_b128 v9, v[84:87] offset:1024\n"
"v_mfma_f32_16x16x32_bf16 v[52:55], v[180:183], v[92:95], v[52:55]\n"
"ds_write_b32 v4, v4 offset:544\n"
"v_mfma_f32_16x16x32_bf16 v[56:59], v[184:187], v[92:95], v[56:59]\n"
"s_waitcnt vmcnt(2)\n"
"v_max_u32 v38, v96, v97\n"
"v_max3_u32 v38, v98, v99, v38\n"
"v_mfma_f32_16x16x32_bf16 v[60:63], v[188:191], v[92:95], v[60:63]\n"
"v_cmp_eq_u32 vcc, -1, v38\n"
"v_mov_b32 v119, s16\n"
"v_mfma_f32_16x16x32_bf16 v[64:67], v[192:195], v[92:95], v[64:67]\n"
"s_cbranch_vccnz .Lsl_329\n"
".Lbk_330:\n"
"v_mfma_f32_16x16x32_bf16 v[48:51], v[196:199], v[96:99], v[48:51]\n"
"v_mov_b32 v121, s16\n"
"v_mov_b32 v123, s16\n"
"v_mfma_f32_16x16x32_bf16 v[52:55], v[200:203], v[96:99], v[52:55]\n"
"v_mov_b32 v125, s16\n"
"v_mov_b32 v127, s16\n"
"v_mfma_f32_16x16x32_bf16 v[56:59], v[204:207], v[96:99], v[56:59]\n"
"s_waitcnt vmcnt(1)\n"
"v_max_u32 v38, v100, v101\n"
"v_max3_u32 v38, v102, v103, v38\n"
"v_mfma_f32_16x16x32_bf16 v[60:63], v[208:211], v[96:99], v[60:63]\n"
"v_cmp_eq_u32 vcc, -1, v38\n"
"ds_read_b32 v3, v5 offset:544\n"
"ds_read_b128 v[76:79], v12 offset:2048\n"
"v_mfma_f32_16x16x32_bf16 v[64:67], v[212:215], v[96:99], v[64:67]\n"
"ds_read_b128 v[108:111], v13 offset:2048\n"
"s_cbranch_vccnz .Lsl_331\n"
".Lbk_332:\n"
"v_mfma_f32_16x16x32_bf16 v[56:59], v[224:227], v[100:103], v[56:59]\n"
"s_waitcnt vmcnt(0)\n"
"v_max_u32 v38, v104, v105\n"
"v_max3_u32 v38, v106, v107, v38\n"
"ds_read_b128 v[68:71], v12 offset:0\n"
"v_mfma_f32_16x16x32_bf16 v[60:63], v[228:231], v[100:103], v[60:63]\n"
"v_cmp_eq_u32 vcc, -1, v38\n"
"ds_read_b128 v[80:83], v13 offset:0\n"
"v_mfma_f32_16x16x32_bf16 v[64:67], v[232:235], v[100:103], v[64:67]\n"
"s_cbranch_vccnz .Lsl_333\n"
".Lbk_334:\n"
"v_mfma_f32_16x16x32_bf16 v[56:59], v[244:247], v[104:107], v[56:59]\n"
"ds_read_b128 v[72:75], v12 offset:1024\n"
"ds_read_b128 v[84:87], v13 offset:1024\n"
"v_mfma_f32_16x16x32_bf16 v[60:63], v[248:251], v[104:107], v[60:63]\n"
"v_mfma_f32_16x16x32_bf16 v[64:67], v[252:255], v[104:107], v[64:67]\n"
"v_mfma_f32_16x16x32_bf16 v[48:51], v[216:219], v[100:103], v[48:51]\n"
"ds_write_b128 v7, v[56:59] offset:0\n"
"v_mfma_f32_16x16x32_bf16 v[52:55], v[220:223], v[100:103], v[52:55]\n"
"ds_write_b128 v6, v[60:63] offset:0\n"
"v_mfma_f32_16x16x32_bf16 v[48:51], v[236:239], v[104:107], v[48:51]\n"
"ds_write_b128 v6, v[64:67] offset:1024\n"
"v_mfma_f32_16x16x32_bf16 v[52:55], v[240:243], v[104:107], v[52:55]\n"
"ds_write_b32 v4, v4 offset:560\n"
"s_mov_b32 s17, 0x100000\n"
"s_waitcnt lgkmcnt(10)\n"
"v_cmp_eq_u32 vcc, 0, v3\n"
"s_cbranch_vccz .Lok_336\n"
".Ltp_335:\n"
"ds_read_b32 v3, v5 offset:544\n"
"ds_read_b128 v[76:79], v12 offset:2048\n"
"ds_read_b128 v[108:111], v13 offset:2048\n"
"ds_read_b128 v[68:71], v12 offset:0\n"
"ds_read_b128 v[80:83], v13 offset:0\n"
"ds_read_b128 v[72:75], v12 offset:1024\n"
"ds_read_b128 v[84:87], v13 offset:1024\n"
"s_sub_u32 s17, s17, 1\n"
"s_cbranch_scc1 .Lok_336\n"
"s_waitcnt lgkmcnt(6)\n"
"v_cmp_eq_u32 vcc, 0, v3\n"
"s_cbranch_vccnz .Ltp_335\n"
"s_waitcnt lgkmcnt(0)\n"
".Lok_336:\n"
"s_waitcnt lgkmcnt(8)\n"
"v_add_f32 v76, v76, v108\n"
"v_add_f32 v77, v77, v109\n"
"v_add_f32 v78, v78, v110\n"
"v_add_f32 v79, v79, v111\n"
"ds_read_b32 v3, v5 offset:560\n"
"ds_read_b128 v[112:115], v11 offset:2048\n"
"ds_read_b128 v[60:63], v11 offset:0\n"
"ds_read_b128 v[64:67], v11 offset:1024\n"
"s_waitcnt lgkmcnt(10)\n"
"v_add_f32 v68, v68, v80\n"
"v_add_f32 v69, v69, v81\n"
"v_add_f32 v70, v70, v82\n"
"v_add_f32 v71, v71, v83\n"
"s_waitcnt lgkmcnt(8)\n"
"v_add_f32 v72, v72, v84\n"
"v_add_f32 v73, v73, v85\n"
"v_add_f32 v74, v74, v86\n"
"v_add_f32 v75, v75, v87\n"
"v_add_f32 v56, v56, v76\n"
"v_add_f32 v57, v57, v77\n"
"v_add_f32 v58, v58, v78\n"
"v_add_f32 v59, v59, v79\n"
"v_add_f32 v48, v48, v68\n"
"v_add_f32 v49, v49, v69\n"
"v_add_f32 v50, v50, v70\n"
"v_add_f32 v51, v51, v71\n"
"v_add_f32 v52, v52, v72\n"
"v_add_f32 v53, v53, v73\n"
"v_add_f32 v54, v54, v74\n"
"v_add_f32 v55, v55, v75\n"
"s_mov_b32 s17, 0x100000\n"
"s_waitcnt lgkmcnt(3)\n"
"v_cmp_eq_u32 vcc, 0, v3\n"
"s_cbranch_vccz .Lok_338\n"
".Ltp_337:\n"
"ds_read_b32 v3, v5 offset:560\n"
"ds_read_b128 v[112:115], v11 offset:2048\n"
"ds_read_b128 v[60:63], v11 offset:0\n"
"ds_read_b128 v[64:67], v11 offset:1024\n"
"s_sub_u32 s17, s17, 1\n"
"s_cbranch_scc1 .Lok_338\n"
"s_waitcnt lgkmcnt(3)\n"
"v_cmp_eq_u32 vcc, 0, v3\n"
"s_cbranch_vccnz .Ltp_337\n"
"s_waitcnt lgkmcnt(0)\n"
".Lok_338:\n"
"s_waitcnt lgkmcnt(2)\n"
"v_add_f32 v56, v56, v112\n"
"v_add_f32 v57, v57, v113\n"
"v_add_f32 v58, v58, v114\n"
"v_add_f32 v59, v59, v115\n"
"v_cvt_pk_bf16_f32 v44, v56, v57\n"
"v_cvt_pk_bf16_f32 v45, v58, v59\n"
"v_and_b32 v44, v19, v44\n"
"v_and_b32 v45, v19, v45\n"
"s_waitcnt lgkmcnt(1)\n"
"v_add_f32 v48, v48, v60\n"
"v_add_f32 v49, v49, v61\n"
"v_add_f32 v50, v50, v62\n"
"v_add_f32 v51, v51, v63\n"
"v_cvt_pk_bf16_f32 v40, v48, v49\n"
"v_cvt_pk_bf16_f32 v41, v50, v51\n"
"v_mfma_f32_16x16x32_bf16 v[88:91], v[44:47], v[44:47], 0\n"
"v_mfma_f32_16x16x32_bf16 v[92:95], v[20:23], v[44:47], 0\n"
"s_waitcnt lgkmcnt(0)\n"
"v_add_f32 v52, v52, v64\n"
"v_add_f32 v53, v53, v65\n"
"v_add_f32 v54, v54, v66\n"
"v_add_f32 v55, v55, v67\n"
"v_cvt_pk_bf16_f32 v42, v52, v53\n"
"v_cvt_pk_bf16_f32 v43, v54, v55\n"
"v_cvt_pk_bf16_f32 v116, v48, v49\n"
"v_cvt_pk_bf16_f32 v118, v50, v51\n"
"v_mfma_f32_16x16x32_bf16 v[88:91], v[40:43], v[40:43], v[88:91]\n"
"v_mfma_f32_16x16x32_bf16 v[92:95], v[20:23], v[40:43], v[92:95]\n"
"v_cvt_pk_bf16_f32 v120, v52, v53\n"
"v_cvt_pk_bf16_f32 v122, v54, v55\n"
"v_cvt_pk_bf16_f32 v124, v56, v57\n"
"v_cvt_pk_bf16_f32 v126, v58, v59\n"
"s_nop 0\n"
"v_cndmask_b32 v101, v88, v90, s[44:45]\n"
"v_cndmask_b32 v88, v89, v91, s[44:45]\n"
"s_nop 0\n"
"v_min_u32 v100, -2, v92\n"
"v_min_u32 v88, -2, v88\n"
"v_min_u32 v101, -2, v101\n"
"v_mov_b32_dpp v102, v100 quad_perm:[1,0,3,2] row_mask:0xf bank_mask:0xf\n"
"v_mov_b32_dpp v103, v88 quad_perm:[1,0,3,2] row_mask:0xf bank_mask:0xf\n"
"buffer_store_dwordx4 v[100:103], v16, s[4:7], s38 offen sc1\n"
"buffer_store_dwordx4 v[116:119], v17, s[4:7], s37 offen offset:0 sc1\n"
"buffer_store_dwordx4 v[120:123], v17, s[4:7], s37 offen offset:1024 sc1\n"
"buffer_store_dwordx4 v[124:127], v18, s[4:7], s37 offen offset:2048 sc1\n"
"ds_write_b32 v4, v4 offset:160\n"
"s_xor_b32 s79, s3, 1\n"
"s_lshl_b32 s79, s79, 8\n"
"s_add_u32 s32, s19, s79\n"
"s_cmp_ge_u32 s32, 64\n"
"s_cbranch_scc1 .Lnr_339\n"
"s_lshl_b32 s79, s19, 11\n"
"s_add_u32 s32, s79, 0xae8000\n"
"s_lshl_b32 s79, s19, 7\n"
"s_add_u32 s33, s79, 0xb28000\n"
"v_lshrrev_b32 v99, 3, v0\n"
"v_and_b32 v98, 7, v0\n"
"v_lshlrev_b32 v98, 4, v98\n"
"v_lshl_add_u32 v40, v99, 14, v98\n"
"v_mov_b32 v41, 0x3727c5ac\n"
"s_sleep 8\n"
"buffer_load_dwordx4 v[48:51], v2, s[4:7], s32 offen offset:0 sc1\n"
"buffer_load_dwordx4 v[52:55], v2, s[4:7], s32 offen offset:1024 sc1\n"
"s_mov_b32 s17, 32768\n"
".Ltp_340:\n"
"s_waitcnt vmcnt(0)\n"
"s_sub_u32 s17, s17, 1\n"
"s_cbranch_scc1 .Lou_341\n"
"v_max_u32 v38, v48, v49\n"
"v_max3_u32 v38, v50, v51, v38\n"
"v_max3_u32 v38, v52, v53, v38\n"
"v_max3_u32 v38, v54, v55, v38\n"
"v_cmp_eq_u32 vcc, -1, v38\n"
"s_cbranch_vccz .Lou_341\n"
"v_max_u32 v38, v50, v51\n"
"v_max3_u32 v38, v48, v49, v38\n"
"v_cmp_eq_u32 vcc, -1, v38\n"
"s_cbranch_vccz .Lsk_342\n"
"s_mov_b64 exec, vcc\n"
"buffer_load_dwordx4 v[48:51], v2, s[4:7], s32 offen offset:0 sc1\n"
"s_mov_b64 exec, -1\n"
".Lsk_342:\n"
"v_max_u32 v38, v54, v55\n"
"v_max3_u32 v38, v52, v53, v38\n"
"v_cmp_eq_u32 vcc, -1, v38\n"
"s_cbranch_vccz .Lsk_343\n"
"s_mov_b64 exec, vcc\n"
"buffer_load_dwordx4 v[52:55], v2, s[4:7], s32 offen offset:1024 sc1\n"
"s_mov_b64 exec, -1\n"
".Lsk_343:\n"
"s_branch .Ltp_340\n"
".Lou_341:\n"
"s_waitcnt vmcnt(0)\n"
"v_add_f32 v48, v48, v52\n"
"v_add_f32 v49, v49, v53\n"
"v_add_f32 v50, v50, v54\n"
"v_add_f32 v51, v51, v55\n"
"v_add_f32_dpp v52, v48, v48 row_ror:8 row_mask:0xf bank_mask:0xf\n"
"v_add_f32_dpp v53, v49, v49 row_ror:8 row_mask:0xf bank_mask:0xf\n"
"v_add_f32_dpp v54, v50, v50 row_ror:8 row_mask:0xf bank_mask:0xf\n"
"v_add_f32_dpp v55, v51, v51 row_ror:8 row_mask:0xf bank_mask:0xf\n"
"v_mov_b32 v48, v52\n"
"v_mov_b32 v49, v53\n"
"v_mov_b32 v50, v54\n"
"v_mov_b32 v51, v55\n"
"v_permlane16_swap_b32 v48, v52\n"
"v_permlane16_swap_b32 v49, v53\n"
"v_permlane16_swap_b32 v50, v54\n"
"v_permlane16_swap_b32 v51, v55\n"
"v_add_f32 v52, v48, v52\n"
"v_add_f32 v53, v49, v53\n"
"v_add_f32 v54, v50, v54\n"
"v_add_f32 v55, v51, v55\n"
"v_mov_b32 v48, v52\n"
"v_mov_b32 v49, v53\n"
"v_mov_b32 v50, v54\n"
"v_mov_b32 v51, v55\n"
"v_permlane32_swap_b32 v48, v52\n"
"v_permlane32_swap_b32 v49, v53\n"
"v_permlane32_swap_b32 v50, v54\n"
"v_permlane32_swap_b32 v51, v55\n"
"v_add_f32 v52, v48, v52\n"
"v_add_f32 v53, v49, v53\n"
"v_add_f32 v54, v50, v54\n"
"v_add_f32 v55, v51, v55\n"
"v_mul_f32 v48, 0x3acccccd, v52\n"
"v_mul_f32 v50, 0x3acccccd, v54\n"
"v_fma_f32 v53, -v52, v48, v53\n"
"v_fma_f32 v55, -v54, v50, v55\n"
"v_fmamk_f32 v53, v53, 0x3acccccd, v41\n"
"v_fmamk_f32 v55, v55, 0x3acccccd, v41\n"
"v_max_f32 v53, v53, v41\n"
"v_max_f32 v55, v55, v41\n"
"v_rsq_f32 v49, v53\n"
"v_rsq_f32 v51, v55\n"
"v_mul_f32 v48, -v48, v49\n"
"v_mul_f32 v50, -v50, v51\n"
"v_min_u32 v49, -2, v49\n"
"v_min_u32 v51, -2, v51\n"
"v_min_u32 v48, -2, v48\n"
"v_min_u32 v50, -2, v50\n"
"buffer_store_dwordx4 v[48:51], v40, s[4:7], s33 offen sc1\n"
"s_nop 3\n"
".Lnr_339:\n"
"v_lshlrev_b32 v99, 4, v0\n"
"v_mov_b32 v98, 0x40000000\n"
"v_cmp_gt_i32 vcc, 16, v0\n"
"s_nop 1\n"
"v_cndmask_b32 v43, v98, v99, vcc\n"
"v_mov_b32 v44, -1\n"
"v_mov_b32 v45, -1\n"
"v_mov_b32 v46, -1\n"
"v_mov_b32 v47, -1\n"
"s_add_u32 s49, s31, 0xa60000\n"
"buffer_store_dwordx4 v[44:47], v2, s[4:7], s49 offen offset:0 sc1\n"
"buffer_store_dwordx4 v[44:47], v43, s[4:7], s49 offen offset:1024 sc1\n"
"s_add_u32 s49, s31, 0xba0000\n"
"buffer_store_dwordx4 v[44:47], v2, s[4:7], s49 offen offset:0 sc1\n"
"buffer_store_dwordx4 v[44:47], v43, s[4:7], s49 offen offset:1024 sc1\n"
"s_lshl_b32 s79, s2, 2\n"
"s_add_u32 s79, s79, s3\n"
"s_lshl_b32 s79, s79, 10\n"
"s_add_u32 s49, s79, 0x1548000\n"
"s_sub_u32 s79, 0x15a7fff, s49\n"
"s_and_b32 s79, s79, 0x80000000\n"
"s_or_b32 s49, s49, s79\n"
"buffer_store_dwordx4 v[44:47], v2, s[4:7], s49 offen sc1\n"
"s_nop 1\n"
"s_setprio 0\n"
"s_cmp_lg_u32 s43, 0\n"
"s_cbranch_scc1 .Lsk_345\n"
"s_call_b64 s[46:47], .Lst_288\n"
".Lsk_345:\n"
"s_cmp_lg_u32 s43, 1\n"
"s_cbranch_scc1 .Lsk_346\n"
"s_call_b64 s[46:47], .Lst_289\n"
".Lsk_346:\n"
"s_cmp_lg_u32 s43, 2\n"
"s_cbranch_scc1 .Lsk_347\n"
"ds_read_b32 v3, v5 offset:128\n"
"s_waitcnt lgkmcnt(0)\n"
"v_cmp_eq_u32 vcc, 0, v3\n"
"s_cbranch_vccz .Len_344\n"
"s_call_b64 s[46:47], .Lst_290\n"
".Lsk_347:\n"
".Len_344:\n"
"s_setprio 2\n"
"s_mul_i32 s79, s28, 0xa0\n"
"s_add_u32 s79, s30, s79\n"
"s_and_b32 s78, s3, 1\n"
"s_mul_i32 s78, s78, 0x60\n"
"s_add_u32 s79, s79, s78\n"
"s_add_u32 s36, s79, 0x140000\n"
"s_mul_i32 s79, s3, 0x1400\n"
"s_add_u32 s79, s30, s79\n"
"s_add_u32 s32, s79, 0x4a8000\n"
"s_add_u32 s33, s32, 0x1000\n"
"s_lshl_b32 s79, s29, 1\n"
"s_lshr_b32 s78, s3, 1\n"
"s_xor_b32 s78, s78, 1\n"
"s_add_u32 s79, s79, s78\n"
"s_mul_i32 s79, s79, 0x5000\n"
"s_mul_i32 s78, s3, 0x1400\n"
"s_add_u32 s79, s79, s78\n"
"s_add_u32 s34, s79, 0x4a8000\n"
"s_add_u32 s35, s34, 0x1000\n"
"s_lshl_b32 s79, s30, 1\n"
"s_mul_i32 s78, s28, 0x1400\n"
"s_add_u32 s79, s79, s78\n"
"s_and_b32 s78, s3, 1\n"
"s_mul_i32 s78, s78, 0xc00\n"
"s_add_u32 s79, s79, s78\n"
"s_add_u32 s37, s79, 0x868000\n"
"s_lshl_b32 s79, s29, 1\n"
"s_lshr_b32 s78, s3, 1\n"
"s_add_u32 s79, s79, s78\n"
"s_lshl_b32 s79, s79, 11\n"
"s_lshl_b32 s78, s28, 8\n"
"s_add_u32 s79, s79, s78\n"
"s_and_b32 s78, s3, 1\n"
"s_lshl_b32 s78, s78, 7\n"
"s_add_u32 s79, s79, s78\n"
"s_add_u32 s38, s79, 0xb08000\n"
"buffer_load_dwordx2 v[40:41], v14, s[8:11], s36 offen offset:0\n"
"buffer_load_dwordx2 v[42:43], v14, s[8:11], s36 offen offset:32\n"
"buffer_load_dwordx2 v[44:45], v15, s[8:11], s36 offen offset:64\n"
"s_mov_b32 s17, 0x100000\n"
".Ltp_348:\n"
"ds_read_b32 v3, v5 offset:160\n"
"s_sub_u32 s17, s17, 1\n"
"s_cbranch_scc1 .Lou_349\n"
"s_waitcnt lgkmcnt(0)\n"
"v_cmp_eq_u32 vcc, 0, v3\n"
"s_cbranch_vccnz .Ltp_348\n"
".Lou_349:\n"
"s_waitcnt lgkmcnt(0)\n"
"s_mov_b32 s17, 0x100000\n"
".Ltp_350:\n"
"s_sleep 1\n"
"ds_read_b32 v3, v5 offset:144\n"
"s_sub_u32 s17, s17, 1\n"
"s_cbranch_scc1 .Lou_351\n"
"s_waitcnt lgkmcnt(0)\n"
"v_cmp_eq_u32 vcc, 0, v3\n"
"s_cbranch_vccnz .Ltp_350\n"
".Lou_351:\n"
"s_waitcnt lgkmcnt(0)\n"
"s_sleep 6\n"
"s_cmp_eq_u32 s17, 0xfffff\n"
"s_cbranch_scc1 .Lsk_352\n"
"s_sleep 14\n"
".Lsk_352:\n"
"buffer_load_dwordx4 v[108:111], v2, s[4:7], s34 offen offset:0 sc1\n"
"s_nop 6\n"
"buffer_load_dwordx4 v[112:115], v2, s[4:7], s34 offen offset:1024 sc1\n"
"s_nop 6\n"
"buffer_load_dwordx4 v[116:119], v2, s[4:7], s34 offen offset:2048 sc1\n"
"s_nop 6\n"
"buffer_load_dwordx4 v[120:123], v2, s[4:7], s34 offen offset:3072 sc1\n"
"s_nop 6\n"
"buffer_load_dwordx4 v[124:127], v2, s[4:7], s35 offen offset:0 sc1\n"
"s_nop 6\n"
"buffer_load_dwordx4 v[88:91], v2, s[4:7], s32 offen offset:0 sc1\n"
"s_nop 6\n"
"buffer_load_dwordx4 v[92:95], v2, s[4:7], s32 offen offset:1024 sc1\n"
"s_nop 6\n"
"buffer_load_dwordx4 v[96:99], v2, s[4:7], s32 offen offset:2048 sc1\n"
"s_nop 6\n"
"buffer_load_dwordx4 v[100:103], v2, s[4:7], s32 offen offset:3072 sc1\n"
"s_nop 6\n"
"buffer_load_dwordx4 v[104:107], v2, s[4:7], s33 offen offset:0 sc1\n"
"s_waitcnt vmcnt(12)\n"
"v_lshlrev_b32 v48, 16, v40\n"
"v_and_b32 v49, 0xffff0000, v40\n"
"v_lshlrev_b32 v50, 16, v41\n"
"v_and_b32 v51, 0xffff0000, v41\n"
"s_waitcnt vmcnt(11)\n"
"v_lshlrev_b32 v52, 16, v42\n"
"v_and_b32 v53, 0xffff0000, v42\n"
"v_lshlrev_b32 v54, 16, v43\n"
"v_and_b32 v55, 0xffff0000, v43\n"
"s_waitcnt vmcnt(10)\n"
"v_lshlrev_b32 v56, 16, v44\n"
"v_and_b32 v57, 0xffff0000, v44\n"
"v_lshlrev_b32 v58, 16, v45\n"
"v_and_b32 v59, 0xffff0000, v45\n"
"v_mov_b32 v46, 0\n"
"v_mov_b32 v47, 0\n"
"s_waitcnt vmcnt(9)\n"
"v_max_u32 v38, v108, v109\n"
"v_max3_u32 v38, v110, v111, v38\n"
"v_cmp_eq_u32 vcc, -1, v38\n"
"s_cbranch_vccnz .Lsl_353\n"
".Lbk_354:\n"
"v_mfma_f32_16x16x32_bf16 v[68:71], v[156:159], v[108:111], 0\n"
"v_mfma_f32_16x16x32_bf16 v[72:75], v[160:163], v[108:111], 0\n"
"v_mfma_f32_16x16x32_bf16 v[76:79], v[164:167], v[108:111], 0\n"
"s_waitcnt vmcnt(8)\n"
"v_max_u32 v38, v112, v113\n"
"v_max3_u32 v38, v114, v115, v38\n"
"v_mfma_f32_16x16x32_bf16 v[80:83], v[168:171], v[108:111], 0\n"
"v_cmp_eq_u32 vcc, -1, v38\n"
"v_mfma_f32_16x16x32_bf16 v[84:87], v[172:175], v[108:111], 0\n"
"s_cbranch_vccnz .Lsl_355\n"
".Lbk_356:\n"
"v_mfma_f32_16x16x32_bf16 v[68:71], v[176:179], v[112:115], v[68:71]\n"
"v_mfma_f32_16x16x32_bf16 v[72:75], v[180:183], v[112:115], v[72:75]\n"
"v_mfma_f32_16x16x32_bf16 v[76:79], v[184:187], v[112:115], v[76:79]\n"
"s_waitcnt vmcnt(7)\n"
"v_max_u32 v38, v116, v117\n"
"v_max3_u32 v38, v118, v119, v38\n"
"v_mfma_f32_16x16x32_bf16 v[80:83], v[188:191], v[112:115], v[80:83]\n"
"v_cmp_eq_u32 vcc, -1, v38\n"
"v_mfma_f32_16x16x32_bf16 v[84:87], v[192:195], v[112:115], v[84:87]\n"
"s_cbranch_vccnz .Lsl_357\n"
".Lbk_358:\n"
"v_mfma_f32_16x16x32_bf16 v[68:71], v[196:199], v[116:119], v[68:71]\n"
"v_mfma_f32_16x16x32_bf16 v[72:75], v[200:203], v[116:119], v[72:75]\n"
"v_mfma_f32_16x16x32_bf16 v[76:79], v[204:207], v[116:119], v[76:79]\n"
"s_waitcnt vmcnt(6)\n"
"v_max_u32 v38, v120, v121\n"
"v_max3_u32 v38, v122, v123, v38\n"
"v_mfma_f32_16x16x32_bf16 v[80:83], v[208:211], v[116:119], v[80:83]\n"
"v_cmp_eq_u32 vcc, -1, v38\n"
"v_mfma_f32_16x16x32_bf16 v[84:87], v[212:215], v[116:119], v[84:87]\n"
"s_cbranch_vccnz .Lsl_359\n"
".Lbk_360:\n"
"v_mfma_f32_16x16x32_bf16 v[68:71], v[216:219], v[120:123], v[68:71]\n"
"v_mfma_f32_16x16x32_bf16 v[72:75], v[220:223], v[120:123], v[72:75]\n"
"v_mfma_f32_16x16x32_bf16 v[76:79], v[224:227], v[120:123], v[76:79]\n"
"s_waitcnt vmcnt(5)\n"
"v_max_u32 v38, v124, v125\n"
"v_max3_u32 v38, v126, v127, v38\n"
"v_mfma_f32_16x16x32_bf16 v[80:83], v[228:231], v[120:123], v[80:83]\n"
"v_cmp_eq_u32 vcc, -1, v38\n"
"v_mfma_f32_16x16x32_bf16 v[84:87], v[232:235], v[120:123], v[84:87]\n"
"s_cbranch_vccnz .Lsl_361\n"
".Lbk_362:\n"
"v_mfma_f32_16x16x32_bf16 v[68:71], v[236:239], v[124:127], v[68:71]\n"
"v_mfma_f32_16x16x32_bf16 v[72:75], v[240:243], v[124:127], v[72:75]\n"
"v_mfma_f32_16x16x32_bf16 v[76:79], v[244:247], v[124:127], v[76:79]\n"
"s_waitcnt vmcnt(4)\n"
"v_max_u32 v38, v88, v89\n"
"v_max3_u32 v38, v90, v91, v38\n"
"v_mfma_f32_16x16x32_bf16 v[80:83], v[248:251], v[124:127], v[80:83]\n"
"v_cmp_eq_u32 vcc, -1, v38\n"
"v_mfma_f32_16x16x32_bf16 v[84:87], v[252:255], v[124:127], v[84:87]\n"
"ds_write_b128 v8, v[68:71] offset:0\n"
"s_cbranch_vccnz .Lsl_363\n"
".Lbk_364:\n"
"v_mfma_f32_16x16x32_bf16 v[48:51], v[156:159], v[88:91], v[48:51]\n"
"ds_write_b128 v8, v[72:75] offset:1024\n"
"v_mfma_f32_16x16x32_bf16 v[52:55], v[160:163], v[88:91], v[52:55]\n"
"ds_write_b128 v10, v[76:79] offset:0\n"
"v_mfma_f32_16x16x32_bf16 v[56:59], v[164:167], v[88:91], v[56:59]\n"
"s_waitcnt vmcnt(3)\n"
"v_max_u32 v38, v92, v93\n"
"v_max3_u32 v38, v94, v95, v38\n"
"v_mfma_f32_16x16x32_bf16 v[60:63], v[168:171], v[88:91], 0\n"
"v_cmp_eq_u32 vcc, -1, v38\n"
"v_mov_b32 v117, s16\n"
"v_mfma_f32_16x16x32_bf16 v[64:67], v[172:175], v[88:91], 0\n"
"ds_write_b128 v9, v[80:83] offset:0\n"
"s_cbranch_vccnz .Lsl_365\n"
".Lbk_366:\n"
"v_mfma_f32_16x16x32_bf16 v[48:51], v[176:179], v[92:95], v[48:51]\n"
"ds_write_b128 v9, v[84:87] offset:1024\n"
"v_mfma_f32_16x16x32_bf16 v[52:55], v[180:183], v[92:95], v[52:55]\n"
"ds_write_b32 v4, v4 offset:576\n"
"v_mfma_f32_16x16x32_bf16 v[56:59], v[184:187], v[92:95], v[56:59]\n"
"s_waitcnt vmcnt(2)\n"
"v_max_u32 v38, v96, v97\n"
"v_max3_u32 v38, v98, v99, v38\n"
"v_mfma_f32_16x16x32_bf16 v[60:63], v[188:191], v[92:95], v[60:63]\n"
"v_cmp_eq_u32 vcc, -1, v38\n"
"v_mov_b32 v119, s16\n"
"v_mfma_f32_16x16x32_bf16 v[64:67], v[192:195], v[92:95], v[64:67]\n"
"s_cbranch_vccnz .Lsl_367\n"
".Lbk_368:\n"
"v_mfma_f32_16x16x32_bf16 v[48:51], v[196:199], v[96:99], v[48:51]\n"
"v_mov_b32 v121, s16\n"
"v_mov_b32 v123, s16\n"
"v_mfma_f32_16x16x32_bf16 v[52:55], v[200:203], v[96:99], v[52:55]\n"
"v_mov_b32 v125, s16\n"
"v_mov_b32 v127, s16\n"
"v_mfma_f32_16x16x32_bf16 v[56:59], v[204:207], v[96:99], v[56:59]\n"
"s_waitcnt vmcnt(1)\n"
"v_max_u32 v38, v100, v101\n"
"v_max3_u32 v38, v102, v103, v38\n"
"v_mfma_f32_16x16x32_bf16 v[60:63], v[208:211], v[96:99], v[60:63]\n"
"v_cmp_eq_u32 vcc, -1, v38\n"
"ds_read_b32 v3, v5 offset:576\n"
"ds_read_b128 v[76:79], v12 offset:2048\n"
"v_mfma_f32_16x16x32_bf16 v[64:67], v[212:215], v[96:99], v[64:67]\n"
"ds_read_b128 v[108:111], v13 offset:2048\n"
"s_cbranch_vccnz .Lsl_369\n"
".Lbk_370:\n"
"v_mfma_f32_16x16x32_bf16 v[56:59], v[224:227], v[100:103], v[56:59]\n"
"s_waitcnt vmcnt(0)\n"
"v_max_u32 v38, v104, v105\n"
"v_max3_u32 v38, v106, v107, v38\n"
"ds_read_b128 v[68:71], v12 offset:0\n"
"v_mfma_f32_16x16x32_bf16 v[60:63], v[228:231], v[100:103], v[60:63]\n"
"v_cmp_eq_u32 vcc, -1, v38\n"
"ds_read_b128 v[80:83], v13 offset:0\n"
"v_mfma_f32_16x16x32_bf16 v[64:67], v[232:235], v[100:103], v[64:67]\n"
"s_cbranch_vccnz .Lsl_371\n"
".Lbk_372:\n"
"v_mfma_f32_16x16x32_bf16 v[56:59], v[244:247], v[104:107], v[56:59]\n"
"ds_read_b128 v[72:75], v12 offset:1024\n"
"ds_read_b128 v[84:87], v13 offset:1024\n"
"v_mfma_f32_16x16x32_bf16 v[60:63], v[248:251], v[104:107], v[60:63]\n"
"v_mfma_f32_16x16x32_bf16 v[64:67], v[252:255], v[104:107], v[64:67]\n"
"v_mfma_f32_16x16x32_bf16 v[48:51], v[216:219], v[100:103], v[48:51]\n"
"ds_write_b128 v7, v[56:59] offset:0\n"
"v_mfma_f32_16x16x32_bf16 v[52:55], v[220:223], v[100:103], v[52:55]\n"
"ds_write_b128 v6, v[60:63] offset:0\n"
"v_mfma_f32_16x16x32_bf16 v[48:51], v[236:239], v[104:107], v[48:51]\n"
"ds_write_b128 v6, v[64:67] offset:1024\n"
"v_mfma_f32_16x16x32_bf16 v[52:55], v[240:243], v[104:107], v[52:55]\n"
"ds_write_b32 v4, v4 offset:592\n"
"s_mov_b32 s17, 0x100000\n"
"s_waitcnt lgkmcnt(10)\n"
"v_cmp_eq_u32 vcc, 0, v3\n"
"s_cbranch_vccz .Lok_374\n"
".Ltp_373:\n"
"ds_read_b32 v3, v5 offset:576\n"
"ds_read_b128 v[76:79], v12 offset:2048\n"
"ds_read_b128 v[108:111], v13 offset:2048\n"
"ds_read_b128 v[68:71], v12 offset:0\n"
"ds_read_b128 v[80:83], v13 offset:0\n"
"ds_read_b128 v[72:75], v12 offset:1024\n"
"ds_read_b128 v[84:87], v13 offset:1024\n"
"s_sub_u32 s17, s17, 1\n"
"s_cbranch_scc1 .Lok_374\n"
"s_waitcnt lgkmcnt(6)\n"
"v_cmp_eq_u32 vcc, 0, v3\n"
"s_cbranch_vccnz .Ltp_373\n"
"s_waitcnt lgkmcnt(0)\n"
".Lok_374:\n"
"s_waitcnt lgkmcnt(8)\n"
"v_add_f32 v76, v76, v108\n"
"v_add_f32 v77, v77, v109\n"
"v_add_f32 v78, v78, v110\n"
"v_add_f32 v79, v79, v111\n"
"ds_read_b32 v3, v5 offset:592\n"
"ds_read_b128 v[112:115], v11 offset:2048\n"
"ds_read_b128 v[60:63], v11 offset:0\n"
"ds_read_b128 v[64:67], v11 offset:1024\n"
"s_waitcnt lgkmcnt(10)\n"
"v_add_f32 v68, v68, v80\n"
"v_add_f32 v69, v69, v81\n"
"v_add_f32 v70, v70, v82\n"
"v_add_f32 v71, v71, v83\n"
"s_waitcnt lgkmcnt(8)\n"
"v_add_f32 v72, v72, v84\n"
"v_add_f32 v73, v73, v85\n"
"v_add_f32 v74, v74, v86\n"
"v_add_f32 v75, v75, v87\n"
"v_add_f32 v56, v56, v76\n"
"v_add_f32 v57, v57, v77\n"
"v_add_f32 v58, v58, v78\n"
"v_add_f32 v59, v59, v79\n"
"v_add_f32 v48, v48, v68\n"
"v_add_f32 v49, v49, v69\n"
"v_add_f32 v50, v50, v70\n"
"v_add_f32 v51, v51, v71\n"
"v_add_f32 v52, v52, v72\n"
"v_add_f32 v53, v53, v73\n"
"v_add_f32 v54, v54, v74\n"
"v_add_f32 v55, v55, v75\n"
"s_mov_b32 s17, 0x100000\n"
"s_waitcnt lgkmcnt(3)\n"
"v_cmp_eq_u32 vcc, 0, v3\n"
"s_cbranch_vccz .Lok_376\n"
".Ltp_375:\n"
"ds_read_b32 v3, v5 offset:592\n"
"ds_read_b128 v[112:115], v11 offset:2048\n"
"ds_read_b128 v[60:63], v11 offset:0\n"
"ds_read_b128 v[64:67], v11 offset:1024\n"
"s_sub_u32 s17, s17, 1\n"
"s_cbranch_scc1 .Lok_376\n"
"s_waitcnt lgkmcnt(3)\n"
"v_cmp_eq_u32 vcc, 0, v3\n"
"s_cbranch_vccnz .Ltp_375\n"
"s_waitcnt lgkmcnt(0)\n"
".Lok_376:\n"
"s_waitcnt lgkmcnt(2)\n"
"v_add_f32 v56, v56, v112\n"
"v_add_f32 v57, v57, v113\n"
"v_add_f32 v58, v58, v114\n"
"v_add_f32 v59, v59, v115\n"
"v_cvt_pk_bf16_f32 v44, v56, v57\n"
"v_cvt_pk_bf16_f32 v45, v58, v59\n"
"v_and_b32 v44, v19, v44\n"
"v_and_b32 v45, v19, v45\n"
"s_waitcnt lgkmcnt(1)\n"
"v_add_f32 v48, v48, v60\n"
"v_add_f32 v49, v49, v61\n"
"v_add_f32 v50, v50, v62\n"
"v_add_f32 v51, v51, v63\n"
"v_cvt_pk_bf16_f32 v40, v48, v49\n"
"v_cvt_pk_bf16_f32 v41, v50, v51\n"
"v_mfma_f32_16x16x32_bf16 v[88:91], v[44:47], v[44:47], 0\n"
"v_mfma_f32_16x16x32_bf16 v[92:95], v[20:23], v[44:47], 0\n"
"s_waitcnt lgkmcnt(0)\n"
"v_add_f32 v52, v52, v64\n"
"v_add_f32 v53, v53, v65\n"
"v_add_f32 v54, v54, v66\n"
"v_add_f32 v55, v55, v67\n"
"v_cvt_pk_bf16_f32 v42, v52, v53\n"
"v_cvt_pk_bf16_f32 v43, v54, v55\n"
"v_cvt_pk_bf16_f32 v116, v48, v49\n"
"v_cvt_pk_bf16_f32 v118, v50, v51\n"
"v_mfma_f32_16x16x32_bf16 v[88:91], v[40:43], v[40:43], v[88:91]\n"
"v_mfma_f32_16x16x32_bf16 v[92:95], v[20:23], v[40:43], v[92:95]\n"
"v_cvt_pk_bf16_f32 v120, v52, v53\n"
"v_cvt_pk_bf16_f32 v122, v54, v55\n"
"v_cvt_pk_bf16_f32 v124, v56, v57\n"
"v_cvt_pk_bf16_f32 v126, v58, v59\n"
"s_nop 0\n"
"v_cndmask_b32 v101, v88, v90, s[44:45]\n"
"v_cndmask_b32 v88, v89, v91, s[44:45]\n"
"s_nop 0\n"
"v_min_u32 v100, -2, v92\n"
"v_min_u32 v88, -2, v88\n"
"v_min_u32 v101, -2, v101\n"
"v_mov_b32_dpp v102, v100 quad_perm:[1,0,3,2] row_mask:0xf bank_mask:0xf\n"
"v_mov_b32_dpp v103, v88 quad_perm:[1,0,3,2] row_mask:0xf bank_mask:0xf\n"
"buffer_store_dwordx4 v[100:103], v16, s[4:7], s38 offen sc1\n"
"buffer_store_dwordx4 v[116:119], v17, s[4:7], s37 offen offset:0 sc1\n"
"buffer_store_dwordx4 v[120:123], v17, s[4:7], s37 offen offset:1024 sc1\n"
"buffer_store_dwordx4 v[124:127], v18, s[4:7], s37 offen offset:2048 sc1\n"
"ds_write_b32 v4, v4 offset:336\n"
"s_xor_b32 s79, s3, 1\n"
"s_lshl_b32 s79, s79, 8\n"
"s_add_u32 s32, s19, s79\n"
"s_cmp_ge_u32 s32, 64\n"
"s_cbranch_scc1 .Lnr_377\n"
"s_lshl_b32 s79, s19, 11\n"
"s_add_u32 s32, s79, 0xb08000\n"
"s_lshl_b32 s79, s19, 7\n"
"s_add_u32 s33, s79, 0xb2a000\n"
"v_lshrrev_b32 v99, 3, v0\n"
"v_and_b32 v98, 7, v0\n"
"v_lshlrev_b32 v98, 4, v98\n"
"v_lshl_add_u32 v40, v99, 14, v98\n"
"v_mov_b32 v41, 0x3727c5ac\n"
"s_sleep 8\n"
"buffer_load_dwordx4 v[48:51], v2, s[4:7], s32 offen offset:0 sc1\n"
"buffer_load_dwordx4 v[52:55], v2, s[4:7], s32 offen offset:1024 sc1\n"
"s_mov_b32 s17, 32768\n"
".Ltp_378:\n"
"s_waitcnt vmcnt(0)\n"
"s_sub_u32 s17, s17, 1\n"
"s_cbranch_scc1 .Lou_379\n"
"v_max_u32 v38, v48, v49\n"
"v_max3_u32 v38, v50, v51, v38\n"
"v_max3_u32 v38, v52, v53, v38\n"
"v_max3_u32 v38, v54, v55, v38\n"
"v_cmp_eq_u32 vcc, -1, v38\n"
"s_cbranch_vccz .Lou_379\n"
"v_max_u32 v38, v50, v51\n"
"v_max3_u32 v38, v48, v49, v38\n"
"v_cmp_eq_u32 vcc, -1, v38\n"
"s_cbranch_vccz .Lsk_380\n"
"s_mov_b64 exec, vcc\n"
"buffer_load_dwordx4 v[48:51], v2, s[4:7], s32 offen offset:0 sc1\n"
"s_mov_b64 exec, -1\n"
".Lsk_380:\n"
"v_max_u32 v38, v54, v55\n"
"v_max3_u32 v38, v52, v53, v38\n"
"v_cmp_eq_u32 vcc, -1, v38\n"
"s_cbranch_vccz .Lsk_381\n"
"s_mov_b64 exec, vcc\n"
"buffer_load_dwordx4 v[52:55], v2, s[4:7], s32 offen offset:1024 sc1\n"
"s_mov_b64 exec, -1\n"
".Lsk_381:\n"
"s_branch .Ltp_378\n"
".Lou_379:\n"
"s_waitcnt vmcnt(0)\n"
"v_add_f32 v48, v48, v52\n"
"v_add_f32 v49, v49, v53\n"
"v_add_f32 v50, v50, v54\n"
"v_add_f32 v51, v51, v55\n"
"v_add_f32_dpp v52, v48, v48 row_ror:8 row_mask:0xf bank_mask:0xf\n"
"v_add_f32_dpp v53, v49, v49 row_ror:8 row_mask:0xf bank_mask:0xf\n"
"v_add_f32_dpp v54, v50, v50 row_ror:8 row_mask:0xf bank_mask:0xf\n"
"v_add_f32_dpp v55, v51, v51 row_ror:8 row_mask:0xf bank_mask:0xf\n"
"v_mov_b32 v48, v52\n"
"v_mov_b32 v49, v53\n"
"v_mov_b32 v50, v54\n"
"v_mov_b32 v51, v55\n"
"v_permlane16_swap_b32 v48, v52\n"
"v_permlane16_swap_b32 v49, v53\n"
"v_permlane16_swap_b32 v50, v54\n"
"v_permlane16_swap_b32 v51, v55\n"
"v_add_f32 v52, v48, v52\n"
"v_add_f32 v53, v49, v53\n"
"v_add_f32 v54, v50, v54\n"
"v_add_f32 v55, v51, v55\n"
"v_mov_b32 v48, v52\n"
"v_mov_b32 v49, v53\n"
"v_mov_b32 v50, v54\n"
"v_mov_b32 v51, v55\n"
"v_permlane32_swap_b32 v48, v52\n"
"v_permlane32_swap_b32 v49, v53\n"
"v_permlane32_swap_b32 v50, v54\n"
"v_permlane32_swap_b32 v51, v55\n"
"v_add_f32 v52, v48, v52\n"
"v_add_f32 v53, v49, v53\n"
"v_add_f32 v54, v50, v54\n"
"v_add_f32 v55, v51, v55\n"
"v_mul_f32 v48, 0x3acccccd, v52\n"
"v_mul_f32 v50, 0x3acccccd, v54\n"
"v_fma_f32 v53, -v52, v48, v53\n"
"v_fma_f32 v55, -v54, v50, v55\n"
"v_fmamk_f32 v53, v53, 0x3acccccd, v41\n"
"v_fmamk_f32 v55, v55, 0x3acccccd, v41\n"
"v_max_f32 v53, v53, v41\n"
"v_max_f32 v55, v55, v41\n"
"v_rsq_f32 v49, v53\n"
"v_rsq_f32 v51, v55\n"
"v_mul_f32 v48, -v48, v49\n"
"v_mul_f32 v50, -v50, v51\n"
"v_min_u32 v49, -2, v49\n"
"v_min_u32 v51, -2, v51\n"
"v_min_u32 v48, -2, v48\n"
"v_min_u32 v50, -2, v50\n"
"buffer_store_dwordx4 v[48:51], v40, s[4:7], s33 offen sc1\n"
"s_nop 3\n"
".Lnr_377:\n"
"s_cmp_lt_i32 s18, 0\n"
"s_cbranch_scc1 .Lnc_382\n"
"s_nop 0\n"
"s_setprio 0\n"
"s_cmp_lg_u32 s43, 0\n"
"s_cbranch_scc1 .Lsk_384\n"
"s_call_b64 s[46:47], .Lst_288\n"
".Lsk_384:\n"
"s_cmp_lg_u32 s43, 1\n"
"s_cbranch_scc1 .Lsk_385\n"
"s_call_b64 s[46:47], .Lst_289\n"
".Lsk_385:\n"
"s_cmp_lg_u32 s43, 2\n"
"s_cbranch_scc1 .Lsk_386\n"
"s_call_b64 s[46:47], .Lst_290\n"
".Lsk_386:\n"
".Len_383:\n"
"s_setprio 2\n"
"s_mov_b32 s17, 0x100000\n"
".Ltp_387:\n"
"s_sleep 1\n"
"ds_read_b32 v3, v5 offset:192\n"
"s_sub_u32 s17, s17, 1\n"
"s_cbranch_scc1 .Lou_388\n"
"s_waitcnt lgkmcnt(0)\n"
"v_cmp_eq_u32 vcc, 0, v3\n"
"s_cbranch_vccnz .Ltp_387\n"
".Lou_388:\n"
"s_waitcnt lgkmcnt(0)\n"
"s_setprio 0\n"
"s_lshl_b32 s79, s18, 8\n"
"s_add_u32 s32, s79, 0x868000\n"
"s_add_u32 s33, s32, 0x140000\n"
"s_and_b32 s79, s2, 7\n"
"s_lshl_b32 s79, s79, 14\n"
"s_add_u32 s34, s79, 0xb2a000\n"
"s_add_u32 s35, s34, 0x1000\n"
"v_and_b32 v126, 0xffffff80, v29\n"
"v_add_u32 v126, v126, v29\n"
"ds_read_b128 v[88:91], v25 offset:448\n"
"ds_read_b128 v[92:95], v25 offset:432\n"
"ds_read_b64 v[96:97], v24 offset:448\n"
"ds_read_b64 v[98:99], v24 offset:456\n"
"ds_read_b128 v[100:103], v25 offset:960\n"
"ds_read_b128 v[104:107], v25 offset:1472\n"
"s_waitcnt lgkmcnt(4)\n"
"v_mfma_f32_16x16x32_f16 v[64:67], v[88:91], v[144:147], 0\n"
"ds_read_b128 v[40:43], v25 offset:1984\n"
"ds_read_b64 v[44:45], v24 offset:960\n"
"v_mfma_f32_16x16x32_f16 v[68:71], v[92:95], v[148:151], 0\n"
"ds_read_b64 v[46:47], v24 offset:968\n"
"ds_read_b64 v[72:73], v24 offset:1472\n"
"v_mfma_f32_16x16x32_f16 v[48:51], v[152:155], v[88:91], 0\n"
"ds_read_b64 v[74:75], v24 offset:1480\n"
"s_waitcnt lgkmcnt(4)\n"
"v_mfma_f32_16x16x32_f16 v[48:51], v[96:99], v[100:103], v[48:51]\n"
"ds_read_b64 v[76:77], v24 offset:384\n"
"ds_read_b64 v[78:79], v24 offset:392\n"
"v_mfma_f32_16x16x32_f16 v[52:55], v[96:99], v[104:107], 0\n"
"ds_read_b128 v[84:87], v25 offset:896\n"
"ds_read_b128 v[92:95], v25 offset:1408\n"
"v_mfma_f32_16x16x32_f16 v[56:59], v[96:99], v[40:43], 0\n"
"ds_read_b128 v[88:91], v25 offset:1920\n"
"s_waitcnt lgkmcnt(5)\n"
"v_mfma_f32_16x16x32_f16 v[52:55], v[44:47], v[40:43], v[52:55]\n"
"ds_read_b64 v[100:101], v24 offset:896\n"
"ds_read_b64 v[102:103], v24 offset:904\n"
"v_mfma_f32_16x16x32_f16 v[48:51], v[44:47], v[104:107], v[48:51]\n"
"ds_read_b64 v[96:97], v24 offset:1408\n"
"v_cvt_pk_f16_f32 v80, v64, v65\n"
"v_mfma_f32_16x16x32_f16 v[48:51], v[72:75], v[40:43], v[48:51]\n"
"ds_read_b64 v[98:99], v24 offset:1416\n"
"s_waitcnt lgkmcnt(4)\n"
"v_mfma_f32_16x16x32_f16 v[48:51], v[76:79], v[84:87], v[48:51]\n"
"ds_read_b128 v[104:107], v25 offset:960\n"
"v_cvt_pk_f16_f32 v81, v66, v67\n"
"v_mfma_f32_16x16x32_f16 v[52:55], v[76:79], v[92:95], v[52:55]\n"
"ds_read_b128 v[44:47], v25 offset:944\n"
"v_cvt_pk_f16_f32 v82, v68, v69\n"
"v_mfma_f32_16x16x32_f16 v[56:59], v[76:79], v[88:91], v[56:59]\n"
"v_cvt_pk_f16_f32 v83, v70, v71\n"
"s_waitcnt lgkmcnt(2)\n"
"v_mfma_f32_16x16x32_f16 v[52:55], v[100:103], v[88:91], v[52:55]\n"
"ds_read_b64 v[40:41], v24 offset:320\n"
"ds_read_b64 v[42:43], v24 offset:328\n"
"v_mfma_f32_16x16x32_f16 v[48:51], v[100:103], v[92:95], v[48:51]\n"
"ds_read_b128 v[72:75], v25 offset:832\n"
"v_mfma_f32_16x16x32_f16 v[48:51], v[96:99], v[88:91], v[48:51]\n"
"ds_read_b128 v[84:87], v25 offset:1344\n"
"v_mfma_f32_16x16x32_f16 v[48:51], v[80:83], v[128:131], v[48:51]\n"
"ds_read_b128 v[76:79], v25 offset:1856\n"
"ds_read_b64 v[92:93], v24 offset:832\n"
"v_mfma_f32_16x16x32_f16 v[52:55], v[80:83], v[132:135], v[52:55]\n"
"ds_read_b64 v[94:95], v24 offset:840\n"
"v_mfma_f32_16x16x32_f16 v[56:59], v[80:83], v[136:139], v[56:59]\n"
"ds_read_b64 v[100:101], v24 offset:1344\n"
"s_waitcnt lgkmcnt(8)\n"
"v_mfma_f32_16x16x32_f16 v[60:63], v[80:83], v[140:143], 0\n"
"ds_read_b64 v[102:103], v24 offset:1352\n"
"ds_read_b64 v[88:89], v24 offset:256\n"
"v_mfma_f32_16x16x32_f16 v[64:67], v[104:107], v[144:147], 0\n"
"ds_read_b64 v[90:91], v24 offset:264\n"
"v_mfma_f32_16x16x32_f16 v[68:71], v[44:47], v[148:151], 0\n"
"ds_read_b128 v[96:99], v25 offset:768\n"
"s_waitcnt lgkmcnt(8)\n"
"v_mfma_f32_16x16x32_f16 v[52:55], v[152:155], v[104:107], v[52:55]\n"
"ds_read_b128 v[44:47], v25 offset:1280\n"
"v_mfma_f32_16x16x32_f16 v[48:51], v[40:43], v[72:75], v[48:51]\n"
"ds_read_b128 v[104:107], v25 offset:1792\n"
"ds_read_b64 v[72:73], v24 offset:768\n"
"v_mfma_f32_16x16x32_f16 v[52:55], v[40:43], v[84:87], v[52:55]\n"
"ds_read_b64 v[74:75], v24 offset:776\n"
"s_waitcnt lgkmcnt(9)\n"
"v_mfma_f32_16x16x32_f16 v[56:59], v[40:43], v[76:79], v[56:59]\n"
"ds_read_b64 v[40:41], v24 offset:1280\n"
"v_mfma_f32_16x16x32_f16 v[52:55], v[92:95], v[76:79], v[52:55]\n"
"ds_read_b64 v[42:43], v24 offset:1288\n"
"v_cvt_pk_f16_f32 v80, v64, v65\n"
"v_mfma_f32_16x16x32_f16 v[48:51], v[92:95], v[84:87], v[48:51]\n"
"v_cvt_pk_f16_f32 v81, v66, v67\n"
"s_waitcnt lgkmcnt(5)\n"
"v_mfma_f32_16x16x32_f16 v[48:51], v[100:103], v[76:79], v[48:51]\n"
"ds_read_b128 v[84:87], v25 offset:1472\n"
"v_cvt_pk_f16_f32 v82, v68, v69\n"
"v_mfma_f32_16x16x32_f16 v[48:51], v[88:91], v[96:99], v[48:51]\n"
"ds_read_b128 v[92:95], v25 offset:1456\n"
"v_cvt_pk_f16_f32 v83, v70, v71\n"
"v_mfma_f32_16x16x32_f16 v[52:55], v[88:91], v[44:47], v[52:55]\n"
"s_waitcnt lgkmcnt(4)\n"
"v_mfma_f32_16x16x32_f16 v[56:59], v[88:91], v[104:107], v[56:59]\n"
"ds_read_b64 v[76:77], v24 offset:192\n"
"ds_read_b64 v[78:79], v24 offset:200\n"
"v_mfma_f32_16x16x32_f16 v[52:55], v[72:75], v[104:107], v[52:55]\n"
"ds_read_b128 v[100:103], v25 offset:704\n"
"v_mfma_f32_16x16x32_f16 v[48:51], v[72:75], v[44:47], v[48:51]\n"
"ds_read_b128 v[96:99], v25 offset:1216\n"
"s_waitcnt lgkmcnt(6)\n"
"v_mfma_f32_16x16x32_f16 v[48:51], v[40:43], v[104:107], v[48:51]\n"
"ds_read_b128 v[88:91], v25 offset:1728\n"
"ds_read_b64 v[44:45], v24 offset:704\n"
"v_mfma_f32_16x16x32_f16 v[52:55], v[80:83], v[128:131], v[52:55]\n"
"ds_read_b64 v[46:47], v24 offset:712\n"
"v_mfma_f32_16x16x32_f16 v[56:59], v[80:83], v[132:135], v[56:59]\n"
"ds_read_b64 v[72:73], v24 offset:1216\n"
"s_waitcnt lgkmcnt(8)\n"
"v_mfma_f32_16x16x32_f16 v[60:63], v[80:83], v[136:139], v[60:63]\n"
"ds_read_b64 v[74:75], v24 offset:1224\n"
"ds_read_b64 v[104:105], v24 offset:128\n"
"v_mfma_f32_16x16x32_f16 v[64:67], v[84:87], v[144:147], 0\n"
"ds_read_b64 v[106:107], v24 offset:136\n"
"v_mfma_f32_16x16x32_f16 v[68:71], v[92:95], v[148:151], 0\n"
"ds_read_b128 v[40:43], v25 offset:640\n"
"s_waitcnt lgkmcnt(8)\n"
"v_mfma_f32_16x16x32_f16 v[56:59], v[152:155], v[84:87], v[56:59]\n"
"ds_read_b128 v[92:95], v25 offset:1152\n"
"v_mfma_f32_16x16x32_f16 v[48:51], v[76:79], v[100:103], v[48:51]\n"
"ds_read_b128 v[84:87], v25 offset:1664\n"
"ds_read_b64 v[100:101], v24 offset:640\n"
"v_mfma_f32_16x16x32_f16 v[52:55], v[76:79], v[96:99], v[52:55]\n"
"ds_read_b64 v[102:103], v24 offset:648\n"
"s_waitcnt lgkmcnt(9)\n"
"v_mfma_f32_16x16x32_f16 v[56:59], v[76:79], v[88:91], v[56:59]\n"
"ds_read_b64 v[76:77], v24 offset:1152\n"
"v_mfma_f32_16x16x32_f16 v[52:55], v[44:47], v[88:91], v[52:55]\n"
"ds_read_b64 v[78:79], v24 offset:1160\n"
"v_cvt_pk_f16_f32 v80, v64, v65\n"
"v_mfma_f32_16x16x32_f16 v[48:51], v[44:47], v[96:99], v[48:51]\n"
"ds_read_b128 v[96:99], v25 offset:1984\n"
"s_waitcnt lgkmcnt(6)\n"
"v_mfma_f32_16x16x32_f16 v[48:51], v[72:75], v[88:91], v[48:51]\n"
"ds_read_b128 v[44:47], v25 offset:1968\n"
"v_cvt_pk_f16_f32 v81, v66, v67\n"
"v_mfma_f32_16x16x32_f16 v[48:51], v[104:107], v[40:43], v[48:51]\n"
"v_cvt_pk_f16_f32 v82, v68, v69\n"
"v_cvt_pk_f16_f32 v83, v70, v71\n"
"v_mfma_f32_16x16x32_f16 v[52:55], v[104:107], v[92:95], v[52:55]\n"
"ds_read_b64 v[88:89], v24 offset:64\n"
"s_waitcnt lgkmcnt(5)\n"
"v_mfma_f32_16x16x32_f16 v[56:59], v[104:107], v[84:87], v[56:59]\n"
"ds_read_b64 v[90:91], v24 offset:72\n"
"ds_read_b128 v[72:75], v25 offset:576\n"
"v_mfma_f32_16x16x32_f16 v[52:55], v[100:103], v[84:87], v[52:55]\n"
"ds_read_b128 v[40:43], v25 offset:1088\n"
"v_mfma_f32_16x16x32_f16 v[48:51], v[100:103], v[92:95], v[48:51]\n"
"ds_read_b128 v[104:107], v25 offset:1600\n"
"s_waitcnt lgkmcnt(7)\n"
"v_mfma_f32_16x16x32_f16 v[48:51], v[76:79], v[84:87], v[48:51]\n"
"ds_read_b64 v[92:93], v24 offset:576\n"
"v_mfma_f32_16x16x32_f16 v[56:59], v[80:83], v[128:131], v[56:59]\n"
"ds_read_b64 v[94:95], v24 offset:584\n"
"ds_read_b64 v[100:101], v24 offset:1088\n"
"v_mfma_f32_16x16x32_f16 v[60:63], v[80:83], v[132:135], v[60:63]\n"
"ds_read_b64 v[102:103], v24 offset:1096\n"
"s_waitcnt lgkmcnt(9)\n"
"v_mfma_f32_16x16x32_f16 v[64:67], v[96:99], v[144:147], 0\n"
"ds_read_b64 v[84:85], v24 offset:0\n"
"ds_read_b64 v[86:87], v24 offset:8\n"
"v_mfma_f32_16x16x32_f16 v[68:71], v[44:47], v[148:151], 0\n"
"ds_read_b128 v[76:79], v25 offset:512\n"
"v_mfma_f32_16x16x32_f16 v[60:63], v[152:155], v[96:99], v[60:63]\n"
"ds_read_b128 v[44:47], v25 offset:1024\n"
"s_waitcnt lgkmcnt(8)\n"
"v_mfma_f32_16x16x32_f16 v[48:51], v[88:91], v[72:75], v[48:51]\n"
"ds_read_b128 v[96:99], v25 offset:1536\n"
"ds_read_b64 v[72:73], v24 offset:512\n"
"v_mfma_f32_16x16x32_f16 v[52:55], v[88:91], v[40:43], v[52:55]\n"
"ds_read_b64 v[74:75], v24 offset:520\n"
"v_mfma_f32_16x16x32_f16 v[56:59], v[88:91], v[104:107], v[56:59]\n"
"ds_read_b64 v[88:89], v24 offset:1024\n"
"s_waitcnt lgkmcnt(8)\n"
"v_mfma_f32_16x16x32_f16 v[52:55], v[92:95], v[104:107], v[52:55]\n"
"ds_read_b64 v[90:91], v24 offset:1032\n"
"v_cvt_pk_f16_f32 v80, v64, v65\n"
"v_mfma_f32_16x16x32_f16 v[48:51], v[92:95], v[40:43], v[48:51]\n"
"v_cvt_pk_f16_f32 v81, v66, v67\n"
"v_cvt_pk_f16_f32 v82, v68, v69\n"
"v_mfma_f32_16x16x32_f16 v[48:51], v[100:103], v[104:107], v[48:51]\n"
"v_cvt_pk_f16_f32 v83, v70, v71\n"
"s_waitcnt lgkmcnt(4)\n"
"v_mfma_f32_16x16x32_f16 v[48:51], v[84:87], v[76:79], v[48:51]\n"
"v_mfma_f32_16x16x32_f16 v[52:55], v[84:87], v[44:47], v[52:55]\n"
"v_mfma_f32_16x16x32_f16 v[56:59], v[84:87], v[96:99], v[56:59]\n"
"s_waitcnt lgkmcnt(0)\n"
"v_mfma_f32_16x16x32_f16 v[52:55], v[72:75], v[96:99], v[52:55]\n"
"v_mfma_f32_16x16x32_f16 v[48:51], v[72:75], v[44:47], v[48:51]\n"
"v_mfma_f32_16x16x32_f16 v[48:51], v[88:91], v[96:99], v[48:51]\n"
"v_mfma_f32_16x16x32_f16 v[60:63], v[80:83], v[128:131], v[60:63]\n"
"s_setprio 2\n"
"s_mov_b32 s17, 0x100000\n"
".Ltp_389:\n"
"ds_read_b32 v3, v5 offset:336\n"
"s_sub_u32 s17, s17, 1\n"
"s_cbranch_scc1 .Lou_390\n"
"s_waitcnt lgkmcnt(0)\n"
"v_cmp_eq_u32 vcc, 0, v3\n"
"s_cbranch_vccnz .Ltp_389\n"
".Lou_390:\n"
"s_waitcnt lgkmcnt(0)\n"
"buffer_load_dwordx4 v[110:113], v126, s[4:7], s32 offen offset:0 sc1\n"
"buffer_load_dwordx4 v[114:117], v126, s[4:7], s32 offen offset:128 sc1\n"
"buffer_load_dwordx4 v[118:121], v126, s[4:7], s33 offen offset:0 sc1\n"
"buffer_load_dwordx4 v[122:125], v126, s[4:7], s33 offen offset:128 sc1\n"
"s_mov_b32 s38, 0x05040100\n"
"s_mov_b32 s39, 0x07060302\n"
"s_mov_b32 s17, 32768\n"
".Ltp_391:\n"
"s_waitcnt vmcnt(0)\n"
"s_sub_u32 s17, s17, 1\n"
"s_cbranch_scc1 .Lou_392\n"
"v_bitop3_b32 v64, v111, v113, s16 bitop3:0x7e\n"
"v_bitop3_b32 v65, v115, v117, s16 bitop3:0x7e\n"
"v_bitop3_b32 v66, v119, v121, s16 bitop3:0x7e\n"
"v_bitop3_b32 v67, v123, v125, s16 bitop3:0x7e\n"
"v_or3_b32 v68, v64, v65, v66\n"
"v_or_b32 v68, v68, v67\n"
"v_cmp_ne_u32 vcc, 0, v68\n"
"s_cbranch_vccz .Lou_392\n"
"s_sleep 2\n"
"v_cmp_ne_u32 vcc, 0, v64\n"
"s_cbranch_vccz .Lsk_393\n"
"s_mov_b64 exec, vcc\n"
"buffer_load_dwordx4 v[110:113], v126, s[4:7], s32 offen offset:0 sc1\n"
"s_mov_b64 exec, -1\n"
".Lsk_393:\n"
"v_cmp_ne_u32 vcc, 0, v65\n"
"s_cbranch_vccz .Lsk_394\n"
"s_mov_b64 exec, vcc\n"
"buffer_load_dwordx4 v[114:117], v126, s[4:7], s32 offen offset:128 sc1\n"
"s_mov_b64 exec, -1\n"
".Lsk_394:\n"
"v_cmp_ne_u32 vcc, 0, v66\n"
"s_cbranch_vccz .Lsk_395\n"
"s_mov_b64 exec, vcc\n"
"buffer_load_dwordx4 v[118:121], v126, s[4:7], s33 offen offset:0 sc1\n"
"s_mov_b64 exec, -1\n"
".Lsk_395:\n"
"v_cmp_ne_u32 vcc, 0, v67\n"
"s_cbranch_vccz .Lsk_396\n"
"s_mov_b64 exec, vcc\n"
"buffer_load_dwordx4 v[122:125], v126, s[4:7], s33 offen offset:128 sc1\n"
"s_mov_b64 exec, -1\n"
".Lsk_396:\n"
"s_branch .Ltp_391\n"
".Lou_392:\n"
"s_waitcnt vmcnt(0)\n"
"buffer_load_dwordx4 v[88:91], v30, s[4:7], s34 offen sc1\n"
"buffer_load_dwordx4 v[92:95], v30, s[4:7], s35 offen sc1\n"
"v_perm_b32 v64, v114, v110, s38\n"
"v_perm_b32 v65, v114, v110, s39\n"
"v_perm_b32 v66, v116, v112, s38\n"
"v_perm_b32 v67, v116, v112, s39\n"
"ds_write_b32 v32, v64 offset:0\n"
"ds_write_b32 v33, v65 offset:2048\n"
"ds_write_b32 v32, v66 offset:4224\n"
"ds_write_b32 v33, v67 offset:6272\n"
"v_perm_b32 v64, v122, v118, s38\n"
"v_perm_b32 v65, v122, v118, s39\n"
"v_perm_b32 v66, v124, v120, s38\n"
"v_perm_b32 v67, v124, v120, s39\n"
"ds_write_b32 v32, v64 offset:1024\n"
"ds_write_b32 v33, v65 offset:3072\n"
"ds_write_b32 v32, v66 offset:5248\n"
"ds_write_b32 v33, v67 offset:7296\n"
"v_lshlrev_b32 v64, 16, v110\n"
"v_lshlrev_b32 v65, 16, v114\n"
"v_and_b32 v66, 0xffff0000, v110\n"
"v_and_b32 v67, 0xffff0000, v114\n"
"v_lshlrev_b32 v68, 16, v112\n"
"v_lshlrev_b32 v69, 16, v116\n"
"v_and_b32 v70, 0xffff0000, v112\n"
"v_and_b32 v71, 0xffff0000, v116\n"
"v_lshlrev_b32 v72, 16, v118\n"
"v_lshlrev_b32 v73, 16, v122\n"
"v_and_b32 v74, 0xffff0000, v118\n"
"v_and_b32 v75, 0xffff0000, v122\n"
"v_lshlrev_b32 v76, 16, v120\n"
"v_lshlrev_b32 v77, 16, v124\n"
"v_and_b32 v78, 0xffff0000, v120\n"
"v_and_b32 v79, 0xffff0000, v124\n"
"s_mov_b32 s17, 32768\n"
".Ltp_397:\n"
"s_waitcnt vmcnt(0)\n"
"s_sub_u32 s17, s17, 1\n"
"s_cbranch_scc1 .Lou_398\n"
"v_max_u32 v110, v88, v89\n"
"v_max3_u32 v110, v90, v91, v110\n"
"v_max3_u32 v110, v92, v93, v110\n"
"v_max3_u32 v110, v94, v95, v110\n"
"v_cmp_eq_u32 vcc, -1, v110\n"
"s_cbranch_vccz .Lou_398\n"
"v_max_u32 v110, v90, v91\n"
"v_max3_u32 v110, v88, v89, v110\n"
"v_cmp_eq_u32 vcc, -1, v110\n"
"s_cbranch_vccz .Lsk_399\n"
"s_mov_b64 exec, vcc\n"
"buffer_load_dwordx4 v[88:91], v30, s[4:7], s34 offen sc1\n"
"s_mov_b64 exec, -1\n"
".Lsk_399:\n"
"v_max_u32 v110, v94, v95\n"
"v_max3_u32 v110, v92, v93, v110\n"
"v_cmp_eq_u32 vcc, -1, v110\n"
"s_cbranch_vccz .Lsk_400\n"
"s_mov_b64 exec, vcc\n"
"buffer_load_dwordx4 v[92:95], v30, s[4:7], s35 offen sc1\n"
"s_mov_b64 exec, -1\n"
".Lsk_400:\n"
"s_branch .Ltp_397\n"
".Lou_398:\n"
"s_waitcnt vmcnt(0)\n"
"v_fma_f32 v64, v64, v89, v88\n"
"v_fma_f32 v65, v65, v91, v90\n"
"v_cvt_pk_f16_f32 v64, v64, v65\n"
"v_fma_f32 v66, v66, v89, v88\n"
"v_fma_f32 v67, v67, v91, v90\n"
"v_cvt_pk_f16_f32 v66, v66, v67\n"
"v_fma_f32 v68, v68, v89, v88\n"
"v_fma_f32 v69, v69, v91, v90\n"
"v_cvt_pk_f16_f32 v68, v68, v69\n"
"v_fma_f32 v70, v70, v89, v88\n"
"v_fma_f32 v71, v71, v91, v90\n"
"v_cvt_pk_f16_f32 v70, v70, v71\n"
"ds_write2st64_b32 v31, v64, v66 offset0:0 offset1:18\n"
"ds_write2st64_b32 v31, v68, v70 offset0:36 offset1:54\n"
"v_fma_f32 v72, v72, v93, v92\n"
"v_fma_f32 v73, v73, v95, v94\n"
"v_cvt_pk_f16_f32 v72, v72, v73\n"
"v_fma_f32 v74, v74, v93, v92\n"
"v_fma_f32 v75, v75, v95, v94\n"
"v_cvt_pk_f16_f32 v74, v74, v75\n"
"v_fma_f32 v76, v76, v93, v92\n"
"v_fma_f32 v77, v77, v95, v94\n"
"v_cvt_pk_f16_f32 v76, v76, v77\n"
"v_fma_f32 v78, v78, v93, v92\n"
"v_fma_f32 v79, v79, v95, v94\n"
"v_cvt_pk_f16_f32 v78, v78, v79\n"
"ds_write2st64_b32 v31, v72, v74 offset0:4 offset1:22\n"
"ds_write2st64_b32 v31, v76, v78 offset0:40 offset1:58\n"
"ds_write_b32 v4, v4 offset:608\n"
"s_mov_b32 s17, 0x100000\n"
".Ltp_401:\n"
"ds_read_b32 v3, v5 offset:608\n"
"s_sub_u32 s17, s17, 1\n"
"s_cbranch_scc1 .Lou_402\n"
"s_waitcnt lgkmcnt(0)\n"
"v_cmp_eq_u32 vcc, 0, v3\n"
"s_cbranch_vccnz .Ltp_401\n"
".Lou_402:\n"
"s_waitcnt lgkmcnt(0)\n"
"s_mov_b32 s36, 0xfffefffe\n"
"ds_read_b64 v[88:89], v24 offset:1984\n"
"ds_read_b64 v[90:91], v24 offset:1992\n"
"ds_read_b128 v[92:95], v25 offset:4032\n"
"ds_read_b128 v[96:99], v25 offset:3520\n"
"ds_read_b64 v[100:101], v24 offset:1472\n"
"ds_read_b64 v[102:103], v24 offset:1480\n"
"ds_read_b128 v[104:107], v25 offset:3008\n"
"s_waitcnt lgkmcnt(1)\n"
"v_mfma_f32_16x16x32_f16 v[60:63], v[88:91], v[92:95], v[60:63]\n"
"ds_read_b64 v[108:109], v24 offset:960\n"
"ds_read_b64 v[110:111], v24 offset:968\n"
"v_mfma_f32_16x16x32_f16 v[56:59], v[88:91], v[96:99], v[56:59]\n"
"ds_read_b128 v[112:115], v25 offset:2496\n"
"ds_read_b64 v[116:117], v24 offset:448\n"
"v_mfma_f32_16x16x32_f16 v[60:63], v[100:103], v[96:99], v[60:63]\n"
"ds_read_b64 v[118:119], v24 offset:456\n"
"s_waitcnt lgkmcnt(3)\n"
"v_mfma_f32_16x16x32_f16 v[52:55], v[88:91], v[104:107], v[52:55]\n"
"ds_read_b64 v[120:121], v24 offset:1920\n"
"ds_read_b64 v[122:123], v24 offset:1928\n"
"v_mfma_f32_16x16x32_f16 v[56:59], v[100:103], v[104:107], v[56:59]\n"
"ds_read_b128 v[124:127], v25 offset:3968\n"
"ds_read_b128 v[40:43], v25 offset:3456\n"
"v_mfma_f32_16x16x32_f16 v[60:63], v[108:111], v[104:107], v[60:63]\n"
"ds_read_b64 v[44:45], v24 offset:1408\n"
"s_waitcnt lgkmcnt(7)\n"
"v_mfma_f32_16x16x32_f16 v[48:51], v[88:91], v[112:115], v[48:51]\n"
"ds_read_b64 v[46:47], v24 offset:1416\n"
"ds_read_b128 v[92:95], v25 offset:2944\n"
"v_mfma_f32_16x16x32_f16 v[52:55], v[100:103], v[112:115], v[52:55]\n"
"ds_read_b64 v[96:97], v24 offset:896\n"
"ds_read_b64 v[98:99], v24 offset:904\n"
"v_mfma_f32_16x16x32_f16 v[56:59], v[108:111], v[112:115], v[56:59]\n"
"ds_read_b128 v[104:107], v25 offset:2432\n"
"s_waitcnt lgkmcnt(6)\n"
"v_mfma_f32_16x16x32_f16 v[60:63], v[116:119], v[112:115], v[60:63]\n"
"ds_read_b64 v[88:89], v24 offset:384\n"
"v_mfma_f32_16x16x32_f16 v[60:63], v[120:123], v[124:127], v[60:63]\n"
"ds_read_b64 v[90:91], v24 offset:392\n"
"ds_read_b64 v[100:101], v24 offset:1856\n"
"v_mfma_f32_16x16x32_f16 v[56:59], v[120:123], v[40:43], v[56:59]\n"
"ds_read_b64 v[102:103], v24 offset:1864\n"
"s_waitcnt lgkmcnt(7)\n"
"v_mfma_f32_16x16x32_f16 v[60:63], v[44:47], v[40:43], v[60:63]\n"
"ds_read_b128 v[108:111], v25 offset:3904\n"
"ds_read_b128 v[112:115], v25 offset:3392\n"
"v_mfma_f32_16x16x32_f16 v[52:55], v[120:123], v[92:95], v[52:55]\n"
"ds_read_b64 v[116:117], v26 offset:0\n"
"ds_read_b64 v[124:125], v24 offset:1344\n"
"v_mfma_f32_16x16x32_f16 v[56:59], v[44:47], v[92:95], v[56:59]\n"
"ds_read_b64 v[126:127], v24 offset:1352\n"
"s_waitcnt lgkmcnt(9)\n"
"v_mfma_f32_16x16x32_f16 v[60:63], v[96:99], v[92:95], v[60:63]\n"
"ds_read_b128 v[40:43], v25 offset:2880\n"
"v_mfma_f32_16x16x32_f16 v[48:51], v[120:123], v[104:107], v[48:51]\n"
"ds_read_b64 v[92:93], v24 offset:832\n"
"v_mfma_f32_16x16x32_f16 v[52:55], v[44:47], v[104:107], v[52:55]\n"
"ds_read_b64 v[94:95], v24 offset:840\n"
"s_waitcnt lgkmcnt(7)\n"
"v_mfma_f32_16x16x32_f16 v[56:59], v[96:99], v[104:107], v[56:59]\n"
"ds_read_b128 v[120:123], v25 offset:2368\n"
"v_mfma_f32_16x16x32_f16 v[60:63], v[88:91], v[104:107], v[60:63]\n"
"v_mfma_f32_16x16x32_f16 v[60:63], v[100:103], v[108:111], v[60:63]\n"
"ds_read_b64 v[44:45], v24 offset:320\n"
"s_waitcnt lgkmcnt(5)\n"
"v_mfma_f32_16x16x32_f16 v[56:59], v[100:103], v[112:115], v[56:59]\n"
"ds_read_b64 v[46:47], v24 offset:328\n"
"s_and_b32 s79, s18, 0xfc\n"
"v_mfma_f32_16x16x16_bf16 v[48:51], v[34:35], v[116:117], v[48:51]\n"
"ds_read_b64 v[96:97], v26 offset:512\n"
"ds_read_b64 v[104:105], v24 offset:1792\n"
"v_mfma_f32_16x16x32_f16 v[60:63], v[124:127], v[112:115], v[60:63]\n"
"ds_read_b64 v[106:107], v24 offset:1800\n"
"s_waitcnt lgkmcnt(6)\n"
"v_mfma_f32_16x16x32_f16 v[52:55], v[100:103], v[40:43], v[52:55]\n"
"ds_read_b128 v[88:91], v25 offset:3840\n"
"ds_read_b128 v[108:111], v25 offset:3328\n"
"v_mfma_f32_16x16x32_f16 v[56:59], v[124:127], v[40:43], v[56:59]\n"
"ds_read_b64 v[116:117], v24 offset:1280\n"
"ds_read_b64 v[118:119], v24 offset:1288\n"
"v_mfma_f32_16x16x32_f16 v[60:63], v[92:95], v[40:43], v[60:63]\n"
"ds_read_b128 v[112:115], v25 offset:2816\n"
"s_waitcnt lgkmcnt(10)\n"
"v_mfma_f32_16x16x32_f16 v[48:51], v[100:103], v[120:123], v[48:51]\n"
"ds_read_b64 v[40:41], v24 offset:768\n"
"s_and_b32 s78, s18, 1\n"
"v_mfma_f32_16x16x32_f16 v[52:55], v[124:127], v[120:123], v[52:55]\n"
"ds_read_b64 v[42:43], v24 offset:776\n"
"s_lshl_b32 s78, s78, 1\n"
"v_mfma_f32_16x16x32_f16 v[56:59], v[92:95], v[120:123], v[56:59]\n"
"ds_read_b128 v[100:103], v25 offset:2304\n"
"s_waitcnt lgkmcnt(7)\n"
"v_mfma_f32_16x16x32_f16 v[60:63], v[44:47], v[120:123], v[60:63]\n"
"s_add_u32 s79, s79, s78\n"
"s_lshr_b32 s78, s18, 1\n"
"v_mfma_f32_16x16x16_bf16 v[52:55], v[34:35], v[96:97], v[52:55]\n"
"ds_read_b64 v[124:125], v24 offset:256\n"
"s_and_b32 s78, s78, 1\n"
"v_mfma_f32_16x16x32_f16 v[60:63], v[104:107], v[88:91], v[60:63]\n"
"ds_read_b64 v[126:127], v24 offset:264\n"
"s_waitcnt lgkmcnt(5)\n"
"v_mfma_f32_16x16x32_f16 v[56:59], v[104:107], v[108:111], v[56:59]\n"
"ds_read_b64 v[92:93], v24 offset:1728\n"
"ds_read_b64 v[94:95], v24 offset:1736\n"
"v_mfma_f32_16x16x32_f16 v[60:63], v[116:119], v[108:111], v[60:63]\n"
"ds_read_b128 v[120:123], v25 offset:3776\n"
"ds_read_b128 v[44:47], v25 offset:3264\n"
"v_mfma_f32_16x16x32_f16 v[52:55], v[104:107], v[112:115], v[52:55]\n"
"ds_read_b64 v[96:97], v24 offset:1216\n"
"s_waitcnt lgkmcnt(7)\n"
"v_mfma_f32_16x16x32_f16 v[56:59], v[116:119], v[112:115], v[56:59]\n"
"ds_read_b64 v[98:99], v24 offset:1224\n"
"ds_read_b128 v[88:91], v25 offset:2752\n"
"v_mfma_f32_16x16x32_f16 v[60:63], v[40:43], v[112:115], v[60:63]\n"
"ds_read_b64 v[108:109], v26 offset:1024\n"
"s_add_u32 s79, s79, s78\n"
"v_mfma_f32_16x16x32_f16 v[48:51], v[104:107], v[100:103], v[48:51]\n"
"ds_read_b64 v[112:113], v24 offset:704\n"
"s_waitcnt lgkmcnt(9)\n"
"v_mfma_f32_16x16x32_f16 v[52:55], v[116:119], v[100:103], v[52:55]\n"
"ds_read_b64 v[114:115], v24 offset:712\n"
"s_lshl_b32 s79, s79, 7\n"
"v_mfma_f32_16x16x32_f16 v[56:59], v[40:43], v[100:103], v[56:59]\n"
"ds_read_b128 v[104:107], v25 offset:2240\n"
"s_add_u32 s32, s79, 0xc88000\n"
"v_mfma_f32_16x16x32_f16 v[60:63], v[124:127], v[100:103], v[60:63]\n"
"s_waitcnt lgkmcnt(5)\n"
"v_mfma_f32_16x16x32_f16 v[60:63], v[92:95], v[120:123], v[60:63]\n"
"ds_read_b64 v[116:117], v24 offset:192\n"
"v_mfma_f32_16x16x32_f16 v[56:59], v[92:95], v[44:47], v[56:59]\n"
"ds_read_b64 v[118:119], v24 offset:200\n"
"ds_read_b64 v[40:41], v24 offset:1664\n"
"v_mfma_f32_16x16x32_f16 v[60:63], v[96:99], v[44:47], v[60:63]\n"
"ds_read_b64 v[42:43], v24 offset:1672\n"
"s_waitcnt lgkmcnt(7)\n"
"v_mfma_f32_16x16x32_f16 v[52:55], v[92:95], v[88:91], v[52:55]\n"
"ds_read_b128 v[100:103], v25 offset:3712\n"
"ds_read_b128 v[124:127], v25 offset:3200\n"
"v_mfma_f32_16x16x16_bf16 v[56:59], v[34:35], v[108:109], v[56:59]\n"
"ds_read_b64 v[120:121], v24 offset:1152\n"
"ds_read_b64 v[122:123], v24 offset:1160\n"
"s_nop 3\n"
"v_mfma_f32_16x16x32_f16 v[56:59], v[96:99], v[88:91], v[56:59]\n"
"ds_read_b128 v[44:47], v25 offset:2688\n"
"s_waitcnt lgkmcnt(9)\n"
"v_mfma_f32_16x16x32_f16 v[60:63], v[112:115], v[88:91], v[60:63]\n"
"ds_read_b64 v[108:109], v24 offset:640\n"
"v_mfma_f32_16x16x32_f16 v[48:51], v[92:95], v[104:107], v[48:51]\n"
"ds_read_b64 v[110:111], v24 offset:648\n"
"v_mfma_f32_16x16x32_f16 v[52:55], v[96:99], v[104:107], v[52:55]\n"
"ds_read_b128 v[88:91], v25 offset:2176\n"
"s_waitcnt lgkmcnt(7)\n"
"v_mfma_f32_16x16x32_f16 v[56:59], v[112:115], v[104:107], v[56:59]\n"
"ds_read_b64 v[92:93], v26 offset:1536\n"
"v_mfma_f32_16x16x32_f16 v[60:63], v[116:119], v[104:107], v[60:63]\n"
"v_mfma_f32_16x16x32_f16 v[60:63], v[40:43], v[100:103], v[60:63]\n"
"ds_read_b64 v[96:97], v24 offset:128\n"
"s_waitcnt lgkmcnt(5)\n"
"v_mfma_f32_16x16x32_f16 v[56:59], v[40:43], v[124:127], v[56:59]\n"
"ds_read_b64 v[98:99], v24 offset:136\n"
"ds_read_b64 v[112:113], v24 offset:1600\n"
"v_mfma_f32_16x16x32_f16 v[60:63], v[120:123], v[124:127], v[60:63]\n"
"ds_read_b64 v[114:115], v24 offset:1608\n"
"ds_read_b128 v[104:107], v25 offset:3648\n"
"v_mfma_f32_16x16x32_f16 v[52:55], v[40:43], v[44:47], v[52:55]\n"
"ds_read_b128 v[116:119], v25 offset:3136\n"
"s_waitcnt lgkmcnt(7)\n"
"v_mfma_f32_16x16x32_f16 v[56:59], v[120:123], v[44:47], v[56:59]\n"
"ds_read_b64 v[100:101], v24 offset:1088\n"
"ds_read_b64 v[102:103], v24 offset:1096\n"
"v_mfma_f32_16x16x32_f16 v[60:63], v[108:111], v[44:47], v[60:63]\n"
"ds_read_b128 v[124:127], v25 offset:2624\n"
"v_mfma_f32_16x16x32_f16 v[48:51], v[40:43], v[88:91], v[48:51]\n"
"ds_read_b64 v[44:45], v24 offset:576\n"
"s_waitcnt lgkmcnt(10)\n"
"s_nop 0\n"
"v_mfma_f32_16x16x16_bf16 v[60:63], v[34:35], v[92:93], v[60:63]\n"
"ds_read_b64 v[46:47], v24 offset:584\n"
"v_mfma_f32_16x16x32_f16 v[52:55], v[120:123], v[88:91], v[52:55]\n"
"ds_read_b128 v[40:43], v25 offset:2112\n"
"v_mfma_f32_16x16x32_f16 v[56:59], v[108:111], v[88:91], v[56:59]\n"
"s_waitcnt lgkmcnt(6)\n"
"v_mfma_f32_16x16x32_f16 v[60:63], v[96:99], v[88:91], v[60:63]\n"
"ds_read_b64 v[92:93], v24 offset:64\n"
"v_mfma_f32_16x16x32_f16 v[60:63], v[112:115], v[104:107], v[60:63]\n"
"ds_read_b64 v[94:95], v24 offset:72\n"
"ds_read_b64 v[120:121], v24 offset:1536\n"
"v_mfma_f32_16x16x32_f16 v[56:59], v[112:115], v[116:119], v[56:59]\n"
"ds_read_b64 v[122:123], v24 offset:1544\n"
"s_waitcnt lgkmcnt(7)\n"
"v_mfma_f32_16x16x32_f16 v[60:63], v[100:103], v[116:119], v[60:63]\n"
"ds_read_b128 v[108:111], v25 offset:2048\n"
"ds_read_b128 v[88:91], v25 offset:2560\n"
"v_mfma_f32_16x16x32_f16 v[52:55], v[112:115], v[124:127], v[52:55]\n"
"ds_read_b64 v[96:97], v24 offset:1024\n"
"ds_read_b64 v[98:99], v24 offset:1032\n"
"v_mfma_f32_16x16x32_f16 v[56:59], v[100:103], v[124:127], v[56:59]\n"
"ds_read_b128 v[104:107], v25 offset:3072\n"
"s_waitcnt lgkmcnt(9)\n"
"v_mfma_f32_16x16x32_f16 v[60:63], v[44:47], v[124:127], v[60:63]\n"
"ds_read_b64 v[116:117], v24 offset:512\n"
"v_mfma_f32_16x16x32_f16 v[48:51], v[112:115], v[40:43], v[48:51]\n"
"ds_read_b64 v[118:119], v24 offset:520\n"
"v_mfma_f32_16x16x32_f16 v[52:55], v[100:103], v[40:43], v[52:55]\n"
"ds_read_b128 v[124:127], v25 offset:3584\n"
"s_waitcnt lgkmcnt(7)\n"
"v_mfma_f32_16x16x32_f16 v[56:59], v[44:47], v[40:43], v[56:59]\n"
"v_mfma_f32_16x16x32_f16 v[60:63], v[92:95], v[40:43], v[60:63]\n"
"ds_read_b64 v[112:113], v24 offset:0\n"
"v_mfma_f32_16x16x32_f16 v[48:51], v[120:123], v[108:111], v[48:51]\n"
"ds_read_b64 v[114:115], v24 offset:8\n"
"s_waitcnt lgkmcnt(5)\n"
"v_mfma_f32_16x16x32_f16 v[52:55], v[120:123], v[88:91], v[52:55]\n"
"v_mfma_f32_16x16x32_f16 v[52:55], v[96:99], v[108:111], v[52:55]\n"
"v_mfma_f32_16x16x32_f16 v[56:59], v[120:123], v[104:107], v[56:59]\n"
"v_mul_f32 v48, v36, v48\n"
"v_mul_f32 v49, v36, v49\n"
"v_mul_f32 v50, v36, v50\n"
"s_waitcnt lgkmcnt(2)\n"
"v_mfma_f32_16x16x32_f16 v[56:59], v[96:99], v[88:91], v[56:59]\n"
"v_mul_f32 v51, v36, v51\n"
"v_cvt_pk_bf16_f32 v48, v48, v49\n"
"v_cvt_pk_bf16_f32 v49, v50, v51\n"
"v_mfma_f32_16x16x32_f16 v[56:59], v[116:119], v[108:111], v[56:59]\n"
"v_pk_min_u16 v48, v48, s36\n"
"v_pk_min_u16 v49, v49, s36\n"
"ds_write_b64 v26, v[48:49] offset:0\n"
"v_mfma_f32_16x16x32_f16 v[60:63], v[120:123], v[124:127], v[60:63]\n"
"s_waitcnt lgkmcnt(1)\n"
"v_mfma_f32_16x16x32_f16 v[60:63], v[96:99], v[104:107], v[60:63]\n"
"v_mul_f32 v52, v36, v52\n"
"v_mul_f32 v53, v36, v53\n"
"v_mfma_f32_16x16x32_f16 v[60:63], v[116:119], v[88:91], v[60:63]\n"
"v_mul_f32 v54, v36, v54\n"
"v_mul_f32 v55, v36, v55\n"
"v_mfma_f32_16x16x32_f16 v[60:63], v[112:115], v[108:111], v[60:63]\n"
"v_cvt_pk_bf16_f32 v52, v52, v53\n"
"v_cvt_pk_bf16_f32 v53, v54, v55\n"
"v_pk_min_u16 v52, v52, s36\n"
"v_pk_min_u16 v53, v53, s36\n"
"ds_write_b64 v26, v[52:53] offset:512\n"
"v_mul_f32 v56, v36, v56\n"
"v_mul_f32 v57, v36, v57\n"
"v_mul_f32 v58, v36, v58\n"
"v_mul_f32 v59, v36, v59\n"
"v_cvt_pk_bf16_f32 v56, v56, v57\n"
"v_cvt_pk_bf16_f32 v57, v58, v59\n"
"v_pk_min_u16 v56, v56, s36\n"
"v_pk_min_u16 v57, v57, s36\n"
"ds_write_b64 v26, v[56:57] offset:1024\n"
"v_mul_f32 v60, v36, v60\n"
"v_mul_f32 v61, v36, v61\n"
"v_mul_f32 v62, v36, v62\n"
"v_mul_f32 v63, v36, v63\n"
"v_cvt_pk_bf16_f32 v60, v60, v61\n"
"v_cvt_pk_bf16_f32 v61, v62, v63\n"
"v_pk_min_u16 v60, v60, s36\n"
"v_pk_min_u16 v61, v61, s36\n"
"ds_write_b64 v26, v[60:61] offset:1536\n"
"ds_write_b32 v4, v4 offset:624\n"
"s_mov_b32 s17, 0x100000\n"
".Ltp_403:\n"
"ds_read_b32 v3, v5 offset:624\n"
"s_sub_u32 s17, s17, 1\n"
"s_cbranch_scc1 .Lou_404\n"
"s_waitcnt lgkmcnt(0)\n"
"v_cmp_eq_u32 vcc, 0, v3\n"
"s_cbranch_vccnz .Ltp_403\n"
".Lou_404:\n"
"s_waitcnt lgkmcnt(0)\n"
"ds_read_b64_tr_b16 v[64:65], v27 offset:0\n"
"ds_read_b64_tr_b16 v[66:67], v28 offset:0\n"
"ds_read_b64_tr_b16 v[68:69], v27 offset:1024\n"
"ds_read_b64_tr_b16 v[70:71], v28 offset:1024\n"
"s_add_u32 s33, s32, 0xa0000\n"
"s_waitcnt lgkmcnt(2)\n"
"buffer_store_dwordx4 v[64:67], v29, s[4:7], s32 offen sc1\n"
"s_waitcnt lgkmcnt(0)\n"
"buffer_store_dwordx4 v[68:71], v29, s[4:7], s33 offen sc1\n"
"ds_write_b32 v4, v4 offset:640\n"
"s_nop 0\n"
"s_branch .Ljc_405\n"
".Lnc_382:\n"
"s_mov_b32 s17, 0x100000\n"
".Ltp_406:\n"
"s_sleep 2\n"
"ds_read_b32 v3, v5 offset:240\n"
"s_sub_u32 s17, s17, 1\n"
"s_cbranch_scc1 .Lou_407\n"
"s_waitcnt lgkmcnt(0)\n"
"v_cmp_eq_u32 vcc, 0, v3\n"
"s_cbranch_vccnz .Ltp_406\n"
".Lou_407:\n"
"s_waitcnt lgkmcnt(0)\n"
"s_and_b32 s79, s2, 7\n"
"s_lshl_b32 s79, s79, 14\n"
"s_add_u32 s32, s79, 0xb2a000\n"
"s_add_u32 s33, s32, 0x1000\n"
"buffer_load_dwordx4 v[48:51], v2, s[4:7], s32 offen offset:0 sc1\n"
"buffer_load_dwordx4 v[52:55], v2, s[4:7], s32 offen offset:1024 sc1\n"
"buffer_load_dwordx4 v[56:59], v2, s[4:7], s32 offen offset:2048 sc1\n"
"buffer_load_dwordx4 v[60:63], v2, s[4:7], s32 offen offset:3072 sc1\n"
"buffer_load_dwordx4 v[64:67], v2, s[4:7], s33 offen offset:0 sc1\n"
"buffer_load_dwordx4 v[68:71], v2, s[4:7], s33 offen offset:1024 sc1\n"
"buffer_load_dwordx4 v[72:75], v2, s[4:7], s33 offen offset:2048 sc1\n"
"buffer_load_dwordx4 v[76:79], v2, s[4:7], s33 offen offset:3072 sc1\n"
"s_mov_b32 s17, 32768\n"
".Ltp_408:\n"
"s_waitcnt vmcnt(0)\n"
"s_sub_u32 s17, s17, 1\n"
"s_cbranch_scc1 .Lou_409\n"
"v_max_u32 v80, v48, v49\n"
"v_max3_u32 v80, v50, v51, v80\n"
"v_max3_u32 v80, v52, v53, v80\n"
"v_max3_u32 v80, v54, v55, v80\n"
"v_max3_u32 v80, v56, v57, v80\n"
"v_max3_u32 v80, v58, v59, v80\n"
"v_max3_u32 v80, v60, v61, v80\n"
"v_max3_u32 v80, v62, v63, v80\n"
"v_max3_u32 v80, v64, v65, v80\n"
"v_max3_u32 v80, v66, v67, v80\n"
"v_max3_u32 v80, v68, v69, v80\n"
"v_max3_u32 v80, v70, v71, v80\n"
"v_max3_u32 v80, v72, v73, v80\n"
"v_max3_u32 v80, v74, v75, v80\n"
"v_max3_u32 v80, v76, v77, v80\n"
"v_max3_u32 v80, v78, v79, v80\n"
"v_cmp_eq_u32 vcc, -1, v80\n"
"s_cbranch_vccz .Lou_409\n"
"s_sleep 4\n"
"v_max_u32 v80, v50, v51\n"
"v_max3_u32 v80, v48, v49, v80\n"
"v_cmp_eq_u32 vcc, -1, v80\n"
"s_cbranch_vccz .Lsk_410\n"
"s_mov_b64 exec, vcc\n"
"buffer_load_dwordx4 v[48:51], v2, s[4:7], s32 offen offset:0 sc1\n"
"s_mov_b64 exec, -1\n"
".Lsk_410:\n"
"v_max_u32 v80, v54, v55\n"
"v_max3_u32 v80, v52, v53, v80\n"
"v_cmp_eq_u32 vcc, -1, v80\n"
"s_cbranch_vccz .Lsk_411\n"
"s_mov_b64 exec, vcc\n"
"buffer_load_dwordx4 v[52:55], v2, s[4:7], s32 offen offset:1024 sc1\n"
"s_mov_b64 exec, -1\n"
".Lsk_411:\n"
"v_max_u32 v80, v58, v59\n"
"v_max3_u32 v80, v56, v57, v80\n"
"v_cmp_eq_u32 vcc, -1, v80\n"
"s_cbranch_vccz .Lsk_412\n"
"s_mov_b64 exec, vcc\n"
"buffer_load_dwordx4 v[56:59], v2, s[4:7], s32 offen offset:2048 sc1\n"
"s_mov_b64 exec, -1\n"
".Lsk_412:\n"
"v_max_u32 v80, v62, v63\n"
"v_max3_u32 v80, v60, v61, v80\n"
"v_cmp_eq_u32 vcc, -1, v80\n"
"s_cbranch_vccz .Lsk_413\n"
"s_mov_b64 exec, vcc\n"
"buffer_load_dwordx4 v[60:63], v2, s[4:7], s32 offen offset:3072 sc1\n"
"s_mov_b64 exec, -1\n"
".Lsk_413:\n"
"v_max_u32 v80, v66, v67\n"
"v_max3_u32 v80, v64, v65, v80\n"
"v_cmp_eq_u32 vcc, -1, v80\n"
"s_cbranch_vccz .Lsk_414\n"
"s_mov_b64 exec, vcc\n"
"buffer_load_dwordx4 v[64:67], v2, s[4:7], s33 offen offset:0 sc1\n"
"s_mov_b64 exec, -1\n"
".Lsk_414:\n"
"v_max_u32 v80, v70, v71\n"
"v_max3_u32 v80, v68, v69, v80\n"
"v_cmp_eq_u32 vcc, -1, v80\n"
"s_cbranch_vccz .Lsk_415\n"
"s_mov_b64 exec, vcc\n"
"buffer_load_dwordx4 v[68:71], v2, s[4:7], s33 offen offset:1024 sc1\n"
"s_mov_b64 exec, -1\n"
".Lsk_415:\n"
"v_max_u32 v80, v74, v75\n"
"v_max3_u32 v80, v72, v73, v80\n"
"v_cmp_eq_u32 vcc, -1, v80\n"
"s_cbranch_vccz .Lsk_416\n"
"s_mov_b64 exec, vcc\n"
"buffer_load_dwordx4 v[72:75], v2, s[4:7], s33 offen offset:2048 sc1\n"
"s_mov_b64 exec, -1\n"
".Lsk_416:\n"
"v_max_u32 v80, v78, v79\n"
"v_max3_u32 v80, v76, v77, v80\n"
"v_cmp_eq_u32 vcc, -1, v80\n"
"s_cbranch_vccz .Lsk_417\n"
"s_mov_b64 exec, vcc\n"
"buffer_load_dwordx4 v[76:79], v2, s[4:7], s33 offen offset:3072 sc1\n"
"s_mov_b64 exec, -1\n"
".Lsk_417:\n"
"s_branch .Ltp_408\n"
".Lou_409:\n"
"s_waitcnt vmcnt(0)\n"
".Ljc_405:\n"
"s_setprio 0\n"
"s_lshr_b32 s79, s2, 1\n"
"s_and_b32 s79, s79, 3\n"
"s_lshl_b32 s79, s79, 2\n"
"s_lshr_b32 s78, s2, 3\n"
"s_lshr_b32 s78, s78, 3\n"
"s_add_u32 s79, s79, s78\n"
"s_lshl_b32 s79, s79, 2\n"
"s_add_u32 s79, s79, 64\n"
"s_mul_i32 s79, s79, 0x5000\n"
"s_mul_i32 s78, s3, 0x1400\n"
"s_add_u32 s79, s79, s78\n"
"s_add_u32 s50, s79, 0xb48000\n"
"s_add_u32 s51, s50, 0x1000\n"
"v_min_i32 v99, 7, v0\n"
"v_mul_i32_i24 v48, 0x280, v99\n"
"s_mul_i32 s79, s3, 0x1400\n"
"s_add_u32 s79, s79, 0xa000\n"
"v_add_u32 v50, s79, v2\n"
"s_and_b32 s79, s2, 7\n"
"s_lshl_b32 s79, s79, 3\n"
"s_add_u32 s79, s79, 0x47\n"
"s_mul_i32 s79, s79, 0x5000\n"
"s_and_b32 s78, s2, 7\n"
"s_lshr_b32 s78, s78, 1\n"
"s_lshl_b32 s78, s78, 7\n"
"s_add_u32 s79, s79, s78\n"
"s_mul_i32 s78, s3, 0x1400\n"
"s_add_u32 s79, s79, s78\n"
"s_add_u32 s49, s79, 0xb48000\n"
"s_mov_b32 s17, 0x100000\n"
".Ltp_418:\n"
"s_sleep 2\n"
"ds_read_b32 v3, v5 offset:3264\n"
"s_sub_u32 s17, s17, 1\n"
"s_cbranch_scc1 .Lou_419\n"
"s_waitcnt lgkmcnt(0)\n"
"v_cmp_eq_u32 vcc, -1, v3\n"
"s_cbranch_vccnz .Ltp_418\n"
".Lou_419:\n"
"s_waitcnt lgkmcnt(0)\n"
"s_mov_b32 s17, 32768\n"
".Ltp_420:\n"
"buffer_load_dword v49, v48, s[4:7], s49 offen sc1\n"
"s_sub_u32 s17, s17, 1\n"
"s_cbranch_scc1 .Lou_421\n"
"s_waitcnt vmcnt(0)\n"
"v_cmp_eq_u32 vcc, -1, v49\n"
"s_cbranch_vccz .Lou_421\n"
"s_sleep 2\n"
"s_branch .Ltp_420\n"
".Lou_421:\n"
"s_waitcnt vmcnt(0)\n"
"buffer_load_dwordx4 v[52:55], v2, s[4:7], s50 offen offset:0 sc1\n"
"buffer_load_dwordx4 v[56:59], v2, s[4:7], s50 offen offset:1024 sc1\n"
"buffer_load_dwordx4 v[60:63], v2, s[4:7], s50 offen offset:2048 sc1\n"
"buffer_load_dwordx4 v[64:67], v2, s[4:7], s50 offen offset:3072 sc1\n"
"buffer_load_dwordx4 v[68:71], v2, s[4:7], s51 offen offset:0 sc1\n"
"s_mov_b32 s17, 32768\n"
".Ltp_422:\n"
"s_waitcnt vmcnt(0)\n"
"s_sub_u32 s17, s17, 1\n"
"s_cbranch_scc1 .Lou_423\n"
"v_max_u32 v38, v52, v53\n"
"v_max3_u32 v38, v54, v55, v38\n"
"v_max3_u32 v38, v56, v57, v38\n"
"v_max3_u32 v38, v58, v59, v38\n"
"v_max3_u32 v38, v60, v61, v38\n"
"v_max3_u32 v38, v62, v63, v38\n"
"v_max3_u32 v38, v64, v65, v38\n"
"v_max3_u32 v38, v66, v67, v38\n"
"v_max3_u32 v38, v68, v69, v38\n"
"v_max3_u32 v38, v70, v71, v38\n"
"v_cmp_eq_u32 vcc, -1, v38\n"
"s_cbranch_vccz .Lou_423\n"
"s_sleep 1\n"
"v_max_u32 v38, v54, v55\n"
"v_max3_u32 v38, v52, v53, v38\n"
"v_cmp_eq_u32 vcc, -1, v38\n"
"s_cbranch_vccz .Lsk_424\n"
"s_mov_b64 exec, vcc\n"
"buffer_load_dwordx4 v[52:55], v2, s[4:7], s50 offen offset:0 sc1\n"
"s_mov_b64 exec, -1\n"
".Lsk_424:\n"
"v_max_u32 v38, v58, v59\n"
"v_max3_u32 v38, v56, v57, v38\n"
"v_cmp_eq_u32 vcc, -1, v38\n"
"s_cbranch_vccz .Lsk_425\n"
"s_mov_b64 exec, vcc\n"
"buffer_load_dwordx4 v[56:59], v2, s[4:7], s50 offen offset:1024 sc1\n"
"s_mov_b64 exec, -1\n"
".Lsk_425:\n"
"v_max_u32 v38, v62, v63\n"
"v_max3_u32 v38, v60, v61, v38\n"
"v_cmp_eq_u32 vcc, -1, v38\n"
"s_cbranch_vccz .Lsk_426\n"
"s_mov_b64 exec, vcc\n"
"buffer_load_dwordx4 v[60:63], v2, s[4:7], s50 offen offset:2048 sc1\n"
"s_mov_b64 exec, -1\n"
".Lsk_426:\n"
"v_max_u32 v38, v66, v67\n"
"v_max3_u32 v38, v64, v65, v38\n"
"v_cmp_eq_u32 vcc, -1, v38\n"
"s_cbranch_vccz .Lsk_427\n"
"s_mov_b64 exec, vcc\n"
"buffer_load_dwordx4 v[64:67], v2, s[4:7], s50 offen offset:3072 sc1\n"
"s_mov_b64 exec, -1\n"
".Lsk_427:\n"
"v_max_u32 v38, v70, v71\n"
"v_max3_u32 v38, v68, v69, v38\n"
"v_cmp_eq_u32 vcc, -1, v38\n"
"s_cbranch_vccz .Lsk_428\n"
"s_mov_b64 exec, vcc\n"
"buffer_load_dwordx4 v[68:71], v2, s[4:7], s51 offen offset:0 sc1\n"
"s_mov_b64 exec, -1\n"
".Lsk_428:\n"
"s_branch .Ltp_422\n"
".Lou_423:\n"
"s_waitcnt vmcnt(0)\n"
"s_mov_b32 s17, 0x100000\n"
".Ltp_429:\n"
"s_sleep 2\n"
"ds_read_b32 v3, v5 offset:3584\n"
"s_sub_u32 s17, s17, 1\n"
"s_cbranch_scc1 .Lou_430\n"
"s_waitcnt lgkmcnt(0)\n"
"v_cmp_eq_u32 vcc, -1, v3\n"
"s_cbranch_vccnz .Ltp_429\n"
".Lou_430:\n"
"s_waitcnt lgkmcnt(0)\n"
"ds_write_b128 v50, v[52:55] offset:0\n"
"ds_write_b128 v50, v[56:59] offset:1024\n"
"ds_write_b128 v50, v[60:63] offset:2048\n"
"ds_write_b128 v50, v[64:67] offset:3072\n"
"ds_write_b128 v50, v[68:71] offset:4096\n"
"ds_write_b32 v4, v4 offset:384\n"
"s_mov_b32 s17, 0x100000\n"
".Ltp_431:\n"
"s_sleep 2\n"
"ds_read_b32 v3, v5 offset:3904\n"
"s_sub_u32 s17, s17, 1\n"
"s_cbranch_scc1 .Lou_432\n"
"s_waitcnt lgkmcnt(0)\n"
"v_cmp_eq_u32 vcc, -1, v3\n"
"s_cbranch_vccnz .Ltp_431\n"
".Lou_432:\n"
"s_waitcnt lgkmcnt(0)\n"
"s_mul_i32 s79, s3, 0x1400\n"
"s_add_u32 m0, s79, 0xf000\n"
"s_add_u32 s49, s50, 0x5000\n"
"buffer_load_dwordx4 v2, s[4:7], s49 offen offset:0 sc1 lds\n"
"buffer_load_dwordx4 v2, s[4:7], s49 offen offset:1024 sc1 lds\n"
"buffer_load_dwordx4 v2, s[4:7], s49 offen offset:2048 sc1 lds\n"
"buffer_load_dwordx4 v2, s[4:7], s49 offen offset:3072 sc1 lds\n"
"s_add_u32 m0, m0, 1024\n"
"s_add_u32 s49, s49, 1024\n"
"buffer_load_dwordx4 v2, s[4:7], s49 offen offset:3072 sc1 lds\n"
"s_waitcnt vmcnt(0)\n"
"ds_write_b32 v4, v4 offset:272\n"
"s_setprio 2\n"
"v_add_u32 v14, v39, v14\n"
"v_add_u32 v15, v39, v15\n"
"s_mul_i32 s79, s28, 0xa00\n"
"s_add_u32 s79, s30, s79\n"
"s_and_b32 s78, s3, 1\n"
"s_mul_i32 s78, s78, 0x600\n"
"s_add_u32 s79, s79, s78\n"
"s_add_u32 s36, s79, 0xb48000\n"
"s_mul_i32 s79, s3, 0x1400\n"
"s_add_u32 s79, s30, s79\n"
"s_add_u32 s32, s79, 0xdc8000\n"
"s_add_u32 s33, s32, 0x1000\n"
"s_lshl_b32 s79, s29, 1\n"
"s_lshr_b32 s78, s3, 1\n"
"s_xor_b32 s78, s78, 1\n"
"s_add_u32 s79, s79, s78\n"
"s_mul_i32 s79, s79, 0x5000\n"
"s_mul_i32 s78, s3, 0x1400\n"
"s_add_u32 s79, s79, s78\n"
"s_add_u32 s34, s79, 0xdc8000\n"
"s_add_u32 s35, s34, 0x1000\n"
"s_lshl_b32 s79, s30, 1\n"
"s_mul_i32 s78, s28, 0x1400\n"
"s_add_u32 s79, s79, s78\n"
"s_and_b32 s78, s3, 1\n"
"s_mul_i32 s78, s78, 0xc00\n"
"s_add_u32 s79, s79, s78\n"
"s_add_u32 s37, s79, 0x1048000\n"
"s_lshl_b32 s79, s29, 1\n"
"s_lshr_b32 s78, s3, 1\n"
"s_add_u32 s79, s79, s78\n"
"s_lshl_b32 s79, s79, 11\n"
"s_lshl_b32 s78, s28, 8\n"
"s_add_u32 s79, s79, s78\n"
"s_and_b32 s78, s3, 1\n"
"s_lshl_b32 s78, s78, 7\n"
"s_add_u32 s79, s79, s78\n"
"s_add_u32 s38, s79, 0x1548000\n"
"s_mov_b32 s17, 0x100000\n"
".Ltp_433:\n"
"ds_read_b32 v3, v5 offset:336\n"
"s_sub_u32 s17, s17, 1\n"
"s_cbranch_scc1 .Lou_434\n"
"s_waitcnt lgkmcnt(0)\n"
"v_cmp_eq_u32 vcc, 0, v3\n"
"s_cbranch_vccnz .Ltp_433\n"
".Lou_434:\n"
"s_waitcnt lgkmcnt(0)\n"
"s_cmp_lt_i32 s18, 0\n"
"s_cbranch_scc1 .Lcw_435\n"
"s_mov_b32 s17, 0x100000\n"
".Ltp_436:\n"
"ds_read_b32 v3, v5 offset:640\n"
"s_sub_u32 s17, s17, 1\n"
"s_cbranch_scc1 .Lou_437\n"
"s_waitcnt lgkmcnt(0)\n"
"v_cmp_eq_u32 vcc, 0, v3\n"
"s_cbranch_vccnz .Ltp_436\n"
".Lou_437:\n"
"s_waitcnt lgkmcnt(0)\n"
".Lcw_435:\n"
"s_mov_b32 s17, 0x100000\n"
".Ltp_438:\n"
"s_sleep 2\n"
"ds_read_b32 v3, v5 offset:2944\n"
"s_sub_u32 s17, s17, 1\n"
"s_cbranch_scc1 .Lou_439\n"
"s_waitcnt lgkmcnt(0)\n"
"v_cmp_eq_u32 vcc, -1, v3\n"
"s_cbranch_vccnz .Ltp_438\n"
".Lou_439:\n"
"s_waitcnt lgkmcnt(0)\n"
"s_mov_b32 s17, 32768\n"
".Ltp_440:\n"
"buffer_load_dwordx2 v[40:41], v14, s[4:7], s36 offen offset:0 sc1\n"
"buffer_load_dwordx2 v[42:43], v14, s[4:7], s36 offen offset:512 sc1\n"
"buffer_load_dwordx2 v[44:45], v15, s[4:7], s36 offen offset:1024 sc1\n"
"s_sub_u32 s17, s17, 1\n"
"s_cbranch_scc1 .Lou_441\n"
"s_waitcnt vmcnt(1)\n"
"v_max3_u32 v38, v40, v41, v42\n"
"s_waitcnt vmcnt(0)\n"
"v_max3_u32 v38, v43, v44, v38\n"
"v_max_u32 v38, v45, v38\n"
"v_cmp_eq_u32 vcc, -1, v38\n"
"s_cbranch_vccnz .Ltp_440\n"
".Lou_441:\n"
"s_waitcnt vmcnt(0)\n"
"s_mov_b32 s17, 0x100000\n"
".Ltp_442:\n"
"s_sleep 1\n"
"ds_read_b32 v3, v5 offset:320\n"
"s_sub_u32 s17, s17, 1\n"
"s_cbranch_scc1 .Lou_443\n"
"s_waitcnt lgkmcnt(0)\n"
"v_cmp_eq_u32 vcc, 0, v3\n"
"s_cbranch_vccnz .Ltp_442\n"
".Lou_443:\n"
"s_waitcnt lgkmcnt(0)\n"
"s_mul_i32 m0, s3, 0x1400\n"
"s_add_u32 s49, s50, 0xa000\n"
"buffer_load_dwordx4 v2, s[4:7], s49 offen offset:0 sc1 lds\n"
"buffer_load_dwordx4 v2, s[4:7], s49 offen offset:1024 sc1 lds\n"
"buffer_load_dwordx4 v2, s[4:7], s49 offen offset:2048 sc1 lds\n"
"buffer_load_dwordx4 v2, s[4:7], s49 offen offset:3072 sc1 lds\n"
"s_add_u32 m0, m0, 1024\n"
"s_add_u32 s49, s49, 1024\n"
"buffer_load_dwordx4 v2, s[4:7], s49 offen offset:3072 sc1 lds\n"
"s_mov_b32 s17, 0x100000\n"
".Ltp_444:\n"
"s_sleep 1\n"
"ds_read_b32 v3, v5 offset:352\n"
"s_sub_u32 s17, s17, 1\n"
"s_cbranch_scc1 .Lou_445\n"
"s_waitcnt lgkmcnt(0)\n"
"v_cmp_eq_u32 vcc, 0, v3\n"
"s_cbranch_vccnz .Ltp_444\n"
".Lou_445:\n"
"s_waitcnt lgkmcnt(0)\n"
"s_mul_i32 s79, s3, 0x1400\n"
"s_add_u32 m0, s79, 0x5000\n"
"s_add_u32 s49, s50, 0xf000\n"
"buffer_load_dwordx4 v2, s[4:7], s49 offen offset:0 sc1 lds\n"
"buffer_load_dwordx4 v2, s[4:7], s49 offen offset:1024 sc1 lds\n"
"buffer_load_dwordx4 v2, s[4:7], s49 offen offset:2048 sc1 lds\n"
"buffer_load_dwordx4 v2, s[4:7], s49 offen offset:3072 sc1 lds\n"
"s_add_u32 m0, m0, 1024\n"
"s_add_u32 s49, s49, 1024\n"
"buffer_load_dwordx4 v2, s[4:7], s49 offen offset:3072 sc1 lds\n"
"s_waitcnt vmcnt(0)\n"
"ds_write_b32 v4, v4 offset:400\n"
"s_sleep 8\n"
"buffer_load_dwordx4 v[108:111], v2, s[4:7], s34 offen offset:0 sc1\n"
"s_nop 6\n"
"buffer_load_dwordx4 v[112:115], v2, s[4:7], s34 offen offset:1024 sc1\n"
"s_nop 6\n"
"buffer_load_dwordx4 v[116:119], v2, s[4:7], s34 offen offset:2048 sc1\n"
"s_nop 6\n"
"buffer_load_dwordx4 v[120:123], v2, s[4:7], s34 offen offset:3072 sc1\n"
"s_nop 6\n"
"buffer_load_dwordx4 v[124:127], v2, s[4:7], s35 offen offset:0 sc1\n"
"s_nop 6\n"
"buffer_load_dwordx4 v[88:91], v2, s[4:7], s32 offen offset:0 sc1\n"
"s_nop 6\n"
"buffer_load_dwordx4 v[92:95], v2, s[4:7], s32 offen offset:1024 sc1\n"
"s_nop 6\n"
"buffer_load_dwordx4 v[96:99], v2, s[4:7], s32 offen offset:2048 sc1\n"
"s_nop 6\n"
"buffer_load_dwordx4 v[100:103], v2, s[4:7], s32 offen offset:3072 sc1\n"
"s_nop 6\n"
"buffer_load_dwordx4 v[104:107], v2, s[4:7], s33 offen offset:0 sc1\n"
"v_lshlrev_b32 v48, 16, v40\n"
"v_and_b32 v49, 0xffff0000, v40\n"
"v_lshlrev_b32 v50, 16, v41\n"
"v_and_b32 v51, 0xffff0000, v41\n"
"v_lshlrev_b32 v52, 16, v42\n"
"v_and_b32 v53, 0xffff0000, v42\n"
"v_lshlrev_b32 v54, 16, v43\n"
"v_and_b32 v55, 0xffff0000, v43\n"
"v_lshlrev_b32 v56, 16, v44\n"
"v_and_b32 v57, 0xffff0000, v44\n"
"v_lshlrev_b32 v58, 16, v45\n"
"v_and_b32 v59, 0xffff0000, v45\n"
"v_mov_b32 v46, 0\n"
"v_mov_b32 v47, 0\n"
"s_waitcnt vmcnt(9)\n"
"v_max_u32 v38, v108, v109\n"
"v_max3_u32 v38, v110, v111, v38\n"
"v_cmp_eq_u32 vcc, -1, v38\n"
"s_cbranch_vccnz .Lsl_446\n"
".Lbk_447:\n"
"v_mfma_f32_16x16x32_bf16 v[68:71], v[156:159], v[108:111], 0\n"
"v_mfma_f32_16x16x32_bf16 v[72:75], v[160:163], v[108:111], 0\n"
"v_mfma_f32_16x16x32_bf16 v[76:79], v[164:167], v[108:111], 0\n"
"s_waitcnt vmcnt(8)\n"
"v_max_u32 v38, v112, v113\n"
"v_max3_u32 v38, v114, v115, v38\n"
"v_mfma_f32_16x16x32_bf16 v[80:83], v[168:171], v[108:111], 0\n"
"v_cmp_eq_u32 vcc, -1, v38\n"
"v_mfma_f32_16x16x32_bf16 v[84:87], v[172:175], v[108:111], 0\n"
"s_cbranch_vccnz .Lsl_448\n"
".Lbk_449:\n"
"v_mfma_f32_16x16x32_bf16 v[68:71], v[176:179], v[112:115], v[68:71]\n"
"v_mfma_f32_16x16x32_bf16 v[72:75], v[180:183], v[112:115], v[72:75]\n"
"v_mfma_f32_16x16x32_bf16 v[76:79], v[184:187], v[112:115], v[76:79]\n"
"s_waitcnt vmcnt(7)\n"
"v_max_u32 v38, v116, v117\n"
"v_max3_u32 v38, v118, v119, v38\n"
"v_mfma_f32_16x16x32_bf16 v[80:83], v[188:191], v[112:115], v[80:83]\n"
"v_cmp_eq_u32 vcc, -1, v38\n"
"v_mfma_f32_16x16x32_bf16 v[84:87], v[192:195], v[112:115], v[84:87]\n"
"s_cbranch_vccnz .Lsl_450\n"
".Lbk_451:\n"
"v_mfma_f32_16x16x32_bf16 v[68:71], v[196:199], v[116:119], v[68:71]\n"
"v_mfma_f32_16x16x32_bf16 v[72:75], v[200:203], v[116:119], v[72:75]\n"
"v_mfma_f32_16x16x32_bf16 v[76:79], v[204:207], v[116:119], v[76:79]\n"
"s_waitcnt vmcnt(6)\n"
"v_max_u32 v38, v120, v121\n"
"v_max3_u32 v38, v122, v123, v38\n"
"v_mfma_f32_16x16x32_bf16 v[80:83], v[208:211], v[116:119], v[80:83]\n"
"v_cmp_eq_u32 vcc, -1, v38\n"
"v_mfma_f32_16x16x32_bf16 v[84:87], v[212:215], v[116:119], v[84:87]\n"
"s_cbranch_vccnz .Lsl_452\n"
".Lbk_453:\n"
"v_mfma_f32_16x16x32_bf16 v[68:71], v[216:219], v[120:123], v[68:71]\n"
"v_mfma_f32_16x16x32_bf16 v[72:75], v[220:223], v[120:123], v[72:75]\n"
"v_mfma_f32_16x16x32_bf16 v[76:79], v[224:227], v[120:123], v[76:79]\n"
"s_waitcnt vmcnt(5)\n"
"v_max_u32 v38, v124, v125\n"
"v_max3_u32 v38, v126, v127, v38\n"
"v_mfma_f32_16x16x32_bf16 v[80:83], v[228:231], v[120:123], v[80:83]\n"
"v_cmp_eq_u32 vcc, -1, v38\n"
"v_mfma_f32_16x16x32_bf16 v[84:87], v[232:235], v[120:123], v[84:87]\n"
"s_cbranch_vccnz .Lsl_454\n"
".Lbk_455:\n"
"v_mfma_f32_16x16x32_bf16 v[68:71], v[236:239], v[124:127], v[68:71]\n"
"v_mfma_f32_16x16x32_bf16 v[72:75], v[240:243], v[124:127], v[72:75]\n"
"v_mfma_f32_16x16x32_bf16 v[76:79], v[244:247], v[124:127], v[76:79]\n"
"s_waitcnt vmcnt(4)\n"
"v_max_u32 v38, v88, v89\n"
"v_max3_u32 v38, v90, v91, v38\n"
"v_mfma_f32_16x16x32_bf16 v[80:83], v[248:251], v[124:127], v[80:83]\n"
"v_cmp_eq_u32 vcc, -1, v38\n"
"v_mfma_f32_16x16x32_bf16 v[84:87], v[252:255], v[124:127], v[84:87]\n"
"ds_write_b128 v8, v[68:71] offset:0\n"
"s_cbranch_vccnz .Lsl_456\n"
".Lbk_457:\n"
"v_mfma_f32_16x16x32_bf16 v[48:51], v[156:159], v[88:91], v[48:51]\n"
"ds_write_b128 v8, v[72:75] offset:1024\n"
"v_mfma_f32_16x16x32_bf16 v[52:55], v[160:163], v[88:91], v[52:55]\n"
"ds_write_b128 v10, v[76:79] offset:0\n"
"v_mfma_f32_16x16x32_bf16 v[56:59], v[164:167], v[88:91], v[56:59]\n"
"s_waitcnt vmcnt(3)\n"
"v_max_u32 v38, v92, v93\n"
"v_max3_u32 v38, v94, v95, v38\n"
"v_mfma_f32_16x16x32_bf16 v[60:63], v[168:171], v[88:91], 0\n"
"v_cmp_eq_u32 vcc, -1, v38\n"
"v_mov_b32 v117, s16\n"
"v_mfma_f32_16x16x32_bf16 v[64:67], v[172:175], v[88:91], 0\n"
"ds_write_b128 v9, v[80:83] offset:0\n"
"s_cbranch_vccnz .Lsl_458\n"
".Lbk_459:\n"
"v_mfma_f32_16x16x32_bf16 v[48:51], v[176:179], v[92:95], v[48:51]\n"
"ds_write_b128 v9, v[84:87] offset:1024\n"
"v_mfma_f32_16x16x32_bf16 v[52:55], v[180:183], v[92:95], v[52:55]\n"
"ds_write_b32 v4, v4 offset:656\n"
"v_mfma_f32_16x16x32_bf16 v[56:59], v[184:187], v[92:95], v[56:59]\n"
"s_waitcnt vmcnt(2)\n"
"v_max_u32 v38, v96, v97\n"
"v_max3_u32 v38, v98, v99, v38\n"
"v_mfma_f32_16x16x32_bf16 v[60:63], v[188:191], v[92:95], v[60:63]\n"
"v_cmp_eq_u32 vcc, -1, v38\n"
"v_mov_b32 v119, s16\n"
"v_mfma_f32_16x16x32_bf16 v[64:67], v[192:195], v[92:95], v[64:67]\n"
"s_cbranch_vccnz .Lsl_460\n"
".Lbk_461:\n"
"v_mfma_f32_16x16x32_bf16 v[48:51], v[196:199], v[96:99], v[48:51]\n"
"v_mov_b32 v121, s16\n"
"v_mov_b32 v123, s16\n"
"v_mfma_f32_16x16x32_bf16 v[52:55], v[200:203], v[96:99], v[52:55]\n"
"v_mov_b32 v125, s16\n"
"v_mov_b32 v127, s16\n"
"v_mfma_f32_16x16x32_bf16 v[56:59], v[204:207], v[96:99], v[56:59]\n"
"s_waitcnt vmcnt(1)\n"
"v_max_u32 v38, v100, v101\n"
"v_max3_u32 v38, v102, v103, v38\n"
"v_mfma_f32_16x16x32_bf16 v[60:63], v[208:211], v[96:99], v[60:63]\n"
"v_cmp_eq_u32 vcc, -1, v38\n"
"ds_read_b32 v3, v5 offset:656\n"
"ds_read_b128 v[76:79], v12 offset:2048\n"
"v_mfma_f32_16x16x32_bf16 v[64:67], v[212:215], v[96:99], v[64:67]\n"
"ds_read_b128 v[108:111], v13 offset:2048\n"
"s_cbranch_vccnz .Lsl_462\n"
".Lbk_463:\n"
"v_mfma_f32_16x16x32_bf16 v[56:59], v[224:227], v[100:103], v[56:59]\n"
"s_waitcnt vmcnt(0)\n"
"v_max_u32 v38, v104, v105\n"
"v_max3_u32 v38, v106, v107, v38\n"
"ds_read_b128 v[68:71], v12 offset:0\n"
"v_mfma_f32_16x16x32_bf16 v[60:63], v[228:231], v[100:103], v[60:63]\n"
"v_cmp_eq_u32 vcc, -1, v38\n"
"ds_read_b128 v[80:83], v13 offset:0\n"
"v_mfma_f32_16x16x32_bf16 v[64:67], v[232:235], v[100:103], v[64:67]\n"
"s_cbranch_vccnz .Lsl_464\n"
".Lbk_465:\n"
"v_mfma_f32_16x16x32_bf16 v[56:59], v[244:247], v[104:107], v[56:59]\n"
"ds_read_b128 v[72:75], v12 offset:1024\n"
"ds_read_b128 v[84:87], v13 offset:1024\n"
"v_mfma_f32_16x16x32_bf16 v[60:63], v[248:251], v[104:107], v[60:63]\n"
"v_mfma_f32_16x16x32_bf16 v[64:67], v[252:255], v[104:107], v[64:67]\n"
"v_mfma_f32_16x16x32_bf16 v[48:51], v[216:219], v[100:103], v[48:51]\n"
"ds_write_b128 v7, v[56:59] offset:0\n"
"v_mfma_f32_16x16x32_bf16 v[52:55], v[220:223], v[100:103], v[52:55]\n"
"ds_write_b128 v6, v[60:63] offset:0\n"
"v_mfma_f32_16x16x32_bf16 v[48:51], v[236:239], v[104:107], v[48:51]\n"
"ds_write_b128 v6, v[64:67] offset:1024\n"
"v_mfma_f32_16x16x32_bf16 v[52:55], v[240:243], v[104:107], v[52:55]\n"
"ds_write_b32 v4, v4 offset:672\n"
"s_mov_b32 s17, 0x100000\n"
"s_waitcnt lgkmcnt(10)\n"
"v_cmp_eq_u32 vcc, 0, v3\n"
"s_cbranch_vccz .Lok_467\n"
".Ltp_466:\n"
"ds_read_b32 v3, v5 offset:656\n"
"ds_read_b128 v[76:79], v12 offset:2048\n"
"ds_read_b128 v[108:111], v13 offset:2048\n"
"ds_read_b128 v[68:71], v12 offset:0\n"
"ds_read_b128 v[80:83], v13 offset:0\n"
"ds_read_b128 v[72:75], v12 offset:1024\n"
"ds_read_b128 v[84:87], v13 offset:1024\n"
"s_sub_u32 s17, s17, 1\n"
"s_cbranch_scc1 .Lok_467\n"
"s_waitcnt lgkmcnt(6)\n"
"v_cmp_eq_u32 vcc, 0, v3\n"
"s_cbranch_vccnz .Ltp_466\n"
"s_waitcnt lgkmcnt(0)\n"
".Lok_467:\n"
"s_waitcnt lgkmcnt(8)\n"
"v_add_f32 v76, v76, v108\n"
"v_add_f32 v77, v77, v109\n"
"v_add_f32 v78, v78, v110\n"
"v_add_f32 v79, v79, v111\n"
"ds_read_b32 v3, v5 offset:672\n"
"ds_read_b128 v[112:115], v11 offset:2048\n"
"ds_read_b128 v[60:63], v11 offset:0\n"
"ds_read_b128 v[64:67], v11 offset:1024\n"
"s_waitcnt lgkmcnt(10)\n"
"v_add_f32 v68, v68, v80\n"
"v_add_f32 v69, v69, v81\n"
"v_add_f32 v70, v70, v82\n"
"v_add_f32 v71, v71, v83\n"
"s_waitcnt lgkmcnt(8)\n"
"v_add_f32 v72, v72, v84\n"
"v_add_f32 v73, v73, v85\n"
"v_add_f32 v74, v74, v86\n"
"v_add_f32 v75, v75, v87\n"
"v_add_f32 v56, v56, v76\n"
"v_add_f32 v57, v57, v77\n"
"v_add_f32 v58, v58, v78\n"
"v_add_f32 v59, v59, v79\n"
"v_add_f32 v48, v48, v68\n"
"v_add_f32 v49, v49, v69\n"
"v_add_f32 v50, v50, v70\n"
"v_add_f32 v51, v51, v71\n"
"v_add_f32 v52, v52, v72\n"
"v_add_f32 v53, v53, v73\n"
"v_add_f32 v54, v54, v74\n"
"v_add_f32 v55, v55, v75\n"
"s_mov_b32 s17, 0x100000\n"
"s_waitcnt lgkmcnt(3)\n"
"v_cmp_eq_u32 vcc, 0, v3\n"
"s_cbranch_vccz .Lok_469\n"
".Ltp_468:\n"
"ds_read_b32 v3, v5 offset:672\n"
"ds_read_b128 v[112:115], v11 offset:2048\n"
"ds_read_b128 v[60:63], v11 offset:0\n"
"ds_read_b128 v[64:67], v11 offset:1024\n"
"s_sub_u32 s17, s17, 1\n"
"s_cbranch_scc1 .Lok_469\n"
"s_waitcnt lgkmcnt(3)\n"
"v_cmp_eq_u32 vcc, 0, v3\n"
"s_cbranch_vccnz .Ltp_468\n"
"s_waitcnt lgkmcnt(0)\n"
".Lok_469:\n"
"s_waitcnt lgkmcnt(2)\n"
"v_add_f32 v56, v56, v112\n"
"v_add_f32 v57, v57, v113\n"
"v_add_f32 v58, v58, v114\n"
"v_add_f32 v59, v59, v115\n"
"v_cvt_pk_bf16_f32 v44, v56, v57\n"
"v_cvt_pk_bf16_f32 v45, v58, v59\n"
"v_and_b32 v44, v19, v44\n"
"v_and_b32 v45, v19, v45\n"
"s_waitcnt lgkmcnt(1)\n"
"v_add_f32 v48, v48, v60\n"
"v_add_f32 v49, v49, v61\n"
"v_add_f32 v50, v50, v62\n"
"v_add_f32 v51, v51, v63\n"
"v_cvt_pk_bf16_f32 v40, v48, v49\n"
"v_cvt_pk_bf16_f32 v41, v50, v51\n"
"v_mfma_f32_16x16x32_bf16 v[88:91], v[44:47], v[44:47], 0\n"
"v_mfma_f32_16x16x32_bf16 v[92:95], v[20:23], v[44:47], 0\n"
"s_waitcnt lgkmcnt(0)\n"
"v_add_f32 v52, v52, v64\n"
"v_add_f32 v53, v53, v65\n"
"v_add_f32 v54, v54, v66\n"
"v_add_f32 v55, v55, v67\n"
"v_cvt_pk_bf16_f32 v42, v52, v53\n"
"v_cvt_pk_bf16_f32 v43, v54, v55\n"
"v_cvt_pk_bf16_f32 v116, v48, v49\n"
"v_cvt_pk_bf16_f32 v118, v50, v51\n"
"v_mfma_f32_16x16x32_bf16 v[88:91], v[40:43], v[40:43], v[88:91]\n"
"v_mfma_f32_16x16x32_bf16 v[92:95], v[20:23], v[40:43], v[92:95]\n"
"v_cvt_pk_bf16_f32 v120, v52, v53\n"
"v_cvt_pk_bf16_f32 v122, v54, v55\n"
"v_cvt_pk_bf16_f32 v124, v56, v57\n"
"v_cvt_pk_bf16_f32 v126, v58, v59\n"
"s_nop 0\n"
"v_cndmask_b32 v101, v88, v90, s[44:45]\n"
"v_cndmask_b32 v88, v89, v91, s[44:45]\n"
"s_nop 0\n"
"v_min_u32 v100, -2, v92\n"
"v_min_u32 v88, -2, v88\n"
"v_min_u32 v101, -2, v101\n"
"v_mov_b32_dpp v102, v100 quad_perm:[1,0,3,2] row_mask:0xf bank_mask:0xf\n"
"v_mov_b32_dpp v103, v88 quad_perm:[1,0,3,2] row_mask:0xf bank_mask:0xf\n"
"buffer_store_dwordx4 v[100:103], v16, s[4:7], s38 offen sc1\n"
"buffer_store_dwordx4 v[116:119], v17, s[4:7], s37 offen offset:0 sc1\n"
"buffer_store_dwordx4 v[120:123], v17, s[4:7], s37 offen offset:1024 sc1\n"
"buffer_store_dwordx4 v[124:127], v18, s[4:7], s37 offen offset:2048 sc1\n"
"ds_write_b32 v4, v4 offset:480\n"
"s_xor_b32 s79, s3, 1\n"
"s_lshl_b32 s79, s79, 8\n"
"s_add_u32 s32, s19, s79\n"
"s_cmp_ge_u32 s32, 64\n"
"s_cbranch_scc1 .Lnr_470\n"
"s_lshl_b32 s79, s19, 11\n"
"s_add_u32 s32, s79, 0x1548000\n"
"s_lshl_b32 s79, s19, 7\n"
"s_add_u32 s33, s79, 0x1588000\n"
"v_lshrrev_b32 v99, 3, v0\n"
"v_and_b32 v98, 7, v0\n"
"v_lshlrev_b32 v98, 4, v98\n"
"v_lshl_add_u32 v40, v99, 14, v98\n"
"v_mov_b32 v41, 0x3727c5ac\n"
"s_sleep 8\n"
"buffer_load_dwordx4 v[48:51], v2, s[4:7], s32 offen offset:0 sc1\n"
"buffer_load_dwordx4 v[52:55], v2, s[4:7], s32 offen offset:1024 sc1\n"
"s_mov_b32 s17, 32768\n"
".Ltp_471:\n"
"s_waitcnt vmcnt(0)\n"
"s_sub_u32 s17, s17, 1\n"
"s_cbranch_scc1 .Lou_472\n"
"v_max_u32 v38, v48, v49\n"
"v_max3_u32 v38, v50, v51, v38\n"
"v_max3_u32 v38, v52, v53, v38\n"
"v_max3_u32 v38, v54, v55, v38\n"
"v_cmp_eq_u32 vcc, -1, v38\n"
"s_cbranch_vccz .Lou_472\n"
"v_max_u32 v38, v50, v51\n"
"v_max3_u32 v38, v48, v49, v38\n"
"v_cmp_eq_u32 vcc, -1, v38\n"
"s_cbranch_vccz .Lsk_473\n"
"s_mov_b64 exec, vcc\n"
"buffer_load_dwordx4 v[48:51], v2, s[4:7], s32 offen offset:0 sc1\n"
"s_mov_b64 exec, -1\n"
".Lsk_473:\n"
"v_max_u32 v38, v54, v55\n"
"v_max3_u32 v38, v52, v53, v38\n"
"v_cmp_eq_u32 vcc, -1, v38\n"
"s_cbranch_vccz .Lsk_474\n"
"s_mov_b64 exec, vcc\n"
"buffer_load_dwordx4 v[52:55], v2, s[4:7], s32 offen offset:1024 sc1\n"
"s_mov_b64 exec, -1\n"
".Lsk_474:\n"
"s_branch .Ltp_471\n"
".Lou_472:\n"
"s_waitcnt vmcnt(0)\n"
"v_add_f32 v48, v48, v52\n"
"v_add_f32 v49, v49, v53\n"
"v_add_f32 v50, v50, v54\n"
"v_add_f32 v51, v51, v55\n"
"v_add_f32_dpp v52, v48, v48 row_ror:8 row_mask:0xf bank_mask:0xf\n"
"v_add_f32_dpp v53, v49, v49 row_ror:8 row_mask:0xf bank_mask:0xf\n"
"v_add_f32_dpp v54, v50, v50 row_ror:8 row_mask:0xf bank_mask:0xf\n"
"v_add_f32_dpp v55, v51, v51 row_ror:8 row_mask:0xf bank_mask:0xf\n"
"v_mov_b32 v48, v52\n"
"v_mov_b32 v49, v53\n"
"v_mov_b32 v50, v54\n"
"v_mov_b32 v51, v55\n"
"v_permlane16_swap_b32 v48, v52\n"
"v_permlane16_swap_b32 v49, v53\n"
"v_permlane16_swap_b32 v50, v54\n"
"v_permlane16_swap_b32 v51, v55\n"
"v_add_f32 v52, v48, v52\n"
"v_add_f32 v53, v49, v53\n"
"v_add_f32 v54, v50, v54\n"
"v_add_f32 v55, v51, v55\n"
"v_mov_b32 v48, v52\n"
"v_mov_b32 v49, v53\n"
"v_mov_b32 v50, v54\n"
"v_mov_b32 v51, v55\n"
"v_permlane32_swap_b32 v48, v52\n"
"v_permlane32_swap_b32 v49, v53\n"
"v_permlane32_swap_b32 v50, v54\n"
"v_permlane32_swap_b32 v51, v55\n"
"v_add_f32 v52, v48, v52\n"
"v_add_f32 v53, v49, v53\n"
"v_add_f32 v54, v50, v54\n"
"v_add_f32 v55, v51, v55\n"
"v_mul_f32 v48, 0x3acccccd, v52\n"
"v_mul_f32 v50, 0x3acccccd, v54\n"
"v_fma_f32 v53, -v52, v48, v53\n"
"v_fma_f32 v55, -v54, v50, v55\n"
"v_fmamk_f32 v53, v53, 0x3acccccd, v41\n"
"v_fmamk_f32 v55, v55, 0x3acccccd, v41\n"
"v_max_f32 v53, v53, v41\n"
"v_max_f32 v55, v55, v41\n"
"v_rsq_f32 v49, v53\n"
"v_rsq_f32 v51, v55\n"
"v_mul_f32 v48, -v48, v49\n"
"v_mul_f32 v50, -v50, v51\n"
"v_min_u32 v49, -2, v49\n"
"v_min_u32 v51, -2, v51\n"
"v_min_u32 v48, -2, v48\n"
"v_min_u32 v50, -2, v50\n"
"buffer_store_dwordx4 v[48:51], v40, s[4:7], s33 offen sc1\n"
"s_nop 3\n"
".Lnr_470:\n"
"s_mul_i32 s79, s28, 0xa00\n"
"s_add_u32 s79, s30, s79\n"
"s_and_b32 s78, s3, 1\n"
"s_mul_i32 s78, s78, 0x600\n"
"s_add_u32 s79, s79, s78\n"
"s_add_u32 s36, s79, 0xc88000\n"
"s_mul_i32 s79, s3, 0x1400\n"
"s_add_u32 s79, s30, s79\n"
"s_add_u32 s32, s79, 0xf08000\n"
"s_add_u32 s33, s32, 0x1000\n"
"s_lshl_b32 s79, s29, 1\n"
"s_lshr_b32 s78, s3, 1\n"
"s_xor_b32 s78, s78, 1\n"
"s_add_u32 s79, s79, s78\n"
"s_mul_i32 s79, s79, 0x5000\n"
"s_mul_i32 s78, s3, 0x1400\n"
"s_add_u32 s79, s79, s78\n"
"s_add_u32 s34, s79, 0xf08000\n"
"s_add_u32 s35, s34, 0x1000\n"
"s_lshl_b32 s79, s30, 1\n"
"s_mul_i32 s78, s28, 0x1400\n"
"s_add_u32 s79, s79, s78\n"
"s_and_b32 s78, s3, 1\n"
"s_mul_i32 s78, s78, 0xc00\n"
"s_add_u32 s79, s79, s78\n"
"s_add_u32 s37, s79, 0x12c8000\n"
"s_lshl_b32 s79, s29, 1\n"
"s_lshr_b32 s78, s3, 1\n"
"s_add_u32 s79, s79, s78\n"
"s_lshl_b32 s79, s79, 11\n"
"s_lshl_b32 s78, s28, 8\n"
"s_add_u32 s79, s79, s78\n"
"s_and_b32 s78, s3, 1\n"
"s_lshl_b32 s78, s78, 7\n"
"s_add_u32 s79, s79, s78\n"
"s_add_u32 s38, s79, 0x1568000\n"
"s_mov_b32 s17, 0x100000\n"
".Ltp_475:\n"
"ds_read_b32 v3, v5 offset:480\n"
"s_sub_u32 s17, s17, 1\n"
"s_cbranch_scc1 .Lou_476\n"
"s_waitcnt lgkmcnt(0)\n"
"v_cmp_eq_u32 vcc, 0, v3\n"
"s_cbranch_vccnz .Ltp_475\n"
".Lou_476:\n"
"s_waitcnt lgkmcnt(0)\n"
"s_mov_b32 s17, 0x100000\n"
".Ltp_477:\n"
"s_sleep 2\n"
"ds_read_b32 v3, v5 offset:4800\n"
"s_sub_u32 s17, s17, 1\n"
"s_cbranch_scc1 .Lou_478\n"
"s_waitcnt lgkmcnt(0)\n"
"v_cmp_eq_u32 vcc, -1, v3\n"
"s_cbranch_vccnz .Ltp_477\n"
".Lou_478:\n"
"s_waitcnt lgkmcnt(0)\n"
"s_mov_b32 s17, 32768\n"
".Ltp_479:\n"
"buffer_load_dwordx2 v[40:41], v14, s[4:7], s36 offen offset:0 sc1\n"
"buffer_load_dwordx2 v[42:43], v14, s[4:7], s36 offen offset:512 sc1\n"
"buffer_load_dwordx2 v[44:45], v15, s[4:7], s36 offen offset:1024 sc1\n"
"s_sub_u32 s17, s17, 1\n"
"s_cbranch_scc1 .Lou_480\n"
"s_waitcnt vmcnt(1)\n"
"v_max3_u32 v38, v40, v41, v42\n"
"s_waitcnt vmcnt(0)\n"
"v_max3_u32 v38, v43, v44, v38\n"
"v_max_u32 v38, v45, v38\n"
"v_cmp_eq_u32 vcc, -1, v38\n"
"s_cbranch_vccnz .Ltp_479\n"
".Lou_480:\n"
"s_waitcnt vmcnt(0)\n"
"s_mov_b32 s17, 0x100000\n"
".Ltp_481:\n"
"s_sleep 1\n"
"ds_read_b32 v3, v5 offset:464\n"
"s_sub_u32 s17, s17, 1\n"
"s_cbranch_scc1 .Lou_482\n"
"s_waitcnt lgkmcnt(0)\n"
"v_cmp_eq_u32 vcc, 0, v3\n"
"s_cbranch_vccnz .Ltp_481\n"
".Lou_482:\n"
"s_waitcnt lgkmcnt(0)\n"
"s_sleep 6\n"
"s_cmp_eq_u32 s17, 0xfffff\n"
"s_cbranch_scc1 .Lsk_483\n"
"s_sleep 14\n"
".Lsk_483:\n"
"buffer_load_dwordx4 v[108:111], v2, s[4:7], s34 offen offset:0 sc1\n"
"s_nop 6\n"
"buffer_load_dwordx4 v[112:115], v2, s[4:7], s34 offen offset:1024 sc1\n"
"s_nop 6\n"
"buffer_load_dwordx4 v[116:119], v2, s[4:7], s34 offen offset:2048 sc1\n"
"s_nop 6\n"
"buffer_load_dwordx4 v[120:123], v2, s[4:7], s34 offen offset:3072 sc1\n"
"s_nop 6\n"
"buffer_load_dwordx4 v[124:127], v2, s[4:7], s35 offen offset:0 sc1\n"
"s_nop 6\n"
"buffer_load_dwordx4 v[88:91], v2, s[4:7], s32 offen offset:0 sc1\n"
"s_nop 6\n"
"buffer_load_dwordx4 v[92:95], v2, s[4:7], s32 offen offset:1024 sc1\n"
"s_nop 6\n"
"buffer_load_dwordx4 v[96:99], v2, s[4:7], s32 offen offset:2048 sc1\n"
"s_nop 6\n"
"buffer_load_dwordx4 v[100:103], v2, s[4:7], s32 offen offset:3072 sc1\n"
"s_nop 6\n"
"buffer_load_dwordx4 v[104:107], v2, s[4:7], s33 offen offset:0 sc1\n"
"v_lshlrev_b32 v48, 16, v40\n"
"v_and_b32 v49, 0xffff0000, v40\n"
"v_lshlrev_b32 v50, 16, v41\n"
"v_and_b32 v51, 0xffff0000, v41\n"
"v_lshlrev_b32 v52, 16, v42\n"
"v_and_b32 v53, 0xffff0000, v42\n"
"v_lshlrev_b32 v54, 16, v43\n"
"v_and_b32 v55, 0xffff0000, v43\n"
"v_lshlrev_b32 v56, 16, v44\n"
"v_and_b32 v57, 0xffff0000, v44\n"
"v_lshlrev_b32 v58, 16, v45\n"
"v_and_b32 v59, 0xffff0000, v45\n"
"v_mov_b32 v46, 0\n"
"v_mov_b32 v47, 0\n"
"s_waitcnt vmcnt(9)\n"
"v_max_u32 v38, v108, v109\n"
"v_max3_u32 v38, v110, v111, v38\n"
"v_cmp_eq_u32 vcc, -1, v38\n"
"s_cbranch_vccnz .Lsl_484\n"
".Lbk_485:\n"
"v_mfma_f32_16x16x32_bf16 v[68:71], v[156:159], v[108:111], 0\n"
"v_mfma_f32_16x16x32_bf16 v[72:75], v[160:163], v[108:111], 0\n"
"v_mfma_f32_16x16x32_bf16 v[76:79], v[164:167], v[108:111], 0\n"
"s_waitcnt vmcnt(8)\n"
"v_max_u32 v38, v112, v113\n"
"v_max3_u32 v38, v114, v115, v38\n"
"v_mfma_f32_16x16x32_bf16 v[80:83], v[168:171], v[108:111], 0\n"
"v_cmp_eq_u32 vcc, -1, v38\n"
"v_mfma_f32_16x16x32_bf16 v[84:87], v[172:175], v[108:111], 0\n"
"s_cbranch_vccnz .Lsl_486\n"
".Lbk_487:\n"
"v_mfma_f32_16x16x32_bf16 v[68:71], v[176:179], v[112:115], v[68:71]\n"
"v_mfma_f32_16x16x32_bf16 v[72:75], v[180:183], v[112:115], v[72:75]\n"
"v_mfma_f32_16x16x32_bf16 v[76:79], v[184:187], v[112:115], v[76:79]\n"
"s_waitcnt vmcnt(7)\n"
"v_max_u32 v38, v116, v117\n"
"v_max3_u32 v38, v118, v119, v38\n"
"v_mfma_f32_16x16x32_bf16 v[80:83], v[188:191], v[112:115], v[80:83]\n"
"v_cmp_eq_u32 vcc, -1, v38\n"
"v_mfma_f32_16x16x32_bf16 v[84:87], v[192:195], v[112:115], v[84:87]\n"
"s_cbranch_vccnz .Lsl_488\n"
".Lbk_489:\n"
"v_mfma_f32_16x16x32_bf16 v[68:71], v[196:199], v[116:119], v[68:71]\n"
"v_mfma_f32_16x16x32_bf16 v[72:75], v[200:203], v[116:119], v[72:75]\n"
"v_mfma_f32_16x16x32_bf16 v[76:79], v[204:207], v[116:119], v[76:79]\n"
"s_waitcnt vmcnt(6)\n"
"v_max_u32 v38, v120, v121\n"
"v_max3_u32 v38, v122, v123, v38\n"
"v_mfma_f32_16x16x32_bf16 v[80:83], v[208:211], v[116:119], v[80:83]\n"
"v_cmp_eq_u32 vcc, -1, v38\n"
"v_mfma_f32_16x16x32_bf16 v[84:87], v[212:215], v[116:119], v[84:87]\n"
"s_cbranch_vccnz .Lsl_490\n"
".Lbk_491:\n"
"v_mfma_f32_16x16x32_bf16 v[68:71], v[216:219], v[120:123], v[68:71]\n"
"v_mfma_f32_16x16x32_bf16 v[72:75], v[220:223], v[120:123], v[72:75]\n"
"v_mfma_f32_16x16x32_bf16 v[76:79], v[224:227], v[120:123], v[76:79]\n"
"s_waitcnt vmcnt(5)\n"
"v_max_u32 v38, v124, v125\n"
"v_max3_u32 v38, v126, v127, v38\n"
"v_mfma_f32_16x16x32_bf16 v[80:83], v[228:231], v[120:123], v[80:83]\n"
"v_cmp_eq_u32 vcc, -1, v38\n"
"v_mfma_f32_16x16x32_bf16 v[84:87], v[232:235], v[120:123], v[84:87]\n"
"s_cbranch_vccnz .Lsl_492\n"
".Lbk_493:\n"
"v_mfma_f32_16x16x32_bf16 v[68:71], v[236:239], v[124:127], v[68:71]\n"
"v_mfma_f32_16x16x32_bf16 v[72:75], v[240:243], v[124:127], v[72:75]\n"
"v_mfma_f32_16x16x32_bf16 v[76:79], v[244:247], v[124:127], v[76:79]\n"
"s_waitcnt vmcnt(4)\n"
"v_max_u32 v38, v88, v89\n"
"v_max3_u32 v38, v90, v91, v38\n"
"v_mfma_f32_16x16x32_bf16 v[80:83], v[248:251], v[124:127], v[80:83]\n"
"v_cmp_eq_u32 vcc, -1, v38\n"
"v_mfma_f32_16x16x32_bf16 v[84:87], v[252:255], v[124:127], v[84:87]\n"
"ds_write_b128 v8, v[68:71] offset:0\n"
"s_cbranch_vccnz .Lsl_494\n"
".Lbk_495:\n"
"v_mfma_f32_16x16x32_bf16 v[48:51], v[156:159], v[88:91], v[48:51]\n"
"ds_write_b128 v8, v[72:75] offset:1024\n"
"v_mfma_f32_16x16x32_bf16 v[52:55], v[160:163], v[88:91], v[52:55]\n"
"ds_write_b128 v10, v[76:79] offset:0\n"
"v_mfma_f32_16x16x32_bf16 v[56:59], v[164:167], v[88:91], v[56:59]\n"
"s_waitcnt vmcnt(3)\n"
"v_max_u32 v38, v92, v93\n"
"v_max3_u32 v38, v94, v95, v38\n"
"v_mfma_f32_16x16x32_bf16 v[60:63], v[168:171], v[88:91], 0\n"
"v_cmp_eq_u32 vcc, -1, v38\n"
"v_mov_b32 v117, s16\n"
"v_mfma_f32_16x16x32_bf16 v[64:67], v[172:175], v[88:91], 0\n"
"ds_write_b128 v9, v[80:83] offset:0\n"
"s_cbranch_vccnz .Lsl_496\n"
".Lbk_497:\n"
"v_mfma_f32_16x16x32_bf16 v[48:51], v[176:179], v[92:95], v[48:51]\n"
"ds_write_b128 v9, v[84:87] offset:1024\n"
"v_mfma_f32_16x16x32_bf16 v[52:55], v[180:183], v[92:95], v[52:55]\n"
"ds_write_b32 v4, v4 offset:688\n"
"v_mfma_f32_16x16x32_bf16 v[56:59], v[184:187], v[92:95], v[56:59]\n"
"s_waitcnt vmcnt(2)\n"
"v_max_u32 v38, v96, v97\n"
"v_max3_u32 v38, v98, v99, v38\n"
"v_mfma_f32_16x16x32_bf16 v[60:63], v[188:191], v[92:95], v[60:63]\n"
"v_cmp_eq_u32 vcc, -1, v38\n"
"v_mov_b32 v119, s16\n"
"v_mfma_f32_16x16x32_bf16 v[64:67], v[192:195], v[92:95], v[64:67]\n"
"s_cbranch_vccnz .Lsl_498\n"
".Lbk_499:\n"
"v_mfma_f32_16x16x32_bf16 v[48:51], v[196:199], v[96:99], v[48:51]\n"
"v_mov_b32 v121, s16\n"
"v_mov_b32 v123, s16\n"
"v_mfma_f32_16x16x32_bf16 v[52:55], v[200:203], v[96:99], v[52:55]\n"
"v_mov_b32 v125, s16\n"
"v_mov_b32 v127, s16\n"
"v_mfma_f32_16x16x32_bf16 v[56:59], v[204:207], v[96:99], v[56:59]\n"
"s_waitcnt vmcnt(1)\n"
"v_max_u32 v38, v100, v101\n"
"v_max3_u32 v38, v102, v103, v38\n"
"v_mfma_f32_16x16x32_bf16 v[60:63], v[208:211], v[96:99], v[60:63]\n"
"v_cmp_eq_u32 vcc, -1, v38\n"
"ds_read_b32 v3, v5 offset:688\n"
"ds_read_b128 v[76:79], v12 offset:2048\n"
"v_mfma_f32_16x16x32_bf16 v[64:67], v[212:215], v[96:99], v[64:67]\n"
"ds_read_b128 v[108:111], v13 offset:2048\n"
"s_cbranch_vccnz .Lsl_500\n"
".Lbk_501:\n"
"v_mfma_f32_16x16x32_bf16 v[56:59], v[224:227], v[100:103], v[56:59]\n"
"s_waitcnt vmcnt(0)\n"
"v_max_u32 v38, v104, v105\n"
"v_max3_u32 v38, v106, v107, v38\n"
"ds_read_b128 v[68:71], v12 offset:0\n"
"v_mfma_f32_16x16x32_bf16 v[60:63], v[228:231], v[100:103], v[60:63]\n"
"v_cmp_eq_u32 vcc, -1, v38\n"
"ds_read_b128 v[80:83], v13 offset:0\n"
"v_mfma_f32_16x16x32_bf16 v[64:67], v[232:235], v[100:103], v[64:67]\n"
"s_cbranch_vccnz .Lsl_502\n"
".Lbk_503:\n"
"v_mfma_f32_16x16x32_bf16 v[56:59], v[244:247], v[104:107], v[56:59]\n"
"ds_read_b128 v[72:75], v12 offset:1024\n"
"ds_read_b128 v[84:87], v13 offset:1024\n"
"v_mfma_f32_16x16x32_bf16 v[60:63], v[248:251], v[104:107], v[60:63]\n"
"v_mfma_f32_16x16x32_bf16 v[64:67], v[252:255], v[104:107], v[64:67]\n"
"v_mfma_f32_16x16x32_bf16 v[48:51], v[216:219], v[100:103], v[48:51]\n"
"ds_write_b128 v7, v[56:59] offset:0\n"
"v_mfma_f32_16x16x32_bf16 v[52:55], v[220:223], v[100:103], v[52:55]\n"
"ds_write_b128 v6, v[60:63] offset:0\n"
"v_mfma_f32_16x16x32_bf16 v[48:51], v[236:239], v[104:107], v[48:51]\n"
"ds_write_b128 v6, v[64:67] offset:1024\n"
"v_mfma_f32_16x16x32_bf16 v[52:55], v[240:243], v[104:107], v[52:55]\n"
"ds_write_b32 v4, v4 offset:704\n"
"s_mov_b32 s17, 0x100000\n"
"s_waitcnt lgkmcnt(10)\n"
"v_cmp_eq_u32 vcc, 0, v3\n"
"s_cbranch_vccz .Lok_505\n"
".Ltp_504:\n"
"ds_read_b32 v3, v5 offset:688\n"
"ds_read_b128 v[76:79], v12 offset:2048\n"
"ds_read_b128 v[108:111], v13 offset:2048\n"
"ds_read_b128 v[68:71], v12 offset:0\n"
"ds_read_b128 v[80:83], v13 offset:0\n"
"ds_read_b128 v[72:75], v12 offset:1024\n"
"ds_read_b128 v[84:87], v13 offset:1024\n"
"s_sub_u32 s17, s17, 1\n"
"s_cbranch_scc1 .Lok_505\n"
"s_waitcnt lgkmcnt(6)\n"
"v_cmp_eq_u32 vcc, 0, v3\n"
"s_cbranch_vccnz .Ltp_504\n"
"s_waitcnt lgkmcnt(0)\n"
".Lok_505:\n"
"s_waitcnt lgkmcnt(8)\n"
"v_add_f32 v76, v76, v108\n"
"v_add_f32 v77, v77, v109\n"
"v_add_f32 v78, v78, v110\n"
"v_add_f32 v79, v79, v111\n"
"ds_read_b32 v3, v5 offset:704\n"
"ds_read_b128 v[112:115], v11 offset:2048\n"
"ds_read_b128 v[60:63], v11 offset:0\n"
"ds_read_b128 v[64:67], v11 offset:1024\n"
"s_waitcnt lgkmcnt(10)\n"
"v_add_f32 v68, v68, v80\n"
"v_add_f32 v69, v69, v81\n"
"v_add_f32 v70, v70, v82\n"
"v_add_f32 v71, v71, v83\n"
"s_waitcnt lgkmcnt(8)\n"
"v_add_f32 v72, v72, v84\n"
"v_add_f32 v73, v73, v85\n"
"v_add_f32 v74, v74, v86\n"
"v_add_f32 v75, v75, v87\n"
"v_add_f32 v56, v56, v76\n"
"v_add_f32 v57, v57, v77\n"
"v_add_f32 v58, v58, v78\n"
"v_add_f32 v59, v59, v79\n"
"v_add_f32 v48, v48, v68\n"
"v_add_f32 v49, v49, v69\n"
"v_add_f32 v50, v50, v70\n"
"v_add_f32 v51, v51, v71\n"
"v_add_f32 v52, v52, v72\n"
"v_add_f32 v53, v53, v73\n"
"v_add_f32 v54, v54, v74\n"
"v_add_f32 v55, v55, v75\n"
"s_mov_b32 s17, 0x100000\n"
"s_waitcnt lgkmcnt(3)\n"
"v_cmp_eq_u32 vcc, 0, v3\n"
"s_cbranch_vccz .Lok_507\n"
".Ltp_506:\n"
"ds_read_b32 v3, v5 offset:704\n"
"ds_read_b128 v[112:115], v11 offset:2048\n"
"ds_read_b128 v[60:63], v11 offset:0\n"
"ds_read_b128 v[64:67], v11 offset:1024\n"
"s_sub_u32 s17, s17, 1\n"
"s_cbranch_scc1 .Lok_507\n"
"s_waitcnt lgkmcnt(3)\n"
"v_cmp_eq_u32 vcc, 0, v3\n"
"s_cbranch_vccnz .Ltp_506\n"
"s_waitcnt lgkmcnt(0)\n"
".Lok_507:\n"
"s_waitcnt lgkmcnt(2)\n"
"v_add_f32 v56, v56, v112\n"
"v_add_f32 v57, v57, v113\n"
"v_add_f32 v58, v58, v114\n"
"v_add_f32 v59, v59, v115\n"
"v_cvt_pk_bf16_f32 v44, v56, v57\n"
"v_cvt_pk_bf16_f32 v45, v58, v59\n"
"v_and_b32 v44, v19, v44\n"
"v_and_b32 v45, v19, v45\n"
"s_waitcnt lgkmcnt(1)\n"
"v_add_f32 v48, v48, v60\n"
"v_add_f32 v49, v49, v61\n"
"v_add_f32 v50, v50, v62\n"
"v_add_f32 v51, v51, v63\n"
"v_cvt_pk_bf16_f32 v40, v48, v49\n"
"v_cvt_pk_bf16_f32 v41, v50, v51\n"
"v_mfma_f32_16x16x32_bf16 v[88:91], v[44:47], v[44:47], 0\n"
"v_mfma_f32_16x16x32_bf16 v[92:95], v[20:23], v[44:47], 0\n"
"s_waitcnt lgkmcnt(0)\n"
"v_add_f32 v52, v52, v64\n"
"v_add_f32 v53, v53, v65\n"
"v_add_f32 v54, v54, v66\n"
"v_add_f32 v55, v55, v67\n"
"v_cvt_pk_bf16_f32 v42, v52, v53\n"
"v_cvt_pk_bf16_f32 v43, v54, v55\n"
"v_cvt_pk_bf16_f32 v116, v48, v49\n"
"v_cvt_pk_bf16_f32 v118, v50, v51\n"
"v_mfma_f32_16x16x32_bf16 v[88:91], v[40:43], v[40:43], v[88:91]\n"
"v_mfma_f32_16x16x32_bf16 v[92:95], v[20:23], v[40:43], v[92:95]\n"
"v_cvt_pk_bf16_f32 v120, v52, v53\n"
"v_cvt_pk_bf16_f32 v122, v54, v55\n"
"v_cvt_pk_bf16_f32 v124, v56, v57\n"
"v_cvt_pk_bf16_f32 v126, v58, v59\n"
"s_nop 0\n"
"v_cndmask_b32 v101, v88, v90, s[44:45]\n"
"v_cndmask_b32 v88, v89, v91, s[44:45]\n"
"s_nop 0\n"
"v_min_u32 v100, -2, v92\n"
"v_min_u32 v88, -2, v88\n"
"v_min_u32 v101, -2, v101\n"
"v_mov_b32_dpp v102, v100 quad_perm:[1,0,3,2] row_mask:0xf bank_mask:0xf\n"
"v_mov_b32_dpp v103, v88 quad_perm:[1,0,3,2] row_mask:0xf bank_mask:0xf\n"
"buffer_store_dwordx4 v[100:103], v16, s[4:7], s38 offen sc1\n"
"buffer_store_dwordx4 v[116:119], v17, s[4:7], s37 offen offset:0 sc1\n"
"buffer_store_dwordx4 v[120:123], v17, s[4:7], s37 offen offset:1024 sc1\n"
"buffer_store_dwordx4 v[124:127], v18, s[4:7], s37 offen offset:2048 sc1\n"
"ds_write_b32 v4, v4 offset:720\n"
"s_xor_b32 s79, s3, 1\n"
"s_lshl_b32 s79, s79, 8\n"
"s_add_u32 s32, s19, s79\n"
"s_cmp_ge_u32 s32, 64\n"
"s_cbranch_scc1 .Lnr_508\n"
"s_lshl_b32 s79, s19, 11\n"
"s_add_u32 s32, s79, 0x1568000\n"
"s_lshl_b32 s79, s19, 7\n"
"s_add_u32 s33, s79, 0x158a000\n"
"v_lshrrev_b32 v99, 3, v0\n"
"v_and_b32 v98, 7, v0\n"
"v_lshlrev_b32 v98, 4, v98\n"
"v_lshl_add_u32 v40, v99, 14, v98\n"
"v_mov_b32 v41, 0x3727c5ac\n"
"s_sleep 8\n"
"buffer_load_dwordx4 v[48:51], v2, s[4:7], s32 offen offset:0 sc1\n"
"buffer_load_dwordx4 v[52:55], v2, s[4:7], s32 offen offset:1024 sc1\n"
"s_mov_b32 s17, 32768\n"
".Ltp_509:\n"
"s_waitcnt vmcnt(0)\n"
"s_sub_u32 s17, s17, 1\n"
"s_cbranch_scc1 .Lou_510\n"
"v_max_u32 v38, v48, v49\n"
"v_max3_u32 v38, v50, v51, v38\n"
"v_max3_u32 v38, v52, v53, v38\n"
"v_max3_u32 v38, v54, v55, v38\n"
"v_cmp_eq_u32 vcc, -1, v38\n"
"s_cbranch_vccz .Lou_510\n"
"v_max_u32 v38, v50, v51\n"
"v_max3_u32 v38, v48, v49, v38\n"
"v_cmp_eq_u32 vcc, -1, v38\n"
"s_cbranch_vccz .Lsk_511\n"
"s_mov_b64 exec, vcc\n"
"buffer_load_dwordx4 v[48:51], v2, s[4:7], s32 offen offset:0 sc1\n"
"s_mov_b64 exec, -1\n"
".Lsk_511:\n"
"v_max_u32 v38, v54, v55\n"
"v_max3_u32 v38, v52, v53, v38\n"
"v_cmp_eq_u32 vcc, -1, v38\n"
"s_cbranch_vccz .Lsk_512\n"
"s_mov_b64 exec, vcc\n"
"buffer_load_dwordx4 v[52:55], v2, s[4:7], s32 offen offset:1024 sc1\n"
"s_mov_b64 exec, -1\n"
".Lsk_512:\n"
"s_branch .Ltp_509\n"
".Lou_510:\n"
"s_waitcnt vmcnt(0)\n"
"v_add_f32 v48, v48, v52\n"
"v_add_f32 v49, v49, v53\n"
"v_add_f32 v50, v50, v54\n"
"v_add_f32 v51, v51, v55\n"
"v_add_f32_dpp v52, v48, v48 row_ror:8 row_mask:0xf bank_mask:0xf\n"
"v_add_f32_dpp v53, v49, v49 row_ror:8 row_mask:0xf bank_mask:0xf\n"
"v_add_f32_dpp v54, v50, v50 row_ror:8 row_mask:0xf bank_mask:0xf\n"
"v_add_f32_dpp v55, v51, v51 row_ror:8 row_mask:0xf bank_mask:0xf\n"
"v_mov_b32 v48, v52\n"
"v_mov_b32 v49, v53\n"
"v_mov_b32 v50, v54\n"
"v_mov_b32 v51, v55\n"
"v_permlane16_swap_b32 v48, v52\n"
"v_permlane16_swap_b32 v49, v53\n"
"v_permlane16_swap_b32 v50, v54\n"
"v_permlane16_swap_b32 v51, v55\n"
"v_add_f32 v52, v48, v52\n"
"v_add_f32 v53, v49, v53\n"
"v_add_f32 v54, v50, v54\n"
"v_add_f32 v55, v51, v55\n"
"v_mov_b32 v48, v52\n"
"v_mov_b32 v49, v53\n"
"v_mov_b32 v50, v54\n"
"v_mov_b32 v51, v55\n"
"v_permlane32_swap_b32 v48, v52\n"
"v_permlane32_swap_b32 v49, v53\n"
"v_permlane32_swap_b32 v50, v54\n"
"v_permlane32_swap_b32 v51, v55\n"
"v_add_f32 v52, v48, v52\n"
"v_add_f32 v53, v49, v53\n"
"v_add_f32 v54, v50, v54\n"
"v_add_f32 v55, v51, v55\n"
"v_mul_f32 v48, 0x3acccccd, v52\n"
"v_mul_f32 v50, 0x3acccccd, v54\n"
"v_fma_f32 v53, -v52, v48, v53\n"
"v_fma_f32 v55, -v54, v50, v55\n"
"v_fmamk_f32 v53, v53, 0x3acccccd, v41\n"
"v_fmamk_f32 v55, v55, 0x3acccccd, v41\n"
"v_max_f32 v53, v53, v41\n"
"v_max_f32 v55, v55, v41\n"
"v_rsq_f32 v49, v53\n"
"v_rsq_f32 v51, v55\n"
"v_mul_f32 v48, -v48, v49\n"
"v_mul_f32 v50, -v50, v51\n"
"v_min_u32 v49, -2, v49\n"
"v_min_u32 v51, -2, v51\n"
"v_min_u32 v48, -2, v48\n"
"v_min_u32 v50, -2, v50\n"
"buffer_store_dwordx4 v[48:51], v40, s[4:7], s33 offen sc1\n"
"s_nop 3\n"
".Lnr_508:\n"
"s_cmp_lt_i32 s18, 0\n"
"s_cbranch_scc1 .Lnc_513\n"
"s_mov_b32 s17, 0x100000\n"
".Ltp_514:\n"
"s_sleep 1\n"
"ds_read_b32 v3, v5 offset:496\n"
"s_sub_u32 s17, s17, 1\n"
"s_cbranch_scc1 .Lou_515\n"
"s_waitcnt lgkmcnt(0)\n"
"v_cmp_eq_u32 vcc, 0, v3\n"
"s_cbranch_vccnz .Ltp_514\n"
".Lou_515:\n"
"s_waitcnt lgkmcnt(0)\n"
"s_setprio 0\n"
"s_lshl_b32 s79, s18, 8\n"
"s_add_u32 s32, s79, 0x12c8000\n"
"s_add_u32 s33, s32, 0x140000\n"
"s_and_b32 s79, s2, 7\n"
"s_lshl_b32 s79, s79, 14\n"
"s_add_u32 s34, s79, 0x158a000\n"
"s_add_u32 s35, s34, 0x1000\n"
"v_and_b32 v126, 0xffffff80, v29\n"
"v_add_u32 v126, v126, v29\n"
"ds_read_b128 v[88:91], v25 offset:448\n"
"ds_read_b128 v[92:95], v25 offset:432\n"
"ds_read_b64 v[96:97], v24 offset:448\n"
"ds_read_b64 v[98:99], v24 offset:456\n"
"ds_read_b128 v[100:103], v25 offset:960\n"
"ds_read_b128 v[104:107], v25 offset:1472\n"
"s_waitcnt lgkmcnt(4)\n"
"v_mfma_f32_16x16x32_f16 v[64:67], v[88:91], v[144:147], 0\n"
"ds_read_b128 v[40:43], v25 offset:1984\n"
"ds_read_b64 v[44:45], v24 offset:960\n"
"v_mfma_f32_16x16x32_f16 v[68:71], v[92:95], v[148:151], 0\n"
"ds_read_b64 v[46:47], v24 offset:968\n"
"ds_read_b64 v[72:73], v24 offset:1472\n"
"v_mfma_f32_16x16x32_f16 v[48:51], v[152:155], v[88:91], 0\n"
"ds_read_b64 v[74:75], v24 offset:1480\n"
"s_waitcnt lgkmcnt(4)\n"
"v_mfma_f32_16x16x32_f16 v[48:51], v[96:99], v[100:103], v[48:51]\n"
"ds_read_b64 v[76:77], v24 offset:384\n"
"ds_read_b64 v[78:79], v24 offset:392\n"
"v_mfma_f32_16x16x32_f16 v[52:55], v[96:99], v[104:107], 0\n"
"ds_read_b128 v[84:87], v25 offset:896\n"
"ds_read_b128 v[92:95], v25 offset:1408\n"
"v_mfma_f32_16x16x32_f16 v[56:59], v[96:99], v[40:43], 0\n"
"ds_read_b128 v[88:91], v25 offset:1920\n"
"s_waitcnt lgkmcnt(5)\n"
"v_mfma_f32_16x16x32_f16 v[52:55], v[44:47], v[40:43], v[52:55]\n"
"ds_read_b64 v[100:101], v24 offset:896\n"
"ds_read_b64 v[102:103], v24 offset:904\n"
"v_mfma_f32_16x16x32_f16 v[48:51], v[44:47], v[104:107], v[48:51]\n"
"ds_read_b64 v[96:97], v24 offset:1408\n"
"v_cvt_pk_f16_f32 v80, v64, v65\n"
"v_mfma_f32_16x16x32_f16 v[48:51], v[72:75], v[40:43], v[48:51]\n"
"ds_read_b64 v[98:99], v24 offset:1416\n"
"s_waitcnt lgkmcnt(4)\n"
"v_mfma_f32_16x16x32_f16 v[48:51], v[76:79], v[84:87], v[48:51]\n"
"ds_read_b128 v[104:107], v25 offset:960\n"
"v_cvt_pk_f16_f32 v81, v66, v67\n"
"v_mfma_f32_16x16x32_f16 v[52:55], v[76:79], v[92:95], v[52:55]\n"
"ds_read_b128 v[44:47], v25 offset:944\n"
"v_cvt_pk_f16_f32 v82, v68, v69\n"
"v_mfma_f32_16x16x32_f16 v[56:59], v[76:79], v[88:91], v[56:59]\n"
"v_cvt_pk_f16_f32 v83, v70, v71\n"
"s_waitcnt lgkmcnt(2)\n"
"v_mfma_f32_16x16x32_f16 v[52:55], v[100:103], v[88:91], v[52:55]\n"
"ds_read_b64 v[40:41], v24 offset:320\n"
"ds_read_b64 v[42:43], v24 offset:328\n"
"v_mfma_f32_16x16x32_f16 v[48:51], v[100:103], v[92:95], v[48:51]\n"
"ds_read_b128 v[72:75], v25 offset:832\n"
"v_mfma_f32_16x16x32_f16 v[48:51], v[96:99], v[88:91], v[48:51]\n"
"ds_read_b128 v[84:87], v25 offset:1344\n"
"v_mfma_f32_16x16x32_f16 v[48:51], v[80:83], v[128:131], v[48:51]\n"
"ds_read_b128 v[76:79], v25 offset:1856\n"
"ds_read_b64 v[92:93], v24 offset:832\n"
"v_mfma_f32_16x16x32_f16 v[52:55], v[80:83], v[132:135], v[52:55]\n"
"ds_read_b64 v[94:95], v24 offset:840\n"
"v_mfma_f32_16x16x32_f16 v[56:59], v[80:83], v[136:139], v[56:59]\n"
"ds_read_b64 v[100:101], v24 offset:1344\n"
"s_waitcnt lgkmcnt(8)\n"
"v_mfma_f32_16x16x32_f16 v[60:63], v[80:83], v[140:143], 0\n"
"ds_read_b64 v[102:103], v24 offset:1352\n"
"ds_read_b64 v[88:89], v24 offset:256\n"
"v_mfma_f32_16x16x32_f16 v[64:67], v[104:107], v[144:147], 0\n"
"ds_read_b64 v[90:91], v24 offset:264\n"
"v_mfma_f32_16x16x32_f16 v[68:71], v[44:47], v[148:151], 0\n"
"ds_read_b128 v[96:99], v25 offset:768\n"
"s_waitcnt lgkmcnt(8)\n"
"v_mfma_f32_16x16x32_f16 v[52:55], v[152:155], v[104:107], v[52:55]\n"
"ds_read_b128 v[44:47], v25 offset:1280\n"
"v_mfma_f32_16x16x32_f16 v[48:51], v[40:43], v[72:75], v[48:51]\n"
"ds_read_b128 v[104:107], v25 offset:1792\n"
"ds_read_b64 v[72:73], v24 offset:768\n"
"v_mfma_f32_16x16x32_f16 v[52:55], v[40:43], v[84:87], v[52:55]\n"
"ds_read_b64 v[74:75], v24 offset:776\n"
"s_waitcnt lgkmcnt(9)\n"
"v_mfma_f32_16x16x32_f16 v[56:59], v[40:43], v[76:79], v[56:59]\n"
"ds_read_b64 v[40:41], v24 offset:1280\n"
"v_mfma_f32_16x16x32_f16 v[52:55], v[92:95], v[76:79], v[52:55]\n"
"ds_read_b64 v[42:43], v24 offset:1288\n"
"v_cvt_pk_f16_f32 v80, v64, v65\n"
"v_mfma_f32_16x16x32_f16 v[48:51], v[92:95], v[84:87], v[48:51]\n"
"v_cvt_pk_f16_f32 v81, v66, v67\n"
"s_waitcnt lgkmcnt(5)\n"
"v_mfma_f32_16x16x32_f16 v[48:51], v[100:103], v[76:79], v[48:51]\n"
"ds_read_b128 v[84:87], v25 offset:1472\n"
"v_cvt_pk_f16_f32 v82, v68, v69\n"
"v_mfma_f32_16x16x32_f16 v[48:51], v[88:91], v[96:99], v[48:51]\n"
"ds_read_b128 v[92:95], v25 offset:1456\n"
"v_cvt_pk_f16_f32 v83, v70, v71\n"
"v_mfma_f32_16x16x32_f16 v[52:55], v[88:91], v[44:47], v[52:55]\n"
"s_waitcnt lgkmcnt(4)\n"
"v_mfma_f32_16x16x32_f16 v[56:59], v[88:91], v[104:107], v[56:59]\n"
"ds_read_b64 v[76:77], v24 offset:192\n"
"ds_read_b64 v[78:79], v24 offset:200\n"
"v_mfma_f32_16x16x32_f16 v[52:55], v[72:75], v[104:107], v[52:55]\n"
"ds_read_b128 v[100:103], v25 offset:704\n"
"v_mfma_f32_16x16x32_f16 v[48:51], v[72:75], v[44:47], v[48:51]\n"
"ds_read_b128 v[96:99], v25 offset:1216\n"
"s_waitcnt lgkmcnt(6)\n"
"v_mfma_f32_16x16x32_f16 v[48:51], v[40:43], v[104:107], v[48:51]\n"
"ds_read_b128 v[88:91], v25 offset:1728\n"
"ds_read_b64 v[44:45], v24 offset:704\n"
"v_mfma_f32_16x16x32_f16 v[52:55], v[80:83], v[128:131], v[52:55]\n"
"ds_read_b64 v[46:47], v24 offset:712\n"
"v_mfma_f32_16x16x32_f16 v[56:59], v[80:83], v[132:135], v[56:59]\n"
"ds_read_b64 v[72:73], v24 offset:1216\n"
"s_waitcnt lgkmcnt(8)\n"
"v_mfma_f32_16x16x32_f16 v[60:63], v[80:83], v[136:139], v[60:63]\n"
"ds_read_b64 v[74:75], v24 offset:1224\n"
"ds_read_b64 v[104:105], v24 offset:128\n"
"v_mfma_f32_16x16x32_f16 v[64:67], v[84:87], v[144:147], 0\n"
"ds_read_b64 v[106:107], v24 offset:136\n"
"v_mfma_f32_16x16x32_f16 v[68:71], v[92:95], v[148:151], 0\n"
"ds_read_b128 v[40:43], v25 offset:640\n"
"s_waitcnt lgkmcnt(8)\n"
"v_mfma_f32_16x16x32_f16 v[56:59], v[152:155], v[84:87], v[56:59]\n"
"ds_read_b128 v[92:95], v25 offset:1152\n"
"v_mfma_f32_16x16x32_f16 v[48:51], v[76:79], v[100:103], v[48:51]\n"
"ds_read_b128 v[84:87], v25 offset:1664\n"
"ds_read_b64 v[100:101], v24 offset:640\n"
"v_mfma_f32_16x16x32_f16 v[52:55], v[76:79], v[96:99], v[52:55]\n"
"ds_read_b64 v[102:103], v24 offset:648\n"
"s_waitcnt lgkmcnt(9)\n"
"v_mfma_f32_16x16x32_f16 v[56:59], v[76:79], v[88:91], v[56:59]\n"
"ds_read_b64 v[76:77], v24 offset:1152\n"
"v_mfma_f32_16x16x32_f16 v[52:55], v[44:47], v[88:91], v[52:55]\n"
"ds_read_b64 v[78:79], v24 offset:1160\n"
"v_cvt_pk_f16_f32 v80, v64, v65\n"
"v_mfma_f32_16x16x32_f16 v[48:51], v[44:47], v[96:99], v[48:51]\n"
"ds_read_b128 v[96:99], v25 offset:1984\n"
"s_waitcnt lgkmcnt(6)\n"
"v_mfma_f32_16x16x32_f16 v[48:51], v[72:75], v[88:91], v[48:51]\n"
"ds_read_b128 v[44:47], v25 offset:1968\n"
"v_cvt_pk_f16_f32 v81, v66, v67\n"
"v_mfma_f32_16x16x32_f16 v[48:51], v[104:107], v[40:43], v[48:51]\n"
"v_cvt_pk_f16_f32 v82, v68, v69\n"
"v_cvt_pk_f16_f32 v83, v70, v71\n"
"v_mfma_f32_16x16x32_f16 v[52:55], v[104:107], v[92:95], v[52:55]\n"
"ds_read_b64 v[88:89], v24 offset:64\n"
"s_waitcnt lgkmcnt(5)\n"
"v_mfma_f32_16x16x32_f16 v[56:59], v[104:107], v[84:87], v[56:59]\n"
"ds_read_b64 v[90:91], v24 offset:72\n"
"ds_read_b128 v[72:75], v25 offset:576\n"
"v_mfma_f32_16x16x32_f16 v[52:55], v[100:103], v[84:87], v[52:55]\n"
"ds_read_b128 v[40:43], v25 offset:1088\n"
"v_mfma_f32_16x16x32_f16 v[48:51], v[100:103], v[92:95], v[48:51]\n"
"ds_read_b128 v[104:107], v25 offset:1600\n"
"s_waitcnt lgkmcnt(7)\n"
"v_mfma_f32_16x16x32_f16 v[48:51], v[76:79], v[84:87], v[48:51]\n"
"ds_read_b64 v[92:93], v24 offset:576\n"
"v_mfma_f32_16x16x32_f16 v[56:59], v[80:83], v[128:131], v[56:59]\n"
"ds_read_b64 v[94:95], v24 offset:584\n"
"ds_read_b64 v[100:101], v24 offset:1088\n"
"v_mfma_f32_16x16x32_f16 v[60:63], v[80:83], v[132:135], v[60:63]\n"
"ds_read_b64 v[102:103], v24 offset:1096\n"
"s_waitcnt lgkmcnt(9)\n"
"v_mfma_f32_16x16x32_f16 v[64:67], v[96:99], v[144:147], 0\n"
"ds_read_b64 v[84:85], v24 offset:0\n"
"ds_read_b64 v[86:87], v24 offset:8\n"
"v_mfma_f32_16x16x32_f16 v[68:71], v[44:47], v[148:151], 0\n"
"ds_read_b128 v[76:79], v25 offset:512\n"
"v_mfma_f32_16x16x32_f16 v[60:63], v[152:155], v[96:99], v[60:63]\n"
"ds_read_b128 v[44:47], v25 offset:1024\n"
"s_waitcnt lgkmcnt(8)\n"
"v_mfma_f32_16x16x32_f16 v[48:51], v[88:91], v[72:75], v[48:51]\n"
"ds_read_b128 v[96:99], v25 offset:1536\n"
"ds_read_b64 v[72:73], v24 offset:512\n"
"v_mfma_f32_16x16x32_f16 v[52:55], v[88:91], v[40:43], v[52:55]\n"
"ds_read_b64 v[74:75], v24 offset:520\n"
"v_mfma_f32_16x16x32_f16 v[56:59], v[88:91], v[104:107], v[56:59]\n"
"ds_read_b64 v[88:89], v24 offset:1024\n"
"s_waitcnt lgkmcnt(8)\n"
"v_mfma_f32_16x16x32_f16 v[52:55], v[92:95], v[104:107], v[52:55]\n"
"ds_read_b64 v[90:91], v24 offset:1032\n"
"v_cvt_pk_f16_f32 v80, v64, v65\n"
"v_mfma_f32_16x16x32_f16 v[48:51], v[92:95], v[40:43], v[48:51]\n"
"v_cvt_pk_f16_f32 v81, v66, v67\n"
"v_cvt_pk_f16_f32 v82, v68, v69\n"
"v_mfma_f32_16x16x32_f16 v[48:51], v[100:103], v[104:107], v[48:51]\n"
"v_cvt_pk_f16_f32 v83, v70, v71\n"
"s_waitcnt lgkmcnt(4)\n"
"v_mfma_f32_16x16x32_f16 v[48:51], v[84:87], v[76:79], v[48:51]\n"
"v_mfma_f32_16x16x32_f16 v[52:55], v[84:87], v[44:47], v[52:55]\n"
"v_mfma_f32_16x16x32_f16 v[56:59], v[84:87], v[96:99], v[56:59]\n"
"s_waitcnt lgkmcnt(0)\n"
"v_mfma_f32_16x16x32_f16 v[52:55], v[72:75], v[96:99], v[52:55]\n"
"v_mfma_f32_16x16x32_f16 v[48:51], v[72:75], v[44:47], v[48:51]\n"
"v_mfma_f32_16x16x32_f16 v[48:51], v[88:91], v[96:99], v[48:51]\n"
"v_mfma_f32_16x16x32_f16 v[60:63], v[80:83], v[128:131], v[60:63]\n"
"s_setprio 2\n"
"s_mov_b32 s17, 0x100000\n"
".Ltp_516:\n"
"ds_read_b32 v3, v5 offset:720\n"
"s_sub_u32 s17, s17, 1\n"
"s_cbranch_scc1 .Lou_517\n"
"s_waitcnt lgkmcnt(0)\n"
"v_cmp_eq_u32 vcc, 0, v3\n"
"s_cbranch_vccnz .Ltp_516\n"
".Lou_517:\n"
"s_waitcnt lgkmcnt(0)\n"
"buffer_load_dwordx4 v[110:113], v126, s[4:7], s32 offen offset:0 sc1\n"
"buffer_load_dwordx4 v[114:117], v126, s[4:7], s32 offen offset:128 sc1\n"
"buffer_load_dwordx4 v[118:121], v126, s[4:7], s33 offen offset:0 sc1\n"
"buffer_load_dwordx4 v[122:125], v126, s[4:7], s33 offen offset:128 sc1\n"
"s_mov_b32 s38, 0x05040100\n"
"s_mov_b32 s39, 0x07060302\n"
"s_mov_b32 s17, 32768\n"
".Ltp_518:\n"
"s_waitcnt vmcnt(0)\n"
"s_sub_u32 s17, s17, 1\n"
"s_cbranch_scc1 .Lou_519\n"
"v_bitop3_b32 v64, v111, v113, s16 bitop3:0x7e\n"
"v_bitop3_b32 v65, v115, v117, s16 bitop3:0x7e\n"
"v_bitop3_b32 v66, v119, v121, s16 bitop3:0x7e\n"
"v_bitop3_b32 v67, v123, v125, s16 bitop3:0x7e\n"
"v_or3_b32 v68, v64, v65, v66\n"
"v_or_b32 v68, v68, v67\n"
"v_cmp_ne_u32 vcc, 0, v68\n"
"s_cbranch_vccz .Lou_519\n"
"s_sleep 2\n"
"v_cmp_ne_u32 vcc, 0, v64\n"
"s_cbranch_vccz .Lsk_520\n"
"s_mov_b64 exec, vcc\n"
"buffer_load_dwordx4 v[110:113], v126, s[4:7], s32 offen offset:0 sc1\n"
"s_mov_b64 exec, -1\n"
".Lsk_520:\n"
"v_cmp_ne_u32 vcc, 0, v65\n"
"s_cbranch_vccz .Lsk_521\n"
"s_mov_b64 exec, vcc\n"
"buffer_load_dwordx4 v[114:117], v126, s[4:7], s32 offen offset:128 sc1\n"
"s_mov_b64 exec, -1\n"
".Lsk_521:\n"
"v_cmp_ne_u32 vcc, 0, v66\n"
"s_cbranch_vccz .Lsk_522\n"
"s_mov_b64 exec, vcc\n"
"buffer_load_dwordx4 v[118:121], v126, s[4:7], s33 offen offset:0 sc1\n"
"s_mov_b64 exec, -1\n"
".Lsk_522:\n"
"v_cmp_ne_u32 vcc, 0, v67\n"
"s_cbranch_vccz .Lsk_523\n"
"s_mov_b64 exec, vcc\n"
"buffer_load_dwordx4 v[122:125], v126, s[4:7], s33 offen offset:128 sc1\n"
"s_mov_b64 exec, -1\n"
".Lsk_523:\n"
"s_branch .Ltp_518\n"
".Lou_519:\n"
"s_waitcnt vmcnt(0)\n"
"buffer_load_dwordx4 v[88:91], v30, s[4:7], s34 offen sc1\n"
"buffer_load_dwordx4 v[92:95], v30, s[4:7], s35 offen sc1\n"
"v_perm_b32 v64, v114, v110, s38\n"
"v_perm_b32 v65, v114, v110, s39\n"
"v_perm_b32 v66, v116, v112, s38\n"
"v_perm_b32 v67, v116, v112, s39\n"
"ds_write_b32 v32, v64 offset:0\n"
"ds_write_b32 v33, v65 offset:2048\n"
"ds_write_b32 v32, v66 offset:4224\n"
"ds_write_b32 v33, v67 offset:6272\n"
"v_perm_b32 v64, v122, v118, s38\n"
"v_perm_b32 v65, v122, v118, s39\n"
"v_perm_b32 v66, v124, v120, s38\n"
"v_perm_b32 v67, v124, v120, s39\n"
"ds_write_b32 v32, v64 offset:1024\n"
"ds_write_b32 v33, v65 offset:3072\n"
"ds_write_b32 v32, v66 offset:5248\n"
"ds_write_b32 v33, v67 offset:7296\n"
"v_lshlrev_b32 v64, 16, v110\n"
"v_lshlrev_b32 v65, 16, v114\n"
"v_and_b32 v66, 0xffff0000, v110\n"
"v_and_b32 v67, 0xffff0000, v114\n"
"v_lshlrev_b32 v68, 16, v112\n"
"v_lshlrev_b32 v69, 16, v116\n"
"v_and_b32 v70, 0xffff0000, v112\n"
"v_and_b32 v71, 0xffff0000, v116\n"
"v_lshlrev_b32 v72, 16, v118\n"
"v_lshlrev_b32 v73, 16, v122\n"
"v_and_b32 v74, 0xffff0000, v118\n"
"v_and_b32 v75, 0xffff0000, v122\n"
"v_lshlrev_b32 v76, 16, v120\n"
"v_lshlrev_b32 v77, 16, v124\n"
"v_and_b32 v78, 0xffff0000, v120\n"
"v_and_b32 v79, 0xffff0000, v124\n"
"s_mov_b32 s17, 32768\n"
".Ltp_524:\n"
"s_waitcnt vmcnt(0)\n"
"s_sub_u32 s17, s17, 1\n"
"s_cbranch_scc1 .Lou_525\n"
"v_max_u32 v110, v88, v89\n"
"v_max3_u32 v110, v90, v91, v110\n"
"v_max3_u32 v110, v92, v93, v110\n"
"v_max3_u32 v110, v94, v95, v110\n"
"v_cmp_eq_u32 vcc, -1, v110\n"
"s_cbranch_vccz .Lou_525\n"
"v_max_u32 v110, v90, v91\n"
"v_max3_u32 v110, v88, v89, v110\n"
"v_cmp_eq_u32 vcc, -1, v110\n"
"s_cbranch_vccz .Lsk_526\n"
"s_mov_b64 exec, vcc\n"
"buffer_load_dwordx4 v[88:91], v30, s[4:7], s34 offen sc1\n"
"s_mov_b64 exec, -1\n"
".Lsk_526:\n"
"v_max_u32 v110, v94, v95\n"
"v_max3_u32 v110, v92, v93, v110\n"
"v_cmp_eq_u32 vcc, -1, v110\n"
"s_cbranch_vccz .Lsk_527\n"
"s_mov_b64 exec, vcc\n"
"buffer_load_dwordx4 v[92:95], v30, s[4:7], s35 offen sc1\n"
"s_mov_b64 exec, -1\n"
".Lsk_527:\n"
"s_branch .Ltp_524\n"
".Lou_525:\n"
"s_waitcnt vmcnt(0)\n"
"v_fma_f32 v64, v64, v89, v88\n"
"v_fma_f32 v65, v65, v91, v90\n"
"v_cvt_pk_f16_f32 v64, v64, v65\n"
"v_fma_f32 v66, v66, v89, v88\n"
"v_fma_f32 v67, v67, v91, v90\n"
"v_cvt_pk_f16_f32 v66, v66, v67\n"
"v_fma_f32 v68, v68, v89, v88\n"
"v_fma_f32 v69, v69, v91, v90\n"
"v_cvt_pk_f16_f32 v68, v68, v69\n"
"v_fma_f32 v70, v70, v89, v88\n"
"v_fma_f32 v71, v71, v91, v90\n"
"v_cvt_pk_f16_f32 v70, v70, v71\n"
"ds_write2st64_b32 v31, v64, v66 offset0:0 offset1:18\n"
"ds_write2st64_b32 v31, v68, v70 offset0:36 offset1:54\n"
"v_fma_f32 v72, v72, v93, v92\n"
"v_fma_f32 v73, v73, v95, v94\n"
"v_cvt_pk_f16_f32 v72, v72, v73\n"
"v_fma_f32 v74, v74, v93, v92\n"
"v_fma_f32 v75, v75, v95, v94\n"
"v_cvt_pk_f16_f32 v74, v74, v75\n"
"v_fma_f32 v76, v76, v93, v92\n"
"v_fma_f32 v77, v77, v95, v94\n"
"v_cvt_pk_f16_f32 v76, v76, v77\n"
"v_fma_f32 v78, v78, v93, v92\n"
"v_fma_f32 v79, v79, v95, v94\n"
"v_cvt_pk_f16_f32 v78, v78, v79\n"
"ds_write2st64_b32 v31, v72, v74 offset0:4 offset1:22\n"
"ds_write2st64_b32 v31, v76, v78 offset0:40 offset1:58\n"
"ds_write_b32 v4, v4 offset:528\n"
"s_mov_b32 s17, 0x100000\n"
".Ltp_528:\n"
"ds_read_b32 v3, v5 offset:528\n"
"s_sub_u32 s17, s17, 1\n"
"s_cbranch_scc1 .Lou_529\n"
"s_waitcnt lgkmcnt(0)\n"
"v_cmp_eq_u32 vcc, 0, v3\n"
"s_cbranch_vccnz .Ltp_528\n"
".Lou_529:\n"
"s_waitcnt lgkmcnt(0)\n"
"s_mov_b32 s36, 0xfffefffe\n"
"ds_read_b64 v[88:89], v24 offset:1984\n"
"ds_read_b64 v[90:91], v24 offset:1992\n"
"ds_read_b128 v[92:95], v25 offset:4032\n"
"ds_read_b128 v[96:99], v25 offset:3520\n"
"ds_read_b64 v[100:101], v24 offset:1472\n"
"ds_read_b64 v[102:103], v24 offset:1480\n"
"ds_read_b128 v[104:107], v25 offset:3008\n"
"s_waitcnt lgkmcnt(1)\n"
"v_mfma_f32_16x16x32_f16 v[60:63], v[88:91], v[92:95], v[60:63]\n"
"ds_read_b64 v[108:109], v24 offset:960\n"
"ds_read_b64 v[110:111], v24 offset:968\n"
"v_mfma_f32_16x16x32_f16 v[56:59], v[88:91], v[96:99], v[56:59]\n"
"ds_read_b128 v[112:115], v25 offset:2496\n"
"ds_read_b64 v[116:117], v24 offset:448\n"
"v_mfma_f32_16x16x32_f16 v[60:63], v[100:103], v[96:99], v[60:63]\n"
"ds_read_b64 v[118:119], v24 offset:456\n"
"s_waitcnt lgkmcnt(3)\n"
"v_mfma_f32_16x16x32_f16 v[52:55], v[88:91], v[104:107], v[52:55]\n"
"ds_read_b64 v[120:121], v24 offset:1920\n"
"ds_read_b64 v[122:123], v24 offset:1928\n"
"v_mfma_f32_16x16x32_f16 v[56:59], v[100:103], v[104:107], v[56:59]\n"
"ds_read_b128 v[124:127], v25 offset:3968\n"
"ds_read_b128 v[40:43], v25 offset:3456\n"
"v_mfma_f32_16x16x32_f16 v[60:63], v[108:111], v[104:107], v[60:63]\n"
"ds_read_b64 v[44:45], v24 offset:1408\n"
"s_waitcnt lgkmcnt(7)\n"
"v_mfma_f32_16x16x32_f16 v[48:51], v[88:91], v[112:115], v[48:51]\n"
"ds_read_b64 v[46:47], v24 offset:1416\n"
"ds_read_b128 v[92:95], v25 offset:2944\n"
"v_mfma_f32_16x16x32_f16 v[52:55], v[100:103], v[112:115], v[52:55]\n"
"ds_read_b64 v[96:97], v24 offset:896\n"
"ds_read_b64 v[98:99], v24 offset:904\n"
"v_mfma_f32_16x16x32_f16 v[56:59], v[108:111], v[112:115], v[56:59]\n"
"ds_read_b128 v[104:107], v25 offset:2432\n"
"s_waitcnt lgkmcnt(6)\n"
"v_mfma_f32_16x16x32_f16 v[60:63], v[116:119], v[112:115], v[60:63]\n"
"ds_read_b64 v[88:89], v24 offset:384\n"
"v_mfma_f32_16x16x32_f16 v[60:63], v[120:123], v[124:127], v[60:63]\n"
"ds_read_b64 v[90:91], v24 offset:392\n"
"ds_read_b64 v[100:101], v24 offset:1856\n"
"v_mfma_f32_16x16x32_f16 v[56:59], v[120:123], v[40:43], v[56:59]\n"
"ds_read_b64 v[102:103], v24 offset:1864\n"
"s_waitcnt lgkmcnt(7)\n"
"v_mfma_f32_16x16x32_f16 v[60:63], v[44:47], v[40:43], v[60:63]\n"
"ds_read_b128 v[108:111], v25 offset:3904\n"
"ds_read_b128 v[112:115], v25 offset:3392\n"
"v_mfma_f32_16x16x32_f16 v[52:55], v[120:123], v[92:95], v[52:55]\n"
"ds_read_b64 v[116:117], v26 offset:0\n"
"ds_read_b64 v[124:125], v24 offset:1344\n"
"v_mfma_f32_16x16x32_f16 v[56:59], v[44:47], v[92:95], v[56:59]\n"
"ds_read_b64 v[126:127], v24 offset:1352\n"
"s_waitcnt lgkmcnt(9)\n"
"v_mfma_f32_16x16x32_f16 v[60:63], v[96:99], v[92:95], v[60:63]\n"
"ds_read_b128 v[40:43], v25 offset:2880\n"
"v_mfma_f32_16x16x32_f16 v[48:51], v[120:123], v[104:107], v[48:51]\n"
"ds_read_b64 v[92:93], v24 offset:832\n"
"v_mfma_f32_16x16x32_f16 v[52:55], v[44:47], v[104:107], v[52:55]\n"
"ds_read_b64 v[94:95], v24 offset:840\n"
"s_waitcnt lgkmcnt(7)\n"
"v_mfma_f32_16x16x32_f16 v[56:59], v[96:99], v[104:107], v[56:59]\n"
"ds_read_b128 v[120:123], v25 offset:2368\n"
"v_mfma_f32_16x16x32_f16 v[60:63], v[88:91], v[104:107], v[60:63]\n"
"v_mfma_f32_16x16x32_f16 v[60:63], v[100:103], v[108:111], v[60:63]\n"
"ds_read_b64 v[44:45], v24 offset:320\n"
"s_waitcnt lgkmcnt(5)\n"
"v_mfma_f32_16x16x32_f16 v[56:59], v[100:103], v[112:115], v[56:59]\n"
"ds_read_b64 v[46:47], v24 offset:328\n"
"s_lshl_b32 s79, s18, 3\n"
"v_mfma_f32_16x16x16_bf16 v[48:51], v[34:35], v[116:117], v[48:51]\n"
"ds_read_b64 v[96:97], v26 offset:512\n"
"ds_read_b64 v[104:105], v24 offset:1792\n"
"v_mfma_f32_16x16x32_f16 v[60:63], v[124:127], v[112:115], v[60:63]\n"
"ds_read_b64 v[106:107], v24 offset:1800\n"
"s_waitcnt lgkmcnt(6)\n"
"v_mfma_f32_16x16x32_f16 v[52:55], v[100:103], v[40:43], v[52:55]\n"
"ds_read_b128 v[88:91], v25 offset:3840\n"
"ds_read_b128 v[108:111], v25 offset:3328\n"
"v_mfma_f32_16x16x32_f16 v[56:59], v[124:127], v[40:43], v[56:59]\n"
"ds_read_b64 v[116:117], v24 offset:1280\n"
"ds_read_b64 v[118:119], v24 offset:1288\n"
"v_mfma_f32_16x16x32_f16 v[60:63], v[92:95], v[40:43], v[60:63]\n"
"ds_read_b128 v[112:115], v25 offset:2816\n"
"s_waitcnt lgkmcnt(10)\n"
"v_mfma_f32_16x16x32_f16 v[48:51], v[100:103], v[120:123], v[48:51]\n"
"ds_read_b64 v[40:41], v24 offset:768\n"
"s_add_u32 s32, s79, 0x140000\n"
"v_mfma_f32_16x16x32_f16 v[52:55], v[124:127], v[120:123], v[52:55]\n"
"ds_read_b64 v[42:43], v24 offset:776\n"
"v_mfma_f32_16x16x32_f16 v[56:59], v[92:95], v[120:123], v[56:59]\n"
"ds_read_b128 v[100:103], v25 offset:2304\n"
"s_waitcnt lgkmcnt(7)\n"
"v_mfma_f32_16x16x32_f16 v[60:63], v[44:47], v[120:123], v[60:63]\n"
"v_mfma_f32_16x16x16_bf16 v[52:55], v[34:35], v[96:97], v[52:55]\n"
"ds_read_b64 v[124:125], v24 offset:256\n"
"v_mfma_f32_16x16x32_f16 v[60:63], v[104:107], v[88:91], v[60:63]\n"
"ds_read_b64 v[126:127], v24 offset:264\n"
"s_waitcnt lgkmcnt(5)\n"
"v_mfma_f32_16x16x32_f16 v[56:59], v[104:107], v[108:111], v[56:59]\n"
"ds_read_b64 v[92:93], v24 offset:1728\n"
"ds_read_b64 v[94:95], v24 offset:1736\n"
"v_mfma_f32_16x16x32_f16 v[60:63], v[116:119], v[108:111], v[60:63]\n"
"ds_read_b128 v[120:123], v25 offset:3776\n"
"ds_read_b128 v[44:47], v25 offset:3264\n"
"v_mfma_f32_16x16x32_f16 v[52:55], v[104:107], v[112:115], v[52:55]\n"
"ds_read_b64 v[96:97], v24 offset:1216\n"
"s_waitcnt lgkmcnt(7)\n"
"v_mfma_f32_16x16x32_f16 v[56:59], v[116:119], v[112:115], v[56:59]\n"
"ds_read_b64 v[98:99], v24 offset:1224\n"
"ds_read_b128 v[88:91], v25 offset:2752\n"
"v_mfma_f32_16x16x32_f16 v[60:63], v[40:43], v[112:115], v[60:63]\n"
"ds_read_b64 v[108:109], v26 offset:1024\n"
"v_mfma_f32_16x16x32_f16 v[48:51], v[104:107], v[100:103], v[48:51]\n"
"ds_read_b64 v[112:113], v24 offset:704\n"
"s_waitcnt lgkmcnt(9)\n"
"v_mfma_f32_16x16x32_f16 v[52:55], v[116:119], v[100:103], v[52:55]\n"
"ds_read_b64 v[114:115], v24 offset:712\n"
"v_mfma_f32_16x16x32_f16 v[56:59], v[40:43], v[100:103], v[56:59]\n"
"ds_read_b128 v[104:107], v25 offset:2240\n"
"v_mfma_f32_16x16x32_f16 v[60:63], v[124:127], v[100:103], v[60:63]\n"
"s_waitcnt lgkmcnt(5)\n"
"v_mfma_f32_16x16x32_f16 v[60:63], v[92:95], v[120:123], v[60:63]\n"
"ds_read_b64 v[116:117], v24 offset:192\n"
"v_mfma_f32_16x16x32_f16 v[56:59], v[92:95], v[44:47], v[56:59]\n"
"ds_read_b64 v[118:119], v24 offset:200\n"
"ds_read_b64 v[40:41], v24 offset:1664\n"
"v_mfma_f32_16x16x32_f16 v[60:63], v[96:99], v[44:47], v[60:63]\n"
"ds_read_b64 v[42:43], v24 offset:1672\n"
"s_waitcnt lgkmcnt(7)\n"
"v_mfma_f32_16x16x32_f16 v[52:55], v[92:95], v[88:91], v[52:55]\n"
"ds_read_b128 v[100:103], v25 offset:3712\n"
"ds_read_b128 v[124:127], v25 offset:3200\n"
"v_mfma_f32_16x16x16_bf16 v[56:59], v[34:35], v[108:109], v[56:59]\n"
"ds_read_b64 v[120:121], v24 offset:1152\n"
"ds_read_b64 v[122:123], v24 offset:1160\n"
"s_nop 3\n"
"v_mfma_f32_16x16x32_f16 v[56:59], v[96:99], v[88:91], v[56:59]\n"
"ds_read_b128 v[44:47], v25 offset:2688\n"
"s_waitcnt lgkmcnt(9)\n"
"v_mfma_f32_16x16x32_f16 v[60:63], v[112:115], v[88:91], v[60:63]\n"
"ds_read_b64 v[108:109], v24 offset:640\n"
"v_mfma_f32_16x16x32_f16 v[48:51], v[92:95], v[104:107], v[48:51]\n"
"ds_read_b64 v[110:111], v24 offset:648\n"
"v_mfma_f32_16x16x32_f16 v[52:55], v[96:99], v[104:107], v[52:55]\n"
"ds_read_b128 v[88:91], v25 offset:2176\n"
"s_waitcnt lgkmcnt(7)\n"
"v_mfma_f32_16x16x32_f16 v[56:59], v[112:115], v[104:107], v[56:59]\n"
"ds_read_b64 v[92:93], v26 offset:1536\n"
"v_mfma_f32_16x16x32_f16 v[60:63], v[116:119], v[104:107], v[60:63]\n"
"v_mfma_f32_16x16x32_f16 v[60:63], v[40:43], v[100:103], v[60:63]\n"
"ds_read_b64 v[96:97], v24 offset:128\n"
"s_waitcnt lgkmcnt(5)\n"
"v_mfma_f32_16x16x32_f16 v[56:59], v[40:43], v[124:127], v[56:59]\n"
"ds_read_b64 v[98:99], v24 offset:136\n"
"ds_read_b64 v[112:113], v24 offset:1600\n"
"v_mfma_f32_16x16x32_f16 v[60:63], v[120:123], v[124:127], v[60:63]\n"
"ds_read_b64 v[114:115], v24 offset:1608\n"
"ds_read_b128 v[104:107], v25 offset:3648\n"
"v_mfma_f32_16x16x32_f16 v[52:55], v[40:43], v[44:47], v[52:55]\n"
"ds_read_b128 v[116:119], v25 offset:3136\n"
"s_waitcnt lgkmcnt(7)\n"
"v_mfma_f32_16x16x32_f16 v[56:59], v[120:123], v[44:47], v[56:59]\n"
"ds_read_b64 v[100:101], v24 offset:1088\n"
"ds_read_b64 v[102:103], v24 offset:1096\n"
"v_mfma_f32_16x16x32_f16 v[60:63], v[108:111], v[44:47], v[60:63]\n"
"ds_read_b128 v[124:127], v25 offset:2624\n"
"v_mfma_f32_16x16x32_f16 v[48:51], v[40:43], v[88:91], v[48:51]\n"
"ds_read_b64 v[44:45], v24 offset:576\n"
"s_waitcnt lgkmcnt(10)\n"
"s_nop 0\n"
"v_mfma_f32_16x16x16_bf16 v[60:63], v[34:35], v[92:93], v[60:63]\n"
"ds_read_b64 v[46:47], v24 offset:584\n"
"v_mfma_f32_16x16x32_f16 v[52:55], v[120:123], v[88:91], v[52:55]\n"
"ds_read_b128 v[40:43], v25 offset:2112\n"
"v_mfma_f32_16x16x32_f16 v[56:59], v[108:111], v[88:91], v[56:59]\n"
"s_waitcnt lgkmcnt(6)\n"
"v_mfma_f32_16x16x32_f16 v[60:63], v[96:99], v[88:91], v[60:63]\n"
"ds_read_b64 v[92:93], v24 offset:64\n"
"v_mfma_f32_16x16x32_f16 v[60:63], v[112:115], v[104:107], v[60:63]\n"
"ds_read_b64 v[94:95], v24 offset:72\n"
"ds_read_b64 v[120:121], v24 offset:1536\n"
"v_mfma_f32_16x16x32_f16 v[56:59], v[112:115], v[116:119], v[56:59]\n"
"ds_read_b64 v[122:123], v24 offset:1544\n"
"s_waitcnt lgkmcnt(7)\n"
"v_mfma_f32_16x16x32_f16 v[60:63], v[100:103], v[116:119], v[60:63]\n"
"ds_read_b128 v[108:111], v25 offset:2048\n"
"ds_read_b128 v[88:91], v25 offset:2560\n"
"v_mfma_f32_16x16x32_f16 v[52:55], v[112:115], v[124:127], v[52:55]\n"
"ds_read_b64 v[96:97], v24 offset:1024\n"
"ds_read_b64 v[98:99], v24 offset:1032\n"
"v_mfma_f32_16x16x32_f16 v[56:59], v[100:103], v[124:127], v[56:59]\n"
"ds_read_b128 v[104:107], v25 offset:3072\n"
"s_waitcnt lgkmcnt(9)\n"
"v_mfma_f32_16x16x32_f16 v[60:63], v[44:47], v[124:127], v[60:63]\n"
"ds_read_b64 v[116:117], v24 offset:512\n"
"v_mfma_f32_16x16x32_f16 v[48:51], v[112:115], v[40:43], v[48:51]\n"
"ds_read_b64 v[118:119], v24 offset:520\n"
"v_mfma_f32_16x16x32_f16 v[52:55], v[100:103], v[40:43], v[52:55]\n"
"ds_read_b128 v[124:127], v25 offset:3584\n"
"s_waitcnt lgkmcnt(7)\n"
"v_mfma_f32_16x16x32_f16 v[56:59], v[44:47], v[40:43], v[56:59]\n"
"v_mfma_f32_16x16x32_f16 v[60:63], v[92:95], v[40:43], v[60:63]\n"
"ds_read_b64 v[112:113], v24 offset:0\n"
"v_mfma_f32_16x16x32_f16 v[48:51], v[120:123], v[108:111], v[48:51]\n"
"ds_read_b64 v[114:115], v24 offset:8\n"
"s_waitcnt lgkmcnt(5)\n"
"v_mfma_f32_16x16x32_f16 v[52:55], v[120:123], v[88:91], v[52:55]\n"
"v_mfma_f32_16x16x32_f16 v[52:55], v[96:99], v[108:111], v[52:55]\n"
"v_mfma_f32_16x16x32_f16 v[56:59], v[120:123], v[104:107], v[56:59]\n"
"v_mul_f32 v48, v36, v48\n"
"v_mul_f32 v49, v36, v49\n"
"v_mul_f32 v50, v36, v50\n"
"s_waitcnt lgkmcnt(2)\n"
"v_mfma_f32_16x16x32_f16 v[56:59], v[96:99], v[88:91], v[56:59]\n"
"v_mul_f32 v51, v36, v51\n"
"v_cvt_pk_bf16_f32 v48, v48, v49\n"
"v_cvt_pk_bf16_f32 v49, v50, v51\n"
"v_mfma_f32_16x16x32_f16 v[56:59], v[116:119], v[108:111], v[56:59]\n"
"v_pk_min_u16 v48, v48, s36\n"
"v_pk_min_u16 v49, v49, s36\n"
"ds_write_b64 v26, v[48:49] offset:0\n"
"v_mfma_f32_16x16x32_f16 v[60:63], v[120:123], v[124:127], v[60:63]\n"
"s_waitcnt lgkmcnt(1)\n"
"v_mfma_f32_16x16x32_f16 v[60:63], v[96:99], v[104:107], v[60:63]\n"
"v_mul_f32 v52, v36, v52\n"
"v_mul_f32 v53, v36, v53\n"
"v_mfma_f32_16x16x32_f16 v[60:63], v[116:119], v[88:91], v[60:63]\n"
"v_mul_f32 v54, v36, v54\n"
"v_mul_f32 v55, v36, v55\n"
"v_mfma_f32_16x16x32_f16 v[60:63], v[112:115], v[108:111], v[60:63]\n"
"v_cvt_pk_bf16_f32 v52, v52, v53\n"
"v_cvt_pk_bf16_f32 v53, v54, v55\n"
"v_pk_min_u16 v52, v52, s36\n"
"v_pk_min_u16 v53, v53, s36\n"
"ds_write_b64 v26, v[52:53] offset:512\n"
"v_mul_f32 v56, v36, v56\n"
"v_mul_f32 v57, v36, v57\n"
"v_mul_f32 v58, v36, v58\n"
"v_mul_f32 v59, v36, v59\n"
"v_cvt_pk_bf16_f32 v56, v56, v57\n"
"v_cvt_pk_bf16_f32 v57, v58, v59\n"
"v_pk_min_u16 v56, v56, s36\n"
"v_pk_min_u16 v57, v57, s36\n"
"ds_write_b64 v26, v[56:57] offset:1024\n"
"v_mul_f32 v60, v36, v60\n"
"v_mul_f32 v61, v36, v61\n"
"v_mul_f32 v62, v36, v62\n"
"v_mul_f32 v63, v36, v63\n"
"v_cvt_pk_bf16_f32 v60, v60, v61\n"
"v_cvt_pk_bf16_f32 v61, v62, v63\n"
"v_pk_min_u16 v60, v60, s36\n"
"v_pk_min_u16 v61, v61, s36\n"
"ds_write_b64 v26, v[60:61] offset:1536\n"
"ds_write_b32 v4, v4 offset:736\n"
"s_mov_b32 s17, 0x100000\n"
".Ltp_530:\n"
"ds_read_b32 v3, v5 offset:736\n"
"s_sub_u32 s17, s17, 1\n"
"s_cbranch_scc1 .Lou_531\n"
"s_waitcnt lgkmcnt(0)\n"
"v_cmp_eq_u32 vcc, 0, v3\n"
"s_cbranch_vccnz .Ltp_530\n"
".Lou_531:\n"
"s_waitcnt lgkmcnt(0)\n"
"ds_read_b64_tr_b16 v[64:65], v27 offset:0\n"
"ds_read_b64_tr_b16 v[66:67], v28 offset:0\n"
"ds_read_b64_tr_b16 v[68:69], v27 offset:1024\n"
"ds_read_b64_tr_b16 v[70:71], v28 offset:1024\n"
"s_add_u32 s33, s32, 0x1400\n"
"s_add_u32 s34, s32, 0xa0000\n"
"s_add_u32 s35, s32, 0xa1400\n"
"s_waitcnt lgkmcnt(3)\n"
"buffer_store_dwordx2 v[64:65], v37, s[12:15], s32 offen\n"
"s_waitcnt lgkmcnt(2)\n"
"buffer_store_dwordx2 v[66:67], v37, s[12:15], s33 offen\n"
"s_waitcnt lgkmcnt(1)\n"
"buffer_store_dwordx2 v[68:69], v37, s[12:15], s34 offen\n"
"s_waitcnt lgkmcnt(0)\n"
"buffer_store_dwordx2 v[70:71], v37, s[12:15], s35 offen\n"
"s_nop 1\n"
".Lnc_513:\n"
"s_branch .L_yend\n"
".Lst_288:\n"
"s_nop 7\n"
"s_lshl_b32 s79, s3, 2\n"
"s_add_u32 s79, s79, 0x26800\n"
"v_mov_b32 v36, s79\n"
"v_and_b32 v79, 15, v0\n"
"v_lshrrev_b32 v78, 4, v0\n"
"v_lshlrev_b32 v77, 3, v78\n"
"v_sub_u32 v76, v77, v79\n"
"v_lshlrev_b32 v75, 4, v78\n"
"v_lshlrev_b32 v74, 8, v78\n"
"v_lshlrev_b32 v73, 3, v79\n"
"v_add_u32 v72, v74, v73\n"
"v_sub_u32 v71, 0x40a, v76\n"
"v_sub_u32 v70, 0x40b, v76\n"
"v_sub_u32 v69, 0x40c, v76\n"
"v_sub_u32 v68, 0x40d, v76\n"
"v_sub_u32 v67, 0x40e, v76\n"
"v_sub_u32 v66, 0x40f, v76\n"
"v_sub_u32 v65, 0x410, v76\n"
"s_mul_i32 s79, s3, 0x2100\n"
"s_add_u32 s79, s79, 0x15100\n"
"s_add_u32 s79, s79, 0x800\n"
"v_add_u32 v64, s79, v72\n"
"s_mul_i32 s79, s3, 0x2100\n"
"s_add_u32 s79, s79, 0x15100\n"
"v_lshl_add_u32 v99, v79, 6, v75\n"
"v_add_u32 v80, s79, v99\n"
"s_mul_i32 s79, s3, 0x2100\n"
"s_add_u32 s79, s79, 0x15100\n"
"s_add_u32 s79, s79, 0x400\n"
"v_lshl_add_u32 v99, v79, 5, v75\n"
"v_lshl_add_u32 v98, v79, 4, v74\n"
"v_cmp_gt_i32 vcc, 2, v78\n"
"s_nop 1\n"
"v_cndmask_b32 v99, v98, v99, vcc\n"
"v_add_u32 v81, s79, v99\n"
"v_mov_b32 v99, 0x40000\n"
"v_cmp_ge_i32 vcc, 32, v0\n"
"s_nop 1\n"
"v_cndmask_b32 v82, v99, v64, vcc\n"
"v_mov_b32 v99, 0x40000\n"
"v_cmp_gt_i32 vcc, 32, v0\n"
"s_nop 1\n"
"v_cndmask_b32 v83, v99, v64, vcc\n"
"s_mul_i32 s79, s3, 0x2100\n"
"s_add_u32 s79, s79, 0x15100\n"
"s_add_u32 s79, s79, 0x7ec\n"
"v_lshlrev_b32 v99, 5, v78\n"
"v_sub_u32 v99, v73, v99\n"
"v_add_u32 v84, s79, v99\n"
"s_mul_i32 s79, s3, 0x2100\n"
"s_add_u32 s79, s79, 0x15100\n"
"s_add_u32 s79, s79, 0x400\n"
"v_sub_u32 v99, 0x411, v76\n"
"v_bfe_u32 v98, v65, 2, 1\n"
"v_add_u32 v99, v99, v98\n"
"v_and_b32 v99, 0xffc, v99\n"
"v_min_i32 v99, 0x400, v99\n"
"v_add_u32 v100, s79, v99\n"
"s_mul_i32 s79, s3, 0x2100\n"
"s_add_u32 s79, s79, 0x15100\n"
"s_add_u32 s79, s79, 0x400\n"
"v_bfe_u32 v99, v66, 2, 1\n"
"v_add_u32 v99, v65, v99\n"
"v_and_b32 v99, 0xffc, v99\n"
"v_min_i32 v99, 0x400, v99\n"
"v_add_u32 v101, s79, v99\n"
"s_mul_i32 s79, s3, 0x2100\n"
"s_add_u32 s79, s79, 0x15100\n"
"s_add_u32 s79, s79, 0x400\n"
"v_bfe_u32 v99, v67, 2, 1\n"
"v_add_u32 v99, v66, v99\n"
"v_and_b32 v99, 0xffc, v99\n"
"v_min_i32 v99, 0x400, v99\n"
"v_add_u32 v102, s79, v99\n"
"s_mul_i32 s79, s3, 0x2100\n"
"s_add_u32 s79, s79, 0x15100\n"
"s_add_u32 s79, s79, 0x400\n"
"v_bfe_u32 v99, v68, 2, 1\n"
"v_add_u32 v99, v67, v99\n"
"v_and_b32 v99, 0xffc, v99\n"
"v_min_i32 v99, 0x400, v99\n"
"v_add_u32 v103, s79, v99\n"
"s_mul_i32 s79, s3, 0x2100\n"
"s_add_u32 s79, s79, 0x15100\n"
"s_add_u32 s79, s79, 0x400\n"
"v_bfe_u32 v99, v69, 2, 1\n"
"v_add_u32 v99, v68, v99\n"
"v_and_b32 v99, 0xffc, v99\n"
"v_min_i32 v99, 0x400, v99\n"
"v_add_u32 v104, s79, v99\n"
"s_mul_i32 s79, s3, 0x2100\n"
"s_add_u32 s79, s79, 0x15100\n"
"s_add_u32 s79, s79, 0x400\n"
"v_bfe_u32 v99, v70, 2, 1\n"
"v_add_u32 v99, v69, v99\n"
"v_and_b32 v99, 0xffc, v99\n"
"v_min_i32 v99, 0x400, v99\n"
"v_add_u32 v105, s79, v99\n"
"s_mul_i32 s79, s3, 0x2100\n"
"s_add_u32 s79, s79, 0x15100\n"
"s_add_u32 s79, s79, 0x400\n"
"v_bfe_u32 v99, v71, 2, 1\n"
"v_add_u32 v99, v70, v99\n"
"v_and_b32 v99, 0xffc, v99\n"
"v_min_i32 v99, 0x400, v99\n"
"v_add_u32 v106, s79, v99\n"
"s_mul_i32 s79, s3, 0x2100\n"
"s_add_u32 s79, s79, 0x15100\n"
"s_add_u32 s79, s79, 0x400\n"
"v_sub_u32 v99, 0x409, v76\n"
"v_bfe_u32 v99, v99, 2, 1\n"
"v_add_u32 v99, v71, v99\n"
"v_and_b32 v99, 0xffc, v99\n"
"v_min_i32 v99, 0x400, v99\n"
"v_add_u32 v107, s79, v99\n"
"v_cmp_eq_u32 vcc, 0, v0\n"
"v_lshlrev_b32 v140, 16, v140\n"
"v_mul_f32 v140, 0.5, v140\n"
"v_cndmask_b32 v140, 0, v140, vcc\n"
"v_add_f32 v128, v128, v140\n"
"v_cndmask_b32 v136, v136, 0, s[56:57]\n"
"v_cndmask_b32 v137, v137, 0, s[58:59]\n"
"v_cndmask_b32 v138, v138, 0, s[58:59]\n"
"v_cndmask_b32 v139, v139, 0, s[58:59]\n"
"v_max3_f32 v85, |v128|, |v129|, 0\n"
"v_max3_f32 v85, |v130|, |v131|, v85\n"
"v_max3_f32 v85, |v132|, |v133|, v85\n"
"v_max3_f32 v85, |v134|, |v135|, v85\n"
"v_max3_f32 v85, |v136|, |v137|, v85\n"
"v_max3_f32 v85, |v138|, |v139|, v85\n"
"s_nop 1\n"
"v_max_u32_dpp v85, v85, v85 row_ror:8 row_mask:0xf bank_mask:0xf\n"
"s_nop 1\n"
"v_max_u32_dpp v85, v85, v85 row_ror:4 row_mask:0xf bank_mask:0xf\n"
"s_nop 1\n"
"v_max_u32_dpp v85, v85, v85 row_ror:2 row_mask:0xf bank_mask:0xf\n"
"s_nop 1\n"
"v_max_u32_dpp v85, v85, v85 row_ror:1 row_mask:0xf bank_mask:0xf\n"
"v_mov_b32 v86, v85\n"
"s_nop 1\n"
"v_permlane16_swap_b32 v85, v86\n"
"v_max_u32 v85, v85, v86\n"
"v_mov_b32 v86, v85\n"
"s_nop 1\n"
"v_permlane32_swap_b32 v85, v86\n"
"v_max_u32 v85, v85, v86\n"
"s_nop 0\n"
"v_readfirstlane_b32 s51, v85\n"
"s_nop 2\n"
"s_bfe_u32 s50, s51, 0x80017\n"
"s_max_i32 s50, s50, 77\n"
"s_min_i32 s50, s50, 227\n"
"s_cmp_gt_i32 s51, 0\n"
"s_cselect_b32 s50, s50, 127\n"
"s_sub_u32 s51, 268, s50\n"
"s_lshl_b32 s51, s51, 23\n"
"s_sub_u32 s50, s50, 13\n"
"s_lshl_b32 s50, s50, 23\n"
"v_mul_f32 v128, s51, v128\n"
"v_mul_f32 v129, s51, v129\n"
"v_mul_f32 v130, s51, v130\n"
"v_mul_f32 v131, s51, v131\n"
"v_mul_f32 v132, s51, v132\n"
"v_mul_f32 v133, s51, v133\n"
"v_mul_f32 v134, s51, v134\n"
"v_mul_f32 v135, s51, v135\n"
"v_mul_f32 v136, s51, v136\n"
"v_mul_f32 v137, s51, v137\n"
"v_mul_f32 v138, s51, v138\n"
"v_mul_f32 v139, s51, v139\n"
"ds_write_b128 v80, v[128:131]\n"
"ds_write_b128 v81, v[132:135]\n"
"ds_write_b64 v82, v[136:137]\n"
"ds_write_b64 v83, v[138:139] offset:128\n"
"v_mov_b32 v87, s50\n"
"ds_write_b32 v36, v87\n"
"ds_read_b32 v100, v100\n"
"ds_read_b32 v101, v101\n"
"ds_read_b32 v102, v102\n"
"ds_read_b32 v103, v103\n"
"ds_read_b32 v104, v104\n"
"ds_read_b32 v105, v105\n"
"ds_read_b32 v106, v106\n"
"ds_read_b32 v107, v107\n"
"ds_read2_b32 v[48:49], v84 offset0:6 offset1:4\n"
"ds_read2_b32 v[50:51], v84 offset0:2 offset1:0\n"
"ds_read2_b32 v[52:53], v84 offset0:7 offset1:5\n"
"ds_read2_b32 v[54:55], v84 offset0:3 offset1:1\n"
"ds_read2_b32 v[56:57], v84 offset0:38 offset1:36\n"
"ds_read2_b32 v[58:59], v84 offset0:34 offset1:32\n"
"ds_read2_b32 v[60:61], v84 offset0:39 offset1:37\n"
"ds_read2_b32 v[62:63], v84 offset0:35 offset1:33\n"
"ds_read2_b32 v[64:65], v84 offset0:70 offset1:68\n"
"ds_read2_b32 v[66:67], v84 offset0:66 offset1:64\n"
"ds_read2_b32 v[68:69], v84 offset0:71 offset1:69\n"
"ds_read2_b32 v[70:71], v84 offset0:67 offset1:65\n"
"ds_read2_b32 v[72:73], v84 offset0:102 offset1:100\n"
"ds_read2_b32 v[74:75], v84 offset0:98 offset1:96\n"
"ds_read2_b32 v[76:77], v84 offset0:103 offset1:101\n"
"ds_read2_b32 v[78:79], v84 offset0:99 offset1:97\n"
"s_waitcnt lgkmcnt(15)\n"
"v_cvt_pk_f16_f32 v152, v100, v101\n"
"v_cvt_pk_f16_f32 v153, v102, v103\n"
"v_cvt_pk_f16_f32 v154, v104, v105\n"
"v_cvt_pk_f16_f32 v155, v106, v107\n"
"v_cvt_pk_f16_f32 v128, v48, v49\n"
"s_waitcnt lgkmcnt(14)\n"
"v_cvt_pk_f16_f32 v129, v50, v51\n"
"s_waitcnt lgkmcnt(13)\n"
"v_cvt_pk_f16_f32 v130, v52, v53\n"
"s_waitcnt lgkmcnt(12)\n"
"v_cvt_pk_f16_f32 v131, v54, v55\n"
"s_waitcnt lgkmcnt(11)\n"
"v_cvt_pk_f16_f32 v132, v56, v57\n"
"s_waitcnt lgkmcnt(10)\n"
"v_cvt_pk_f16_f32 v133, v58, v59\n"
"s_waitcnt lgkmcnt(9)\n"
"v_cvt_pk_f16_f32 v134, v60, v61\n"
"s_waitcnt lgkmcnt(8)\n"
"v_cvt_pk_f16_f32 v135, v62, v63\n"
"s_waitcnt lgkmcnt(7)\n"
"v_cvt_pk_f16_f32 v136, v64, v65\n"
"s_waitcnt lgkmcnt(6)\n"
"v_cvt_pk_f16_f32 v137, v66, v67\n"
"s_waitcnt lgkmcnt(5)\n"
"v_cvt_pk_f16_f32 v138, v68, v69\n"
"s_waitcnt lgkmcnt(4)\n"
"v_cvt_pk_f16_f32 v139, v70, v71\n"
"s_waitcnt lgkmcnt(3)\n"
"v_cvt_pk_f16_f32 v140, v72, v73\n"
"s_waitcnt lgkmcnt(2)\n"
"v_cvt_pk_f16_f32 v141, v74, v75\n"
"s_waitcnt lgkmcnt(1)\n"
"v_cvt_pk_f16_f32 v142, v76, v77\n"
"s_waitcnt lgkmcnt(0)\n"
"v_cvt_pk_f16_f32 v143, v78, v79\n"
"s_mov_b32 s43, 1\n"
"s_nop 3\n"
"s_setpc_b64 s[46:47]\n"
".Lst_289:\n"
"s_nop 7\n"
"v_lshlrev_b32 v79, 3, v0\n"
"v_lshlrev_b32 v78, 4, v0\n"
"v_lshlrev_b32 v77, 5, v0\n"
"v_lshrrev_b32 v76, 5, v0\n"
"v_sub_u32 v75, 0x5f0, v78\n"
"v_sub_u32 v74, 0x7e0, v77\n"
"v_cmp_le_i32 vcc, 32, v0\n"
"s_nop 1\n"
"v_cndmask_b32 v73, v75, v74, vcc\n"
"s_mul_i32 s79, s3, 0x2100\n"
"s_add_u32 s79, s79, 0x15100\n"
"s_sub_u32 s79, s79, 8\n"
"v_add_u32 v72, s79, v73\n"
"s_mul_i32 s79, s3, 0x2100\n"
"s_add_u32 s79, s79, 0x15100\n"
"s_add_u32 s79, s79, 0x7f4\n"
"v_sub_u32 v108, s79, v79\n"
"v_mov_b32 v99, 4\n"
"v_cmp_eq_i32 vcc, 63, v0\n"
"s_nop 1\n"
"v_cndmask_b32 v99, 0, v99, vcc\n"
"v_sub_u32 v109, v108, v99\n"
"v_mov_b32 v99, 4\n"
"v_cmp_le_i32 vcc, 31, v0\n"
"s_nop 1\n"
"v_cndmask_b32 v99, 0, v99, vcc\n"
"v_sub_u32 v110, v72, v99\n"
"s_mul_i32 s79, s3, 0x2100\n"
"s_add_u32 s79, s79, 0x14000\n"
"v_add_u32 v115, s79, v78\n"
"s_mul_i32 s79, s3, 0x2100\n"
"s_add_u32 s79, s79, 0x14000\n"
"s_add_u32 s79, s79, 0x800\n"
"v_and_b32 v99, 15, v0\n"
"v_lshrrev_b32 v98, 4, v0\n"
"v_mul_i32_i24 v98, 0x840, v98\n"
"v_lshl_add_u32 v99, v99, 2, v98\n"
"v_add_u32 v116, s79, v99\n"
"v_mov_b32 v117, 0\n"
"v_mad_i32_i24 v111, v76, 0, v72\n"
"v_lshl_add_u32 v112, v76, 2, v72\n"
"v_lshl_add_u32 v113, v76, 3, v72\n"
"v_mad_i32_i24 v114, v76, 12, v72\n"
"ds_read_b32 v48, v108 offset:12\n"
"ds_read_b32 v49, v108 offset:8\n"
"ds_read_b32 v50, v108 offset:4\n"
"ds_read_b32 v51, v109 offset:4\n"
"ds_read_b32 v52, v109 offset:0\n"
"ds_read_b32 v53, v114 offset:24\n"
"ds_read_b32 v54, v114 offset:20\n"
"ds_read_b32 v55, v114 offset:16\n"
"ds_read_b32 v56, v113 offset:16\n"
"ds_read_b32 v57, v112 offset:16\n"
"ds_read_b32 v58, v112 offset:12\n"
"ds_read_b32 v59, v112 offset:8\n"
"ds_read_b32 v60, v111 offset:8\n"
"ds_read_b32 v61, v110 offset:8\n"
"ds_read_b32 v62, v110 offset:4\n"
"ds_read_b32 v63, v110 offset:0\n"
"v_cmp_ne_u32 vcc, 63, v0\n"
"s_waitcnt lgkmcnt(2)\n"
"s_nop 0\n"
"v_cndmask_b32 v61, 0, v61, vcc\n"
"s_waitcnt lgkmcnt(1)\n"
"v_cndmask_b32 v62, 0, v62, vcc\n"
"s_waitcnt lgkmcnt(0)\n"
"v_cndmask_b32 v63, 0, v63, vcc\n"
"v_cvt_pk_f16_f32 v64, v48, v48\n"
"v_cvt_pk_f16_f32 v65, v49, v49\n"
"v_cvt_pk_f16_f32 v66, v49, v50\n"
"v_cvt_pk_f16_f32 v67, v50, v50\n"
"ds_write_b128 v115, v[64:67] offset:0\n"
"v_cvt_pk_f16_f32 v68, v48, v49\n"
"v_cvt_pk_f16_f32 v69, v49, v49\n"
"v_cvt_pk_f16_f32 v70, v50, v50\n"
"v_cvt_pk_f16_f32 v71, v50, v50\n"
"ds_write_b128 v115, v[68:71] offset:2112\n"
"v_cvt_pk_f16_f32 v64, v49, v49\n"
"v_cvt_pk_f16_f32 v65, v49, v50\n"
"v_cvt_pk_f16_f32 v66, v50, v50\n"
"v_cvt_pk_f16_f32 v67, v50, v51\n"
"ds_write_b128 v115, v[64:67] offset:4224\n"
"v_cvt_pk_f16_f32 v68, v49, v49\n"
"v_cvt_pk_f16_f32 v69, v50, v50\n"
"v_cvt_pk_f16_f32 v70, v50, v50\n"
"v_cvt_pk_f16_f32 v71, v51, v52\n"
"ds_write_b128 v115, v[68:71] offset:6336\n"
"v_cvt_pk_f16_f32 v64, v53, v54\n"
"v_cvt_pk_f16_f32 v65, v55, v56\n"
"v_cvt_pk_f16_f32 v66, v57, v58\n"
"v_cvt_pk_f16_f32 v67, v59, v60\n"
"ds_write_b128 v115, v[64:67] offset:1024\n"
"v_cvt_pk_f16_f32 v68, v54, v55\n"
"v_cvt_pk_f16_f32 v69, v56, v57\n"
"v_cvt_pk_f16_f32 v70, v58, v59\n"
"v_cvt_pk_f16_f32 v71, v60, v61\n"
"ds_write_b128 v115, v[68:71] offset:3136\n"
"v_cvt_pk_f16_f32 v64, v55, v56\n"
"v_cvt_pk_f16_f32 v65, v57, v58\n"
"v_cvt_pk_f16_f32 v66, v59, v60\n"
"v_cvt_pk_f16_f32 v67, v61, v62\n"
"ds_write_b128 v115, v[64:67] offset:5248\n"
"v_cvt_pk_f16_f32 v68, v56, v57\n"
"v_cvt_pk_f16_f32 v69, v58, v59\n"
"v_cvt_pk_f16_f32 v70, v60, v61\n"
"v_cvt_pk_f16_f32 v71, v62, v63\n"
"ds_write_b128 v115, v[68:71] offset:7360\n"
"ds_write_b32 v116, v117\n"
"ds_write_b32 v4, v4 offset:176\n"
"s_mov_b32 s43, 2\n"
"s_nop 0\n"
"s_setpc_b64 s[46:47]\n"
".Lst_290:\n"
"s_nop 7\n"
"v_and_b32 v79, 15, v0\n"
"v_and_b32 v78, 1, v79\n"
"v_and_b32 v77, 2, v79\n"
"v_lshlrev_b32 v76, 4, v0\n"
"v_lshrrev_b32 v75, 4, v0\n"
"v_xor_b32 v74, s3, v79\n"
"v_and_b32 v73, 1, v74\n"
"v_lshlrev_b32 v72, 1, v73\n"
"v_lshlrev_b32 v71, 1, v75\n"
"v_lshlrev_b32 v70, 3, v75\n"
"v_sub_u32 v69, v70, v79\n"
"v_lshlrev_b32 v68, 6, v75\n"
"v_lshlrev_b32 v67, 4, v77\n"
"v_lshlrev_b32 v66, 5, v79\n"
"v_lshrrev_b32 v65, 1, v75\n"
"v_lshrrev_b32 v64, 1, v79\n"
"v_lshrrev_b32 v63, 2, v79\n"
"v_lshrrev_b32 v62, 3, v79\n"
"v_lshlrev_b32 v61, 7, v62\n"
"v_xor_b32 v60, v63, v65\n"
"s_lshl_b32 s79, s3, 10\n"
"v_add_u32 v30, s79, v76\n"
"v_lshrrev_b32 v99, 2, v30\n"
"v_lshrrev_b32 v98, 6, v30\n"
"v_and_b32 v98, 24, v98\n"
"v_xor_b32 v99, v99, v98\n"
"v_add_u32 v32, 0x20c00, v99\n"
"s_lshl_b32 s79, s3, 8\n"
"s_add_u32 s79, s79, 0x20c00\n"
"v_and_b32 v99, 1, v60\n"
"v_add_u32 v99, v72, v99\n"
"v_lshl_add_u32 v98, v63, 11, v61\n"
"v_add_u32 v98, v98, v68\n"
"v_add_u32 v98, v98, v67\n"
"v_lshl_add_u32 v99, v99, 3, v98\n"
"v_add_u32 v27, s79, v99\n"
"s_mul_i32 s79, s3, 0x2100\n"
"s_add_u32 s79, s79, 0x13840\n"
"v_add_u32 v99, 0x3ef, v69\n"
"v_lshrrev_b32 v99, 2, v99\n"
"v_add_u32 v98, 0x3ef, v69\n"
"v_and_b32 v98, 3, v98\n"
"v_mul_i32_i24 v98, 0x840, v98\n"
"v_lshl_add_u32 v99, v99, 3, v98\n"
"v_add_u32 v24, s79, v99\n"
"s_mul_i32 s79, s3, 0x1200\n"
"s_add_u32 s79, s79, 0x1c420\n"
"v_lshl_add_u32 v99, v75, 4, v66\n"
"v_add_u32 v25, s79, v99\n"
"s_lshl_b32 s79, s3, 11\n"
"s_lshr_b32 s78, s3, 1\n"
"s_lshl_b32 s78, s78, 7\n"
"s_add_u32 s79, s79, s78\n"
"s_add_u32 s79, s79, 0x20c00\n"
"s_and_b32 s78, s3, 1\n"
"v_xor_b32 v99, v75, v63\n"
"v_xor_b32 v99, s78, v99\n"
"v_and_b32 v99, 3, v99\n"
"v_lshl_add_u32 v99, v99, 3, v66\n"
"v_add_u32 v26, s79, v99\n"
"v_xor_b32 v28, 8, v27\n"
"v_lshrrev_b32 v99, 7, v30\n"
"v_and_b32 v98, 0x70, v30\n"
"s_mov_b32 s79, 0x5000\n"
"v_mad_i32_i24 v29, v99, s79, v98\n"
"v_lshrrev_b32 v99, 2, v30\n"
"v_add_u32 v31, 0x1ce00, v99\n"
"v_xor_b32 v33, 8, v32\n"
"v_mov_b32 v99, 0xffff0000\n"
"v_mov_b32 v98, 0xffff\n"
"v_cmp_eq_i32 vcc, 1, v78\n"
"s_nop 1\n"
"v_cndmask_b32 v99, v98, v99, vcc\n"
"v_cmp_eq_i32 vcc, v64, v71\n"
"s_nop 1\n"
"v_cndmask_b32 v34, 0, v99, vcc\n"
"v_mov_b32 v99, 0xffff0000\n"
"v_mov_b32 v98, 0xffff\n"
"v_cmp_eq_i32 vcc, 1, v78\n"
"s_nop 1\n"
"v_cndmask_b32 v99, v98, v99, vcc\n"
"v_add_u32 v98, 1, v71\n"
"v_cmp_eq_i32 vcc, v64, v98\n"
"s_nop 1\n"
"v_cndmask_b32 v35, 0, v99, vcc\n"
"s_lshl_b32 s79, s3, 2\n"
"s_add_u32 s79, s79, 0x26800\n"
"v_mov_b32 v36, s79\n"
"v_lshrrev_b32 v99, 6, v30\n"
"v_and_b32 v98, 48, v30\n"
"v_mul_i32_i24 v98, 0x50, v98\n"
"s_mov_b32 s79, 0x2800\n"
"v_mad_i32_i24 v37, v99, s79, v98\n"
"v_lshrrev_b32 v99, 4, v0\n"
"v_lshlrev_b32 v99, 3, v99\n"
"v_and_b32 v98, 15, v0\n"
"v_sub_u32 v60, v99, v98\n"
"v_add_u32 v99, 15, v60\n"
"v_min_i32 v99, 31, v99\n"
"v_mov_b32 v98, 0x7f00000\n"
"v_lshrrev_b32 v61, v99, v98\n"
"v_add_u32 v99, 16, v60\n"
"v_min_i32 v99, 31, v99\n"
"v_mov_b32 v98, 0x1ff00000\n"
"v_lshrrev_b32 v60, v99, v98\n"
"v_bfe_u32 v99, v61, 1, 1\n"
"v_and_b32 v98, 1, v61\n"
"v_lshl_add_u32 v144, v99, 16, v98\n"
"v_mul_u32_u24 v144, 0x3c00, v144\n"
"v_bfe_u32 v99, v61, 3, 1\n"
"v_bfe_u32 v98, v61, 2, 1\n"
"v_lshl_add_u32 v145, v99, 16, v98\n"
"v_mul_u32_u24 v145, 0x3c00, v145\n"
"v_bfe_u32 v99, v61, 5, 1\n"
"v_bfe_u32 v98, v61, 4, 1\n"
"v_lshl_add_u32 v146, v99, 16, v98\n"
"v_mul_u32_u24 v146, 0x3c00, v146\n"
"v_bfe_u32 v99, v61, 7, 1\n"
"v_bfe_u32 v98, v61, 6, 1\n"
"v_lshl_add_u32 v147, v99, 16, v98\n"
"v_mul_u32_u24 v147, 0x3c00, v147\n"
"v_bfe_u32 v99, v60, 1, 1\n"
"v_and_b32 v98, 1, v60\n"
"v_lshl_add_u32 v148, v99, 16, v98\n"
"v_mul_u32_u24 v148, 0x3c00, v148\n"
"v_bfe_u32 v99, v60, 3, 1\n"
"v_bfe_u32 v98, v60, 2, 1\n"
"v_lshl_add_u32 v149, v99, 16, v98\n"
"v_mul_u32_u24 v149, 0x3c00, v149\n"
"v_bfe_u32 v99, v60, 5, 1\n"
"v_bfe_u32 v98, v60, 4, 1\n"
"v_lshl_add_u32 v150, v99, 16, v98\n"
"v_mul_u32_u24 v150, 0x3c00, v150\n"
"v_bfe_u32 v99, v60, 7, 1\n"
"v_bfe_u32 v98, v60, 6, 1\n"
"v_lshl_add_u32 v151, v99, 16, v98\n"
"v_mul_u32_u24 v151, 0x3c00, v151\n"
"v_lshrrev_b32 v99, 4, v0\n"
"v_lshlrev_b32 v99, 3, v99\n"
"v_and_b32 v98, 15, v0\n"
"v_sub_u32 v99, v99, v98\n"
"v_add_u32 v99, 15, v99\n"
"v_min_i32 v119, 31, v99\n"
"v_mov_b32 v121, 0xf8000000\n"
"v_ashrrev_i32 v121, v119, v121\n"
"v_lshrrev_b32 v99, 4, v0\n"
"v_and_b32 v98, 15, v0\n"
"v_sub_u32 v98, 15, v98\n"
"v_lshl_add_u32 v99, v99, 2, v98\n"
"v_mov_b32 v98, 0xffff\n"
"v_lshrrev_b32 v118, v99, v98\n"
"v_bfe_u32 v99, v121, 1, 1\n"
"v_and_b32 v98, 1, v121\n"
"v_lshl_add_u32 v122, v99, 16, v98\n"
"v_mul_u32_u24 v122, 0xffff, v122\n"
"v_bfe_u32 v99, v121, 3, 1\n"
"v_bfe_u32 v98, v121, 2, 1\n"
"v_lshl_add_u32 v123, v99, 16, v98\n"
"v_mul_u32_u24 v123, 0xffff, v123\n"
"v_bfe_u32 v99, v121, 5, 1\n"
"v_bfe_u32 v98, v121, 4, 1\n"
"v_lshl_add_u32 v124, v99, 16, v98\n"
"v_mul_u32_u24 v124, 0xffff, v124\n"
"v_bfe_u32 v99, v121, 7, 1\n"
"v_bfe_u32 v98, v121, 6, 1\n"
"v_lshl_add_u32 v125, v99, 16, v98\n"
"v_mul_u32_u24 v125, 0xffff, v125\n"
"v_bfe_u32 v99, v118, 1, 1\n"
"v_and_b32 v98, 1, v118\n"
"v_lshl_add_u32 v126, v99, 16, v98\n"
"v_mul_u32_u24 v126, 0xffff, v126\n"
"v_bfe_u32 v99, v118, 3, 1\n"
"v_bfe_u32 v98, v118, 2, 1\n"
"v_lshl_add_u32 v127, v99, 16, v98\n"
"v_mul_u32_u24 v127, 0xffff, v127\n"
"v_and_b32 v152, v122, v152\n"
"v_and_b32 v128, v126, v128\n"
"v_and_b32 v153, v123, v153\n"
"v_and_b32 v129, v127, v129\n"
"v_and_b32 v154, v124, v154\n"
"v_and_b32 v130, v126, v130\n"
"v_and_b32 v155, v125, v155\n"
"v_and_b32 v131, v127, v131\n"
"ds_read_b32 v36, v36\n"
"s_waitcnt lgkmcnt(0)\n"
"v_sub_u32 v76, 0x7f000000, v36\n"
"v_lshrrev_b32 v77, 16, v76\n"
"v_or_b32 v76, v76, v77\n"
"v_and_b32 v34, v76, v34\n"
"v_and_b32 v35, v76, v35\n"
"s_mov_b32 s43, 3\n"
"s_nop 3\n"
"s_setpc_b64 s[46:47]\n"
".Loa_291:\n"
"s_nop 7\n"
"s_mov_b32 s17, 32768\n"
".Ltp_292:\n"
"s_waitcnt vmcnt(0)\n"
"s_sub_u32 s17, s17, 1\n"
"s_cbranch_scc1 .Lou_293\n"
"v_max_u32 v38, v88, v89\n"
"v_max3_u32 v38, v90, v91, v38\n"
"v_max3_u32 v38, v92, v93, v38\n"
"v_max3_u32 v38, v94, v95, v38\n"
"v_max3_u32 v38, v96, v97, v38\n"
"v_max3_u32 v38, v98, v99, v38\n"
"v_max3_u32 v38, v100, v101, v38\n"
"v_max3_u32 v38, v102, v103, v38\n"
"v_max3_u32 v38, v104, v105, v38\n"
"v_max3_u32 v38, v106, v107, v38\n"
"v_max3_u32 v38, v108, v109, v38\n"
"v_max3_u32 v38, v110, v111, v38\n"
"v_max3_u32 v38, v112, v113, v38\n"
"v_max3_u32 v38, v114, v115, v38\n"
"v_max3_u32 v38, v116, v117, v38\n"
"v_max3_u32 v38, v118, v119, v38\n"
"v_max3_u32 v38, v120, v121, v38\n"
"v_max3_u32 v38, v122, v123, v38\n"
"v_max3_u32 v38, v124, v125, v38\n"
"v_max3_u32 v38, v126, v127, v38\n"
"v_cmp_eq_u32 vcc, -1, v38\n"
"s_cbranch_vccz .Lou_293\n"
"s_sleep 1\n"
"v_max_u32 v38, v90, v91\n"
"v_max3_u32 v38, v88, v89, v38\n"
"v_cmp_eq_u32 vcc, -1, v38\n"
"s_cbranch_vccz .Lsk_294\n"
"s_mov_b64 exec, vcc\n"
"buffer_load_dwordx4 v[88:91], v2, s[4:7], s32 offen offset:0 sc1\n"
"s_mov_b64 exec, -1\n"
".Lsk_294:\n"
"v_max_u32 v38, v94, v95\n"
"v_max3_u32 v38, v92, v93, v38\n"
"v_cmp_eq_u32 vcc, -1, v38\n"
"s_cbranch_vccz .Lsk_295\n"
"s_mov_b64 exec, vcc\n"
"buffer_load_dwordx4 v[92:95], v2, s[4:7], s32 offen offset:1024 sc1\n"
"s_mov_b64 exec, -1\n"
".Lsk_295:\n"
"v_max_u32 v38, v98, v99\n"
"v_max3_u32 v38, v96, v97, v38\n"
"v_cmp_eq_u32 vcc, -1, v38\n"
"s_cbranch_vccz .Lsk_296\n"
"s_mov_b64 exec, vcc\n"
"buffer_load_dwordx4 v[96:99], v2, s[4:7], s32 offen offset:2048 sc1\n"
"s_mov_b64 exec, -1\n"
".Lsk_296:\n"
"v_max_u32 v38, v102, v103\n"
"v_max3_u32 v38, v100, v101, v38\n"
"v_cmp_eq_u32 vcc, -1, v38\n"
"s_cbranch_vccz .Lsk_297\n"
"s_mov_b64 exec, vcc\n"
"buffer_load_dwordx4 v[100:103], v2, s[4:7], s32 offen offset:3072 sc1\n"
"s_mov_b64 exec, -1\n"
".Lsk_297:\n"
"v_max_u32 v38, v106, v107\n"
"v_max3_u32 v38, v104, v105, v38\n"
"v_cmp_eq_u32 vcc, -1, v38\n"
"s_cbranch_vccz .Lsk_298\n"
"s_mov_b64 exec, vcc\n"
"buffer_load_dwordx4 v[104:107], v2, s[4:7], s33 offen offset:0 sc1\n"
"s_mov_b64 exec, -1\n"
".Lsk_298:\n"
"v_max_u32 v38, v110, v111\n"
"v_max3_u32 v38, v108, v109, v38\n"
"v_cmp_eq_u32 vcc, -1, v38\n"
"s_cbranch_vccz .Lsk_299\n"
"s_mov_b64 exec, vcc\n"
"buffer_load_dwordx4 v[108:111], v2, s[4:7], s34 offen offset:0 sc1\n"
"s_mov_b64 exec, -1\n"
".Lsk_299:\n"
"v_max_u32 v38, v114, v115\n"
"v_max3_u32 v38, v112, v113, v38\n"
"v_cmp_eq_u32 vcc, -1, v38\n"
"s_cbranch_vccz .Lsk_300\n"
"s_mov_b64 exec, vcc\n"
"buffer_load_dwordx4 v[112:115], v2, s[4:7], s34 offen offset:1024 sc1\n"
"s_mov_b64 exec, -1\n"
".Lsk_300:\n"
"v_max_u32 v38, v118, v119\n"
"v_max3_u32 v38, v116, v117, v38\n"
"v_cmp_eq_u32 vcc, -1, v38\n"
"s_cbranch_vccz .Lsk_301\n"
"s_mov_b64 exec, vcc\n"
"buffer_load_dwordx4 v[116:119], v2, s[4:7], s34 offen offset:2048 sc1\n"
"s_mov_b64 exec, -1\n"
".Lsk_301:\n"
"v_max_u32 v38, v122, v123\n"
"v_max3_u32 v38, v120, v121, v38\n"
"v_cmp_eq_u32 vcc, -1, v38\n"
"s_cbranch_vccz .Lsk_302\n"
"s_mov_b64 exec, vcc\n"
"buffer_load_dwordx4 v[120:123], v2, s[4:7], s34 offen offset:3072 sc1\n"
"s_mov_b64 exec, -1\n"
".Lsk_302:\n"
"v_max_u32 v38, v126, v127\n"
"v_max3_u32 v38, v124, v125, v38\n"
"v_cmp_eq_u32 vcc, -1, v38\n"
"s_cbranch_vccz .Lsk_303\n"
"s_mov_b64 exec, vcc\n"
"buffer_load_dwordx4 v[124:127], v2, s[4:7], s35 offen offset:0 sc1\n"
"s_mov_b64 exec, -1\n"
".Lsk_303:\n"
"s_branch .Ltp_292\n"
".Lou_293:\n"
"s_waitcnt vmcnt(0)\n"
"s_setpc_b64 s[46:47]\n"
".Lsl_315:\n"
"s_nop 7\n"
"s_call_b64 s[46:47], .Loa_291\n"
"s_branch .Lbk_316\n"
".Lsl_317:\n"
"s_nop 7\n"
"s_call_b64 s[46:47], .Loa_291\n"
"s_branch .Lbk_318\n"
".Lsl_319:\n"
"s_nop 7\n"
"s_call_b64 s[46:47], .Loa_291\n"
"s_branch .Lbk_320\n"
".Lsl_321:\n"
"s_nop 7\n"
"s_call_b64 s[46:47], .Loa_291\n"
"s_branch .Lbk_322\n"
".Lsl_323:\n"
"s_nop 7\n"
"s_call_b64 s[46:47], .Loa_291\n"
"s_branch .Lbk_324\n"
".Lsl_325:\n"
"s_nop 7\n"
"s_call_b64 s[46:47], .Loa_291\n"
"s_branch .Lbk_326\n"
".Lsl_327:\n"
"s_nop 7\n"
"s_call_b64 s[46:47], .Loa_291\n"
"s_branch .Lbk_328\n"
".Lsl_329:\n"
"s_nop 7\n"
"s_call_b64 s[46:47], .Loa_291\n"
"s_branch .Lbk_330\n"
".Lsl_331:\n"
"s_nop 7\n"
"s_call_b64 s[46:47], .Loa_291\n"
"s_branch .Lbk_332\n"
".Lsl_333:\n"
"s_nop 7\n"
"s_call_b64 s[46:47], .Loa_291\n"
"s_branch .Lbk_334\n"
".Lsl_353:\n"
"s_nop 7\n"
"s_call_b64 s[46:47], .Loa_291\n"
"s_branch .Lbk_354\n"
".Lsl_355:\n"
"s_nop 7\n"
"s_call_b64 s[46:47], .Loa_291\n"
"s_branch .Lbk_356\n"
".Lsl_357:\n"
"s_nop 7\n"
"s_call_b64 s[46:47], .Loa_291\n"
"s_branch .Lbk_358\n"
".Lsl_359:\n"
"s_nop 7\n"
"s_call_b64 s[46:47], .Loa_291\n"
"s_branch .Lbk_360\n"
".Lsl_361:\n"
"s_nop 7\n"
"s_call_b64 s[46:47], .Loa_291\n"
"s_branch .Lbk_362\n"
".Lsl_363:\n"
"s_nop 7\n"
"s_call_b64 s[46:47], .Loa_291\n"
"s_branch .Lbk_364\n"
".Lsl_365:\n"
"s_nop 7\n"
"s_call_b64 s[46:47], .Loa_291\n"
"s_branch .Lbk_366\n"
".Lsl_367:\n"
"s_nop 7\n"
"s_call_b64 s[46:47], .Loa_291\n"
"s_branch .Lbk_368\n"
".Lsl_369:\n"
"s_nop 7\n"
"s_call_b64 s[46:47], .Loa_291\n"
"s_branch .Lbk_370\n"
".Lsl_371:\n"
"s_nop 7\n"
"s_call_b64 s[46:47], .Loa_291\n"
"s_branch .Lbk_372\n"
".Lsl_446:\n"
"s_nop 7\n"
"s_call_b64 s[46:47], .Loa_291\n"
"s_branch .Lbk_447\n"
".Lsl_448:\n"
"s_nop 7\n"
"s_call_b64 s[46:47], .Loa_291\n"
"s_branch .Lbk_449\n"
".Lsl_450:\n"
"s_nop 7\n"
"s_call_b64 s[46:47], .Loa_291\n"
"s_branch .Lbk_451\n"
".Lsl_452:\n"
"s_nop 7\n"
"s_call_b64 s[46:47], .Loa_291\n"
"s_branch .Lbk_453\n"
".Lsl_454:\n"
"s_nop 7\n"
"s_call_b64 s[46:47], .Loa_291\n"
"s_branch .Lbk_455\n"
".Lsl_456:\n"
"s_nop 7\n"
"s_call_b64 s[46:47], .Loa_291\n"
"s_branch .Lbk_457\n"
".Lsl_458:\n"
"s_nop 7\n"
"s_call_b64 s[46:47], .Loa_291\n"
"s_branch .Lbk_459\n"
".Lsl_460:\n"
"s_nop 7\n"
"s_call_b64 s[46:47], .Loa_291\n"
"s_branch .Lbk_461\n"
".Lsl_462:\n"
"s_nop 7\n"
"s_call_b64 s[46:47], .Loa_291\n"
"s_branch .Lbk_463\n"
".Lsl_464:\n"
"s_nop 7\n"
"s_call_b64 s[46:47], .Loa_291\n"
"s_branch .Lbk_465\n"
".Lsl_484:\n"
"s_nop 7\n"
"s_call_b64 s[46:47], .Loa_291\n"
"s_branch .Lbk_485\n"
".Lsl_486:\n"
"s_nop 7\n"
"s_call_b64 s[46:47], .Loa_291\n"
"s_branch .Lbk_487\n"
".Lsl_488:\n"
"s_nop 7\n"
"s_call_b64 s[46:47], .Loa_291\n"
"s_branch .Lbk_489\n"
".Lsl_490:\n"
"s_nop 7\n"
"s_call_b64 s[46:47], .Loa_291\n"
"s_branch .Lbk_491\n"
".Lsl_492:\n"
"s_nop 7\n"
"s_call_b64 s[46:47], .Loa_291\n"
"s_branch .Lbk_493\n"
".Lsl_494:\n"
"s_nop 7\n"
"s_call_b64 s[46:47], .Loa_291\n"
"s_branch .Lbk_495\n"
".Lsl_496:\n"
"s_nop 7\n"
"s_call_b64 s[46:47], .Loa_291\n"
"s_branch .Lbk_497\n"
".Lsl_498:\n"
"s_nop 7\n"
"s_call_b64 s[46:47], .Loa_291\n"
"s_branch .Lbk_499\n"
".Lsl_500:\n"
"s_nop 7\n"
"s_call_b64 s[46:47], .Loa_291\n"
"s_branch .Lbk_501\n"
".Lsl_502:\n"
"s_nop 7\n"
"s_call_b64 s[46:47], .Loa_291\n"
"s_branch .Lbk_503\n"
".L_yend:\n"
      :: "{s[0:1]}"(__builtin_amdgcn_kernarg_segment_ptr()), "{s2}"((int)__builtin_amdgcn_workgroup_id_x()),
         "{s3}"(__builtin_amdgcn_readfirstlane(tid >> 6)), "{v0}"(tid),
         "{v1}"((unsigned)(size_t)(__attribute__((address_space(3))) unsigned char*)smem)
      : "memory", "vcc", "scc", "m0", "v255", "s95");
}

extern "C" void kernel_launch(void* const* d_in, const int* in_sizes, int n_in,
                              void* d_out, int out_size, void* d_ws, size_t ws_size,
                              hipStream_t stream) {
  mega_kernel<<<256, 512, 0, stream>>>((const u16*)d_in[0], (const u16*)d_in[1], (const u16*)d_in[2],
                                       (const u16*)d_in[4], (const u16*)d_in[5], (const u16*)d_in[6],
                                       (const u16*)d_in[7], (const u16*)d_in[8], (const u16*)d_in[9],
                                       (u16*)d_out, (unsigned char*)d_ws);
}
```

```cpp
#include <hip/hip_runtime.h>
typedef unsigned short u16;

__global__ __launch_bounds__(512, 2) void mega_kernel(
    const u16* __restrict__ x_, const u16* __restrict__ wqkv_, const u16* __restrict__ wo_,
    const u16* __restrict__ log_A_real_, const u16* __restrict__ A_imag_,
    const u16* __restrict__ C_real_, const u16* __restrict__ C_imag_, const u16* __restrict__ log_dt_,
    const u16* __restrict__ D_skip, u16* __restrict__ out, unsigned char* __restrict__ ws_) {
  __shared__ __attribute__((aligned(16))) unsigned char smem[163840];
  const int tid = (int)__builtin_amdgcn_workitem_id_x();
  asm volatile(
"s_cmp_lt_u32 s3, 4\n"
"s_cbranch_scc1 .Lxs_1\n"
"s_load_dwordx8 s[8:15], s[0:1], 0x18\n"
"s_load_dwordx4 s[16:19], s[0:1], 0x38\n"
"s_load_dwordx2 s[22:23], s[0:1], 0x0\n"
"s_load_dwordx4 s[24:27], s[0:1], 0x48\n"
"s_getpc_b64 s[4:5]\n"
".Lmk_a:\n"
"v_lshrrev_b32 v79, 6, v0\n"
"v_and_b32 v78, 7, v0\n"
"v_lshl_add_u32 v79, v79, 3, v78\n"
"s_lshr_b32 s79, s2, 3\n"
"s_lshl_b32 s79, s79, 7\n"
"s_sub_u32 s79, s79, 0x20000\n"
"v_lshl_add_u32 v2, v79, 12, s79\n"
"v_min_u32 v2, .L_yend-.Lmk_a-4, v2\n"
"s_mov_b32 m0, 163328\n"
"v_mov_b32 v4, 0\n"
"global_load_lds_dword v2, s[4:5]\n"
"v_lshlrev_b32 v79, 2, v0\n"
"v_add_u32 v2, 0x26000, v79\n"
"ds_write_b32 v2, v4\n"
"v_lshlrev_b32 v79, 4, v0\n"
"v_add_u32 v2, 0x25840, v79\n"
"v_mov_b32 v8, -1\n"
"v_mov_b32 v9, -1\n"
"v_mov_b32 v10, -1\n"
"v_mov_b32 v11, -1\n"
"ds_write_b128 v2, v[8:11]\n"
"ds_write_b128 v2, v[8:11] offset:4096\n"
"s_and_b32 s79, s2, 7\n"
"s_lshl_b32 s79, s79, 4\n"
"s_lshr_b32 s78, s2, 3\n"
"s_add_u32 s79, s79, s78\n"
"s_and_b32 s78, s2, 3\n"
"s_lshl_b32 s78, s78, 3\n"
"s_and_b32 s77, s2, 4\n"
"s_cmp_eq_i32 s77, 4\n"
"s_cselect_b32 s78, 32, s78\n"
"s_add_u32 s79, s79, s78\n"
"s_lshl_b32 s79, s79, 2\n"
"s_and_b32 s78, s3, 3\n"
"s_add_u32 s6, s79, s78\n"
"v_and_b32 v79, 63, v0\n"
"s_mul_i32 s79, s6, 0x180\n"
"v_lshl_add_u32 v2, v79, 1, s79\n"
"s_lshl_b32 s79, s6, 1\n"
"s_and_b32 s20, s79, 0xffc\n"
"s_and_b32 s79, s2, 4\n"
"s_lshl_b32 s79, s79, 4\n"
"s_add_u32 s7, s2, s79\n"
"s_waitcnt lgkmcnt(0)\n"
"s_barrier\n"
"s_cmp_ge_u32 s7, 192\n"
"s_cbranch_scc1 .Lnl_2\n"
"s_load_dword s40, s[16:17], s20\n"
"global_load_ushort v40, v2, s[8:9] offset:0\n"
"global_load_ushort v41, v2, s[10:11] offset:0\n"
"global_load_ushort v42, v2, s[12:13] offset:0\n"
"global_load_ushort v43, v2, s[14:15] offset:0\n"
"global_load_ushort v44, v2, s[8:9] offset:128\n"
"global_load_ushort v45, v2, s[10:11] offset:128\n"
"global_load_ushort v46, v2, s[12:13] offset:128\n"
"global_load_ushort v47, v2, s[14:15] offset:128\n"
"global_load_ushort v48, v2, s[8:9] offset:256\n"
"global_load_ushort v49, v2, s[10:11] offset:256\n"
"global_load_ushort v50, v2, s[12:13] offset:256\n"
"global_load_ushort v51, v2, s[14:15] offset:256\n"
"s_load_dword s41, s[18:19], s20\n"
".Lnl_2:\n"
"s_load_dword s16, s[26:27], 0x0\n"
"s_branch .L_xend\n"
".Lxs_1:\n"
"s_load_dwordx4 s[20:23], s[0:1], 0x0\n"
"s_load_dwordx2 s[56:57], s[0:1], 0x10\n"
"s_load_dwordx4 s[24:27], s[0:1], 0x48\n"
"v_and_b32 v0, 63, v0\n"
"s_barrier\n"
"s_lshl_b32 s79, s2, 2\n"
"s_add_u32 s31, s79, s3\n"
"s_mul_i32 s79, s31, 0xcd\n"
"s_lshr_b32 s46, s79, 11\n"
"s_mul_i32 s79, s46, 10\n"
"s_sub_u32 s47, s31, s79\n"
"s_lshl_b32 s79, s47, 6\n"
"v_add_u32 v68, s79, v0\n"
"v_lshlrev_b32 v80, 1, v68\n"
"s_mul_i32 s48, s46, 0x7800\n"
"s_mov_b32 s14, 0x258000\n"
"s_mov_b32 s15, 0x20000\n"
"s_waitcnt lgkmcnt(0)\n"
"s_mov_b64 s[12:13], s[22:23]\n"
"s_cmp_ge_u32 s2, 200\n"
"s_cbranch_scc1 .Lnorep_3\n"
"buffer_load_ushort v28, v80, s[12:15], s48 offen offset:0\n"
"buffer_load_ushort v36, v80, s[12:15], s48 offen offset:1280\n"
"buffer_load_ushort v44, v80, s[12:15], s48 offen offset:2560\n"
"s_add_u32 s48, s48, 3840\n"
"buffer_load_ushort v29, v80, s[12:15], s48 offen offset:0\n"
"buffer_load_ushort v37, v80, s[12:15], s48 offen offset:1280\n"
"buffer_load_ushort v45, v80, s[12:15], s48 offen offset:2560\n"
"s_add_u32 s48, s48, 3840\n"
"buffer_load_ushort v30, v80, s[12:15], s48 offen offset:0\n"
"buffer_load_ushort v38, v80, s[12:15], s48 offen offset:1280\n"
"buffer_load_ushort v46, v80, s[12:15], s48 offen offset:2560\n"
"s_add_u32 s48, s48, 3840\n"
"buffer_load_ushort v31, v80, s[12:15], s48 offen offset:0\n"
"buffer_load_ushort v39, v80, s[12:15], s48 offen offset:1280\n"
"buffer_load_ushort v47, v80, s[12:15], s48 offen offset:2560\n"
"s_add_u32 s48, s48, 3840\n"
"buffer_load_ushort v32, v80, s[12:15], s48 offen offset:0\n"
"buffer_load_ushort v40, v80, s[12:15], s48 offen offset:1280\n"
"buffer_load_ushort v48, v80, s[12:15], s48 offen offset:2560\n"
"s_add_u32 s48, s48, 3840\n"
"buffer_load_ushort v33, v80, s[12:15], s48 offen offset:0\n"
"buffer_load_ushort v41, v80, s[12:15], s48 offen offset:1280\n"
"buffer_load_ushort v49, v80, s[12:15], s48 offen offset:2560\n"
"s_add_u32 s48, s48, 3840\n"
"buffer_load_ushort v34, v80, s[12:15], s48 offen offset:0\n"
"buffer_load_ushort v42, v80, s[12:15], s48 offen offset:1280\n"
"buffer_load_ushort v50, v80, s[12:15], s48 offen offset:2560\n"
"s_add_u32 s48, s48, 3840\n"
"buffer_load_ushort v35, v80, s[12:15], s48 offen offset:0\n"
"buffer_load_ushort v43, v80, s[12:15], s48 offen offset:1280\n"
"buffer_load_ushort v51, v80, s[12:15], s48 offen offset:2560\n"
"s_branch .Lrep_4\n"
".Lnorep_3:\n"
"s_sleep 20\n"
"s_mov_b64 s[12:13], s[56:57]\n"
"s_cmp_lt_i32 s31, 0x3e8\n"
"s_cselect_b32 s14, 0xc8000, 0\n"
"v_lshlrev_b32 v80, 1, v0\n"
"s_lshl_b32 s79, s31, 2\n"
"s_add_u32 s79, s79, 0xfffff380\n"
"s_mul_i32 s79, s79, 0xcd\n"
"s_lshr_b32 s80, s79, 11\n"
"s_lshl_b32 s79, s31, 2\n"
"s_add_u32 s79, s79, 0xfffff380\n"
"s_mul_i32 s78, s80, 10\n"
"s_sub_u32 s84, s79, s78\n"
"s_mul_i32 s79, s80, 0x67\n"
"s_lshr_b32 s79, s79, 9\n"
"s_mul_i32 s79, s79, 20\n"
"s_lshl_b32 s78, s80, 2\n"
"s_add_u32 s79, s79, s78\n"
"s_mul_i32 s79, s79, 0x500\n"
"s_lshl_b32 s78, s84, 7\n"
"s_add_u32 s48, s79, s78\n"
"s_add_u32 s49, s48, 0x6400\n"
"buffer_load_ushort v100, v80, s[12:15], s48 offen offset:0\n"
"buffer_load_ushort v101, v80, s[12:15], s48 offen offset:1280\n"
"buffer_load_ushort v102, v80, s[12:15], s48 offen offset:2560\n"
"buffer_load_ushort v103, v80, s[12:15], s48 offen offset:3840\n"
"buffer_load_ushort v104, v80, s[12:15], s49 offen offset:0\n"
"buffer_load_ushort v105, v80, s[12:15], s49 offen offset:1280\n"
"buffer_load_ushort v106, v80, s[12:15], s49 offen offset:2560\n"
"buffer_load_ushort v107, v80, s[12:15], s49 offen offset:3840\n"
"s_lshl_b32 s79, s31, 2\n"
"s_add_u32 s79, s79, 0xfffff381\n"
"s_mul_i32 s79, s79, 0xcd\n"
"s_lshr_b32 s81, s79, 11\n"
"s_lshl_b32 s79, s31, 2\n"
"s_add_u32 s79, s79, 0xfffff381\n"
"s_mul_i32 s78, s81, 10\n"
"s_sub_u32 s85, s79, s78\n"
"s_mul_i32 s79, s81, 0x67\n"
"s_lshr_b32 s79, s79, 9\n"
"s_mul_i32 s79, s79, 20\n"
"s_lshl_b32 s78, s81, 2\n"
"s_add_u32 s79, s79, s78\n"
"s_mul_i32 s79, s79, 0x500\n"
"s_lshl_b32 s78, s85, 7\n"
"s_add_u32 s48, s79, s78\n"
"s_add_u32 s49, s48, 0x6400\n"
"buffer_load_ushort v108, v80, s[12:15], s48 offen offset:0\n"
"buffer_load_ushort v109, v80, s[12:15], s48 offen offset:1280\n"
"buffer_load_ushort v110, v80, s[12:15], s48 offen offset:2560\n"
"buffer_load_ushort v111, v80, s[12:15], s48 offen offset:3840\n"
"buffer_load_ushort v112, v80, s[12:15], s49 offen offset:0\n"
"buffer_load_ushort v113, v80, s[12:15], s49 offen offset:1280\n"
"buffer_load_ushort v114, v80, s[12:15], s49 offen offset:2560\n"
"buffer_load_ushort v115, v80, s[12:15], s49 offen offset:3840\n"
"s_lshl_b32 s79, s31, 2\n"
"s_add_u32 s79, s79, 0xfffff382\n"
"s_mul_i32 s79, s79, 0xcd\n"
"s_lshr_b32 s82, s79, 11\n"
"s_lshl_b32 s79, s31, 2\n"
"s_add_u32 s79, s79, 0xfffff382\n"
"s_mul_i32 s78, s82, 10\n"
"s_sub_u32 s86, s79, s78\n"
"s_mul_i32 s79, s82, 0x67\n"
"s_lshr_b32 s79, s79, 9\n"
"s_mul_i32 s79, s79, 20\n"
"s_lshl_b32 s78, s82, 2\n"
"s_add_u32 s79, s79, s78\n"
"s_mul_i32 s79, s79, 0x500\n"
"s_lshl_b32 s78, s86, 7\n"
"s_add_u32 s48, s79, s78\n"
"s_add_u32 s49, s48, 0x6400\n"
"buffer_load_ushort v116, v80, s[12:15], s48 offen offset:0\n"
"buffer_load_ushort v117, v80, s[12:15], s48 offen offset:1280\n"
"buffer_load_ushort v118, v80, s[12:15], s48 offen offset:2560\n"
"buffer_load_ushort v119, v80, s[12:15], s48 offen offset:3840\n"
"buffer_load_ushort v120, v80, s[12:15], s49 offen offset:0\n"
"buffer_load_ushort v121, v80, s[12:15], s49 offen offset:1280\n"
"buffer_load_ushort v122, v80, s[12:15], s49 offen offset:2560\n"
"buffer_load_ushort v123, v80, s[12:15], s49 offen offset:3840\n"
"s_lshl_b32 s79, s31, 2\n"
"s_add_u32 s79, s79, 0xfffff383\n"
"s_mul_i32 s79, s79, 0xcd\n"
"s_lshr_b32 s83, s79, 11\n"
"s_lshl_b32 s79, s31, 2\n"
"s_add_u32 s79, s79, 0xfffff383\n"
"s_mul_i32 s78, s83, 10\n"
"s_sub_u32 s87, s79, s78\n"
"s_mul_i32 s79, s83, 0x67\n"
"s_lshr_b32 s79, s79, 9\n"
"s_mul_i32 s79, s79, 20\n"
"s_lshl_b32 s78, s83, 2\n"
"s_add_u32 s79, s79, s78\n"
"s_mul_i32 s79, s79, 0x500\n"
"s_lshl_b32 s78, s87, 7\n"
"s_add_u32 s48, s79, s78\n"
"s_add_u32 s49, s48, 0x6400\n"
"buffer_load_ushort v124, v80, s[12:15], s48 offen offset:0\n"
"buffer_load_ushort v125, v80, s[12:15], s48 offen offset:1280\n"
"buffer_load_ushort v126, v80, s[12:15], s48 offen offset:2560\n"
"buffer_load_ushort v127, v80, s[12:15], s48 offen offset:3840\n"
"buffer_load_ushort v128, v80, s[12:15], s49 offen offset:0\n"
"buffer_load_ushort v129, v80, s[12:15], s49 offen offset:1280\n"
"buffer_load_ushort v130, v80, s[12:15], s49 offen offset:2560\n"
"buffer_load_ushort v131, v80, s[12:15], s49 offen offset:3840\n"
".Lrep_4:\n"
"s_mov_b32 s4, s26\n"
"s_mov_b32 s5, s27\n"
"s_mov_b32 s6, 0x15a8000\n"
"s_mov_b32 s7, 0x20000\n"
"s_load_dword s16, s[26:27], 0x0\n"
"v_lshlrev_b32 v1, 4, v0\n"
"s_and_b32 s79, s2, 7\n"
"s_lshl_b32 s79, s79, 4\n"
"s_lshr_b32 s78, s2, 3\n"
"s_add_u32 s79, s79, s78\n"
"s_and_b32 s78, s2, 3\n"
"s_lshl_b32 s78, s78, 3\n"
"s_and_b32 s77, s2, 4\n"
"s_cmp_eq_i32 s77, 4\n"
"s_cselect_b32 s78, 32, s78\n"
"s_add_u32 s79, s79, s78\n"
"s_and_b32 s78, s2, 4\n"
"s_lshl_b32 s78, s78, 4\n"
"s_add_u32 s78, s2, s78\n"
"s_cmp_lt_i32 s78, 0xc0\n"
"s_cselect_b32 s45, s79, -1\n"
"s_mov_b32 s8, s20\n"
"s_mov_b32 s9, s21\n"
"s_mov_b32 s10, 0x280000\n"
"s_mov_b32 s11, 0x20000\n"
"s_mov_b32 s12, s24\n"
"s_mov_b32 s13, s25\n"
"s_mov_b32 s14, 0x280000\n"
"s_mov_b32 s15, 0x20000\n"
"s_and_b32 s79, s2, 1\n"
"s_lshl_b32 s79, s79, 3\n"
"s_lshr_b32 s78, s2, 3\n"
"s_and_b32 s78, s78, 7\n"
"s_add_u32 s18, s79, s78\n"
"s_lshr_b32 s79, s2, 1\n"
"s_and_b32 s79, s79, 3\n"
"s_lshl_b32 s79, s79, 2\n"
"s_lshr_b32 s78, s2, 3\n"
"s_lshr_b32 s78, s78, 3\n"
"s_add_u32 s19, s79, s78\n"
"s_mul_i32 s79, s19, 0\n"
"s_add_u32 s79, s3, s79\n"
"s_and_b32 s43, s79, 3\n"
"v_mul_i32_i24 v79, 0xcd, v68\n"
"v_lshrrev_b32 v84, 13, v79\n"
"v_mul_i32_i24 v79, 40, v84\n"
"v_sub_u32 v85, v68, v79\n"
"v_mul_i32_i24 v79, 0xa0, v84\n"
"v_lshlrev_b32 v84, 10, v79\n"
"v_subrev_u32 v79, 24, v85\n"
"v_and_b32 v78, 15, v85\n"
"v_cmp_le_i32 vcc, 32, v85\n"
"s_nop 1\n"
"v_cndmask_b32 v79, v78, v79, vcc\n"
"v_lshrrev_b32 v78, 4, v85\n"
"s_mov_b32 s79, 0xa000\n"
"v_mad_i32_i24 v78, v78, s79, v84\n"
"v_lshl_add_u32 v64, v79, 4, v78\n"
"v_mov_b32 v86, v85\n"
"v_and_b32 v79, 15, v86\n"
"v_lshrrev_b32 v78, 4, v86\n"
"v_min_i32 v78, 3, v78\n"
"s_mov_b32 s79, 0xa000\n"
"v_mad_i32_i24 v78, v78, s79, v84\n"
"v_mov_b32 v77, 0\n"
"v_mov_b32 v76, 0x400\n"
"v_lshrrev_b32 v75, 4, v86\n"
"v_cmp_eq_i32 vcc, 3, v75\n"
"s_nop 1\n"
"v_cndmask_b32 v77, v76, v77, vcc\n"
"v_add_u32 v78, v78, v77\n"
"v_lshl_add_u32 v65, v79, 4, v78\n"
"v_add_u32 v86, 40, v85\n"
"v_and_b32 v79, 15, v86\n"
"v_lshrrev_b32 v78, 4, v86\n"
"v_min_i32 v78, 3, v78\n"
"s_mov_b32 s79, 0xa000\n"
"v_mad_i32_i24 v78, v78, s79, v84\n"
"v_mov_b32 v77, 0\n"
"v_mov_b32 v76, 0x400\n"
"v_lshrrev_b32 v75, 4, v86\n"
"v_cmp_eq_i32 vcc, 3, v75\n"
"s_nop 1\n"
"v_cndmask_b32 v77, v76, v77, vcc\n"
"v_add_u32 v78, v78, v77\n"
"v_lshl_add_u32 v66, v79, 4, v78\n"
"v_subrev_u32 v79, 0x80, v64\n"
"v_mov_b32 v78, 0x40000000\n"
"v_cmp_le_i32 vcc, 32, v85\n"
"s_nop 1\n"
"v_cndmask_b32 v67, v78, v79, vcc\n"
"s_lshr_b32 s79, s46, 2\n"
"s_lshl_b32 s79, s79, 11\n"
"s_and_b32 s78, s46, 3\n"
"s_lshl_b32 s78, s78, 8\n"
"s_add_u32 s79, s79, s78\n"
"s_add_u32 s48, s79, 0x20000\n"
"s_lshl_b32 s79, s47, 9\n"
"s_lshl_b32 s78, s46, 2\n"
"s_add_u32 s79, s79, s78\n"
"s_lshl_b32 s78, s31, 2\n"
"s_add_u32 s78, s78, 0x780\n"
"s_cmp_lt_i32 s31, 0x320\n"
"s_cselect_b32 s79, s79, s78\n"
"s_add_u32 s32, s79, 0x1000\n"
"v_mov_b32 v20, 0x3f803f80\n"
"v_mov_b32 v21, 0x3f803f80\n"
"v_mov_b32 v22, 0x3f803f80\n"
"v_mov_b32 v23, 0x3f803f80\n"
"s_mov_b32 s79, 0x55\n"
"s_cmp_lt_i32 s47, 5\n"
"s_cselect_b32 s79, s79, 0xaa\n"
"s_cmp_lt_i32 s31, 0x320\n"
"s_cselect_b32 s58, s79, 0xff\n"
"s_mov_b32 s59, 0\n"
"s_mul_i32 s79, s19, 0x14000\n"
"s_mul_i32 s78, s43, 0x140\n"
"s_add_u32 s28, s79, s78\n"
"s_mul_i32 s29, s43, 0x1400\n"
"s_lshr_b32 s79, s2, 3\n"
"s_and_b32 s31, s79, 7\n"
"s_mul_i32 s33, s43, 0x140\n"
"s_sub_u32 s79, s31, 5\n"
"s_cmp_ge_i32 s31, 5\n"
"s_cselect_b32 s31, s79, s31\n"
"s_mul_i32 s79, s31, 0x5000\n"
"s_add_u32 s79, s79, s28\n"
"s_sub_u32 s79, s79, 0x5000\n"
"s_max_i32 s60, s79, s33\n"
"s_mul_i32 s79, s31, 0x5000\n"
"s_mov_b32 s78, 0x12400\n"
"s_cmp_ge_i32 s31, 2\n"
"s_cselect_b32 s78, s78, 0xa000\n"
"s_add_u32 s79, s79, s78\n"
"s_cmp_eq_i32 s31, 4\n"
"s_cselect_b32 s79, 0, s79\n"
"s_add_u32 s61, s29, s79\n"
"s_sub_u32 s79, s31, 4\n"
"s_add_u32 s78, s31, 1\n"
"s_cmp_ge_i32 s31, 4\n"
"s_cselect_b32 s31, s79, s78\n"
"s_mul_i32 s79, s31, 0x5000\n"
"s_add_u32 s79, s79, s28\n"
"s_sub_u32 s79, s79, 0x5000\n"
"s_max_i32 s62, s79, s33\n"
"s_mul_i32 s79, s31, 0x5000\n"
"s_mov_b32 s78, 0x12400\n"
"s_cmp_ge_i32 s31, 2\n"
"s_cselect_b32 s78, s78, 0xa000\n"
"s_add_u32 s79, s79, s78\n"
"s_cmp_eq_i32 s31, 4\n"
"s_cselect_b32 s79, 0, s79\n"
"s_add_u32 s63, s29, s79\n"
"s_sub_u32 s79, s31, 4\n"
"s_add_u32 s78, s31, 1\n"
"s_cmp_ge_i32 s31, 4\n"
"s_cselect_b32 s31, s79, s78\n"
"s_mul_i32 s79, s31, 0x5000\n"
"s_add_u32 s79, s79, s28\n"
"s_sub_u32 s79, s79, 0x5000\n"
"s_max_i32 s64, s79, s33\n"
"s_mul_i32 s79, s31, 0x5000\n"
"s_mov_b32 s78, 0x12400\n"
"s_cmp_ge_i32 s31, 2\n"
"s_cselect_b32 s78, s78, 0xa000\n"
"s_add_u32 s79, s79, s78\n"
"s_cmp_eq_i32 s31, 4\n"
"s_cselect_b32 s79, 0, s79\n"
"s_add_u32 s65, s29, s79\n"
"s_sub_u32 s79, s31, 4\n"
"s_add_u32 s78, s31, 1\n"
"s_cmp_ge_i32 s31, 4\n"
"s_cselect_b32 s31, s79, s78\n"
"s_mul_i32 s79, s31, 0x5000\n"
"s_add_u32 s79, s79, s28\n"
"s_sub_u32 s79, s79, 0x5000\n"
"s_max_i32 s66, s79, s33\n"
"s_mul_i32 s79, s31, 0x5000\n"
"s_mov_b32 s78, 0x12400\n"
"s_cmp_ge_i32 s31, 2\n"
"s_cselect_b32 s78, s78, 0xa000\n"
"s_add_u32 s79, s79, s78\n"
"s_cmp_eq_i32 s31, 4\n"
"s_cselect_b32 s79, 0, s79\n"
"s_add_u32 s67, s29, s79\n"
"s_sub_u32 s79, s31, 4\n"
"s_add_u32 s78, s31, 1\n"
"s_cmp_ge_i32 s31, 4\n"
"s_cselect_b32 s31, s79, s78\n"
"s_mul_i32 s79, s31, 0x5000\n"
"s_add_u32 s79, s79, s28\n"
"s_sub_u32 s79, s79, 0x5000\n"
"s_max_i32 s68, s79, s33\n"
"s_mul_i32 s79, s31, 0x5000\n"
"s_mov_b32 s78, 0x12400\n"
"s_cmp_ge_i32 s31, 2\n"
"s_cselect_b32 s78, s78, 0xa000\n"
"s_add_u32 s79, s79, s78\n"
"s_cmp_eq_i32 s31, 4\n"
"s_cselect_b32 s79, 0, s79\n"
"s_add_u32 s69, s29, s79\n"
"v_and_b32 v79, 3, v0\n"
"v_lshrrev_b32 v78, 5, v0\n"
"v_lshlrev_b32 v78, 1, v78\n"
"v_xor_b32 v79, v79, v78\n"
"v_lshrrev_b32 v78, 2, v0\n"
"v_mul_i32_i24 v78, 0x500, v78\n"
"v_lshl_add_u32 v2, v79, 4, v78\n"
"v_min_i32 v79, 24, v0\n"
"s_lshr_b32 s79, s2, 3\n"
"s_and_b32 s79, s79, 7\n"
"s_lshl_b32 s79, s79, 2\n"
"s_add_u32 s79, s79, s3\n"
"s_mul_i32 s79, s79, 0xc80\n"
"s_lshl_b32 s78, s19, 6\n"
"s_add_u32 s78, s78, 0x3f0\n"
"s_mul_i32 s78, s78, 0x500\n"
"s_add_u32 s79, s79, s78\n"
"v_lshl_add_u32 v89, v79, 7, s79\n"
"s_mov_b32 s31, 0x05040100\n"
"s_cmp_ge_u32 s2, 200\n"
"s_cbranch_scc1 .Lnorep_5\n"
"buffer_store_dwordx4 v[20:23], v67, s[4:7], s48 offen sc1\n"
"s_waitcnt vmcnt(1)\n"
"v_perm_b32 v52, v29, v28, s31\n"
"v_perm_b32 v53, v31, v30, s31\n"
"v_perm_b32 v54, v33, v32, s31\n"
"v_perm_b32 v55, v35, v34, s31\n"
"buffer_store_dwordx4 v[52:55], v64, s[4:7], s48 offen sc1\n"
"v_perm_b32 v56, v37, v36, s31\n"
"v_perm_b32 v57, v39, v38, s31\n"
"v_perm_b32 v58, v41, v40, s31\n"
"v_perm_b32 v59, v43, v42, s31\n"
"buffer_store_dwordx4 v[56:59], v65, s[4:7], s48 offen sc1\n"
"v_perm_b32 v60, v45, v44, s31\n"
"v_perm_b32 v61, v47, v46, s31\n"
"v_perm_b32 v62, v49, v48, s31\n"
"v_perm_b32 v63, v51, v50, s31\n"
"buffer_store_dwordx4 v[60:63], v66, s[4:7], s48 offen sc1\n"
"s_branch .Lrep_6\n"
".Lnorep_5:\n"
"s_lshl_b32 s79, s84, 6\n"
"v_add_u32 v68, s79, v0\n"
"v_mul_i32_i24 v79, 0xcd, v68\n"
"v_lshrrev_b32 v81, 14, v79\n"
"s_lshr_b32 s79, s80, 2\n"
"s_mul_i32 s79, s79, 13\n"
"s_lshr_b32 s79, s79, 6\n"
"s_and_b32 s79, s79, 1\n"
"s_lshl_b32 s79, s79, 1\n"
"v_mul_i32_i24 v79, 0x50, v81\n"
"v_sub_u32 v79, v68, v79\n"
"v_lshrrev_b32 v79, 4, v79\n"
"v_add_u32 v82, s79, v79\n"
"v_subrev_u32 v79, 5, v82\n"
"v_cmp_le_i32 vcc, 5, v82\n"
"s_nop 1\n"
"v_cndmask_b32 v79, v82, v79, vcc\n"
"s_mov_b32 s79, 0x64\n"
"v_mad_i32_i24 v79, v81, s79, v79\n"
"v_and_b32 v78, 15, v68\n"
"v_lshlrev_b32 v78, 4, v78\n"
"v_lshl_add_u32 v64, v79, 10, v78\n"
"s_lshr_b32 s79, s80, 2\n"
"s_mul_i32 s79, s79, 0x1400\n"
"s_and_b32 s78, s80, 3\n"
"s_lshl_b32 s78, s78, 8\n"
"s_add_u32 s79, s79, s78\n"
"s_add_u32 s79, s79, 0x2a0000\n"
"s_cmp_lt_i32 s80, 0x50\n"
"s_cselect_b32 s80, s79, 0x40000000\n"
"s_lshl_b32 s79, s85, 6\n"
"v_add_u32 v68, s79, v0\n"
"v_mul_i32_i24 v79, 0xcd, v68\n"
"v_lshrrev_b32 v81, 14, v79\n"
"s_lshr_b32 s79, s81, 2\n"
"s_mul_i32 s79, s79, 13\n"
"s_lshr_b32 s79, s79, 6\n"
"s_and_b32 s79, s79, 1\n"
"s_lshl_b32 s79, s79, 1\n"
"v_mul_i32_i24 v79, 0x50, v81\n"
"v_sub_u32 v79, v68, v79\n"
"v_lshrrev_b32 v79, 4, v79\n"
"v_add_u32 v82, s79, v79\n"
"v_subrev_u32 v79, 5, v82\n"
"v_cmp_le_i32 vcc, 5, v82\n"
"s_nop 1\n"
"v_cndmask_b32 v79, v82, v79, vcc\n"
"s_mov_b32 s79, 0x64\n"
"v_mad_i32_i24 v79, v81, s79, v79\n"
"v_and_b32 v78, 15, v68\n"
"v_lshlrev_b32 v78, 4, v78\n"
"v_lshl_add_u32 v65, v79, 10, v78\n"
"s_lshr_b32 s79, s81, 2\n"
"s_mul_i32 s79, s79, 0x1400\n"
"s_and_b32 s78, s81, 3\n"
"s_lshl_b32 s78, s78, 8\n"
"s_add_u32 s79, s79, s78\n"
"s_add_u32 s79, s79, 0x2a0000\n"
"s_cmp_lt_i32 s81, 0x50\n"
"s_cselect_b32 s81, s79, 0x40000000\n"
"s_lshl_b32 s79, s86, 6\n"
"v_add_u32 v68, s79, v0\n"
"v_mul_i32_i24 v79, 0xcd, v68\n"
"v_lshrrev_b32 v81, 14, v79\n"
"s_lshr_b32 s79, s82, 2\n"
"s_mul_i32 s79, s79, 13\n"
"s_lshr_b32 s79, s79, 6\n"
"s_and_b32 s79, s79, 1\n"
"s_lshl_b32 s79, s79, 1\n"
"v_mul_i32_i24 v79, 0x50, v81\n"
"v_sub_u32 v79, v68, v79\n"
"v_lshrrev_b32 v79, 4, v79\n"
"v_add_u32 v82, s79, v79\n"
"v_subrev_u32 v79, 5, v82\n"
"v_cmp_le_i32 vcc, 5, v82\n"
"s_nop 1\n"
"v_cndmask_b32 v79, v82, v79, vcc\n"
"s_mov_b32 s79, 0x64\n"
"v_mad_i32_i24 v79, v81, s79, v79\n"
"v_and_b32 v78, 15, v68\n"
"v_lshlrev_b32 v78, 4, v78\n"
"v_lshl_add_u32 v66, v79, 10, v78\n"
"s_lshr_b32 s79, s82, 2\n"
"s_mul_i32 s79, s79, 0x1400\n"
"s_and_b32 s78, s82, 3\n"
"s_lshl_b32 s78, s78, 8\n"
"s_add_u32 s79, s79, s78\n"
"s_add_u32 s79, s79, 0x2a0000\n"
"s_cmp_lt_i32 s82, 0x50\n"
"s_cselect_b32 s82, s79, 0x40000000\n"
"s_lshl_b32 s79, s87, 6\n"
"v_add_u32 v68, s79, v0\n"
"v_mul_i32_i24 v79, 0xcd, v68\n"
"v_lshrrev_b32 v81, 14, v79\n"
"s_lshr_b32 s79, s83, 2\n"
"s_mul_i32 s79, s79, 13\n"
"s_lshr_b32 s79, s79, 6\n"
"s_and_b32 s79, s79, 1\n"
"s_lshl_b32 s79, s79, 1\n"
"v_mul_i32_i24 v79, 0x50, v81\n"
"v_sub_u32 v79, v68, v79\n"
"v_lshrrev_b32 v79, 4, v79\n"
"v_add_u32 v82, s79, v79\n"
"v_subrev_u32 v79, 5, v82\n"
"v_cmp_le_i32 vcc, 5, v82\n"
"s_nop 1\n"
"v_cndmask_b32 v79, v82, v79, vcc\n"
"s_mov_b32 s79, 0x64\n"
"v_mad_i32_i24 v79, v81, s79, v79\n"
"v_and_b32 v78, 15, v68\n"
"v_lshlrev_b32 v78, 4, v78\n"
"v_lshl_add_u32 v67, v79, 10, v78\n"
"s_lshr_b32 s79, s83, 2\n"
"s_mul_i32 s79, s79, 0x1400\n"
"s_and_b32 s78, s83, 3\n"
"s_lshl_b32 s78, s78, 8\n"
"s_add_u32 s79, s79, s78\n"
"s_add_u32 s79, s79, 0x2a0000\n"
"s_cmp_lt_i32 s83, 0x50\n"
"s_cselect_b32 s83, s79, 0x40000000\n"
"s_waitcnt vmcnt(0)\n"
"v_perm_b32 v28, v101, v100, s31\n"
"v_perm_b32 v29, v103, v102, s31\n"
"v_perm_b32 v30, v105, v104, s31\n"
"v_perm_b32 v31, v107, v106, s31\n"
"buffer_store_dwordx4 v[28:31], v64, s[4:7], s80 offen sc1\n"
"v_perm_b32 v32, v109, v108, s31\n"
"v_perm_b32 v33, v111, v110, s31\n"
"v_perm_b32 v34, v113, v112, s31\n"
"v_perm_b32 v35, v115, v114, s31\n"
"buffer_store_dwordx4 v[32:35], v65, s[4:7], s81 offen sc1\n"
"v_perm_b32 v36, v117, v116, s31\n"
"v_perm_b32 v37, v119, v118, s31\n"
"v_perm_b32 v38, v121, v120, s31\n"
"v_perm_b32 v39, v123, v122, s31\n"
"buffer_store_dwordx4 v[36:39], v66, s[4:7], s82 offen sc1\n"
"v_perm_b32 v40, v125, v124, s31\n"
"v_perm_b32 v41, v127, v126, s31\n"
"v_perm_b32 v42, v129, v128, s31\n"
"v_perm_b32 v43, v131, v130, s31\n"
"buffer_store_dwordx4 v[40:43], v67, s[4:7], s83 offen sc1\n"
".Lrep_6:\n"
"v_lshlrev_b32 v80, 13, v0\n"
"s_mov_b32 s34, 0xaaaaaaaa\n"
"s_mov_b32 s35, 0xaaaaaaaa\n"
"s_mov_b32 s40, 0xffff\n"
"s_mov_b32 s41, 0\n"
"s_mov_b32 s36, 0xfff0ffff\n"
"s_mov_b32 s37, 0xf000ff00\n"
"s_mov_b32 s38, 0xffc0fffc\n"
"s_mov_b32 s39, 0xc000fc00\n"
"s_lshl_b32 s79, s19, 2\n"
"s_add_u32 s79, s79, s3\n"
"s_mul_i32 s79, s79, 0x5000\n"
"s_mul_i32 s78, s18, 0x500\n"
"s_add_u32 s79, s79, s78\n"
"s_add_u32 s30, s79, 0x368000\n"
"s_mul_i32 s79, s18, 40\n"
"s_lshr_b32 s79, s79, 6\n"
"s_lshl_b32 s79, s79, 9\n"
"v_lshl_add_u32 v79, v0, 4, s79\n"
"v_min_i32 v78, 39, v0\n"
"v_subrev_u32 v78, 20, v78\n"
"s_mul_i32 s79, s18, 40\n"
"s_add_u32 s79, s79, 39\n"
"s_lshr_b32 s79, s79, 6\n"
"s_lshl_b32 s79, s79, 9\n"
"v_lshl_add_u32 v78, v78, 4, s79\n"
"v_cmp_gt_i32 vcc, 20, v0\n"
"s_nop 1\n"
"v_cndmask_b32 v19, v78, v79, vcc\n"
"s_and_b32 s79, s2, 7\n"
"s_lshl_b32 s79, s79, 13\n"
"s_add_u32 s50, s79, 0x1000\n"
"s_mul_i32 s79, s19, 0x14000\n"
"s_mul_i32 s78, s43, 0x1400\n"
"s_add_u32 s79, s79, s78\n"
"s_add_u32 s44, s79, 0xb48000\n"
"s_add_u32 s79, s19, s3\n"
"s_cmp_eq_i32 s79, 0\n"
"s_cselect_b32 s42, 0xfe967699, 0\n"
"s_lshl_b32 s79, s18, 2\n"
"s_add_u32 s79, s79, s3\n"
"s_mul_i32 s79, s79, 0xa000\n"
"s_add_u32 s31, s79, 0x20000\n"
"s_lshl_b32 s79, s43, 11\n"
"s_add_u32 s52, s31, s79\n"
"s_mov_b32 s79, 0xffffe800\n"
"s_cmp_ge_i32 s43, 3\n"
"s_cselect_b32 s79, s79, 0x8800\n"
"s_add_u32 s53, s52, s79\n"
"s_mov_b32 s79, 0xfffff000\n"
"s_cmp_ge_i32 s43, 2\n"
"s_cselect_b32 s79, s79, 0x9000\n"
"s_add_u32 s54, s52, s79\n"
"s_mov_b32 s79, 0xfffff800\n"
"s_cmp_ge_i32 s43, 1\n"
"s_cselect_b32 s79, s79, 0x9800\n"
"s_add_u32 s55, s52, s79\n"
"s_mov_b32 s20, 0xcccccccc\n"
"s_mov_b32 s21, 0xcccccccc\n"
"s_mov_b32 s22, 0xf0000f\n"
"s_mov_b32 s23, 0xf0000f00\n"
"v_and_b32 v169, 3, v0\n"
"v_and_b32 v168, 15, v0\n"
"v_lshrrev_b32 v167, 4, v0\n"
"v_and_b32 v166, 3, v168\n"
"v_lshlrev_b32 v165, 2, v166\n"
"v_lshlrev_b32 v164, 3, v167\n"
"v_lshlrev_b32 v163, 8, v167\n"
"v_lshlrev_b32 v162, 2, v168\n"
"v_lshlrev_b32 v161, 4, v168\n"
"v_lshlrev_b32 v160, 6, v168\n"
"v_lshrrev_b32 v159, 2, v168\n"
"v_lshrrev_b32 v158, 3, v168\n"
"v_lshlrev_b32 v157, 1, v158\n"
"v_mul_i32_i24 v156, 0xf8, v168\n"
"v_add_u32 v155, v161, v165\n"
"v_add_u32 v154, v155, v163\n"
"v_xor_b32 v153, v167, v157\n"
"v_lshlrev_b32 v152, 4, v153\n"
"v_add_u32 v151, v160, v152\n"
"v_add_u32 v17, 0x5000, v154\n"
"v_add_u32 v12, 0x26940, v162\n"
"s_lshl_b32 s79, s3, 4\n"
"v_add_u32 v150, s79, v168\n"
"v_mul_i32_i24 v79, 0xf8, v150\n"
"v_add_u32 v24, 0x5000, v79\n"
"s_lshl_b32 s79, s3, 11\n"
"s_add_u32 s79, s79, 0x5000\n"
"v_add_u32 v79, s79, v161\n"
"v_mov_b32 v78, 0x40000\n"
"v_cmp_eq_i32 vcc, v167, v159\n"
"s_nop 1\n"
"v_cndmask_b32 v92, v78, v79, vcc\n"
"v_mov_b32 v79, 0xf0\n"
"v_add_u32 v78, 48, v164\n"
"v_cmp_gt_i32 vcc, 2, v167\n"
"s_nop 1\n"
"v_cndmask_b32 v79, v78, v79, vcc\n"
"s_lshl_b32 s79, s3, 5\n"
"s_cmp_eq_i32 s3, 3\n"
"s_cselect_b32 s79, 0xb0, s79\n"
"v_add_u32 v78, s79, v164\n"
"v_mov_b32 v77, s3\n"
"v_cmp_eq_i32 vcc, 2, v77\n"
"s_nop 1\n"
"v_cndmask_b32 v79, v78, v79, vcc\n"
"s_mov_b32 s79, 0x5000\n"
"v_add3_u32 v7, v156, v79, s79\n"
"s_lshl_b32 s79, s3, 5\n"
"s_add_u32 s79, s79, 0x50\n"
"s_cmp_eq_i32 s3, 3\n"
"s_cselect_b32 s79, 0xd0, s79\n"
"s_add_u32 s79, s79, 0x5000\n"
"v_add3_u32 v8, v156, v164, s79\n"
"s_lshl_b32 s79, s3, 2\n"
"s_add_u32 s79, s79, 0x26840\n"
"v_add_u32 v79, s79, v161\n"
"v_mov_b32 v78, 0x40000\n"
"s_xor_b32 s79, s3, 2\n"
"s_lshl_b32 s79, s79, 2\n"
"v_add_u32 v77, s79, v167\n"
"v_mov_b32 v76, 1\n"
"v_cmp_gt_i32 vcc, 4, v168\n"
"s_nop 1\n"
"v_cndmask_b32 v76, 0, v76, vcc\n"
"v_cmp_eq_i32 vcc, v77, v76\n"
"s_nop 1\n"
"v_cndmask_b32 v78, v78, v12, vcc\n"
"v_cmp_eq_i32 vcc, v167, v159\n"
"s_nop 1\n"
"v_cndmask_b32 v9, v78, v79, vcc\n"
"v_add_u32 v11, 0x26840, v161\n"
"s_lshl_b32 s79, s3, 2\n"
"s_add_u32 s79, s79, 0x26840\n"
"v_add_u32 v79, s79, v161\n"
"v_mov_b32 v78, 0x40000\n"
"v_cmp_eq_i32 vcc, 0, v167\n"
"s_nop 1\n"
"v_cndmask_b32 v10, v78, v79, vcc\n"
"s_lshl_b32 s79, s3, 2\n"
"s_add_u32 s79, s79, 0x26400\n"
"v_mov_b32 v79, s79\n"
"v_mov_b32 v78, 0x40000\n"
"v_cmp_eq_i32 vcc, 0, v0\n"
"s_nop 1\n"
"v_cndmask_b32 v13, v78, v79, vcc\n"
"v_lshlrev_b32 v79, 2, v169\n"
"v_add_u32 v14, 0x26400, v79\n"
"v_add_u32 v25, v24, v164\n"
"s_lshl_b32 s79, s3, 4\n"
"v_lshl_add_u32 v79, v167, 2, s79\n"
"v_add_u32 v79, v79, v159\n"
"v_mul_i32_i24 v79, 0xf8, v79\n"
"v_lshl_add_u32 v79, v166, 3, v79\n"
"v_add_u32 v26, 0x50a0, v79\n"
"v_mov_b32 v27, 0x3727c5ac\n"
"v_xor_b32 v18, 4, v17\n"
"v_xor_b32 v79, 1, v166\n"
"v_lshl_add_u32 v91, v79, 2, v92\n"
"s_mul_i32 s79, s43, 0x1400\n"
"v_lshrrev_b32 v79, 2, v0\n"
"v_lshlrev_b32 v78, 4, v169\n"
"v_lshl_add_u32 v79, v79, 10, v78\n"
"v_add_u32 v79, s79, v79\n"
"v_mov_b32 v78, 0x40000\n"
"v_cmp_gt_i32 vcc, 20, v0\n"
"s_nop 1\n"
"v_cndmask_b32 v93, v78, v79, vcc\n"
"v_lshlrev_b32 v79, 3, v168\n"
"v_add_u32 v94, 0x9d80, v79\n"
"v_lshlrev_b32 v79, 3, v0\n"
"v_add_u32 v16, 0x9d80, v79\n"
"s_mul_i32 s79, s43, 0x1400\n"
"v_add_u32 v88, s79, v151\n"
"s_lshl_b32 s79, s43, 10\n"
"v_add_u32 v3, s79, v151\n"
"s_lshl_b32 s79, s43, 10\n"
"s_add_u32 s79, s79, 0xfffff400\n"
"s_lshl_b32 s78, s43, 10\n"
"s_add_u32 s78, s78, 0x4400\n"
"s_cmp_ge_i32 s43, 3\n"
"s_cselect_b32 s79, s79, s78\n"
"v_add_u32 v4, s79, v151\n"
"s_lshl_b32 s79, s43, 10\n"
"s_add_u32 s79, s79, 0xfffff800\n"
"s_lshl_b32 s78, s43, 10\n"
"s_add_u32 s78, s78, 0x4800\n"
"s_cmp_ge_i32 s43, 2\n"
"s_cselect_b32 s79, s79, s78\n"
"v_add_u32 v5, s79, v151\n"
"s_lshl_b32 s79, s43, 10\n"
"s_add_u32 s79, s79, 0xfffffc00\n"
"s_lshl_b32 s78, s43, 10\n"
"s_add_u32 s78, s78, 0x4c00\n"
"s_cmp_ge_i32 s43, 1\n"
"s_cselect_b32 s79, s79, s78\n"
"v_add_u32 v6, s79, v151\n"
"s_waitcnt lgkmcnt(0)\n"
"s_mul_i32 s16, s16, 0x2c9277b5\n"
"s_add_u32 s16, s16, 0xac564b05\n"
"v_mov_b32 v82, s16\n"
"s_waitcnt vmcnt(0)\n"
"s_mov_b64 exec, s[58:59]\n"
"buffer_store_dword v82, v80, s[4:7], s32 offen sc1\n"
"s_mov_b64 exec, -1\n"
"s_setprio 1\n"
"s_add_u32 m0, s61, 0\n"
"s_nop 0\n"
"buffer_load_dwordx4 v2, s[8:11], s60 offen offset:0 lds\n"
"s_add_u32 m0, s61, 0x3c0\n"
"s_nop 0\n"
"buffer_load_dwordx4 v2, s[8:11], s60 offen offset:64 lds\n"
"s_add_u32 m0, s61, 0x780\n"
"s_nop 0\n"
"buffer_load_dwordx4 v2, s[8:11], s60 offen offset:128 lds\n"
"s_add_u32 m0, s61, 0xb40\n"
"s_nop 0\n"
"buffer_load_dwordx4 v2, s[8:11], s60 offen offset:192 lds\n"
"s_add_u32 m0, s61, 0xf00\n"
"s_nop 0\n"
"buffer_load_dwordx4 v2, s[8:11], s60 offen offset:256 lds\n"
"s_add_u32 m0, s63, 0\n"
"s_nop 0\n"
"buffer_load_dwordx4 v2, s[8:11], s62 offen offset:0 lds\n"
"s_add_u32 m0, s63, 0x3c0\n"
"s_nop 0\n"
"buffer_load_dwordx4 v2, s[8:11], s62 offen offset:64 lds\n"
"s_add_u32 m0, s63, 0x780\n"
"s_nop 0\n"
"buffer_load_dwordx4 v2, s[8:11], s62 offen offset:128 lds\n"
"s_add_u32 m0, s63, 0xb40\n"
"s_nop 0\n"
"buffer_load_dwordx4 v2, s[8:11], s62 offen offset:192 lds\n"
"s_add_u32 m0, s63, 0xf00\n"
"s_nop 0\n"
"buffer_load_dwordx4 v2, s[8:11], s62 offen offset:256 lds\n"
"s_add_u32 m0, s65, 0\n"
"s_nop 0\n"
"buffer_load_dwordx4 v2, s[8:11], s64 offen offset:0 lds\n"
"s_add_u32 m0, s65, 0x3c0\n"
"s_nop 0\n"
"buffer_load_dwordx4 v2, s[8:11], s64 offen offset:64 lds\n"
"s_add_u32 m0, s65, 0x780\n"
"s_nop 0\n"
"buffer_load_dwordx4 v2, s[8:11], s64 offen offset:128 lds\n"
"s_add_u32 m0, s65, 0xb40\n"
"s_nop 0\n"
"buffer_load_dwordx4 v2, s[8:11], s64 offen offset:192 lds\n"
"s_add_u32 m0, s65, 0xf00\n"
"s_nop 0\n"
"buffer_load_dwordx4 v2, s[8:11], s64 offen offset:256 lds\n"
"s_add_u32 m0, s67, 0\n"
"s_nop 0\n"
"buffer_load_dwordx4 v2, s[8:11], s66 offen offset:0 lds\n"
"s_add_u32 m0, s67, 0x3c0\n"
"s_nop 0\n"
"buffer_load_dwordx4 v2, s[8:11], s66 offen offset:64 lds\n"
"s_add_u32 m0, s67, 0x780\n"
"s_nop 0\n"
"buffer_load_dwordx4 v2, s[8:11], s66 offen offset:128 lds\n"
"s_add_u32 m0, s67, 0xb40\n"
"s_nop 0\n"
"buffer_load_dwordx4 v2, s[8:11], s66 offen offset:192 lds\n"
"s_add_u32 m0, s67, 0xf00\n"
"s_nop 0\n"
"buffer_load_dwordx4 v2, s[8:11], s66 offen offset:256 lds\n"
"s_add_u32 m0, s69, 0\n"
"s_nop 0\n"
"buffer_load_dwordx4 v2, s[8:11], s68 offen offset:0 lds\n"
"s_add_u32 m0, s69, 0x3c0\n"
"s_nop 0\n"
"buffer_load_dwordx4 v2, s[8:11], s68 offen offset:64 lds\n"
"s_add_u32 m0, s69, 0x780\n"
"s_nop 0\n"
"buffer_load_dwordx4 v2, s[8:11], s68 offen offset:128 lds\n"
"s_add_u32 m0, s69, 0xb40\n"
"s_nop 0\n"
"buffer_load_dwordx4 v2, s[8:11], s68 offen offset:192 lds\n"
"s_add_u32 m0, s69, 0xf00\n"
"s_nop 0\n"
"buffer_load_dwordx4 v2, s[8:11], s68 offen offset:256 lds\n"
"buffer_load_dwordx4 v[96:99], v19, s[4:7], s50 offen sc1\n"
"s_waitcnt vmcnt(1)\n"
"ds_write_b128 v93, v[20:23] offset:40960\n"
"ds_write_b32 v13, v13 offset:0\n"
"v_add_u32 v88, 0xa000, v88\n"
"ds_read_b128 v[68:71], v88 offset:0\n"
"ds_read_b128 v[72:75], v88 offset:1024\n"
"ds_read_b128 v[76:79], v88 offset:2048\n"
"ds_read_b128 v[80:83], v88 offset:3072\n"
"s_waitcnt vmcnt(0)\n"
"v_xor_b32 v96, s16, v96\n"
"v_xor_b32 v97, s16, v97\n"
"v_xor_b32 v98, s16, v98\n"
"v_xor_b32 v99, s16, v99\n"
"v_or_b32 v96, v96, v97\n"
"v_or3_b32 v96, v96, v98, v99\n"
"v_cmp_ne_u32 vcc, 0, v96\n"
"s_cbranch_vccnz .Lwt_11\n"
"buffer_load_dwordx4 v[96:99], v1, s[4:7], s52 offen offset:0 sc1\n"
"s_waitcnt lgkmcnt(3)\n"
"v_mfma_f32_16x16x32_bf16 v[28:31], v[68:71], v[68:71], 0\n"
"ds_read_b128 v[84:87], v88 offset:4096\n"
"v_mfma_f32_16x16x32_bf16 v[32:35], v[20:23], v[68:71], 0\n"
"s_waitcnt lgkmcnt(3)\n"
"v_mfma_f32_16x16x32_bf16 v[28:31], v[72:75], v[72:75], v[28:31]\n"
"buffer_load_dwordx4 v[100:103], v1, s[4:7], s52 offen offset:1024 sc1\n"
"ds_read_b128 v[68:71], v88 offset:20480\n"
"v_mfma_f32_16x16x32_bf16 v[32:35], v[20:23], v[72:75], v[32:35]\n"
"s_waitcnt lgkmcnt(3)\n"
"v_mfma_f32_16x16x32_bf16 v[28:31], v[76:79], v[76:79], v[28:31]\n"
"ds_read_b128 v[72:75], v88 offset:21504\n"
"v_mfma_f32_16x16x32_bf16 v[32:35], v[20:23], v[76:79], v[32:35]\n"
"buffer_load_dwordx4 v[104:107], v1, s[4:7], s52 offen offset:2048 sc1\n"
"s_waitcnt lgkmcnt(3)\n"
"v_mfma_f32_16x16x32_bf16 v[28:31], v[80:83], v[80:83], v[28:31]\n"
"ds_read_b128 v[76:79], v88 offset:22528\n"
"v_mfma_f32_16x16x32_bf16 v[32:35], v[20:23], v[80:83], v[32:35]\n"
"s_waitcnt lgkmcnt(3)\n"
"v_mfma_f32_16x16x32_bf16 v[28:31], v[84:87], v[84:87], v[28:31]\n"
"ds_read_b128 v[80:83], v88 offset:23552\n"
"buffer_load_dwordx4 v[108:111], v1, s[4:7], s52 offen offset:3072 sc1\n"
"v_mfma_f32_16x16x32_bf16 v[32:35], v[20:23], v[84:87], v[32:35]\n"
"s_waitcnt lgkmcnt(3)\n"
"v_mfma_f32_16x16x32_bf16 v[36:39], v[68:71], v[68:71], 0\n"
"ds_read_b128 v[84:87], v88 offset:24576\n"
"v_mfma_f32_16x16x32_bf16 v[40:43], v[20:23], v[68:71], 0\n"
"s_waitcnt lgkmcnt(3)\n"
"v_mfma_f32_16x16x32_bf16 v[36:39], v[72:75], v[72:75], v[36:39]\n"
"s_add_u32 s52, s52, 4096\n"
"buffer_load_dwordx4 v[112:115], v1, s[4:7], s52 offen offset:0 sc1\n"
"v_add_u32 v88, 0x12400, v88\n"
"ds_read_b128 v[68:71], v88 offset:0\n"
"v_mfma_f32_16x16x32_bf16 v[40:43], v[20:23], v[72:75], v[40:43]\n"
"ds_write_b128 v92, v[28:31] offset:0\n"
"buffer_load_dwordx4 v[116:119], v1, s[4:7], s52 offen offset:1024 sc1\n"
"s_waitcnt lgkmcnt(4)\n"
"v_mfma_f32_16x16x32_bf16 v[36:39], v[76:79], v[76:79], v[36:39]\n"
"ds_read_b128 v[72:75], v88 offset:1024\n"
"v_mfma_f32_16x16x32_bf16 v[40:43], v[20:23], v[76:79], v[40:43]\n"
"ds_write_b32 v91, v32 offset:0\n"
"s_waitcnt lgkmcnt(5)\n"
"v_mfma_f32_16x16x32_bf16 v[36:39], v[80:83], v[80:83], v[36:39]\n"
"buffer_load_dwordx4 v[120:123], v1, s[4:7], s52 offen offset:2048 sc1\n"
"ds_read_b128 v[76:79], v88 offset:2048\n"
"v_mfma_f32_16x16x32_bf16 v[40:43], v[20:23], v[80:83], v[40:43]\n"
"s_waitcnt lgkmcnt(5)\n"
"v_mfma_f32_16x16x32_bf16 v[36:39], v[84:87], v[84:87], v[36:39]\n"
"ds_read_b128 v[80:83], v88 offset:3072\n"
"v_mfma_f32_16x16x32_bf16 v[40:43], v[20:23], v[84:87], v[40:43]\n"
"buffer_load_dwordx4 v[124:127], v1, s[4:7], s52 offen offset:3072 sc1\n"
"s_waitcnt lgkmcnt(5)\n"
"v_mfma_f32_16x16x32_bf16 v[44:47], v[68:71], v[68:71], 0\n"
"ds_read_b128 v[84:87], v88 offset:4096\n"
"v_mfma_f32_16x16x32_bf16 v[48:51], v[20:23], v[68:71], 0\n"
"s_waitcnt lgkmcnt(4)\n"
"v_mfma_f32_16x16x32_bf16 v[44:47], v[72:75], v[72:75], v[44:47]\n"
"ds_read_b128 v[68:71], v88 offset:20480\n"
"s_add_u32 s52, s52, 4096\n"
"buffer_load_dwordx4 v[128:131], v1, s[4:7], s52 offen offset:0 sc1\n"
"v_mfma_f32_16x16x32_bf16 v[48:51], v[20:23], v[72:75], v[48:51]\n"
"ds_write_b128 v92, v[36:39] offset:256\n"
"s_waitcnt lgkmcnt(4)\n"
"v_mfma_f32_16x16x32_bf16 v[44:47], v[76:79], v[76:79], v[44:47]\n"
"ds_read_b128 v[72:75], v88 offset:21504\n"
"buffer_load_dwordx4 v[132:135], v1, s[4:7], s52 offen offset:1024 sc1\n"
"v_mfma_f32_16x16x32_bf16 v[48:51], v[20:23], v[76:79], v[48:51]\n"
"ds_write_b32 v91, v40 offset:256\n"
"s_waitcnt lgkmcnt(5)\n"
"v_mfma_f32_16x16x32_bf16 v[44:47], v[80:83], v[80:83], v[44:47]\n"
"ds_read_b128 v[76:79], v88 offset:22528\n"
"v_mfma_f32_16x16x32_bf16 v[48:51], v[20:23], v[80:83], v[48:51]\n"
"buffer_load_dwordx4 v[136:139], v1, s[4:7], s52 offen offset:2048 sc1\n"
"s_waitcnt lgkmcnt(5)\n"
"v_mfma_f32_16x16x32_bf16 v[44:47], v[84:87], v[84:87], v[44:47]\n"
"ds_read_b128 v[80:83], v88 offset:23552\n"
"v_mfma_f32_16x16x32_bf16 v[48:51], v[20:23], v[84:87], v[48:51]\n"
"s_waitcnt lgkmcnt(5)\n"
"v_mfma_f32_16x16x32_bf16 v[52:55], v[68:71], v[68:71], 0\n"
"ds_read_b128 v[84:87], v88 offset:24576\n"
"buffer_load_dwordx4 v[140:143], v1, s[4:7], s52 offen offset:3072 sc1\n"
"v_mfma_f32_16x16x32_bf16 v[56:59], v[20:23], v[68:71], 0\n"
"s_waitcnt lgkmcnt(4)\n"
"v_mfma_f32_16x16x32_bf16 v[52:55], v[72:75], v[72:75], v[52:55]\n"
"v_add_u32 v88, 0xfffe3c00, v88\n"
"ds_read_b128 v[68:71], v88 offset:0\n"
"v_mfma_f32_16x16x32_bf16 v[56:59], v[20:23], v[72:75], v[56:59]\n"
"s_add_u32 s52, s52, 4096\n"
"buffer_load_dwordx4 v[144:147], v1, s[4:7], s52 offen offset:0 sc1\n"
"ds_write_b128 v92, v[44:47] offset:512\n"
"s_waitcnt lgkmcnt(4)\n"
"v_mfma_f32_16x16x32_bf16 v[52:55], v[76:79], v[76:79], v[52:55]\n"
"ds_read_b128 v[72:75], v88 offset:1024\n"
"v_mfma_f32_16x16x32_bf16 v[56:59], v[20:23], v[76:79], v[56:59]\n"
"buffer_load_dwordx4 v[148:151], v1, s[4:7], s52 offen offset:1024 sc1\n"
"ds_write_b32 v91, v48 offset:512\n"
"ds_read_b32 v90, v14 offset:0\n"
"s_waitcnt lgkmcnt(6)\n"
"v_mfma_f32_16x16x32_bf16 v[52:55], v[80:83], v[80:83], v[52:55]\n"
"ds_read_b128 v[76:79], v88 offset:2048\n"
"v_mfma_f32_16x16x32_bf16 v[56:59], v[20:23], v[80:83], v[56:59]\n"
"buffer_load_dwordx4 v[152:155], v1, s[4:7], s52 offen offset:2048 sc1\n"
"s_waitcnt lgkmcnt(6)\n"
"v_mfma_f32_16x16x32_bf16 v[52:55], v[84:87], v[84:87], v[52:55]\n"
"ds_read_b128 v[80:83], v88 offset:3072\n"
"v_mfma_f32_16x16x32_bf16 v[56:59], v[20:23], v[84:87], v[56:59]\n"
"s_waitcnt lgkmcnt(6)\n"
"v_mfma_f32_16x16x32_bf16 v[60:63], v[68:71], v[68:71], 0\n"
"ds_read_b128 v[84:87], v88 offset:4096\n"
"buffer_load_dwordx4 v[156:159], v1, s[4:7], s52 offen offset:3072 sc1\n"
"v_mfma_f32_16x16x32_bf16 v[64:67], v[20:23], v[68:71], 0\n"
"s_waitcnt lgkmcnt(3)\n"
"v_cmp_eq_u32 vcc, 0, v90\n"
"v_mfma_f32_16x16x32_bf16 v[60:63], v[72:75], v[72:75], v[60:63]\n"
"v_mfma_f32_16x16x32_bf16 v[64:67], v[20:23], v[72:75], v[64:67]\n"
"ds_write_b128 v92, v[52:55] offset:768\n"
"s_cbranch_vccnz .Lsl_7\n"
".Lbk_8:\n"
"s_waitcnt lgkmcnt(3)\n"
"v_mfma_f32_16x16x32_bf16 v[60:63], v[76:79], v[76:79], v[60:63]\n"
"v_add_u32 v3, 0x5000, v3\n"
"v_mfma_f32_16x16x32_bf16 v[64:67], v[20:23], v[76:79], v[64:67]\n"
"ds_read_b128 v[68:71], v3 offset:20480\n"
"ds_write_b32 v91, v56 offset:768\n"
"ds_read_b128 v[72:75], v3 offset:40960\n"
"s_waitcnt lgkmcnt(5)\n"
"v_mfma_f32_16x16x32_bf16 v[60:63], v[80:83], v[80:83], v[60:63]\n"
"v_add_u32 v95, 0x17400, v3\n"
"v_mfma_f32_16x16x32_bf16 v[64:67], v[20:23], v[80:83], v[64:67]\n"
"ds_read_b128 v[76:79], v95 offset:0\n"
"s_waitcnt lgkmcnt(5)\n"
"v_mfma_f32_16x16x32_bf16 v[60:63], v[84:87], v[84:87], v[60:63]\n"
"ds_read_b128 v[80:83], v95 offset:20480\n"
"v_mfma_f32_16x16x32_bf16 v[64:67], v[20:23], v[84:87], v[64:67]\n"
"s_nop 4\n"
"ds_write_b128 v92, v[60:63] offset:1024\n"
"s_nop 1\n"
"ds_write_b32 v91, v64 offset:1024\n"
"s_branch .Ljn_12\n"
".Lwt_11:\n"
"s_waitcnt lgkmcnt(0)\n"
"v_mfma_f32_16x16x32_bf16 v[28:31], v[68:71], v[68:71], 0\n"
"ds_read_b128 v[84:87], v88 offset:4096\n"
"v_mfma_f32_16x16x32_bf16 v[32:35], v[20:23], v[68:71], 0\n"
"v_mfma_f32_16x16x32_bf16 v[28:31], v[72:75], v[72:75], v[28:31]\n"
"ds_read_b128 v[68:71], v88 offset:20480\n"
"v_mfma_f32_16x16x32_bf16 v[32:35], v[20:23], v[72:75], v[32:35]\n"
"v_mfma_f32_16x16x32_bf16 v[28:31], v[76:79], v[76:79], v[28:31]\n"
"ds_read_b128 v[72:75], v88 offset:21504\n"
"v_mfma_f32_16x16x32_bf16 v[32:35], v[20:23], v[76:79], v[32:35]\n"
"v_mfma_f32_16x16x32_bf16 v[28:31], v[80:83], v[80:83], v[28:31]\n"
"ds_read_b128 v[76:79], v88 offset:22528\n"
"v_mfma_f32_16x16x32_bf16 v[32:35], v[20:23], v[80:83], v[32:35]\n"
"s_waitcnt lgkmcnt(3)\n"
"v_mfma_f32_16x16x32_bf16 v[28:31], v[84:87], v[84:87], v[28:31]\n"
"ds_read_b128 v[80:83], v88 offset:23552\n"
"v_mfma_f32_16x16x32_bf16 v[32:35], v[20:23], v[84:87], v[32:35]\n"
"s_waitcnt lgkmcnt(3)\n"
"v_mfma_f32_16x16x32_bf16 v[36:39], v[68:71], v[68:71], 0\n"
"ds_read_b128 v[84:87], v88 offset:24576\n"
"v_mfma_f32_16x16x32_bf16 v[40:43], v[20:23], v[68:71], 0\n"
"s_waitcnt lgkmcnt(3)\n"
"v_mfma_f32_16x16x32_bf16 v[36:39], v[72:75], v[72:75], v[36:39]\n"
"v_add_u32 v88, 0x12400, v88\n"
"ds_read_b128 v[68:71], v88 offset:0\n"
"v_mfma_f32_16x16x32_bf16 v[40:43], v[20:23], v[72:75], v[40:43]\n"
"ds_write_b128 v92, v[28:31] offset:0\n"
"s_waitcnt lgkmcnt(4)\n"
"v_mfma_f32_16x16x32_bf16 v[36:39], v[76:79], v[76:79], v[36:39]\n"
"ds_read_b128 v[72:75], v88 offset:1024\n"
"v_mfma_f32_16x16x32_bf16 v[40:43], v[20:23], v[76:79], v[40:43]\n"
"ds_write_b32 v91, v32 offset:0\n"
"s_waitcnt lgkmcnt(5)\n"
"v_mfma_f32_16x16x32_bf16 v[36:39], v[80:83], v[80:83], v[36:39]\n"
"ds_read_b128 v[76:79], v88 offset:2048\n"
"v_mfma_f32_16x16x32_bf16 v[40:43], v[20:23], v[80:83], v[40:43]\n"
"s_waitcnt lgkmcnt(5)\n"
"v_mfma_f32_16x16x32_bf16 v[36:39], v[84:87], v[84:87], v[36:39]\n"
"ds_read_b128 v[80:83], v88 offset:3072\n"
"v_mfma_f32_16x16x32_bf16 v[40:43], v[20:23], v[84:87], v[40:43]\n"
"s_waitcnt lgkmcnt(5)\n"
"v_mfma_f32_16x16x32_bf16 v[44:47], v[68:71], v[68:71], 0\n"
"ds_read_b128 v[84:87], v88 offset:4096\n"
"v_mfma_f32_16x16x32_bf16 v[48:51], v[20:23], v[68:71], 0\n"
"s_waitcnt lgkmcnt(4)\n"
"v_mfma_f32_16x16x32_bf16 v[44:47], v[72:75], v[72:75], v[44:47]\n"
"ds_read_b128 v[68:71], v88 offset:20480\n"
"v_mfma_f32_16x16x32_bf16 v[48:51], v[20:23], v[72:75], v[48:51]\n"
"ds_write_b128 v92, v[36:39] offset:256\n"
"s_waitcnt lgkmcnt(4)\n"
"v_mfma_f32_16x16x32_bf16 v[44:47], v[76:79], v[76:79], v[44:47]\n"
"ds_read_b128 v[72:75], v88 offset:21504\n"
"v_mfma_f32_16x16x32_bf16 v[48:51], v[20:23], v[76:79], v[48:51]\n"
"ds_write_b32 v91, v40 offset:256\n"
"s_waitcnt lgkmcnt(5)\n"
"v_mfma_f32_16x16x32_bf16 v[44:47], v[80:83], v[80:83], v[44:47]\n"
"ds_read_b128 v[76:79], v88 offset:22528\n"
"v_mfma_f32_16x16x32_bf16 v[48:51], v[20:23], v[80:83], v[48:51]\n"
"s_waitcnt lgkmcnt(5)\n"
"v_mfma_f32_16x16x32_bf16 v[44:47], v[84:87], v[84:87], v[44:47]\n"
"ds_read_b128 v[80:83], v88 offset:23552\n"
"v_mfma_f32_16x16x32_bf16 v[48:51], v[20:23], v[84:87], v[48:51]\n"
"s_waitcnt lgkmcnt(5)\n"
"v_mfma_f32_16x16x32_bf16 v[52:55], v[68:71], v[68:71], 0\n"
"ds_read_b128 v[84:87], v88 offset:24576\n"
"v_mfma_f32_16x16x32_bf16 v[56:59], v[20:23], v[68:71], 0\n"
"s_waitcnt lgkmcnt(4)\n"
"v_mfma_f32_16x16x32_bf16 v[52:55], v[72:75], v[72:75], v[52:55]\n"
"v_add_u32 v88, 0xfffe3c00, v88\n"
"ds_read_b128 v[68:71], v88 offset:0\n"
"v_mfma_f32_16x16x32_bf16 v[56:59], v[20:23], v[72:75], v[56:59]\n"
"ds_write_b128 v92, v[44:47] offset:512\n"
"s_waitcnt lgkmcnt(4)\n"
"v_mfma_f32_16x16x32_bf16 v[52:55], v[76:79], v[76:79], v[52:55]\n"
"ds_read_b128 v[72:75], v88 offset:1024\n"
"v_mfma_f32_16x16x32_bf16 v[56:59], v[20:23], v[76:79], v[56:59]\n"
"ds_write_b32 v91, v48 offset:512\n"
"ds_read_b32 v90, v14 offset:0\n"
"s_waitcnt lgkmcnt(6)\n"
"v_mfma_f32_16x16x32_bf16 v[52:55], v[80:83], v[80:83], v[52:55]\n"
"ds_read_b128 v[76:79], v88 offset:2048\n"
"v_mfma_f32_16x16x32_bf16 v[56:59], v[20:23], v[80:83], v[56:59]\n"
"s_waitcnt lgkmcnt(6)\n"
"v_mfma_f32_16x16x32_bf16 v[52:55], v[84:87], v[84:87], v[52:55]\n"
"ds_read_b128 v[80:83], v88 offset:3072\n"
"v_mfma_f32_16x16x32_bf16 v[56:59], v[20:23], v[84:87], v[56:59]\n"
"s_waitcnt lgkmcnt(6)\n"
"v_mfma_f32_16x16x32_bf16 v[60:63], v[68:71], v[68:71], 0\n"
"ds_read_b128 v[84:87], v88 offset:4096\n"
"v_mfma_f32_16x16x32_bf16 v[64:67], v[20:23], v[68:71], 0\n"
"s_waitcnt lgkmcnt(3)\n"
"v_cmp_eq_u32 vcc, 0, v90\n"
"v_mfma_f32_16x16x32_bf16 v[60:63], v[72:75], v[72:75], v[60:63]\n"
"v_mfma_f32_16x16x32_bf16 v[64:67], v[20:23], v[72:75], v[64:67]\n"
"ds_write_b128 v92, v[52:55] offset:768\n"
"s_waitcnt lgkmcnt(3)\n"
"v_mfma_f32_16x16x32_bf16 v[60:63], v[76:79], v[76:79], v[60:63]\n"
"v_mfma_f32_16x16x32_bf16 v[64:67], v[20:23], v[76:79], v[64:67]\n"
"ds_write_b32 v91, v56 offset:768\n"
"s_waitcnt lgkmcnt(3)\n"
"v_mfma_f32_16x16x32_bf16 v[60:63], v[80:83], v[80:83], v[60:63]\n"
"v_mfma_f32_16x16x32_bf16 v[64:67], v[20:23], v[80:83], v[64:67]\n"
"s_waitcnt lgkmcnt(2)\n"
"v_mfma_f32_16x16x32_bf16 v[60:63], v[84:87], v[84:87], v[60:63]\n"
"v_mfma_f32_16x16x32_bf16 v[64:67], v[20:23], v[84:87], v[64:67]\n"
"buffer_load_dwordx4 v[96:99], v19, s[4:7], s50 offen sc1\n"
"s_mov_b32 s17, 0x100000\n"
".Ltp_13:\n"
"ds_read_b32 v90, v14 offset:0\n"
"s_sub_u32 s17, s17, 1\n"
"s_cbranch_scc1 .Lou_14\n"
"s_waitcnt lgkmcnt(0)\n"
"v_cmp_eq_u32 vcc, 0, v90\n"
"s_cbranch_vccnz .Ltp_13\n"
".Lou_14:\n"
"s_waitcnt lgkmcnt(0)\n"
"v_add_u32 v3, 0x5000, v3\n"
"ds_read_b128 v[68:71], v3 offset:20480\n"
"ds_read_b128 v[72:75], v3 offset:40960\n"
"v_add_u32 v95, 0x17400, v3\n"
"ds_read_b128 v[76:79], v95 offset:0\n"
"ds_read_b128 v[80:83], v95 offset:20480\n"
"ds_write_b128 v92, v[60:63] offset:1024\n"
"ds_write_b32 v91, v64 offset:1024\n"
"s_waitcnt vmcnt(0) lgkmcnt(2)\n"
"s_mov_b32 s17, 32768\n"
"s_branch .Lmd_17\n"
".Ltp_15:\n"
"buffer_load_dwordx4 v[96:99], v19, s[4:7], s50 offen sc1\n"
".Lmd_17:\n"
"s_sub_u32 s17, s17, 1\n"
"s_cbranch_scc1 .Lou_16\n"
"s_waitcnt vmcnt(0)\n"
"v_xor_b32 v96, s16, v96\n"
"v_xor_b32 v97, s16, v97\n"
"v_xor_b32 v98, s16, v98\n"
"v_xor_b32 v99, s16, v99\n"
"v_or_b32 v96, v96, v97\n"
"v_or3_b32 v96, v96, v98, v99\n"
"v_cmp_ne_u32 vcc, 0, v96\n"
"s_cbranch_vccz .Lou_16\n"
"s_sleep 1\n"
"s_branch .Ltp_15\n"
".Lou_16:\n"
"s_waitcnt vmcnt(0)\n"
"buffer_load_dwordx4 v[96:99], v1, s[4:7], s52 offen offset:0 sc1\n"
"buffer_load_dwordx4 v[100:103], v1, s[4:7], s52 offen offset:1024 sc1\n"
"buffer_load_dwordx4 v[104:107], v1, s[4:7], s52 offen offset:2048 sc1\n"
"buffer_load_dwordx4 v[108:111], v1, s[4:7], s52 offen offset:3072 sc1\n"
"s_add_u32 s52, s52, 4096\n"
"buffer_load_dwordx4 v[112:115], v1, s[4:7], s52 offen offset:0 sc1\n"
"buffer_load_dwordx4 v[116:119], v1, s[4:7], s52 offen offset:1024 sc1\n"
"buffer_load_dwordx4 v[120:123], v1, s[4:7], s52 offen offset:2048 sc1\n"
"buffer_load_dwordx4 v[124:127], v1, s[4:7], s52 offen offset:3072 sc1\n"
"s_add_u32 s52, s52, 4096\n"
"buffer_load_dwordx4 v[128:131], v1, s[4:7], s52 offen offset:0 sc1\n"
"buffer_load_dwordx4 v[132:135], v1, s[4:7], s52 offen offset:1024 sc1\n"
"buffer_load_dwordx4 v[136:139], v1, s[4:7], s52 offen offset:2048 sc1\n"
"buffer_load_dwordx4 v[140:143], v1, s[4:7], s52 offen offset:3072 sc1\n"
"s_add_u32 s52, s52, 4096\n"
"buffer_load_dwordx4 v[144:147], v1, s[4:7], s52 offen offset:0 sc1\n"
"buffer_load_dwordx4 v[148:151], v1, s[4:7], s52 offen offset:1024 sc1\n"
"buffer_load_dwordx4 v[152:155], v1, s[4:7], s52 offen offset:2048 sc1\n"
"buffer_load_dwordx4 v[156:159], v1, s[4:7], s52 offen offset:3072 sc1\n"
".Ljn_12:\n"
"s_waitcnt lgkmcnt(2)\n"
"ds_write_b32 v13, v13 offset:16\n"
"s_waitcnt vmcnt(14)\n"
"v_mfma_f32_16x16x32_bf16 v[28:31], v[96:99], v[68:71], 0\n"
"ds_read_b128 v[84:87], v3 offset:21504\n"
"s_add_u32 s52, s52, 4096\n"
"v_mfma_f32_16x16x32_bf16 v[32:35], v[100:103], v[68:71], 0\n"
"v_mfma_f32_16x16x32_bf16 v[36:39], v[96:99], v[72:75], 0\n"
"ds_read_b128 v[68:71], v3 offset:41984\n"
"buffer_load_dwordx4 v[160:163], v1, s[4:7], s52 offen offset:0 sc1\n"
"v_mfma_f32_16x16x32_bf16 v[40:43], v[100:103], v[72:75], 0\n"
"v_mfma_f32_16x16x32_bf16 v[44:47], v[96:99], v[76:79], 0\n"
"ds_read_b128 v[72:75], v95 offset:1024\n"
"v_mfma_f32_16x16x32_bf16 v[48:51], v[100:103], v[76:79], 0\n"
"buffer_load_dwordx4 v[164:167], v1, s[4:7], s52 offen offset:1024 sc1\n"
"v_mfma_f32_16x16x32_bf16 v[52:55], v[96:99], v[80:83], 0\n"
"ds_read_b128 v[76:79], v95 offset:21504\n"
"v_mfma_f32_16x16x32_bf16 v[56:59], v[100:103], v[80:83], 0\n"
"s_waitcnt vmcnt(14) lgkmcnt(2)\n"
"v_mfma_f32_16x16x32_bf16 v[28:31], v[104:107], v[84:87], v[28:31]\n"
"ds_read_b128 v[80:83], v3 offset:22528\n"
"buffer_load_dwordx4 v[168:171], v1, s[4:7], s52 offen offset:2048 sc1\n"
"v_mfma_f32_16x16x32_bf16 v[32:35], v[108:111], v[84:87], v[32:35]\n"
"v_mfma_f32_16x16x32_bf16 v[36:39], v[104:107], v[68:71], v[36:39]\n"
"ds_read_b128 v[84:87], v3 offset:43008\n"
"v_mfma_f32_16x16x32_bf16 v[40:43], v[108:111], v[68:71], v[40:43]\n"
"buffer_load_dwordx4 v[172:175], v1, s[4:7], s52 offen offset:3072 sc1\n"
"v_mov_b32 v20, -1\n"
"s_waitcnt lgkmcnt(2)\n"
"v_mfma_f32_16x16x32_bf16 v[44:47], v[104:107], v[72:75], v[44:47]\n"
"ds_read_b128 v[68:71], v95 offset:2048\n"
"s_add_u32 s52, s52, 4096\n"
"v_mov_b32 v21, -1\n"
"v_mfma_f32_16x16x32_bf16 v[48:51], v[108:111], v[72:75], v[48:51]\n"
"v_mov_b32 v22, -1\n"
"v_mov_b32 v23, -1\n"
"v_cndmask_b32 v91, 2.0, v1, s[40:41]\n"
"v_mfma_f32_16x16x32_bf16 v[52:55], v[104:107], v[76:79], v[52:55]\n"
"ds_read_b128 v[72:75], v95 offset:22528\n"
"buffer_load_dwordx4 v[176:179], v1, s[4:7], s52 offen offset:0 sc1\n"
"s_mov_b32 s33, s30\n"
"v_mfma_f32_16x16x32_bf16 v[56:59], v[108:111], v[76:79], v[56:59]\n"
"buffer_store_dwordx4 v[20:23], v1, s[4:7], s33 offen offset:0 sc1\n"
"s_waitcnt vmcnt(16) lgkmcnt(2)\n"
"v_mfma_f32_16x16x32_bf16 v[28:31], v[112:115], v[80:83], v[28:31]\n"
"ds_read_b128 v[76:79], v3 offset:23552\n"
"v_mfma_f32_16x16x32_bf16 v[32:35], v[116:119], v[80:83], v[32:35]\n"
"buffer_load_dwordx4 v[180:183], v1, s[4:7], s52 offen offset:1024 sc1\n"
"v_mfma_f32_16x16x32_bf16 v[36:39], v[112:115], v[84:87], v[36:39]\n"
"ds_read_b128 v[80:83], v3 offset:44032\n"
"v_mfma_f32_16x16x32_bf16 v[40:43], v[116:119], v[84:87], v[40:43]\n"
"s_waitcnt lgkmcnt(2)\n"
"v_mfma_f32_16x16x32_bf16 v[44:47], v[112:115], v[68:71], v[44:47]\n"
"ds_read_b128 v[84:87], v95 offset:3072\n"
"buffer_load_dwordx4 v[184:187], v1, s[4:7], s52 offen offset:2048 sc1\n"
"buffer_store_dwordx4 v[20:23], v91, s[4:7], s33 offen offset:1024 sc1\n"
"v_mfma_f32_16x16x32_bf16 v[48:51], v[116:119], v[68:71], v[48:51]\n"
"s_add_u32 s33, s30, 0x140000\n"
"ds_read_b32 v90, v14 offset:16\n"
"v_mfma_f32_16x16x32_bf16 v[52:55], v[112:115], v[72:75], v[52:55]\n"
"ds_read_b128 v[68:71], v95 offset:23552\n"
"v_mfma_f32_16x16x32_bf16 v[56:59], v[116:119], v[72:75], v[56:59]\n"
"buffer_load_dwordx4 v[188:191], v1, s[4:7], s52 offen offset:3072 sc1\n"
"s_waitcnt vmcnt(18) lgkmcnt(3)\n"
"v_mfma_f32_16x16x32_bf16 v[28:31], v[120:123], v[76:79], v[28:31]\n"
"ds_read_b128 v[72:75], v3 offset:24576\n"
"s_add_u32 s52, s52, 4096\n"
"v_mfma_f32_16x16x32_bf16 v[32:35], v[124:127], v[76:79], v[32:35]\n"
"buffer_store_dwordx4 v[20:23], v1, s[4:7], s33 offen offset:0 sc1\n"
"v_mfma_f32_16x16x32_bf16 v[36:39], v[120:123], v[80:83], v[36:39]\n"
"ds_read_b128 v[76:79], v3 offset:45056\n"
"buffer_load_dwordx4 v[192:195], v1, s[4:7], s52 offen offset:0 sc1\n"
"v_mfma_f32_16x16x32_bf16 v[40:43], v[124:127], v[80:83], v[40:43]\n"
"s_waitcnt lgkmcnt(2)\n"
"v_mfma_f32_16x16x32_bf16 v[44:47], v[120:123], v[84:87], v[44:47]\n"
"ds_read_b128 v[80:83], v95 offset:4096\n"
"v_mfma_f32_16x16x32_bf16 v[48:51], v[124:127], v[84:87], v[48:51]\n"
"buffer_load_dwordx4 v[196:199], v1, s[4:7], s52 offen offset:1024 sc1\n"
"v_mfma_f32_16x16x32_bf16 v[52:55], v[120:123], v[68:71], v[52:55]\n"
"ds_read_b128 v[84:87], v95 offset:24576\n"
"buffer_store_dwordx4 v[20:23], v91, s[4:7], s33 offen offset:1024 sc1\n"
"v_mfma_f32_16x16x32_bf16 v[56:59], v[124:127], v[68:71], v[56:59]\n"
"s_lshl_b32 s79, s2, 2\n"
"s_add_u32 s79, s79, s3\n"
"v_cmp_eq_u32 vcc, 0, v90\n"
"s_waitcnt vmcnt(20) lgkmcnt(2)\n"
"v_mfma_f32_16x16x32_bf16 v[28:31], v[128:131], v[72:75], v[28:31]\n"
"ds_read_b128 v[68:71], v3 offset:25600\n"
"buffer_load_dwordx4 v[200:203], v1, s[4:7], s52 offen offset:2048 sc1\n"
"s_lshl_b32 s79, s79, 10\n"
"v_mfma_f32_16x16x32_bf16 v[32:35], v[132:135], v[72:75], v[32:35]\n"
"s_add_u32 s33, s79, 0xae8000\n"
"s_sub_u32 s79, 0xb47fff, s33\n"
"s_and_b32 s79, s79, 0x80000000\n"
"s_cbranch_vccnz .Lsl_26\n"
".Lbk_27:\n"
"v_mfma_f32_16x16x32_bf16 v[36:39], v[128:131], v[76:79], v[36:39]\n"
"ds_read_b128 v[72:75], v3 offset:46080\n"
"s_or_b32 s33, s33, s79\n"
"v_mfma_f32_16x16x32_bf16 v[40:43], v[132:135], v[76:79], v[40:43]\n"
"buffer_load_dwordx4 v[204:207], v1, s[4:7], s52 offen offset:3072 sc1\n"
"buffer_store_dwordx4 v[20:23], v1, s[4:7], s33 offen sc1\n"
"ds_read2st64_b32 v[60:61], v17 offset0:0 offset1:8\n"
"s_waitcnt lgkmcnt(3)\n"
"v_mfma_f32_16x16x32_bf16 v[44:47], v[128:131], v[80:83], v[44:47]\n"
"ds_read_b128 v[76:79], v95 offset:5120\n"
"s_add_u32 s52, s52, 4096\n"
"v_mfma_f32_16x16x32_bf16 v[48:51], v[132:135], v[80:83], v[48:51]\n"
"ds_read2st64_b32 v[62:63], v17 offset0:16 offset1:24\n"
"v_mfma_f32_16x16x32_bf16 v[52:55], v[128:131], v[84:87], v[52:55]\n"
"ds_read_b128 v[80:83], v95 offset:25600\n"
"buffer_load_dwordx4 v[208:211], v1, s[4:7], s52 offen offset:0 sc1\n"
"v_mfma_f32_16x16x32_bf16 v[56:59], v[132:135], v[84:87], v[56:59]\n"
"ds_read2st64_b32 v[64:65], v18 offset0:0 offset1:8\n"
"s_waitcnt vmcnt(22) lgkmcnt(5)\n"
"v_mfma_f32_16x16x32_bf16 v[28:31], v[136:139], v[68:71], v[28:31]\n"
"ds_read_b128 v[84:87], v3 offset:26624\n"
"v_mfma_f32_16x16x32_bf16 v[32:35], v[140:143], v[68:71], v[32:35]\n"
"buffer_load_dwordx4 v[212:215], v1, s[4:7], s52 offen offset:1024 sc1\n"
"ds_read2st64_b32 v[66:67], v18 offset0:16 offset1:24\n"
"v_mfma_f32_16x16x32_bf16 v[36:39], v[136:139], v[72:75], v[36:39]\n"
"ds_read_b128 v[68:71], v3 offset:47104\n"
"v_mfma_f32_16x16x32_bf16 v[40:43], v[140:143], v[72:75], v[40:43]\n"
"s_waitcnt lgkmcnt(4)\n"
"v_mfma_f32_16x16x32_bf16 v[44:47], v[136:139], v[76:79], v[44:47]\n"
"ds_read_b128 v[72:75], v95 offset:6144\n"
"buffer_load_dwordx4 v[216:219], v1, s[4:7], s52 offen offset:2048 sc1\n"
"v_mfma_f32_16x16x32_bf16 v[48:51], v[140:143], v[76:79], v[48:51]\n"
"v_mfma_f32_16x16x32_bf16 v[52:55], v[136:139], v[80:83], v[52:55]\n"
"ds_read_b128 v[76:79], v95 offset:26624\n"
"v_mfma_f32_16x16x32_bf16 v[56:59], v[140:143], v[80:83], v[56:59]\n"
"buffer_load_dwordx4 v[220:223], v1, s[4:7], s52 offen offset:3072 sc1\n"
"s_waitcnt vmcnt(23) lgkmcnt(2)\n"
"v_mfma_f32_16x16x32_bf16 v[28:31], v[144:147], v[84:87], v[28:31]\n"
"ds_read_b128 v[80:83], v3 offset:27648\n"
"s_add_u32 s52, s52, 4096\n"
"v_mfma_f32_16x16x32_bf16 v[32:35], v[148:151], v[84:87], v[32:35]\n"
"v_mfma_f32_16x16x32_bf16 v[36:39], v[144:147], v[68:71], v[36:39]\n"
"ds_read_b128 v[84:87], v3 offset:48128\n"
"buffer_load_dwordx4 v[224:227], v1, s[4:7], s52 offen offset:0 sc1\n"
"v_mfma_f32_16x16x32_bf16 v[40:43], v[148:151], v[68:71], v[40:43]\n"
"v_add_f32 v60, v60, v61\n"
"s_waitcnt lgkmcnt(2)\n"
"v_mfma_f32_16x16x32_bf16 v[44:47], v[144:147], v[72:75], v[44:47]\n"
"ds_read_b128 v[68:71], v95 offset:7168\n"
"v_add_f32 v62, v62, v63\n"
"v_mfma_f32_16x16x32_bf16 v[48:51], v[148:151], v[72:75], v[48:51]\n"
"buffer_load_dwordx4 v[228:231], v1, s[4:7], s52 offen offset:1024 sc1\n"
"v_add_f32 v64, v64, v65\n"
"v_mfma_f32_16x16x32_bf16 v[52:55], v[144:147], v[76:79], v[52:55]\n"
"ds_read_b128 v[72:75], v95 offset:27648\n"
"v_add_f32 v66, v66, v67\n"
"v_mfma_f32_16x16x32_bf16 v[56:59], v[148:151], v[76:79], v[56:59]\n"
"v_add_f32 v64, v64, v66\n"
"s_waitcnt vmcnt(23) lgkmcnt(2)\n"
"v_mfma_f32_16x16x32_bf16 v[28:31], v[152:155], v[80:83], v[28:31]\n"
"ds_read_b128 v[76:79], v3 offset:28672\n"
"buffer_load_dwordx4 v[232:235], v1, s[4:7], s53 offen offset:0 sc1\n"
"v_mfma_f32_16x16x32_bf16 v[32:35], v[156:159], v[80:83], v[32:35]\n"
"v_add_f32 v60, v60, v62\n"
"v_mul_f32 v63, 0x3acccccd, v64\n"
"v_mfma_f32_16x16x32_bf16 v[36:39], v[152:155], v[84:87], v[36:39]\n"
"ds_read_b128 v[80:83], v3 offset:49152\n"
"v_fma_f32 v60, -v64, v63, v60\n"
"v_mfma_f32_16x16x32_bf16 v[40:43], v[156:159], v[84:87], v[40:43]\n"
"buffer_load_dwordx4 v[236:239], v1, s[4:7], s53 offen offset:1024 sc1\n"
"s_waitcnt lgkmcnt(2)\n"
"v_mfma_f32_16x16x32_bf16 v[44:47], v[152:155], v[68:71], v[44:47]\n"
"ds_read_b128 v[84:87], v95 offset:8192\n"
"v_fmamk_f32 v60, v60, 0x3acccccd, v27\n"
"v_mfma_f32_16x16x32_bf16 v[48:51], v[156:159], v[68:71], v[48:51]\n"
"v_max_f32 v60, v60, v27\n"
"v_rsq_f32 v62, v60\n"
"v_mfma_f32_16x16x32_bf16 v[52:55], v[152:155], v[72:75], v[52:55]\n"
"ds_read_b128 v[68:71], v95 offset:28672\n"
"buffer_load_dwordx4 v[240:243], v1, s[4:7], s54 offen offset:0 sc1\n"
"v_mfma_f32_16x16x32_bf16 v[56:59], v[156:159], v[72:75], v[56:59]\n"
"v_mul_f32 v63, -v63, v62\n"
"s_waitcnt vmcnt(24) lgkmcnt(2)\n"
"v_mfma_f32_16x16x32_bf16 v[28:31], v[160:163], v[76:79], v[28:31]\n"
"ds_read_b128 v[72:75], v3 offset:29696\n"
"v_mfma_f32_16x16x32_bf16 v[32:35], v[164:167], v[76:79], v[32:35]\n"
"buffer_load_dwordx4 v[244:247], v1, s[4:7], s54 offen offset:1024 sc1\n"
"v_mfma_f32_16x16x32_bf16 v[36:39], v[160:163], v[80:83], v[36:39]\n"
"ds_read_b128 v[76:79], v3 offset:50176\n"
"v_mfma_f32_16x16x32_bf16 v[40:43], v[164:167], v[80:83], v[40:43]\n"
"ds_write_b64 v16, v[62:63] offset:0\n"
"s_waitcnt lgkmcnt(3)\n"
"v_mfma_f32_16x16x32_bf16 v[44:47], v[160:163], v[84:87], v[44:47]\n"
"ds_read_b128 v[80:83], v95 offset:9216\n"
"buffer_load_dwordx4 v[248:251], v1, s[4:7], s55 offen offset:0 sc1\n"
"v_mfma_f32_16x16x32_bf16 v[48:51], v[164:167], v[84:87], v[48:51]\n"
"v_mfma_f32_16x16x32_bf16 v[52:55], v[160:163], v[68:71], v[52:55]\n"
"ds_read_b128 v[84:87], v95 offset:29696\n"
"v_mfma_f32_16x16x32_bf16 v[56:59], v[164:167], v[68:71], v[56:59]\n"
"buffer_load_dwordx4 v[252:255], v1, s[4:7], s55 offen offset:1024 sc1\n"
"ds_read2st64_b32 v[60:61], v17 offset0:4 offset1:12\n"
"s_waitcnt vmcnt(25) lgkmcnt(4)\n"
"v_mfma_f32_16x16x32_bf16 v[28:31], v[168:171], v[72:75], v[28:31]\n"
"ds_read_b128 v[68:71], v3 offset:30720\n"
"s_mov_b32 m0, 163328\n"
"v_mfma_f32_16x16x32_bf16 v[32:35], v[172:175], v[72:75], v[32:35]\n"
"ds_read2st64_b32 v[62:63], v17 offset0:20 offset1:28\n"
"v_mfma_f32_16x16x32_bf16 v[36:39], v[168:171], v[76:79], v[36:39]\n"
"ds_read_b128 v[72:75], v3 offset:51200\n"
"buffer_load_dword v89, s[8:11], 0 offen lds\n"
"v_mfma_f32_16x16x32_bf16 v[40:43], v[172:175], v[76:79], v[40:43]\n"
"ds_read2st64_b32 v[64:65], v18 offset0:4 offset1:12\n"
"s_waitcnt lgkmcnt(5)\n"
"v_mfma_f32_16x16x32_bf16 v[44:47], v[168:171], v[80:83], v[44:47]\n"
"ds_read_b128 v[76:79], v95 offset:10240\n"
"ds_write_b32 v13, v13 offset:32\n"
"v_mfma_f32_16x16x32_bf16 v[48:51], v[172:175], v[80:83], v[48:51]\n"
"ds_read2st64_b32 v[66:67], v18 offset0:20 offset1:28\n"
"v_mfma_f32_16x16x32_bf16 v[52:55], v[168:171], v[84:87], v[52:55]\n"
"ds_read_b128 v[80:83], v95 offset:30720\n"
"v_mfma_f32_16x16x32_bf16 v[56:59], v[172:175], v[84:87], v[56:59]\n"
"s_waitcnt vmcnt(23) lgkmcnt(5)\n"
"v_mfma_f32_16x16x32_bf16 v[28:31], v[176:179], v[68:71], v[28:31]\n"
"ds_read_b128 v[84:87], v3 offset:31744\n"
"v_mfma_f32_16x16x32_bf16 v[32:35], v[180:183], v[68:71], v[32:35]\n"
"v_mfma_f32_16x16x32_bf16 v[36:39], v[176:179], v[72:75], v[36:39]\n"
"ds_read_b128 v[68:71], v3 offset:52224\n"
"v_mfma_f32_16x16x32_bf16 v[40:43], v[180:183], v[72:75], v[40:43]\n"
"s_waitcnt lgkmcnt(2)\n"
"v_mfma_f32_16x16x32_bf16 v[44:47], v[176:179], v[76:79], v[44:47]\n"
"ds_read_b128 v[72:75], v95 offset:11264\n"
"v_mfma_f32_16x16x32_bf16 v[48:51], v[180:183], v[76:79], v[48:51]\n"
"s_waitcnt vmcnt(13)\n"
"ds_write_b32 v13, v13 offset:48\n"
"v_mfma_f32_16x16x32_bf16 v[52:55], v[176:179], v[80:83], v[52:55]\n"
"ds_read_b128 v[76:79], v95 offset:31744\n"
"v_mfma_f32_16x16x32_bf16 v[56:59], v[180:183], v[80:83], v[56:59]\n"
"v_add_f32 v60, v60, v61\n"
"s_waitcnt lgkmcnt(3)\n"
"v_mfma_f32_16x16x32_bf16 v[28:31], v[184:187], v[84:87], v[28:31]\n"
"ds_read_b128 v[80:83], v3 offset:32768\n"
"v_add_f32 v62, v62, v63\n"
"v_mfma_f32_16x16x32_bf16 v[32:35], v[188:191], v[84:87], v[32:35]\n"
"v_add_f32 v64, v64, v65\n"
"v_add_f32 v66, v66, v67\n"
"v_mfma_f32_16x16x32_bf16 v[36:39], v[184:187], v[68:71], v[36:39]\n"
"ds_read_b128 v[84:87], v3 offset:53248\n"
"v_add_f32 v64, v64, v66\n"
"v_mfma_f32_16x16x32_bf16 v[40:43], v[188:191], v[68:71], v[40:43]\n"
"v_add_f32 v60, v60, v62\n"
"s_waitcnt lgkmcnt(2)\n"
"v_mfma_f32_16x16x32_bf16 v[44:47], v[184:187], v[72:75], v[44:47]\n"
"ds_read_b128 v[68:71], v95 offset:12288\n"
"v_mul_f32 v63, 0x3acccccd, v64\n"
"v_mfma_f32_16x16x32_bf16 v[48:51], v[188:191], v[72:75], v[48:51]\n"
"v_fma_f32 v60, -v64, v63, v60\n"
"v_fmamk_f32 v60, v60, 0x3acccccd, v27\n"
"v_mfma_f32_16x16x32_bf16 v[52:55], v[184:187], v[76:79], v[52:55]\n"
"ds_read_b128 v[72:75], v95 offset:32768\n"
"v_max_f32 v60, v60, v27\n"
"v_mfma_f32_16x16x32_bf16 v[56:59], v[188:191], v[76:79], v[56:59]\n"
"ds_read_b32 v90, v14 offset:48\n"
"s_waitcnt lgkmcnt(3)\n"
"v_mfma_f32_16x16x32_bf16 v[28:31], v[192:195], v[80:83], v[28:31]\n"
"ds_read_b128 v[76:79], v3 offset:33792\n"
"v_rsq_f32 v62, v60\n"
"v_mfma_f32_16x16x32_bf16 v[32:35], v[196:199], v[80:83], v[32:35]\n"
"v_mul_f32 v63, -v63, v62\n"
"v_cndmask_b32 v16, 2.0, v16, s[40:41]\n"
"v_mfma_f32_16x16x32_bf16 v[36:39], v[192:195], v[84:87], v[36:39]\n"
"ds_read_b128 v[80:83], v3 offset:54272\n"
"v_mfma_f32_16x16x32_bf16 v[40:43], v[196:199], v[84:87], v[40:43]\n"
"s_waitcnt lgkmcnt(3)\n"
"v_mfma_f32_16x16x32_bf16 v[44:47], v[192:195], v[68:71], v[44:47]\n"
"ds_read_b128 v[84:87], v95 offset:13312\n"
"v_mfma_f32_16x16x32_bf16 v[48:51], v[196:199], v[68:71], v[48:51]\n"
"v_mfma_f32_16x16x32_bf16 v[52:55], v[192:195], v[72:75], v[52:55]\n"
"ds_read_b128 v[68:71], v95 offset:33792\n"
"v_mfma_f32_16x16x32_bf16 v[56:59], v[196:199], v[72:75], v[56:59]\n"
"s_waitcnt lgkmcnt(4)\n"
"v_cmp_eq_u32 vcc, 0, v90\n"
"ds_write_b64 v16, v[62:63] offset:512\n"
"s_waitcnt lgkmcnt(3)\n"
"v_mfma_f32_16x16x32_bf16 v[28:31], v[200:203], v[76:79], v[28:31]\n"
"ds_read_b128 v[72:75], v3 offset:34816\n"
"v_mfma_f32_16x16x32_bf16 v[32:35], v[204:207], v[76:79], v[32:35]\n"
"s_cbranch_vccnz .Lsl_18\n"
".Lbk_19:\n"
"ds_write_b32 v13, v13 offset:80\n"
"v_mfma_f32_16x16x32_bf16 v[36:39], v[200:203], v[80:83], v[36:39]\n"
"ds_read_b128 v[76:79], v3 offset:55296\n"
"v_lshrrev_b32 v19, 3, v0\n"
"v_mfma_f32_16x16x32_bf16 v[40:43], v[204:207], v[80:83], v[40:43]\n"
"v_add_u32 v19, s3, v19\n"
"s_waitcnt lgkmcnt(4)\n"
"v_mfma_f32_16x16x32_bf16 v[44:47], v[200:203], v[84:87], v[44:47]\n"
"ds_read_b128 v[80:83], v95 offset:14336\n"
"v_lshlrev_b32 v91, 27, v19\n"
"v_mfma_f32_16x16x32_bf16 v[48:51], v[204:207], v[84:87], v[48:51]\n"
"v_lshl_add_u32 v91, v0, 12, v91\n"
"v_mov_b32 v19, s16\n"
"v_mfma_f32_16x16x32_bf16 v[52:55], v[200:203], v[68:71], v[52:55]\n"
"ds_read_b128 v[84:87], v95 offset:34816\n"
"s_lshl_b32 s79, s2, 2\n"
"v_mfma_f32_16x16x32_bf16 v[56:59], v[204:207], v[68:71], v[56:59]\n"
"s_add_u32 s32, s79, 0x11000\n"
"s_waitcnt vmcnt(11) lgkmcnt(2)\n"
"v_mfma_f32_16x16x32_bf16 v[28:31], v[208:211], v[72:75], v[28:31]\n"
"ds_read_b128 v[68:71], v3 offset:35840\n"
"buffer_store_dword v19, v91, s[4:7], s32 offen sc1\n"
"v_mfma_f32_16x16x32_bf16 v[32:35], v[212:215], v[72:75], v[32:35]\n"
"v_mfma_f32_16x16x32_bf16 v[36:39], v[208:211], v[76:79], v[36:39]\n"
"ds_read_b128 v[72:75], v3 offset:56320\n"
"v_mfma_f32_16x16x32_bf16 v[40:43], v[212:215], v[76:79], v[40:43]\n"
"s_waitcnt lgkmcnt(2)\n"
"v_mfma_f32_16x16x32_bf16 v[44:47], v[208:211], v[80:83], v[44:47]\n"
"ds_read_b128 v[76:79], v95 offset:15360\n"
"v_mfma_f32_16x16x32_bf16 v[48:51], v[212:215], v[80:83], v[48:51]\n"
"v_mfma_f32_16x16x32_bf16 v[52:55], v[208:211], v[84:87], v[52:55]\n"
"ds_read_b128 v[80:83], v95 offset:35840\n"
"v_mfma_f32_16x16x32_bf16 v[56:59], v[212:215], v[84:87], v[56:59]\n"
"s_waitcnt vmcnt(10) lgkmcnt(2)\n"
"v_mfma_f32_16x16x32_bf16 v[28:31], v[216:219], v[68:71], v[28:31]\n"
"ds_read_b128 v[84:87], v3 offset:36864\n"
"v_mfma_f32_16x16x32_bf16 v[32:35], v[220:223], v[68:71], v[32:35]\n"
"ds_read_b32 v90, v14 offset:80\n"
"v_mfma_f32_16x16x32_bf16 v[36:39], v[216:219], v[72:75], v[36:39]\n"
"ds_read_b128 v[68:71], v4 offset:40960\n"
"v_mfma_f32_16x16x32_bf16 v[40:43], v[220:223], v[72:75], v[40:43]\n"
"s_waitcnt lgkmcnt(3)\n"
"v_mfma_f32_16x16x32_bf16 v[44:47], v[216:219], v[76:79], v[44:47]\n"
"ds_read_b128 v[72:75], v5 offset:40960\n"
"v_mfma_f32_16x16x32_bf16 v[48:51], v[220:223], v[76:79], v[48:51]\n"
"ds_read_b64 v[88:89], v94 offset:0\n"
"v_mfma_f32_16x16x32_bf16 v[52:55], v[216:219], v[80:83], v[52:55]\n"
"ds_read_b128 v[76:79], v6 offset:40960\n"
"v_mfma_f32_16x16x32_bf16 v[56:59], v[220:223], v[80:83], v[56:59]\n"
"s_waitcnt vmcnt(6) lgkmcnt(3)\n"
"v_mfma_f32_16x16x32_bf16 v[28:31], v[224:227], v[84:87], v[28:31]\n"
"ds_read_b128 v[80:83], v3 offset:57344\n"
"v_mfma_f32_16x16x32_bf16 v[32:35], v[228:231], v[84:87], v[32:35]\n"
"ds_read_b64 v[92:93], v94 offset:128\n"
"v_mfma_f32_16x16x32_bf16 v[28:31], v[232:235], v[68:71], v[28:31]\n"
"ds_read_b128 v[84:87], v4 offset:61440\n"
"v_mfma_f32_16x16x32_bf16 v[32:35], v[236:239], v[68:71], v[32:35]\n"
"v_cmp_eq_u32 vcc, 0, v90\n"
"s_waitcnt vmcnt(2) lgkmcnt(3)\n"
"v_mfma_f32_16x16x32_bf16 v[28:31], v[240:243], v[72:75], v[28:31]\n"
"ds_read_b128 v[68:71], v5 offset:61440\n"
"v_mfma_f32_16x16x32_bf16 v[32:35], v[244:247], v[72:75], v[32:35]\n"
"s_cbranch_vccnz .Lsl_30\n"
".Lbk_31:\n"
"v_mfma_f32_16x16x32_bf16 v[28:31], v[248:251], v[76:79], v[28:31]\n"
"ds_read_b128 v[72:75], v6 offset:61440\n"
"v_mfma_f32_16x16x32_bf16 v[32:35], v[252:255], v[76:79], v[32:35]\n"
"s_waitcnt lgkmcnt(2)\n"
"v_mfma_f32_16x16x32_bf16 v[36:39], v[224:227], v[80:83], v[36:39]\n"
"ds_read_b128 v[76:79], v95 offset:16384\n"
"v_mfma_f32_16x16x32_bf16 v[40:43], v[228:231], v[80:83], v[40:43]\n"
"v_mov_b32_dpp v16, v28 row_newbcast:0 row_mask:0xf bank_mask:0xf\n"
"v_mov_b32_dpp v17, v29 row_newbcast:0 row_mask:0xf bank_mask:0xf\n"
"v_mov_b32_dpp v18, v30 row_newbcast:0 row_mask:0xf bank_mask:0xf\n"
"v_mfma_f32_16x16x32_bf16 v[36:39], v[232:235], v[84:87], v[36:39]\n"
"v_add_u32 v4, 0x1c400, v4\n"
"ds_read_b128 v[80:83], v4 offset:0\n"
"v_mov_b32_dpp v19, v31 row_newbcast:0 row_mask:0xf bank_mask:0xf\n"
"v_mfma_f32_16x16x32_bf16 v[40:43], v[236:239], v[84:87], v[40:43]\n"
"v_mov_b32_dpp v20, v32 row_newbcast:0 row_mask:0xf bank_mask:0xf\n"
"v_mov_b32_dpp v21, v33 row_newbcast:0 row_mask:0xf bank_mask:0xf\n"
"s_waitcnt lgkmcnt(2)\n"
"v_mfma_f32_16x16x32_bf16 v[36:39], v[240:243], v[68:71], v[36:39]\n"
"v_add_u32 v5, 0x1c400, v5\n"
"ds_read_b128 v[84:87], v5 offset:0\n"
"v_mfma_f32_16x16x32_bf16 v[40:43], v[244:247], v[68:71], v[40:43]\n"
"v_mov_b32_dpp v22, v34 row_newbcast:0 row_mask:0xf bank_mask:0xf\n"
"v_mov_b32_dpp v23, v35 row_newbcast:0 row_mask:0xf bank_mask:0xf\n"
"v_mul_f32 v28, v88, v28\n"
"v_mfma_f32_16x16x32_bf16 v[36:39], v[248:251], v[72:75], v[36:39]\n"
"v_add_u32 v6, 0x1c400, v6\n"
"ds_read_b128 v[68:71], v6 offset:0\n"
"v_mul_f32 v29, v88, v29\n"
"v_mfma_f32_16x16x32_bf16 v[40:43], v[252:255], v[72:75], v[40:43]\n"
"v_mul_f32 v30, v88, v30\n"
"v_mul_f32 v31, v88, v31\n"
"s_waitcnt lgkmcnt(2)\n"
"v_mfma_f32_16x16x32_bf16 v[44:47], v[224:227], v[76:79], v[44:47]\n"
"ds_read_b128 v[72:75], v95 offset:36864\n"
"v_fmac_f32 v28, v89, v16\n"
"v_fmac_f32 v29, v89, v17\n"
"v_mfma_f32_16x16x32_bf16 v[48:51], v[228:231], v[76:79], v[48:51]\n"
"ds_write_b32 v13, v13 offset:64\n"
"v_fmac_f32 v30, v89, v18\n"
"v_mfma_f32_16x16x32_bf16 v[44:47], v[232:235], v[80:83], v[44:47]\n"
"ds_read_b128 v[76:79], v4 offset:20480\n"
"v_fmac_f32 v31, v89, v19\n"
"v_cvt_pk_bf16_f32 v28, v28, v29\n"
"v_mfma_f32_16x16x32_bf16 v[48:51], v[236:239], v[80:83], v[48:51]\n"
"v_cvt_pk_bf16_f32 v29, v30, v31\n"
"ds_write_b64 v7, v[28:29] offset:0\n"
"s_waitcnt lgkmcnt(4)\n"
"v_mfma_f32_16x16x32_bf16 v[44:47], v[240:243], v[84:87], v[44:47]\n"
"ds_read_b128 v[80:83], v5 offset:20480\n"
"v_mul_f32 v32, v88, v32\n"
"v_mul_f32 v33, v88, v33\n"
"v_mfma_f32_16x16x32_bf16 v[48:51], v[244:247], v[84:87], v[48:51]\n"
"v_mul_f32 v34, v88, v34\n"
"v_mul_f32 v35, v88, v35\n"
"v_fmac_f32 v32, v89, v20\n"
"v_mfma_f32_16x16x32_bf16 v[44:47], v[248:251], v[68:71], v[44:47]\n"
"ds_read_b128 v[84:87], v6 offset:20480\n"
"v_fmac_f32 v33, v89, v21\n"
"v_fmac_f32 v34, v89, v22\n"
"v_mfma_f32_16x16x32_bf16 v[48:51], v[252:255], v[68:71], v[48:51]\n"
"v_fmac_f32 v35, v89, v23\n"
"v_cvt_pk_bf16_f32 v32, v32, v33\n"
"s_waitcnt lgkmcnt(3)\n"
"v_mfma_f32_16x16x32_bf16 v[52:55], v[224:227], v[72:75], v[52:55]\n"
"v_add_u32 v3, 0xffffb000, v3\n"
"ds_read_b128 v[68:71], v3 offset:0\n"
"v_cvt_pk_bf16_f32 v33, v34, v35\n"
"v_mfma_f32_16x16x32_bf16 v[56:59], v[228:231], v[72:75], v[56:59]\n"
"ds_read_b32 v90, v14 offset:64\n"
"ds_write_b64 v8, v[32:33] offset:0\n"
"v_mfma_f32_16x16x32_bf16 v[52:55], v[232:235], v[76:79], v[52:55]\n"
"ds_read_b128 v[72:75], v3 offset:1024\n"
"ds_read_b64 v[88:89], v94 offset:256\n"
"v_mul_f32 v36, v92, v36\n"
"v_mfma_f32_16x16x32_bf16 v[56:59], v[236:239], v[76:79], v[56:59]\n"
"v_mul_f32 v37, v92, v37\n"
"v_mul_f32 v38, v92, v38\n"
"s_waitcnt lgkmcnt(5)\n"
"v_mfma_f32_16x16x32_bf16 v[52:55], v[240:243], v[80:83], v[52:55]\n"
"ds_read_b128 v[76:79], v3 offset:2048\n"
"v_mul_f32 v39, v92, v39\n"
"v_fmac_f32 v36, v93, v16\n"
"v_mfma_f32_16x16x32_bf16 v[56:59], v[244:247], v[80:83], v[56:59]\n"
"v_fmac_f32 v37, v93, v17\n"
"v_fmac_f32 v38, v93, v18\n"
"v_fmac_f32 v39, v93, v19\n"
"v_mfma_f32_16x16x32_bf16 v[52:55], v[248:251], v[84:87], v[52:55]\n"
"ds_read_b128 v[80:83], v3 offset:3072\n"
"v_cvt_pk_bf16_f32 v36, v36, v37\n"
"v_cvt_pk_bf16_f32 v37, v38, v39\n"
"v_mfma_f32_16x16x32_bf16 v[56:59], v[252:255], v[84:87], v[56:59]\n"
"ds_write_b64 v7, v[36:37] offset:3968\n"
"s_waitcnt lgkmcnt(4)\n"
"v_mfma_f32_16x16x32_bf16 v[60:63], v[96:99], v[68:71], 0\n"
"ds_read_b128 v[84:87], v3 offset:4096\n"
"v_mul_f32 v40, v92, v40\n"
"v_mul_f32 v41, v92, v41\n"
"v_mfma_f32_16x16x32_bf16 v[64:67], v[100:103], v[68:71], 0\n"
"v_cmp_eq_u32 vcc, 0, v90\n"
"v_mul_f32 v42, v92, v42\n"
"v_mul_f32 v43, v92, v43\n"
"v_mfma_f32_16x16x32_bf16 v[60:63], v[104:107], v[72:75], v[60:63]\n"
"ds_read_b128 v[68:71], v3 offset:5120\n"
"v_fmac_f32 v40, v93, v20\n"
"v_fmac_f32 v41, v93, v21\n"
"v_mfma_f32_16x16x32_bf16 v[64:67], v[108:111], v[72:75], v[64:67]\n"
"s_cbranch_vccnz .Lsl_22\n"
".Lbk_23:\n"
"s_waitcnt lgkmcnt(3)\n"
"v_mfma_f32_16x16x32_bf16 v[60:63], v[112:115], v[76:79], v[60:63]\n"
"ds_read_b128 v[72:75], v3 offset:6144\n"
"v_fmac_f32 v42, v93, v22\n"
"v_fmac_f32 v43, v93, v23\n"
"v_mfma_f32_16x16x32_bf16 v[64:67], v[116:119], v[76:79], v[64:67]\n"
"v_cvt_pk_bf16_f32 v40, v40, v41\n"
"v_cvt_pk_bf16_f32 v41, v42, v43\n"
"ds_write_b64 v8, v[40:41] offset:3968\n"
"v_mfma_f32_16x16x32_bf16 v[60:63], v[120:123], v[80:83], v[60:63]\n"
"ds_read_b128 v[76:79], v3 offset:7168\n"
"ds_read_b64 v[92:93], v94 offset:384\n"
"v_mul_f32 v44, v88, v44\n"
"v_mfma_f32_16x16x32_bf16 v[64:67], v[124:127], v[80:83], v[64:67]\n"
"s_add_u32 s31, s28, 0x140000\n"
"v_mul_f32 v45, v88, v45\n"
"s_waitcnt lgkmcnt(4)\n"
"v_mfma_f32_16x16x32_bf16 v[60:63], v[128:131], v[84:87], v[60:63]\n"
"ds_read_b128 v[80:83], v3 offset:8192\n"
"s_add_u32 m0, s29, 0xa000\n"
"v_mul_f32 v46, v88, v46\n"
"v_mfma_f32_16x16x32_bf16 v[64:67], v[132:135], v[84:87], v[64:67]\n"
"buffer_load_dwordx4 v2, s[8:11], s31 offen offset:0 lds\n"
"v_mul_f32 v47, v88, v47\n"
"v_fmac_f32 v44, v89, v16\n"
"v_mfma_f32_16x16x32_bf16 v[60:63], v[136:139], v[68:71], v[60:63]\n"
"ds_read_b128 v[84:87], v3 offset:9216\n"
"s_add_u32 m0, s29, 0xa3c0\n"
"v_fmac_f32 v45, v89, v17\n"
"v_mfma_f32_16x16x32_bf16 v[64:67], v[140:143], v[68:71], v[64:67]\n"
"v_fmac_f32 v46, v89, v18\n"
"v_fmac_f32 v47, v89, v19\n"
"s_waitcnt lgkmcnt(3)\n"
"v_mfma_f32_16x16x32_bf16 v[60:63], v[144:147], v[72:75], v[60:63]\n"
"ds_read_b128 v[68:71], v3 offset:10240\n"
"buffer_load_dwordx4 v2, s[8:11], s31 offen offset:64 lds\n"
"v_cvt_pk_bf16_f32 v44, v44, v45\n"
"v_mfma_f32_16x16x32_bf16 v[64:67], v[148:151], v[72:75], v[64:67]\n"
"s_add_u32 m0, s29, 0xa780\n"
"v_cvt_pk_bf16_f32 v45, v46, v47\n"
"ds_write_b64 v7, v[44:45] offset:7936\n"
"v_mfma_f32_16x16x32_bf16 v[60:63], v[152:155], v[76:79], v[60:63]\n"
"ds_read_b128 v[72:75], v3 offset:11264\n"
"v_mul_f32 v48, v88, v48\n"
"v_mul_f32 v49, v88, v49\n"
"v_mfma_f32_16x16x32_bf16 v[64:67], v[156:159], v[76:79], v[64:67]\n"
"buffer_load_dwordx4 v2, s[8:11], s31 offen offset:128 lds\n"
"v_mul_f32 v50, v88, v50\n"
"s_waitcnt lgkmcnt(3)\n"
"v_mfma_f32_16x16x32_bf16 v[60:63], v[160:163], v[80:83], v[60:63]\n"
"ds_read_b128 v[76:79], v3 offset:12288\n"
"s_add_u32 m0, s29, 0xab40\n"
"v_mul_f32 v51, v88, v51\n"
"v_mfma_f32_16x16x32_bf16 v[64:67], v[164:167], v[80:83], v[64:67]\n"
"v_fmac_f32 v48, v89, v20\n"
"v_fmac_f32 v49, v89, v21\n"
"v_fmac_f32 v50, v89, v22\n"
"v_mfma_f32_16x16x32_bf16 v[60:63], v[168:171], v[84:87], v[60:63]\n"
"ds_read_b128 v[80:83], v3 offset:13312\n"
"buffer_load_dwordx4 v2, s[8:11], s31 offen offset:192 lds\n"
"v_fmac_f32 v51, v89, v23\n"
"v_mfma_f32_16x16x32_bf16 v[64:67], v[172:175], v[84:87], v[64:67]\n"
"s_add_u32 m0, s29, 0xaf00\n"
"v_cvt_pk_bf16_f32 v48, v48, v49\n"
"s_waitcnt lgkmcnt(2)\n"
"v_mfma_f32_16x16x32_bf16 v[60:63], v[176:179], v[68:71], v[60:63]\n"
"ds_read_b128 v[84:87], v3 offset:14336\n"
"v_cvt_pk_bf16_f32 v49, v50, v51\n"
"ds_write_b64 v8, v[48:49] offset:7936\n"
"v_mfma_f32_16x16x32_bf16 v[64:67], v[180:183], v[68:71], v[64:67]\n"
"buffer_load_dwordx4 v2, s[8:11], s31 offen offset:256 lds\n"
"ds_read_b64 v[88:89], v94 offset:512\n"
"v_mul_f32 v52, v92, v52\n"
"v_mfma_f32_16x16x32_bf16 v[60:63], v[184:187], v[72:75], v[60:63]\n"
"ds_read_b128 v[68:71], v3 offset:15360\n"
"s_add_u32 s31, s28, 0x145000\n"
"v_mul_f32 v53, v92, v53\n"
"v_mfma_f32_16x16x32_bf16 v[64:67], v[188:191], v[72:75], v[64:67]\n"
"s_add_u32 m0, s29, 0xf000\n"
"v_mul_f32 v54, v92, v54\n"
"s_waitcnt lgkmcnt(4)\n"
"v_mfma_f32_16x16x32_bf16 v[60:63], v[192:195], v[76:79], v[60:63]\n"
"ds_read_b128 v[72:75], v3 offset:16384\n"
"buffer_load_dwordx4 v2, s[8:11], s31 offen offset:0 lds\n"
"v_mul_f32 v55, v92, v55\n"
"v_mfma_f32_16x16x32_bf16 v[64:67], v[196:199], v[76:79], v[64:67]\n"
"s_add_u32 m0, s29, 0xf3c0\n"
"v_fmac_f32 v52, v93, v16\n"
"v_fmac_f32 v53, v93, v17\n"
"v_mfma_f32_16x16x32_bf16 v[60:63], v[200:203], v[80:83], v[60:63]\n"
"v_add_u32 v4, 0xfffe3c00, v4\n"
"ds_read_b128 v[76:79], v4 offset:0\n"
"v_fmac_f32 v54, v93, v18\n"
"v_mfma_f32_16x16x32_bf16 v[64:67], v[204:207], v[80:83], v[64:67]\n"
"buffer_load_dwordx4 v2, s[8:11], s31 offen offset:64 lds\n"
"v_fmac_f32 v55, v93, v19\n"
"s_waitcnt lgkmcnt(2)\n"
"v_mfma_f32_16x16x32_bf16 v[60:63], v[208:211], v[84:87], v[60:63]\n"
"v_add_u32 v5, 0xfffe3c00, v5\n"
"ds_read_b128 v[80:83], v5 offset:0\n"
"v_cvt_pk_bf16_f32 v52, v52, v53\n"
"v_mfma_f32_16x16x32_bf16 v[64:67], v[212:215], v[84:87], v[64:67]\n"
"s_add_u32 m0, s29, 0xf780\n"
"v_cvt_pk_bf16_f32 v53, v54, v55\n"
"ds_write_b64 v7, v[52:53] offset:11904\n"
"v_mfma_f32_16x16x32_bf16 v[60:63], v[216:219], v[68:71], v[60:63]\n"
"v_add_u32 v6, 0xfffe3c00, v6\n"
"ds_read_b128 v[84:87], v6 offset:0\n"
"buffer_load_dwordx4 v2, s[8:11], s31 offen offset:128 lds\n"
"v_mfma_f32_16x16x32_bf16 v[64:67], v[220:223], v[68:71], v[64:67]\n"
"s_add_u32 m0, s29, 0xfb40\n"
"v_mul_f32 v56, v92, v56\n"
"s_waitcnt lgkmcnt(3)\n"
"v_mfma_f32_16x16x32_bf16 v[60:63], v[224:227], v[72:75], v[60:63]\n"
"v_mul_f32 v57, v92, v57\n"
"v_mul_f32 v58, v92, v58\n"
"v_mul_f32 v59, v92, v59\n"
"v_mfma_f32_16x16x32_bf16 v[64:67], v[228:231], v[72:75], v[64:67]\n"
"buffer_load_dwordx4 v2, s[8:11], s31 offen offset:192 lds\n"
"v_fmac_f32 v56, v93, v20\n"
"v_fmac_f32 v57, v93, v21\n"
"v_mfma_f32_16x16x32_bf16 v[60:63], v[232:235], v[76:79], v[60:63]\n"
"s_add_u32 m0, s29, 0xff00\n"
"v_fmac_f32 v58, v93, v22\n"
"v_mfma_f32_16x16x32_bf16 v[64:67], v[236:239], v[76:79], v[64:67]\n"
"v_fmac_f32 v59, v93, v23\n"
"s_waitcnt lgkmcnt(0)\n"
"v_mfma_f32_16x16x32_bf16 v[60:63], v[240:243], v[80:83], v[60:63]\n"
"buffer_load_dwordx4 v2, s[8:11], s31 offen offset:256 lds\n"
"v_cvt_pk_bf16_f32 v56, v56, v57\n"
"v_mfma_f32_16x16x32_bf16 v[64:67], v[244:247], v[80:83], v[64:67]\n"
"v_cvt_pk_bf16_f32 v57, v58, v59\n"
"ds_write_b64 v8, v[56:57] offset:11904\n"
"v_mfma_f32_16x16x32_bf16 v[60:63], v[248:251], v[84:87], v[60:63]\n"
"v_mfma_f32_16x16x32_bf16 v[64:67], v[252:255], v[84:87], v[64:67]\n"
"s_nop 4\n"
"v_mul_f32 v60, v88, v60\n"
"v_mul_f32 v61, v88, v61\n"
"v_mul_f32 v62, v88, v62\n"
"v_mul_f32 v63, v88, v63\n"
"v_fmac_f32 v60, v89, v16\n"
"v_fmac_f32 v61, v89, v17\n"
"v_fmac_f32 v62, v89, v18\n"
"v_fmac_f32 v63, v89, v19\n"
"v_cvt_pk_bf16_f32 v60, v60, v61\n"
"v_cvt_pk_bf16_f32 v61, v62, v63\n"
"ds_write_b64 v7, v[60:61] offset:15872\n"
"v_mul_f32 v64, v88, v64\n"
"v_mul_f32 v65, v88, v65\n"
"v_mul_f32 v66, v88, v66\n"
"v_mul_f32 v67, v88, v67\n"
"v_fmac_f32 v64, v89, v20\n"
"v_fmac_f32 v65, v89, v21\n"
"v_fmac_f32 v66, v89, v22\n"
"v_fmac_f32 v67, v89, v23\n"
"v_cvt_pk_bf16_f32 v64, v64, v65\n"
"v_cvt_pk_bf16_f32 v65, v66, v67\n"
"ds_write_b64 v8, v[64:65] offset:15872\n"
"ds_write_b32 v13, v13 offset:96\n"
"v_mov_b32 v46, 0\n"
"v_mov_b32 v47, 0\n"
"v_mov_b32 v50, 0\n"
"v_mov_b32 v51, 0\n"
"v_mov_b32 v72, s42\n"
"v_mov_b32 v73, s42\n"
"v_mov_b32 v74, s42\n"
"v_mov_b32 v75, s42\n"
"ds_read_b32 v90, v14 offset:96\n"
"s_mov_b64 exec, 0xffff\n"
"ds_read_b64 v[46:47], v24 offset:4000\n"
"ds_read_b64 v[50:51], v24 offset:4040\n"
"s_mov_b64 exec, -1\n"
"ds_read_b64 v[44:45], v25 offset:3968\n"
"ds_read_b64 v[56:57], v25 offset:80\n"
"ds_read_b64 v[58:59], v24 offset:112\n"
"ds_read_b64 v[64:65], v25 offset:4048\n"
"ds_read_b64 v[66:67], v24 offset:4080\n"
"ds_read_b64 v[48:49], v25 offset:4008\n"
"ds_read_b64 v[60:61], v25 offset:120\n"
"ds_read_b64 v[62:63], v24 offset:152\n"
"ds_read_b64 v[68:69], v25 offset:4088\n"
"ds_read_b64 v[70:71], v24 offset:4120\n"
"s_mov_b32 s17, 0x100000\n"
"s_waitcnt lgkmcnt(12)\n"
"v_cmp_eq_u32 vcc, 0, v90\n"
"s_cbranch_vccz .Lok_35\n"
".Ltp_34:\n"
"ds_read_b32 v90, v14 offset:96\n"
"s_mov_b64 exec, 0xffff\n"
"ds_read_b64 v[46:47], v24 offset:4000\n"
"ds_read_b64 v[50:51], v24 offset:4040\n"
"s_mov_b64 exec, -1\n"
"ds_read_b64 v[44:45], v25 offset:3968\n"
"ds_read_b64 v[56:57], v25 offset:80\n"
"ds_read_b64 v[58:59], v24 offset:112\n"
"ds_read_b64 v[64:65], v25 offset:4048\n"
"ds_read_b64 v[66:67], v24 offset:4080\n"
"ds_read_b64 v[48:49], v25 offset:4008\n"
"ds_read_b64 v[60:61], v25 offset:120\n"
"ds_read_b64 v[62:63], v24 offset:152\n"
"ds_read_b64 v[68:69], v25 offset:4088\n"
"ds_read_b64 v[70:71], v24 offset:4120\n"
"s_sub_u32 s17, s17, 1\n"
"s_cbranch_scc1 .Lok_35\n"
"s_waitcnt lgkmcnt(12)\n"
"v_cmp_eq_u32 vcc, 0, v90\n"
"s_cbranch_vccnz .Ltp_34\n"
"s_waitcnt lgkmcnt(0)\n"
".Lok_35:\n"
"s_add_u32 s31, s28, 0x14a000\n"
"s_add_u32 m0, s29, 0\n"
"s_nop 0\n"
"buffer_load_dwordx4 v2, s[8:11], s31 offen offset:0 lds\n"
"ds_read_b64_tr_b16 v[28:29], v26 offset:0\n"
"ds_read_b64_tr_b16 v[30:31], v26 offset:3968\n"
"ds_read_b64_tr_b16 v[32:33], v26 offset:40\n"
"v_mov_b32 v52, 0x0c0c0c0c\n"
"v_mov_b32 v53, 0xc0c0100\n"
"v_mov_b32 v54, 0x1000c0c\n"
"v_cndmask_b32 v53, v53, v54, s[34:35]\n"
"v_cndmask_b32 v80, v53, v52, s[36:37]\n"
"v_cndmask_b32 v82, v52, v53, s[36:37]\n"
"v_mov_b32 v53, 0xc0c0302\n"
"v_mov_b32 v54, 0x3020c0c\n"
"v_cndmask_b32 v53, v53, v54, s[34:35]\n"
"v_cndmask_b32 v81, v53, v52, s[38:39]\n"
"v_cndmask_b32 v83, v52, v53, s[38:39]\n"
"s_waitcnt lgkmcnt(10)\n"
"v_mfma_f32_16x16x32_bf16 v[56:59], v[56:59], v[44:47], v[72:75]\n"
"s_waitcnt lgkmcnt(8)\n"
"v_mfma_f32_16x16x32_bf16 v[64:67], v[64:67], v[44:47], 0\n"
"ds_read_b64_tr_b16 v[34:35], v26 offset:4008\n"
"s_add_u32 m0, s29, 0x3c0\n"
"s_nop 0\n"
"buffer_load_dwordx4 v2, s[8:11], s31 offen offset:64 lds\n"
"ds_read_b64_tr_b16 v[36:37], v26 offset:32\n"
"ds_read_b64_tr_b16 v[38:39], v26 offset:4000\n"
"s_waitcnt lgkmcnt(8)\n"
"v_mfma_f32_16x16x32_bf16 v[60:63], v[60:63], v[48:51], v[72:75]\n"
"s_waitcnt lgkmcnt(6)\n"
"v_mfma_f32_16x16x32_bf16 v[68:71], v[68:71], v[48:51], 0\n"
"ds_read_b64_tr_b16 v[40:41], v26 offset:72\n"
"ds_read_b64_tr_b16 v[42:43], v26 offset:4040\n"
"v_mov_b32 v84, 0x3f803f80\n"
"v_mov_b32 v85, 0x3f803f80\n"
"v_mov_b32 v86, 0x3f803f80\n"
"v_mov_b32 v87, 0x3f803f80\n"
"v_cndmask_b32 v76, v56, v57, s[34:35]\n"
"v_cndmask_b32 v72, v64, v65, s[34:35]\n"
"v_cndmask_b32 v76, v76, v72, s[36:37]\n"
"v_cndmask_b32 v77, v58, v59, s[34:35]\n"
"v_cndmask_b32 v72, v66, v67, s[34:35]\n"
"v_cndmask_b32 v77, v77, v72, s[38:39]\n"
"v_max_f32 v52, v76, v77\n"
"s_add_u32 m0, s29, 0x780\n"
"s_nop 0\n"
"buffer_load_dwordx4 v2, s[8:11], s31 offen offset:128 lds\n"
"v_max_f32 v54, v76, v77\n"
"v_cndmask_b32 v78, v60, v61, s[34:35]\n"
"v_cndmask_b32 v72, v68, v69, s[34:35]\n"
"v_cndmask_b32 v78, v78, v72, s[36:37]\n"
"v_cndmask_b32 v79, v62, v63, s[34:35]\n"
"v_cndmask_b32 v72, v70, v71, s[34:35]\n"
"v_cndmask_b32 v79, v79, v72, s[38:39]\n"
"v_max_f32 v53, v78, v79\n"
"v_max_f32 v55, v78, v79\n"
"v_permlane16_swap_b32 v52, v54\n"
"s_nop 0\n"
"v_permlane16_swap_b32 v53, v55\n"
"v_max_f32 v72, v52, v54\n"
"v_max_f32 v52, v52, v54\n"
"v_max_f32 v73, v53, v55\n"
"v_max_f32 v53, v53, v55\n"
"v_permlane32_swap_b32 v52, v72\n"
"s_nop 0\n"
"v_permlane32_swap_b32 v53, v73\n"
"s_add_u32 m0, s29, 0xb40\n"
"s_nop 0\n"
"buffer_load_dwordx4 v2, s[8:11], s31 offen offset:192 lds\n"
"v_max_f32 v52, v52, v72\n"
"v_max_f32 v53, v53, v73\n"
"v_mul_f32 v52, 0xbea52b5b, v52\n"
"v_mul_f32 v53, 0xbea52b5b, v53\n"
"v_fmamk_f32 v76, v76, 0x3ea52b5b, v52\n"
"v_fmamk_f32 v77, v77, 0x3ea52b5b, v52\n"
"v_fmamk_f32 v78, v78, 0x3ea52b5b, v53\n"
"v_fmamk_f32 v79, v79, 0x3ea52b5b, v53\n"
"v_exp_f32 v76, v76\n"
"v_exp_f32 v77, v77\n"
"v_exp_f32 v78, v78\n"
"v_exp_f32 v79, v79\n"
"v_cvt_pk_bf16_f32 v52, v76, v77\n"
"v_cvt_pk_bf16_f32 v53, v78, v79\n"
"v_perm_b32 v44, v52, v52, v80\n"
"v_perm_b32 v45, v52, v52, v81\n"
"v_perm_b32 v46, v52, v52, v82\n"
"s_add_u32 m0, s29, 0xf00\n"
"s_nop 0\n"
"buffer_load_dwordx4 v2, s[8:11], s31 offen offset:256 lds\n"
"v_perm_b32 v47, v52, v52, v83\n"
"v_perm_b32 v48, v53, v53, v80\n"
"v_perm_b32 v49, v53, v53, v81\n"
"v_perm_b32 v50, v53, v53, v82\n"
"v_perm_b32 v51, v53, v53, v83\n"
"s_waitcnt lgkmcnt(0)\n"
"s_waitcnt vmcnt(5)\n"
"ds_write_b32 v13, v13 offset:112\n"
"v_mfma_f32_16x16x32_bf16 v[56:59], v[84:87], v[44:47], 0\n"
"v_mfma_f32_16x16x32_bf16 v[60:63], v[84:87], v[48:51], 0\n"
"v_mfma_f32_16x16x32_bf16 v[28:31], v[28:31], v[44:47], 0\n"
"v_mfma_f32_16x16x32_bf16 v[32:35], v[32:35], v[48:51], 0\n"
"v_mfma_f32_16x16x32_bf16 v[36:39], v[36:39], v[44:47], 0\n"
"v_mfma_f32_16x16x32_bf16 v[40:43], v[40:43], v[48:51], 0\n"
"v_cndmask_b32 v88, 2.0, v1, s[40:41]\n"
"v_rcp_f32 v52, v56\n"
"v_rcp_f32 v53, v60\n"
"s_mov_b32 s17, 0x100000\n"
".Ltp_36:\n"
"ds_read_b32 v90, v14 offset:112\n"
"s_sub_u32 s17, s17, 1\n"
"s_cbranch_scc1 .Lou_37\n"
"s_waitcnt lgkmcnt(0)\n"
"v_cmp_eq_u32 vcc, 0, v90\n"
"s_cbranch_vccnz .Ltp_36\n"
".Lou_37:\n"
"s_waitcnt lgkmcnt(0)\n"
"ds_read_b128 v[56:59], v3 offset:40960\n"
"s_mov_b32 s31, s30\n"
"v_mul_f32 v28, v52, v28\n"
"v_mul_f32 v29, v52, v29\n"
"v_mul_f32 v30, v52, v30\n"
"v_mul_f32 v31, v52, v31\n"
"v_mul_f32 v32, v53, v32\n"
"ds_read_b128 v[60:63], v3 offset:41984\n"
"v_mul_f32 v33, v53, v33\n"
"v_mul_f32 v34, v53, v34\n"
"v_mul_f32 v35, v53, v35\n"
"v_cvt_pk_bf16_f32 v28, v28, v29\n"
"v_cvt_pk_bf16_f32 v29, v30, v31\n"
"v_cvt_pk_bf16_f32 v30, v32, v33\n"
"ds_read_b128 v[64:67], v3 offset:43008\n"
"v_cvt_pk_bf16_f32 v31, v34, v35\n"
"v_min_u32 v28, -2, v28\n"
"v_min_u32 v29, -2, v29\n"
"v_min_u32 v30, -2, v30\n"
"v_min_u32 v31, -2, v31\n"
"buffer_store_dwordx4 v[28:31], v1, s[4:7], s31 offen offset:0 sc1\n"
"ds_read_b128 v[68:71], v3 offset:44032\n"
"v_mul_f32 v36, v52, v36\n"
"v_mul_f32 v37, v52, v37\n"
"v_mul_f32 v38, v52, v38\n"
"v_mul_f32 v39, v52, v39\n"
"v_mul_f32 v40, v53, v40\n"
"v_mul_f32 v41, v53, v41\n"
"ds_read_b128 v[72:75], v3 offset:45056\n"
"v_mul_f32 v42, v53, v42\n"
"v_mul_f32 v43, v53, v43\n"
"v_cvt_pk_bf16_f32 v36, v36, v37\n"
"v_cvt_pk_bf16_f32 v37, v38, v39\n"
"v_cvt_pk_bf16_f32 v38, v40, v41\n"
"v_cvt_pk_bf16_f32 v39, v42, v43\n"
"v_min_u32 v36, -2, v36\n"
"v_min_u32 v37, -2, v37\n"
"v_min_u32 v38, -2, v38\n"
"v_min_u32 v39, -2, v39\n"
"buffer_store_dwordx4 v[36:39], v88, s[4:7], s31 offen offset:1024 sc1\n"
"ds_write_b32 v13, v13 offset:128\n"
"s_mul_i32 s79, s43, 0x1400\n"
"v_lshrrev_b32 v92, 2, v0\n"
"v_and_b32 v91, 3, v0\n"
"v_lshlrev_b32 v91, 4, v91\n"
"v_lshl_add_u32 v92, v92, 10, v91\n"
"v_add_u32 v92, s79, v92\n"
"v_mov_b32 v91, 0x40000\n"
"v_cmp_gt_i32 vcc, 20, v0\n"
"s_nop 1\n"
"v_cndmask_b32 v93, v91, v92, vcc\n"
"s_waitcnt lgkmcnt(4)\n"
"v_mfma_f32_16x16x32_bf16 v[28:31], v[96:99], v[56:59], 0\n"
"ds_read_b128 v[76:79], v3 offset:46080\n"
"s_add_u32 s31, s28, 0x14f000\n"
"v_mfma_f32_16x16x32_bf16 v[32:35], v[100:103], v[56:59], 0\n"
"s_add_u32 m0, s29, 0x5000\n"
"v_mfma_f32_16x16x32_bf16 v[52:55], v[56:59], v[56:59], 0\n"
"v_mfma_f32_16x16x32_bf16 v[28:31], v[104:107], v[60:63], v[28:31]\n"
"ds_read_b128 v[56:59], v3 offset:47104\n"
"buffer_load_dwordx4 v2, s[8:11], s31 offen offset:0 lds\n"
"v_mfma_f32_16x16x32_bf16 v[32:35], v[108:111], v[60:63], v[32:35]\n"
"s_add_u32 m0, s29, 0x53c0\n"
"s_waitcnt lgkmcnt(4)\n"
"v_mfma_f32_16x16x32_bf16 v[28:31], v[112:115], v[64:67], v[28:31]\n"
"ds_read_b128 v[60:63], v3 offset:48128\n"
"v_mfma_f32_16x16x32_bf16 v[32:35], v[116:119], v[64:67], v[32:35]\n"
"v_mfma_f32_16x16x32_bf16 v[28:31], v[120:123], v[68:71], v[28:31]\n"
"ds_read_b128 v[64:67], v3 offset:49152\n"
"buffer_load_dwordx4 v2, s[8:11], s31 offen offset:64 lds\n"
"v_mfma_f32_16x16x32_bf16 v[32:35], v[124:127], v[68:71], v[32:35]\n"
"s_add_u32 m0, s29, 0x5780\n"
"s_waitcnt lgkmcnt(3)\n"
"v_mfma_f32_16x16x32_bf16 v[28:31], v[128:131], v[72:75], v[28:31]\n"
"ds_read_b128 v[68:71], v3 offset:50176\n"
"v_mfma_f32_16x16x32_bf16 v[32:35], v[132:135], v[72:75], v[32:35]\n"
"v_mfma_f32_16x16x32_bf16 v[52:55], v[72:75], v[72:75], v[52:55]\n"
"buffer_load_dwordx4 v2, s[8:11], s31 offen offset:128 lds\n"
"v_mfma_f32_16x16x32_bf16 v[28:31], v[136:139], v[76:79], v[28:31]\n"
"ds_read_b128 v[72:75], v3 offset:51200\n"
"s_add_u32 m0, s29, 0x5b40\n"
"v_mfma_f32_16x16x32_bf16 v[32:35], v[140:143], v[76:79], v[32:35]\n"
"s_waitcnt lgkmcnt(3)\n"
"v_mfma_f32_16x16x32_bf16 v[28:31], v[144:147], v[56:59], v[28:31]\n"
"ds_read_b128 v[76:79], v3 offset:52224\n"
"v_mfma_f32_16x16x32_bf16 v[32:35], v[148:151], v[56:59], v[32:35]\n"
"buffer_load_dwordx4 v2, s[8:11], s31 offen offset:192 lds\n"
"v_mfma_f32_16x16x32_bf16 v[28:31], v[152:155], v[60:63], v[28:31]\n"
"ds_read_b128 v[56:59], v3 offset:53248\n"
"s_add_u32 m0, s29, 0x5f00\n"
"v_mfma_f32_16x16x32_bf16 v[32:35], v[156:159], v[60:63], v[32:35]\n"
"s_waitcnt lgkmcnt(3)\n"
"v_mfma_f32_16x16x32_bf16 v[28:31], v[160:163], v[64:67], v[28:31]\n"
"ds_read_b128 v[60:63], v3 offset:54272\n"
"v_mfma_f32_16x16x32_bf16 v[32:35], v[164:167], v[64:67], v[32:35]\n"
"buffer_load_dwordx4 v2, s[8:11], s31 offen offset:256 lds\n"
"v_mfma_f32_16x16x32_bf16 v[52:55], v[64:67], v[64:67], v[52:55]\n"
"v_mfma_f32_16x16x32_bf16 v[28:31], v[168:171], v[68:71], v[28:31]\n"
"ds_read_b128 v[64:67], v3 offset:55296\n"
"v_mfma_f32_16x16x32_bf16 v[32:35], v[172:175], v[68:71], v[32:35]\n"
"s_waitcnt lgkmcnt(3)\n"
"v_mfma_f32_16x16x32_bf16 v[28:31], v[176:179], v[72:75], v[28:31]\n"
"ds_read_b128 v[68:71], v3 offset:56320\n"
"v_mfma_f32_16x16x32_bf16 v[32:35], v[180:183], v[72:75], v[32:35]\n"
"v_mfma_f32_16x16x32_bf16 v[28:31], v[184:187], v[76:79], v[28:31]\n"
"ds_read_b128 v[72:75], v3 offset:57344\n"
"v_mfma_f32_16x16x32_bf16 v[32:35], v[188:191], v[76:79], v[32:35]\n"
"s_waitcnt lgkmcnt(3)\n"
"v_mfma_f32_16x16x32_bf16 v[28:31], v[192:195], v[56:59], v[28:31]\n"
"ds_read_b128 v[76:79], v4 offset:40960\n"
"v_mfma_f32_16x16x32_bf16 v[32:35], v[196:199], v[56:59], v[32:35]\n"
"v_mfma_f32_16x16x32_bf16 v[52:55], v[56:59], v[56:59], v[52:55]\n"
"v_mfma_f32_16x16x32_bf16 v[28:31], v[200:203], v[60:63], v[28:31]\n"
"ds_read_b128 v[56:59], v5 offset:40960\n"
"v_mfma_f32_16x16x32_bf16 v[32:35], v[204:207], v[60:63], v[32:35]\n"
"s_waitcnt lgkmcnt(3)\n"
"v_mfma_f32_16x16x32_bf16 v[28:31], v[208:211], v[64:67], v[28:31]\n"
"ds_read_b128 v[60:63], v6 offset:40960\n"
"v_mfma_f32_16x16x32_bf16 v[32:35], v[212:215], v[64:67], v[32:35]\n"
"v_mfma_f32_16x16x32_bf16 v[28:31], v[216:219], v[68:71], v[28:31]\n"
"v_add_u32 v3, 0x5000, v3\n"
"ds_read_b128 v[64:67], v3 offset:40960\n"
"v_mfma_f32_16x16x32_bf16 v[32:35], v[220:223], v[68:71], v[32:35]\n"
"s_waitcnt lgkmcnt(3)\n"
"v_mfma_f32_16x16x32_bf16 v[28:31], v[224:227], v[72:75], v[28:31]\n"
"ds_read_b128 v[68:71], v3 offset:41984\n"
"v_mfma_f32_16x16x32_bf16 v[32:35], v[228:231], v[72:75], v[32:35]\n"
"v_mfma_f32_16x16x32_bf16 v[52:55], v[72:75], v[72:75], v[52:55]\n"
"v_mfma_f32_16x16x32_bf16 v[28:31], v[232:235], v[76:79], v[28:31]\n"
"ds_read_b128 v[72:75], v3 offset:43008\n"
"v_mfma_f32_16x16x32_bf16 v[32:35], v[236:239], v[76:79], v[32:35]\n"
"s_waitcnt lgkmcnt(3)\n"
"v_mfma_f32_16x16x32_bf16 v[28:31], v[240:243], v[56:59], v[28:31]\n"
"ds_read_b128 v[76:79], v3 offset:44032\n"
"v_cndmask_b32 v94, v52, v53, s[34:35]\n"
"v_mfma_f32_16x16x32_bf16 v[32:35], v[244:247], v[56:59], v[32:35]\n"
"v_cndmask_b32 v54, v54, v55, s[34:35]\n"
"v_cndmask_b32 v94, v94, v54, s[20:21]\n"
"v_mfma_f32_16x16x32_bf16 v[28:31], v[248:251], v[60:63], v[28:31]\n"
"ds_read_b128 v[56:59], v3 offset:45056\n"
"v_mfma_f32_16x16x32_bf16 v[32:35], v[252:255], v[60:63], v[32:35]\n"
"s_waitcnt lgkmcnt(3)\n"
"v_mfma_f32_16x16x32_bf16 v[36:39], v[96:99], v[64:67], 0\n"
"ds_read_b128 v[60:63], v3 offset:46080\n"
"v_mfma_f32_16x16x32_bf16 v[40:43], v[100:103], v[64:67], 0\n"
"v_cndmask_b32 v94, v28, v94, s[22:23]\n"
"s_waitcnt vmcnt(7)\n"
"v_mfma_f32_16x16x32_bf16 v[52:55], v[64:67], v[64:67], 0\n"
"ds_write_b32 v9, v94 offset:0\n"
"v_mfma_f32_16x16x32_bf16 v[36:39], v[104:107], v[68:71], v[36:39]\n"
"ds_read_b128 v[64:67], v3 offset:47104\n"
"v_mfma_f32_16x16x32_bf16 v[40:43], v[108:111], v[68:71], v[40:43]\n"
"s_waitcnt lgkmcnt(4)\n"
"v_mfma_f32_16x16x32_bf16 v[36:39], v[112:115], v[72:75], v[36:39]\n"
"ds_read_b128 v[68:71], v3 offset:48128\n"
"v_mfma_f32_16x16x32_bf16 v[40:43], v[116:119], v[72:75], v[40:43]\n"
"v_mfma_f32_16x16x32_bf16 v[36:39], v[120:123], v[76:79], v[36:39]\n"
"ds_read_b128 v[72:75], v3 offset:49152\n"
"v_mfma_f32_16x16x32_bf16 v[40:43], v[124:127], v[76:79], v[40:43]\n"
"s_waitcnt lgkmcnt(4)\n"
"v_mfma_f32_16x16x32_bf16 v[36:39], v[128:131], v[56:59], v[36:39]\n"
"ds_read_b128 v[76:79], v3 offset:50176\n"
"v_mfma_f32_16x16x32_bf16 v[40:43], v[132:135], v[56:59], v[40:43]\n"
"v_mfma_f32_16x16x32_bf16 v[52:55], v[56:59], v[56:59], v[52:55]\n"
"v_mfma_f32_16x16x32_bf16 v[36:39], v[136:139], v[60:63], v[36:39]\n"
"ds_read_b128 v[56:59], v3 offset:51200\n"
"v_mfma_f32_16x16x32_bf16 v[40:43], v[140:143], v[60:63], v[40:43]\n"
"s_waitcnt lgkmcnt(3)\n"
"v_mfma_f32_16x16x32_bf16 v[36:39], v[144:147], v[64:67], v[36:39]\n"
"ds_read_b128 v[60:63], v3 offset:52224\n"
"ds_read_b128 v[84:87], v11 offset:0\n"
"ds_read_b32 v91, v12 offset:0\n"
"v_mfma_f32_16x16x32_bf16 v[40:43], v[148:151], v[64:67], v[40:43]\n"
"v_mfma_f32_16x16x32_bf16 v[36:39], v[152:155], v[68:71], v[36:39]\n"
"ds_read_b128 v[64:67], v3 offset:53248\n"
"v_mfma_f32_16x16x32_bf16 v[40:43], v[156:159], v[68:71], v[40:43]\n"
"s_waitcnt lgkmcnt(5)\n"
"v_mfma_f32_16x16x32_bf16 v[36:39], v[160:163], v[72:75], v[36:39]\n"
"ds_read_b128 v[68:71], v3 offset:54272\n"
"v_mfma_f32_16x16x32_bf16 v[40:43], v[164:167], v[72:75], v[40:43]\n"
"v_mfma_f32_16x16x32_bf16 v[52:55], v[72:75], v[72:75], v[52:55]\n"
"v_mfma_f32_16x16x32_bf16 v[36:39], v[168:171], v[76:79], v[36:39]\n"
"ds_read_b128 v[72:75], v3 offset:55296\n"
"s_waitcnt lgkmcnt(4)\n"
"v_add_f32 v84, v84, v85\n"
"v_mfma_f32_16x16x32_bf16 v[40:43], v[172:175], v[76:79], v[40:43]\n"
"v_add_f32 v86, v86, v87\n"
"v_mfma_f32_16x16x32_bf16 v[36:39], v[176:179], v[56:59], v[36:39]\n"
"ds_read_b128 v[76:79], v3 offset:56320\n"
"v_add_f32 v84, v84, v86\n"
"v_mfma_f32_16x16x32_bf16 v[40:43], v[180:183], v[56:59], v[40:43]\n"
"s_waitcnt lgkmcnt(4)\n"
"v_mul_f32 v89, 0x3acccccd, v91\n"
"v_fma_f32 v84, -v91, v89, v84\n"
"v_mfma_f32_16x16x32_bf16 v[36:39], v[184:187], v[60:63], v[36:39]\n"
"ds_read_b128 v[56:59], v3 offset:57344\n"
"v_fmamk_f32 v84, v84, 0x3acccccd, v27\n"
"v_mfma_f32_16x16x32_bf16 v[40:43], v[188:191], v[60:63], v[40:43]\n"
"v_cmp_u_f32 vcc, v84, v84\n"
"s_waitcnt lgkmcnt(3)\n"
"v_mfma_f32_16x16x32_bf16 v[36:39], v[192:195], v[64:67], v[36:39]\n"
"ds_read_b128 v[60:63], v4 offset:61440\n"
"v_max_f32 v84, v84, v27\n"
"v_mfma_f32_16x16x32_bf16 v[40:43], v[196:199], v[64:67], v[40:43]\n"
"v_rsq_f32 v88, v84\n"
"v_mfma_f32_16x16x32_bf16 v[52:55], v[64:67], v[64:67], v[52:55]\n"
"v_mul_f32 v89, -v89, v88\n"
"v_mfma_f32_16x16x32_bf16 v[36:39], v[200:203], v[68:71], v[36:39]\n"
"ds_read_b128 v[64:67], v5 offset:61440\n"
"s_cbranch_vccnz .Lsl_38\n"
".Lbk_39:\n"
"v_mfma_f32_16x16x32_bf16 v[40:43], v[204:207], v[68:71], v[40:43]\n"
"s_add_u32 s31, s28, 0x13b000\n"
"s_waitcnt lgkmcnt(3)\n"
"v_mfma_f32_16x16x32_bf16 v[36:39], v[208:211], v[72:75], v[36:39]\n"
"ds_read_b128 v[68:71], v6 offset:61440\n"
"s_add_u32 m0, s29, 0xa000\n"
"v_mfma_f32_16x16x32_bf16 v[40:43], v[212:215], v[72:75], v[40:43]\n"
"v_mul_f32 v28, v88, v28\n"
"v_mul_f32 v29, v88, v29\n"
"v_mfma_f32_16x16x32_bf16 v[36:39], v[216:219], v[76:79], v[36:39]\n"
"v_add_u32 v3, 0xffffb000, v3\n"
"ds_read_b128 v[72:75], v3 offset:0\n"
"buffer_load_dwordx4 v2, s[8:11], s31 offen offset:0 lds\n"
"v_mfma_f32_16x16x32_bf16 v[40:43], v[220:223], v[76:79], v[40:43]\n"
"s_add_u32 m0, s29, 0xa3c0\n"
"s_waitcnt lgkmcnt(3)\n"
"v_mfma_f32_16x16x32_bf16 v[36:39], v[224:227], v[56:59], v[36:39]\n"
"ds_read_b128 v[76:79], v3 offset:1024\n"
"v_mul_f32 v30, v88, v30\n"
"v_mfma_f32_16x16x32_bf16 v[40:43], v[228:231], v[56:59], v[40:43]\n"
"v_mul_f32 v31, v88, v31\n"
"v_fmac_f32 v28, v89, v16\n"
"v_mfma_f32_16x16x32_bf16 v[52:55], v[56:59], v[56:59], v[52:55]\n"
"buffer_load_dwordx4 v2, s[8:11], s31 offen offset:64 lds\n"
"v_fmac_f32 v29, v89, v17\n"
"v_mfma_f32_16x16x32_bf16 v[36:39], v[232:235], v[60:63], v[36:39]\n"
"ds_read_b128 v[56:59], v3 offset:2048\n"
"s_add_u32 m0, s29, 0xa780\n"
"v_mfma_f32_16x16x32_bf16 v[40:43], v[236:239], v[60:63], v[40:43]\n"
"v_fmac_f32 v30, v89, v18\n"
"s_waitcnt lgkmcnt(3)\n"
"v_mfma_f32_16x16x32_bf16 v[36:39], v[240:243], v[64:67], v[36:39]\n"
"ds_read_b128 v[60:63], v3 offset:3072\n"
"v_cndmask_b32 v94, v52, v53, s[34:35]\n"
"v_mfma_f32_16x16x32_bf16 v[40:43], v[244:247], v[64:67], v[40:43]\n"
"v_cndmask_b32 v54, v54, v55, s[34:35]\n"
"v_cndmask_b32 v94, v94, v54, s[20:21]\n"
"buffer_load_dwordx4 v2, s[8:11], s31 offen offset:128 lds\n"
"v_mfma_f32_16x16x32_bf16 v[36:39], v[248:251], v[68:71], v[36:39]\n"
"ds_read_b128 v[64:67], v3 offset:4096\n"
"s_add_u32 m0, s29, 0xab40\n"
"v_mfma_f32_16x16x32_bf16 v[40:43], v[252:255], v[68:71], v[40:43]\n"
"v_fmac_f32 v31, v89, v19\n"
"s_waitcnt lgkmcnt(3)\n"
"v_mfma_f32_16x16x32_bf16 v[44:47], v[96:99], v[72:75], 0\n"
"ds_read_b128 v[68:71], v3 offset:5120\n"
"v_cvt_pk_bf16_f32 v28, v28, v29\n"
"v_mfma_f32_16x16x32_bf16 v[48:51], v[100:103], v[72:75], 0\n"
"v_cndmask_b32 v94, v36, v94, s[22:23]\n"
"s_waitcnt vmcnt(3)\n"
"buffer_load_dwordx4 v2, s[8:11], s31 offen offset:192 lds\n"
"v_mfma_f32_16x16x32_bf16 v[52:55], v[72:75], v[72:75], 0\n"
"ds_write_b32 v9, v94 offset:320\n"
"v_mfma_f32_16x16x32_bf16 v[44:47], v[104:107], v[76:79], v[44:47]\n"
"ds_read_b128 v[72:75], v3 offset:6144\n"
"s_add_u32 m0, s29, 0xaf00\n"
"v_mfma_f32_16x16x32_bf16 v[48:51], v[108:111], v[76:79], v[48:51]\n"
"v_cvt_pk_bf16_f32 v29, v30, v31\n"
"s_waitcnt lgkmcnt(4)\n"
"v_mfma_f32_16x16x32_bf16 v[44:47], v[112:115], v[56:59], v[44:47]\n"
"ds_read_b128 v[76:79], v3 offset:7168\n"
"buffer_load_dwordx4 v2, s[8:11], s31 offen offset:256 lds\n"
"v_mfma_f32_16x16x32_bf16 v[48:51], v[116:119], v[56:59], v[48:51]\n"
"v_mul_f32 v32, v88, v32\n"
"v_mul_f32 v33, v88, v33\n"
"v_mfma_f32_16x16x32_bf16 v[44:47], v[120:123], v[60:63], v[44:47]\n"
"ds_read_b128 v[56:59], v3 offset:8192\n"
"v_mul_f32 v34, v88, v34\n"
"v_mfma_f32_16x16x32_bf16 v[48:51], v[124:127], v[60:63], v[48:51]\n"
"v_mul_f32 v35, v88, v35\n"
"s_waitcnt lgkmcnt(4)\n"
"v_mfma_f32_16x16x32_bf16 v[44:47], v[128:131], v[64:67], v[44:47]\n"
"ds_read_b128 v[60:63], v3 offset:9216\n"
"v_fmac_f32 v32, v89, v20\n"
"v_mfma_f32_16x16x32_bf16 v[48:51], v[132:135], v[64:67], v[48:51]\n"
"v_fmac_f32 v33, v89, v21\n"
"v_fmac_f32 v34, v89, v22\n"
"v_mfma_f32_16x16x32_bf16 v[52:55], v[64:67], v[64:67], v[52:55]\n"
"v_fmac_f32 v35, v89, v23\n"
"v_cvt_pk_bf16_f32 v32, v32, v33\n"
"v_mfma_f32_16x16x32_bf16 v[44:47], v[136:139], v[68:71], v[44:47]\n"
"ds_read_b128 v[64:67], v3 offset:10240\n"
"v_cvt_pk_bf16_f32 v33, v34, v35\n"
"v_mfma_f32_16x16x32_bf16 v[48:51], v[140:143], v[68:71], v[48:51]\n"
"s_waitcnt lgkmcnt(3)\n"
"v_mfma_f32_16x16x32_bf16 v[44:47], v[144:147], v[72:75], v[44:47]\n"
"ds_read_b128 v[68:71], v3 offset:11264\n"
"ds_read_b128 v[84:87], v11 offset:320\n"
"ds_read_b32 v91, v12 offset:320\n"
"v_mfma_f32_16x16x32_bf16 v[48:51], v[148:151], v[72:75], v[48:51]\n"
"v_mfma_f32_16x16x32_bf16 v[44:47], v[152:155], v[76:79], v[44:47]\n"
"ds_read_b128 v[72:75], v3 offset:12288\n"
"v_mfma_f32_16x16x32_bf16 v[48:51], v[156:159], v[76:79], v[48:51]\n"
"s_waitcnt lgkmcnt(5)\n"
"v_mfma_f32_16x16x32_bf16 v[44:47], v[160:163], v[56:59], v[44:47]\n"
"ds_read_b128 v[76:79], v3 offset:13312\n"
"v_mfma_f32_16x16x32_bf16 v[48:51], v[164:167], v[56:59], v[48:51]\n"
"v_mfma_f32_16x16x32_bf16 v[52:55], v[56:59], v[56:59], v[52:55]\n"
"v_mfma_f32_16x16x32_bf16 v[44:47], v[168:171], v[60:63], v[44:47]\n"
"ds_read_b128 v[56:59], v3 offset:14336\n"
"s_waitcnt lgkmcnt(4)\n"
"v_add_f32 v84, v84, v85\n"
"v_mfma_f32_16x16x32_bf16 v[48:51], v[172:175], v[60:63], v[48:51]\n"
"v_add_f32 v86, v86, v87\n"
"v_mfma_f32_16x16x32_bf16 v[44:47], v[176:179], v[64:67], v[44:47]\n"
"ds_read_b128 v[60:63], v3 offset:15360\n"
"v_add_f32 v84, v84, v86\n"
"v_mfma_f32_16x16x32_bf16 v[48:51], v[180:183], v[64:67], v[48:51]\n"
"s_waitcnt lgkmcnt(4)\n"
"v_mul_f32 v89, 0x3acccccd, v91\n"
"v_fma_f32 v84, -v91, v89, v84\n"
"v_mfma_f32_16x16x32_bf16 v[44:47], v[184:187], v[68:71], v[44:47]\n"
"ds_read_b128 v[64:67], v3 offset:16384\n"
"v_fmamk_f32 v84, v84, 0x3acccccd, v27\n"
"v_mfma_f32_16x16x32_bf16 v[48:51], v[188:191], v[68:71], v[48:51]\n"
"v_cmp_u_f32 vcc, v84, v84\n"
"s_waitcnt lgkmcnt(3)\n"
"v_mfma_f32_16x16x32_bf16 v[44:47], v[192:195], v[72:75], v[44:47]\n"
"ds_read_b128 v[68:71], v4 offset:0\n"
"v_max_f32 v84, v84, v27\n"
"v_mfma_f32_16x16x32_bf16 v[48:51], v[196:199], v[72:75], v[48:51]\n"
"v_rsq_f32 v88, v84\n"
"v_mfma_f32_16x16x32_bf16 v[52:55], v[72:75], v[72:75], v[52:55]\n"
"v_mul_f32 v89, -v89, v88\n"
"v_mfma_f32_16x16x32_bf16 v[44:47], v[200:203], v[76:79], v[44:47]\n"
"ds_read_b128 v[72:75], v5 offset:0\n"
"s_cbranch_vccnz .Lsl_42\n"
".Lbk_43:\n"
"v_mfma_f32_16x16x32_bf16 v[48:51], v[204:207], v[76:79], v[48:51]\n"
"ds_write_b64 v7, v[28:29] offset:44928\n"
"s_waitcnt lgkmcnt(4)\n"
"v_mfma_f32_16x16x32_bf16 v[44:47], v[208:211], v[56:59], v[44:47]\n"
"ds_read_b128 v[76:79], v6 offset:0\n"
"v_mul_f32 v36, v88, v36\n"
"v_mfma_f32_16x16x32_bf16 v[48:51], v[212:215], v[56:59], v[48:51]\n"
"v_mul_f32 v37, v88, v37\n"
"v_mul_f32 v38, v88, v38\n"
"v_mfma_f32_16x16x32_bf16 v[44:47], v[216:219], v[60:63], v[44:47]\n"
"ds_read_b128 v[56:59], v3 offset:20480\n"
"v_mul_f32 v39, v88, v39\n"
"v_mfma_f32_16x16x32_bf16 v[48:51], v[220:223], v[60:63], v[48:51]\n"
"v_fmac_f32 v36, v89, v16\n"
"s_waitcnt lgkmcnt(4)\n"
"v_mfma_f32_16x16x32_bf16 v[44:47], v[224:227], v[64:67], v[44:47]\n"
"ds_read_b128 v[60:63], v3 offset:21504\n"
"v_fmac_f32 v37, v89, v17\n"
"v_mfma_f32_16x16x32_bf16 v[48:51], v[228:231], v[64:67], v[48:51]\n"
"v_mov_b32 v84, 0x3f803f80\n"
"v_mov_b32 v85, 0x3f803f80\n"
"v_mfma_f32_16x16x32_bf16 v[52:55], v[64:67], v[64:67], v[52:55]\n"
"v_mov_b32 v86, 0x3f803f80\n"
"v_mov_b32 v87, 0x3f803f80\n"
"ds_write_b64 v8, v[32:33] offset:44928\n"
"v_mfma_f32_16x16x32_bf16 v[44:47], v[232:235], v[68:71], v[44:47]\n"
"ds_read_b128 v[64:67], v3 offset:22528\n"
"v_fmac_f32 v38, v89, v18\n"
"v_mfma_f32_16x16x32_bf16 v[48:51], v[236:239], v[68:71], v[48:51]\n"
"v_fmac_f32 v39, v89, v19\n"
"s_waitcnt lgkmcnt(4)\n"
"v_mfma_f32_16x16x32_bf16 v[44:47], v[240:243], v[72:75], v[44:47]\n"
"ds_read_b128 v[68:71], v3 offset:23552\n"
"v_cndmask_b32 v94, v52, v53, s[34:35]\n"
"v_mfma_f32_16x16x32_bf16 v[48:51], v[244:247], v[72:75], v[48:51]\n"
"v_cndmask_b32 v54, v54, v55, s[34:35]\n"
"v_cndmask_b32 v94, v94, v54, s[20:21]\n"
"v_mfma_f32_16x16x32_bf16 v[44:47], v[248:251], v[76:79], v[44:47]\n"
"ds_read_b128 v[72:75], v3 offset:24576\n"
"v_cvt_pk_bf16_f32 v36, v36, v37\n"
"v_mfma_f32_16x16x32_bf16 v[48:51], v[252:255], v[76:79], v[48:51]\n"
"v_cvt_pk_bf16_f32 v37, v38, v39\n"
"s_waitcnt lgkmcnt(4)\n"
"v_mfma_f32_16x16x32_bf16 v[28:31], v[96:99], v[56:59], 0\n"
"ds_read_b128 v[76:79], v3 offset:25600\n"
"v_mul_f32 v40, v88, v40\n"
"v_mfma_f32_16x16x32_bf16 v[32:35], v[100:103], v[56:59], 0\n"
"v_cndmask_b32 v94, v44, v94, s[22:23]\n"
"s_waitcnt vmcnt(0)\n"
"ds_write_b128 v93, v[84:87] offset:40960\n"
"v_mfma_f32_16x16x32_bf16 v[52:55], v[56:59], v[56:59], 0\n"
"ds_write_b32 v9, v94 offset:640\n"
"v_mfma_f32_16x16x32_bf16 v[28:31], v[104:107], v[60:63], v[28:31]\n"
"ds_read_b128 v[56:59], v3 offset:26624\n"
"v_mul_f32 v41, v88, v41\n"
"v_mfma_f32_16x16x32_bf16 v[32:35], v[108:111], v[60:63], v[32:35]\n"
"v_mul_f32 v42, v88, v42\n"
"s_waitcnt lgkmcnt(5)\n"
"v_mfma_f32_16x16x32_bf16 v[28:31], v[112:115], v[64:67], v[28:31]\n"
"ds_read_b128 v[60:63], v3 offset:27648\n"
"v_mul_f32 v43, v88, v43\n"
"v_mfma_f32_16x16x32_bf16 v[32:35], v[116:119], v[64:67], v[32:35]\n"
"v_fmac_f32 v40, v89, v20\n"
"v_fmac_f32 v41, v89, v21\n"
"v_mfma_f32_16x16x32_bf16 v[28:31], v[120:123], v[68:71], v[28:31]\n"
"ds_read_b128 v[64:67], v3 offset:28672\n"
"v_fmac_f32 v42, v89, v22\n"
"v_mfma_f32_16x16x32_bf16 v[32:35], v[124:127], v[68:71], v[32:35]\n"
"v_fmac_f32 v43, v89, v23\n"
"s_waitcnt lgkmcnt(5)\n"
"v_mfma_f32_16x16x32_bf16 v[28:31], v[128:131], v[72:75], v[28:31]\n"
"ds_read_b128 v[68:71], v3 offset:29696\n"
"v_cvt_pk_bf16_f32 v40, v40, v41\n"
"v_mfma_f32_16x16x32_bf16 v[32:35], v[132:135], v[72:75], v[32:35]\n"
"v_cvt_pk_bf16_f32 v41, v42, v43\n"
"v_mfma_f32_16x16x32_bf16 v[52:55], v[72:75], v[72:75], v[52:55]\n"
"v_mfma_f32_16x16x32_bf16 v[28:31], v[136:139], v[76:79], v[28:31]\n"
"ds_read_b128 v[72:75], v3 offset:30720\n"
"v_mfma_f32_16x16x32_bf16 v[32:35], v[140:143], v[76:79], v[32:35]\n"
"s_waitcnt lgkmcnt(3)\n"
"v_mfma_f32_16x16x32_bf16 v[28:31], v[144:147], v[56:59], v[28:31]\n"
"ds_read_b128 v[76:79], v3 offset:31744\n"
"ds_read_b128 v[84:87], v11 offset:640\n"
"ds_read_b32 v91, v12 offset:640\n"
"v_mfma_f32_16x16x32_bf16 v[32:35], v[148:151], v[56:59], v[32:35]\n"
"v_mfma_f32_16x16x32_bf16 v[28:31], v[152:155], v[60:63], v[28:31]\n"
"ds_read_b128 v[56:59], v3 offset:32768\n"
"v_mfma_f32_16x16x32_bf16 v[32:35], v[156:159], v[60:63], v[32:35]\n"
"s_waitcnt lgkmcnt(5)\n"
"v_mfma_f32_16x16x32_bf16 v[28:31], v[160:163], v[64:67], v[28:31]\n"
"ds_read_b128 v[60:63], v3 offset:33792\n"
"v_mfma_f32_16x16x32_bf16 v[32:35], v[164:167], v[64:67], v[32:35]\n"
"v_mfma_f32_16x16x32_bf16 v[52:55], v[64:67], v[64:67], v[52:55]\n"
"v_mfma_f32_16x16x32_bf16 v[28:31], v[168:171], v[68:71], v[28:31]\n"
"ds_read_b128 v[64:67], v3 offset:34816\n"
"s_waitcnt lgkmcnt(4)\n"
"v_add_f32 v84, v84, v85\n"
"v_mfma_f32_16x16x32_bf16 v[32:35], v[172:175], v[68:71], v[32:35]\n"
"v_add_f32 v86, v86, v87\n"
"v_mfma_f32_16x16x32_bf16 v[28:31], v[176:179], v[72:75], v[28:31]\n"
"ds_read_b128 v[68:71], v3 offset:35840\n"
"v_add_f32 v84, v84, v86\n"
"v_mfma_f32_16x16x32_bf16 v[32:35], v[180:183], v[72:75], v[32:35]\n"
"s_waitcnt lgkmcnt(4)\n"
"v_mul_f32 v89, 0x3acccccd, v91\n"
"v_fma_f32 v84, -v91, v89, v84\n"
"v_mfma_f32_16x16x32_bf16 v[28:31], v[184:187], v[76:79], v[28:31]\n"
"ds_read_b128 v[72:75], v3 offset:36864\n"
"v_fmamk_f32 v84, v84, 0x3acccccd, v27\n"
"v_mfma_f32_16x16x32_bf16 v[32:35], v[188:191], v[76:79], v[32:35]\n"
"v_cmp_u_f32 vcc, v84, v84\n"
"s_waitcnt lgkmcnt(3)\n"
"v_mfma_f32_16x16x32_bf16 v[28:31], v[192:195], v[56:59], v[28:31]\n"
"ds_read_b128 v[76:79], v4 offset:20480\n"
"v_max_f32 v84, v84, v27\n"
"v_mfma_f32_16x16x32_bf16 v[32:35], v[196:199], v[56:59], v[32:35]\n"
"v_rsq_f32 v88, v84\n"
"v_mfma_f32_16x16x32_bf16 v[52:55], v[56:59], v[56:59], v[52:55]\n"
"v_mul_f32 v89, -v89, v88\n"
"v_mfma_f32_16x16x32_bf16 v[28:31], v[200:203], v[60:63], v[28:31]\n"
"ds_read_b128 v[56:59], v5 offset:20480\n"
"s_cbranch_vccnz .Lsl_46\n"
".Lbk_47:\n"
"v_mfma_f32_16x16x32_bf16 v[32:35], v[204:207], v[60:63], v[32:35]\n"
"ds_write_b64 v7, v[36:37] offset:48896\n"
"s_waitcnt lgkmcnt(4)\n"
"v_mfma_f32_16x16x32_bf16 v[28:31], v[208:211], v[64:67], v[28:31]\n"
"ds_read_b128 v[60:63], v6 offset:20480\n"
"v_mul_f32 v44, v88, v44\n"
"v_mul_f32 v45, v88, v45\n"
"v_mfma_f32_16x16x32_bf16 v[32:35], v[212:215], v[64:67], v[32:35]\n"
"v_mul_f32 v46, v88, v46\n"
"v_mul_f32 v47, v88, v47\n"
"v_fmac_f32 v44, v89, v16\n"
"v_mfma_f32_16x16x32_bf16 v[28:31], v[216:219], v[68:71], v[28:31]\n"
"ds_read_b128 v[64:67], v3 offset:40960\n"
"v_fmac_f32 v45, v89, v17\n"
"v_fmac_f32 v46, v89, v18\n"
"v_mfma_f32_16x16x32_bf16 v[32:35], v[220:223], v[68:71], v[32:35]\n"
"v_fmac_f32 v47, v89, v19\n"
"s_waitcnt lgkmcnt(4)\n"
"v_mfma_f32_16x16x32_bf16 v[28:31], v[224:227], v[72:75], v[28:31]\n"
"ds_read_b128 v[68:71], v3 offset:45056\n"
"v_cvt_pk_bf16_f32 v44, v44, v45\n"
"v_mfma_f32_16x16x32_bf16 v[32:35], v[228:231], v[72:75], v[32:35]\n"
"v_cvt_pk_bf16_f32 v45, v46, v47\n"
"v_mul_f32 v48, v88, v48\n"
"v_mfma_f32_16x16x32_bf16 v[52:55], v[72:75], v[72:75], v[52:55]\n"
"ds_write_b64 v8, v[40:41] offset:48896\n"
"v_mfma_f32_16x16x32_bf16 v[28:31], v[232:235], v[76:79], v[28:31]\n"
"ds_read_b128 v[72:75], v3 offset:49152\n"
"v_mul_f32 v49, v88, v49\n"
"v_mfma_f32_16x16x32_bf16 v[32:35], v[236:239], v[76:79], v[32:35]\n"
"v_mul_f32 v50, v88, v50\n"
"s_waitcnt lgkmcnt(4)\n"
"v_mfma_f32_16x16x32_bf16 v[28:31], v[240:243], v[56:59], v[28:31]\n"
"ds_read_b128 v[76:79], v3 offset:53248\n"
"v_cndmask_b32 v94, v52, v53, s[34:35]\n"
"v_mfma_f32_16x16x32_bf16 v[32:35], v[244:247], v[56:59], v[32:35]\n"
"v_cndmask_b32 v54, v54, v55, s[34:35]\n"
"v_cndmask_b32 v94, v94, v54, s[20:21]\n"
"v_mfma_f32_16x16x32_bf16 v[28:31], v[248:251], v[60:63], v[28:31]\n"
"ds_read_b128 v[56:59], v3 offset:57344\n"
"v_mul_f32 v51, v88, v51\n"
"v_mfma_f32_16x16x32_bf16 v[32:35], v[252:255], v[60:63], v[32:35]\n"
"v_fmac_f32 v48, v89, v20\n"
"s_waitcnt lgkmcnt(4)\n"
"v_mfma_f32_16x16x32_bf16 v[36:39], v[96:99], v[64:67], 0\n"
"ds_read_b128 v[60:63], v3 offset:41984\n"
"v_fmac_f32 v49, v89, v21\n"
"v_mfma_f32_16x16x32_bf16 v[40:43], v[100:103], v[64:67], 0\n"
"v_cndmask_b32 v94, v28, v94, s[22:23]\n"
"v_fmac_f32 v50, v89, v22\n"
"v_mfma_f32_16x16x32_bf16 v[52:55], v[64:67], v[64:67], 0\n"
"ds_write_b32 v9, v94 offset:960\n"
"v_mfma_f32_16x16x32_bf16 v[36:39], v[128:131], v[68:71], v[36:39]\n"
"ds_read_b128 v[64:67], v3 offset:43008\n"
"v_fmac_f32 v51, v89, v23\n"
"v_mfma_f32_16x16x32_bf16 v[40:43], v[132:135], v[68:71], v[40:43]\n"
"v_cvt_pk_bf16_f32 v48, v48, v49\n"
"v_cvt_pk_bf16_f32 v49, v50, v51\n"
"v_mfma_f32_16x16x32_bf16 v[52:55], v[68:71], v[68:71], v[52:55]\n"
"ds_write_b64 v7, v[44:45] offset:52864\n"
"s_waitcnt lgkmcnt(5)\n"
"v_mfma_f32_16x16x32_bf16 v[36:39], v[160:163], v[72:75], v[36:39]\n"
"ds_read_b128 v[68:71], v3 offset:44032\n"
"v_mfma_f32_16x16x32_bf16 v[40:43], v[164:167], v[72:75], v[40:43]\n"
"v_mfma_f32_16x16x32_bf16 v[52:55], v[72:75], v[72:75], v[52:55]\n"
"ds_write_b64 v8, v[48:49] offset:52864\n"
"v_mfma_f32_16x16x32_bf16 v[36:39], v[192:195], v[76:79], v[36:39]\n"
"ds_read_b128 v[72:75], v3 offset:46080\n"
"v_mfma_f32_16x16x32_bf16 v[40:43], v[196:199], v[76:79], v[40:43]\n"
"v_mfma_f32_16x16x32_bf16 v[52:55], v[76:79], v[76:79], v[52:55]\n"
"ds_read_b128 v[84:87], v11 offset:960\n"
"ds_read_b32 v91, v12 offset:960\n"
"s_waitcnt lgkmcnt(8)\n"
"v_mfma_f32_16x16x32_bf16 v[36:39], v[224:227], v[56:59], v[36:39]\n"
"ds_read_b128 v[76:79], v3 offset:47104\n"
"v_mfma_f32_16x16x32_bf16 v[40:43], v[228:231], v[56:59], v[40:43]\n"
"v_mfma_f32_16x16x32_bf16 v[52:55], v[56:59], v[56:59], v[52:55]\n"
"v_mfma_f32_16x16x32_bf16 v[36:39], v[104:107], v[60:63], v[36:39]\n"
"ds_read_b128 v[56:59], v3 offset:48128\n"
"v_mfma_f32_16x16x32_bf16 v[40:43], v[108:111], v[60:63], v[40:43]\n"
"s_waitcnt lgkmcnt(6)\n"
"v_mfma_f32_16x16x32_bf16 v[36:39], v[112:115], v[64:67], v[36:39]\n"
"ds_read_b128 v[60:63], v3 offset:50176\n"
"ds_write_b32 v10, v52 offset:1600\n"
"v_cndmask_b32 v94, v52, v53, s[34:35]\n"
"v_cndmask_b32 v54, v54, v55, s[34:35]\n"
"v_mfma_f32_16x16x32_bf16 v[40:43], v[116:119], v[64:67], v[40:43]\n"
"s_waitcnt lgkmcnt(5)\n"
"v_add_f32 v84, v84, v85\n"
"v_add_f32 v86, v86, v87\n"
"v_add_f32 v84, v84, v86\n"
"v_cndmask_b32 v94, v94, v54, s[20:21]\n"
"ds_write_b32 v9, v94 offset:1280\n"
"v_mfma_f32_16x16x32_bf16 v[36:39], v[120:123], v[68:71], v[36:39]\n"
"ds_read_b128 v[64:67], v3 offset:51200\n"
"s_waitcnt lgkmcnt(6)\n"
"v_mul_f32 v89, 0x3acccccd, v91\n"
"v_fma_f32 v84, -v91, v89, v84\n"
"v_mfma_f32_16x16x32_bf16 v[40:43], v[124:127], v[68:71], v[40:43]\n"
"v_fmamk_f32 v84, v84, 0x3acccccd, v27\n"
"s_waitcnt lgkmcnt(5)\n"
"v_mfma_f32_16x16x32_bf16 v[36:39], v[136:139], v[72:75], v[36:39]\n"
"ds_read_b128 v[68:71], v3 offset:52224\n"
"v_cmp_u_f32 vcc, v84, v84\n"
"v_mfma_f32_16x16x32_bf16 v[40:43], v[140:143], v[72:75], v[40:43]\n"
"v_max_f32 v84, v84, v27\n"
"v_rsq_f32 v88, v84\n"
"v_mfma_f32_16x16x32_bf16 v[36:39], v[144:147], v[76:79], v[36:39]\n"
"ds_read_b128 v[72:75], v3 offset:54272\n"
"v_mul_f32 v89, -v89, v88\n"
"v_mfma_f32_16x16x32_bf16 v[40:43], v[148:151], v[76:79], v[40:43]\n"
"s_cbranch_vccnz .Lsl_50\n"
".Lbk_51:\n"
"s_waitcnt lgkmcnt(5)\n"
"v_mfma_f32_16x16x32_bf16 v[36:39], v[152:155], v[56:59], v[36:39]\n"
"ds_read_b128 v[76:79], v3 offset:55296\n"
"v_mul_f32 v28, v88, v28\n"
"v_mul_f32 v29, v88, v29\n"
"v_mfma_f32_16x16x32_bf16 v[40:43], v[156:159], v[56:59], v[40:43]\n"
"ds_read_b128 v[44:47], v11 offset:1280\n"
"ds_read_b128 v[48:51], v11 offset:1600\n"
"v_mul_f32 v30, v88, v30\n"
"v_mfma_f32_16x16x32_bf16 v[36:39], v[168:171], v[60:63], v[36:39]\n"
"ds_read_b128 v[56:59], v3 offset:56320\n"
"v_mul_f32 v31, v88, v31\n"
"v_fmac_f32 v28, v89, v16\n"
"v_mfma_f32_16x16x32_bf16 v[40:43], v[172:175], v[60:63], v[40:43]\n"
"v_fmac_f32 v29, v89, v17\n"
"v_fmac_f32 v30, v89, v18\n"
"s_waitcnt lgkmcnt(5)\n"
"v_mfma_f32_16x16x32_bf16 v[36:39], v[176:179], v[64:67], v[36:39]\n"
"ds_read_b128 v[60:63], v4 offset:40960\n"
"v_fmac_f32 v31, v89, v19\n"
"v_cvt_pk_bf16_f32 v28, v28, v29\n"
"v_mfma_f32_16x16x32_bf16 v[40:43], v[180:183], v[64:67], v[40:43]\n"
"v_cvt_pk_bf16_f32 v29, v30, v31\n"
"ds_write_b64 v7, v[28:29] offset:56832\n"
"v_mfma_f32_16x16x32_bf16 v[36:39], v[184:187], v[68:71], v[36:39]\n"
"ds_read_b128 v[64:67], v5 offset:40960\n"
"v_mul_f32 v32, v88, v32\n"
"v_mul_f32 v33, v88, v33\n"
"v_mfma_f32_16x16x32_bf16 v[40:43], v[188:191], v[68:71], v[40:43]\n"
"v_mul_f32 v34, v88, v34\n"
"v_mul_f32 v35, v88, v35\n"
"s_waitcnt lgkmcnt(6)\n"
"v_mfma_f32_16x16x32_bf16 v[36:39], v[200:203], v[72:75], v[36:39]\n"
"ds_read_b128 v[68:71], v6 offset:40960\n"
"s_waitcnt lgkmcnt(6)\n"
"v_add_f32 v44, v44, v45\n"
"v_fmac_f32 v32, v89, v20\n"
"v_mfma_f32_16x16x32_bf16 v[40:43], v[204:207], v[72:75], v[40:43]\n"
"v_add_f32 v46, v46, v47\n"
"s_waitcnt lgkmcnt(5)\n"
"v_add_f32 v48, v48, v49\n"
"v_fmac_f32 v33, v89, v21\n"
"v_mfma_f32_16x16x32_bf16 v[36:39], v[208:211], v[76:79], v[36:39]\n"
"v_add_f32 v50, v50, v51\n"
"v_add_f32 v48, v48, v50\n"
"v_fmac_f32 v34, v89, v22\n"
"v_mfma_f32_16x16x32_bf16 v[40:43], v[212:215], v[76:79], v[40:43]\n"
"v_add_f32 v44, v44, v46\n"
"v_fmac_f32 v35, v89, v23\n"
"s_waitcnt lgkmcnt(3)\n"
"v_mfma_f32_16x16x32_bf16 v[36:39], v[216:219], v[56:59], v[36:39]\n"
"v_mul_f32 v53, 0x3acccccd, v48\n"
"v_fma_f32 v44, -v48, v53, v44\n"
"v_cvt_pk_bf16_f32 v32, v32, v33\n"
"v_mfma_f32_16x16x32_bf16 v[40:43], v[220:223], v[56:59], v[40:43]\n"
"v_fmamk_f32 v44, v44, 0x3acccccd, v27\n"
"v_cmp_u_f32 vcc, v44, v44\n"
"v_mfma_f32_16x16x32_bf16 v[36:39], v[232:235], v[60:63], v[36:39]\n"
"v_max_f32 v44, v44, v27\n"
"v_rsq_f32 v52, v44\n"
"v_mfma_f32_16x16x32_bf16 v[40:43], v[236:239], v[60:63], v[40:43]\n"
"v_mul_f32 v53, -v53, v52\n"
"s_waitcnt lgkmcnt(0)\n"
"v_mfma_f32_16x16x32_bf16 v[36:39], v[240:243], v[64:67], v[36:39]\n"
"s_cbranch_vccnz .Lsl_54\n"
".Lbk_55:\n"
"v_mfma_f32_16x16x32_bf16 v[40:43], v[244:247], v[64:67], v[40:43]\n"
"v_cvt_pk_bf16_f32 v33, v34, v35\n"
"ds_write_b64 v8, v[32:33] offset:56832\n"
"v_mfma_f32_16x16x32_bf16 v[36:39], v[248:251], v[68:71], v[36:39]\n"
"v_mfma_f32_16x16x32_bf16 v[40:43], v[252:255], v[68:71], v[40:43]\n"
"s_nop 4\n"
"v_mul_f32 v36, v52, v36\n"
"v_mul_f32 v37, v52, v37\n"
"v_mul_f32 v38, v52, v38\n"
"v_mul_f32 v39, v52, v39\n"
"v_fmac_f32 v36, v53, v16\n"
"v_fmac_f32 v37, v53, v17\n"
"v_fmac_f32 v38, v53, v18\n"
"v_fmac_f32 v39, v53, v19\n"
"v_cvt_pk_bf16_f32 v36, v36, v37\n"
"v_cvt_pk_bf16_f32 v37, v38, v39\n"
"ds_write_b64 v7, v[36:37] offset:40960\n"
"v_mul_f32 v40, v52, v40\n"
"v_mul_f32 v41, v52, v41\n"
"v_mul_f32 v42, v52, v42\n"
"v_mul_f32 v43, v52, v43\n"
"v_fmac_f32 v40, v53, v20\n"
"v_fmac_f32 v41, v53, v21\n"
"v_fmac_f32 v42, v53, v22\n"
"v_fmac_f32 v43, v53, v23\n"
"v_cvt_pk_bf16_f32 v40, v40, v41\n"
"v_cvt_pk_bf16_f32 v41, v42, v43\n"
"ds_write_b64 v8, v[40:41] offset:40960\n"
"ds_write_b32 v13, v13 offset:144\n"
"v_mov_b32 v46, 0\n"
"v_mov_b32 v47, 0\n"
"v_mov_b32 v50, 0\n"
"v_mov_b32 v51, 0\n"
"ds_read_b32 v90, v14 offset:144\n"
"s_mov_b64 exec, 0xffff\n"
"ds_read_b64 v[46:47], v24 offset:44960\n"
"ds_read_b64 v[50:51], v24 offset:45000\n"
"s_mov_b64 exec, -1\n"
"ds_read_b64 v[44:45], v25 offset:44928\n"
"ds_read_b64 v[56:57], v25 offset:41040\n"
"ds_read_b64 v[58:59], v24 offset:41072\n"
"ds_read_b64 v[64:65], v25 offset:45008\n"
"ds_read_b64 v[66:67], v24 offset:45040\n"
"ds_read_b64 v[48:49], v25 offset:44968\n"
"ds_read_b64 v[60:61], v25 offset:41080\n"
"ds_read_b64 v[62:63], v24 offset:41112\n"
"ds_read_b64 v[68:69], v25 offset:45048\n"
"ds_read_b64 v[70:71], v24 offset:45080\n"
"s_mov_b32 s17, 0x100000\n"
"s_waitcnt lgkmcnt(12)\n"
"v_cmp_eq_u32 vcc, 0, v90\n"
"s_cbranch_vccz .Lok_59\n"
".Ltp_58:\n"
"ds_read_b32 v90, v14 offset:144\n"
"s_mov_b64 exec, 0xffff\n"
"ds_read_b64 v[46:47], v24 offset:44960\n"
"ds_read_b64 v[50:51], v24 offset:45000\n"
"s_mov_b64 exec, -1\n"
"ds_read_b64 v[44:45], v25 offset:44928\n"
"ds_read_b64 v[56:57], v25 offset:41040\n"
"ds_read_b64 v[58:59], v24 offset:41072\n"
"ds_read_b64 v[64:65], v25 offset:45008\n"
"ds_read_b64 v[66:67], v24 offset:45040\n"
"ds_read_b64 v[48:49], v25 offset:44968\n"
"ds_read_b64 v[60:61], v25 offset:41080\n"
"ds_read_b64 v[62:63], v24 offset:41112\n"
"ds_read_b64 v[68:69], v25 offset:45048\n"
"ds_read_b64 v[70:71], v24 offset:45080\n"
"s_sub_u32 s17, s17, 1\n"
"s_cbranch_scc1 .Lok_59\n"
"s_waitcnt lgkmcnt(12)\n"
"v_cmp_eq_u32 vcc, 0, v90\n"
"s_cbranch_vccnz .Ltp_58\n"
"s_waitcnt lgkmcnt(0)\n"
".Lok_59:\n"
"ds_read_b64_tr_b16 v[28:29], v26 offset:40960\n"
"ds_read_b64_tr_b16 v[30:31], v26 offset:44928\n"
"ds_read_b64_tr_b16 v[32:33], v26 offset:41000\n"
"s_waitcnt lgkmcnt(10)\n"
"v_mfma_f32_16x16x32_bf16 v[56:59], v[56:59], v[44:47], 0\n"
"s_waitcnt lgkmcnt(8)\n"
"v_mfma_f32_16x16x32_bf16 v[64:67], v[64:67], v[44:47], 0\n"
"ds_read_b64_tr_b16 v[34:35], v26 offset:44968\n"
"ds_read_b64_tr_b16 v[36:37], v26 offset:40992\n"
"ds_read_b64_tr_b16 v[38:39], v26 offset:44960\n"
"s_waitcnt lgkmcnt(8)\n"
"v_mfma_f32_16x16x32_bf16 v[60:63], v[60:63], v[48:51], 0\n"
"s_waitcnt lgkmcnt(6)\n"
"v_mfma_f32_16x16x32_bf16 v[68:71], v[68:71], v[48:51], 0\n"
"ds_read_b64_tr_b16 v[40:41], v26 offset:41032\n"
"ds_read_b64_tr_b16 v[42:43], v26 offset:45000\n"
"v_mov_b32 v84, 0x3f803f80\n"
"v_mov_b32 v85, 0x3f803f80\n"
"v_mov_b32 v86, 0x3f803f80\n"
"v_mov_b32 v87, 0x3f803f80\n"
"v_cndmask_b32 v76, v56, v57, s[34:35]\n"
"v_cndmask_b32 v72, v64, v65, s[34:35]\n"
"v_cndmask_b32 v76, v76, v72, s[36:37]\n"
"v_cndmask_b32 v77, v58, v59, s[34:35]\n"
"v_cndmask_b32 v72, v66, v67, s[34:35]\n"
"v_cndmask_b32 v77, v77, v72, s[38:39]\n"
"v_max_f32 v52, v76, v77\n"
"v_max_f32 v54, v76, v77\n"
"v_cndmask_b32 v78, v60, v61, s[34:35]\n"
"v_cndmask_b32 v72, v68, v69, s[34:35]\n"
"v_cndmask_b32 v78, v78, v72, s[36:37]\n"
"v_cndmask_b32 v79, v62, v63, s[34:35]\n"
"v_cndmask_b32 v72, v70, v71, s[34:35]\n"
"v_cndmask_b32 v79, v79, v72, s[38:39]\n"
"v_max_f32 v53, v78, v79\n"
"v_max_f32 v55, v78, v79\n"
"v_permlane16_swap_b32 v52, v54\n"
"s_nop 0\n"
"v_permlane16_swap_b32 v53, v55\n"
"v_max_f32 v72, v52, v54\n"
"v_max_f32 v52, v52, v54\n"
"v_max_f32 v73, v53, v55\n"
"v_max_f32 v53, v53, v55\n"
"v_permlane32_swap_b32 v52, v72\n"
"s_nop 0\n"
"v_permlane32_swap_b32 v53, v73\n"
"v_max_f32 v52, v52, v72\n"
"v_max_f32 v53, v53, v73\n"
"v_mul_f32 v52, 0xbea52b5b, v52\n"
"v_mul_f32 v53, 0xbea52b5b, v53\n"
"v_fmamk_f32 v76, v76, 0x3ea52b5b, v52\n"
"v_fmamk_f32 v77, v77, 0x3ea52b5b, v52\n"
"v_fmamk_f32 v78, v78, 0x3ea52b5b, v53\n"
"v_fmamk_f32 v79, v79, 0x3ea52b5b, v53\n"
"v_exp_f32 v76, v76\n"
"v_exp_f32 v77, v77\n"
"v_exp_f32 v78, v78\n"
"v_exp_f32 v79, v79\n"
"v_cvt_pk_bf16_f32 v52, v76, v77\n"
"v_cvt_pk_bf16_f32 v53, v78, v79\n"
"v_perm_b32 v44, v52, v52, v80\n"
"v_perm_b32 v45, v52, v52, v81\n"
"v_perm_b32 v46, v52, v52, v82\n"
"v_perm_b32 v47, v52, v52, v83\n"
"v_perm_b32 v48, v53, v53, v80\n"
"v_perm_b32 v49, v53, v53, v81\n"
"v_perm_b32 v50, v53, v53, v82\n"
"v_perm_b32 v51, v53, v53, v83\n"
"v_mfma_f32_16x16x32_bf16 v[56:59], v[84:87], v[44:47], 0\n"
"s_nop 0\n"
"v_mfma_f32_16x16x32_bf16 v[60:63], v[84:87], v[48:51], 0\n"
"s_waitcnt lgkmcnt(6)\n"
"v_mfma_f32_16x16x32_bf16 v[28:31], v[28:31], v[44:47], 0\n"
"s_waitcnt lgkmcnt(4)\n"
"v_mfma_f32_16x16x32_bf16 v[32:35], v[32:35], v[48:51], 0\n"
"s_waitcnt lgkmcnt(2)\n"
"v_mfma_f32_16x16x32_bf16 v[36:39], v[36:39], v[44:47], 0\n"
"s_waitcnt lgkmcnt(0)\n"
"v_mfma_f32_16x16x32_bf16 v[40:43], v[40:43], v[48:51], 0\n"
"v_cndmask_b32 v88, 2.0, v1, s[40:41]\n"
"v_rcp_f32 v52, v56\n"
"v_rcp_f32 v53, v60\n"
"s_add_u32 s31, s30, 0x140000\n"
"v_mul_f32 v28, v52, v28\n"
"v_mul_f32 v29, v52, v29\n"
"v_mul_f32 v30, v52, v30\n"
"v_mul_f32 v31, v52, v31\n"
"v_mul_f32 v32, v53, v32\n"
"v_mul_f32 v33, v53, v33\n"
"v_mul_f32 v34, v53, v34\n"
"v_mul_f32 v35, v53, v35\n"
"v_cvt_pk_bf16_f32 v28, v28, v29\n"
"v_cvt_pk_bf16_f32 v29, v30, v31\n"
"v_cvt_pk_bf16_f32 v30, v32, v33\n"
"v_cvt_pk_bf16_f32 v31, v34, v35\n"
"v_min_u32 v28, -2, v28\n"
"v_min_u32 v29, -2, v29\n"
"v_min_u32 v30, -2, v30\n"
"v_min_u32 v31, -2, v31\n"
"buffer_store_dwordx4 v[28:31], v1, s[4:7], s31 offen offset:0 sc1\n"
"v_mul_f32 v36, v52, v36\n"
"v_mul_f32 v37, v52, v37\n"
"v_mul_f32 v38, v52, v38\n"
"v_mul_f32 v39, v52, v39\n"
"v_mul_f32 v40, v53, v40\n"
"v_mul_f32 v41, v53, v41\n"
"v_mul_f32 v42, v53, v42\n"
"v_mul_f32 v43, v53, v43\n"
"v_cvt_pk_bf16_f32 v36, v36, v37\n"
"v_cvt_pk_bf16_f32 v37, v38, v39\n"
"v_cvt_pk_bf16_f32 v38, v40, v41\n"
"v_cvt_pk_bf16_f32 v39, v42, v43\n"
"v_min_u32 v36, -2, v36\n"
"v_min_u32 v37, -2, v37\n"
"v_min_u32 v38, -2, v38\n"
"v_min_u32 v39, -2, v39\n"
"buffer_store_dwordx4 v[36:39], v88, s[4:7], s31 offen offset:1024 sc1\n"
"ds_write_b32 v13, v13 offset:160\n"
"s_nop 2\n"
"s_cmp_lt_i32 s45, 0\n"
"s_cbranch_scc1 .Lnc_60\n"
"s_setprio 1\n"
"s_lshl_b32 s79, s3, 10\n"
"v_lshl_add_u32 v34, v0, 4, s79\n"
"v_lshrrev_b32 v59, 7, v34\n"
"v_and_b32 v58, 0x70, v34\n"
"s_mov_b32 s79, 0x5000\n"
"v_mad_i32_i24 v33, v59, s79, v58\n"
"s_mov_b32 s17, 0x100000\n"
".Ltp_61:\n"
"s_sleep 1\n"
"ds_read_b32 v42, v14 offset:176\n"
"s_sub_u32 s17, s17, 1\n"
"s_cbranch_scc1 .Lou_62\n"
"s_waitcnt lgkmcnt(0)\n"
"v_cmp_eq_u32 vcc, 0, v42\n"
"s_cbranch_vccnz .Ltp_61\n"
".Lou_62:\n"
"s_waitcnt lgkmcnt(0)\n"
"s_lshl_b32 s79, s45, 8\n"
"s_add_u32 s46, s79, 0x5e8000\n"
"s_add_u32 s47, s46, 0x140000\n"
"s_and_b32 s79, s2, 7\n"
"s_lshl_b32 s79, s79, 14\n"
"s_add_u32 s48, s79, 0xb28000\n"
"s_add_u32 s49, s48, 0x1000\n"
"v_and_b32 v94, 0xffffff80, v33\n"
"v_add_u32 v94, v94, v33\n"
"buffer_load_dwordx4 v[78:81], v94, s[4:7], s46 offen offset:0 sc1\n"
"buffer_load_dwordx4 v[82:85], v94, s[4:7], s46 offen offset:128 sc1\n"
"buffer_load_dwordx4 v[86:89], v94, s[4:7], s47 offen offset:0 sc1\n"
"buffer_load_dwordx4 v[90:93], v94, s[4:7], s47 offen offset:128 sc1\n"
"s_mul_i32 s79, s3, 0x1200\n"
"s_add_u32 s79, s79, 0x1c400\n"
"v_lshl_add_u32 v44, v0, 3, s79\n"
"v_mov_b32 v46, 0\n"
"v_mov_b32 v47, 0\n"
"ds_write_b64 v44, v[46:47]\n"
"v_and_b32 v51, 15, v0\n"
"v_and_b32 v50, 1, v51\n"
"v_and_b32 v49, 2, v51\n"
"v_lshlrev_b32 v48, 4, v0\n"
"v_lshrrev_b32 v47, 4, v0\n"
"v_xor_b32 v46, s3, v51\n"
"v_and_b32 v45, 1, v46\n"
"v_lshlrev_b32 v44, 1, v45\n"
"s_lshl_b32 s79, s3, 10\n"
"v_add_u32 v59, s79, v48\n"
"v_lshrrev_b32 v59, 2, v59\n"
"s_lshl_b32 s79, s3, 10\n"
"v_add_u32 v58, s79, v48\n"
"v_lshrrev_b32 v58, 6, v58\n"
"v_and_b32 v58, 24, v58\n"
"v_xor_b32 v59, v59, v58\n"
"v_add_u32 v36, 0xa000, v59\n"
"s_lshl_b32 s79, s3, 8\n"
"s_add_u32 s79, s79, 0xa000\n"
"v_lshrrev_b32 v59, 2, v51\n"
"v_lshrrev_b32 v58, 1, v47\n"
"v_xor_b32 v59, v59, v58\n"
"v_and_b32 v59, 1, v59\n"
"v_add_u32 v59, v44, v59\n"
"v_lshrrev_b32 v58, 2, v51\n"
"v_lshrrev_b32 v57, 3, v51\n"
"v_lshlrev_b32 v57, 7, v57\n"
"v_lshl_add_u32 v58, v58, 11, v57\n"
"v_lshl_add_u32 v58, v47, 6, v58\n"
"v_lshl_add_u32 v58, v49, 4, v58\n"
"v_lshl_add_u32 v59, v59, 3, v58\n"
"v_add_u32 v31, s79, v59\n"
"s_mul_i32 s79, s3, 0x2100\n"
"s_add_u32 s79, s79, 0x13840\n"
"v_lshlrev_b32 v59, 3, v47\n"
"v_sub_u32 v59, v59, v51\n"
"v_add_u32 v59, 0x3ef, v59\n"
"v_lshrrev_b32 v59, 2, v59\n"
"v_lshlrev_b32 v58, 3, v47\n"
"v_sub_u32 v58, v58, v51\n"
"v_add_u32 v58, 0x3ef, v58\n"
"v_and_b32 v58, 3, v58\n"
"v_mul_i32_i24 v58, 0x840, v58\n"
"v_lshl_add_u32 v59, v59, 3, v58\n"
"v_add_u32 v28, s79, v59\n"
"s_mul_i32 s79, s3, 0x1200\n"
"s_add_u32 s79, s79, 0x1c420\n"
"v_lshlrev_b32 v59, 4, v47\n"
"v_lshl_add_u32 v59, v51, 5, v59\n"
"v_add_u32 v29, s79, v59\n"
"s_lshl_b32 s79, s3, 11\n"
"s_lshr_b32 s78, s3, 1\n"
"s_lshl_b32 s78, s78, 7\n"
"s_add_u32 s79, s79, s78\n"
"s_add_u32 s79, s79, 0xa000\n"
"s_and_b32 s78, s3, 1\n"
"v_lshrrev_b32 v59, 2, v51\n"
"v_xor_b32 v59, v47, v59\n"
"v_xor_b32 v59, s78, v59\n"
"v_and_b32 v59, 3, v59\n"
"v_lshlrev_b32 v59, 3, v59\n"
"v_lshl_add_u32 v59, v51, 5, v59\n"
"v_add_u32 v30, s79, v59\n"
"v_xor_b32 v32, 8, v31\n"
"s_lshl_b32 s79, s3, 10\n"
"v_add_u32 v59, s79, v48\n"
"v_lshrrev_b32 v59, 2, v59\n"
"v_add_u32 v35, 0x1c600, v59\n"
"v_xor_b32 v37, 8, v36\n"
"v_mov_b32 v59, 0xffff0000\n"
"v_mov_b32 v58, 0xffff\n"
"v_cmp_eq_i32 vcc, 1, v50\n"
"s_nop 1\n"
"v_cndmask_b32 v59, v58, v59, vcc\n"
"v_lshrrev_b32 v58, 1, v51\n"
"v_lshlrev_b32 v57, 1, v47\n"
"v_cmp_eq_i32 vcc, v58, v57\n"
"s_nop 1\n"
"v_cndmask_b32 v38, 0, v59, vcc\n"
"v_mov_b32 v59, 0xffff0000\n"
"v_mov_b32 v58, 0xffff\n"
"v_cmp_eq_i32 vcc, 1, v50\n"
"s_nop 1\n"
"v_cndmask_b32 v59, v58, v59, vcc\n"
"v_lshrrev_b32 v58, 1, v51\n"
"v_lshl_add_u32 v57, v47, 1, 1\n"
"v_cmp_eq_i32 vcc, v58, v57\n"
"s_nop 1\n"
"v_cndmask_b32 v39, 0, v59, vcc\n"
"s_lshl_b32 s79, s3, 2\n"
"s_add_u32 s79, s79, 0x26800\n"
"v_mov_b32 v40, s79\n"
"s_lshl_b32 s79, s3, 10\n"
"v_add_u32 v59, s79, v48\n"
"v_lshrrev_b32 v59, 6, v59\n"
"s_lshl_b32 s79, s3, 10\n"
"v_add_u32 v58, s79, v48\n"
"v_and_b32 v58, 48, v58\n"
"v_mul_i32_i24 v58, 0x50, v58\n"
"s_mov_b32 s79, 0x2800\n"
"v_mad_i32_i24 v41, v59, s79, v58\n"
"s_mov_b32 s17, 0x100000\n"
".Ltp_63:\n"
"s_sleep 1\n"
"ds_read_b32 v42, v14 offset:192\n"
"s_sub_u32 s17, s17, 1\n"
"s_cbranch_scc1 .Lou_64\n"
"s_waitcnt lgkmcnt(0)\n"
"v_cmp_eq_u32 vcc, 0, v42\n"
"s_cbranch_vccnz .Ltp_63\n"
".Lou_64:\n"
"s_waitcnt lgkmcnt(0)\n"
"ds_read_b32 v40, v40\n"
"s_waitcnt lgkmcnt(0)\n"
"v_sub_u32 v44, 0x7f000000, v40\n"
"v_lshrrev_b32 v45, 16, v44\n"
"v_or_b32 v44, v44, v45\n"
"v_and_b32 v38, v44, v38\n"
"v_and_b32 v39, v44, v39\n"
"s_mov_b32 s52, 0x05040100\n"
"s_mov_b32 s53, 0x07060302\n"
"s_mov_b32 s17, 32768\n"
".Ltp_65:\n"
"s_waitcnt vmcnt(0)\n"
"s_sub_u32 s17, s17, 1\n"
"s_cbranch_scc1 .Lou_66\n"
"v_bitop3_b32 v54, v79, v81, s16 bitop3:0x7e\n"
"v_bitop3_b32 v55, v83, v85, s16 bitop3:0x7e\n"
"v_bitop3_b32 v56, v87, v89, s16 bitop3:0x7e\n"
"v_bitop3_b32 v57, v91, v93, s16 bitop3:0x7e\n"
"v_or3_b32 v58, v54, v55, v56\n"
"v_or_b32 v58, v58, v57\n"
"v_cmp_ne_u32 vcc, 0, v58\n"
"s_cbranch_vccz .Lou_66\n"
"s_sleep 2\n"
"v_cmp_ne_u32 vcc, 0, v54\n"
"s_cbranch_vccz .Lsk_67\n"
"s_mov_b64 exec, vcc\n"
"buffer_load_dwordx4 v[78:81], v94, s[4:7], s46 offen offset:0 sc1\n"
"s_mov_b64 exec, -1\n"
".Lsk_67:\n"
"v_cmp_ne_u32 vcc, 0, v55\n"
"s_cbranch_vccz .Lsk_68\n"
"s_mov_b64 exec, vcc\n"
"buffer_load_dwordx4 v[82:85], v94, s[4:7], s46 offen offset:128 sc1\n"
"s_mov_b64 exec, -1\n"
".Lsk_68:\n"
"v_cmp_ne_u32 vcc, 0, v56\n"
"s_cbranch_vccz .Lsk_69\n"
"s_mov_b64 exec, vcc\n"
"buffer_load_dwordx4 v[86:89], v94, s[4:7], s47 offen offset:0 sc1\n"
"s_mov_b64 exec, -1\n"
".Lsk_69:\n"
"v_cmp_ne_u32 vcc, 0, v57\n"
"s_cbranch_vccz .Lsk_70\n"
"s_mov_b64 exec, vcc\n"
"buffer_load_dwordx4 v[90:93], v94, s[4:7], s47 offen offset:128 sc1\n"
"s_mov_b64 exec, -1\n"
".Lsk_70:\n"
"s_branch .Ltp_65\n"
".Lou_66:\n"
"s_waitcnt vmcnt(0)\n"
"buffer_load_dwordx4 v[70:73], v34, s[4:7], s48 offen sc1\n"
"buffer_load_dwordx4 v[74:77], v34, s[4:7], s49 offen sc1\n"
"v_perm_b32 v54, v82, v78, s52\n"
"v_perm_b32 v55, v82, v78, s53\n"
"v_perm_b32 v56, v84, v80, s52\n"
"v_perm_b32 v57, v84, v80, s53\n"
"ds_write_b32 v36, v54 offset:0\n"
"ds_write_b32 v37, v55 offset:2048\n"
"ds_write_b32 v36, v56 offset:4224\n"
"ds_write_b32 v37, v57 offset:6272\n"
"v_perm_b32 v54, v90, v86, s52\n"
"v_perm_b32 v55, v90, v86, s53\n"
"v_perm_b32 v56, v92, v88, s52\n"
"v_perm_b32 v57, v92, v88, s53\n"
"ds_write_b32 v36, v54 offset:1024\n"
"ds_write_b32 v37, v55 offset:3072\n"
"ds_write_b32 v36, v56 offset:5248\n"
"ds_write_b32 v37, v57 offset:7296\n"
"v_lshlrev_b32 v54, 16, v78\n"
"v_lshlrev_b32 v55, 16, v82\n"
"v_and_b32 v56, 0xffff0000, v78\n"
"v_and_b32 v57, 0xffff0000, v82\n"
"v_lshlrev_b32 v58, 16, v80\n"
"v_lshlrev_b32 v59, 16, v84\n"
"v_and_b32 v60, 0xffff0000, v80\n"
"v_and_b32 v61, 0xffff0000, v84\n"
"v_lshlrev_b32 v62, 16, v86\n"
"v_lshlrev_b32 v63, 16, v90\n"
"v_and_b32 v64, 0xffff0000, v86\n"
"v_and_b32 v65, 0xffff0000, v90\n"
"v_lshlrev_b32 v66, 16, v88\n"
"v_lshlrev_b32 v67, 16, v92\n"
"v_and_b32 v68, 0xffff0000, v88\n"
"v_and_b32 v69, 0xffff0000, v92\n"
"s_mov_b32 s17, 32768\n"
".Ltp_71:\n"
"s_waitcnt vmcnt(0)\n"
"s_sub_u32 s17, s17, 1\n"
"s_cbranch_scc1 .Lou_72\n"
"v_max_u32 v78, v70, v71\n"
"v_max3_u32 v78, v72, v73, v78\n"
"v_max3_u32 v78, v74, v75, v78\n"
"v_max3_u32 v78, v76, v77, v78\n"
"v_cmp_eq_u32 vcc, -1, v78\n"
"s_cbranch_vccz .Lou_72\n"
"v_max_u32 v78, v72, v73\n"
"v_max3_u32 v78, v70, v71, v78\n"
"v_cmp_eq_u32 vcc, -1, v78\n"
"s_cbranch_vccz .Lsk_73\n"
"s_mov_b64 exec, vcc\n"
"buffer_load_dwordx4 v[70:73], v34, s[4:7], s48 offen sc1\n"
"s_mov_b64 exec, -1\n"
".Lsk_73:\n"
"v_max_u32 v78, v76, v77\n"
"v_max3_u32 v78, v74, v75, v78\n"
"v_cmp_eq_u32 vcc, -1, v78\n"
"s_cbranch_vccz .Lsk_74\n"
"s_mov_b64 exec, vcc\n"
"buffer_load_dwordx4 v[74:77], v34, s[4:7], s49 offen sc1\n"
"s_mov_b64 exec, -1\n"
".Lsk_74:\n"
"s_branch .Ltp_71\n"
".Lou_72:\n"
"s_waitcnt vmcnt(0)\n"
"v_fma_f32 v54, v54, v71, v70\n"
"v_fma_f32 v55, v55, v73, v72\n"
"v_cvt_pk_f16_f32 v54, v54, v55\n"
"v_fma_f32 v56, v56, v71, v70\n"
"v_fma_f32 v57, v57, v73, v72\n"
"v_cvt_pk_f16_f32 v56, v56, v57\n"
"v_fma_f32 v58, v58, v71, v70\n"
"v_fma_f32 v59, v59, v73, v72\n"
"v_cvt_pk_f16_f32 v58, v58, v59\n"
"v_fma_f32 v60, v60, v71, v70\n"
"v_fma_f32 v61, v61, v73, v72\n"
"v_cvt_pk_f16_f32 v60, v60, v61\n"
"ds_write2st64_b32 v35, v54, v56 offset0:0 offset1:18\n"
"ds_write2st64_b32 v35, v58, v60 offset0:36 offset1:54\n"
"v_fma_f32 v62, v62, v75, v74\n"
"v_fma_f32 v63, v63, v77, v76\n"
"v_cvt_pk_f16_f32 v62, v62, v63\n"
"v_fma_f32 v64, v64, v75, v74\n"
"v_fma_f32 v65, v65, v77, v76\n"
"v_cvt_pk_f16_f32 v64, v64, v65\n"
"v_fma_f32 v66, v66, v75, v74\n"
"v_fma_f32 v67, v67, v77, v76\n"
"v_cvt_pk_f16_f32 v66, v66, v67\n"
"v_fma_f32 v68, v68, v75, v74\n"
"v_fma_f32 v69, v69, v77, v76\n"
"v_cvt_pk_f16_f32 v68, v68, v69\n"
"ds_write2st64_b32 v35, v62, v64 offset0:4 offset1:22\n"
"ds_write2st64_b32 v35, v66, v68 offset0:40 offset1:58\n"
"ds_write_b32 v13, v13 offset:208\n"
"s_mov_b32 s17, 0x100000\n"
".Ltp_75:\n"
"ds_read_b32 v42, v14 offset:208\n"
"s_sub_u32 s17, s17, 1\n"
"s_cbranch_scc1 .Lou_76\n"
"s_waitcnt lgkmcnt(0)\n"
"v_cmp_eq_u32 vcc, 0, v42\n"
"s_cbranch_vccnz .Ltp_75\n"
".Lou_76:\n"
"s_waitcnt lgkmcnt(0)\n"
"s_mov_b32 s50, 0xfffefffe\n"
"ds_read_b64 v[60:61], v28 offset:1984\n"
"ds_read_b64 v[62:63], v28 offset:1992\n"
"ds_read_b128 v[64:67], v29 offset:1984\n"
"ds_read_b128 v[68:71], v29 offset:1472\n"
"ds_read_b64 v[72:73], v28 offset:1472\n"
"ds_read_b64 v[74:75], v28 offset:1480\n"
"ds_read_b128 v[76:79], v29 offset:960\n"
"s_waitcnt lgkmcnt(0)\n"
"v_mfma_f32_16x16x32_f16 v[56:59], v[60:63], v[64:67], 0\n"
"ds_read_b64 v[80:81], v28 offset:960\n"
"ds_read_b64 v[82:83], v28 offset:968\n"
"v_mfma_f32_16x16x32_f16 v[52:55], v[60:63], v[68:71], 0\n"
"ds_read_b128 v[84:87], v29 offset:448\n"
"ds_read_b64 v[88:89], v28 offset:448\n"
"v_mfma_f32_16x16x32_f16 v[56:59], v[72:75], v[68:71], v[56:59]\n"
"ds_read_b64 v[90:91], v28 offset:456\n"
"ds_read_b64 v[92:93], v28 offset:1920\n"
"v_mfma_f32_16x16x32_f16 v[48:51], v[60:63], v[76:79], 0\n"
"ds_read_b64 v[94:95], v28 offset:1928\n"
"s_waitcnt lgkmcnt(4)\n"
"v_mfma_f32_16x16x32_f16 v[52:55], v[72:75], v[76:79], v[52:55]\n"
"ds_read_b128 v[2:5], v29 offset:1920\n"
"ds_read_b128 v[34:37], v29 offset:1408\n"
"v_mfma_f32_16x16x32_f16 v[56:59], v[80:83], v[76:79], v[56:59]\n"
"ds_read_b64 v[64:65], v28 offset:1408\n"
"ds_read_b64 v[66:67], v28 offset:1416\n"
"v_mfma_f32_16x16x32_f16 v[44:47], v[60:63], v[84:87], 0\n"
"ds_read_b128 v[68:71], v29 offset:896\n"
"ds_read_b64 v[76:77], v28 offset:896\n"
"v_mfma_f32_16x16x32_f16 v[48:51], v[72:75], v[84:87], v[48:51]\n"
"ds_read_b64 v[78:79], v28 offset:904\n"
"s_waitcnt lgkmcnt(5)\n"
"v_mfma_f32_16x16x32_f16 v[52:55], v[80:83], v[84:87], v[52:55]\n"
"ds_read_b128 v[60:63], v29 offset:384\n"
"ds_read_b64 v[72:73], v28 offset:384\n"
"v_mfma_f32_16x16x32_f16 v[56:59], v[88:91], v[84:87], v[56:59]\n"
"ds_read_b64 v[74:75], v28 offset:392\n"
"ds_read_b64 v[80:81], v28 offset:1856\n"
"v_mfma_f32_16x16x32_f16 v[56:59], v[92:95], v[2:5], v[56:59]\n"
"ds_read_b64 v[82:83], v28 offset:1864\n"
"ds_read_b128 v[84:87], v29 offset:1856\n"
"v_mfma_f32_16x16x32_f16 v[52:55], v[92:95], v[34:37], v[52:55]\n"
"ds_read_b128 v[88:91], v29 offset:1344\n"
"s_waitcnt lgkmcnt(7)\n"
"v_mfma_f32_16x16x32_f16 v[56:59], v[64:67], v[34:37], v[56:59]\n"
"ds_read_b64 v[2:3], v30 offset:0\n"
"ds_read_b64 v[34:35], v28 offset:1344\n"
"v_mfma_f32_16x16x32_f16 v[48:51], v[92:95], v[68:71], v[48:51]\n"
"ds_read_b64 v[36:37], v28 offset:1352\n"
"v_mfma_f32_16x16x32_f16 v[52:55], v[64:67], v[68:71], v[52:55]\n"
"v_mfma_f32_16x16x32_f16 v[56:59], v[76:79], v[68:71], v[56:59]\n"
"ds_read_b128 v[68:71], v29 offset:832\n"
"s_waitcnt lgkmcnt(8)\n"
"v_mfma_f32_16x16x32_f16 v[44:47], v[92:95], v[60:63], v[44:47]\n"
"ds_read_b64 v[92:93], v28 offset:832\n"
"v_mfma_f32_16x16x32_f16 v[48:51], v[64:67], v[60:63], v[48:51]\n"
"ds_read_b64 v[94:95], v28 offset:840\n"
"v_mfma_f32_16x16x32_f16 v[52:55], v[76:79], v[60:63], v[52:55]\n"
"ds_read_b128 v[64:67], v29 offset:320\n"
"ds_read_b64 v[76:77], v28 offset:320\n"
"v_mfma_f32_16x16x32_f16 v[56:59], v[72:75], v[60:63], v[56:59]\n"
"ds_read_b64 v[78:79], v28 offset:328\n"
"s_waitcnt lgkmcnt(6)\n"
"v_mfma_f32_16x16x32_f16 v[56:59], v[80:83], v[84:87], v[56:59]\n"
"ds_read_b64 v[60:61], v30 offset:512\n"
"ds_read_b64 v[72:73], v28 offset:1792\n"
"v_mfma_f32_16x16x32_f16 v[52:55], v[80:83], v[88:91], v[52:55]\n"
"ds_read_b64 v[74:75], v28 offset:1800\n"
"s_and_b32 s79, s45, 0xfc\n"
"v_mfma_f32_16x16x16_bf16 v[44:47], v[38:39], v[2:3], v[44:47]\n"
"ds_read_b128 v[84:87], v29 offset:1792\n"
"s_and_b32 s78, s45, 1\n"
"v_mfma_f32_16x16x32_f16 v[56:59], v[34:37], v[88:91], v[56:59]\n"
"ds_read_b128 v[2:5], v29 offset:1280\n"
"s_waitcnt lgkmcnt(7)\n"
"v_mfma_f32_16x16x32_f16 v[48:51], v[80:83], v[68:71], v[48:51]\n"
"ds_read_b64 v[88:89], v28 offset:1280\n"
"s_lshl_b32 s78, s78, 1\n"
"v_mfma_f32_16x16x32_f16 v[52:55], v[34:37], v[68:71], v[52:55]\n"
"ds_read_b64 v[90:91], v28 offset:1288\n"
"s_add_u32 s79, s79, s78\n"
"v_mfma_f32_16x16x32_f16 v[56:59], v[92:95], v[68:71], v[56:59]\n"
"ds_read_b128 v[68:71], v29 offset:768\n"
"s_lshr_b32 s78, s45, 1\n"
"v_mfma_f32_16x16x32_f16 v[44:47], v[80:83], v[64:67], v[44:47]\n"
"ds_read_b64 v[80:81], v28 offset:768\n"
"s_waitcnt lgkmcnt(8)\n"
"v_mfma_f32_16x16x32_f16 v[48:51], v[34:37], v[64:67], v[48:51]\n"
"ds_read_b64 v[82:83], v28 offset:776\n"
"s_and_b32 s78, s78, 1\n"
"v_mfma_f32_16x16x32_f16 v[52:55], v[92:95], v[64:67], v[52:55]\n"
"ds_read_b128 v[34:37], v29 offset:256\n"
"ds_read_b64 v[92:93], v28 offset:256\n"
"v_mfma_f32_16x16x32_f16 v[56:59], v[76:79], v[64:67], v[56:59]\n"
"ds_read_b64 v[94:95], v28 offset:264\n"
"ds_read_b64 v[64:65], v28 offset:1728\n"
"v_mfma_f32_16x16x16_bf16 v[48:51], v[38:39], v[60:61], v[48:51]\n"
"ds_read_b64 v[66:67], v28 offset:1736\n"
"s_waitcnt lgkmcnt(7)\n"
"v_mfma_f32_16x16x32_f16 v[56:59], v[72:75], v[84:87], v[56:59]\n"
"ds_read_b128 v[76:79], v29 offset:1728\n"
"ds_read_b128 v[60:63], v29 offset:1216\n"
"v_mfma_f32_16x16x32_f16 v[52:55], v[72:75], v[2:5], v[52:55]\n"
"ds_read_b64 v[84:85], v28 offset:1216\n"
"s_add_u32 s79, s79, s78\n"
"v_mfma_f32_16x16x32_f16 v[56:59], v[88:91], v[2:5], v[56:59]\n"
"ds_read_b64 v[86:87], v28 offset:1224\n"
"s_lshl_b32 s79, s79, 7\n"
"v_mfma_f32_16x16x32_f16 v[48:51], v[72:75], v[68:71], v[48:51]\n"
"ds_read_b128 v[2:5], v29 offset:704\n"
"s_waitcnt lgkmcnt(9)\n"
"v_mfma_f32_16x16x32_f16 v[52:55], v[88:91], v[68:71], v[52:55]\n"
"s_add_u32 s46, s79, 0xb48000\n"
"v_mfma_f32_16x16x32_f16 v[56:59], v[80:83], v[68:71], v[56:59]\n"
"ds_read_b64 v[68:69], v30 offset:1024\n"
"v_mfma_f32_16x16x32_f16 v[44:47], v[72:75], v[34:37], v[44:47]\n"
"ds_read_b64 v[72:73], v28 offset:704\n"
"v_mfma_f32_16x16x32_f16 v[48:51], v[88:91], v[34:37], v[48:51]\n"
"ds_read_b64 v[74:75], v28 offset:712\n"
"s_waitcnt lgkmcnt(6)\n"
"v_mfma_f32_16x16x32_f16 v[52:55], v[80:83], v[34:37], v[52:55]\n"
"ds_read_b128 v[88:91], v29 offset:192\n"
"ds_read_b64 v[80:81], v28 offset:192\n"
"v_mfma_f32_16x16x32_f16 v[56:59], v[92:95], v[34:37], v[56:59]\n"
"ds_read_b64 v[82:83], v28 offset:200\n"
"ds_read_b64 v[34:35], v28 offset:1664\n"
"v_mfma_f32_16x16x32_f16 v[56:59], v[64:67], v[76:79], v[56:59]\n"
"ds_read_b64 v[36:37], v28 offset:1672\n"
"ds_read_b128 v[92:95], v29 offset:1664\n"
"v_mfma_f32_16x16x32_f16 v[52:55], v[64:67], v[60:63], v[52:55]\n"
"ds_read_b128 v[76:79], v29 offset:1152\n"
"s_waitcnt lgkmcnt(9)\n"
"v_mfma_f32_16x16x32_f16 v[56:59], v[84:87], v[60:63], v[56:59]\n"
"ds_read_b64 v[60:61], v28 offset:1152\n"
"v_mfma_f32_16x16x32_f16 v[48:51], v[64:67], v[2:5], v[48:51]\n"
"ds_read_b64 v[62:63], v28 offset:1160\n"
"v_mfma_f32_16x16x16_bf16 v[52:55], v[38:39], v[68:69], v[52:55]\n"
"ds_read_b128 v[68:71], v29 offset:640\n"
"s_nop 4\n"
"v_mfma_f32_16x16x32_f16 v[52:55], v[84:87], v[2:5], v[52:55]\n"
"s_waitcnt lgkmcnt(9)\n"
"v_mfma_f32_16x16x32_f16 v[56:59], v[72:75], v[2:5], v[56:59]\n"
"ds_read_b64 v[2:3], v28 offset:640\n"
"v_mfma_f32_16x16x32_f16 v[44:47], v[64:67], v[88:91], v[44:47]\n"
"ds_read_b64 v[4:5], v28 offset:648\n"
"v_mfma_f32_16x16x32_f16 v[48:51], v[84:87], v[88:91], v[48:51]\n"
"ds_read_b128 v[64:67], v29 offset:128\n"
"v_mfma_f32_16x16x32_f16 v[52:55], v[72:75], v[88:91], v[52:55]\n"
"ds_read_b64 v[84:85], v30 offset:1536\n"
"s_waitcnt lgkmcnt(5)\n"
"v_mfma_f32_16x16x32_f16 v[56:59], v[80:83], v[88:91], v[56:59]\n"
"ds_read_b64 v[72:73], v28 offset:128\n"
"ds_read_b64 v[74:75], v28 offset:136\n"
"v_mfma_f32_16x16x32_f16 v[56:59], v[34:37], v[92:95], v[56:59]\n"
"ds_read_b64 v[88:89], v28 offset:1600\n"
"ds_read_b64 v[90:91], v28 offset:1608\n"
"v_mfma_f32_16x16x32_f16 v[52:55], v[34:37], v[76:79], v[52:55]\n"
"ds_read_b128 v[80:83], v29 offset:1600\n"
"v_mfma_f32_16x16x32_f16 v[56:59], v[60:63], v[76:79], v[56:59]\n"
"ds_read_b128 v[92:95], v29 offset:1088\n"
"s_waitcnt lgkmcnt(7)\n"
"v_mfma_f32_16x16x32_f16 v[48:51], v[34:37], v[68:71], v[48:51]\n"
"ds_read_b64 v[76:77], v28 offset:1088\n"
"v_mfma_f32_16x16x32_f16 v[52:55], v[60:63], v[68:71], v[52:55]\n"
"ds_read_b64 v[78:79], v28 offset:1096\n"
"v_mfma_f32_16x16x32_f16 v[56:59], v[2:5], v[68:71], v[56:59]\n"
"ds_read_b128 v[68:71], v29 offset:576\n"
"v_mfma_f32_16x16x32_f16 v[44:47], v[34:37], v[64:67], v[44:47]\n"
"ds_read_b64 v[34:35], v28 offset:576\n"
"s_waitcnt lgkmcnt(8)\n"
"s_nop 0\n"
"v_mfma_f32_16x16x16_bf16 v[56:59], v[38:39], v[84:85], v[56:59]\n"
"ds_read_b64 v[36:37], v28 offset:584\n"
"v_mfma_f32_16x16x32_f16 v[48:51], v[60:63], v[64:67], v[48:51]\n"
"ds_read_b128 v[84:87], v29 offset:64\n"
"ds_read_b64 v[60:61], v28 offset:64\n"
"v_mfma_f32_16x16x32_f16 v[52:55], v[2:5], v[64:67], v[52:55]\n"
"ds_read_b64 v[62:63], v28 offset:72\n"
"ds_read_b64 v[2:3], v28 offset:1536\n"
"v_mfma_f32_16x16x32_f16 v[56:59], v[72:75], v[64:67], v[56:59]\n"
"ds_read_b64 v[4:5], v28 offset:1544\n"
"s_waitcnt lgkmcnt(7)\n"
"v_mfma_f32_16x16x32_f16 v[56:59], v[88:91], v[80:83], v[56:59]\n"
"ds_read_b128 v[64:67], v29 offset:0\n"
"ds_read_b128 v[72:75], v29 offset:512\n"
"v_mfma_f32_16x16x32_f16 v[52:55], v[88:91], v[92:95], v[52:55]\n"
"ds_read_b64 v[80:81], v28 offset:1024\n"
"v_mfma_f32_16x16x32_f16 v[56:59], v[76:79], v[92:95], v[56:59]\n"
"ds_read_b64 v[82:83], v28 offset:1032\n"
"v_mfma_f32_16x16x32_f16 v[48:51], v[88:91], v[68:71], v[48:51]\n"
"ds_read_b128 v[92:95], v29 offset:1024\n"
"s_waitcnt lgkmcnt(9)\n"
"v_mfma_f32_16x16x32_f16 v[52:55], v[76:79], v[68:71], v[52:55]\n"
"v_mfma_f32_16x16x32_f16 v[56:59], v[34:37], v[68:71], v[56:59]\n"
"ds_read_b64 v[68:69], v28 offset:512\n"
"v_mfma_f32_16x16x32_f16 v[44:47], v[88:91], v[84:87], v[44:47]\n"
"ds_read_b64 v[70:71], v28 offset:520\n"
"v_mfma_f32_16x16x32_f16 v[48:51], v[76:79], v[84:87], v[48:51]\n"
"ds_read_b128 v[88:91], v29 offset:1536\n"
"s_waitcnt lgkmcnt(6)\n"
"v_mfma_f32_16x16x32_f16 v[52:55], v[34:37], v[84:87], v[52:55]\n"
"ds_read_b64 v[76:77], v28 offset:0\n"
"v_mfma_f32_16x16x32_f16 v[56:59], v[60:63], v[84:87], v[56:59]\n"
"ds_read_b64 v[78:79], v28 offset:8\n"
"v_mfma_f32_16x16x32_f16 v[44:47], v[2:5], v[64:67], v[44:47]\n"
"v_mfma_f32_16x16x32_f16 v[48:51], v[2:5], v[72:75], v[48:51]\n"
"s_waitcnt lgkmcnt(3)\n"
"v_mfma_f32_16x16x32_f16 v[48:51], v[80:83], v[64:67], v[48:51]\n"
"v_mfma_f32_16x16x32_f16 v[52:55], v[2:5], v[92:95], v[52:55]\n"
"v_mul_f32 v44, v40, v44\n"
"v_mul_f32 v45, v40, v45\n"
"v_mul_f32 v46, v40, v46\n"
"v_mfma_f32_16x16x32_f16 v[52:55], v[80:83], v[72:75], v[52:55]\n"
"v_mul_f32 v47, v40, v47\n"
"v_cvt_pk_bf16_f32 v44, v44, v45\n"
"v_cvt_pk_bf16_f32 v45, v46, v47\n"
"v_mfma_f32_16x16x32_f16 v[52:55], v[68:71], v[64:67], v[52:55]\n"
"v_pk_min_u16 v44, v44, s50\n"
"v_pk_min_u16 v45, v45, s50\n"
"s_waitcnt lgkmcnt(0)\n"
"v_mfma_f32_16x16x32_f16 v[56:59], v[2:5], v[88:91], v[56:59]\n"
"ds_write_b64 v30, v[44:45] offset:0\n"
"v_mfma_f32_16x16x32_f16 v[56:59], v[80:83], v[92:95], v[56:59]\n"
"v_mul_f32 v48, v40, v48\n"
"v_mul_f32 v49, v40, v49\n"
"v_mfma_f32_16x16x32_f16 v[56:59], v[68:71], v[72:75], v[56:59]\n"
"v_mul_f32 v50, v40, v50\n"
"v_mul_f32 v51, v40, v51\n"
"v_mfma_f32_16x16x32_f16 v[56:59], v[76:79], v[64:67], v[56:59]\n"
"v_cvt_pk_bf16_f32 v48, v48, v49\n"
"v_cvt_pk_bf16_f32 v49, v50, v51\n"
"v_pk_min_u16 v48, v48, s50\n"
"v_pk_min_u16 v49, v49, s50\n"
"ds_write_b64 v30, v[48:49] offset:512\n"
"v_mul_f32 v52, v40, v52\n"
"v_mul_f32 v53, v40, v53\n"
"v_mul_f32 v54, v40, v54\n"
"v_mul_f32 v55, v40, v55\n"
"v_cvt_pk_bf16_f32 v52, v52, v53\n"
"v_cvt_pk_bf16_f32 v53, v54, v55\n"
"v_pk_min_u16 v52, v52, s50\n"
"v_pk_min_u16 v53, v53, s50\n"
"ds_write_b64 v30, v[52:53] offset:1024\n"
"v_mul_f32 v56, v40, v56\n"
"v_mul_f32 v57, v40, v57\n"
"v_mul_f32 v58, v40, v58\n"
"v_mul_f32 v59, v40, v59\n"
"v_cvt_pk_bf16_f32 v56, v56, v57\n"
"v_cvt_pk_bf16_f32 v57, v58, v59\n"
"v_pk_min_u16 v56, v56, s50\n"
"v_pk_min_u16 v57, v57, s50\n"
"ds_write_b64 v30, v[56:57] offset:1536\n"
"ds_write_b32 v13, v13 offset:224\n"
"s_mov_b32 s17, 0x100000\n"
".Ltp_77:\n"
"ds_read_b32 v42, v14 offset:224\n"
"s_sub_u32 s17, s17, 1\n"
"s_cbranch_scc1 .Lou_78\n"
"s_waitcnt lgkmcnt(0)\n"
"v_cmp_eq_u32 vcc, 0, v42\n"
"s_cbranch_vccnz .Ltp_77\n"
".Lou_78:\n"
"s_waitcnt lgkmcnt(0)\n"
"ds_read_b64_tr_b16 v[60:61], v31 offset:0\n"
"ds_read_b64_tr_b16 v[62:63], v32 offset:0\n"
"ds_read_b64_tr_b16 v[64:65], v31 offset:1024\n"
"ds_read_b64_tr_b16 v[66:67], v32 offset:1024\n"
"s_add_u32 s47, s46, 0xa0000\n"
"s_waitcnt lgkmcnt(2)\n"
"buffer_store_dwordx4 v[60:63], v33, s[4:7], s46 offen sc1\n"
"s_waitcnt lgkmcnt(0)\n"
"buffer_store_dwordx4 v[64:67], v33, s[4:7], s47 offen sc1\n"
"ds_write_b32 v13, v13 offset:240\n"
"s_setprio 1\n"
"s_mov_b32 s17, 0x100000\n"
".Ltp_79:\n"
"ds_read_b32 v42, v14 offset:240\n"
"s_sub_u32 s17, s17, 1\n"
"s_cbranch_scc1 .Lou_80\n"
"s_waitcnt lgkmcnt(0)\n"
"v_cmp_eq_u32 vcc, 0, v42\n"
"s_cbranch_vccnz .Ltp_79\n"
".Lou_80:\n"
"s_waitcnt lgkmcnt(0)\n"
"s_nop 2\n"
".Lnc_60:\n"
"v_and_b32 v35, 15, v0\n"
"v_and_b32 v34, 3, v35\n"
"v_and_b32 v33, 4, v35\n"
"v_lshrrev_b32 v32, 4, v0\n"
"v_and_b32 v31, 1, v32\n"
"v_lshlrev_b32 v30, 7, v31\n"
"v_lshlrev_b32 v29, 1, v33\n"
"v_lshlrev_b32 v28, 4, v34\n"
"v_lshrrev_b32 v79, 3, v35\n"
"v_lshrrev_b32 v78, 1, v32\n"
"v_lshl_add_u32 v78, v78, 9, v30\n"
"v_add_u32 v78, v78, v28\n"
"v_lshl_add_u32 v79, v79, 6, v78\n"
"s_lshl_b32 s79, s43, 10\n"
"v_add3_u32 v3, v79, v29, s79\n"
"v_lshrrev_b32 v79, 3, v35\n"
"v_lshrrev_b32 v78, 1, v32\n"
"v_lshl_add_u32 v78, v78, 9, v30\n"
"v_add_u32 v78, v78, v28\n"
"v_lshl_add_u32 v79, v79, 6, v78\n"
"s_lshl_b32 s79, s43, 10\n"
"s_add_u32 s79, s79, 0xfffff400\n"
"s_lshl_b32 s78, s43, 10\n"
"s_add_u32 s78, s78, 0x4400\n"
"s_cmp_ge_i32 s43, 3\n"
"s_cselect_b32 s79, s79, s78\n"
"v_add3_u32 v4, v79, v29, s79\n"
"v_lshrrev_b32 v79, 3, v35\n"
"v_lshrrev_b32 v78, 1, v32\n"
"v_lshl_add_u32 v78, v78, 9, v30\n"
"v_add_u32 v78, v78, v28\n"
"v_lshl_add_u32 v79, v79, 6, v78\n"
"s_lshl_b32 s79, s43, 10\n"
"s_add_u32 s79, s79, 0xfffff800\n"
"s_lshl_b32 s78, s43, 10\n"
"s_add_u32 s78, s78, 0x4800\n"
"s_cmp_ge_i32 s43, 2\n"
"s_cselect_b32 s79, s79, s78\n"
"v_add3_u32 v5, v79, v29, s79\n"
"v_lshrrev_b32 v79, 3, v35\n"
"v_lshrrev_b32 v78, 1, v32\n"
"v_lshl_add_u32 v78, v78, 9, v30\n"
"v_add_u32 v78, v78, v28\n"
"v_lshl_add_u32 v79, v79, 6, v78\n"
"s_lshl_b32 s79, s43, 10\n"
"s_add_u32 s79, s79, 0xfffffc00\n"
"s_lshl_b32 s78, s43, 10\n"
"s_add_u32 s78, s78, 0x4c00\n"
"s_cmp_ge_i32 s43, 1\n"
"s_cselect_b32 s79, s79, s78\n"
"v_add3_u32 v6, v79, v29, s79\n"
"s_mul_i32 s79, s43, 0x1400\n"
"v_lshl_add_u32 v79, v0, 7, s79\n"
"v_mov_b32 v78, 0x40000\n"
"v_cmp_gt_i32 vcc, 40, v0\n"
"s_nop 1\n"
"v_cndmask_b32 v93, v78, v79, vcc\n"
"v_mov_b32 v28, 0x0c0c0c0c\n"
"v_mov_b32 v29, 0xc0c0100\n"
"v_mov_b32 v30, 0x1000c0c\n"
"v_cndmask_b32 v29, v29, v30, s[34:35]\n"
"v_cndmask_b32 v80, v29, v28, s[36:37]\n"
"v_cndmask_b32 v82, v28, v29, s[36:37]\n"
"v_mov_b32 v29, 0xc0c0302\n"
"v_mov_b32 v30, 0x3020c0c\n"
"v_cndmask_b32 v29, v29, v30, s[34:35]\n"
"v_cndmask_b32 v81, v29, v28, s[38:39]\n"
"v_cndmask_b32 v83, v28, v29, s[38:39]\n"
"s_mov_b32 s17, 0x100000\n"
".Ltp_81:\n"
"ds_read_b32 v90, v14 offset:160\n"
"s_sub_u32 s17, s17, 1\n"
"s_cbranch_scc1 .Lou_82\n"
"s_waitcnt lgkmcnt(0)\n"
"v_cmp_eq_u32 vcc, 0, v90\n"
"s_cbranch_vccnz .Ltp_81\n"
".Lou_82:\n"
"s_waitcnt lgkmcnt(0)\n"
"s_cmp_ge_i32 s45, 0\n"
"s_cbranch_scc1 .Lnw_83\n"
"s_mov_b32 s17, 0x100000\n"
".Ltp_84:\n"
"s_sleep 1\n"
"ds_read_b32 v90, v14 offset:176\n"
"s_sub_u32 s17, s17, 1\n"
"s_cbranch_scc1 .Lou_85\n"
"s_waitcnt lgkmcnt(0)\n"
"v_cmp_eq_u32 vcc, 0, v90\n"
"s_cbranch_vccnz .Ltp_84\n"
".Lou_85:\n"
"s_waitcnt lgkmcnt(0)\n"
"s_mov_b32 s89, 30\n"
".Ldl_86:\n"
"s_sub_u32 s89, s89, 1\n"
"s_cbranch_scc1 .Lde_87\n"
"s_sleep 1\n"
"s_branch .Ldl_86\n"
".Lde_87:\n"
"s_and_b32 s79, s2, 7\n"
"s_lshl_b32 s79, s79, 14\n"
"s_add_u32 s46, s79, 0xb28000\n"
"s_add_u32 s47, s46, 0x1000\n"
"buffer_load_dwordx4 v[28:31], v1, s[4:7], s46 offen offset:0 sc1\n"
"buffer_load_dwordx4 v[32:35], v1, s[4:7], s46 offen offset:1024 sc1\n"
"buffer_load_dwordx4 v[36:39], v1, s[4:7], s46 offen offset:2048 sc1\n"
"buffer_load_dwordx4 v[40:43], v1, s[4:7], s46 offen offset:3072 sc1\n"
"buffer_load_dwordx4 v[44:47], v1, s[4:7], s47 offen offset:0 sc1\n"
"buffer_load_dwordx4 v[48:51], v1, s[4:7], s47 offen offset:1024 sc1\n"
"buffer_load_dwordx4 v[52:55], v1, s[4:7], s47 offen offset:2048 sc1\n"
"buffer_load_dwordx4 v[56:59], v1, s[4:7], s47 offen offset:3072 sc1\n"
"s_mov_b32 s17, 32768\n"
".Ltp_88:\n"
"s_waitcnt vmcnt(0)\n"
"s_sub_u32 s17, s17, 1\n"
"s_cbranch_scc1 .Lou_89\n"
"v_max_u32 v60, v28, v29\n"
"v_max3_u32 v60, v30, v31, v60\n"
"v_max3_u32 v60, v32, v33, v60\n"
"v_max3_u32 v60, v34, v35, v60\n"
"v_max3_u32 v60, v36, v37, v60\n"
"v_max3_u32 v60, v38, v39, v60\n"
"v_max3_u32 v60, v40, v41, v60\n"
"v_max3_u32 v60, v42, v43, v60\n"
"v_max3_u32 v60, v44, v45, v60\n"
"v_max3_u32 v60, v46, v47, v60\n"
"v_max3_u32 v60, v48, v49, v60\n"
"v_max3_u32 v60, v50, v51, v60\n"
"v_max3_u32 v60, v52, v53, v60\n"
"v_max3_u32 v60, v54, v55, v60\n"
"v_max3_u32 v60, v56, v57, v60\n"
"v_max3_u32 v60, v58, v59, v60\n"
"v_cmp_eq_u32 vcc, -1, v60\n"
"s_cbranch_vccz .Lou_89\n"
"s_sleep 4\n"
"v_max_u32 v60, v30, v31\n"
"v_max3_u32 v60, v28, v29, v60\n"
"v_cmp_eq_u32 vcc, -1, v60\n"
"s_cbranch_vccz .Lsk_90\n"
"s_mov_b64 exec, vcc\n"
"buffer_load_dwordx4 v[28:31], v1, s[4:7], s46 offen offset:0 sc1\n"
"s_mov_b64 exec, -1\n"
".Lsk_90:\n"
"v_max_u32 v60, v34, v35\n"
"v_max3_u32 v60, v32, v33, v60\n"
"v_cmp_eq_u32 vcc, -1, v60\n"
"s_cbranch_vccz .Lsk_91\n"
"s_mov_b64 exec, vcc\n"
"buffer_load_dwordx4 v[32:35], v1, s[4:7], s46 offen offset:1024 sc1\n"
"s_mov_b64 exec, -1\n"
".Lsk_91:\n"
"v_max_u32 v60, v38, v39\n"
"v_max3_u32 v60, v36, v37, v60\n"
"v_cmp_eq_u32 vcc, -1, v60\n"
"s_cbranch_vccz .Lsk_92\n"
"s_mov_b64 exec, vcc\n"
"buffer_load_dwordx4 v[36:39], v1, s[4:7], s46 offen offset:2048 sc1\n"
"s_mov_b64 exec, -1\n"
".Lsk_92:\n"
"v_max_u32 v60, v42, v43\n"
"v_max3_u32 v60, v40, v41, v60\n"
"v_cmp_eq_u32 vcc, -1, v60\n"
"s_cbranch_vccz .Lsk_93\n"
"s_mov_b64 exec, vcc\n"
"buffer_load_dwordx4 v[40:43], v1, s[4:7], s46 offen offset:3072 sc1\n"
"s_mov_b64 exec, -1\n"
".Lsk_93:\n"
"v_max_u32 v60, v46, v47\n"
"v_max3_u32 v60, v44, v45, v60\n"
"v_cmp_eq_u32 vcc, -1, v60\n"
"s_cbranch_vccz .Lsk_94\n"
"s_mov_b64 exec, vcc\n"
"buffer_load_dwordx4 v[44:47], v1, s[4:7], s47 offen offset:0 sc1\n"
"s_mov_b64 exec, -1\n"
".Lsk_94:\n"
"v_max_u32 v60, v50, v51\n"
"v_max3_u32 v60, v48, v49, v60\n"
"v_cmp_eq_u32 vcc, -1, v60\n"
"s_cbranch_vccz .Lsk_95\n"
"s_mov_b64 exec, vcc\n"
"buffer_load_dwordx4 v[48:51], v1, s[4:7], s47 offen offset:1024 sc1\n"
"s_mov_b64 exec, -1\n"
".Lsk_95:\n"
"v_max_u32 v60, v54, v55\n"
"v_max3_u32 v60, v52, v53, v60\n"
"v_cmp_eq_u32 vcc, -1, v60\n"
"s_cbranch_vccz .Lsk_96\n"
"s_mov_b64 exec, vcc\n"
"buffer_load_dwordx4 v[52:55], v1, s[4:7], s47 offen offset:2048 sc1\n"
"s_mov_b64 exec, -1\n"
".Lsk_96:\n"
"v_max_u32 v60, v58, v59\n"
"v_max3_u32 v60, v56, v57, v60\n"
"v_cmp_eq_u32 vcc, -1, v60\n"
"s_cbranch_vccz .Lsk_97\n"
"s_mov_b64 exec, vcc\n"
"buffer_load_dwordx4 v[56:59], v1, s[4:7], s47 offen offset:3072 sc1\n"
"s_mov_b64 exec, -1\n"
".Lsk_97:\n"
"s_branch .Ltp_88\n"
".Lou_89:\n"
"s_waitcnt vmcnt(0)\n"
"s_mov_b32 s89, 30\n"
".Ldl_98:\n"
"s_sub_u32 s89, s89, 1\n"
"s_cbranch_scc1 .Lde_99\n"
"s_sleep 1\n"
"s_branch .Ldl_98\n"
".Lde_99:\n"
".Lnw_83:\n"
"v_min_i32 v79, 3, v0\n"
"v_mul_i32_i24 v28, 0x500, v79\n"
"s_and_b32 s79, s2, 7\n"
"s_lshl_b32 s79, s79, 3\n"
"s_add_u32 s79, s79, 7\n"
"s_mul_i32 s79, s79, 0x5000\n"
"s_and_b32 s78, s2, 7\n"
"s_lshr_b32 s78, s78, 1\n"
"s_lshl_b32 s78, s78, 7\n"
"s_add_u32 s79, s79, s78\n"
"s_add_u32 s79, s79, s29\n"
"s_add_u32 s46, s79, 0xb48000\n"
"s_mov_b32 s17, 32768\n"
".Ltp_101:\n"
"buffer_load_dword v29, v28, s[4:7], s46 offen sc1\n"
"s_sub_u32 s17, s17, 1\n"
"s_cbranch_scc1 .Lou_102\n"
"s_waitcnt vmcnt(0)\n"
"v_cmp_eq_u32 vcc, -1, v29\n"
"s_cbranch_vccz .Lou_102\n"
"s_mov_b32 s89, 2\n"
".Ldl_103:\n"
"s_sub_u32 s89, s89, 1\n"
"s_cbranch_scc1 .Lde_104\n"
"s_sleep 1\n"
"s_branch .Ldl_103\n"
".Lde_104:\n"
"s_branch .Ltp_101\n"
".Lou_102:\n"
"s_waitcnt vmcnt(0)\n"
".Lnw_100:\n"
"s_mov_b32 s17, 0x100000\n"
".Ltp_105:\n"
"ds_read_b32 v90, v14 offset:256\n"
"s_sub_u32 s17, s17, 1\n"
"s_cbranch_scc1 .Lou_106\n"
"s_waitcnt lgkmcnt(0)\n"
"v_cmp_eq_u32 vcc, 0, v90\n"
"s_cbranch_vccnz .Ltp_105\n"
".Lou_106:\n"
"s_waitcnt lgkmcnt(0)\n"
"s_mov_b32 s54, 0\n"
"s_nop 1\n"
".Lag_107:\n"
"v_mov_b32 v92, 0\n"
"s_mov_b32 s46, s44\n"
"s_add_u32 s47, s46, 0x1000\n"
"v_add_u32 v2, s29, v1\n"
"buffer_load_dwordx4 v[28:31], v1, s[4:7], s46 offen offset:0 sc1\n"
"buffer_load_dwordx4 v[32:35], v1, s[4:7], s46 offen offset:1024 sc1\n"
"buffer_load_dwordx4 v[36:39], v1, s[4:7], s46 offen offset:2048 sc1\n"
"buffer_load_dwordx4 v[40:43], v1, s[4:7], s46 offen offset:3072 sc1\n"
"buffer_load_dwordx4 v[44:47], v1, s[4:7], s47 offen offset:0 sc1\n"
"s_mov_b32 s17, 32768\n"
".Ltp_109:\n"
"s_waitcnt vmcnt(0)\n"
"s_sub_u32 s17, s17, 1\n"
"s_cbranch_scc1 .Lou_110\n"
"v_max_u32 v48, v28, v29\n"
"v_max3_u32 v48, v30, v31, v48\n"
"v_max3_u32 v48, v32, v33, v48\n"
"v_max3_u32 v48, v34, v35, v48\n"
"v_max3_u32 v48, v36, v37, v48\n"
"v_max3_u32 v48, v38, v39, v48\n"
"v_max3_u32 v48, v40, v41, v48\n"
"v_max3_u32 v48, v42, v43, v48\n"
"v_max3_u32 v48, v44, v45, v48\n"
"v_max3_u32 v48, v46, v47, v48\n"
"v_cmp_eq_u32 vcc, -1, v48\n"
"s_cbranch_vccz .Lou_110\n"
"s_sleep 1\n"
"v_max_u32 v48, v30, v31\n"
"v_max3_u32 v48, v28, v29, v48\n"
"v_cmp_eq_u32 vcc, -1, v48\n"
"s_cbranch_vccz .Lsk_111\n"
"s_mov_b64 exec, vcc\n"
"buffer_load_dwordx4 v[28:31], v1, s[4:7], s46 offen offset:0 sc1\n"
"s_mov_b64 exec, -1\n"
".Lsk_111:\n"
"v_max_u32 v48, v34, v35\n"
"v_max3_u32 v48, v32, v33, v48\n"
"v_cmp_eq_u32 vcc, -1, v48\n"
"s_cbranch_vccz .Lsk_112\n"
"s_mov_b64 exec, vcc\n"
"buffer_load_dwordx4 v[32:35], v1, s[4:7], s46 offen offset:1024 sc1\n"
"s_mov_b64 exec, -1\n"
".Lsk_112:\n"
"v_max_u32 v48, v38, v39\n"
"v_max3_u32 v48, v36, v37, v48\n"
"v_cmp_eq_u32 vcc, -1, v48\n"
"s_cbranch_vccz .Lsk_113\n"
"s_mov_b64 exec, vcc\n"
"buffer_load_dwordx4 v[36:39], v1, s[4:7], s46 offen offset:2048 sc1\n"
"s_mov_b64 exec, -1\n"
".Lsk_113:\n"
"v_max_u32 v48, v42, v43\n"
"v_max3_u32 v48, v40, v41, v48\n"
"v_cmp_eq_u32 vcc, -1, v48\n"
"s_cbranch_vccz .Lsk_114\n"
"s_mov_b64 exec, vcc\n"
"buffer_load_dwordx4 v[40:43], v1, s[4:7], s46 offen offset:3072 sc1\n"
"s_mov_b64 exec, -1\n"
".Lsk_114:\n"
"v_max_u32 v48, v46, v47\n"
"v_max3_u32 v48, v44, v45, v48\n"
"v_cmp_eq_u32 vcc, -1, v48\n"
"s_cbranch_vccz .Lsk_115\n"
"s_mov_b64 exec, vcc\n"
"buffer_load_dwordx4 v[44:47], v1, s[4:7], s47 offen offset:0 sc1\n"
"s_mov_b64 exec, -1\n"
".Lsk_115:\n"
"s_branch .Ltp_109\n"
".Lou_110:\n"
"s_waitcnt vmcnt(0)\n"
"s_add_u32 m0, s29, 0x5000\n"
"s_add_u32 s31, s44, 0x5000\n"
"buffer_load_dwordx4 v1, s[4:7], s31 offen offset:0 sc1 lds\n"
"ds_write_b128 v2, v[28:31] offset:0\n"
"buffer_load_dwordx4 v1, s[4:7], s31 offen offset:1024 sc1 lds\n"
"ds_write_b128 v2, v[32:35] offset:1024\n"
"buffer_load_dwordx4 v1, s[4:7], s31 offen offset:2048 sc1 lds\n"
"ds_write_b128 v2, v[36:39] offset:2048\n"
"buffer_load_dwordx4 v1, s[4:7], s31 offen offset:3072 sc1 lds\n"
"ds_write_b128 v2, v[40:43] offset:3072\n"
"s_add_u32 m0, m0, 1024\n"
"s_add_u32 s31, s31, 1024\n"
"buffer_load_dwordx4 v1, s[4:7], s31 offen offset:3072 sc1 lds\n"
"ds_write_b128 v2, v[44:47] offset:4096\n"
"ds_write_b32 v13, v13 offset:272\n"
"s_mov_b32 s17, 0x100000\n"
".Ltp_116:\n"
"ds_read_b32 v90, v14 offset:272\n"
"s_sub_u32 s17, s17, 1\n"
"s_cbranch_scc1 .Lou_117\n"
"s_waitcnt lgkmcnt(0)\n"
"v_cmp_eq_u32 vcc, 0, v90\n"
"s_cbranch_vccnz .Ltp_116\n"
".Lou_117:\n"
"s_waitcnt lgkmcnt(0)\n"
"ds_read_b64 v[56:57], v3 offset:0\n"
"ds_read_b64 v[58:59], v3 offset:256\n"
"ds_read_b64 v[60:61], v3 offset:1024\n"
"ds_read_b64 v[62:63], v3 offset:1280\n"
"ds_read_b64 v[64:65], v3 offset:2048\n"
"ds_read_b64 v[66:67], v3 offset:2304\n"
"ds_read_b64 v[68:69], v3 offset:3072\n"
"ds_read_b64 v[70:71], v3 offset:3328\n"
"ds_read_b64 v[72:73], v3 offset:4096\n"
"ds_read_b64 v[74:75], v3 offset:4352\n"
"s_waitcnt lgkmcnt(6)\n"
"v_mfma_f32_16x16x32_bf16 v[28:31], v[96:99], v[56:59], 0\n"
"ds_read_b64 v[76:77], v3 offset:5120\n"
"s_add_u32 m0, s29, 0xa000\n"
"v_mfma_f32_16x16x32_bf16 v[32:35], v[100:103], v[56:59], 0\n"
"ds_read_b64 v[78:79], v3 offset:5376\n"
"s_add_u32 s31, s44, 0xa000\n"
"v_mfma_f32_16x16x32_bf16 v[52:55], v[56:59], v[56:59], 0\n"
"v_mfma_f32_16x16x32_bf16 v[28:31], v[104:107], v[60:63], v[28:31]\n"
"ds_read_b64 v[56:57], v3 offset:6144\n"
"buffer_load_dwordx4 v1, s[4:7], s31 offen offset:0 sc1 lds\n"
"v_mfma_f32_16x16x32_bf16 v[32:35], v[108:111], v[60:63], v[32:35]\n"
"ds_read_b64 v[58:59], v3 offset:6400\n"
"s_waitcnt lgkmcnt(6)\n"
"v_mfma_f32_16x16x32_bf16 v[28:31], v[112:115], v[64:67], v[28:31]\n"
"ds_read_b64 v[60:61], v3 offset:7168\n"
"v_mfma_f32_16x16x32_bf16 v[32:35], v[116:119], v[64:67], v[32:35]\n"
"ds_read_b64 v[62:63], v3 offset:7424\n"
"v_mfma_f32_16x16x32_bf16 v[28:31], v[120:123], v[68:71], v[28:31]\n"
"ds_read_b64 v[64:65], v3 offset:8192\n"
"buffer_load_dwordx4 v1, s[4:7], s31 offen offset:1024 sc1 lds\n"
"v_mfma_f32_16x16x32_bf16 v[32:35], v[124:127], v[68:71], v[32:35]\n"
"ds_read_b64 v[66:67], v3 offset:8448\n"
"s_waitcnt lgkmcnt(6)\n"
"v_mfma_f32_16x16x32_bf16 v[28:31], v[128:131], v[72:75], v[28:31]\n"
"ds_read_b64 v[68:69], v3 offset:9216\n"
"v_mfma_f32_16x16x32_bf16 v[32:35], v[132:135], v[72:75], v[32:35]\n"
"ds_read_b64 v[70:71], v3 offset:9472\n"
"v_mfma_f32_16x16x32_bf16 v[52:55], v[72:75], v[72:75], v[52:55]\n"
"buffer_load_dwordx4 v1, s[4:7], s31 offen offset:2048 sc1 lds\n"
"v_mfma_f32_16x16x32_bf16 v[28:31], v[136:139], v[76:79], v[28:31]\n"
"ds_read_b64 v[72:73], v3 offset:10240\n"
"v_mfma_f32_16x16x32_bf16 v[32:35], v[140:143], v[76:79], v[32:35]\n"
"ds_read_b64 v[74:75], v3 offset:10496\n"
"s_waitcnt lgkmcnt(6)\n"
"v_mfma_f32_16x16x32_bf16 v[28:31], v[144:147], v[56:59], v[28:31]\n"
"ds_read_b64 v[76:77], v3 offset:11264\n"
"v_mfma_f32_16x16x32_bf16 v[32:35], v[148:151], v[56:59], v[32:35]\n"
"ds_read_b64 v[78:79], v3 offset:11520\n"
"buffer_load_dwordx4 v1, s[4:7], s31 offen offset:3072 sc1 lds\n"
"v_mfma_f32_16x16x32_bf16 v[28:31], v[152:155], v[60:63], v[28:31]\n"
"ds_read_b64 v[56:57], v3 offset:12288\n"
"s_add_u32 m0, m0, 1024\n"
"v_mfma_f32_16x16x32_bf16 v[32:35], v[156:159], v[60:63], v[32:35]\n"
"ds_read_b64 v[58:59], v3 offset:12544\n"
"s_waitcnt lgkmcnt(6)\n"
"v_mfma_f32_16x16x32_bf16 v[28:31], v[160:163], v[64:67], v[28:31]\n"
"ds_read_b64 v[60:61], v3 offset:13312\n"
"s_add_u32 s31, s31, 1024\n"
"v_mfma_f32_16x16x32_bf16 v[32:35], v[164:167], v[64:67], v[32:35]\n"
"ds_read_b64 v[62:63], v3 offset:13568\n"
"buffer_load_dwordx4 v1, s[4:7], s31 offen offset:3072 sc1 lds\n"
"v_mfma_f32_16x16x32_bf16 v[52:55], v[64:67], v[64:67], v[52:55]\n"
"s_waitcnt vmcnt(5)\n"
"ds_write_b32 v13, v13 offset:288\n"
"v_mfma_f32_16x16x32_bf16 v[28:31], v[168:171], v[68:71], v[28:31]\n"
"ds_read_b64 v[64:65], v3 offset:14336\n"
"s_add_u32 m0, s29, 0xf000\n"
"s_add_u32 s31, s44, 0xf000\n"
"v_mfma_f32_16x16x32_bf16 v[32:35], v[172:175], v[68:71], v[32:35]\n"
"ds_read_b64 v[66:67], v3 offset:14592\n"
"s_waitcnt lgkmcnt(7)\n"
"v_mfma_f32_16x16x32_bf16 v[28:31], v[176:179], v[72:75], v[28:31]\n"
"ds_read_b64 v[68:69], v3 offset:15360\n"
"buffer_load_dwordx4 v1, s[4:7], s31 offen offset:0 sc1 lds\n"
"v_mfma_f32_16x16x32_bf16 v[32:35], v[180:183], v[72:75], v[32:35]\n"
"ds_read_b64 v[70:71], v3 offset:15616\n"
"v_mfma_f32_16x16x32_bf16 v[28:31], v[184:187], v[76:79], v[28:31]\n"
"ds_read_b64 v[72:73], v3 offset:16384\n"
"v_mfma_f32_16x16x32_bf16 v[32:35], v[188:191], v[76:79], v[32:35]\n"
"ds_read_b64 v[74:75], v3 offset:16640\n"
"s_waitcnt lgkmcnt(7)\n"
"v_mfma_f32_16x16x32_bf16 v[28:31], v[192:195], v[56:59], v[28:31]\n"
"ds_read_b64 v[76:77], v4 offset:0\n"
"buffer_load_dwordx4 v1, s[4:7], s31 offen offset:1024 sc1 lds\n"
"v_mfma_f32_16x16x32_bf16 v[32:35], v[196:199], v[56:59], v[32:35]\n"
"ds_read_b64 v[78:79], v4 offset:256\n"
"ds_read_b32 v90, v14 offset:288\n"
"v_mfma_f32_16x16x32_bf16 v[52:55], v[56:59], v[56:59], v[52:55]\n"
"v_mfma_f32_16x16x32_bf16 v[28:31], v[200:203], v[60:63], v[28:31]\n"
"ds_read_b64 v[56:57], v5 offset:0\n"
"v_mfma_f32_16x16x32_bf16 v[32:35], v[204:207], v[60:63], v[32:35]\n"
"ds_read_b64 v[58:59], v5 offset:256\n"
"buffer_load_dwordx4 v1, s[4:7], s31 offen offset:2048 sc1 lds\n"
"s_waitcnt lgkmcnt(7)\n"
"v_mfma_f32_16x16x32_bf16 v[28:31], v[208:211], v[64:67], v[28:31]\n"
"ds_read_b64 v[60:61], v6 offset:0\n"
"s_waitcnt lgkmcnt(3)\n"
"v_cmp_eq_u32 vcc, 0, v90\n"
"v_mfma_f32_16x16x32_bf16 v[32:35], v[212:215], v[64:67], v[32:35]\n"
"ds_read_b64 v[62:63], v6 offset:256\n"
"v_mfma_f32_16x16x32_bf16 v[28:31], v[216:219], v[68:71], v[28:31]\n"
"s_cbranch_vccnz .Lsl_118\n"
".Lbk_119:\n"
"ds_read_b64 v[64:65], v3 offset:20480\n"
"v_mfma_f32_16x16x32_bf16 v[32:35], v[220:223], v[68:71], v[32:35]\n"
"ds_read_b64 v[66:67], v3 offset:20736\n"
"buffer_load_dwordx4 v1, s[4:7], s31 offen offset:3072 sc1 lds\n"
"v_mfma_f32_16x16x32_bf16 v[28:31], v[224:227], v[72:75], v[28:31]\n"
"ds_read_b64 v[68:69], v3 offset:21504\n"
"s_add_u32 m0, m0, 1024\n"
"v_mfma_f32_16x16x32_bf16 v[32:35], v[228:231], v[72:75], v[32:35]\n"
"ds_read_b64 v[70:71], v3 offset:21760\n"
"s_add_u32 s31, s31, 1024\n"
"v_mfma_f32_16x16x32_bf16 v[52:55], v[72:75], v[72:75], v[52:55]\n"
"v_mfma_f32_16x16x32_bf16 v[28:31], v[232:235], v[76:79], v[28:31]\n"
"ds_read_b64 v[72:73], v3 offset:22528\n"
"buffer_load_dwordx4 v1, s[4:7], s31 offen offset:3072 sc1 lds\n"
"v_mfma_f32_16x16x32_bf16 v[32:35], v[236:239], v[76:79], v[32:35]\n"
"ds_read_b64 v[74:75], v3 offset:22784\n"
"s_waitcnt lgkmcnt(6)\n"
"v_mfma_f32_16x16x32_bf16 v[28:31], v[240:243], v[56:59], v[28:31]\n"
"ds_read_b64 v[76:77], v3 offset:23552\n"
"v_cndmask_b32 v94, v52, v53, s[34:35]\n"
"v_mfma_f32_16x16x32_bf16 v[32:35], v[244:247], v[56:59], v[32:35]\n"
"ds_read_b64 v[78:79], v3 offset:23808\n"
"v_cndmask_b32 v54, v54, v55, s[34:35]\n"
"v_mfma_f32_16x16x32_bf16 v[28:31], v[248:251], v[60:63], v[28:31]\n"
"ds_read_b64 v[56:57], v3 offset:24576\n"
"v_cndmask_b32 v94, v94, v54, s[20:21]\n"
"v_mfma_f32_16x16x32_bf16 v[32:35], v[252:255], v[60:63], v[32:35]\n"
"ds_read_b64 v[58:59], v3 offset:24832\n"
"s_waitcnt lgkmcnt(6)\n"
"v_mfma_f32_16x16x32_bf16 v[36:39], v[96:99], v[64:67], 0\n"
"ds_read_b64 v[60:61], v3 offset:25600\n"
"v_mfma_f32_16x16x32_bf16 v[40:43], v[100:103], v[64:67], 0\n"
"ds_read_b64 v[62:63], v3 offset:25856\n"
"v_cndmask_b32 v94, v28, v94, s[22:23]\n"
"s_waitcnt vmcnt(5)\n"
"v_mfma_f32_16x16x32_bf16 v[52:55], v[64:67], v[64:67], 0\n"
"ds_write_b32 v9, v94 offset:1856\n"
"v_mfma_f32_16x16x32_bf16 v[36:39], v[104:107], v[68:71], v[36:39]\n"
"ds_read_b64 v[64:65], v3 offset:26624\n"
"v_mfma_f32_16x16x32_bf16 v[40:43], v[108:111], v[68:71], v[40:43]\n"
"ds_read_b64 v[66:67], v3 offset:26880\n"
"s_waitcnt lgkmcnt(7)\n"
"v_mfma_f32_16x16x32_bf16 v[36:39], v[112:115], v[72:75], v[36:39]\n"
"ds_read_b64 v[68:69], v3 offset:27648\n"
"v_mfma_f32_16x16x32_bf16 v[40:43], v[116:119], v[72:75], v[40:43]\n"
"ds_read_b64 v[70:71], v3 offset:27904\n"
"v_mfma_f32_16x16x32_bf16 v[36:39], v[120:123], v[76:79], v[36:39]\n"
"ds_read_b64 v[72:73], v3 offset:28672\n"
"v_mfma_f32_16x16x32_bf16 v[40:43], v[124:127], v[76:79], v[40:43]\n"
"ds_read_b64 v[74:75], v3 offset:28928\n"
"s_waitcnt lgkmcnt(7)\n"
"v_mfma_f32_16x16x32_bf16 v[36:39], v[128:131], v[56:59], v[36:39]\n"
"ds_read_b64 v[76:77], v3 offset:29696\n"
"v_mfma_f32_16x16x32_bf16 v[40:43], v[132:135], v[56:59], v[40:43]\n"
"ds_read_b64 v[78:79], v3 offset:29952\n"
"v_mfma_f32_16x16x32_bf16 v[52:55], v[56:59], v[56:59], v[52:55]\n"
"v_mfma_f32_16x16x32_bf16 v[36:39], v[136:139], v[60:63], v[36:39]\n"
"ds_read_b64 v[56:57], v3 offset:30720\n"
"v_mfma_f32_16x16x32_bf16 v[40:43], v[140:143], v[60:63], v[40:43]\n"
"ds_read_b64 v[58:59], v3 offset:30976\n"
"s_waitcnt lgkmcnt(6)\n"
"v_mfma_f32_16x16x32_bf16 v[36:39], v[144:147], v[64:67], v[36:39]\n"
"ds_read_b64 v[60:61], v3 offset:31744\n"
"ds_read_b128 v[84:87], v11 offset:1856\n"
"ds_read_b32 v91, v12 offset:1856\n"
"v_mfma_f32_16x16x32_bf16 v[40:43], v[148:151], v[64:67], v[40:43]\n"
"ds_read_b64 v[62:63], v3 offset:32000\n"
"v_mfma_f32_16x16x32_bf16 v[36:39], v[152:155], v[68:71], v[36:39]\n"
"ds_read_b64 v[64:65], v3 offset:32768\n"
"v_mfma_f32_16x16x32_bf16 v[40:43], v[156:159], v[68:71], v[40:43]\n"
"ds_read_b64 v[66:67], v3 offset:33024\n"
"s_waitcnt lgkmcnt(8)\n"
"v_mfma_f32_16x16x32_bf16 v[36:39], v[160:163], v[72:75], v[36:39]\n"
"ds_read_b64 v[68:69], v3 offset:33792\n"
"v_mfma_f32_16x16x32_bf16 v[40:43], v[164:167], v[72:75], v[40:43]\n"
"ds_read_b64 v[70:71], v3 offset:34048\n"
"v_mfma_f32_16x16x32_bf16 v[52:55], v[72:75], v[72:75], v[52:55]\n"
"v_mfma_f32_16x16x32_bf16 v[36:39], v[168:171], v[76:79], v[36:39]\n"
"ds_read_b64 v[72:73], v3 offset:34816\n"
"s_waitcnt lgkmcnt(7)\n"
"v_add_f32 v84, v84, v85\n"
"v_add_f32 v86, v86, v87\n"
"v_mfma_f32_16x16x32_bf16 v[40:43], v[172:175], v[76:79], v[40:43]\n"
"ds_read_b64 v[74:75], v3 offset:35072\n"
"v_add_f32 v84, v84, v86\n"
"s_waitcnt lgkmcnt(6)\n"
"v_mfma_f32_16x16x32_bf16 v[36:39], v[176:179], v[56:59], v[36:39]\n"
"ds_read_b64 v[76:77], v3 offset:35840\n"
"v_mul_f32 v89, 0x3acccccd, v91\n"
"v_fma_f32 v84, -v91, v89, v84\n"
"v_mfma_f32_16x16x32_bf16 v[40:43], v[180:183], v[56:59], v[40:43]\n"
"ds_read_b64 v[78:79], v3 offset:36096\n"
"v_fmamk_f32 v84, v84, 0x3acccccd, v27\n"
"v_mfma_f32_16x16x32_bf16 v[36:39], v[184:187], v[60:63], v[36:39]\n"
"ds_read_b64 v[56:57], v3 offset:36864\n"
"v_cmp_u_f32 vcc, v84, v84\n"
"v_mfma_f32_16x16x32_bf16 v[40:43], v[188:191], v[60:63], v[40:43]\n"
"ds_read_b64 v[58:59], v3 offset:37120\n"
"s_waitcnt lgkmcnt(6)\n"
"v_mfma_f32_16x16x32_bf16 v[36:39], v[192:195], v[64:67], v[36:39]\n"
"ds_read_b64 v[60:61], v4 offset:20480\n"
"v_max_f32 v84, v84, v27\n"
"v_mfma_f32_16x16x32_bf16 v[40:43], v[196:199], v[64:67], v[40:43]\n"
"ds_read_b64 v[62:63], v4 offset:20736\n"
"v_rsq_f32 v88, v84\n"
"v_mfma_f32_16x16x32_bf16 v[52:55], v[64:67], v[64:67], v[52:55]\n"
"v_mul_f32 v89, -v89, v88\n"
"v_mfma_f32_16x16x32_bf16 v[36:39], v[200:203], v[68:71], v[36:39]\n"
"ds_read_b64 v[64:65], v5 offset:20480\n"
"s_cbranch_vccnz .Lsl_122\n"
".Lbk_123:\n"
"v_mfma_f32_16x16x32_bf16 v[40:43], v[204:207], v[68:71], v[40:43]\n"
"ds_read_b64 v[66:67], v5 offset:20736\n"
"s_add_u32 m0, s29, 0\n"
"s_waitcnt lgkmcnt(6)\n"
"v_mfma_f32_16x16x32_bf16 v[36:39], v[208:211], v[72:75], v[36:39]\n"
"ds_read_b64 v[68:69], v6 offset:20480\n"
"s_sub_u32 s79, s44, 0x5000\n"
"s_add_u32 s78, s29, 0xb48000\n"
"v_mfma_f32_16x16x32_bf16 v[40:43], v[212:215], v[72:75], v[40:43]\n"
"ds_read_b64 v[70:71], v6 offset:20736\n"
"s_max_i32 s31, s79, s78\n"
"v_mul_f32 v28, v88, v28\n"
"v_mfma_f32_16x16x32_bf16 v[36:39], v[216:219], v[76:79], v[36:39]\n"
"ds_read_b64 v[72:73], v3 offset:40960\n"
"buffer_load_dwordx4 v1, s[4:7], s31 offen offset:0 sc1 lds\n"
"v_mul_f32 v29, v88, v29\n"
"v_mfma_f32_16x16x32_bf16 v[40:43], v[220:223], v[76:79], v[40:43]\n"
"ds_read_b64 v[74:75], v3 offset:41216\n"
"s_waitcnt lgkmcnt(6)\n"
"v_mfma_f32_16x16x32_bf16 v[36:39], v[224:227], v[56:59], v[36:39]\n"
"ds_read_b64 v[76:77], v3 offset:41984\n"
"v_mul_f32 v30, v88, v30\n"
"v_mfma_f32_16x16x32_bf16 v[40:43], v[228:231], v[56:59], v[40:43]\n"
"ds_read_b64 v[78:79], v3 offset:42240\n"
"v_mul_f32 v31, v88, v31\n"
"v_mfma_f32_16x16x32_bf16 v[52:55], v[56:59], v[56:59], v[52:55]\n"
"buffer_load_dwordx4 v1, s[4:7], s31 offen offset:1024 sc1 lds\n"
"v_fmac_f32 v28, v89, v16\n"
"v_mfma_f32_16x16x32_bf16 v[36:39], v[232:235], v[60:63], v[36:39]\n"
"ds_read_b64 v[56:57], v3 offset:43008\n"
"v_fmac_f32 v29, v89, v17\n"
"v_mfma_f32_16x16x32_bf16 v[40:43], v[236:239], v[60:63], v[40:43]\n"
"ds_read_b64 v[58:59], v3 offset:43264\n"
"s_waitcnt lgkmcnt(6)\n"
"v_mfma_f32_16x16x32_bf16 v[36:39], v[240:243], v[64:67], v[36:39]\n"
"ds_read_b64 v[60:61], v3 offset:44032\n"
"v_cndmask_b32 v94, v52, v53, s[34:35]\n"
"v_mfma_f32_16x16x32_bf16 v[40:43], v[244:247], v[64:67], v[40:43]\n"
"ds_read_b64 v[62:63], v3 offset:44288\n"
"v_cndmask_b32 v54, v54, v55, s[34:35]\n"
"buffer_load_dwordx4 v1, s[4:7], s31 offen offset:2048 sc1 lds\n"
"v_mfma_f32_16x16x32_bf16 v[36:39], v[248:251], v[68:71], v[36:39]\n"
"ds_read_b64 v[64:65], v3 offset:45056\n"
"v_cndmask_b32 v94, v94, v54, s[20:21]\n"
"v_mfma_f32_16x16x32_bf16 v[40:43], v[252:255], v[68:71], v[40:43]\n"
"ds_read_b64 v[66:67], v3 offset:45312\n"
"s_waitcnt lgkmcnt(6)\n"
"v_mfma_f32_16x16x32_bf16 v[44:47], v[96:99], v[72:75], 0\n"
"ds_read_b64 v[68:69], v3 offset:46080\n"
"v_fmac_f32 v30, v89, v18\n"
"v_mfma_f32_16x16x32_bf16 v[48:51], v[100:103], v[72:75], 0\n"
"ds_read_b64 v[70:71], v3 offset:46336\n"
"v_cndmask_b32 v94, v36, v94, s[22:23]\n"
"s_waitcnt vmcnt(3)\n"
"buffer_load_dwordx4 v1, s[4:7], s31 offen offset:3072 sc1 lds\n"
"v_mfma_f32_16x16x32_bf16 v[52:55], v[72:75], v[72:75], 0\n"
"ds_write_b32 v9, v94 offset:2176\n"
"s_add_u32 m0, m0, 1024\n"
"v_mfma_f32_16x16x32_bf16 v[44:47], v[104:107], v[76:79], v[44:47]\n"
"ds_read_b64 v[72:73], v3 offset:47104\n"
"s_add_u32 s31, s31, 1024\n"
"v_mfma_f32_16x16x32_bf16 v[48:51], v[108:111], v[76:79], v[48:51]\n"
"ds_read_b64 v[74:75], v3 offset:47360\n"
"s_waitcnt lgkmcnt(7)\n"
"v_mfma_f32_16x16x32_bf16 v[44:47], v[112:115], v[56:59], v[44:47]\n"
"ds_read_b64 v[76:77], v3 offset:48128\n"
"buffer_load_dwordx4 v1, s[4:7], s31 offen offset:3072 sc1 lds\n"
"v_mfma_f32_16x16x32_bf16 v[48:51], v[116:119], v[56:59], v[48:51]\n"
"ds_read_b64 v[78:79], v3 offset:48384\n"
"v_fmac_f32 v31, v89, v19\n"
"v_mfma_f32_16x16x32_bf16 v[44:47], v[120:123], v[60:63], v[44:47]\n"
"ds_read_b64 v[56:57], v3 offset:49152\n"
"v_cvt_pk_bf16_f32 v28, v28, v29\n"
"v_mfma_f32_16x16x32_bf16 v[48:51], v[124:127], v[60:63], v[48:51]\n"
"ds_read_b64 v[58:59], v3 offset:49408\n"
"s_waitcnt lgkmcnt(7)\n"
"v_mfma_f32_16x16x32_bf16 v[44:47], v[128:131], v[64:67], v[44:47]\n"
"ds_read_b64 v[60:61], v3 offset:50176\n"
"v_cvt_pk_bf16_f32 v29, v30, v31\n"
"v_mfma_f32_16x16x32_bf16 v[48:51], v[132:135], v[64:67], v[48:51]\n"
"ds_read_b64 v[62:63], v3 offset:50432\n"
"v_mul_f32 v32, v88, v32\n"
"v_mfma_f32_16x16x32_bf16 v[52:55], v[64:67], v[64:67], v[52:55]\n"
"v_mul_f32 v33, v88, v33\n"
"v_mul_f32 v34, v88, v34\n"
"v_mfma_f32_16x16x32_bf16 v[44:47], v[136:139], v[68:71], v[44:47]\n"
"ds_read_b64 v[64:65], v3 offset:51200\n"
"v_mul_f32 v35, v88, v35\n"
"v_mfma_f32_16x16x32_bf16 v[48:51], v[140:143], v[68:71], v[48:51]\n"
"ds_read_b64 v[66:67], v3 offset:51456\n"
"s_waitcnt lgkmcnt(6)\n"
"v_mfma_f32_16x16x32_bf16 v[44:47], v[144:147], v[72:75], v[44:47]\n"
"ds_read_b64 v[68:69], v3 offset:52224\n"
"ds_read_b128 v[84:87], v11 offset:2176\n"
"ds_read_b32 v91, v12 offset:2176\n"
"v_mfma_f32_16x16x32_bf16 v[48:51], v[148:151], v[72:75], v[48:51]\n"
"ds_read_b64 v[70:71], v3 offset:52480\n"
"v_fmac_f32 v32, v89, v20\n"
"v_mfma_f32_16x16x32_bf16 v[44:47], v[152:155], v[76:79], v[44:47]\n"
"ds_read_b64 v[72:73], v3 offset:53248\n"
"v_fmac_f32 v33, v89, v21\n"
"v_mfma_f32_16x16x32_bf16 v[48:51], v[156:159], v[76:79], v[48:51]\n"
"ds_read_b64 v[74:75], v3 offset:53504\n"
"s_waitcnt lgkmcnt(8)\n"
"v_mfma_f32_16x16x32_bf16 v[44:47], v[160:163], v[56:59], v[44:47]\n"
"ds_read_b64 v[76:77], v3 offset:54272\n"
"v_fmac_f32 v34, v89, v22\n"
"v_mfma_f32_16x16x32_bf16 v[48:51], v[164:167], v[56:59], v[48:51]\n"
"ds_read_b64 v[78:79], v3 offset:54528\n"
"v_fmac_f32 v35, v89, v23\n"
"v_mfma_f32_16x16x32_bf16 v[52:55], v[56:59], v[56:59], v[52:55]\n"
"v_cvt_pk_bf16_f32 v32, v32, v33\n"
"v_cvt_pk_bf16_f32 v33, v34, v35\n"
"v_mfma_f32_16x16x32_bf16 v[44:47], v[168:171], v[60:63], v[44:47]\n"
"ds_read_b64 v[56:57], v3 offset:55296\n"
"s_waitcnt lgkmcnt(7)\n"
"v_add_f32 v84, v84, v85\n"
"v_add_f32 v86, v86, v87\n"
"v_mfma_f32_16x16x32_bf16 v[48:51], v[172:175], v[60:63], v[48:51]\n"
"ds_read_b64 v[58:59], v3 offset:55552\n"
"v_add_f32 v84, v84, v86\n"
"s_waitcnt lgkmcnt(6)\n"
"v_mfma_f32_16x16x32_bf16 v[44:47], v[176:179], v[64:67], v[44:47]\n"
"ds_read_b64 v[60:61], v3 offset:56320\n"
"v_mul_f32 v89, 0x3acccccd, v91\n"
"v_fma_f32 v84, -v91, v89, v84\n"
"v_mfma_f32_16x16x32_bf16 v[48:51], v[180:183], v[64:67], v[48:51]\n"
"ds_read_b64 v[62:63], v3 offset:56576\n"
"v_fmamk_f32 v84, v84, 0x3acccccd, v27\n"
"v_mfma_f32_16x16x32_bf16 v[44:47], v[184:187], v[68:71], v[44:47]\n"
"ds_read_b64 v[64:65], v3 offset:57344\n"
"v_cmp_u_f32 vcc, v84, v84\n"
"v_mfma_f32_16x16x32_bf16 v[48:51], v[188:191], v[68:71], v[48:51]\n"
"ds_read_b64 v[66:67], v3 offset:57600\n"
"s_waitcnt lgkmcnt(6)\n"
"v_mfma_f32_16x16x32_bf16 v[44:47], v[192:195], v[72:75], v[44:47]\n"
"ds_read_b64 v[68:69], v4 offset:40960\n"
"v_max_f32 v84, v84, v27\n"
"v_mfma_f32_16x16x32_bf16 v[48:51], v[196:199], v[72:75], v[48:51]\n"
"ds_read_b64 v[70:71], v4 offset:41216\n"
"v_rsq_f32 v88, v84\n"
"v_mfma_f32_16x16x32_bf16 v[52:55], v[72:75], v[72:75], v[52:55]\n"
"v_mul_f32 v89, -v89, v88\n"
"v_mfma_f32_16x16x32_bf16 v[44:47], v[200:203], v[76:79], v[44:47]\n"
"ds_read_b64 v[72:73], v5 offset:40960\n"
"s_cbranch_vccnz .Lsl_126\n"
".Lbk_127:\n"
"v_mfma_f32_16x16x32_bf16 v[48:51], v[204:207], v[76:79], v[48:51]\n"
"ds_read_b64 v[74:75], v5 offset:41216\n"
"ds_write_b64 v7, v[28:29] offset:3968\n"
"s_waitcnt lgkmcnt(7)\n"
"v_mfma_f32_16x16x32_bf16 v[44:47], v[208:211], v[56:59], v[44:47]\n"
"ds_read_b64 v[76:77], v6 offset:40960\n"
"v_mul_f32 v36, v88, v36\n"
"v_mfma_f32_16x16x32_bf16 v[48:51], v[212:215], v[56:59], v[48:51]\n"
"ds_read_b64 v[78:79], v6 offset:41216\n"
"v_mul_f32 v37, v88, v37\n"
"v_mfma_f32_16x16x32_bf16 v[44:47], v[216:219], v[60:63], v[44:47]\n"
"v_add_u32 v3, 0x5000, v3\n"
"ds_read_b64 v[56:57], v3 offset:40960\n"
"v_mfma_f32_16x16x32_bf16 v[48:51], v[220:223], v[60:63], v[48:51]\n"
"ds_read_b64 v[58:59], v3 offset:41216\n"
"s_waitcnt lgkmcnt(7)\n"
"v_mfma_f32_16x16x32_bf16 v[44:47], v[224:227], v[64:67], v[44:47]\n"
"ds_read_b64 v[60:61], v3 offset:41984\n"
"v_mul_f32 v38, v88, v38\n"
"v_mfma_f32_16x16x32_bf16 v[48:51], v[228:231], v[64:67], v[48:51]\n"
"ds_read_b64 v[62:63], v3 offset:42240\n"
"v_mov_b32 v84, 0x3f803f80\n"
"v_mfma_f32_16x16x32_bf16 v[52:55], v[64:67], v[64:67], v[52:55]\n"
"v_mov_b32 v85, 0x3f803f80\n"
"ds_write_b64 v8, v[32:33] offset:3968\n"
"v_mfma_f32_16x16x32_bf16 v[44:47], v[232:235], v[68:71], v[44:47]\n"
"ds_read_b64 v[64:65], v3 offset:43008\n"
"v_mul_f32 v39, v88, v39\n"
"v_mfma_f32_16x16x32_bf16 v[48:51], v[236:239], v[68:71], v[48:51]\n"
"ds_read_b64 v[66:67], v3 offset:43264\n"
"s_waitcnt lgkmcnt(7)\n"
"v_mfma_f32_16x16x32_bf16 v[44:47], v[240:243], v[72:75], v[44:47]\n"
"ds_read_b64 v[68:69], v3 offset:44032\n"
"v_cndmask_b32 v94, v52, v53, s[34:35]\n"
"v_mfma_f32_16x16x32_bf16 v[48:51], v[244:247], v[72:75], v[48:51]\n"
"ds_read_b64 v[70:71], v3 offset:44288\n"
"v_cndmask_b32 v54, v54, v55, s[34:35]\n"
"v_mfma_f32_16x16x32_bf16 v[44:47], v[248:251], v[76:79], v[44:47]\n"
"ds_read_b64 v[72:73], v3 offset:45056\n"
"v_cndmask_b32 v94, v94, v54, s[20:21]\n"
"v_mfma_f32_16x16x32_bf16 v[48:51], v[252:255], v[76:79], v[48:51]\n"
"ds_read_b64 v[74:75], v3 offset:45312\n"
"s_waitcnt lgkmcnt(7)\n"
"v_mfma_f32_16x16x32_bf16 v[28:31], v[96:99], v[56:59], 0\n"
"ds_read_b64 v[76:77], v3 offset:46080\n"
"v_fmac_f32 v36, v89, v16\n"
"v_mfma_f32_16x16x32_bf16 v[32:35], v[100:103], v[56:59], 0\n"
"ds_read_b64 v[78:79], v3 offset:46336\n"
"v_cndmask_b32 v94, v44, v94, s[22:23]\n"
"s_waitcnt vmcnt(0)\n"
"ds_write_b64 v93, v[84:85] offset:0\n"
"v_mfma_f32_16x16x32_bf16 v[52:55], v[56:59], v[56:59], 0\n"
"ds_write_b32 v9, v94 offset:2496\n"
"v_mfma_f32_16x16x32_bf16 v[28:31], v[104:107], v[60:63], v[28:31]\n"
"ds_read_b64 v[56:57], v3 offset:47104\n"
"v_fmac_f32 v37, v89, v17\n"
"v_mfma_f32_16x16x32_bf16 v[32:35], v[108:111], v[60:63], v[32:35]\n"
"ds_read_b64 v[58:59], v3 offset:47360\n"
"s_waitcnt lgkmcnt(8)\n"
"v_mfma_f32_16x16x32_bf16 v[28:31], v[112:115], v[64:67], v[28:31]\n"
"ds_read_b64 v[60:61], v3 offset:48128\n"
"v_fmac_f32 v38, v89, v18\n"
"v_mfma_f32_16x16x32_bf16 v[32:35], v[116:119], v[64:67], v[32:35]\n"
"ds_read_b64 v[62:63], v3 offset:48384\n"
"v_fmac_f32 v39, v89, v19\n"
"v_mfma_f32_16x16x32_bf16 v[28:31], v[120:123], v[68:71], v[28:31]\n"
"ds_read_b64 v[64:65], v3 offset:49152\n"
"v_cvt_pk_bf16_f32 v36, v36, v37\n"
"v_mfma_f32_16x16x32_bf16 v[32:35], v[124:127], v[68:71], v[32:35]\n"
"ds_read_b64 v[66:67], v3 offset:49408\n"
"s_waitcnt lgkmcnt(8)\n"
"v_mfma_f32_16x16x32_bf16 v[28:31], v[128:131], v[72:75], v[28:31]\n"
"ds_read_b64 v[68:69], v3 offset:50176\n"
"v_cvt_pk_bf16_f32 v37, v38, v39\n"
"v_mfma_f32_16x16x32_bf16 v[32:35], v[132:135], v[72:75], v[32:35]\n"
"ds_read_b64 v[70:71], v3 offset:50432\n"
"v_mul_f32 v40, v88, v40\n"
"v_mfma_f32_16x16x32_bf16 v[52:55], v[72:75], v[72:75], v[52:55]\n"
"v_mul_f32 v41, v88, v41\n"
"v_mul_f32 v42, v88, v42\n"
"v_mfma_f32_16x16x32_bf16 v[28:31], v[136:139], v[76:79], v[28:31]\n"
"ds_read_b64 v[72:73], v3 offset:51200\n"
"v_mul_f32 v43, v88, v43\n"
"v_mfma_f32_16x16x32_bf16 v[32:35], v[140:143], v[76:79], v[32:35]\n"
"ds_read_b64 v[74:75], v3 offset:51456\n"
"s_waitcnt lgkmcnt(6)\n"
"v_mfma_f32_16x16x32_bf16 v[28:31], v[144:147], v[56:59], v[28:31]\n"
"ds_read_b64 v[76:77], v3 offset:52224\n"
"ds_read_b128 v[84:87], v11 offset:2496\n"
"ds_read_b32 v91, v12 offset:2496\n"
"v_mfma_f32_16x16x32_bf16 v[32:35], v[148:151], v[56:59], v[32:35]\n"
"ds_read_b64 v[78:79], v3 offset:52480\n"
"v_fmac_f32 v40, v89, v20\n"
"v_mfma_f32_16x16x32_bf16 v[28:31], v[152:155], v[60:63], v[28:31]\n"
"ds_read_b64 v[56:57], v3 offset:53248\n"
"v_fmac_f32 v41, v89, v21\n"
"v_mfma_f32_16x16x32_bf16 v[32:35], v[156:159], v[60:63], v[32:35]\n"
"ds_read_b64 v[58:59], v3 offset:53504\n"
"s_waitcnt lgkmcnt(8)\n"
"v_mfma_f32_16x16x32_bf16 v[28:31], v[160:163], v[64:67], v[28:31]\n"
"ds_read_b64 v[60:61], v3 offset:54272\n"
"v_fmac_f32 v42, v89, v22\n"
"v_mfma_f32_16x16x32_bf16 v[32:35], v[164:167], v[64:67], v[32:35]\n"
"ds_read_b64 v[62:63], v3 offset:54528\n"
"v_fmac_f32 v43, v89, v23\n"
"v_mfma_f32_16x16x32_bf16 v[52:55], v[64:67], v[64:67], v[52:55]\n"
"v_cvt_pk_bf16_f32 v40, v40, v41\n"
"v_cvt_pk_bf16_f32 v41, v42, v43\n"
"v_mfma_f32_16x16x32_bf16 v[28:31], v[168:171], v[68:71], v[28:31]\n"
"ds_read_b64 v[64:65], v3 offset:55296\n"
"s_waitcnt lgkmcnt(7)\n"
"v_add_f32 v84, v84, v85\n"
"v_add_f32 v86, v86, v87\n"
"v_mfma_f32_16x16x32_bf16 v[32:35], v[172:175], v[68:71], v[32:35]\n"
"ds_read_b64 v[66:67], v3 offset:55552\n"
"v_add_f32 v84, v84, v86\n"
"s_waitcnt lgkmcnt(6)\n"
"v_mfma_f32_16x16x32_bf16 v[28:31], v[176:179], v[72:75], v[28:31]\n"
"ds_read_b64 v[68:69], v3 offset:56320\n"
"v_mul_f32 v89, 0x3acccccd, v91\n"
"v_fma_f32 v84, -v91, v89, v84\n"
"v_mfma_f32_16x16x32_bf16 v[32:35], v[180:183], v[72:75], v[32:35]\n"
"ds_read_b64 v[70:71], v3 offset:56576\n"
"v_fmamk_f32 v84, v84, 0x3acccccd, v27\n"
"v_mfma_f32_16x16x32_bf16 v[28:31], v[184:187], v[76:79], v[28:31]\n"
"ds_read_b64 v[72:73], v3 offset:57344\n"
"v_cmp_u_f32 vcc, v84, v84\n"
"v_mfma_f32_16x16x32_bf16 v[32:35], v[188:191], v[76:79], v[32:35]\n"
"ds_read_b64 v[74:75], v3 offset:57600\n"
"s_waitcnt lgkmcnt(6)\n"
"v_mfma_f32_16x16x32_bf16 v[28:31], v[192:195], v[56:59], v[28:31]\n"
"ds_read_b64 v[76:77], v4 offset:61440\n"
"v_max_f32 v84, v84, v27\n"
"v_mfma_f32_16x16x32_bf16 v[32:35], v[196:199], v[56:59], v[32:35]\n"
"ds_read_b64 v[78:79], v4 offset:61696\n"
"v_rsq_f32 v88, v84\n"
"v_mfma_f32_16x16x32_bf16 v[52:55], v[56:59], v[56:59], v[52:55]\n"
"v_mul_f32 v89, -v89, v88\n"
"v_mfma_f32_16x16x32_bf16 v[28:31], v[200:203], v[60:63], v[28:31]\n"
"ds_read_b64 v[56:57], v5 offset:61440\n"
"s_cbranch_vccnz .Lsl_130\n"
".Lbk_131:\n"
"v_mfma_f32_16x16x32_bf16 v[32:35], v[204:207], v[60:63], v[32:35]\n"
"ds_read_b64 v[58:59], v5 offset:61696\n"
"ds_write_b64 v7, v[36:37] offset:7936\n"
"s_waitcnt lgkmcnt(7)\n"
"v_mfma_f32_16x16x32_bf16 v[28:31], v[208:211], v[64:67], v[28:31]\n"
"ds_read_b64 v[60:61], v6 offset:61440\n"
"v_mul_f32 v44, v88, v44\n"
"v_mul_f32 v45, v88, v45\n"
"v_mfma_f32_16x16x32_bf16 v[32:35], v[212:215], v[64:67], v[32:35]\n"
"ds_read_b64 v[62:63], v6 offset:61696\n"
"v_mul_f32 v46, v88, v46\n"
"v_mul_f32 v47, v88, v47\n"
"v_mfma_f32_16x16x32_bf16 v[28:31], v[216:219], v[68:71], v[28:31]\n"
"v_add_u32 v3, 0xffffb000, v3\n"
"ds_read_b64 v[64:65], v3 offset:0\n"
"v_fmac_f32 v44, v89, v16\n"
"v_mfma_f32_16x16x32_bf16 v[32:35], v[220:223], v[68:71], v[32:35]\n"
"ds_read_b64 v[66:67], v3 offset:256\n"
"v_fmac_f32 v45, v89, v17\n"
"s_waitcnt lgkmcnt(7)\n"
"v_mfma_f32_16x16x32_bf16 v[28:31], v[224:227], v[72:75], v[28:31]\n"
"ds_read_b64 v[68:69], v3 offset:4096\n"
"v_fmac_f32 v46, v89, v18\n"
"v_fmac_f32 v47, v89, v19\n"
"v_mfma_f32_16x16x32_bf16 v[32:35], v[228:231], v[72:75], v[32:35]\n"
"ds_read_b64 v[70:71], v3 offset:4352\n"
"v_cvt_pk_bf16_f32 v44, v44, v45\n"
"v_cvt_pk_bf16_f32 v45, v46, v47\n"
"v_mfma_f32_16x16x32_bf16 v[52:55], v[72:75], v[72:75], v[52:55]\n"
"ds_write_b64 v8, v[40:41] offset:7936\n"
"v_mfma_f32_16x16x32_bf16 v[28:31], v[232:235], v[76:79], v[28:31]\n"
"ds_read_b64 v[72:73], v3 offset:8192\n"
"v_mul_f32 v48, v88, v48\n"
"v_mul_f32 v49, v88, v49\n"
"v_mfma_f32_16x16x32_bf16 v[32:35], v[236:239], v[76:79], v[32:35]\n"
"ds_read_b64 v[74:75], v3 offset:8448\n"
"v_mul_f32 v50, v88, v50\n"
"s_waitcnt lgkmcnt(7)\n"
"v_mfma_f32_16x16x32_bf16 v[28:31], v[240:243], v[56:59], v[28:31]\n"
"ds_read_b64 v[76:77], v3 offset:12288\n"
"v_cndmask_b32 v94, v52, v53, s[34:35]\n"
"v_mul_f32 v51, v88, v51\n"
"v_mfma_f32_16x16x32_bf16 v[32:35], v[244:247], v[56:59], v[32:35]\n"
"ds_read_b64 v[78:79], v3 offset:12544\n"
"v_cndmask_b32 v54, v54, v55, s[34:35]\n"
"v_fmac_f32 v48, v89, v20\n"
"v_mfma_f32_16x16x32_bf16 v[28:31], v[248:251], v[60:63], v[28:31]\n"
"ds_read_b64 v[56:57], v3 offset:16384\n"
"v_cndmask_b32 v94, v94, v54, s[20:21]\n"
"v_fmac_f32 v49, v89, v21\n"
"v_mfma_f32_16x16x32_bf16 v[32:35], v[252:255], v[60:63], v[32:35]\n"
"ds_read_b64 v[58:59], v3 offset:16640\n"
"v_fmac_f32 v50, v89, v22\n"
"s_waitcnt lgkmcnt(7)\n"
"v_mfma_f32_16x16x32_bf16 v[36:39], v[96:99], v[64:67], 0\n"
"ds_read_b64 v[60:61], v3 offset:1024\n"
"v_fmac_f32 v51, v89, v23\n"
"v_mfma_f32_16x16x32_bf16 v[40:43], v[100:103], v[64:67], 0\n"
"ds_read_b64 v[62:63], v3 offset:1280\n"
"v_cndmask_b32 v94, v28, v94, s[22:23]\n"
"v_mfma_f32_16x16x32_bf16 v[52:55], v[64:67], v[64:67], 0\n"
"ds_write_b32 v9, v94 offset:2816\n"
"v_mfma_f32_16x16x32_bf16 v[36:39], v[128:131], v[68:71], v[36:39]\n"
"ds_read_b64 v[64:65], v3 offset:2048\n"
"v_cvt_pk_bf16_f32 v48, v48, v49\n"
"v_mfma_f32_16x16x32_bf16 v[40:43], v[132:135], v[68:71], v[40:43]\n"
"ds_read_b64 v[66:67], v3 offset:2304\n"
"v_cvt_pk_bf16_f32 v49, v50, v51\n"
"v_mfma_f32_16x16x32_bf16 v[52:55], v[68:71], v[68:71], v[52:55]\n"
"ds_write_b64 v7, v[44:45] offset:11904\n"
"s_waitcnt lgkmcnt(8)\n"
"v_mfma_f32_16x16x32_bf16 v[36:39], v[160:163], v[72:75], v[36:39]\n"
"ds_read_b64 v[68:69], v3 offset:3072\n"
"v_mfma_f32_16x16x32_bf16 v[40:43], v[164:167], v[72:75], v[40:43]\n"
"ds_read_b64 v[70:71], v3 offset:3328\n"
"v_mfma_f32_16x16x32_bf16 v[52:55], v[72:75], v[72:75], v[52:55]\n"
"ds_write_b64 v8, v[48:49] offset:11904\n"
"v_mfma_f32_16x16x32_bf16 v[36:39], v[192:195], v[76:79], v[36:39]\n"
"ds_read_b64 v[72:73], v3 offset:5120\n"
"v_mfma_f32_16x16x32_bf16 v[40:43], v[196:199], v[76:79], v[40:43]\n"
"ds_read_b64 v[74:75], v3 offset:5376\n"
"v_mfma_f32_16x16x32_bf16 v[52:55], v[76:79], v[76:79], v[52:55]\n"
"ds_read_b128 v[84:87], v11 offset:2816\n"
"ds_read_b32 v91, v12 offset:2816\n"
"s_waitcnt lgkmcnt(11)\n"
"v_mfma_f32_16x16x32_bf16 v[36:39], v[224:227], v[56:59], v[36:39]\n"
"ds_read_b64 v[76:77], v3 offset:6144\n"
"v_mfma_f32_16x16x32_bf16 v[40:43], v[228:231], v[56:59], v[40:43]\n"
"ds_read_b64 v[78:79], v3 offset:6400\n"
"v_mfma_f32_16x16x32_bf16 v[52:55], v[56:59], v[56:59], v[52:55]\n"
"v_mfma_f32_16x16x32_bf16 v[36:39], v[104:107], v[60:63], v[36:39]\n"
"ds_read_b64 v[56:57], v3 offset:7168\n"
"v_mfma_f32_16x16x32_bf16 v[40:43], v[108:111], v[60:63], v[40:43]\n"
"ds_read_b64 v[58:59], v3 offset:7424\n"
"s_waitcnt lgkmcnt(9)\n"
"v_mfma_f32_16x16x32_bf16 v[36:39], v[112:115], v[64:67], v[36:39]\n"
"ds_read_b64 v[60:61], v3 offset:9216\n"
"ds_write_b32 v10, v52 offset:3456\n"
"v_cndmask_b32 v94, v52, v53, s[34:35]\n"
"v_cndmask_b32 v54, v54, v55, s[34:35]\n"
"v_mfma_f32_16x16x32_bf16 v[40:43], v[116:119], v[64:67], v[40:43]\n"
"ds_read_b64 v[62:63], v3 offset:9472\n"
"s_waitcnt lgkmcnt(8)\n"
"v_add_f32 v84, v84, v85\n"
"v_add_f32 v86, v86, v87\n"
"v_cndmask_b32 v94, v94, v54, s[20:21]\n"
"ds_write_b32 v9, v94 offset:3136\n"
"v_mfma_f32_16x16x32_bf16 v[36:39], v[120:123], v[68:71], v[36:39]\n"
"ds_read_b64 v[64:65], v3 offset:10240\n"
"v_add_f32 v84, v84, v86\n"
"s_waitcnt lgkmcnt(9)\n"
"v_mul_f32 v89, 0x3acccccd, v91\n"
"v_mfma_f32_16x16x32_bf16 v[40:43], v[124:127], v[68:71], v[40:43]\n"
"ds_read_b64 v[66:67], v3 offset:10496\n"
"v_fma_f32 v84, -v91, v89, v84\n"
"s_waitcnt lgkmcnt(8)\n"
"v_mfma_f32_16x16x32_bf16 v[36:39], v[136:139], v[72:75], v[36:39]\n"
"ds_read_b64 v[68:69], v3 offset:11264\n"
"v_fmamk_f32 v84, v84, 0x3acccccd, v27\n"
"v_cmp_u_f32 vcc, v84, v84\n"
"v_mfma_f32_16x16x32_bf16 v[40:43], v[140:143], v[72:75], v[40:43]\n"
"ds_read_b64 v[70:71], v3 offset:11520\n"
"v_max_f32 v84, v84, v27\n"
"v_rsq_f32 v88, v84\n"
"v_mfma_f32_16x16x32_bf16 v[36:39], v[144:147], v[76:79], v[36:39]\n"
"ds_read_b64 v[72:73], v3 offset:13312\n"
"v_mul_f32 v89, -v89, v88\n"
"v_mfma_f32_16x16x32_bf16 v[40:43], v[148:151], v[76:79], v[40:43]\n"
"ds_read_b64 v[74:75], v3 offset:13568\n"
"s_cbranch_vccnz .Lsl_134\n"
".Lbk_135:\n"
"s_waitcnt lgkmcnt(7)\n"
"v_mfma_f32_16x16x32_bf16 v[36:39], v[152:155], v[56:59], v[36:39]\n"
"ds_read_b64 v[76:77], v3 offset:14336\n"
"v_mul_f32 v28, v88, v28\n"
"v_mul_f32 v29, v88, v29\n"
"v_mfma_f32_16x16x32_bf16 v[40:43], v[156:159], v[56:59], v[40:43]\n"
"ds_read_b64 v[78:79], v3 offset:14592\n"
"ds_read_b128 v[44:47], v11 offset:3136\n"
"ds_read_b128 v[48:51], v11 offset:3456\n"
"v_mfma_f32_16x16x32_bf16 v[36:39], v[168:171], v[60:63], v[36:39]\n"
"ds_read_b64 v[56:57], v3 offset:15360\n"
"v_mul_f32 v30, v88, v30\n"
"v_mul_f32 v31, v88, v31\n"
"v_mfma_f32_16x16x32_bf16 v[40:43], v[172:175], v[60:63], v[40:43]\n"
"ds_read_b64 v[58:59], v3 offset:15616\n"
"v_fmac_f32 v28, v89, v16\n"
"s_waitcnt lgkmcnt(8)\n"
"v_mfma_f32_16x16x32_bf16 v[36:39], v[176:179], v[64:67], v[36:39]\n"
"ds_read_b64 v[60:61], v4 offset:0\n"
"v_fmac_f32 v29, v89, v17\n"
"v_fmac_f32 v30, v89, v18\n"
"v_mfma_f32_16x16x32_bf16 v[40:43], v[180:183], v[64:67], v[40:43]\n"
"ds_read_b64 v[62:63], v4 offset:256\n"
"v_fmac_f32 v31, v89, v19\n"
"v_cvt_pk_bf16_f32 v28, v28, v29\n"
"v_mfma_f32_16x16x32_bf16 v[36:39], v[184:187], v[68:71], v[36:39]\n"
"ds_read_b64 v[64:65], v5 offset:0\n"
"v_cvt_pk_bf16_f32 v29, v30, v31\n"
"ds_write_b64 v7, v[28:29] offset:15872\n"
"v_mfma_f32_16x16x32_bf16 v[40:43], v[188:191], v[68:71], v[40:43]\n"
"ds_read_b64 v[66:67], v5 offset:256\n"
"v_mul_f32 v32, v88, v32\n"
"s_waitcnt lgkmcnt(9)\n"
"v_mfma_f32_16x16x32_bf16 v[36:39], v[200:203], v[72:75], v[36:39]\n"
"ds_read_b64 v[68:69], v6 offset:0\n"
"s_waitcnt lgkmcnt(9)\n"
"v_add_f32 v44, v44, v45\n"
"v_mul_f32 v33, v88, v33\n"
"v_mfma_f32_16x16x32_bf16 v[40:43], v[204:207], v[72:75], v[40:43]\n"
"ds_read_b64 v[70:71], v6 offset:256\n"
"v_add_f32 v46, v46, v47\n"
"v_mul_f32 v34, v88, v34\n"
"v_mfma_f32_16x16x32_bf16 v[36:39], v[208:211], v[76:79], v[36:39]\n"
"s_waitcnt lgkmcnt(9)\n"
"v_add_f32 v48, v48, v49\n"
"v_add_f32 v50, v50, v51\n"
"v_mul_f32 v35, v88, v35\n"
"v_mfma_f32_16x16x32_bf16 v[40:43], v[212:215], v[76:79], v[40:43]\n"
"v_add_f32 v48, v48, v50\n"
"v_fmac_f32 v32, v89, v20\n"
"s_waitcnt lgkmcnt(5)\n"
"v_mfma_f32_16x16x32_bf16 v[36:39], v[216:219], v[56:59], v[36:39]\n"
"v_add_f32 v44, v44, v46\n"
"v_mul_f32 v53, 0x3acccccd, v48\n"
"v_fmac_f32 v33, v89, v21\n"
"v_mfma_f32_16x16x32_bf16 v[40:43], v[220:223], v[56:59], v[40:43]\n"
"v_fma_f32 v44, -v48, v53, v44\n"
"v_fmamk_f32 v44, v44, 0x3acccccd, v27\n"
"v_fmac_f32 v34, v89, v22\n"
"v_mfma_f32_16x16x32_bf16 v[36:39], v[232:235], v[60:63], v[36:39]\n"
"v_cmp_u_f32 vcc, v44, v44\n"
"v_max_f32 v44, v44, v27\n"
"v_fmac_f32 v35, v89, v23\n"
"v_mfma_f32_16x16x32_bf16 v[40:43], v[236:239], v[60:63], v[40:43]\n"
"v_rsq_f32 v52, v44\n"
"s_waitcnt lgkmcnt(0)\n"
"v_mfma_f32_16x16x32_bf16 v[36:39], v[240:243], v[64:67], v[36:39]\n"
"v_mul_f32 v53, -v53, v52\n"
"v_cvt_pk_bf16_f32 v32, v32, v33\n"
"v_cvt_pk_bf16_f32 v33, v34, v35\n"
"v_mfma_f32_16x16x32_bf16 v[40:43], v[244:247], v[64:67], v[40:43]\n"
"s_cbranch_vccnz .Lsl_138\n"
".Lbk_139:\n"
"ds_write_b64 v8, v[32:33] offset:15872\n"
"v_mfma_f32_16x16x32_bf16 v[36:39], v[248:251], v[68:71], v[36:39]\n"
"v_mfma_f32_16x16x32_bf16 v[40:43], v[252:255], v[68:71], v[40:43]\n"
"v_cmp_u_f32 vcc, v92, v92\n"
"s_nop 3\n"
"v_mul_f32 v36, v52, v36\n"
"v_mul_f32 v37, v52, v37\n"
"v_mul_f32 v38, v52, v38\n"
"v_mul_f32 v39, v52, v39\n"
"v_fmac_f32 v36, v53, v16\n"
"v_fmac_f32 v37, v53, v17\n"
"v_fmac_f32 v38, v53, v18\n"
"v_fmac_f32 v39, v53, v19\n"
"v_cvt_pk_bf16_f32 v36, v36, v37\n"
"v_cvt_pk_bf16_f32 v37, v38, v39\n"
"ds_write_b64 v7, v[36:37] offset:0\n"
"v_mul_f32 v40, v52, v40\n"
"v_mul_f32 v41, v52, v41\n"
"v_mul_f32 v42, v52, v42\n"
"v_mul_f32 v43, v52, v43\n"
"v_fmac_f32 v40, v53, v20\n"
"v_fmac_f32 v41, v53, v21\n"
"v_fmac_f32 v42, v53, v22\n"
"v_fmac_f32 v43, v53, v23\n"
"v_cvt_pk_bf16_f32 v40, v40, v41\n"
"v_cvt_pk_bf16_f32 v41, v42, v43\n"
"ds_write_b64 v8, v[40:41] offset:0\n"
"s_cbranch_vccnz .Lrd_142\n"
".Lgo_108:\n"
"ds_write_b32 v13, v13 offset:352\n"
"v_mov_b32 v46, 0\n"
"v_mov_b32 v47, 0\n"
"v_mov_b32 v50, 0\n"
"v_mov_b32 v51, 0\n"
"v_mov_b32 v72, s42\n"
"v_mov_b32 v73, s42\n"
"v_mov_b32 v74, s42\n"
"v_mov_b32 v75, s42\n"
"ds_read_b32 v90, v14 offset:352\n"
"s_mov_b64 exec, 0xffff\n"
"ds_read_b64 v[46:47], v24 offset:4000\n"
"ds_read_b64 v[50:51], v24 offset:4040\n"
"s_mov_b64 exec, -1\n"
"ds_read_b64 v[44:45], v25 offset:3968\n"
"ds_read_b64 v[56:57], v25 offset:80\n"
"ds_read_b64 v[58:59], v24 offset:112\n"
"ds_read_b64 v[64:65], v25 offset:4048\n"
"ds_read_b64 v[66:67], v24 offset:4080\n"
"ds_read_b64 v[48:49], v25 offset:4008\n"
"ds_read_b64 v[60:61], v25 offset:120\n"
"ds_read_b64 v[62:63], v24 offset:152\n"
"ds_read_b64 v[68:69], v25 offset:4088\n"
"ds_read_b64 v[70:71], v24 offset:4120\n"
"s_mov_b32 s17, 0x100000\n"
"s_waitcnt lgkmcnt(12)\n"
"v_cmp_eq_u32 vcc, 0, v90\n"
"s_cbranch_vccz .Lok_185\n"
".Ltp_184:\n"
"ds_read_b32 v90, v14 offset:352\n"
"s_mov_b64 exec, 0xffff\n"
"ds_read_b64 v[46:47], v24 offset:4000\n"
"ds_read_b64 v[50:51], v24 offset:4040\n"
"s_mov_b64 exec, -1\n"
"ds_read_b64 v[44:45], v25 offset:3968\n"
"ds_read_b64 v[56:57], v25 offset:80\n"
"ds_read_b64 v[58:59], v24 offset:112\n"
"ds_read_b64 v[64:65], v25 offset:4048\n"
"ds_read_b64 v[66:67], v24 offset:4080\n"
"ds_read_b64 v[48:49], v25 offset:4008\n"
"ds_read_b64 v[60:61], v25 offset:120\n"
"ds_read_b64 v[62:63], v24 offset:152\n"
"ds_read_b64 v[68:69], v25 offset:4088\n"
"ds_read_b64 v[70:71], v24 offset:4120\n"
"s_sub_u32 s17, s17, 1\n"
"s_cbranch_scc1 .Lok_185\n"
"s_waitcnt lgkmcnt(12)\n"
"v_cmp_eq_u32 vcc, 0, v90\n"
"s_cbranch_vccnz .Ltp_184\n"
"s_waitcnt lgkmcnt(0)\n"
".Lok_185:\n"
"ds_read_b64_tr_b16 v[28:29], v26 offset:0\n"
"ds_read_b64_tr_b16 v[30:31], v26 offset:3968\n"
"ds_read_b64_tr_b16 v[32:33], v26 offset:40\n"
"s_waitcnt lgkmcnt(10)\n"
"v_mfma_f32_16x16x32_bf16 v[56:59], v[56:59], v[44:47], v[72:75]\n"
"s_waitcnt lgkmcnt(8)\n"
"v_mfma_f32_16x16x32_bf16 v[64:67], v[64:67], v[44:47], 0\n"
"ds_read_b64_tr_b16 v[34:35], v26 offset:4008\n"
"ds_read_b64_tr_b16 v[36:37], v26 offset:32\n"
"ds_read_b64_tr_b16 v[38:39], v26 offset:4000\n"
"s_waitcnt lgkmcnt(8)\n"
"v_mfma_f32_16x16x32_bf16 v[60:63], v[60:63], v[48:51], v[72:75]\n"
"s_waitcnt lgkmcnt(6)\n"
"v_mfma_f32_16x16x32_bf16 v[68:71], v[68:71], v[48:51], 0\n"
"ds_read_b64_tr_b16 v[40:41], v26 offset:72\n"
"ds_read_b64_tr_b16 v[42:43], v26 offset:4040\n"
"v_mov_b32 v84, 0x3f803f80\n"
"v_mov_b32 v85, 0x3f803f80\n"
"v_mov_b32 v86, 0x3f803f80\n"
"v_mov_b32 v87, 0x3f803f80\n"
"v_cndmask_b32 v76, v56, v57, s[34:35]\n"
"v_cndmask_b32 v72, v64, v65, s[34:35]\n"
"v_cndmask_b32 v76, v76, v72, s[36:37]\n"
"v_cndmask_b32 v77, v58, v59, s[34:35]\n"
"v_cndmask_b32 v72, v66, v67, s[34:35]\n"
"v_cndmask_b32 v77, v77, v72, s[38:39]\n"
"v_max_f32 v52, v76, v77\n"
"v_max_f32 v54, v76, v77\n"
"v_cndmask_b32 v78, v60, v61, s[34:35]\n"
"v_cndmask_b32 v72, v68, v69, s[34:35]\n"
"v_cndmask_b32 v78, v78, v72, s[36:37]\n"
"v_cndmask_b32 v79, v62, v63, s[34:35]\n"
"v_cndmask_b32 v72, v70, v71, s[34:35]\n"
"v_cndmask_b32 v79, v79, v72, s[38:39]\n"
"v_max_f32 v53, v78, v79\n"
"v_max_f32 v55, v78, v79\n"
"v_permlane16_swap_b32 v52, v54\n"
"s_nop 0\n"
"v_permlane16_swap_b32 v53, v55\n"
"v_max_f32 v72, v52, v54\n"
"v_max_f32 v52, v52, v54\n"
"v_max_f32 v73, v53, v55\n"
"v_max_f32 v53, v53, v55\n"
"v_permlane32_swap_b32 v52, v72\n"
"s_nop 0\n"
"v_permlane32_swap_b32 v53, v73\n"
"v_max_f32 v52, v52, v72\n"
"v_max_f32 v53, v53, v73\n"
"v_mul_f32 v52, 0xbea52b5b, v52\n"
"v_mul_f32 v53, 0xbea52b5b, v53\n"
"v_fmamk_f32 v76, v76, 0x3ea52b5b, v52\n"
"v_fmamk_f32 v77, v77, 0x3ea52b5b, v52\n"
"v_fmamk_f32 v78, v78, 0x3ea52b5b, v53\n"
"v_fmamk_f32 v79, v79, 0x3ea52b5b, v53\n"
"v_exp_f32 v76, v76\n"
"v_exp_f32 v77, v77\n"
"v_exp_f32 v78, v78\n"
"v_exp_f32 v79, v79\n"
"v_cvt_pk_bf16_f32 v52, v76, v77\n"
"v_cvt_pk_bf16_f32 v53, v78, v79\n"
"v_perm_b32 v44, v52, v52, v80\n"
"v_perm_b32 v45, v52, v52, v81\n"
"v_perm_b32 v46, v52, v52, v82\n"
"v_perm_b32 v47, v52, v52, v83\n"
"v_perm_b32 v48, v53, v53, v80\n"
"v_perm_b32 v49, v53, v53, v81\n"
"v_perm_b32 v50, v53, v53, v82\n"
"v_perm_b32 v51, v53, v53, v83\n"
"v_mfma_f32_16x16x32_bf16 v[56:59], v[84:87], v[44:47], 0\n"
"s_nop 0\n"
"v_mfma_f32_16x16x32_bf16 v[60:63], v[84:87], v[48:51], 0\n"
"s_waitcnt lgkmcnt(6)\n"
"v_mfma_f32_16x16x32_bf16 v[28:31], v[28:31], v[44:47], 0\n"
"s_waitcnt lgkmcnt(4)\n"
"v_mfma_f32_16x16x32_bf16 v[32:35], v[32:35], v[48:51], 0\n"
"s_waitcnt lgkmcnt(2)\n"
"v_mfma_f32_16x16x32_bf16 v[36:39], v[36:39], v[44:47], 0\n"
"s_waitcnt lgkmcnt(0)\n"
"v_mfma_f32_16x16x32_bf16 v[40:43], v[40:43], v[48:51], 0\n"
"v_cndmask_b32 v88, 2.0, v1, s[40:41]\n"
"v_rcp_f32 v52, v56\n"
"v_rcp_f32 v53, v60\n"
"ds_read_b32 v90, v14 offset:368\n"
"ds_read_b64 v[56:57], v3 offset:40960\n"
"ds_read_b64 v[58:59], v3 offset:41216\n"
"s_add_u32 s31, s30, 0xa60000\n"
"v_mul_f32 v28, v52, v28\n"
"v_mul_f32 v29, v52, v29\n"
"v_mul_f32 v30, v52, v30\n"
"v_mul_f32 v31, v52, v31\n"
"v_mul_f32 v32, v53, v32\n"
"ds_read_b64 v[60:61], v3 offset:41984\n"
"ds_read_b64 v[62:63], v3 offset:42240\n"
"v_mul_f32 v33, v53, v33\n"
"v_mul_f32 v34, v53, v34\n"
"v_mul_f32 v35, v53, v35\n"
"v_cvt_pk_bf16_f32 v28, v28, v29\n"
"v_cvt_pk_bf16_f32 v29, v30, v31\n"
"v_cvt_pk_bf16_f32 v30, v32, v33\n"
"ds_read_b64 v[64:65], v3 offset:43008\n"
"ds_read_b64 v[66:67], v3 offset:43264\n"
"v_cvt_pk_bf16_f32 v31, v34, v35\n"
"v_min_u32 v28, -2, v28\n"
"v_min_u32 v29, -2, v29\n"
"v_min_u32 v30, -2, v30\n"
"v_min_u32 v31, -2, v31\n"
"buffer_store_dwordx4 v[28:31], v1, s[4:7], s31 offen offset:0 sc1\n"
"ds_read_b64 v[68:69], v3 offset:44032\n"
"ds_read_b64 v[70:71], v3 offset:44288\n"
"v_mul_f32 v36, v52, v36\n"
"v_mul_f32 v37, v52, v37\n"
"v_mul_f32 v38, v52, v38\n"
"v_mul_f32 v39, v52, v39\n"
"v_mul_f32 v40, v53, v40\n"
"v_mul_f32 v41, v53, v41\n"
"ds_read_b64 v[72:73], v3 offset:45056\n"
"ds_read_b64 v[74:75], v3 offset:45312\n"
"v_mul_f32 v42, v53, v42\n"
"v_mul_f32 v43, v53, v43\n"
"v_cvt_pk_bf16_f32 v36, v36, v37\n"
"v_cvt_pk_bf16_f32 v37, v38, v39\n"
"v_cvt_pk_bf16_f32 v38, v40, v41\n"
"v_cvt_pk_bf16_f32 v39, v42, v43\n"
"v_min_u32 v36, -2, v36\n"
"v_min_u32 v37, -2, v37\n"
"v_min_u32 v38, -2, v38\n"
"v_min_u32 v39, -2, v39\n"
"buffer_store_dwordx4 v[36:39], v88, s[4:7], s31 offen offset:1024 sc1\n"
"ds_write_b32 v13, v13 offset:384\n"
"s_cmp_lg_u32 s54, 0\n"
"s_cbranch_scc1 .Lfx_188\n"
".Lbk_189:\n"
"s_mov_b32 s54, 0\n"
".Lag_186:\n"
"v_mov_b32 v92, 0\n"
"s_waitcnt lgkmcnt(11)\n"
"v_cmp_eq_u32 vcc, 0, v90\n"
"s_cbranch_vccnz .Lsl_224\n"
".Lbk_225:\n"
"s_waitcnt lgkmcnt(7)\n"
"v_mfma_f32_16x16x32_bf16 v[28:31], v[96:99], v[56:59], 0\n"
"ds_read_b64 v[76:77], v3 offset:46080\n"
"v_mfma_f32_16x16x32_bf16 v[32:35], v[100:103], v[56:59], 0\n"
"ds_read_b64 v[78:79], v3 offset:46336\n"
"v_mfma_f32_16x16x32_bf16 v[52:55], v[56:59], v[56:59], 0\n"
"v_mfma_f32_16x16x32_bf16 v[28:31], v[104:107], v[60:63], v[28:31]\n"
"ds_read_b64 v[56:57], v3 offset:47104\n"
"v_mfma_f32_16x16x32_bf16 v[32:35], v[108:111], v[60:63], v[32:35]\n"
"ds_read_b64 v[58:59], v3 offset:47360\n"
"s_waitcnt lgkmcnt(7)\n"
"v_mfma_f32_16x16x32_bf16 v[28:31], v[112:115], v[64:67], v[28:31]\n"
"ds_read_b64 v[60:61], v3 offset:48128\n"
"v_mfma_f32_16x16x32_bf16 v[32:35], v[116:119], v[64:67], v[32:35]\n"
"ds_read_b64 v[62:63], v3 offset:48384\n"
"v_mfma_f32_16x16x32_bf16 v[28:31], v[120:123], v[68:71], v[28:31]\n"
"ds_read_b64 v[64:65], v3 offset:49152\n"
"v_mfma_f32_16x16x32_bf16 v[32:35], v[124:127], v[68:71], v[32:35]\n"
"ds_read_b64 v[66:67], v3 offset:49408\n"
"s_waitcnt lgkmcnt(6)\n"
"v_mfma_f32_16x16x32_bf16 v[28:31], v[128:131], v[72:75], v[28:31]\n"
"ds_read_b64 v[68:69], v3 offset:50176\n"
"v_mfma_f32_16x16x32_bf16 v[32:35], v[132:135], v[72:75], v[32:35]\n"
"ds_read_b64 v[70:71], v3 offset:50432\n"
"v_mfma_f32_16x16x32_bf16 v[52:55], v[72:75], v[72:75], v[52:55]\n"
"v_mfma_f32_16x16x32_bf16 v[28:31], v[136:139], v[76:79], v[28:31]\n"
"ds_read_b64 v[72:73], v3 offset:51200\n"
"v_mfma_f32_16x16x32_bf16 v[32:35], v[140:143], v[76:79], v[32:35]\n"
"ds_read_b64 v[74:75], v3 offset:51456\n"
"s_waitcnt lgkmcnt(6)\n"
"v_mfma_f32_16x16x32_bf16 v[28:31], v[144:147], v[56:59], v[28:31]\n"
"ds_read_b64 v[76:77], v3 offset:52224\n"
"v_mfma_f32_16x16x32_bf16 v[32:35], v[148:151], v[56:59], v[32:35]\n"
"ds_read_b64 v[78:79], v3 offset:52480\n"
"v_mfma_f32_16x16x32_bf16 v[28:31], v[152:155], v[60:63], v[28:31]\n"
"ds_read_b64 v[56:57], v3 offset:53248\n"
"v_mfma_f32_16x16x32_bf16 v[32:35], v[156:159], v[60:63], v[32:35]\n"
"ds_read_b64 v[58:59], v3 offset:53504\n"
"s_waitcnt lgkmcnt(6)\n"
"v_mfma_f32_16x16x32_bf16 v[28:31], v[160:163], v[64:67], v[28:31]\n"
"ds_read_b64 v[60:61], v3 offset:54272\n"
"v_mfma_f32_16x16x32_bf16 v[32:35], v[164:167], v[64:67], v[32:35]\n"
"ds_read_b64 v[62:63], v3 offset:54528\n"
"v_mfma_f32_16x16x32_bf16 v[52:55], v[64:67], v[64:67], v[52:55]\n"
"v_mfma_f32_16x16x32_bf16 v[28:31], v[168:171], v[68:71], v[28:31]\n"
"ds_read_b64 v[64:65], v3 offset:55296\n"
"v_mfma_f32_16x16x32_bf16 v[32:35], v[172:175], v[68:71], v[32:35]\n"
"ds_read_b64 v[66:67], v3 offset:55552\n"
"s_waitcnt lgkmcnt(6)\n"
"v_mfma_f32_16x16x32_bf16 v[28:31], v[176:179], v[72:75], v[28:31]\n"
"ds_read_b64 v[68:69], v3 offset:56320\n"
"v_mfma_f32_16x16x32_bf16 v[32:35], v[180:183], v[72:75], v[32:35]\n"
"ds_read_b64 v[70:71], v3 offset:56576\n"
"v_mfma_f32_16x16x32_bf16 v[28:31], v[184:187], v[76:79], v[28:31]\n"
"ds_read_b64 v[72:73], v3 offset:57344\n"
"v_mfma_f32_16x16x32_bf16 v[32:35], v[188:191], v[76:79], v[32:35]\n"
"ds_read_b64 v[74:75], v3 offset:57600\n"
"s_waitcnt lgkmcnt(6)\n"
"v_mfma_f32_16x16x32_bf16 v[28:31], v[192:195], v[56:59], v[28:31]\n"
"ds_read_b64 v[76:77], v4 offset:40960\n"
"v_mfma_f32_16x16x32_bf16 v[32:35], v[196:199], v[56:59], v[32:35]\n"
"ds_read_b64 v[78:79], v4 offset:41216\n"
"ds_read_b32 v90, v14 offset:304\n"
"v_mfma_f32_16x16x32_bf16 v[52:55], v[56:59], v[56:59], v[52:55]\n"
"v_mfma_f32_16x16x32_bf16 v[28:31], v[200:203], v[60:63], v[28:31]\n"
"ds_read_b64 v[56:57], v5 offset:40960\n"
"v_mfma_f32_16x16x32_bf16 v[32:35], v[204:207], v[60:63], v[32:35]\n"
"ds_read_b64 v[58:59], v5 offset:41216\n"
"s_waitcnt lgkmcnt(7)\n"
"v_mfma_f32_16x16x32_bf16 v[28:31], v[208:211], v[64:67], v[28:31]\n"
"ds_read_b64 v[60:61], v6 offset:40960\n"
"s_waitcnt lgkmcnt(3)\n"
"v_cmp_eq_u32 vcc, 0, v90\n"
"v_mfma_f32_16x16x32_bf16 v[32:35], v[212:215], v[64:67], v[32:35]\n"
"ds_read_b64 v[62:63], v6 offset:41216\n"
"v_mfma_f32_16x16x32_bf16 v[28:31], v[216:219], v[68:71], v[28:31]\n"
"s_cbranch_vccnz .Lsl_192\n"
".Lbk_193:\n"
"v_add_u32 v3, 0x5000, v3\n"
"ds_read_b64 v[64:65], v3 offset:40960\n"
"v_mfma_f32_16x16x32_bf16 v[32:35], v[220:223], v[68:71], v[32:35]\n"
"ds_read_b64 v[66:67], v3 offset:41216\n"
"v_mfma_f32_16x16x32_bf16 v[28:31], v[224:227], v[72:75], v[28:31]\n"
"ds_read_b64 v[68:69], v3 offset:41984\n"
"v_mfma_f32_16x16x32_bf16 v[32:35], v[228:231], v[72:75], v[32:35]\n"
"ds_read_b64 v[70:71], v3 offset:42240\n"
"v_mfma_f32_16x16x32_bf16 v[52:55], v[72:75], v[72:75], v[52:55]\n"
"v_mfma_f32_16x16x32_bf16 v[28:31], v[232:235], v[76:79], v[28:31]\n"
"ds_read_b64 v[72:73], v3 offset:43008\n"
"v_mfma_f32_16x16x32_bf16 v[32:35], v[236:239], v[76:79], v[32:35]\n"
"ds_read_b64 v[74:75], v3 offset:43264\n"
"s_waitcnt lgkmcnt(6)\n"
"v_mfma_f32_16x16x32_bf16 v[28:31], v[240:243], v[56:59], v[28:31]\n"
"ds_read_b64 v[76:77], v3 offset:44032\n"
"v_cndmask_b32 v94, v52, v53, s[34:35]\n"
"v_mfma_f32_16x16x32_bf16 v[32:35], v[244:247], v[56:59], v[32:35]\n"
"ds_read_b64 v[78:79], v3 offset:44288\n"
"v_cndmask_b32 v54, v54, v55, s[34:35]\n"
"v_mfma_f32_16x16x32_bf16 v[28:31], v[248:251], v[60:63], v[28:31]\n"
"ds_read_b64 v[56:57], v3 offset:45056\n"
"v_cndmask_b32 v94, v94, v54, s[20:21]\n"
"v_mfma_f32_16x16x32_bf16 v[32:35], v[252:255], v[60:63], v[32:35]\n"
"ds_read_b64 v[58:59], v3 offset:45312\n"
"s_waitcnt lgkmcnt(6)\n"
"v_mfma_f32_16x16x32_bf16 v[36:39], v[96:99], v[64:67], 0\n"
"ds_read_b64 v[60:61], v3 offset:46080\n"
"v_mfma_f32_16x16x32_bf16 v[40:43], v[100:103], v[64:67], 0\n"
"ds_read_b64 v[62:63], v3 offset:46336\n"
"v_cndmask_b32 v94, v28, v94, s[22:23]\n"
"v_mfma_f32_16x16x32_bf16 v[52:55], v[64:67], v[64:67], 0\n"
"ds_write_b32 v9, v94 offset:3712\n"
"v_mfma_f32_16x16x32_bf16 v[36:39], v[104:107], v[68:71], v[36:39]\n"
"ds_read_b64 v[64:65], v3 offset:47104\n"
"v_mfma_f32_16x16x32_bf16 v[40:43], v[108:111], v[68:71], v[40:43]\n"
"ds_read_b64 v[66:67], v3 offset:47360\n"
"s_waitcnt lgkmcnt(7)\n"
"v_mfma_f32_16x16x32_bf16 v[36:39], v[112:115], v[72:75], v[36:39]\n"
"ds_read_b64 v[68:69], v3 offset:48128\n"
"v_mfma_f32_16x16x32_bf16 v[40:43], v[116:119], v[72:75], v[40:43]\n"
"ds_read_b64 v[70:71], v3 offset:48384\n"
"v_mfma_f32_16x16x32_bf16 v[36:39], v[120:123], v[76:79], v[36:39]\n"
"ds_read_b64 v[72:73], v3 offset:49152\n"
"v_mfma_f32_16x16x32_bf16 v[40:43], v[124:127], v[76:79], v[40:43]\n"
"ds_read_b64 v[74:75], v3 offset:49408\n"
"s_waitcnt lgkmcnt(7)\n"
"v_mfma_f32_16x16x32_bf16 v[36:39], v[128:131], v[56:59], v[36:39]\n"
"ds_read_b64 v[76:77], v3 offset:50176\n"
"v_mfma_f32_16x16x32_bf16 v[40:43], v[132:135], v[56:59], v[40:43]\n"
"ds_read_b64 v[78:79], v3 offset:50432\n"
"v_mfma_f32_16x16x32_bf16 v[52:55], v[56:59], v[56:59], v[52:55]\n"
"v_mfma_f32_16x16x32_bf16 v[36:39], v[136:139], v[60:63], v[36:39]\n"
"ds_read_b64 v[56:57], v3 offset:51200\n"
"v_mfma_f32_16x16x32_bf16 v[40:43], v[140:143], v[60:63], v[40:43]\n"
"ds_read_b64 v[58:59], v3 offset:51456\n"
"s_waitcnt lgkmcnt(6)\n"
"v_mfma_f32_16x16x32_bf16 v[36:39], v[144:147], v[64:67], v[36:39]\n"
"ds_read_b64 v[60:61], v3 offset:52224\n"
"ds_read_b128 v[84:87], v11 offset:3712\n"
"ds_read_b32 v91, v12 offset:3712\n"
"v_mfma_f32_16x16x32_bf16 v[40:43], v[148:151], v[64:67], v[40:43]\n"
"ds_read_b64 v[62:63], v3 offset:52480\n"
"v_mfma_f32_16x16x32_bf16 v[36:39], v[152:155], v[68:71], v[36:39]\n"
"ds_read_b64 v[64:65], v3 offset:53248\n"
"v_mfma_f32_16x16x32_bf16 v[40:43], v[156:159], v[68:71], v[40:43]\n"
"ds_read_b64 v[66:67], v3 offset:53504\n"
"s_waitcnt lgkmcnt(8)\n"
"v_mfma_f32_16x16x32_bf16 v[36:39], v[160:163], v[72:75], v[36:39]\n"
"ds_read_b64 v[68:69], v3 offset:54272\n"
"v_mfma_f32_16x16x32_bf16 v[40:43], v[164:167], v[72:75], v[40:43]\n"
"ds_read_b64 v[70:71], v3 offset:54528\n"
"v_mfma_f32_16x16x32_bf16 v[52:55], v[72:75], v[72:75], v[52:55]\n"
"v_mfma_f32_16x16x32_bf16 v[36:39], v[168:171], v[76:79], v[36:39]\n"
"ds_read_b64 v[72:73], v3 offset:55296\n"
"s_waitcnt lgkmcnt(7)\n"
"v_add_f32 v84, v84, v85\n"
"v_add_f32 v86, v86, v87\n"
"v_mfma_f32_16x16x32_bf16 v[40:43], v[172:175], v[76:79], v[40:43]\n"
"ds_read_b64 v[74:75], v3 offset:55552\n"
"v_add_f32 v84, v84, v86\n"
"s_waitcnt lgkmcnt(6)\n"
"v_mfma_f32_16x16x32_bf16 v[36:39], v[176:179], v[56:59], v[36:39]\n"
"ds_read_b64 v[76:77], v3 offset:56320\n"
"v_mul_f32 v89, 0x3acccccd, v91\n"
"v_fma_f32 v84, -v91, v89, v84\n"
"v_mfma_f32_16x16x32_bf16 v[40:43], v[180:183], v[56:59], v[40:43]\n"
"ds_read_b64 v[78:79], v3 offset:56576\n"
"v_fmamk_f32 v84, v84, 0x3acccccd, v27\n"
"v_cmp_u_f32 vcc, v84, v84\n"
"v_mfma_f32_16x16x32_bf16 v[36:39], v[184:187], v[60:63], v[36:39]\n"
"ds_read_b64 v[56:57], v3 offset:57344\n"
"v_max_f32 v84, v84, v27\n"
"v_mfma_f32_16x16x32_bf16 v[40:43], v[188:191], v[60:63], v[40:43]\n"
"ds_read_b64 v[58:59], v3 offset:57600\n"
"s_waitcnt lgkmcnt(6)\n"
"v_mfma_f32_16x16x32_bf16 v[36:39], v[192:195], v[64:67], v[36:39]\n"
"ds_read_b64 v[60:61], v4 offset:61440\n"
"v_rsq_f32 v88, v84\n"
"v_mfma_f32_16x16x32_bf16 v[40:43], v[196:199], v[64:67], v[40:43]\n"
"ds_read_b64 v[62:63], v4 offset:61696\n"
"ds_read_b32 v90, v14 offset:416\n"
"v_mfma_f32_16x16x32_bf16 v[52:55], v[64:67], v[64:67], v[52:55]\n"
"v_mul_f32 v89, -v89, v88\n"
"v_mfma_f32_16x16x32_bf16 v[36:39], v[200:203], v[68:71], v[36:39]\n"
"ds_read_b64 v[64:65], v5 offset:61440\n"
"s_cbranch_vccnz .Lsl_204\n"
".Lbk_205:\n"
"v_mfma_f32_16x16x32_bf16 v[40:43], v[204:207], v[68:71], v[40:43]\n"
"ds_read_b64 v[66:67], v5 offset:61696\n"
"s_add_u32 m0, s29, 0xa000\n"
"s_waitcnt lgkmcnt(7)\n"
"v_mfma_f32_16x16x32_bf16 v[36:39], v[208:211], v[72:75], v[36:39]\n"
"ds_read_b64 v[68:69], v6 offset:61440\n"
"s_waitcnt lgkmcnt(3)\n"
"v_cmp_eq_u32 vcc, 0, v90\n"
"v_mul_f32 v28, v88, v28\n"
"v_mfma_f32_16x16x32_bf16 v[40:43], v[212:215], v[72:75], v[40:43]\n"
"ds_read_b64 v[70:71], v6 offset:61696\n"
"s_add_u32 s31, s44, 0x13b000\n"
"v_mul_f32 v29, v88, v29\n"
"v_mfma_f32_16x16x32_bf16 v[36:39], v[216:219], v[76:79], v[36:39]\n"
"s_cbranch_vccnz .Lsl_196\n"
".Lbk_197:\n"
"v_add_u32 v3, 0xffffb000, v3\n"
"ds_read_b64 v[72:73], v3 offset:0\n"
"buffer_load_dwordx4 v1, s[4:7], s31 offen offset:0 sc1 lds\n"
"v_mfma_f32_16x16x32_bf16 v[40:43], v[220:223], v[76:79], v[40:43]\n"
"ds_read_b64 v[74:75], v3 offset:256\n"
"v_mfma_f32_16x16x32_bf16 v[36:39], v[224:227], v[56:59], v[36:39]\n"
"ds_read_b64 v[76:77], v3 offset:1024\n"
"v_mul_f32 v30, v88, v30\n"
"v_mfma_f32_16x16x32_bf16 v[40:43], v[228:231], v[56:59], v[40:43]\n"
"ds_read_b64 v[78:79], v3 offset:1280\n"
"v_mul_f32 v31, v88, v31\n"
"v_mfma_f32_16x16x32_bf16 v[52:55], v[56:59], v[56:59], v[52:55]\n"
"buffer_load_dwordx4 v1, s[4:7], s31 offen offset:1024 sc1 lds\n"
"v_fmac_f32 v28, v89, v16\n"
"v_mfma_f32_16x16x32_bf16 v[36:39], v[232:235], v[60:63], v[36:39]\n"
"ds_read_b64 v[56:57], v3 offset:2048\n"
"v_fmac_f32 v29, v89, v17\n"
"v_mfma_f32_16x16x32_bf16 v[40:43], v[236:239], v[60:63], v[40:43]\n"
"ds_read_b64 v[58:59], v3 offset:2304\n"
"s_waitcnt lgkmcnt(6)\n"
"v_mfma_f32_16x16x32_bf16 v[36:39], v[240:243], v[64:67], v[36:39]\n"
"ds_read_b64 v[60:61], v3 offset:3072\n"
"v_cndmask_b32 v94, v52, v53, s[34:35]\n"
"v_mfma_f32_16x16x32_bf16 v[40:43], v[244:247], v[64:67], v[40:43]\n"
"ds_read_b64 v[62:63], v3 offset:3328\n"
"v_cndmask_b32 v54, v54, v55, s[34:35]\n"
"buffer_load_dwordx4 v1, s[4:7], s31 offen offset:2048 sc1 lds\n"
"v_mfma_f32_16x16x32_bf16 v[36:39], v[248:251], v[68:71], v[36:39]\n"
"ds_read_b64 v[64:65], v3 offset:4096\n"
"v_cndmask_b32 v94, v94, v54, s[20:21]\n"
"v_mfma_f32_16x16x32_bf16 v[40:43], v[252:255], v[68:71], v[40:43]\n"
"ds_read_b64 v[66:67], v3 offset:4352\n"
"s_waitcnt lgkmcnt(6)\n"
"v_mfma_f32_16x16x32_bf16 v[44:47], v[96:99], v[72:75], 0\n"
"ds_read_b64 v[68:69], v3 offset:5120\n"
"v_fmac_f32 v30, v89, v18\n"
"v_mfma_f32_16x16x32_bf16 v[48:51], v[100:103], v[72:75], 0\n"
"ds_read_b64 v[70:71], v3 offset:5376\n"
"v_cndmask_b32 v94, v36, v94, s[22:23]\n"
"buffer_load_dwordx4 v1, s[4:7], s31 offen offset:3072 sc1 lds\n"
"v_mfma_f32_16x16x32_bf16 v[52:55], v[72:75], v[72:75], 0\n"
"ds_write_b32 v9, v94 offset:4032\n"
"s_add_u32 m0, m0, 1024\n"
"v_mfma_f32_16x16x32_bf16 v[44:47], v[104:107], v[76:79], v[44:47]\n"
"ds_read_b64 v[72:73], v3 offset:6144\n"
"s_add_u32 s31, s31, 1024\n"
"v_mfma_f32_16x16x32_bf16 v[48:51], v[108:111], v[76:79], v[48:51]\n"
"ds_read_b64 v[74:75], v3 offset:6400\n"
"s_waitcnt lgkmcnt(7)\n"
"v_mfma_f32_16x16x32_bf16 v[44:47], v[112:115], v[56:59], v[44:47]\n"
"ds_read_b64 v[76:77], v3 offset:7168\n"
"buffer_load_dwordx4 v1, s[4:7], s31 offen offset:3072 sc1 lds\n"
"v_mfma_f32_16x16x32_bf16 v[48:51], v[116:119], v[56:59], v[48:51]\n"
"ds_read_b64 v[78:79], v3 offset:7424\n"
"v_fmac_f32 v31, v89, v19\n"
"v_mfma_f32_16x16x32_bf16 v[44:47], v[120:123], v[60:63], v[44:47]\n"
"ds_read_b64 v[56:57], v3 offset:8192\n"
"v_cvt_pk_bf16_f32 v28, v28, v29\n"
"v_mfma_f32_16x16x32_bf16 v[48:51], v[124:127], v[60:63], v[48:51]\n"
"ds_read_b64 v[58:59], v3 offset:8448\n"
"s_waitcnt lgkmcnt(7)\n"
"v_mfma_f32_16x16x32_bf16 v[44:47], v[128:131], v[64:67], v[44:47]\n"
"ds_read_b64 v[60:61], v3 offset:9216\n"
"v_cvt_pk_bf16_f32 v29, v30, v31\n"
"v_mfma_f32_16x16x32_bf16 v[48:51], v[132:135], v[64:67], v[48:51]\n"
"ds_read_b64 v[62:63], v3 offset:9472\n"
"v_mul_f32 v32, v88, v32\n"
"v_mfma_f32_16x16x32_bf16 v[52:55], v[64:67], v[64:67], v[52:55]\n"
"v_mul_f32 v33, v88, v33\n"
"v_mul_f32 v34, v88, v34\n"
"v_mfma_f32_16x16x32_bf16 v[44:47], v[136:139], v[68:71], v[44:47]\n"
"ds_read_b64 v[64:65], v3 offset:10240\n"
"v_mul_f32 v35, v88, v35\n"
"v_mfma_f32_16x16x32_bf16 v[48:51], v[140:143], v[68:71], v[48:51]\n"
"ds_read_b64 v[66:67], v3 offset:10496\n"
"s_waitcnt lgkmcnt(6)\n"
"v_mfma_f32_16x16x32_bf16 v[44:47], v[144:147], v[72:75], v[44:47]\n"
"ds_read_b64 v[68:69], v3 offset:11264\n"
"ds_read_b128 v[84:87], v11 offset:4032\n"
"ds_read_b32 v91, v12 offset:4032\n"
"v_mfma_f32_16x16x32_bf16 v[48:51], v[148:151], v[72:75], v[48:51]\n"
"ds_read_b64 v[70:71], v3 offset:11520\n"
"v_fmac_f32 v32, v89, v20\n"
"v_mfma_f32_16x16x32_bf16 v[44:47], v[152:155], v[76:79], v[44:47]\n"
"ds_read_b64 v[72:73], v3 offset:12288\n"
"v_fmac_f32 v33, v89, v21\n"
"v_mfma_f32_16x16x32_bf16 v[48:51], v[156:159], v[76:79], v[48:51]\n"
"ds_read_b64 v[74:75], v3 offset:12544\n"
"s_waitcnt lgkmcnt(8)\n"
"v_mfma_f32_16x16x32_bf16 v[44:47], v[160:163], v[56:59], v[44:47]\n"
"ds_read_b64 v[76:77], v3 offset:13312\n"
"v_fmac_f32 v34, v89, v22\n"
"v_mfma_f32_16x16x32_bf16 v[48:51], v[164:167], v[56:59], v[48:51]\n"
"ds_read_b64 v[78:79], v3 offset:13568\n"
"v_fmac_f32 v35, v89, v23\n"
"v_mfma_f32_16x16x32_bf16 v[52:55], v[56:59], v[56:59], v[52:55]\n"
"v_cvt_pk_bf16_f32 v32, v32, v33\n"
"v_cvt_pk_bf16_f32 v33, v34, v35\n"
"v_mfma_f32_16x16x32_bf16 v[44:47], v[168:171], v[60:63], v[44:47]\n"
"ds_read_b64 v[56:57], v3 offset:14336\n"
"s_waitcnt lgkmcnt(7)\n"
"v_add_f32 v84, v84, v85\n"
"v_add_f32 v86, v86, v87\n"
"v_mfma_f32_16x16x32_bf16 v[48:51], v[172:175], v[60:63], v[48:51]\n"
"ds_read_b64 v[58:59], v3 offset:14592\n"
"v_add_f32 v84, v84, v86\n"
"s_waitcnt lgkmcnt(6)\n"
"v_mfma_f32_16x16x32_bf16 v[44:47], v[176:179], v[64:67], v[44:47]\n"
"ds_read_b64 v[60:61], v3 offset:15360\n"
"v_mul_f32 v89, 0x3acccccd, v91\n"
"v_fma_f32 v84, -v91, v89, v84\n"
"v_mfma_f32_16x16x32_bf16 v[48:51], v[180:183], v[64:67], v[48:51]\n"
"ds_read_b64 v[62:63], v3 offset:15616\n"
"v_fmamk_f32 v84, v84, 0x3acccccd, v27\n"
"v_cmp_u_f32 vcc, v84, v84\n"
"v_mfma_f32_16x16x32_bf16 v[44:47], v[184:187], v[68:71], v[44:47]\n"
"ds_read_b64 v[64:65], v3 offset:16384\n"
"v_max_f32 v84, v84, v27\n"
"v_mfma_f32_16x16x32_bf16 v[48:51], v[188:191], v[68:71], v[48:51]\n"
"ds_read_b64 v[66:67], v3 offset:16640\n"
"s_waitcnt lgkmcnt(6)\n"
"v_mfma_f32_16x16x32_bf16 v[44:47], v[192:195], v[72:75], v[44:47]\n"
"ds_read_b64 v[68:69], v4 offset:0\n"
"v_rsq_f32 v88, v84\n"
"v_mfma_f32_16x16x32_bf16 v[48:51], v[196:199], v[72:75], v[48:51]\n"
"ds_read_b64 v[70:71], v4 offset:256\n"
"ds_read_b32 v90, v14 offset:432\n"
"v_mfma_f32_16x16x32_bf16 v[52:55], v[72:75], v[72:75], v[52:55]\n"
"v_mul_f32 v89, -v89, v88\n"
"v_mfma_f32_16x16x32_bf16 v[44:47], v[200:203], v[76:79], v[44:47]\n"
"ds_read_b64 v[72:73], v5 offset:0\n"
"s_cbranch_vccnz .Lsl_208\n"
".Lbk_209:\n"
"v_mfma_f32_16x16x32_bf16 v[48:51], v[204:207], v[76:79], v[48:51]\n"
"ds_read_b64 v[74:75], v5 offset:256\n"
"ds_write_b64 v7, v[28:29] offset:44928\n"
"s_waitcnt lgkmcnt(8)\n"
"v_mfma_f32_16x16x32_bf16 v[44:47], v[208:211], v[56:59], v[44:47]\n"
"ds_read_b64 v[76:77], v6 offset:0\n"
"s_waitcnt lgkmcnt(4)\n"
"v_cmp_eq_u32 vcc, 0, v90\n"
"v_mul_f32 v36, v88, v36\n"
"v_mfma_f32_16x16x32_bf16 v[48:51], v[212:215], v[56:59], v[48:51]\n"
"ds_read_b64 v[78:79], v6 offset:256\n"
"v_mul_f32 v37, v88, v37\n"
"v_mfma_f32_16x16x32_bf16 v[44:47], v[216:219], v[60:63], v[44:47]\n"
"s_cbranch_vccnz .Lsl_200\n"
".Lbk_201:\n"
"ds_read_b64 v[56:57], v3 offset:20480\n"
"v_mfma_f32_16x16x32_bf16 v[48:51], v[220:223], v[60:63], v[48:51]\n"
"ds_read_b64 v[58:59], v3 offset:20736\n"
"v_mfma_f32_16x16x32_bf16 v[44:47], v[224:227], v[64:67], v[44:47]\n"
"ds_read_b64 v[60:61], v3 offset:21504\n"
"v_mul_f32 v38, v88, v38\n"
"v_mfma_f32_16x16x32_bf16 v[48:51], v[228:231], v[64:67], v[48:51]\n"
"ds_read_b64 v[62:63], v3 offset:21760\n"
"v_mov_b32 v84, 0x3f803f80\n"
"v_mfma_f32_16x16x32_bf16 v[52:55], v[64:67], v[64:67], v[52:55]\n"
"v_mov_b32 v85, 0x3f803f80\n"
"ds_write_b64 v8, v[32:33] offset:44928\n"
"v_mfma_f32_16x16x32_bf16 v[44:47], v[232:235], v[68:71], v[44:47]\n"
"ds_read_b64 v[64:65], v3 offset:22528\n"
"v_mul_f32 v39, v88, v39\n"
"v_mfma_f32_16x16x32_bf16 v[48:51], v[236:239], v[68:71], v[48:51]\n"
"ds_read_b64 v[66:67], v3 offset:22784\n"
"s_waitcnt lgkmcnt(7)\n"
"v_mfma_f32_16x16x32_bf16 v[44:47], v[240:243], v[72:75], v[44:47]\n"
"ds_read_b64 v[68:69], v3 offset:23552\n"
"v_cndmask_b32 v94, v52, v53, s[34:35]\n"
"v_mfma_f32_16x16x32_bf16 v[48:51], v[244:247], v[72:75], v[48:51]\n"
"ds_read_b64 v[70:71], v3 offset:23808\n"
"v_cndmask_b32 v54, v54, v55, s[34:35]\n"
"v_mfma_f32_16x16x32_bf16 v[44:47], v[248:251], v[76:79], v[44:47]\n"
"ds_read_b64 v[72:73], v3 offset:24576\n"
"v_cndmask_b32 v94, v94, v54, s[20:21]\n"
"v_mfma_f32_16x16x32_bf16 v[48:51], v[252:255], v[76:79], v[48:51]\n"
"ds_read_b64 v[74:75], v3 offset:24832\n"
"s_waitcnt lgkmcnt(7)\n"
"v_mfma_f32_16x16x32_bf16 v[28:31], v[96:99], v[56:59], 0\n"
"ds_read_b64 v[76:77], v3 offset:25600\n"
"v_fmac_f32 v36, v89, v16\n"
"v_mfma_f32_16x16x32_bf16 v[32:35], v[100:103], v[56:59], 0\n"
"ds_read_b64 v[78:79], v3 offset:25856\n"
"v_cndmask_b32 v94, v44, v94, s[22:23]\n"
"s_waitcnt vmcnt(0)\n"
"ds_write_b64 v93, v[84:85] offset:40960\n"
"v_mfma_f32_16x16x32_bf16 v[52:55], v[56:59], v[56:59], 0\n"
"ds_write_b32 v9, v94 offset:4352\n"
"v_mfma_f32_16x16x32_bf16 v[28:31], v[104:107], v[60:63], v[28:31]\n"
"ds_read_b64 v[56:57], v3 offset:26624\n"
"v_fmac_f32 v37, v89, v17\n"
"v_mfma_f32_16x16x32_bf16 v[32:35], v[108:111], v[60:63], v[32:35]\n"
"ds_read_b64 v[58:59], v3 offset:26880\n"
"s_waitcnt lgkmcnt(8)\n"
"v_mfma_f32_16x16x32_bf16 v[28:31], v[112:115], v[64:67], v[28:31]\n"
"ds_read_b64 v[60:61], v3 offset:27648\n"
"v_fmac_f32 v38, v89, v18\n"
"v_mfma_f32_16x16x32_bf16 v[32:35], v[116:119], v[64:67], v[32:35]\n"
"ds_read_b64 v[62:63], v3 offset:27904\n"
"v_fmac_f32 v39, v89, v19\n"
"v_mfma_f32_16x16x32_bf16 v[28:31], v[120:123], v[68:71], v[28:31]\n"
"ds_read_b64 v[64:65], v3 offset:28672\n"
"v_cvt_pk_bf16_f32 v36, v36, v37\n"
"v_mfma_f32_16x16x32_bf16 v[32:35], v[124:127], v[68:71], v[32:35]\n"
"ds_read_b64 v[66:67], v3 offset:28928\n"
"s_waitcnt lgkmcnt(8)\n"
"v_mfma_f32_16x16x32_bf16 v[28:31], v[128:131], v[72:75], v[28:31]\n"
"ds_read_b64 v[68:69], v3 offset:29696\n"
"v_cvt_pk_bf16_f32 v37, v38, v39\n"
"v_mfma_f32_16x16x32_bf16 v[32:35], v[132:135], v[72:75], v[32:35]\n"
"ds_read_b64 v[70:71], v3 offset:29952\n"
"v_mul_f32 v40, v88, v40\n"
"v_mfma_f32_16x16x32_bf16 v[52:55], v[72:75], v[72:75], v[52:55]\n"
"v_mul_f32 v41, v88, v41\n"
"v_mul_f32 v42, v88, v42\n"
"v_mfma_f32_16x16x32_bf16 v[28:31], v[136:139], v[76:79], v[28:31]\n"
"ds_read_b64 v[72:73], v3 offset:30720\n"
"v_mul_f32 v43, v88, v43\n"
"v_mfma_f32_16x16x32_bf16 v[32:35], v[140:143], v[76:79], v[32:35]\n"
"ds_read_b64 v[74:75], v3 offset:30976\n"
"s_waitcnt lgkmcnt(6)\n"
"v_mfma_f32_16x16x32_bf16 v[28:31], v[144:147], v[56:59], v[28:31]\n"
"ds_read_b64 v[76:77], v3 offset:31744\n"
"ds_read_b128 v[84:87], v11 offset:4352\n"
"ds_read_b32 v91, v12 offset:4352\n"
"v_mfma_f32_16x16x32_bf16 v[32:35], v[148:151], v[56:59], v[32:35]\n"
"ds_read_b64 v[78:79], v3 offset:32000\n"
"v_fmac_f32 v40, v89, v20\n"
"v_mfma_f32_16x16x32_bf16 v[28:31], v[152:155], v[60:63], v[28:31]\n"
"ds_read_b64 v[56:57], v3 offset:32768\n"
"v_fmac_f32 v41, v89, v21\n"
"v_mfma_f32_16x16x32_bf16 v[32:35], v[156:159], v[60:63], v[32:35]\n"
"ds_read_b64 v[58:59], v3 offset:33024\n"
"s_waitcnt lgkmcnt(8)\n"
"v_mfma_f32_16x16x32_bf16 v[28:31], v[160:163], v[64:67], v[28:31]\n"
"ds_read_b64 v[60:61], v3 offset:33792\n"
"v_fmac_f32 v42, v89, v22\n"
"v_mfma_f32_16x16x32_bf16 v[32:35], v[164:167], v[64:67], v[32:35]\n"
"ds_read_b64 v[62:63], v3 offset:34048\n"
"v_fmac_f32 v43, v89, v23\n"
"v_mfma_f32_16x16x32_bf16 v[52:55], v[64:67], v[64:67], v[52:55]\n"
"v_cvt_pk_bf16_f32 v40, v40, v41\n"
"v_cvt_pk_bf16_f32 v41, v42, v43\n"
"v_mfma_f32_16x16x32_bf16 v[28:31], v[168:171], v[68:71], v[28:31]\n"
"ds_read_b64 v[64:65], v3 offset:34816\n"
"s_waitcnt lgkmcnt(7)\n"
"v_add_f32 v84, v84, v85\n"
"v_add_f32 v86, v86, v87\n"
"v_mfma_f32_16x16x32_bf16 v[32:35], v[172:175], v[68:71], v[32:35]\n"
"ds_read_b64 v[66:67], v3 offset:35072\n"
"v_add_f32 v84, v84, v86\n"
"s_waitcnt lgkmcnt(6)\n"
"v_mfma_f32_16x16x32_bf16 v[28:31], v[176:179], v[72:75], v[28:31]\n"
"ds_read_b64 v[68:69], v3 offset:35840\n"
"v_mul_f32 v89, 0x3acccccd, v91\n"
"v_fma_f32 v84, -v91, v89, v84\n"
"v_mfma_f32_16x16x32_bf16 v[32:35], v[180:183], v[72:75], v[32:35]\n"
"ds_read_b64 v[70:71], v3 offset:36096\n"
"v_fmamk_f32 v84, v84, 0x3acccccd, v27\n"
"v_mfma_f32_16x16x32_bf16 v[28:31], v[184:187], v[76:79], v[28:31]\n"
"ds_read_b64 v[72:73], v3 offset:36864\n"
"v_cmp_u_f32 vcc, v84, v84\n"
"v_mfma_f32_16x16x32_bf16 v[32:35], v[188:191], v[76:79], v[32:35]\n"
"ds_read_b64 v[74:75], v3 offset:37120\n"
"s_waitcnt lgkmcnt(6)\n"
"v_mfma_f32_16x16x32_bf16 v[28:31], v[192:195], v[56:59], v[28:31]\n"
"ds_read_b64 v[76:77], v4 offset:20480\n"
"v_max_f32 v84, v84, v27\n"
"v_mfma_f32_16x16x32_bf16 v[32:35], v[196:199], v[56:59], v[32:35]\n"
"ds_read_b64 v[78:79], v4 offset:20736\n"
"v_rsq_f32 v88, v84\n"
"v_mfma_f32_16x16x32_bf16 v[52:55], v[56:59], v[56:59], v[52:55]\n"
"v_mul_f32 v89, -v89, v88\n"
"v_mfma_f32_16x16x32_bf16 v[28:31], v[200:203], v[60:63], v[28:31]\n"
"ds_read_b64 v[56:57], v5 offset:20480\n"
"s_cbranch_vccnz .Lsl_212\n"
".Lbk_213:\n"
"v_mfma_f32_16x16x32_bf16 v[32:35], v[204:207], v[60:63], v[32:35]\n"
"ds_read_b64 v[58:59], v5 offset:20736\n"
"ds_write_b64 v7, v[36:37] offset:48896\n"
"s_waitcnt lgkmcnt(7)\n"
"v_mfma_f32_16x16x32_bf16 v[28:31], v[208:211], v[64:67], v[28:31]\n"
"ds_read_b64 v[60:61], v6 offset:20480\n"
"v_mul_f32 v44, v88, v44\n"
"v_mul_f32 v45, v88, v45\n"
"v_mfma_f32_16x16x32_bf16 v[32:35], v[212:215], v[64:67], v[32:35]\n"
"ds_read_b64 v[62:63], v6 offset:20736\n"
"v_mul_f32 v46, v88, v46\n"
"v_mul_f32 v47, v88, v47\n"
"v_mfma_f32_16x16x32_bf16 v[28:31], v[216:219], v[68:71], v[28:31]\n"
"ds_read_b64 v[64:65], v3 offset:40960\n"
"v_fmac_f32 v44, v89, v16\n"
"v_fmac_f32 v45, v89, v17\n"
"v_mfma_f32_16x16x32_bf16 v[32:35], v[220:223], v[68:71], v[32:35]\n"
"ds_read_b64 v[66:67], v3 offset:41216\n"
"v_fmac_f32 v46, v89, v18\n"
"s_waitcnt lgkmcnt(7)\n"
"v_mfma_f32_16x16x32_bf16 v[28:31], v[224:227], v[72:75], v[28:31]\n"
"ds_read_b64 v[68:69], v3 offset:45056\n"
"v_fmac_f32 v47, v89, v19\n"
"v_cvt_pk_bf16_f32 v44, v44, v45\n"
"v_mfma_f32_16x16x32_bf16 v[32:35], v[228:231], v[72:75], v[32:35]\n"
"ds_read_b64 v[70:71], v3 offset:45312\n"
"v_cvt_pk_bf16_f32 v45, v46, v47\n"
"v_mul_f32 v48, v88, v48\n"
"v_mfma_f32_16x16x32_bf16 v[52:55], v[72:75], v[72:75], v[52:55]\n"
"ds_write_b64 v8, v[40:41] offset:48896\n"
"v_mfma_f32_16x16x32_bf16 v[28:31], v[232:235], v[76:79], v[28:31]\n"
"ds_read_b64 v[72:73], v3 offset:49152\n"
"v_mul_f32 v49, v88, v49\n"
"v_mul_f32 v50, v88, v50\n"
"v_mfma_f32_16x16x32_bf16 v[32:35], v[236:239], v[76:79], v[32:35]\n"
"ds_read_b64 v[74:75], v3 offset:49408\n"
"v_mul_f32 v51, v88, v51\n"
"s_waitcnt lgkmcnt(7)\n"
"v_mfma_f32_16x16x32_bf16 v[28:31], v[240:243], v[56:59], v[28:31]\n"
"ds_read_b64 v[76:77], v3 offset:53248\n"
"v_cndmask_b32 v94, v52, v53, s[34:35]\n"
"v_fmac_f32 v48, v89, v20\n"
"v_mfma_f32_16x16x32_bf16 v[32:35], v[244:247], v[56:59], v[32:35]\n"
"ds_read_b64 v[78:79], v3 offset:53504\n"
"v_cndmask_b32 v54, v54, v55, s[34:35]\n"
"v_fmac_f32 v49, v89, v21\n"
"v_mfma_f32_16x16x32_bf16 v[28:31], v[248:251], v[60:63], v[28:31]\n"
"ds_read_b64 v[56:57], v3 offset:57344\n"
"v_cndmask_b32 v94, v94, v54, s[20:21]\n"
"v_fmac_f32 v50, v89, v22\n"
"v_mfma_f32_16x16x32_bf16 v[32:35], v[252:255], v[60:63], v[32:35]\n"
"ds_read_b64 v[58:59], v3 offset:57600\n"
"s_waitcnt lgkmcnt(7)\n"
"v_mfma_f32_16x16x32_bf16 v[36:39], v[96:99], v[64:67], 0\n"
"ds_read_b64 v[60:61], v3 offset:41984\n"
"v_fmac_f32 v51, v89, v23\n"
"v_mfma_f32_16x16x32_bf16 v[40:43], v[100:103], v[64:67], 0\n"
"ds_read_b64 v[62:63], v3 offset:42240\n"
"v_cndmask_b32 v94, v28, v94, s[22:23]\n"
"v_mfma_f32_16x16x32_bf16 v[52:55], v[64:67], v[64:67], 0\n"
"ds_write_b32 v9, v94 offset:4672\n"
"v_mfma_f32_16x16x32_bf16 v[36:39], v[128:131], v[68:71], v[36:39]\n"
"ds_read_b64 v[64:65], v3 offset:43008\n"
"v_cvt_pk_bf16_f32 v48, v48, v49\n"
"v_mfma_f32_16x16x32_bf16 v[40:43], v[132:135], v[68:71], v[40:43]\n"
"ds_read_b64 v[66:67], v3 offset:43264\n"
"v_cvt_pk_bf16_f32 v49, v50, v51\n"
"v_mfma_f32_16x16x32_bf16 v[52:55], v[68:71], v[68:71], v[52:55]\n"
"ds_write_b64 v7, v[44:45] offset:52864\n"
"s_waitcnt lgkmcnt(8)\n"
"v_mfma_f32_16x16x32_bf16 v[36:39], v[160:163], v[72:75], v[36:39]\n"
"ds_read_b64 v[68:69], v3 offset:44032\n"
"v_mfma_f32_16x16x32_bf16 v[40:43], v[164:167], v[72:75], v[40:43]\n"
"ds_read_b64 v[70:71], v3 offset:44288\n"
"v_mfma_f32_16x16x32_bf16 v[52:55], v[72:75], v[72:75], v[52:55]\n"
"ds_write_b64 v8, v[48:49] offset:52864\n"
"v_mfma_f32_16x16x32_bf16 v[36:39], v[192:195], v[76:79], v[36:39]\n"
"ds_read_b64 v[72:73], v3 offset:46080\n"
"v_mfma_f32_16x16x32_bf16 v[40:43], v[196:199], v[76:79], v[40:43]\n"
"ds_read_b64 v[74:75], v3 offset:46336\n"
"v_mfma_f32_16x16x32_bf16 v[52:55], v[76:79], v[76:79], v[52:55]\n"
"ds_read_b128 v[84:87], v11 offset:4672\n"
"ds_read_b32 v91, v12 offset:4672\n"
"s_waitcnt lgkmcnt(11)\n"
"v_mfma_f32_16x16x32_bf16 v[36:39], v[224:227], v[56:59], v[36:39]\n"
"ds_read_b64 v[76:77], v3 offset:47104\n"
"v_mfma_f32_16x16x32_bf16 v[40:43], v[228:231], v[56:59], v[40:43]\n"
"ds_read_b64 v[78:79], v3 offset:47360\n"
"v_mfma_f32_16x16x32_bf16 v[52:55], v[56:59], v[56:59], v[52:55]\n"
"v_mfma_f32_16x16x32_bf16 v[36:39], v[104:107], v[60:63], v[36:39]\n"
"ds_read_b64 v[56:57], v3 offset:48128\n"
"v_mfma_f32_16x16x32_bf16 v[40:43], v[108:111], v[60:63], v[40:43]\n"
"ds_read_b64 v[58:59], v3 offset:48384\n"
"s_waitcnt lgkmcnt(9)\n"
"v_mfma_f32_16x16x32_bf16 v[36:39], v[112:115], v[64:67], v[36:39]\n"
"ds_read_b64 v[60:61], v3 offset:50176\n"
"ds_write_b32 v10, v52 offset:5312\n"
"v_cndmask_b32 v94, v52, v53, s[34:35]\n"
"v_cndmask_b32 v54, v54, v55, s[34:35]\n"
"v_mfma_f32_16x16x32_bf16 v[40:43], v[116:119], v[64:67], v[40:43]\n"
"ds_read_b64 v[62:63], v3 offset:50432\n"
"s_waitcnt lgkmcnt(8)\n"
"v_add_f32 v84, v84, v85\n"
"v_add_f32 v86, v86, v87\n"
"v_cndmask_b32 v94, v94, v54, s[20:21]\n"
"ds_write_b32 v9, v94 offset:4992\n"
"v_mfma_f32_16x16x32_bf16 v[36:39], v[120:123], v[68:71], v[36:39]\n"
"ds_read_b64 v[64:65], v3 offset:51200\n"
"v_add_f32 v84, v84, v86\n"
"s_waitcnt lgkmcnt(9)\n"
"v_mul_f32 v89, 0x3acccccd, v91\n"
"v_mfma_f32_16x16x32_bf16 v[40:43], v[124:127], v[68:71], v[40:43]\n"
"ds_read_b64 v[66:67], v3 offset:51456\n"
"v_fma_f32 v84, -v91, v89, v84\n"
"s_waitcnt lgkmcnt(8)\n"
"v_mfma_f32_16x16x32_bf16 v[36:39], v[136:139], v[72:75], v[36:39]\n"
"ds_read_b64 v[68:69], v3 offset:52224\n"
"v_fmamk_f32 v84, v84, 0x3acccccd, v27\n"
"v_cmp_u_f32 vcc, v84, v84\n"
"v_mfma_f32_16x16x32_bf16 v[40:43], v[140:143], v[72:75], v[40:43]\n"
"ds_read_b64 v[70:71], v3 offset:52480\n"
"v_max_f32 v84, v84, v27\n"
"v_rsq_f32 v88, v84\n"
"v_mfma_f32_16x16x32_bf16 v[36:39], v[144:147], v[76:79], v[36:39]\n"
"ds_read_b64 v[72:73], v3 offset:54272\n"
"v_mul_f32 v89, -v89, v88\n"
"v_mfma_f32_16x16x32_bf16 v[40:43], v[148:151], v[76:79], v[40:43]\n"
"ds_read_b64 v[74:75], v3 offset:54528\n"
"s_cbranch_vccnz .Lsl_216\n"
".Lbk_217:\n"
"s_waitcnt lgkmcnt(7)\n"
"v_mfma_f32_16x16x32_bf16 v[36:39], v[152:155], v[56:59], v[36:39]\n"
"ds_read_b64 v[76:77], v3 offset:55296\n"
"v_mul_f32 v28, v88, v28\n"
"v_mul_f32 v29, v88, v29\n"
"v_mfma_f32_16x16x32_bf16 v[40:43], v[156:159], v[56:59], v[40:43]\n"
"ds_read_b64 v[78:79], v3 offset:55552\n"
"ds_read_b128 v[44:47], v11 offset:4992\n"
"ds_read_b128 v[48:51], v11 offset:5312\n"
"v_mfma_f32_16x16x32_bf16 v[36:39], v[168:171], v[60:63], v[36:39]\n"
"ds_read_b64 v[56:57], v3 offset:56320\n"
"v_mul_f32 v30, v88, v30\n"
"v_mul_f32 v31, v88, v31\n"
"v_mfma_f32_16x16x32_bf16 v[40:43], v[172:175], v[60:63], v[40:43]\n"
"ds_read_b64 v[58:59], v3 offset:56576\n"
"v_fmac_f32 v28, v89, v16\n"
"s_waitcnt lgkmcnt(8)\n"
"v_mfma_f32_16x16x32_bf16 v[36:39], v[176:179], v[64:67], v[36:39]\n"
"ds_read_b64 v[60:61], v4 offset:40960\n"
"v_fmac_f32 v29, v89, v17\n"
"v_fmac_f32 v30, v89, v18\n"
"v_mfma_f32_16x16x32_bf16 v[40:43], v[180:183], v[64:67], v[40:43]\n"
"ds_read_b64 v[62:63], v4 offset:41216\n"
"v_fmac_f32 v31, v89, v19\n"
"v_cvt_pk_bf16_f32 v28, v28, v29\n"
"v_mfma_f32_16x16x32_bf16 v[36:39], v[184:187], v[68:71], v[36:39]\n"
"ds_read_b64 v[64:65], v5 offset:40960\n"
"v_cvt_pk_bf16_f32 v29, v30, v31\n"
"ds_write_b64 v7, v[28:29] offset:56832\n"
"v_mfma_f32_16x16x32_bf16 v[40:43], v[188:191], v[68:71], v[40:43]\n"
"ds_read_b64 v[66:67], v5 offset:41216\n"
"v_mul_f32 v32, v88, v32\n"
"s_waitcnt lgkmcnt(9)\n"
"v_mfma_f32_16x16x32_bf16 v[36:39], v[200:203], v[72:75], v[36:39]\n"
"ds_read_b64 v[68:69], v6 offset:40960\n"
"s_waitcnt lgkmcnt(9)\n"
"v_add_f32 v44, v44, v45\n"
"v_mul_f32 v33, v88, v33\n"
"v_mfma_f32_16x16x32_bf16 v[40:43], v[204:207], v[72:75], v[40:43]\n"
"ds_read_b64 v[70:71], v6 offset:41216\n"
"v_add_f32 v46, v46, v47\n"
"v_mul_f32 v34, v88, v34\n"
"v_mfma_f32_16x16x32_bf16 v[36:39], v[208:211], v[76:79], v[36:39]\n"
"s_waitcnt lgkmcnt(9)\n"
"v_add_f32 v48, v48, v49\n"
"v_add_f32 v50, v50, v51\n"
"v_mul_f32 v35, v88, v35\n"
"v_mfma_f32_16x16x32_bf16 v[40:43], v[212:215], v[76:79], v[40:43]\n"
"v_add_f32 v48, v48, v50\n"
"v_fmac_f32 v32, v89, v20\n"
"s_waitcnt lgkmcnt(5)\n"
"v_mfma_f32_16x16x32_bf16 v[36:39], v[216:219], v[56:59], v[36:39]\n"
"v_add_f32 v44, v44, v46\n"
"v_mul_f32 v53, 0x3acccccd, v48\n"
"v_fmac_f32 v33, v89, v21\n"
"v_mfma_f32_16x16x32_bf16 v[40:43], v[220:223], v[56:59], v[40:43]\n"
"v_fma_f32 v44, -v48, v53, v44\n"
"v_fmamk_f32 v44, v44, 0x3acccccd, v27\n"
"v_fmac_f32 v34, v89, v22\n"
"v_mfma_f32_16x16x32_bf16 v[36:39], v[232:235], v[60:63], v[36:39]\n"
"v_cmp_u_f32 vcc, v44, v44\n"
"v_max_f32 v44, v44, v27\n"
"v_fmac_f32 v35, v89, v23\n"
"v_mfma_f32_16x16x32_bf16 v[40:43], v[236:239], v[60:63], v[40:43]\n"
"v_rsq_f32 v52, v44\n"
"s_waitcnt lgkmcnt(0)\n"
"v_mfma_f32_16x16x32_bf16 v[36:39], v[240:243], v[64:67], v[36:39]\n"
"v_mul_f32 v53, -v53, v52\n"
"v_cvt_pk_bf16_f32 v32, v32, v33\n"
"v_cvt_pk_bf16_f32 v33, v34, v35\n"
"v_mfma_f32_16x16x32_bf16 v[40:43], v[244:247], v[64:67], v[40:43]\n"
"s_cbranch_vccnz .Lsl_220\n"
".Lbk_221:\n"
"ds_write_b64 v8, v[32:33] offset:56832\n"
"v_mfma_f32_16x16x32_bf16 v[36:39], v[248:251], v[68:71], v[36:39]\n"
"v_mfma_f32_16x16x32_bf16 v[40:43], v[252:255], v[68:71], v[40:43]\n"
"v_cmp_u_f32 vcc, v92, v92\n"
"s_nop 3\n"
"v_mul_f32 v36, v52, v36\n"
"v_mul_f32 v37, v52, v37\n"
"v_mul_f32 v38, v52, v38\n"
"v_mul_f32 v39, v52, v39\n"
"v_fmac_f32 v36, v53, v16\n"
"v_fmac_f32 v37, v53, v17\n"
"v_fmac_f32 v38, v53, v18\n"
"v_fmac_f32 v39, v53, v19\n"
"v_cvt_pk_bf16_f32 v36, v36, v37\n"
"v_cvt_pk_bf16_f32 v37, v38, v39\n"
"ds_write_b64 v7, v[36:37] offset:40960\n"
"v_mul_f32 v40, v52, v40\n"
"v_mul_f32 v41, v52, v41\n"
"v_mul_f32 v42, v52, v42\n"
"v_mul_f32 v43, v52, v43\n"
"v_fmac_f32 v40, v53, v20\n"
"v_fmac_f32 v41, v53, v21\n"
"v_fmac_f32 v42, v53, v22\n"
"v_fmac_f32 v43, v53, v23\n"
"v_cvt_pk_bf16_f32 v40, v40, v41\n"
"v_cvt_pk_bf16_f32 v41, v42, v43\n"
"ds_write_b64 v8, v[40:41] offset:40960\n"
"s_cbranch_vccnz .Lrd_228\n"
".Lgo_187:\n"
"ds_write_b32 v13, v13 offset:480\n"
"v_mov_b32 v46, 0\n"
"v_mov_b32 v47, 0\n"
"v_mov_b32 v50, 0\n"
"v_mov_b32 v51, 0\n"
"ds_read_b32 v90, v14 offset:480\n"
"s_mov_b64 exec, 0xffff\n"
"ds_read_b64 v[46:47], v24 offset:44960\n"
"ds_read_b64 v[50:51], v24 offset:45000\n"
"s_mov_b64 exec, -1\n"
"ds_read_b64 v[44:45], v25 offset:44928\n"
"ds_read_b64 v[56:57], v25 offset:41040\n"
"ds_read_b64 v[58:59], v24 offset:41072\n"
"ds_read_b64 v[64:65], v25 offset:45008\n"
"ds_read_b64 v[66:67], v24 offset:45040\n"
"ds_read_b64 v[48:49], v25 offset:44968\n"
"ds_read_b64 v[60:61], v25 offset:41080\n"
"ds_read_b64 v[62:63], v24 offset:41112\n"
"ds_read_b64 v[68:69], v25 offset:45048\n"
"ds_read_b64 v[70:71], v24 offset:45080\n"
"s_mov_b32 s17, 0x100000\n"
"s_waitcnt lgkmcnt(12)\n"
"v_cmp_eq_u32 vcc, 0, v90\n"
"s_cbranch_vccz .Lok_269\n"
".Ltp_268:\n"
"ds_read_b32 v90, v14 offset:480\n"
"s_mov_b64 exec, 0xffff\n"
"ds_read_b64 v[46:47], v24 offset:44960\n"
"ds_read_b64 v[50:51], v24 offset:45000\n"
"s_mov_b64 exec, -1\n"
"ds_read_b64 v[44:45], v25 offset:44928\n"
"ds_read_b64 v[56:57], v25 offset:41040\n"
"ds_read_b64 v[58:59], v24 offset:41072\n"
"ds_read_b64 v[64:65], v25 offset:45008\n"
"ds_read_b64 v[66:67], v24 offset:45040\n"
"ds_read_b64 v[48:49], v25 offset:44968\n"
"ds_read_b64 v[60:61], v25 offset:41080\n"
"ds_read_b64 v[62:63], v24 offset:41112\n"
"ds_read_b64 v[68:69], v25 offset:45048\n"
"ds_read_b64 v[70:71], v24 offset:45080\n"
"s_sub_u32 s17, s17, 1\n"
"s_cbranch_scc1 .Lok_269\n"
"s_waitcnt lgkmcnt(12)\n"
"v_cmp_eq_u32 vcc, 0, v90\n"
"s_cbranch_vccnz .Ltp_268\n"
"s_waitcnt lgkmcnt(0)\n"
".Lok_269:\n"
"ds_read_b64_tr_b16 v[28:29], v26 offset:40960\n"
"ds_read_b64_tr_b16 v[30:31], v26 offset:44928\n"
"ds_read_b64_tr_b16 v[32:33], v26 offset:41000\n"
"s_waitcnt lgkmcnt(10)\n"
"v_mfma_f32_16x16x32_bf16 v[56:59], v[56:59], v[44:47], 0\n"
"s_waitcnt lgkmcnt(8)\n"
"v_mfma_f32_16x16x32_bf16 v[64:67], v[64:67], v[44:47], 0\n"
"ds_read_b64_tr_b16 v[34:35], v26 offset:44968\n"
"ds_read_b64_tr_b16 v[36:37], v26 offset:40992\n"
"ds_read_b64_tr_b16 v[38:39], v26 offset:44960\n"
"s_waitcnt lgkmcnt(8)\n"
"v_mfma_f32_16x16x32_bf16 v[60:63], v[60:63], v[48:51], 0\n"
"s_waitcnt lgkmcnt(6)\n"
"v_mfma_f32_16x16x32_bf16 v[68:71], v[68:71], v[48:51], 0\n"
"ds_read_b64_tr_b16 v[40:41], v26 offset:41032\n"
"ds_read_b64_tr_b16 v[42:43], v26 offset:45000\n"
"v_mov_b32 v84, 0x3f803f80\n"
"v_mov_b32 v85, 0x3f803f80\n"
"v_mov_b32 v86, 0x3f803f80\n"
"v_mov_b32 v87, 0x3f803f80\n"
"v_cndmask_b32 v76, v56, v57, s[34:35]\n"
"v_cndmask_b32 v72, v64, v65, s[34:35]\n"
"v_cndmask_b32 v76, v76, v72, s[36:37]\n"
"v_cndmask_b32 v77, v58, v59, s[34:35]\n"
"v_cndmask_b32 v72, v66, v67, s[34:35]\n"
"v_cndmask_b32 v77, v77, v72, s[38:39]\n"
"v_max_f32 v52, v76, v77\n"
"v_max_f32 v54, v76, v77\n"
"v_cndmask_b32 v78, v60, v61, s[34:35]\n"
"v_cndmask_b32 v72, v68, v69, s[34:35]\n"
"v_cndmask_b32 v78, v78, v72, s[36:37]\n"
"v_cndmask_b32 v79, v62, v63, s[34:35]\n"
"v_cndmask_b32 v72, v70, v71, s[34:35]\n"
"v_cndmask_b32 v79, v79, v72, s[38:39]\n"
"v_max_f32 v53, v78, v79\n"
"v_max_f32 v55, v78, v79\n"
"v_permlane16_swap_b32 v52, v54\n"
"s_nop 0\n"
"v_permlane16_swap_b32 v53, v55\n"
"v_max_f32 v72, v52, v54\n"
"v_max_f32 v52, v52, v54\n"
"v_max_f32 v73, v53, v55\n"
"v_max_f32 v53, v53, v55\n"
"v_permlane32_swap_b32 v52, v72\n"
"s_nop 0\n"
"v_permlane32_swap_b32 v53, v73\n"
"v_max_f32 v52, v52, v72\n"
"v_max_f32 v53, v53, v73\n"
"v_mul_f32 v52, 0xbea52b5b, v52\n"
"v_mul_f32 v53, 0xbea52b5b, v53\n"
"v_fmamk_f32 v76, v76, 0x3ea52b5b, v52\n"
"v_fmamk_f32 v77, v77, 0x3ea52b5b, v52\n"
"v_fmamk_f32 v78, v78, 0x3ea52b5b, v53\n"
"v_fmamk_f32 v79, v79, 0x3ea52b5b, v53\n"
"v_exp_f32 v76, v76\n"
"v_exp_f32 v77, v77\n"
"v_exp_f32 v78, v78\n"
"v_exp_f32 v79, v79\n"
"v_cvt_pk_bf16_f32 v52, v76, v77\n"
"v_cvt_pk_bf16_f32 v53, v78, v79\n"
"v_perm_b32 v44, v52, v52, v80\n"
"v_perm_b32 v45, v52, v52, v81\n"
"v_perm_b32 v46, v52, v52, v82\n"
"v_perm_b32 v47, v52, v52, v83\n"
"v_perm_b32 v48, v53, v53, v80\n"
"v_perm_b32 v49, v53, v53, v81\n"
"v_perm_b32 v50, v53, v53, v82\n"
"v_perm_b32 v51, v53, v53, v83\n"
"v_mfma_f32_16x16x32_bf16 v[56:59], v[84:87], v[44:47], 0\n"
"s_nop 0\n"
"v_mfma_f32_16x16x32_bf16 v[60:63], v[84:87], v[48:51], 0\n"
"s_waitcnt lgkmcnt(6)\n"
"v_mfma_f32_16x16x32_bf16 v[28:31], v[28:31], v[44:47], 0\n"
"s_waitcnt lgkmcnt(4)\n"
"v_mfma_f32_16x16x32_bf16 v[32:35], v[32:35], v[48:51], 0\n"
"s_waitcnt lgkmcnt(2)\n"
"v_mfma_f32_16x16x32_bf16 v[36:39], v[36:39], v[44:47], 0\n"
"s_waitcnt lgkmcnt(0)\n"
"v_mfma_f32_16x16x32_bf16 v[40:43], v[40:43], v[48:51], 0\n"
"v_cndmask_b32 v88, 2.0, v1, s[40:41]\n"
"v_rcp_f32 v52, v56\n"
"v_rcp_f32 v53, v60\n"
"s_add_u32 s31, s30, 0xba0000\n"
"v_mul_f32 v28, v52, v28\n"
"v_mul_f32 v29, v52, v29\n"
"v_mul_f32 v30, v52, v30\n"
"v_mul_f32 v31, v52, v31\n"
"v_mul_f32 v32, v53, v32\n"
"v_mul_f32 v33, v53, v33\n"
"v_mul_f32 v34, v53, v34\n"
"v_mul_f32 v35, v53, v35\n"
"v_cvt_pk_bf16_f32 v28, v28, v29\n"
"v_cvt_pk_bf16_f32 v29, v30, v31\n"
"v_cvt_pk_bf16_f32 v30, v32, v33\n"
"v_cvt_pk_bf16_f32 v31, v34, v35\n"
"v_min_u32 v28, -2, v28\n"
"v_min_u32 v29, -2, v29\n"
"v_min_u32 v30, -2, v30\n"
"v_min_u32 v31, -2, v31\n"
"buffer_store_dwordx4 v[28:31], v1, s[4:7], s31 offen offset:0 sc1\n"
"v_mul_f32 v36, v52, v36\n"
"v_mul_f32 v37, v52, v37\n"
"v_mul_f32 v38, v52, v38\n"
"v_mul_f32 v39, v52, v39\n"
"v_mul_f32 v40, v53, v40\n"
"v_mul_f32 v41, v53, v41\n"
"v_mul_f32 v42, v53, v42\n"
"v_mul_f32 v43, v53, v43\n"
"v_cvt_pk_bf16_f32 v36, v36, v37\n"
"v_cvt_pk_bf16_f32 v37, v38, v39\n"
"v_cvt_pk_bf16_f32 v38, v40, v41\n"
"v_cvt_pk_bf16_f32 v39, v42, v43\n"
"v_min_u32 v36, -2, v36\n"
"v_min_u32 v37, -2, v37\n"
"v_min_u32 v38, -2, v38\n"
"v_min_u32 v39, -2, v39\n"
"buffer_store_dwordx4 v[36:39], v88, s[4:7], s31 offen offset:1024 sc1\n"
"ds_write_b32 v13, v13 offset:496\n"
"s_nop 2\n"
"s_cmp_lt_i32 s45, 0\n"
"s_cbranch_scc1 .Lnc_270\n"
"s_setprio 0\n"
"s_lshl_b32 s79, s3, 10\n"
"v_lshl_add_u32 v34, v0, 4, s79\n"
"v_lshrrev_b32 v59, 7, v34\n"
"v_and_b32 v58, 0x70, v34\n"
"s_mov_b32 s79, 0x5000\n"
"v_mad_i32_i24 v33, v59, s79, v58\n"
"s_mov_b32 s17, 0x100000\n"
".Ltp_271:\n"
"s_sleep 1\n"
"ds_read_b32 v42, v14 offset:512\n"
"s_sub_u32 s17, s17, 1\n"
"s_cbranch_scc1 .Lou_272\n"
"s_waitcnt lgkmcnt(0)\n"
"v_cmp_eq_u32 vcc, 0, v42\n"
"s_cbranch_vccnz .Ltp_271\n"
".Lou_272:\n"
"s_waitcnt lgkmcnt(0)\n"
"s_lshl_b32 s79, s45, 8\n"
"s_add_u32 s46, s79, 0x1048000\n"
"s_add_u32 s47, s46, 0x140000\n"
"s_and_b32 s79, s2, 7\n"
"s_lshl_b32 s79, s79, 14\n"
"s_add_u32 s48, s79, 0x1588000\n"
"s_add_u32 s49, s48, 0x1000\n"
"v_and_b32 v94, 0xffffff80, v33\n"
"v_add_u32 v94, v94, v33\n"
"buffer_load_dwordx4 v[78:81], v94, s[4:7], s46 offen offset:0 sc1\n"
"buffer_load_dwordx4 v[82:85], v94, s[4:7], s46 offen offset:128 sc1\n"
"buffer_load_dwordx4 v[86:89], v94, s[4:7], s47 offen offset:0 sc1\n"
"buffer_load_dwordx4 v[90:93], v94, s[4:7], s47 offen offset:128 sc1\n"
"v_and_b32 v51, 15, v0\n"
"v_and_b32 v50, 1, v51\n"
"v_and_b32 v49, 2, v51\n"
"v_lshlrev_b32 v48, 4, v0\n"
"v_lshrrev_b32 v47, 4, v0\n"
"v_xor_b32 v46, s3, v51\n"
"v_and_b32 v45, 1, v46\n"
"v_lshlrev_b32 v44, 1, v45\n"
"s_lshl_b32 s79, s3, 10\n"
"v_add_u32 v59, s79, v48\n"
"v_lshrrev_b32 v59, 2, v59\n"
"s_lshl_b32 s79, s3, 10\n"
"v_add_u32 v58, s79, v48\n"
"v_lshrrev_b32 v58, 6, v58\n"
"v_and_b32 v58, 24, v58\n"
"v_xor_b32 v59, v59, v58\n"
"v_add_u32 v36, 0xa000, v59\n"
"s_lshl_b32 s79, s3, 8\n"
"s_add_u32 s79, s79, 0xa000\n"
"v_lshrrev_b32 v59, 2, v51\n"
"v_lshrrev_b32 v58, 1, v47\n"
"v_xor_b32 v59, v59, v58\n"
"v_and_b32 v59, 1, v59\n"
"v_add_u32 v59, v44, v59\n"
"v_lshrrev_b32 v58, 2, v51\n"
"v_lshrrev_b32 v57, 3, v51\n"
"v_lshlrev_b32 v57, 7, v57\n"
"v_lshl_add_u32 v58, v58, 11, v57\n"
"v_lshl_add_u32 v58, v47, 6, v58\n"
"v_lshl_add_u32 v58, v49, 4, v58\n"
"v_lshl_add_u32 v59, v59, 3, v58\n"
"v_add_u32 v31, s79, v59\n"
"s_mul_i32 s79, s3, 0x2100\n"
"s_add_u32 s79, s79, 0x13840\n"
"v_lshlrev_b32 v59, 3, v47\n"
"v_sub_u32 v59, v59, v51\n"
"v_add_u32 v59, 0x3ef, v59\n"
"v_lshrrev_b32 v59, 2, v59\n"
"v_lshlrev_b32 v58, 3, v47\n"
"v_sub_u32 v58, v58, v51\n"
"v_add_u32 v58, 0x3ef, v58\n"
"v_and_b32 v58, 3, v58\n"
"v_mul_i32_i24 v58, 0x840, v58\n"
"v_lshl_add_u32 v59, v59, 3, v58\n"
"v_add_u32 v28, s79, v59\n"
"s_mul_i32 s79, s3, 0x1200\n"
"s_add_u32 s79, s79, 0x1c420\n"
"v_lshlrev_b32 v59, 4, v47\n"
"v_lshl_add_u32 v59, v51, 5, v59\n"
"v_add_u32 v29, s79, v59\n"
"s_lshl_b32 s79, s3, 11\n"
"s_lshr_b32 s78, s3, 1\n"
"s_lshl_b32 s78, s78, 7\n"
"s_add_u32 s79, s79, s78\n"
"s_add_u32 s79, s79, 0xa000\n"
"s_and_b32 s78, s3, 1\n"
"v_lshrrev_b32 v59, 2, v51\n"
"v_xor_b32 v59, v47, v59\n"
"v_xor_b32 v59, s78, v59\n"
"v_and_b32 v59, 3, v59\n"
"v_lshlrev_b32 v59, 3, v59\n"
"v_lshl_add_u32 v59, v51, 5, v59\n"
"v_add_u32 v30, s79, v59\n"
"v_xor_b32 v32, 8, v31\n"
"s_lshl_b32 s79, s3, 10\n"
"v_add_u32 v59, s79, v48\n"
"v_lshrrev_b32 v59, 2, v59\n"
"v_add_u32 v35, 0x1c600, v59\n"
"v_xor_b32 v37, 8, v36\n"
"v_mov_b32 v59, 0xffff0000\n"
"v_mov_b32 v58, 0xffff\n"
"v_cmp_eq_i32 vcc, 1, v50\n"
"s_nop 1\n"
"v_cndmask_b32 v59, v58, v59, vcc\n"
"v_lshrrev_b32 v58, 1, v51\n"
"v_lshlrev_b32 v57, 1, v47\n"
"v_cmp_eq_i32 vcc, v58, v57\n"
"s_nop 1\n"
"v_cndmask_b32 v38, 0, v59, vcc\n"
"v_mov_b32 v59, 0xffff0000\n"
"v_mov_b32 v58, 0xffff\n"
"v_cmp_eq_i32 vcc, 1, v50\n"
"s_nop 1\n"
"v_cndmask_b32 v59, v58, v59, vcc\n"
"v_lshrrev_b32 v58, 1, v51\n"
"v_lshl_add_u32 v57, v47, 1, 1\n"
"v_cmp_eq_i32 vcc, v58, v57\n"
"s_nop 1\n"
"v_cndmask_b32 v39, 0, v59, vcc\n"
"s_lshl_b32 s79, s3, 2\n"
"s_add_u32 s79, s79, 0x26800\n"
"v_mov_b32 v40, s79\n"
"s_lshl_b32 s79, s3, 10\n"
"v_add_u32 v59, s79, v48\n"
"v_lshrrev_b32 v59, 6, v59\n"
"s_lshl_b32 s79, s3, 10\n"
"v_add_u32 v58, s79, v48\n"
"v_and_b32 v58, 48, v58\n"
"v_mul_i32_i24 v58, 0x50, v58\n"
"s_mov_b32 s79, 0x2800\n"
"v_mad_i32_i24 v41, v59, s79, v58\n"
"ds_read_b32 v40, v40\n"
"s_waitcnt lgkmcnt(0)\n"
"v_sub_u32 v44, 0x7f000000, v40\n"
"v_lshrrev_b32 v45, 16, v44\n"
"v_or_b32 v44, v44, v45\n"
"v_and_b32 v38, v44, v38\n"
"v_and_b32 v39, v44, v39\n"
"s_mov_b32 s52, 0x05040100\n"
"s_mov_b32 s53, 0x07060302\n"
"s_mov_b32 s17, 32768\n"
".Ltp_273:\n"
"s_waitcnt vmcnt(0)\n"
"s_sub_u32 s17, s17, 1\n"
"s_cbranch_scc1 .Lou_274\n"
"v_bitop3_b32 v54, v79, v81, s16 bitop3:0x7e\n"
"v_bitop3_b32 v55, v83, v85, s16 bitop3:0x7e\n"
"v_bitop3_b32 v56, v87, v89, s16 bitop3:0x7e\n"
"v_bitop3_b32 v57, v91, v93, s16 bitop3:0x7e\n"
"v_or3_b32 v58, v54, v55, v56\n"
"v_or_b32 v58, v58, v57\n"
"v_cmp_ne_u32 vcc, 0, v58\n"
"s_cbranch_vccz .Lou_274\n"
"s_sleep 2\n"
"v_cmp_ne_u32 vcc, 0, v54\n"
"s_cbranch_vccz .Lsk_275\n"
"s_mov_b64 exec, vcc\n"
"buffer_load_dwordx4 v[78:81], v94, s[4:7], s46 offen offset:0 sc1\n"
"s_mov_b64 exec, -1\n"
".Lsk_275:\n"
"v_cmp_ne_u32 vcc, 0, v55\n"
"s_cbranch_vccz .Lsk_276\n"
"s_mov_b64 exec, vcc\n"
"buffer_load_dwordx4 v[82:85], v94, s[4:7], s46 offen offset:128 sc1\n"
"s_mov_b64 exec, -1\n"
".Lsk_276:\n"
"v_cmp_ne_u32 vcc, 0, v56\n"
"s_cbranch_vccz .Lsk_277\n"
"s_mov_b64 exec, vcc\n"
"buffer_load_dwordx4 v[86:89], v94, s[4:7], s47 offen offset:0 sc1\n"
"s_mov_b64 exec, -1\n"
".Lsk_277:\n"
"v_cmp_ne_u32 vcc, 0, v57\n"
"s_cbranch_vccz .Lsk_278\n"
"s_mov_b64 exec, vcc\n"
"buffer_load_dwordx4 v[90:93], v94, s[4:7], s47 offen offset:128 sc1\n"
"s_mov_b64 exec, -1\n"
".Lsk_278:\n"
"s_branch .Ltp_273\n"
".Lou_274:\n"
"s_waitcnt vmcnt(0)\n"
"buffer_load_dwordx4 v[70:73], v34, s[4:7], s48 offen sc1\n"
"buffer_load_dwordx4 v[74:77], v34, s[4:7], s49 offen sc1\n"
"v_perm_b32 v54, v82, v78, s52\n"
"v_perm_b32 v55, v82, v78, s53\n"
"v_perm_b32 v56, v84, v80, s52\n"
"v_perm_b32 v57, v84, v80, s53\n"
"ds_write_b32 v36, v54 offset:0\n"
"ds_write_b32 v37, v55 offset:2048\n"
"ds_write_b32 v36, v56 offset:4224\n"
"ds_write_b32 v37, v57 offset:6272\n"
"v_perm_b32 v54, v90, v86, s52\n"
"v_perm_b32 v55, v90, v86, s53\n"
"v_perm_b32 v56, v92, v88, s52\n"
"v_perm_b32 v57, v92, v88, s53\n"
"ds_write_b32 v36, v54 offset:1024\n"
"ds_write_b32 v37, v55 offset:3072\n"
"ds_write_b32 v36, v56 offset:5248\n"
"ds_write_b32 v37, v57 offset:7296\n"
"v_lshlrev_b32 v54, 16, v78\n"
"v_lshlrev_b32 v55, 16, v82\n"
"v_and_b32 v56, 0xffff0000, v78\n"
"v_and_b32 v57, 0xffff0000, v82\n"
"v_lshlrev_b32 v58, 16, v80\n"
"v_lshlrev_b32 v59, 16, v84\n"
"v_and_b32 v60, 0xffff0000, v80\n"
"v_and_b32 v61, 0xffff0000, v84\n"
"v_lshlrev_b32 v62, 16, v86\n"
"v_lshlrev_b32 v63, 16, v90\n"
"v_and_b32 v64, 0xffff0000, v86\n"
"v_and_b32 v65, 0xffff0000, v90\n"
"v_lshlrev_b32 v66, 16, v88\n"
"v_lshlrev_b32 v67, 16, v92\n"
"v_and_b32 v68, 0xffff0000, v88\n"
"v_and_b32 v69, 0xffff0000, v92\n"
"s_mov_b32 s17, 32768\n"
".Ltp_279:\n"
"s_waitcnt vmcnt(0)\n"
"s_sub_u32 s17, s17, 1\n"
"s_cbranch_scc1 .Lou_280\n"
"v_max_u32 v78, v70, v71\n"
"v_max3_u32 v78, v72, v73, v78\n"
"v_max3_u32 v78, v74, v75, v78\n"
"v_max3_u32 v78, v76, v77, v78\n"
"v_cmp_eq_u32 vcc, -1, v78\n"
"s_cbranch_vccz .Lou_280\n"
"v_max_u32 v78, v72, v73\n"
"v_max3_u32 v78, v70, v71, v78\n"
"v_cmp_eq_u32 vcc, -1, v78\n"
"s_cbranch_vccz .Lsk_281\n"
"s_mov_b64 exec, vcc\n"
"buffer_load_dwordx4 v[70:73], v34, s[4:7], s48 offen sc1\n"
"s_mov_b64 exec, -1\n"
".Lsk_281:\n"
"v_max_u32 v78, v76, v77\n"
"v_max3_u32 v78, v74, v75, v78\n"
"v_cmp_eq_u32 vcc, -1, v78\n"
"s_cbranch_vccz .Lsk_282\n"
"s_mov_b64 exec, vcc\n"
"buffer_load_dwordx4 v[74:77], v34, s[4:7], s49 offen sc1\n"
"s_mov_b64 exec, -1\n"
".Lsk_282:\n"
"s_branch .Ltp_279\n"
".Lou_280:\n"
"s_waitcnt vmcnt(0)\n"
"v_fma_f32 v54, v54, v71, v70\n"
"v_fma_f32 v55, v55, v73, v72\n"
"v_cvt_pk_f16_f32 v54, v54, v55\n"
"v_fma_f32 v56, v56, v71, v70\n"
"v_fma_f32 v57, v57, v73, v72\n"
"v_cvt_pk_f16_f32 v56, v56, v57\n"
"v_fma_f32 v58, v58, v71, v70\n"
"v_fma_f32 v59, v59, v73, v72\n"
"v_cvt_pk_f16_f32 v58, v58, v59\n"
"v_fma_f32 v60, v60, v71, v70\n"
"v_fma_f32 v61, v61, v73, v72\n"
"v_cvt_pk_f16_f32 v60, v60, v61\n"
"ds_write2st64_b32 v35, v54, v56 offset0:0 offset1:18\n"
"ds_write2st64_b32 v35, v58, v60 offset0:36 offset1:54\n"
"v_fma_f32 v62, v62, v75, v74\n"
"v_fma_f32 v63, v63, v77, v76\n"
"v_cvt_pk_f16_f32 v62, v62, v63\n"
"v_fma_f32 v64, v64, v75, v74\n"
"v_fma_f32 v65, v65, v77, v76\n"
"v_cvt_pk_f16_f32 v64, v64, v65\n"
"v_fma_f32 v66, v66, v75, v74\n"
"v_fma_f32 v67, v67, v77, v76\n"
"v_cvt_pk_f16_f32 v66, v66, v67\n"
"v_fma_f32 v68, v68, v75, v74\n"
"v_fma_f32 v69, v69, v77, v76\n"
"v_cvt_pk_f16_f32 v68, v68, v69\n"
"ds_write2st64_b32 v35, v62, v64 offset0:4 offset1:22\n"
"ds_write2st64_b32 v35, v66, v68 offset0:40 offset1:58\n"
"ds_write_b32 v13, v13 offset:528\n"
"s_mov_b32 s17, 0x100000\n"
".Ltp_283:\n"
"ds_read_b32 v42, v14 offset:528\n"
"s_sub_u32 s17, s17, 1\n"
"s_cbranch_scc1 .Lou_284\n"
"s_waitcnt lgkmcnt(0)\n"
"v_cmp_eq_u32 vcc, 0, v42\n"
"s_cbranch_vccnz .Ltp_283\n"
".Lou_284:\n"
"s_waitcnt lgkmcnt(0)\n"
"s_mov_b32 s50, 0xfffefffe\n"
"ds_read_b64 v[60:61], v28 offset:1984\n"
"ds_read_b64 v[62:63], v28 offset:1992\n"
"ds_read_b128 v[64:67], v29 offset:1984\n"
"ds_read_b128 v[68:71], v29 offset:1472\n"
"ds_read_b64 v[72:73], v28 offset:1472\n"
"ds_read_b64 v[74:75], v28 offset:1480\n"
"ds_read_b128 v[76:79], v29 offset:960\n"
"s_waitcnt lgkmcnt(0)\n"
"v_mfma_f32_16x16x32_f16 v[56:59], v[60:63], v[64:67], 0\n"
"ds_read_b64 v[80:81], v28 offset:960\n"
"ds_read_b64 v[82:83], v28 offset:968\n"
"v_mfma_f32_16x16x32_f16 v[52:55], v[60:63], v[68:71], 0\n"
"ds_read_b128 v[84:87], v29 offset:448\n"
"ds_read_b64 v[88:89], v28 offset:448\n"
"v_mfma_f32_16x16x32_f16 v[56:59], v[72:75], v[68:71], v[56:59]\n"
"ds_read_b64 v[90:91], v28 offset:456\n"
"ds_read_b64 v[92:93], v28 offset:1920\n"
"v_mfma_f32_16x16x32_f16 v[48:51], v[60:63], v[76:79], 0\n"
"ds_read_b64 v[94:95], v28 offset:1928\n"
"s_waitcnt lgkmcnt(4)\n"
"v_mfma_f32_16x16x32_f16 v[52:55], v[72:75], v[76:79], v[52:55]\n"
"ds_read_b128 v[2:5], v29 offset:1920\n"
"ds_read_b128 v[34:37], v29 offset:1408\n"
"v_mfma_f32_16x16x32_f16 v[56:59], v[80:83], v[76:79], v[56:59]\n"
"ds_read_b64 v[64:65], v28 offset:1408\n"
"ds_read_b64 v[66:67], v28 offset:1416\n"
"v_mfma_f32_16x16x32_f16 v[44:47], v[60:63], v[84:87], 0\n"
"ds_read_b128 v[68:71], v29 offset:896\n"
"ds_read_b64 v[76:77], v28 offset:896\n"
"v_mfma_f32_16x16x32_f16 v[48:51], v[72:75], v[84:87], v[48:51]\n"
"ds_read_b64 v[78:79], v28 offset:904\n"
"s_waitcnt lgkmcnt(5)\n"
"v_mfma_f32_16x16x32_f16 v[52:55], v[80:83], v[84:87], v[52:55]\n"
"ds_read_b128 v[60:63], v29 offset:384\n"
"ds_read_b64 v[72:73], v28 offset:384\n"
"v_mfma_f32_16x16x32_f16 v[56:59], v[88:91], v[84:87], v[56:59]\n"
"ds_read_b64 v[74:75], v28 offset:392\n"
"ds_read_b64 v[80:81], v28 offset:1856\n"
"v_mfma_f32_16x16x32_f16 v[56:59], v[92:95], v[2:5], v[56:59]\n"
"ds_read_b64 v[82:83], v28 offset:1864\n"
"ds_read_b128 v[84:87], v29 offset:1856\n"
"v_mfma_f32_16x16x32_f16 v[52:55], v[92:95], v[34:37], v[52:55]\n"
"ds_read_b128 v[88:91], v29 offset:1344\n"
"s_waitcnt lgkmcnt(7)\n"
"v_mfma_f32_16x16x32_f16 v[56:59], v[64:67], v[34:37], v[56:59]\n"
"ds_read_b64 v[2:3], v30 offset:0\n"
"ds_read_b64 v[34:35], v28 offset:1344\n"
"v_mfma_f32_16x16x32_f16 v[48:51], v[92:95], v[68:71], v[48:51]\n"
"ds_read_b64 v[36:37], v28 offset:1352\n"
"v_mfma_f32_16x16x32_f16 v[52:55], v[64:67], v[68:71], v[52:55]\n"
"v_mfma_f32_16x16x32_f16 v[56:59], v[76:79], v[68:71], v[56:59]\n"
"ds_read_b128 v[68:71], v29 offset:832\n"
"s_waitcnt lgkmcnt(8)\n"
"v_mfma_f32_16x16x32_f16 v[44:47], v[92:95], v[60:63], v[44:47]\n"
"ds_read_b64 v[92:93], v28 offset:832\n"
"v_mfma_f32_16x16x32_f16 v[48:51], v[64:67], v[60:63], v[48:51]\n"
"ds_read_b64 v[94:95], v28 offset:840\n"
"v_mfma_f32_16x16x32_f16 v[52:55], v[76:79], v[60:63], v[52:55]\n"
"ds_read_b128 v[64:67], v29 offset:320\n"
"ds_read_b64 v[76:77], v28 offset:320\n"
"v_mfma_f32_16x16x32_f16 v[56:59], v[72:75], v[60:63], v[56:59]\n"
"ds_read_b64 v[78:79], v28 offset:328\n"
"s_waitcnt lgkmcnt(6)\n"
"v_mfma_f32_16x16x32_f16 v[56:59], v[80:83], v[84:87], v[56:59]\n"
"ds_read_b64 v[60:61], v30 offset:512\n"
"ds_read_b64 v[72:73], v28 offset:1792\n"
"v_mfma_f32_16x16x32_f16 v[52:55], v[80:83], v[88:91], v[52:55]\n"
"ds_read_b64 v[74:75], v28 offset:1800\n"
"s_lshl_b32 s46, s45, 3\n"
"v_mfma_f32_16x16x16_bf16 v[44:47], v[38:39], v[2:3], v[44:47]\n"
"ds_read_b128 v[84:87], v29 offset:1792\n"
"v_mfma_f32_16x16x32_f16 v[56:59], v[34:37], v[88:91], v[56:59]\n"
"ds_read_b128 v[2:5], v29 offset:1280\n"
"s_waitcnt lgkmcnt(7)\n"
"v_mfma_f32_16x16x32_f16 v[48:51], v[80:83], v[68:71], v[48:51]\n"
"ds_read_b64 v[88:89], v28 offset:1280\n"
"v_mfma_f32_16x16x32_f16 v[52:55], v[34:37], v[68:71], v[52:55]\n"
"ds_read_b64 v[90:91], v28 offset:1288\n"
"v_mfma_f32_16x16x32_f16 v[56:59], v[92:95], v[68:71], v[56:59]\n"
"ds_read_b128 v[68:71], v29 offset:768\n"
"v_mfma_f32_16x16x32_f16 v[44:47], v[80:83], v[64:67], v[44:47]\n"
"ds_read_b64 v[80:81], v28 offset:768\n"
"s_waitcnt lgkmcnt(8)\n"
"v_mfma_f32_16x16x32_f16 v[48:51], v[34:37], v[64:67], v[48:51]\n"
"ds_read_b64 v[82:83], v28 offset:776\n"
"v_mfma_f32_16x16x32_f16 v[52:55], v[92:95], v[64:67], v[52:55]\n"
"ds_read_b128 v[34:37], v29 offset:256\n"
"ds_read_b64 v[92:93], v28 offset:256\n"
"v_mfma_f32_16x16x32_f16 v[56:59], v[76:79], v[64:67], v[56:59]\n"
"ds_read_b64 v[94:95], v28 offset:264\n"
"ds_read_b64 v[64:65], v28 offset:1728\n"
"v_mfma_f32_16x16x16_bf16 v[48:51], v[38:39], v[60:61], v[48:51]\n"
"ds_read_b64 v[66:67], v28 offset:1736\n"
"s_waitcnt lgkmcnt(7)\n"
"v_mfma_f32_16x16x32_f16 v[56:59], v[72:75], v[84:87], v[56:59]\n"
"ds_read_b128 v[76:79], v29 offset:1728\n"
"ds_read_b128 v[60:63], v29 offset:1216\n"
"v_mfma_f32_16x16x32_f16 v[52:55], v[72:75], v[2:5], v[52:55]\n"
"ds_read_b64 v[84:85], v28 offset:1216\n"
"v_mfma_f32_16x16x32_f16 v[56:59], v[88:91], v[2:5], v[56:59]\n"
"ds_read_b64 v[86:87], v28 offset:1224\n"
"v_mfma_f32_16x16x32_f16 v[48:51], v[72:75], v[68:71], v[48:51]\n"
"ds_read_b128 v[2:5], v29 offset:704\n"
"s_waitcnt lgkmcnt(9)\n"
"v_mfma_f32_16x16x32_f16 v[52:55], v[88:91], v[68:71], v[52:55]\n"
"v_mfma_f32_16x16x32_f16 v[56:59], v[80:83], v[68:71], v[56:59]\n"
"ds_read_b64 v[68:69], v30 offset:1024\n"
"v_mfma_f32_16x16x32_f16 v[44:47], v[72:75], v[34:37], v[44:47]\n"
"ds_read_b64 v[72:73], v28 offset:704\n"
"v_mfma_f32_16x16x32_f16 v[48:51], v[88:91], v[34:37], v[48:51]\n"
"ds_read_b64 v[74:75], v28 offset:712\n"
"s_waitcnt lgkmcnt(6)\n"
"v_mfma_f32_16x16x32_f16 v[52:55], v[80:83], v[34:37], v[52:55]\n"
"ds_read_b128 v[88:91], v29 offset:192\n"
"ds_read_b64 v[80:81], v28 offset:192\n"
"v_mfma_f32_16x16x32_f16 v[56:59], v[92:95], v[34:37], v[56:59]\n"
"ds_read_b64 v[82:83], v28 offset:200\n"
"ds_read_b64 v[34:35], v28 offset:1664\n"
"v_mfma_f32_16x16x32_f16 v[56:59], v[64:67], v[76:79], v[56:59]\n"
"ds_read_b64 v[36:37], v28 offset:1672\n"
"ds_read_b128 v[92:95], v29 offset:1664\n"
"v_mfma_f32_16x16x32_f16 v[52:55], v[64:67], v[60:63], v[52:55]\n"
"ds_read_b128 v[76:79], v29 offset:1152\n"
"s_waitcnt lgkmcnt(9)\n"
"v_mfma_f32_16x16x32_f16 v[56:59], v[84:87], v[60:63], v[56:59]\n"
"ds_read_b64 v[60:61], v28 offset:1152\n"
"v_mfma_f32_16x16x32_f16 v[48:51], v[64:67], v[2:5], v[48:51]\n"
"ds_read_b64 v[62:63], v28 offset:1160\n"
"v_mfma_f32_16x16x16_bf16 v[52:55], v[38:39], v[68:69], v[52:55]\n"
"ds_read_b128 v[68:71], v29 offset:640\n"
"s_nop 4\n"
"v_mfma_f32_16x16x32_f16 v[52:55], v[84:87], v[2:5], v[52:55]\n"
"s_waitcnt lgkmcnt(9)\n"
"v_mfma_f32_16x16x32_f16 v[56:59], v[72:75], v[2:5], v[56:59]\n"
"ds_read_b64 v[2:3], v28 offset:640\n"
"v_mfma_f32_16x16x32_f16 v[44:47], v[64:67], v[88:91], v[44:47]\n"
"ds_read_b64 v[4:5], v28 offset:648\n"
"v_mfma_f32_16x16x32_f16 v[48:51], v[84:87], v[88:91], v[48:51]\n"
"ds_read_b128 v[64:67], v29 offset:128\n"
"v_mfma_f32_16x16x32_f16 v[52:55], v[72:75], v[88:91], v[52:55]\n"
"ds_read_b64 v[84:85], v30 offset:1536\n"
"s_waitcnt lgkmcnt(5)\n"
"v_mfma_f32_16x16x32_f16 v[56:59], v[80:83], v[88:91], v[56:59]\n"
"ds_read_b64 v[72:73], v28 offset:128\n"
"ds_read_b64 v[74:75], v28 offset:136\n"
"v_mfma_f32_16x16x32_f16 v[56:59], v[34:37], v[92:95], v[56:59]\n"
"ds_read_b64 v[88:89], v28 offset:1600\n"
"ds_read_b64 v[90:91], v28 offset:1608\n"
"v_mfma_f32_16x16x32_f16 v[52:55], v[34:37], v[76:79], v[52:55]\n"
"ds_read_b128 v[80:83], v29 offset:1600\n"
"v_mfma_f32_16x16x32_f16 v[56:59], v[60:63], v[76:79], v[56:59]\n"
"ds_read_b128 v[92:95], v29 offset:1088\n"
"s_waitcnt lgkmcnt(7)\n"
"v_mfma_f32_16x16x32_f16 v[48:51], v[34:37], v[68:71], v[48:51]\n"
"ds_read_b64 v[76:77], v28 offset:1088\n"
"v_mfma_f32_16x16x32_f16 v[52:55], v[60:63], v[68:71], v[52:55]\n"
"ds_read_b64 v[78:79], v28 offset:1096\n"
"v_mfma_f32_16x16x32_f16 v[56:59], v[2:5], v[68:71], v[56:59]\n"
"ds_read_b128 v[68:71], v29 offset:576\n"
"v_mfma_f32_16x16x32_f16 v[44:47], v[34:37], v[64:67], v[44:47]\n"
"ds_read_b64 v[34:35], v28 offset:576\n"
"s_waitcnt lgkmcnt(8)\n"
"s_nop 0\n"
"v_mfma_f32_16x16x16_bf16 v[56:59], v[38:39], v[84:85], v[56:59]\n"
"ds_read_b64 v[36:37], v28 offset:584\n"
"v_mfma_f32_16x16x32_f16 v[48:51], v[60:63], v[64:67], v[48:51]\n"
"ds_read_b128 v[84:87], v29 offset:64\n"
"ds_read_b64 v[60:61], v28 offset:64\n"
"v_mfma_f32_16x16x32_f16 v[52:55], v[2:5], v[64:67], v[52:55]\n"
"ds_read_b64 v[62:63], v28 offset:72\n"
"ds_read_b64 v[2:3], v28 offset:1536\n"
"v_mfma_f32_16x16x32_f16 v[56:59], v[72:75], v[64:67], v[56:59]\n"
"ds_read_b64 v[4:5], v28 offset:1544\n"
"s_waitcnt lgkmcnt(7)\n"
"v_mfma_f32_16x16x32_f16 v[56:59], v[88:91], v[80:83], v[56:59]\n"
"ds_read_b128 v[64:67], v29 offset:0\n"
"ds_read_b128 v[72:75], v29 offset:512\n"
"v_mfma_f32_16x16x32_f16 v[52:55], v[88:91], v[92:95], v[52:55]\n"
"ds_read_b64 v[80:81], v28 offset:1024\n"
"v_mfma_f32_16x16x32_f16 v[56:59], v[76:79], v[92:95], v[56:59]\n"
"ds_read_b64 v[82:83], v28 offset:1032\n"
"v_mfma_f32_16x16x32_f16 v[48:51], v[88:91], v[68:71], v[48:51]\n"
"ds_read_b128 v[92:95], v29 offset:1024\n"
"s_waitcnt lgkmcnt(9)\n"
"v_mfma_f32_16x16x32_f16 v[52:55], v[76:79], v[68:71], v[52:55]\n"
"v_mfma_f32_16x16x32_f16 v[56:59], v[34:37], v[68:71], v[56:59]\n"
"ds_read_b64 v[68:69], v28 offset:512\n"
"v_mfma_f32_16x16x32_f16 v[44:47], v[88:91], v[84:87], v[44:47]\n"
"ds_read_b64 v[70:71], v28 offset:520\n"
"v_mfma_f32_16x16x32_f16 v[48:51], v[76:79], v[84:87], v[48:51]\n"
"ds_read_b128 v[88:91], v29 offset:1536\n"
"s_waitcnt lgkmcnt(6)\n"
"v_mfma_f32_16x16x32_f16 v[52:55], v[34:37], v[84:87], v[52:55]\n"
"ds_read_b64 v[76:77], v28 offset:0\n"
"v_mfma_f32_16x16x32_f16 v[56:59], v[60:63], v[84:87], v[56:59]\n"
"ds_read_b64 v[78:79], v28 offset:8\n"
"v_mfma_f32_16x16x32_f16 v[44:47], v[2:5], v[64:67], v[44:47]\n"
"v_mfma_f32_16x16x32_f16 v[48:51], v[2:5], v[72:75], v[48:51]\n"
"s_waitcnt lgkmcnt(3)\n"
"v_mfma_f32_16x16x32_f16 v[48:51], v[80:83], v[64:67], v[48:51]\n"
"v_mfma_f32_16x16x32_f16 v[52:55], v[2:5], v[92:95], v[52:55]\n"
"v_mul_f32 v44, v40, v44\n"
"v_mul_f32 v45, v40, v45\n"
"v_mul_f32 v46, v40, v46\n"
"v_mfma_f32_16x16x32_f16 v[52:55], v[80:83], v[72:75], v[52:55]\n"
"v_mul_f32 v47, v40, v47\n"
"v_cvt_pk_bf16_f32 v44, v44, v45\n"
"v_cvt_pk_bf16_f32 v45, v46, v47\n"
"v_mfma_f32_16x16x32_f16 v[52:55], v[68:71], v[64:67], v[52:55]\n"
"v_pk_min_u16 v44, v44, s50\n"
"v_pk_min_u16 v45, v45, s50\n"
"s_waitcnt lgkmcnt(0)\n"
"v_mfma_f32_16x16x32_f16 v[56:59], v[2:5], v[88:91], v[56:59]\n"
"ds_write_b64 v30, v[44:45] offset:0\n"
"v_mfma_f32_16x16x32_f16 v[56:59], v[80:83], v[92:95], v[56:59]\n"
"v_mul_f32 v48, v40, v48\n"
"v_mul_f32 v49, v40, v49\n"
"v_mfma_f32_16x16x32_f16 v[56:59], v[68:71], v[72:75], v[56:59]\n"
"v_mul_f32 v50, v40, v50\n"
"v_mul_f32 v51, v40, v51\n"
"v_mfma_f32_16x16x32_f16 v[56:59], v[76:79], v[64:67], v[56:59]\n"
"v_cvt_pk_bf16_f32 v48, v48, v49\n"
"v_cvt_pk_bf16_f32 v49, v50, v51\n"
"v_pk_min_u16 v48, v48, s50\n"
"v_pk_min_u16 v49, v49, s50\n"
"ds_write_b64 v30, v[48:49] offset:512\n"
"v_mul_f32 v52, v40, v52\n"
"v_mul_f32 v53, v40, v53\n"
"v_mul_f32 v54, v40, v54\n"
"v_mul_f32 v55, v40, v55\n"
"v_cvt_pk_bf16_f32 v52, v52, v53\n"
"v_cvt_pk_bf16_f32 v53, v54, v55\n"
"v_pk_min_u16 v52, v52, s50\n"
"v_pk_min_u16 v53, v53, s50\n"
"ds_write_b64 v30, v[52:53] offset:1024\n"
"v_mul_f32 v56, v40, v56\n"
"v_mul_f32 v57, v40, v57\n"
"v_mul_f32 v58, v40, v58\n"
"v_mul_f32 v59, v40, v59\n"
"v_cvt_pk_bf16_f32 v56, v56, v57\n"
"v_cvt_pk_bf16_f32 v57, v58, v59\n"
"v_pk_min_u16 v56, v56, s50\n"
"v_pk_min_u16 v57, v57, s50\n"
"ds_write_b64 v30, v[56:57] offset:1536\n"
"ds_write_b32 v13, v13 offset:544\n"
"s_mov_b32 s17, 0x100000\n"
".Ltp_285:\n"
"ds_read_b32 v42, v14 offset:544\n"
"s_sub_u32 s17, s17, 1\n"
"s_cbranch_scc1 .Lou_286\n"
"s_waitcnt lgkmcnt(0)\n"
"v_cmp_eq_u32 vcc, 0, v42\n"
"s_cbranch_vccnz .Ltp_285\n"
".Lou_286:\n"
"s_waitcnt lgkmcnt(0)\n"
"ds_read_b64_tr_b16 v[60:61], v31 offset:0\n"
"ds_read_b64_tr_b16 v[62:63], v32 offset:0\n"
"ds_read_b64_tr_b16 v[64:65], v31 offset:1024\n"
"ds_read_b64_tr_b16 v[66:67], v32 offset:1024\n"
"s_add_u32 s47, s46, 0x1400\n"
"s_add_u32 s48, s46, 0xa0000\n"
"s_add_u32 s49, s46, 0xa1400\n"
"s_waitcnt lgkmcnt(3)\n"
"buffer_store_dwordx2 v[60:61], v41, s[12:15], s46 offen\n"
"s_mov_b32 s89, 17\n"
".Ldl_287:\n"
"s_sub_u32 s89, s89, 1\n"
"s_cbranch_scc1 .Lde_288\n"
"s_sleep 1\n"
"s_branch .Ldl_287\n"
".Lde_288:\n"
"s_waitcnt lgkmcnt(2)\n"
"buffer_store_dwordx2 v[62:63], v41, s[12:15], s47 offen\n"
"s_mov_b32 s89, 17\n"
".Ldl_289:\n"
"s_sub_u32 s89, s89, 1\n"
"s_cbranch_scc1 .Lde_290\n"
"s_sleep 1\n"
"s_branch .Ldl_289\n"
".Lde_290:\n"
"s_waitcnt lgkmcnt(1)\n"
"buffer_store_dwordx2 v[64:65], v41, s[12:15], s48 offen\n"
"s_mov_b32 s89, 17\n"
".Ldl_291:\n"
"s_sub_u32 s89, s89, 1\n"
"s_cbranch_scc1 .Lde_292\n"
"s_sleep 1\n"
"s_branch .Ldl_291\n"
".Lde_292:\n"
"s_waitcnt lgkmcnt(0)\n"
"buffer_store_dwordx2 v[66:67], v41, s[12:15], s49 offen\n"
"v_add_u32 v31, 0x16c00, v31\n"
"v_add_u32 v32, 0x16c00, v32\n"
"s_add_u32 s46, s46, 0x140000\n"
"s_add_u32 s47, s47, 0x140000\n"
"s_add_u32 s48, s48, 0x140000\n"
"s_add_u32 s49, s49, 0x140000\n"
"s_lshr_b32 s79, s3, 1\n"
"s_mul_i32 s79, s79, -16\n"
"v_add_u32 v76, s79, v14\n"
"s_mov_b32 s17, 0x100000\n"
".Ltp_293:\n"
"s_sleep 1\n"
"ds_read_b32 v42, v14 offset:592\n"
"s_sub_u32 s17, s17, 1\n"
"s_cbranch_scc1 .Lou_294\n"
"s_waitcnt lgkmcnt(0)\n"
"v_cmp_eq_u32 vcc, 0, v42\n"
"s_cbranch_vccnz .Ltp_293\n"
".Lou_294:\n"
"s_waitcnt lgkmcnt(0)\n"
"ds_read_b64_tr_b16 v[68:69], v31 offset:0\n"
"ds_read_b64_tr_b16 v[70:71], v32 offset:0\n"
"s_waitcnt lgkmcnt(1)\n"
"buffer_store_dwordx2 v[68:69], v41, s[12:15], s46 offen\n"
"s_waitcnt lgkmcnt(0)\n"
"buffer_store_dwordx2 v[70:71], v41, s[12:15], s47 offen\n"
"s_mov_b32 s17, 0x100000\n"
".Ltp_295:\n"
"s_sleep 1\n"
"ds_read_b32 v42, v76 offset:576\n"
"s_sub_u32 s17, s17, 1\n"
"s_cbranch_scc1 .Lou_296\n"
"s_waitcnt lgkmcnt(0)\n"
"v_cmp_eq_u32 vcc, 0, v42\n"
"s_cbranch_vccnz .Ltp_295\n"
".Lou_296:\n"
"s_waitcnt lgkmcnt(0)\n"
"ds_read_b64_tr_b16 v[72:73], v31 offset:1024\n"
"ds_read_b64_tr_b16 v[74:75], v32 offset:1024\n"
"s_waitcnt lgkmcnt(1)\n"
"buffer_store_dwordx2 v[72:73], v41, s[12:15], s48 offen\n"
"s_waitcnt lgkmcnt(0)\n"
"buffer_store_dwordx2 v[74:75], v41, s[12:15], s49 offen\n"
"s_setprio 1\n"
"s_nop 0\n"
".Lnc_270:\n"
"s_add_u32 s31, s2, s3\n"
"s_cmp_lg_u32 s31, 0\n"
"s_cbranch_scc1 .Lfin_297\n"
"v_mov_b32 v32, s16\n"
"s_mov_b64 exec, 1\n"
"buffer_store_dword v32, off, s[4:7], 0 sc1\n"
"s_mov_b64 exec, -1\n"
".Lfin_297:\n"
"s_endpgm\n"
"s_branch .L_xend\n"
".Lsl_7:\n"
"s_nop 7\n"
"s_mov_b32 s17, 0x100000\n"
".Ltp_9:\n"
"ds_read_b32 v90, v14 offset:0\n"
"s_sub_u32 s17, s17, 1\n"
"s_cbranch_scc1 .Lou_10\n"
"s_waitcnt lgkmcnt(0)\n"
"v_cmp_eq_u32 vcc, 0, v90\n"
"s_cbranch_vccnz .Ltp_9\n"
".Lou_10:\n"
"s_waitcnt lgkmcnt(0)\n"
"s_branch .Lbk_8\n"
".Lsl_18:\n"
"s_nop 7\n"
"s_mov_b32 s17, 0x100000\n"
".Ltp_20:\n"
"ds_read_b32 v90, v14 offset:48\n"
"s_sub_u32 s17, s17, 1\n"
"s_cbranch_scc1 .Lou_21\n"
"s_waitcnt lgkmcnt(0)\n"
"v_cmp_eq_u32 vcc, 0, v90\n"
"s_cbranch_vccnz .Ltp_20\n"
".Lou_21:\n"
"s_waitcnt lgkmcnt(0)\n"
"s_branch .Lbk_19\n"
".Lsl_22:\n"
"s_nop 7\n"
"s_mov_b32 s17, 0x100000\n"
".Ltp_24:\n"
"ds_read_b32 v90, v14 offset:64\n"
"s_sub_u32 s17, s17, 1\n"
"s_cbranch_scc1 .Lou_25\n"
"s_waitcnt lgkmcnt(0)\n"
"v_cmp_eq_u32 vcc, 0, v90\n"
"s_cbranch_vccnz .Ltp_24\n"
".Lou_25:\n"
"s_waitcnt lgkmcnt(0)\n"
"s_branch .Lbk_23\n"
".Lsl_26:\n"
"s_nop 7\n"
"s_mov_b32 s17, 0x100000\n"
".Ltp_28:\n"
"ds_read_b32 v90, v14 offset:16\n"
"s_sub_u32 s17, s17, 1\n"
"s_cbranch_scc1 .Lou_29\n"
"s_waitcnt lgkmcnt(0)\n"
"v_cmp_eq_u32 vcc, 0, v90\n"
"s_cbranch_vccnz .Ltp_28\n"
".Lou_29:\n"
"s_waitcnt lgkmcnt(0)\n"
"s_branch .Lbk_27\n"
".Lsl_30:\n"
"s_nop 7\n"
"s_mov_b32 s17, 0x100000\n"
".Ltp_32:\n"
"ds_read_b32 v90, v14 offset:80\n"
"s_sub_u32 s17, s17, 1\n"
"s_cbranch_scc1 .Lou_33\n"
"s_waitcnt lgkmcnt(0)\n"
"v_cmp_eq_u32 vcc, 0, v90\n"
"s_cbranch_vccnz .Ltp_32\n"
".Lou_33:\n"
"s_waitcnt lgkmcnt(0)\n"
"s_branch .Lbk_31\n"
".Lsl_38:\n"
"s_nop 7\n"
"s_mov_b32 s17, 0x100000\n"
".Ltp_40:\n"
"ds_read_b128 v[84:87], v11 offset:0\n"
"ds_read_b32 v91, v12 offset:0\n"
"s_sub_u32 s17, s17, 1\n"
"s_cbranch_scc1 .Lou_41\n"
"s_waitcnt lgkmcnt(0)\n"
"v_max3_u32 v88, v84, v85, v91\n"
"v_max3_u32 v88, v86, v87, v88\n"
"v_cmp_eq_u32 vcc, -1, v88\n"
"s_cbranch_vccnz .Ltp_40\n"
".Lou_41:\n"
"s_waitcnt lgkmcnt(0)\n"
"v_add_f32 v84, v84, v85\n"
"v_add_f32 v86, v86, v87\n"
"v_add_f32 v84, v84, v86\n"
"v_mul_f32 v89, 0x3acccccd, v91\n"
"v_fma_f32 v84, -v91, v89, v84\n"
"v_fmamk_f32 v84, v84, 0x3acccccd, v27\n"
"v_max_f32 v84, v84, v27\n"
"v_rsq_f32 v88, v84\n"
"s_nop 0\n"
"v_mul_f32 v89, -v89, v88\n"
"s_nop 1\n"
"s_branch .Lbk_39\n"
".Lsl_42:\n"
"s_nop 7\n"
"s_mov_b32 s17, 0x100000\n"
".Ltp_44:\n"
"ds_read_b128 v[84:87], v11 offset:320\n"
"ds_read_b32 v91, v12 offset:320\n"
"s_sub_u32 s17, s17, 1\n"
"s_cbranch_scc1 .Lou_45\n"
"s_waitcnt lgkmcnt(0)\n"
"v_max3_u32 v88, v84, v85, v91\n"
"v_max3_u32 v88, v86, v87, v88\n"
"v_cmp_eq_u32 vcc, -1, v88\n"
"s_cbranch_vccnz .Ltp_44\n"
".Lou_45:\n"
"s_waitcnt lgkmcnt(0)\n"
"v_add_f32 v84, v84, v85\n"
"v_add_f32 v86, v86, v87\n"
"v_add_f32 v84, v84, v86\n"
"v_mul_f32 v89, 0x3acccccd, v91\n"
"v_fma_f32 v84, -v91, v89, v84\n"
"v_fmamk_f32 v84, v84, 0x3acccccd, v27\n"
"v_max_f32 v84, v84, v27\n"
"v_rsq_f32 v88, v84\n"
"s_nop 0\n"
"v_mul_f32 v89, -v89, v88\n"
"s_nop 1\n"
"s_branch .Lbk_43\n"
".Lsl_46:\n"
"s_nop 7\n"
"s_mov_b32 s17, 0x100000\n"
".Ltp_48:\n"
"ds_read_b128 v[84:87], v11 offset:640\n"
"ds_read_b32 v91, v12 offset:640\n"
"s_sub_u32 s17, s17, 1\n"
"s_cbranch_scc1 .Lou_49\n"
"s_waitcnt lgkmcnt(0)\n"
"v_max3_u32 v88, v84, v85, v91\n"
"v_max3_u32 v88, v86, v87, v88\n"
"v_cmp_eq_u32 vcc, -1, v88\n"
"s_cbranch_vccnz .Ltp_48\n"
".Lou_49:\n"
"s_waitcnt lgkmcnt(0)\n"
"v_add_f32 v84, v84, v85\n"
"v_add_f32 v86, v86, v87\n"
"v_add_f32 v84, v84, v86\n"
"v_mul_f32 v89, 0x3acccccd, v91\n"
"v_fma_f32 v84, -v91, v89, v84\n"
"v_fmamk_f32 v84, v84, 0x3acccccd, v27\n"
"v_max_f32 v84, v84, v27\n"
"v_rsq_f32 v88, v84\n"
"s_nop 0\n"
"v_mul_f32 v89, -v89, v88\n"
"s_nop 1\n"
"s_branch .Lbk_47\n"
".Lsl_50:\n"
"s_nop 7\n"
"s_mov_b32 s17, 0x100000\n"
".Ltp_52:\n"
"ds_read_b128 v[84:87], v11 offset:960\n"
"ds_read_b32 v91, v12 offset:960\n"
"s_sub_u32 s17, s17, 1\n"
"s_cbranch_scc1 .Lou_53\n"
"s_waitcnt lgkmcnt(0)\n"
"v_max3_u32 v88, v84, v85, v91\n"
"v_max3_u32 v88, v86, v87, v88\n"
"v_cmp_eq_u32 vcc, -1, v88\n"
"s_cbranch_vccnz .Ltp_52\n"
".Lou_53:\n"
"s_waitcnt lgkmcnt(0)\n"
"v_add_f32 v84, v84, v85\n"
"v_add_f32 v86, v86, v87\n"
"v_add_f32 v84, v84, v86\n"
"v_mul_f32 v89, 0x3acccccd, v91\n"
"v_fma_f32 v84, -v91, v89, v84\n"
"v_fmamk_f32 v84, v84, 0x3acccccd, v27\n"
"v_max_f32 v84, v84, v27\n"
"v_rsq_f32 v88, v84\n"
"s_nop 0\n"
"v_mul_f32 v89, -v89, v88\n"
"s_nop 1\n"
"s_branch .Lbk_51\n"
".Lsl_54:\n"
"s_nop 7\n"
"s_mov_b32 s17, 0x100000\n"
".Ltp_56:\n"
"ds_read_b128 v[44:47], v11 offset:1280\n"
"ds_read_b128 v[48:51], v11 offset:1600\n"
"s_sub_u32 s17, s17, 1\n"
"s_cbranch_scc1 .Lou_57\n"
"s_waitcnt lgkmcnt(0)\n"
"v_max3_u32 v52, v44, v45, v51\n"
"v_max3_u32 v52, v46, v47, v52\n"
"v_max3_u32 v52, v48, v49, v52\n"
"v_max3_u32 v52, v50, v51, v52\n"
"v_cmp_eq_u32 vcc, -1, v52\n"
"s_cbranch_vccnz .Ltp_56\n"
".Lou_57:\n"
"s_waitcnt lgkmcnt(0)\n"
"v_add_f32 v44, v44, v45\n"
"v_add_f32 v46, v46, v47\n"
"v_add_f32 v48, v48, v49\n"
"v_add_f32 v50, v50, v51\n"
"v_add_f32 v48, v48, v50\n"
"v_add_f32 v44, v44, v46\n"
"v_mul_f32 v53, 0x3acccccd, v48\n"
"v_fma_f32 v44, -v48, v53, v44\n"
"v_fmamk_f32 v44, v44, 0x3acccccd, v27\n"
"v_max_f32 v44, v44, v27\n"
"v_rsq_f32 v52, v44\n"
"s_nop 0\n"
"v_mul_f32 v53, -v53, v52\n"
"s_nop 1\n"
"s_branch .Lbk_55\n"
".Lsl_118:\n"
"s_nop 7\n"
"s_mov_b32 s17, 0x100000\n"
".Ltp_120:\n"
"ds_read_b32 v90, v14 offset:288\n"
"s_sub_u32 s17, s17, 1\n"
"s_cbranch_scc1 .Lou_121\n"
"s_waitcnt lgkmcnt(0)\n"
"v_cmp_eq_u32 vcc, 0, v90\n"
"s_cbranch_vccnz .Ltp_120\n"
".Lou_121:\n"
"s_waitcnt lgkmcnt(0)\n"
"s_branch .Lbk_119\n"
".Lsl_122:\n"
"s_nop 7\n"
"s_mov_b32 s17, 0x100000\n"
".Ltp_124:\n"
"ds_read_b128 v[84:87], v11 offset:1856\n"
"ds_read_b32 v91, v12 offset:1856\n"
"s_sub_u32 s17, s17, 1\n"
"s_cbranch_scc1 .Lou_125\n"
"s_waitcnt lgkmcnt(0)\n"
"v_max3_u32 v88, v84, v85, v91\n"
"v_max3_u32 v88, v86, v87, v88\n"
"v_cmp_eq_u32 vcc, -1, v88\n"
"s_cbranch_vccnz .Ltp_124\n"
".Lou_125:\n"
"s_waitcnt lgkmcnt(0)\n"
"v_add_f32 v84, v84, v85\n"
"v_add_f32 v86, v86, v87\n"
"v_add_f32 v84, v84, v86\n"
"v_mul_f32 v89, 0x3acccccd, v91\n"
"v_fma_f32 v84, -v91, v89, v84\n"
"v_fmamk_f32 v84, v84, 0x3acccccd, v27\n"
"v_add_f32 v92, v92, v84\n"
"v_max_f32 v84, v84, v27\n"
"v_rsq_f32 v88, v84\n"
"s_nop 0\n"
"v_mul_f32 v89, -v89, v88\n"
"s_nop 1\n"
"s_branch .Lbk_123\n"
".Lsl_126:\n"
"s_nop 7\n"
"s_mov_b32 s17, 0x100000\n"
".Ltp_128:\n"
"ds_read_b128 v[84:87], v11 offset:2176\n"
"ds_read_b32 v91, v12 offset:2176\n"
"s_sub_u32 s17, s17, 1\n"
"s_cbranch_scc1 .Lou_129\n"
"s_waitcnt lgkmcnt(0)\n"
"v_max3_u32 v88, v84, v85, v91\n"
"v_max3_u32 v88, v86, v87, v88\n"
"v_cmp_eq_u32 vcc, -1, v88\n"
"s_cbranch_vccnz .Ltp_128\n"
".Lou_129:\n"
"s_waitcnt lgkmcnt(0)\n"
"v_add_f32 v84, v84, v85\n"
"v_add_f32 v86, v86, v87\n"
"v_add_f32 v84, v84, v86\n"
"v_mul_f32 v89, 0x3acccccd, v91\n"
"v_fma_f32 v84, -v91, v89, v84\n"
"v_fmamk_f32 v84, v84, 0x3acccccd, v27\n"
"v_add_f32 v92, v92, v84\n"
"v_max_f32 v84, v84, v27\n"
"v_rsq_f32 v88, v84\n"
"s_nop 0\n"
"v_mul_f32 v89, -v89, v88\n"
"s_nop 1\n"
"s_branch .Lbk_127\n"
".Lsl_130:\n"
"s_nop 7\n"
"s_mov_b32 s17, 0x100000\n"
".Ltp_132:\n"
"ds_read_b128 v[84:87], v11 offset:2496\n"
"ds_read_b32 v91, v12 offset:2496\n"
"s_sub_u32 s17, s17, 1\n"
"s_cbranch_scc1 .Lou_133\n"
"s_waitcnt lgkmcnt(0)\n"
"v_max3_u32 v88, v84, v85, v91\n"
"v_max3_u32 v88, v86, v87, v88\n"
"v_cmp_eq_u32 vcc, -1, v88\n"
"s_cbranch_vccnz .Ltp_132\n"
".Lou_133:\n"
"s_waitcnt lgkmcnt(0)\n"
"v_add_f32 v84, v84, v85\n"
"v_add_f32 v86, v86, v87\n"
"v_add_f32 v84, v84, v86\n"
"v_mul_f32 v89, 0x3acccccd, v91\n"
"v_fma_f32 v84, -v91, v89, v84\n"
"v_fmamk_f32 v84, v84, 0x3acccccd, v27\n"
"v_add_f32 v92, v92, v84\n"
"v_max_f32 v84, v84, v27\n"
"v_rsq_f32 v88, v84\n"
"s_nop 0\n"
"v_mul_f32 v89, -v89, v88\n"
"s_nop 1\n"
"s_branch .Lbk_131\n"
".Lsl_134:\n"
"s_nop 7\n"
"s_mov_b32 s17, 0x100000\n"
".Ltp_136:\n"
"ds_read_b128 v[84:87], v11 offset:2816\n"
"ds_read_b32 v91, v12 offset:2816\n"
"s_sub_u32 s17, s17, 1\n"
"s_cbranch_scc1 .Lou_137\n"
"s_waitcnt lgkmcnt(0)\n"
"v_max3_u32 v88, v84, v85, v91\n"
"v_max3_u32 v88, v86, v87, v88\n"
"v_cmp_eq_u32 vcc, -1, v88\n"
"s_cbranch_vccnz .Ltp_136\n"
".Lou_137:\n"
"s_waitcnt lgkmcnt(0)\n"
"v_add_f32 v84, v84, v85\n"
"v_add_f32 v86, v86, v87\n"
"v_add_f32 v84, v84, v86\n"
"v_mul_f32 v89, 0x3acccccd, v91\n"
"v_fma_f32 v84, -v91, v89, v84\n"
"v_fmamk_f32 v84, v84, 0x3acccccd, v27\n"
"v_add_f32 v92, v92, v84\n"
"v_max_f32 v84, v84, v27\n"
"v_rsq_f32 v88, v84\n"
"s_nop 0\n"
"v_mul_f32 v89, -v89, v88\n"
"s_nop 1\n"
"s_branch .Lbk_135\n"
".Lsl_138:\n"
"s_nop 7\n"
"s_mov_b32 s17, 0x100000\n"
".Ltp_140:\n"
"ds_read_b128 v[44:47], v11 offset:3136\n"
"ds_read_b128 v[48:51], v11 offset:3456\n"
"s_sub_u32 s17, s17, 1\n"
"s_cbranch_scc1 .Lou_141\n"
"s_waitcnt lgkmcnt(0)\n"
"v_max3_u32 v52, v44, v45, v51\n"
"v_max3_u32 v52, v46, v47, v52\n"
"v_max3_u32 v52, v48, v49, v52\n"
"v_max3_u32 v52, v50, v51, v52\n"
"v_cmp_eq_u32 vcc, -1, v52\n"
"s_cbranch_vccnz .Ltp_140\n"
".Lou_141:\n"
"s_waitcnt lgkmcnt(0)\n"
"v_add_f32 v44, v44, v45\n"
"v_add_f32 v46, v46, v47\n"
"v_add_f32 v48, v48, v49\n"
"v_add_f32 v50, v50, v51\n"
"v_add_f32 v48, v48, v50\n"
"v_add_f32 v44, v44, v46\n"
"v_mul_f32 v53, 0x3acccccd, v48\n"
"v_fma_f32 v44, -v48, v53, v44\n"
"v_fmamk_f32 v44, v44, 0x3acccccd, v27\n"
"v_add_f32 v92, v92, v44\n"
"v_max_f32 v44, v44, v27\n"
"v_rsq_f32 v52, v44\n"
"s_nop 0\n"
"v_mul_f32 v53, -v53, v52\n"
"s_nop 1\n"
"s_branch .Lbk_139\n"
".Lrd_142:\n"
"s_nop 7\n"
"s_cmp_lg_u32 s54, 0\n"
"s_cbranch_scc1 .Lgo_108\n"
"s_mov_b32 s54, 1\n"
"s_sub_u32 s79, s44, 0x5000\n"
"s_add_u32 s78, s29, 0xb48000\n"
"s_max_i32 s46, s79, s78\n"
"s_add_u32 s47, s46, 0x1000\n"
"buffer_load_dwordx4 v[28:31], v1, s[4:7], s46 offen offset:0 sc1\n"
"buffer_load_dwordx4 v[32:35], v1, s[4:7], s46 offen offset:1024 sc1\n"
"buffer_load_dwordx4 v[36:39], v1, s[4:7], s46 offen offset:2048 sc1\n"
"buffer_load_dwordx4 v[40:43], v1, s[4:7], s46 offen offset:3072 sc1\n"
"buffer_load_dwordx4 v[44:47], v1, s[4:7], s47 offen offset:0 sc1\n"
"s_mov_b32 s17, 32768\n"
".Ltp_143:\n"
"s_waitcnt vmcnt(0)\n"
"s_sub_u32 s17, s17, 1\n"
"s_cbranch_scc1 .Lou_144\n"
"v_max_u32 v48, v28, v29\n"
"v_max3_u32 v48, v30, v31, v48\n"
"v_max3_u32 v48, v32, v33, v48\n"
"v_max3_u32 v48, v34, v35, v48\n"
"v_max3_u32 v48, v36, v37, v48\n"
"v_max3_u32 v48, v38, v39, v48\n"
"v_max3_u32 v48, v40, v41, v48\n"
"v_max3_u32 v48, v42, v43, v48\n"
"v_max3_u32 v48, v44, v45, v48\n"
"v_max3_u32 v48, v46, v47, v48\n"
"v_cmp_eq_u32 vcc, -1, v48\n"
"s_cbranch_vccz .Lou_144\n"
"v_max_u32 v48, v30, v31\n"
"v_max3_u32 v48, v28, v29, v48\n"
"v_cmp_eq_u32 vcc, -1, v48\n"
"s_cbranch_vccz .Lsk_145\n"
"s_mov_b64 exec, vcc\n"
"buffer_load_dwordx4 v[28:31], v1, s[4:7], s46 offen offset:0 sc1\n"
"s_mov_b64 exec, -1\n"
".Lsk_145:\n"
"v_max_u32 v48, v34, v35\n"
"v_max3_u32 v48, v32, v33, v48\n"
"v_cmp_eq_u32 vcc, -1, v48\n"
"s_cbranch_vccz .Lsk_146\n"
"s_mov_b64 exec, vcc\n"
"buffer_load_dwordx4 v[32:35], v1, s[4:7], s46 offen offset:1024 sc1\n"
"s_mov_b64 exec, -1\n"
".Lsk_146:\n"
"v_max_u32 v48, v38, v39\n"
"v_max3_u32 v48, v36, v37, v48\n"
"v_cmp_eq_u32 vcc, -1, v48\n"
"s_cbranch_vccz .Lsk_147\n"
"s_mov_b64 exec, vcc\n"
"buffer_load_dwordx4 v[36:39], v1, s[4:7], s46 offen offset:2048 sc1\n"
"s_mov_b64 exec, -1\n"
".Lsk_147:\n"
"v_max_u32 v48, v42, v43\n"
"v_max3_u32 v48, v40, v41, v48\n"
"v_cmp_eq_u32 vcc, -1, v48\n"
"s_cbranch_vccz .Lsk_148\n"
"s_mov_b64 exec, vcc\n"
"buffer_load_dwordx4 v[40:43], v1, s[4:7], s46 offen offset:3072 sc1\n"
"s_mov_b64 exec, -1\n"
".Lsk_148:\n"
"v_max_u32 v48, v46, v47\n"
"v_max3_u32 v48, v44, v45, v48\n"
"v_cmp_eq_u32 vcc, -1, v48\n"
"s_cbranch_vccz .Lsk_149\n"
"s_mov_b64 exec, vcc\n"
"buffer_load_dwordx4 v[44:47], v1, s[4:7], s47 offen offset:0 sc1\n"
"s_mov_b64 exec, -1\n"
".Lsk_149:\n"
"s_branch .Ltp_143\n"
".Lou_144:\n"
"s_waitcnt vmcnt(0)\n"
"s_mov_b32 s46, s44\n"
"s_add_u32 s47, s46, 0x1000\n"
"buffer_load_dwordx4 v[28:31], v1, s[4:7], s46 offen offset:0 sc1\n"
"buffer_load_dwordx4 v[32:35], v1, s[4:7], s46 offen offset:1024 sc1\n"
"buffer_load_dwordx4 v[36:39], v1, s[4:7], s46 offen offset:2048 sc1\n"
"buffer_load_dwordx4 v[40:43], v1, s[4:7], s46 offen offset:3072 sc1\n"
"buffer_load_dwordx4 v[44:47], v1, s[4:7], s47 offen offset:0 sc1\n"
"s_mov_b32 s17, 32768\n"
".Ltp_150:\n"
"s_waitcnt vmcnt(0)\n"
"s_sub_u32 s17, s17, 1\n"
"s_cbranch_scc1 .Lou_151\n"
"v_max_u32 v48, v28, v29\n"
"v_max3_u32 v48, v30, v31, v48\n"
"v_max3_u32 v48, v32, v33, v48\n"
"v_max3_u32 v48, v34, v35, v48\n"
"v_max3_u32 v48, v36, v37, v48\n"
"v_max3_u32 v48, v38, v39, v48\n"
"v_max3_u32 v48, v40, v41, v48\n"
"v_max3_u32 v48, v42, v43, v48\n"
"v_max3_u32 v48, v44, v45, v48\n"
"v_max3_u32 v48, v46, v47, v48\n"
"v_cmp_eq_u32 vcc, -1, v48\n"
"s_cbranch_vccz .Lou_151\n"
"v_max_u32 v48, v30, v31\n"
"v_max3_u32 v48, v28, v29, v48\n"
"v_cmp_eq_u32 vcc, -1, v48\n"
"s_cbranch_vccz .Lsk_152\n"
"s_mov_b64 exec, vcc\n"
"buffer_load_dwordx4 v[28:31], v1, s[4:7], s46 offen offset:0 sc1\n"
"s_mov_b64 exec, -1\n"
".Lsk_152:\n"
"v_max_u32 v48, v34, v35\n"
"v_max3_u32 v48, v32, v33, v48\n"
"v_cmp_eq_u32 vcc, -1, v48\n"
"s_cbranch_vccz .Lsk_153\n"
"s_mov_b64 exec, vcc\n"
"buffer_load_dwordx4 v[32:35], v1, s[4:7], s46 offen offset:1024 sc1\n"
"s_mov_b64 exec, -1\n"
".Lsk_153:\n"
"v_max_u32 v48, v38, v39\n"
"v_max3_u32 v48, v36, v37, v48\n"
"v_cmp_eq_u32 vcc, -1, v48\n"
"s_cbranch_vccz .Lsk_154\n"
"s_mov_b64 exec, vcc\n"
"buffer_load_dwordx4 v[36:39], v1, s[4:7], s46 offen offset:2048 sc1\n"
"s_mov_b64 exec, -1\n"
".Lsk_154:\n"
"v_max_u32 v48, v42, v43\n"
"v_max3_u32 v48, v40, v41, v48\n"
"v_cmp_eq_u32 vcc, -1, v48\n"
"s_cbranch_vccz .Lsk_155\n"
"s_mov_b64 exec, vcc\n"
"buffer_load_dwordx4 v[40:43], v1, s[4:7], s46 offen offset:3072 sc1\n"
"s_mov_b64 exec, -1\n"
".Lsk_155:\n"
"v_max_u32 v48, v46, v47\n"
"v_max3_u32 v48, v44, v45, v48\n"
"v_cmp_eq_u32 vcc, -1, v48\n"
"s_cbranch_vccz .Lsk_156\n"
"s_mov_b64 exec, vcc\n"
"buffer_load_dwordx4 v[44:47], v1, s[4:7], s47 offen offset:0 sc1\n"
"s_mov_b64 exec, -1\n"
".Lsk_156:\n"
"s_branch .Ltp_150\n"
".Lou_151:\n"
"s_waitcnt vmcnt(0)\n"
"s_add_u32 s46, s44, 0x5000\n"
"s_add_u32 s47, s46, 0x1000\n"
"buffer_load_dwordx4 v[28:31], v1, s[4:7], s46 offen offset:0 sc1\n"
"buffer_load_dwordx4 v[32:35], v1, s[4:7], s46 offen offset:1024 sc1\n"
"buffer_load_dwordx4 v[36:39], v1, s[4:7], s46 offen offset:2048 sc1\n"
"buffer_load_dwordx4 v[40:43], v1, s[4:7], s46 offen offset:3072 sc1\n"
"buffer_load_dwordx4 v[44:47], v1, s[4:7], s47 offen offset:0 sc1\n"
"s_mov_b32 s17, 32768\n"
".Ltp_157:\n"
"s_waitcnt vmcnt(0)\n"
"s_sub_u32 s17, s17, 1\n"
"s_cbranch_scc1 .Lou_158\n"
"v_max_u32 v48, v28, v29\n"
"v_max3_u32 v48, v30, v31, v48\n"
"v_max3_u32 v48, v32, v33, v48\n"
"v_max3_u32 v48, v34, v35, v48\n"
"v_max3_u32 v48, v36, v37, v48\n"
"v_max3_u32 v48, v38, v39, v48\n"
"v_max3_u32 v48, v40, v41, v48\n"
"v_max3_u32 v48, v42, v43, v48\n"
"v_max3_u32 v48, v44, v45, v48\n"
"v_max3_u32 v48, v46, v47, v48\n"
"v_cmp_eq_u32 vcc, -1, v48\n"
"s_cbranch_vccz .Lou_158\n"
"v_max_u32 v48, v30, v31\n"
"v_max3_u32 v48, v28, v29, v48\n"
"v_cmp_eq_u32 vcc, -1, v48\n"
"s_cbranch_vccz .Lsk_159\n"
"s_mov_b64 exec, vcc\n"
"buffer_load_dwordx4 v[28:31], v1, s[4:7], s46 offen offset:0 sc1\n"
"s_mov_b64 exec, -1\n"
".Lsk_159:\n"
"v_max_u32 v48, v34, v35\n"
"v_max3_u32 v48, v32, v33, v48\n"
"v_cmp_eq_u32 vcc, -1, v48\n"
"s_cbranch_vccz .Lsk_160\n"
"s_mov_b64 exec, vcc\n"
"buffer_load_dwordx4 v[32:35], v1, s[4:7], s46 offen offset:1024 sc1\n"
"s_mov_b64 exec, -1\n"
".Lsk_160:\n"
"v_max_u32 v48, v38, v39\n"
"v_max3_u32 v48, v36, v37, v48\n"
"v_cmp_eq_u32 vcc, -1, v48\n"
"s_cbranch_vccz .Lsk_161\n"
"s_mov_b64 exec, vcc\n"
"buffer_load_dwordx4 v[36:39], v1, s[4:7], s46 offen offset:2048 sc1\n"
"s_mov_b64 exec, -1\n"
".Lsk_161:\n"
"v_max_u32 v48, v42, v43\n"
"v_max3_u32 v48, v40, v41, v48\n"
"v_cmp_eq_u32 vcc, -1, v48\n"
"s_cbranch_vccz .Lsk_162\n"
"s_mov_b64 exec, vcc\n"
"buffer_load_dwordx4 v[40:43], v1, s[4:7], s46 offen offset:3072 sc1\n"
"s_mov_b64 exec, -1\n"
".Lsk_162:\n"
"v_max_u32 v48, v46, v47\n"
"v_max3_u32 v48, v44, v45, v48\n"
"v_cmp_eq_u32 vcc, -1, v48\n"
"s_cbranch_vccz .Lsk_163\n"
"s_mov_b64 exec, vcc\n"
"buffer_load_dwordx4 v[44:47], v1, s[4:7], s47 offen offset:0 sc1\n"
"s_mov_b64 exec, -1\n"
".Lsk_163:\n"
"s_branch .Ltp_157\n"
".Lou_158:\n"
"s_waitcnt vmcnt(0)\n"
"s_add_u32 s46, s44, 0xa000\n"
"s_add_u32 s47, s46, 0x1000\n"
"buffer_load_dwordx4 v[28:31], v1, s[4:7], s46 offen offset:0 sc1\n"
"buffer_load_dwordx4 v[32:35], v1, s[4:7], s46 offen offset:1024 sc1\n"
"buffer_load_dwordx4 v[36:39], v1, s[4:7], s46 offen offset:2048 sc1\n"
"buffer_load_dwordx4 v[40:43], v1, s[4:7], s46 offen offset:3072 sc1\n"
"buffer_load_dwordx4 v[44:47], v1, s[4:7], s47 offen offset:0 sc1\n"
"s_mov_b32 s17, 32768\n"
".Ltp_164:\n"
"s_waitcnt vmcnt(0)\n"
"s_sub_u32 s17, s17, 1\n"
"s_cbranch_scc1 .Lou_165\n"
"v_max_u32 v48, v28, v29\n"
"v_max3_u32 v48, v30, v31, v48\n"
"v_max3_u32 v48, v32, v33, v48\n"
"v_max3_u32 v48, v34, v35, v48\n"
"v_max3_u32 v48, v36, v37, v48\n"
"v_max3_u32 v48, v38, v39, v48\n"
"v_max3_u32 v48, v40, v41, v48\n"
"v_max3_u32 v48, v42, v43, v48\n"
"v_max3_u32 v48, v44, v45, v48\n"
"v_max3_u32 v48, v46, v47, v48\n"
"v_cmp_eq_u32 vcc, -1, v48\n"
"s_cbranch_vccz .Lou_165\n"
"v_max_u32 v48, v30, v31\n"
"v_max3_u32 v48, v28, v29, v48\n"
"v_cmp_eq_u32 vcc, -1, v48\n"
"s_cbranch_vccz .Lsk_166\n"
"s_mov_b64 exec, vcc\n"
"buffer_load_dwordx4 v[28:31], v1, s[4:7], s46 offen offset:0 sc1\n"
"s_mov_b64 exec, -1\n"
".Lsk_166:\n"
"v_max_u32 v48, v34, v35\n"
"v_max3_u32 v48, v32, v33, v48\n"
"v_cmp_eq_u32 vcc, -1, v48\n"
"s_cbranch_vccz .Lsk_167\n"
"s_mov_b64 exec, vcc\n"
"buffer_load_dwordx4 v[32:35], v1, s[4:7], s46 offen offset:1024 sc1\n"
"s_mov_b64 exec, -1\n"
".Lsk_167:\n"
"v_max_u32 v48, v38, v39\n"
"v_max3_u32 v48, v36, v37, v48\n"
"v_cmp_eq_u32 vcc, -1, v48\n"
"s_cbranch_vccz .Lsk_168\n"
"s_mov_b64 exec, vcc\n"
"buffer_load_dwordx4 v[36:39], v1, s[4:7], s46 offen offset:2048 sc1\n"
"s_mov_b64 exec, -1\n"
".Lsk_168:\n"
"v_max_u32 v48, v42, v43\n"
"v_max3_u32 v48, v40, v41, v48\n"
"v_cmp_eq_u32 vcc, -1, v48\n"
"s_cbranch_vccz .Lsk_169\n"
"s_mov_b64 exec, vcc\n"
"buffer_load_dwordx4 v[40:43], v1, s[4:7], s46 offen offset:3072 sc1\n"
"s_mov_b64 exec, -1\n"
".Lsk_169:\n"
"v_max_u32 v48, v46, v47\n"
"v_max3_u32 v48, v44, v45, v48\n"
"v_cmp_eq_u32 vcc, -1, v48\n"
"s_cbranch_vccz .Lsk_170\n"
"s_mov_b64 exec, vcc\n"
"buffer_load_dwordx4 v[44:47], v1, s[4:7], s47 offen offset:0 sc1\n"
"s_mov_b64 exec, -1\n"
".Lsk_170:\n"
"s_branch .Ltp_164\n"
".Lou_165:\n"
"s_waitcnt vmcnt(0)\n"
"s_add_u32 s46, s44, 0xf000\n"
"s_add_u32 s47, s46, 0x1000\n"
"buffer_load_dwordx4 v[28:31], v1, s[4:7], s46 offen offset:0 sc1\n"
"buffer_load_dwordx4 v[32:35], v1, s[4:7], s46 offen offset:1024 sc1\n"
"buffer_load_dwordx4 v[36:39], v1, s[4:7], s46 offen offset:2048 sc1\n"
"buffer_load_dwordx4 v[40:43], v1, s[4:7], s46 offen offset:3072 sc1\n"
"buffer_load_dwordx4 v[44:47], v1, s[4:7], s47 offen offset:0 sc1\n"
"s_mov_b32 s17, 32768\n"
".Ltp_171:\n"
"s_waitcnt vmcnt(0)\n"
"s_sub_u32 s17, s17, 1\n"
"s_cbranch_scc1 .Lou_172\n"
"v_max_u32 v48, v28, v29\n"
"v_max3_u32 v48, v30, v31, v48\n"
"v_max3_u32 v48, v32, v33, v48\n"
"v_max3_u32 v48, v34, v35, v48\n"
"v_max3_u32 v48, v36, v37, v48\n"
"v_max3_u32 v48, v38, v39, v48\n"
"v_max3_u32 v48, v40, v41, v48\n"
"v_max3_u32 v48, v42, v43, v48\n"
"v_max3_u32 v48, v44, v45, v48\n"
"v_max3_u32 v48, v46, v47, v48\n"
"v_cmp_eq_u32 vcc, -1, v48\n"
"s_cbranch_vccz .Lou_172\n"
"v_max_u32 v48, v30, v31\n"
"v_max3_u32 v48, v28, v29, v48\n"
"v_cmp_eq_u32 vcc, -1, v48\n"
"s_cbranch_vccz .Lsk_173\n"
"s_mov_b64 exec, vcc\n"
"buffer_load_dwordx4 v[28:31], v1, s[4:7], s46 offen offset:0 sc1\n"
"s_mov_b64 exec, -1\n"
".Lsk_173:\n"
"v_max_u32 v48, v34, v35\n"
"v_max3_u32 v48, v32, v33, v48\n"
"v_cmp_eq_u32 vcc, -1, v48\n"
"s_cbranch_vccz .Lsk_174\n"
"s_mov_b64 exec, vcc\n"
"buffer_load_dwordx4 v[32:35], v1, s[4:7], s46 offen offset:1024 sc1\n"
"s_mov_b64 exec, -1\n"
".Lsk_174:\n"
"v_max_u32 v48, v38, v39\n"
"v_max3_u32 v48, v36, v37, v48\n"
"v_cmp_eq_u32 vcc, -1, v48\n"
"s_cbranch_vccz .Lsk_175\n"
"s_mov_b64 exec, vcc\n"
"buffer_load_dwordx4 v[36:39], v1, s[4:7], s46 offen offset:2048 sc1\n"
"s_mov_b64 exec, -1\n"
".Lsk_175:\n"
"v_max_u32 v48, v42, v43\n"
"v_max3_u32 v48, v40, v41, v48\n"
"v_cmp_eq_u32 vcc, -1, v48\n"
"s_cbranch_vccz .Lsk_176\n"
"s_mov_b64 exec, vcc\n"
"buffer_load_dwordx4 v[40:43], v1, s[4:7], s46 offen offset:3072 sc1\n"
"s_mov_b64 exec, -1\n"
".Lsk_176:\n"
"v_max_u32 v48, v46, v47\n"
"v_max3_u32 v48, v44, v45, v48\n"
"v_cmp_eq_u32 vcc, -1, v48\n"
"s_cbranch_vccz .Lsk_177\n"
"s_mov_b64 exec, vcc\n"
"buffer_load_dwordx4 v[44:47], v1, s[4:7], s47 offen offset:0 sc1\n"
"s_mov_b64 exec, -1\n"
".Lsk_177:\n"
"s_branch .Ltp_171\n"
".Lou_172:\n"
"s_waitcnt vmcnt(0)\n"
"s_mov_b32 s17, 0x100000\n"
".Ltp_178:\n"
"ds_read_b32 v90, v14 offset:304\n"
"s_sub_u32 s17, s17, 1\n"
"s_cbranch_scc1 .Lou_179\n"
"s_waitcnt lgkmcnt(0)\n"
"v_cmp_eq_u32 vcc, 0, v90\n"
"s_cbranch_vccnz .Ltp_178\n"
".Lou_179:\n"
"s_waitcnt lgkmcnt(0)\n"
"ds_write_b32 v13, v13 offset:320\n"
"s_mov_b32 s17, 0x100000\n"
".Ltp_180:\n"
"ds_read_b32 v90, v14 offset:320\n"
"s_sub_u32 s17, s17, 1\n"
"s_cbranch_scc1 .Lou_181\n"
"s_waitcnt lgkmcnt(0)\n"
"v_cmp_eq_u32 vcc, 0, v90\n"
"s_cbranch_vccnz .Ltp_180\n"
".Lou_181:\n"
"s_waitcnt lgkmcnt(0)\n"
"v_mov_b32 v90, 0\n"
"ds_write_b32 v13, v90 offset:272\n"
"ds_write_b32 v13, v90 offset:288\n"
"v_mov_b32 v90, -1\n"
"ds_write_b32 v9, v90 offset:1856\n"
"ds_write_b32 v9, v90 offset:2176\n"
"ds_write_b32 v9, v90 offset:2496\n"
"ds_write_b32 v9, v90 offset:2816\n"
"ds_write_b32 v9, v90 offset:3136\n"
"ds_write_b32 v10, v90 offset:3456\n"
"ds_write_b32 v13, v13 offset:336\n"
"s_mov_b32 s17, 0x100000\n"
".Ltp_182:\n"
"ds_read_b32 v90, v14 offset:336\n"
"s_sub_u32 s17, s17, 1\n"
"s_cbranch_scc1 .Lou_183\n"
"s_waitcnt lgkmcnt(0)\n"
"v_cmp_eq_u32 vcc, 0, v90\n"
"s_cbranch_vccnz .Ltp_182\n"
".Lou_183:\n"
"s_waitcnt lgkmcnt(0)\n"
"s_branch .Lag_107\n"
".Lfx_188:\n"
"s_nop 7\n"
"s_add_u32 m0, s29, 0xa000\n"
"s_add_u32 s31, s44, 0x140000\n"
"buffer_load_dwordx4 v1, s[4:7], s31 offen offset:0 sc1 lds\n"
"buffer_load_dwordx4 v1, s[4:7], s31 offen offset:1024 sc1 lds\n"
"buffer_load_dwordx4 v1, s[4:7], s31 offen offset:2048 sc1 lds\n"
"buffer_load_dwordx4 v1, s[4:7], s31 offen offset:3072 sc1 lds\n"
"s_add_u32 m0, m0, 1024\n"
"s_add_u32 s31, s31, 1024\n"
"buffer_load_dwordx4 v1, s[4:7], s31 offen offset:3072 sc1 lds\n"
"s_add_u32 m0, s29, 0xf000\n"
"s_add_u32 s31, s44, 0x145000\n"
"buffer_load_dwordx4 v1, s[4:7], s31 offen offset:0 sc1 lds\n"
"buffer_load_dwordx4 v1, s[4:7], s31 offen offset:1024 sc1 lds\n"
"buffer_load_dwordx4 v1, s[4:7], s31 offen offset:2048 sc1 lds\n"
"buffer_load_dwordx4 v1, s[4:7], s31 offen offset:3072 sc1 lds\n"
"s_add_u32 m0, m0, 1024\n"
"s_add_u32 s31, s31, 1024\n"
"buffer_load_dwordx4 v1, s[4:7], s31 offen offset:3072 sc1 lds\n"
"s_waitcnt vmcnt(0)\n"
"ds_write_b32 v13, v13 offset:400\n"
"s_mov_b32 s17, 0x100000\n"
".Ltp_190:\n"
"ds_read_b32 v90, v14 offset:400\n"
"s_sub_u32 s17, s17, 1\n"
"s_cbranch_scc1 .Lou_191\n"
"s_waitcnt lgkmcnt(0)\n"
"v_cmp_eq_u32 vcc, 0, v90\n"
"s_cbranch_vccnz .Ltp_190\n"
".Lou_191:\n"
"s_waitcnt lgkmcnt(0)\n"
"v_mov_b32 v90, 0\n"
"s_branch .Lbk_189\n"
".Lsl_192:\n"
"s_nop 7\n"
"s_mov_b32 s17, 0x100000\n"
".Ltp_194:\n"
"ds_read_b32 v90, v14 offset:304\n"
"s_sub_u32 s17, s17, 1\n"
"s_cbranch_scc1 .Lou_195\n"
"s_waitcnt lgkmcnt(0)\n"
"v_cmp_eq_u32 vcc, 0, v90\n"
"s_cbranch_vccnz .Ltp_194\n"
".Lou_195:\n"
"s_waitcnt lgkmcnt(0)\n"
"s_branch .Lbk_193\n"
".Lsl_196:\n"
"s_nop 7\n"
"s_mov_b32 s17, 0x100000\n"
".Ltp_198:\n"
"ds_read_b32 v90, v14 offset:416\n"
"s_sub_u32 s17, s17, 1\n"
"s_cbranch_scc1 .Lou_199\n"
"s_waitcnt lgkmcnt(0)\n"
"v_cmp_eq_u32 vcc, 0, v90\n"
"s_cbranch_vccnz .Ltp_198\n"
".Lou_199:\n"
"s_waitcnt lgkmcnt(0)\n"
"s_branch .Lbk_197\n"
".Lsl_200:\n"
"s_nop 7\n"
"s_mov_b32 s17, 0x100000\n"
".Ltp_202:\n"
"ds_read_b32 v90, v14 offset:432\n"
"s_sub_u32 s17, s17, 1\n"
"s_cbranch_scc1 .Lou_203\n"
"s_waitcnt lgkmcnt(0)\n"
"v_cmp_eq_u32 vcc, 0, v90\n"
"s_cbranch_vccnz .Ltp_202\n"
".Lou_203:\n"
"s_waitcnt lgkmcnt(0)\n"
"s_branch .Lbk_201\n"
".Lsl_204:\n"
"s_nop 7\n"
"s_mov_b32 s17, 0x100000\n"
".Ltp_206:\n"
"ds_read_b128 v[84:87], v11 offset:3712\n"
"ds_read_b32 v91, v12 offset:3712\n"
"s_sub_u32 s17, s17, 1\n"
"s_cbranch_scc1 .Lou_207\n"
"s_waitcnt lgkmcnt(0)\n"
"v_max3_u32 v88, v84, v85, v91\n"
"v_max3_u32 v88, v86, v87, v88\n"
"v_cmp_eq_u32 vcc, -1, v88\n"
"s_cbranch_vccnz .Ltp_206\n"
".Lou_207:\n"
"s_waitcnt lgkmcnt(0)\n"
"v_add_f32 v84, v84, v85\n"
"v_add_f32 v86, v86, v87\n"
"v_add_f32 v84, v84, v86\n"
"v_mul_f32 v89, 0x3acccccd, v91\n"
"v_fma_f32 v84, -v91, v89, v84\n"
"v_fmamk_f32 v84, v84, 0x3acccccd, v27\n"
"v_add_f32 v92, v92, v84\n"
"v_max_f32 v84, v84, v27\n"
"v_rsq_f32 v88, v84\n"
"s_nop 0\n"
"v_mul_f32 v89, -v89, v88\n"
"s_nop 1\n"
"s_branch .Lbk_205\n"
".Lsl_208:\n"
"s_nop 7\n"
"s_mov_b32 s17, 0x100000\n"
".Ltp_210:\n"
"ds_read_b128 v[84:87], v11 offset:4032\n"
"ds_read_b32 v91, v12 offset:4032\n"
"s_sub_u32 s17, s17, 1\n"
"s_cbranch_scc1 .Lou_211\n"
"s_waitcnt lgkmcnt(0)\n"
"v_max3_u32 v88, v84, v85, v91\n"
"v_max3_u32 v88, v86, v87, v88\n"
"v_cmp_eq_u32 vcc, -1, v88\n"
"s_cbranch_vccnz .Ltp_210\n"
".Lou_211:\n"
"s_waitcnt lgkmcnt(0)\n"
"v_add_f32 v84, v84, v85\n"
"v_add_f32 v86, v86, v87\n"
"v_add_f32 v84, v84, v86\n"
"v_mul_f32 v89, 0x3acccccd, v91\n"
"v_fma_f32 v84, -v91, v89, v84\n"
"v_fmamk_f32 v84, v84, 0x3acccccd, v27\n"
"v_add_f32 v92, v92, v84\n"
"v_max_f32 v84, v84, v27\n"
"v_rsq_f32 v88, v84\n"
"s_nop 0\n"
"v_mul_f32 v89, -v89, v88\n"
"s_nop 1\n"
"s_branch .Lbk_209\n"
".Lsl_212:\n"
"s_nop 7\n"
"s_mov_b32 s17, 0x100000\n"
".Ltp_214:\n"
"ds_read_b128 v[84:87], v11 offset:4352\n"
"ds_read_b32 v91, v12 offset:4352\n"
"s_sub_u32 s17, s17, 1\n"
"s_cbranch_scc1 .Lou_215\n"
"s_waitcnt lgkmcnt(0)\n"
"v_max3_u32 v88, v84, v85, v91\n"
"v_max3_u32 v88, v86, v87, v88\n"
"v_cmp_eq_u32 vcc, -1, v88\n"
"s_cbranch_vccnz .Ltp_214\n"
".Lou_215:\n"
"s_waitcnt lgkmcnt(0)\n"
"v_add_f32 v84, v84, v85\n"
"v_add_f32 v86, v86, v87\n"
"v_add_f32 v84, v84, v86\n"
"v_mul_f32 v89, 0x3acccccd, v91\n"
"v_fma_f32 v84, -v91, v89, v84\n"
"v_fmamk_f32 v84, v84, 0x3acccccd, v27\n"
"v_add_f32 v92, v92, v84\n"
"v_max_f32 v84, v84, v27\n"
"v_rsq_f32 v88, v84\n"
"s_nop 0\n"
"v_mul_f32 v89, -v89, v88\n"
"s_nop 1\n"
"s_branch .Lbk_213\n"
".Lsl_216:\n"
"s_nop 7\n"
"s_mov_b32 s17, 0x100000\n"
".Ltp_218:\n"
"ds_read_b128 v[84:87], v11 offset:4672\n"
"ds_read_b32 v91, v12 offset:4672\n"
"s_sub_u32 s17, s17, 1\n"
"s_cbranch_scc1 .Lou_219\n"
"s_waitcnt lgkmcnt(0)\n"
"v_max3_u32 v88, v84, v85, v91\n"
"v_max3_u32 v88, v86, v87, v88\n"
"v_cmp_eq_u32 vcc, -1, v88\n"
"s_cbranch_vccnz .Ltp_218\n"
".Lou_219:\n"
"s_waitcnt lgkmcnt(0)\n"
"v_add_f32 v84, v84, v85\n"
"v_add_f32 v86, v86, v87\n"
"v_add_f32 v84, v84, v86\n"
"v_mul_f32 v89, 0x3acccccd, v91\n"
"v_fma_f32 v84, -v91, v89, v84\n"
"v_fmamk_f32 v84, v84, 0x3acccccd, v27\n"
"v_add_f32 v92, v92, v84\n"
"v_max_f32 v84, v84, v27\n"
"v_rsq_f32 v88, v84\n"
"s_nop 0\n"
"v_mul_f32 v89, -v89, v88\n"
"s_nop 1\n"
"s_branch .Lbk_217\n"
".Lsl_220:\n"
"s_nop 7\n"
"s_mov_b32 s17, 0x100000\n"
".Ltp_222:\n"
"ds_read_b128 v[44:47], v11 offset:4992\n"
"ds_read_b128 v[48:51], v11 offset:5312\n"
"s_sub_u32 s17, s17, 1\n"
"s_cbranch_scc1 .Lou_223\n"
"s_waitcnt lgkmcnt(0)\n"
"v_max3_u32 v52, v44, v45, v51\n"
"v_max3_u32 v52, v46, v47, v52\n"
"v_max3_u32 v52, v48, v49, v52\n"
"v_max3_u32 v52, v50, v51, v52\n"
"v_cmp_eq_u32 vcc, -1, v52\n"
"s_cbranch_vccnz .Ltp_222\n"
".Lou_223:\n"
"s_waitcnt lgkmcnt(0)\n"
"v_add_f32 v44, v44, v45\n"
"v_add_f32 v46, v46, v47\n"
"v_add_f32 v48, v48, v49\n"
"v_add_f32 v50, v50, v51\n"
"v_add_f32 v48, v48, v50\n"
"v_add_f32 v44, v44, v46\n"
"v_mul_f32 v53, 0x3acccccd, v48\n"
"v_fma_f32 v44, -v48, v53, v44\n"
"v_fmamk_f32 v44, v44, 0x3acccccd, v27\n"
"v_add_f32 v92, v92, v44\n"
"v_max_f32 v44, v44, v27\n"
"v_rsq_f32 v52, v44\n"
"s_nop 0\n"
"v_mul_f32 v53, -v53, v52\n"
"s_nop 1\n"
"s_branch .Lbk_221\n"
".Lsl_224:\n"
"s_nop 7\n"
"s_mov_b32 s17, 0x100000\n"
".Ltp_226:\n"
"ds_read_b32 v90, v14 offset:368\n"
"s_sub_u32 s17, s17, 1\n"
"s_cbranch_scc1 .Lou_227\n"
"s_waitcnt lgkmcnt(0)\n"
"v_cmp_eq_u32 vcc, 0, v90\n"
"s_cbranch_vccnz .Ltp_226\n"
".Lou_227:\n"
"ds_read_b64 v[56:57], v3 offset:40960\n"
"ds_read_b64 v[58:59], v3 offset:41216\n"
"ds_read_b64 v[60:61], v3 offset:41984\n"
"ds_read_b64 v[62:63], v3 offset:42240\n"
"ds_read_b64 v[64:65], v3 offset:43008\n"
"ds_read_b64 v[66:67], v3 offset:43264\n"
"ds_read_b64 v[68:69], v3 offset:44032\n"
"ds_read_b64 v[70:71], v3 offset:44288\n"
"ds_read_b64 v[72:73], v3 offset:45056\n"
"ds_read_b64 v[74:75], v3 offset:45312\n"
"s_waitcnt lgkmcnt(0)\n"
"s_branch .Lbk_225\n"
".Lrd_228:\n"
"s_nop 7\n"
"s_cmp_lg_u32 s54, 0\n"
"s_cbranch_scc1 .Lgo_187\n"
"s_mov_b32 s54, 1\n"
"s_add_u32 s46, s44, 0x13b000\n"
"s_add_u32 s47, s46, 0x1000\n"
"buffer_load_dwordx4 v[28:31], v1, s[4:7], s46 offen offset:0 sc1\n"
"buffer_load_dwordx4 v[32:35], v1, s[4:7], s46 offen offset:1024 sc1\n"
"buffer_load_dwordx4 v[36:39], v1, s[4:7], s46 offen offset:2048 sc1\n"
"buffer_load_dwordx4 v[40:43], v1, s[4:7], s46 offen offset:3072 sc1\n"
"buffer_load_dwordx4 v[44:47], v1, s[4:7], s47 offen offset:0 sc1\n"
"s_mov_b32 s17, 32768\n"
".Ltp_229:\n"
"s_waitcnt vmcnt(0)\n"
"s_sub_u32 s17, s17, 1\n"
"s_cbranch_scc1 .Lou_230\n"
"v_max_u32 v48, v28, v29\n"
"v_max3_u32 v48, v30, v31, v48\n"
"v_max3_u32 v48, v32, v33, v48\n"
"v_max3_u32 v48, v34, v35, v48\n"
"v_max3_u32 v48, v36, v37, v48\n"
"v_max3_u32 v48, v38, v39, v48\n"
"v_max3_u32 v48, v40, v41, v48\n"
"v_max3_u32 v48, v42, v43, v48\n"
"v_max3_u32 v48, v44, v45, v48\n"
"v_max3_u32 v48, v46, v47, v48\n"
"v_cmp_eq_u32 vcc, -1, v48\n"
"s_cbranch_vccz .Lou_230\n"
"v_max_u32 v48, v30, v31\n"
"v_max3_u32 v48, v28, v29, v48\n"
"v_cmp_eq_u32 vcc, -1, v48\n"
"s_cbranch_vccz .Lsk_231\n"
"s_mov_b64 exec, vcc\n"
"buffer_load_dwordx4 v[28:31], v1, s[4:7], s46 offen offset:0 sc1\n"
"s_mov_b64 exec, -1\n"
".Lsk_231:\n"
"v_max_u32 v48, v34, v35\n"
"v_max3_u32 v48, v32, v33, v48\n"
"v_cmp_eq_u32 vcc, -1, v48\n"
"s_cbranch_vccz .Lsk_232\n"
"s_mov_b64 exec, vcc\n"
"buffer_load_dwordx4 v[32:35], v1, s[4:7], s46 offen offset:1024 sc1\n"
"s_mov_b64 exec, -1\n"
".Lsk_232:\n"
"v_max_u32 v48, v38, v39\n"
"v_max3_u32 v48, v36, v37, v48\n"
"v_cmp_eq_u32 vcc, -1, v48\n"
"s_cbranch_vccz .Lsk_233\n"
"s_mov_b64 exec, vcc\n"
"buffer_load_dwordx4 v[36:39], v1, s[4:7], s46 offen offset:2048 sc1\n"
"s_mov_b64 exec, -1\n"
".Lsk_233:\n"
"v_max_u32 v48, v42, v43\n"
"v_max3_u32 v48, v40, v41, v48\n"
"v_cmp_eq_u32 vcc, -1, v48\n"
"s_cbranch_vccz .Lsk_234\n"
"s_mov_b64 exec, vcc\n"
"buffer_load_dwordx4 v[40:43], v1, s[4:7], s46 offen offset:3072 sc1\n"
"s_mov_b64 exec, -1\n"
".Lsk_234:\n"
"v_max_u32 v48, v46, v47\n"
"v_max3_u32 v48, v44, v45, v48\n"
"v_cmp_eq_u32 vcc, -1, v48\n"
"s_cbranch_vccz .Lsk_235\n"
"s_mov_b64 exec, vcc\n"
"buffer_load_dwordx4 v[44:47], v1, s[4:7], s47 offen offset:0 sc1\n"
"s_mov_b64 exec, -1\n"
".Lsk_235:\n"
"s_branch .Ltp_229\n"
".Lou_230:\n"
"s_waitcnt vmcnt(0)\n"
"s_add_u32 s46, s44, 0x140000\n"
"s_add_u32 s47, s46, 0x1000\n"
"buffer_load_dwordx4 v[28:31], v1, s[4:7], s46 offen offset:0 sc1\n"
"buffer_load_dwordx4 v[32:35], v1, s[4:7], s46 offen offset:1024 sc1\n"
"buffer_load_dwordx4 v[36:39], v1, s[4:7], s46 offen offset:2048 sc1\n"
"buffer_load_dwordx4 v[40:43], v1, s[4:7], s46 offen offset:3072 sc1\n"
"buffer_load_dwordx4 v[44:47], v1, s[4:7], s47 offen offset:0 sc1\n"
"s_mov_b32 s17, 32768\n"
".Ltp_236:\n"
"s_waitcnt vmcnt(0)\n"
"s_sub_u32 s17, s17, 1\n"
"s_cbranch_scc1 .Lou_237\n"
"v_max_u32 v48, v28, v29\n"
"v_max3_u32 v48, v30, v31, v48\n"
"v_max3_u32 v48, v32, v33, v48\n"
"v_max3_u32 v48, v34, v35, v48\n"
"v_max3_u32 v48, v36, v37, v48\n"
"v_max3_u32 v48, v38, v39, v48\n"
"v_max3_u32 v48, v40, v41, v48\n"
"v_max3_u32 v48, v42, v43, v48\n"
"v_max3_u32 v48, v44, v45, v48\n"
"v_max3_u32 v48, v46, v47, v48\n"
"v_cmp_eq_u32 vcc, -1, v48\n"
"s_cbranch_vccz .Lou_237\n"
"v_max_u32 v48, v30, v31\n"
"v_max3_u32 v48, v28, v29, v48\n"
"v_cmp_eq_u32 vcc, -1, v48\n"
"s_cbranch_vccz .Lsk_238\n"
"s_mov_b64 exec, vcc\n"
"buffer_load_dwordx4 v[28:31], v1, s[4:7], s46 offen offset:0 sc1\n"
"s_mov_b64 exec, -1\n"
".Lsk_238:\n"
"v_max_u32 v48, v34, v35\n"
"v_max3_u32 v48, v32, v33, v48\n"
"v_cmp_eq_u32 vcc, -1, v48\n"
"s_cbranch_vccz .Lsk_239\n"
"s_mov_b64 exec, vcc\n"
"buffer_load_dwordx4 v[32:35], v1, s[4:7], s46 offen offset:1024 sc1\n"
"s_mov_b64 exec, -1\n"
".Lsk_239:\n"
"v_max_u32 v48, v38, v39\n"
"v_max3_u32 v48, v36, v37, v48\n"
"v_cmp_eq_u32 vcc, -1, v48\n"
"s_cbranch_vccz .Lsk_240\n"
"s_mov_b64 exec, vcc\n"
"buffer_load_dwordx4 v[36:39], v1, s[4:7], s46 offen offset:2048 sc1\n"
"s_mov_b64 exec, -1\n"
".Lsk_240:\n"
"v_max_u32 v48, v42, v43\n"
"v_max3_u32 v48, v40, v41, v48\n"
"v_cmp_eq_u32 vcc, -1, v48\n"
"s_cbranch_vccz .Lsk_241\n"
"s_mov_b64 exec, vcc\n"
"buffer_load_dwordx4 v[40:43], v1, s[4:7], s46 offen offset:3072 sc1\n"
"s_mov_b64 exec, -1\n"
".Lsk_241:\n"
"v_max_u32 v48, v46, v47\n"
"v_max3_u32 v48, v44, v45, v48\n"
"v_cmp_eq_u32 vcc, -1, v48\n"
"s_cbranch_vccz .Lsk_242\n"
"s_mov_b64 exec, vcc\n"
"buffer_load_dwordx4 v[44:47], v1, s[4:7], s47 offen offset:0 sc1\n"
"s_mov_b64 exec, -1\n"
".Lsk_242:\n"
"s_branch .Ltp_236\n"
".Lou_237:\n"
"s_waitcnt vmcnt(0)\n"
"s_add_u32 s46, s44, 0x145000\n"
"s_add_u32 s47, s46, 0x1000\n"
"buffer_load_dwordx4 v[28:31], v1, s[4:7], s46 offen offset:0 sc1\n"
"buffer_load_dwordx4 v[32:35], v1, s[4:7], s46 offen offset:1024 sc1\n"
"buffer_load_dwordx4 v[36:39], v1, s[4:7], s46 offen offset:2048 sc1\n"
"buffer_load_dwordx4 v[40:43], v1, s[4:7], s46 offen offset:3072 sc1\n"
"buffer_load_dwordx4 v[44:47], v1, s[4:7], s47 offen offset:0 sc1\n"
"s_mov_b32 s17, 32768\n"
".Ltp_243:\n"
"s_waitcnt vmcnt(0)\n"
"s_sub_u32 s17, s17, 1\n"
"s_cbranch_scc1 .Lou_244\n"
"v_max_u32 v48, v28, v29\n"
"v_max3_u32 v48, v30, v31, v48\n"
"v_max3_u32 v48, v32, v33, v48\n"
"v_max3_u32 v48, v34, v35, v48\n"
"v_max3_u32 v48, v36, v37, v48\n"
"v_max3_u32 v48, v38, v39, v48\n"
"v_max3_u32 v48, v40, v41, v48\n"
"v_max3_u32 v48, v42, v43, v48\n"
"v_max3_u32 v48, v44, v45, v48\n"
"v_max3_u32 v48, v46, v47, v48\n"
"v_cmp_eq_u32 vcc, -1, v48\n"
"s_cbranch_vccz .Lou_244\n"
"v_max_u32 v48, v30, v31\n"
"v_max3_u32 v48, v28, v29, v48\n"
"v_cmp_eq_u32 vcc, -1, v48\n"
"s_cbranch_vccz .Lsk_245\n"
"s_mov_b64 exec, vcc\n"
"buffer_load_dwordx4 v[28:31], v1, s[4:7], s46 offen offset:0 sc1\n"
"s_mov_b64 exec, -1\n"
".Lsk_245:\n"
"v_max_u32 v48, v34, v35\n"
"v_max3_u32 v48, v32, v33, v48\n"
"v_cmp_eq_u32 vcc, -1, v48\n"
"s_cbranch_vccz .Lsk_246\n"
"s_mov_b64 exec, vcc\n"
"buffer_load_dwordx4 v[32:35], v1, s[4:7], s46 offen offset:1024 sc1\n"
"s_mov_b64 exec, -1\n"
".Lsk_246:\n"
"v_max_u32 v48, v38, v39\n"
"v_max3_u32 v48, v36, v37, v48\n"
"v_cmp_eq_u32 vcc, -1, v48\n"
"s_cbranch_vccz .Lsk_247\n"
"s_mov_b64 exec, vcc\n"
"buffer_load_dwordx4 v[36:39], v1, s[4:7], s46 offen offset:2048 sc1\n"
"s_mov_b64 exec, -1\n"
".Lsk_247:\n"
"v_max_u32 v48, v42, v43\n"
"v_max3_u32 v48, v40, v41, v48\n"
"v_cmp_eq_u32 vcc, -1, v48\n"
"s_cbranch_vccz .Lsk_248\n"
"s_mov_b64 exec, vcc\n"
"buffer_load_dwordx4 v[40:43], v1, s[4:7], s46 offen offset:3072 sc1\n"
"s_mov_b64 exec, -1\n"
".Lsk_248:\n"
"v_max_u32 v48, v46, v47\n"
"v_max3_u32 v48, v44, v45, v48\n"
"v_cmp_eq_u32 vcc, -1, v48\n"
"s_cbranch_vccz .Lsk_249\n"
"s_mov_b64 exec, vcc\n"
"buffer_load_dwordx4 v[44:47], v1, s[4:7], s47 offen offset:0 sc1\n"
"s_mov_b64 exec, -1\n"
".Lsk_249:\n"
"s_branch .Ltp_243\n"
".Lou_244:\n"
"s_waitcnt vmcnt(0)\n"
"s_add_u32 s46, s44, 0x14a000\n"
"s_add_u32 s47, s46, 0x1000\n"
"buffer_load_dwordx4 v[28:31], v1, s[4:7], s46 offen offset:0 sc1\n"
"buffer_load_dwordx4 v[32:35], v1, s[4:7], s46 offen offset:1024 sc1\n"
"buffer_load_dwordx4 v[36:39], v1, s[4:7], s46 offen offset:2048 sc1\n"
"buffer_load_dwordx4 v[40:43], v1, s[4:7], s46 offen offset:3072 sc1\n"
"buffer_load_dwordx4 v[44:47], v1, s[4:7], s47 offen offset:0 sc1\n"
"s_mov_b32 s17, 32768\n"
".Ltp_250:\n"
"s_waitcnt vmcnt(0)\n"
"s_sub_u32 s17, s17, 1\n"
"s_cbranch_scc1 .Lou_251\n"
"v_max_u32 v48, v28, v29\n"
"v_max3_u32 v48, v30, v31, v48\n"
"v_max3_u32 v48, v32, v33, v48\n"
"v_max3_u32 v48, v34, v35, v48\n"
"v_max3_u32 v48, v36, v37, v48\n"
"v_max3_u32 v48, v38, v39, v48\n"
"v_max3_u32 v48, v40, v41, v48\n"
"v_max3_u32 v48, v42, v43, v48\n"
"v_max3_u32 v48, v44, v45, v48\n"
"v_max3_u32 v48, v46, v47, v48\n"
"v_cmp_eq_u32 vcc, -1, v48\n"
"s_cbranch_vccz .Lou_251\n"
"v_max_u32 v48, v30, v31\n"
"v_max3_u32 v48, v28, v29, v48\n"
"v_cmp_eq_u32 vcc, -1, v48\n"
"s_cbranch_vccz .Lsk_252\n"
"s_mov_b64 exec, vcc\n"
"buffer_load_dwordx4 v[28:31], v1, s[4:7], s46 offen offset:0 sc1\n"
"s_mov_b64 exec, -1\n"
".Lsk_252:\n"
"v_max_u32 v48, v34, v35\n"
"v_max3_u32 v48, v32, v33, v48\n"
"v_cmp_eq_u32 vcc, -1, v48\n"
"s_cbranch_vccz .Lsk_253\n"
"s_mov_b64 exec, vcc\n"
"buffer_load_dwordx4 v[32:35], v1, s[4:7], s46 offen offset:1024 sc1\n"
"s_mov_b64 exec, -1\n"
".Lsk_253:\n"
"v_max_u32 v48, v38, v39\n"
"v_max3_u32 v48, v36, v37, v48\n"
"v_cmp_eq_u32 vcc, -1, v48\n"
"s_cbranch_vccz .Lsk_254\n"
"s_mov_b64 exec, vcc\n"
"buffer_load_dwordx4 v[36:39], v1, s[4:7], s46 offen offset:2048 sc1\n"
"s_mov_b64 exec, -1\n"
".Lsk_254:\n"
"v_max_u32 v48, v42, v43\n"
"v_max3_u32 v48, v40, v41, v48\n"
"v_cmp_eq_u32 vcc, -1, v48\n"
"s_cbranch_vccz .Lsk_255\n"
"s_mov_b64 exec, vcc\n"
"buffer_load_dwordx4 v[40:43], v1, s[4:7], s46 offen offset:3072 sc1\n"
"s_mov_b64 exec, -1\n"
".Lsk_255:\n"
"v_max_u32 v48, v46, v47\n"
"v_max3_u32 v48, v44, v45, v48\n"
"v_cmp_eq_u32 vcc, -1, v48\n"
"s_cbranch_vccz .Lsk_256\n"
"s_mov_b64 exec, vcc\n"
"buffer_load_dwordx4 v[44:47], v1, s[4:7], s47 offen offset:0 sc1\n"
"s_mov_b64 exec, -1\n"
".Lsk_256:\n"
"s_branch .Ltp_250\n"
".Lou_251:\n"
"s_waitcnt vmcnt(0)\n"
"s_add_u32 s46, s44, 0x14f000\n"
"s_add_u32 s47, s46, 0x1000\n"
"buffer_load_dwordx4 v[28:31], v1, s[4:7], s46 offen offset:0 sc1\n"
"buffer_load_dwordx4 v[32:35], v1, s[4:7], s46 offen offset:1024 sc1\n"
"buffer_load_dwordx4 v[36:39], v1, s[4:7], s46 offen offset:2048 sc1\n"
"buffer_load_dwordx4 v[40:43], v1, s[4:7], s46 offen offset:3072 sc1\n"
"buffer_load_dwordx4 v[44:47], v1, s[4:7], s47 offen offset:0 sc1\n"
"s_mov_b32 s17, 32768\n"
".Ltp_257:\n"
"s_waitcnt vmcnt(0)\n"
"s_sub_u32 s17, s17, 1\n"
"s_cbranch_scc1 .Lou_258\n"
"v_max_u32 v48, v28, v29\n"
"v_max3_u32 v48, v30, v31, v48\n"
"v_max3_u32 v48, v32, v33, v48\n"
"v_max3_u32 v48, v34, v35, v48\n"
"v_max3_u32 v48, v36, v37, v48\n"
"v_max3_u32 v48, v38, v39, v48\n"
"v_max3_u32 v48, v40, v41, v48\n"
"v_max3_u32 v48, v42, v43, v48\n"
"v_max3_u32 v48, v44, v45, v48\n"
"v_max3_u32 v48, v46, v47, v48\n"
"v_cmp_eq_u32 vcc, -1, v48\n"
"s_cbranch_vccz .Lou_258\n"
"v_max_u32 v48, v30, v31\n"
"v_max3_u32 v48, v28, v29, v48\n"
"v_cmp_eq_u32 vcc, -1, v48\n"
"s_cbranch_vccz .Lsk_259\n"
"s_mov_b64 exec, vcc\n"
"buffer_load_dwordx4 v[28:31], v1, s[4:7], s46 offen offset:0 sc1\n"
"s_mov_b64 exec, -1\n"
".Lsk_259:\n"
"v_max_u32 v48, v34, v35\n"
"v_max3_u32 v48, v32, v33, v48\n"
"v_cmp_eq_u32 vcc, -1, v48\n"
"s_cbranch_vccz .Lsk_260\n"
"s_mov_b64 exec, vcc\n"
"buffer_load_dwordx4 v[32:35], v1, s[4:7], s46 offen offset:1024 sc1\n"
"s_mov_b64 exec, -1\n"
".Lsk_260:\n"
"v_max_u32 v48, v38, v39\n"
"v_max3_u32 v48, v36, v37, v48\n"
"v_cmp_eq_u32 vcc, -1, v48\n"
"s_cbranch_vccz .Lsk_261\n"
"s_mov_b64 exec, vcc\n"
"buffer_load_dwordx4 v[36:39], v1, s[4:7], s46 offen offset:2048 sc1\n"
"s_mov_b64 exec, -1\n"
".Lsk_261:\n"
"v_max_u32 v48, v42, v43\n"
"v_max3_u32 v48, v40, v41, v48\n"
"v_cmp_eq_u32 vcc, -1, v48\n"
"s_cbranch_vccz .Lsk_262\n"
"s_mov_b64 exec, vcc\n"
"buffer_load_dwordx4 v[40:43], v1, s[4:7], s46 offen offset:3072 sc1\n"
"s_mov_b64 exec, -1\n"
".Lsk_262:\n"
"v_max_u32 v48, v46, v47\n"
"v_max3_u32 v48, v44, v45, v48\n"
"v_cmp_eq_u32 vcc, -1, v48\n"
"s_cbranch_vccz .Lsk_263\n"
"s_mov_b64 exec, vcc\n"
"buffer_load_dwordx4 v[44:47], v1, s[4:7], s47 offen offset:0 sc1\n"
"s_mov_b64 exec, -1\n"
".Lsk_263:\n"
"s_branch .Ltp_257\n"
".Lou_258:\n"
"s_waitcnt vmcnt(0)\n"
"ds_write_b32 v13, v13 offset:448\n"
"s_mov_b32 s17, 0x100000\n"
".Ltp_264:\n"
"ds_read_b32 v90, v14 offset:448\n"
"s_sub_u32 s17, s17, 1\n"
"s_cbranch_scc1 .Lou_265\n"
"s_waitcnt lgkmcnt(0)\n"
"v_cmp_eq_u32 vcc, 0, v90\n"
"s_cbranch_vccnz .Ltp_264\n"
".Lou_265:\n"
"s_waitcnt lgkmcnt(0)\n"
"v_mov_b32 v90, 0\n"
"v_mov_b32 v90, -1\n"
"ds_write_b32 v9, v90 offset:3712\n"
"ds_write_b32 v9, v90 offset:4032\n"
"ds_write_b32 v9, v90 offset:4352\n"
"ds_write_b32 v9, v90 offset:4672\n"
"ds_write_b32 v9, v90 offset:4992\n"
"ds_write_b32 v10, v90 offset:5312\n"
"s_add_u32 m0, s29, 0xa000\n"
"s_add_u32 s31, s44, 0x140000\n"
"buffer_load_dwordx4 v1, s[4:7], s31 offen offset:0 sc1 lds\n"
"buffer_load_dwordx4 v1, s[4:7], s31 offen offset:1024 sc1 lds\n"
"buffer_load_dwordx4 v1, s[4:7], s31 offen offset:2048 sc1 lds\n"
"buffer_load_dwordx4 v1, s[4:7], s31 offen offset:3072 sc1 lds\n"
"s_add_u32 m0, m0, 1024\n"
"s_add_u32 s31, s31, 1024\n"
"buffer_load_dwordx4 v1, s[4:7], s31 offen offset:3072 sc1 lds\n"
"s_add_u32 m0, s29, 0xf000\n"
"s_add_u32 s31, s44, 0x145000\n"
"buffer_load_dwordx4 v1, s[4:7], s31 offen offset:0 sc1 lds\n"
"buffer_load_dwordx4 v1, s[4:7], s31 offen offset:1024 sc1 lds\n"
"buffer_load_dwordx4 v1, s[4:7], s31 offen offset:2048 sc1 lds\n"
"buffer_load_dwordx4 v1, s[4:7], s31 offen offset:3072 sc1 lds\n"
"s_add_u32 m0, m0, 1024\n"
"s_add_u32 s31, s31, 1024\n"
"buffer_load_dwordx4 v1, s[4:7], s31 offen offset:3072 sc1 lds\n"
"s_add_u32 m0, s29, 0\n"
"s_add_u32 s31, s44, 0x14a000\n"
"buffer_load_dwordx4 v1, s[4:7], s31 offen offset:0 sc1 lds\n"
"buffer_load_dwordx4 v1, s[4:7], s31 offen offset:1024 sc1 lds\n"
"buffer_load_dwordx4 v1, s[4:7], s31 offen offset:2048 sc1 lds\n"
"buffer_load_dwordx4 v1, s[4:7], s31 offen offset:3072 sc1 lds\n"
"s_add_u32 m0, m0, 1024\n"
"s_add_u32 s31, s31, 1024\n"
"buffer_load_dwordx4 v1, s[4:7], s31 offen offset:3072 sc1 lds\n"
"s_add_u32 m0, s29, 0x5000\n"
"s_add_u32 s31, s44, 0x14f000\n"
"buffer_load_dwordx4 v1, s[4:7], s31 offen offset:0 sc1 lds\n"
"buffer_load_dwordx4 v1, s[4:7], s31 offen offset:1024 sc1 lds\n"
"buffer_load_dwordx4 v1, s[4:7], s31 offen offset:2048 sc1 lds\n"
"buffer_load_dwordx4 v1, s[4:7], s31 offen offset:3072 sc1 lds\n"
"s_add_u32 m0, m0, 1024\n"
"s_add_u32 s31, s31, 1024\n"
"buffer_load_dwordx4 v1, s[4:7], s31 offen offset:3072 sc1 lds\n"
"s_waitcnt vmcnt(0)\n"
"ds_write_b32 v13, v13 offset:464\n"
"s_mov_b32 s17, 0x100000\n"
".Ltp_266:\n"
"ds_read_b32 v90, v14 offset:464\n"
"s_sub_u32 s17, s17, 1\n"
"s_cbranch_scc1 .Lou_267\n"
"s_waitcnt lgkmcnt(0)\n"
"v_cmp_eq_u32 vcc, 0, v90\n"
"s_cbranch_vccnz .Ltp_266\n"
".Lou_267:\n"
"s_waitcnt lgkmcnt(0)\n"
"v_mov_b32 v90, 0\n"
"s_branch .Lag_186\n"
".L_xend:\n"
"v_and_b32 v0, 63, v0\n"
"s_and_b32 s3, s3, 3\n"
"s_and_b32 s79, s2, 7\n"
"s_lshl_b32 s79, s79, 4\n"
"s_lshr_b32 s78, s2, 3\n"
"s_add_u32 s79, s79, s78\n"
"s_and_b32 s78, s2, 3\n"
"s_lshl_b32 s78, s78, 3\n"
"s_and_b32 s77, s2, 4\n"
"s_cmp_eq_i32 s77, 4\n"
"s_cselect_b32 s78, 32, s78\n"
"s_add_u32 s79, s79, s78\n"
"s_and_b32 s78, s2, 4\n"
"s_lshl_b32 s78, s78, 4\n"
"s_add_u32 s78, s2, s78\n"
"s_cmp_lt_i32 s78, 0xc0\n"
"s_cselect_b32 s18, s79, -1\n"
"s_and_b32 s79, s2, 7\n"
"s_lshl_b32 s79, s79, 4\n"
"s_lshr_b32 s78, s2, 3\n"
"s_add_u32 s79, s79, s78\n"
"s_and_b32 s78, s2, 3\n"
"s_lshl_b32 s78, s78, 3\n"
"s_add_u32 s78, s78, 24\n"
"s_and_b32 s77, s2, 4\n"
"s_cmp_eq_i32 s77, 4\n"
"s_cselect_b32 s78, 48, s78\n"
"s_sub_u32 s79, s79, s78\n"
"s_and_b32 s78, s2, 4\n"
"s_lshl_b32 s78, s78, 4\n"
"s_add_u32 s78, s2, s78\n"
"s_cmp_lt_i32 s78, 0xc0\n"
"s_cselect_b32 s19, 0x3e8, s79\n"
"v_lshlrev_b32 v2, 4, v0\n"
"s_mov_b32 s4, s26\n"
"s_mov_b32 s5, s27\n"
"s_mov_b32 s6, 0x15a8000\n"
"s_mov_b32 s7, 0x20000\n"
"v_lshlrev_b32 v127, 2, v0\n"
"s_mul_i32 s79, s3, 0x2100\n"
"s_add_u32 s79, s79, 0x14000\n"
"v_add_u32 v152, s79, v127\n"
"v_add_u32 v153, 0x440, v152\n"
"v_add_u32 v154, 0x880, v152\n"
"v_add_u32 v155, 0xcc0, v152\n"
"s_mul_i32 s79, s3, 0x2100\n"
"s_add_u32 s79, s79, 0x14000\n"
"v_and_b32 v99, 15, v0\n"
"v_and_b32 v98, 48, v0\n"
"s_mov_b32 s78, 0x110\n"
"v_mad_i32_i24 v99, v99, s78, v98\n"
"v_add_u32 v141, s79, v99\n"
"s_lshl_b32 s79, s3, 2\n"
"s_add_u32 s79, s79, 0x26400\n"
"v_mov_b32 v99, s79\n"
"v_mov_b32 v98, 0x40000\n"
"v_cmp_eq_i32 vcc, 0, v0\n"
"s_nop 1\n"
"v_cndmask_b32 v4, v98, v99, vcc\n"
"v_and_b32 v99, 3, v0\n"
"v_lshlrev_b32 v99, 2, v99\n"
"v_add_u32 v5, 0x26400, v99\n"
"v_min_i32 v99, 49, v0\n"
"v_lshlrev_b32 v38, 4, v99\n"
"s_and_b32 s79, s2, 1\n"
"s_lshl_b32 s79, s79, 2\n"
"s_lshr_b32 s78, s2, 3\n"
"s_and_b32 s78, s78, 3\n"
"s_add_u32 s28, s79, s78\n"
"s_lshr_b32 s79, s2, 1\n"
"s_and_b32 s79, s79, 3\n"
"s_lshl_b32 s79, s79, 3\n"
"s_lshr_b32 s78, s2, 3\n"
"s_lshr_b32 s78, s78, 2\n"
"s_add_u32 s29, s79, s78\n"
"s_and_b32 s79, s2, 7\n"
"s_lshl_b32 s79, s79, 12\n"
"s_add_u32 s48, s79, 0x11000\n"
"s_mov_b32 s8, s22\n"
"s_mov_b32 s9, s23\n"
"s_mov_b32 s10, 0x280000\n"
"s_mov_b32 s11, 0x20000\n"
"s_mov_b32 s12, s24\n"
"s_mov_b32 s13, s25\n"
"s_mov_b32 s14, 0x280000\n"
"s_mov_b32 s15, 0x20000\n"
"s_mov_b32 s44, 0xcccccccc\n"
"s_mov_b32 s45, 0xcccccccc\n"
"s_mov_b32 s56, 0\n"
"s_mov_b32 s57, -2\n"
"s_mov_b32 s58, 0\n"
"s_mov_b32 s59, -1\n"
"s_lshl_b32 s79, s29, 1\n"
"s_lshr_b32 s78, s3, 1\n"
"s_add_u32 s79, s79, s78\n"
"s_mul_i32 s30, s79, 0x5000\n"
"s_and_b32 s79, s2, 7\n"
"s_lshl_b32 s79, s79, 13\n"
"s_add_u32 s33, s79, 0x2400\n"
"s_lshl_b32 s79, s28, 2\n"
"s_add_u32 s79, s79, s3\n"
"s_mul_i32 s79, s79, 0x6400\n"
"s_add_u32 s32, s79, 0x2a0000\n"
"s_lshr_b32 s79, s2, 1\n"
"s_and_b32 s79, s79, 3\n"
"s_lshl_b32 s79, s79, 2\n"
"s_lshr_b32 s78, s2, 3\n"
"s_lshr_b32 s78, s78, 3\n"
"s_add_u32 s79, s79, s78\n"
"s_lshl_b32 s79, s79, 2\n"
"s_add_u32 s79, s79, s3\n"
"s_mul_i32 s79, s79, 0x5000\n"
"s_and_b32 s78, s2, 1\n"
"s_lshl_b32 s78, s78, 3\n"
"s_lshr_b32 s77, s2, 3\n"
"s_and_b32 s77, s77, 7\n"
"s_add_u32 s78, s78, s77\n"
"s_mul_i32 s78, s78, 0x500\n"
"s_add_u32 s79, s79, s78\n"
"s_add_u32 s31, s79, 0x368000\n"
"v_mov_b32 v128, 0\n"
"v_mov_b32 v129, 0\n"
"v_mov_b32 v130, 0\n"
"v_mov_b32 v131, 0\n"
"v_mov_b32 v132, 0\n"
"v_mov_b32 v133, 0\n"
"v_mov_b32 v134, 0\n"
"v_mov_b32 v135, 0\n"
"v_mov_b32 v136, 0\n"
"v_mov_b32 v137, 0\n"
"v_mov_b32 v138, 0\n"
"v_mov_b32 v139, 0\n"
"s_waitcnt lgkmcnt(0)\n"
"s_and_b32 s79, s3, 1\n"
"s_lshl_b32 s60, s79, 4\n"
"s_lshr_b32 s40, s40, s60\n"
"s_lshr_b32 s41, s41, s60\n"
"v_mov_b32 v142, s40\n"
"v_mov_b32 v140, s41\n"
"s_mul_i32 s16, s16, 0x2c9277b5\n"
"s_add_u32 s16, s16, 0xac564b05\n"
"s_cmp_lt_i32 s18, 0\n"
"s_cbranch_scc1 .Lnk_298\n"
"s_setprio 0\n"
"s_mov_b32 s55, 2\n"
"s_mov_b32 s52, 0xc2f00000\n"
"s_mov_b32 s53, 0x3fb8aa3b\n"
"s_mov_b32 s54, 0x3f317218\n"
"v_mov_b32 v31, 0x42f00000\n"
"v_lshlrev_b32 v142, 16, v142\n"
"v_med3_f32 v142, v142, s52, v31\n"
"v_mul_f32 v28, s53, v142\n"
"v_exp_f32 v29, v28\n"
"v_fma_f32 v28, v142, s53, -v28\n"
"v_fmac_f32 v28, 0x32a57060, v142\n"
"v_fma_f32 v28, v28, s54, 1.0\n"
"v_mul_f32 v142, v29, v28\n"
"v_cvt_pk_bf16_f32 v142, v142, v142\n"
"v_lshlrev_b32 v142, 16, v142\n"
"s_waitcnt vmcnt(8)\n"
"s_nop 3\n"
".Lkg_305:\n"
"v_lshlrev_b32 v40, 16, v40\n"
"v_lshlrev_b32 v41, 16, v41\n"
"v_lshlrev_b32 v42, 16, v42\n"
"v_lshlrev_b32 v43, 16, v43\n"
"v_mul_f32 v26, v142, v42\n"
"v_mul_f32 v27, v142, v43\n"
"v_max3_f32 v30, |v26|, |v27|, 0\n"
"s_nop 1\n"
"v_max_u32_dpp v30, v30, v30 row_ror:8 row_mask:0xf bank_mask:0xf\n"
"v_med3_f32 v40, v40, s52, v31\n"
"s_nop 0\n"
"v_max_u32_dpp v30, v30, v30 row_ror:4 row_mask:0xf bank_mask:0xf\n"
"v_mul_f32 v28, s53, v40\n"
"s_nop 0\n"
"v_max_u32_dpp v30, v30, v30 row_ror:2 row_mask:0xf bank_mask:0xf\n"
"v_exp_f32 v29, v28\n"
"s_nop 0\n"
"v_max_u32_dpp v30, v30, v30 row_ror:1 row_mask:0xf bank_mask:0xf\n"
"v_fma_f32 v28, v40, s53, -v28\n"
"v_fmac_f32 v28, 0x32a57060, v40\n"
"v_mov_b32 v42, v30\n"
"v_fma_f32 v28, v28, s54, 1.0\n"
"s_nop 0\n"
"v_permlane16_swap_b32 v30, v42\n"
"v_mul_f32 v40, v29, v28\n"
"v_max_u32 v30, v30, v42\n"
"v_cvt_pk_bf16_f32 v40, v40, v40\n"
"v_lshlrev_b32 v40, 16, v40\n"
"v_mov_b32 v42, v30\n"
"v_mul_f32 v25, v142, v41\n"
"s_nop 0\n"
"v_permlane32_swap_b32 v30, v42\n"
"v_mul_f32 v24, v142, -v40\n"
"v_max_u32 v30, v30, v42\n"
"v_mul_f32 v25, 0.15915494, v25\n"
"v_mul_f32 v24, s53, v24\n"
"v_readfirstlane_b32 s51, v30\n"
"s_nop 2\n"
"s_bfe_u32 s50, s51, 0x80017\n"
"s_max_i32 s50, s50, 37\n"
"s_min_i32 s50, s50, 217\n"
"s_cmp_gt_i32 s51, 0\n"
"s_cselect_b32 s50, s50, 127\n"
"s_sub_u32 s51, 267, s50\n"
"s_lshl_b32 s51, s51, 23\n"
"s_sub_u32 s50, s50, 27\n"
"s_lshl_b32 s50, s50, 23\n"
"v_mul_f32 v26, s51, v26\n"
"v_mul_f32 v27, s51, v27\n"
"v_mul_f32 v111, 0x41800000, v25\n"
"v_mul_f32 v100, 0x41800000, v24\n"
"v_fract_f32 v111, v111\n"
"v_exp_f32 v100, v100\n"
"v_cos_f32 v110, v111\n"
"v_sin_f32 v111, -v111\n"
"v_mul_f32 v110, v110, v100\n"
"v_mul_f32 v111, v111, v100\n"
"v_fract_f32 v113, v25\n"
"v_exp_f32 v100, v24\n"
"v_cos_f32 v112, v113\n"
"v_sin_f32 v113, v113\n"
"v_mul_f32 v112, v112, v100\n"
"v_mul_f32 v113, v113, v100\n"
"v_mul_f32 v115, 0x43800000, v25\n"
"v_mul_f32 v100, 0x43800000, v24\n"
"v_fract_f32 v115, v115\n"
"v_exp_f32 v100, v100\n"
"v_cos_f32 v114, v115\n"
"v_sin_f32 v115, v115\n"
"v_mul_f32 v114, v114, v100\n"
"v_mul_f32 v115, v115, v100\n"
"v_mul_f32 v116, v111, v111\n"
"v_add_f32 v117, v110, v110\n"
"v_fma_f32 v116, v110, v110, -v116\n"
"v_mul_f32 v117, v117, v111\n"
"v_mul_f32 v120, v117, v117\n"
"v_add_f32 v121, v116, v116\n"
"v_fma_f32 v120, v116, v116, -v120\n"
"v_mul_f32 v121, v121, v117\n"
"v_mul_f32 v118, v117, v111\n"
"v_mul_f32 v119, v116, v111\n"
"v_fma_f32 v118, v116, v110, -v118\n"
"v_fmac_f32 v119, v117, v110\n"
"v_mul_f32 v122, v121, v121\n"
"v_add_f32 v123, v120, v120\n"
"v_fma_f32 v122, v120, v120, -v122\n"
"v_mul_f32 v123, v123, v121\n"
"v_mov_b32 v84, 0x7400\n"
"v_mul_f32 v124, 0x46800000, v110\n"
"v_mul_f32 v125, 0x46800000, v111\n"
"v_cvt_pk_f16_f32 v100, v124, v125\n"
"v_mul_f32 v124, 0x46800000, v116\n"
"v_mul_f32 v125, 0x46800000, v117\n"
"v_cvt_pk_f16_f32 v85, v124, v125\n"
"v_mul_f32 v124, 0x46800000, v118\n"
"v_mul_f32 v125, 0x46800000, v119\n"
"v_cvt_pk_f16_f32 v101, v124, v125\n"
"v_cvt_pk_f16_f32 v93, v120, v121\n"
"v_cvt_pk_f16_f32 v94, v122, v123\n"
"v_mul_f32 v124, v123, v121\n"
"v_mul_f32 v125, v122, v121\n"
"v_fma_f32 v124, v122, v120, -v124\n"
"v_fmac_f32 v125, v123, v120\n"
"v_cvt_pk_f16_f32 v95, v124, v125\n"
"ds_write2_b32 v152, v84, v100 offset0:0 offset1:68\n"
"v_pk_mul_f16 v86, v84, v93 op_sel:[0,0] op_sel_hi:[1,0]\n"
"v_pk_mul_f16 v102, v100, v93 op_sel:[0,0] op_sel_hi:[1,0]\n"
"v_pk_fma_f16 v86, v84, v93, v86 op_sel:[1,1,0] op_sel_hi:[0,1,1] neg_lo:[1,0,0]\n"
"v_pk_fma_f16 v102, v100, v93, v102 op_sel:[1,1,0] op_sel_hi:[0,1,1] neg_lo:[1,0,0]\n"
"ds_write2_b32 v152, v85, v101 offset0:136 offset1:204\n"
"v_pk_mul_f16 v87, v85, v93 op_sel:[0,0] op_sel_hi:[1,0]\n"
"v_pk_mul_f16 v103, v101, v93 op_sel:[0,0] op_sel_hi:[1,0]\n"
"v_pk_fma_f16 v87, v85, v93, v87 op_sel:[1,1,0] op_sel_hi:[0,1,1] neg_lo:[1,0,0]\n"
"v_pk_fma_f16 v103, v101, v93, v103 op_sel:[1,1,0] op_sel_hi:[0,1,1] neg_lo:[1,0,0]\n"
"ds_write2_b32 v153, v86, v102 offset0:0 offset1:68\n"
"v_pk_mul_f16 v88, v84, v94 op_sel:[0,0] op_sel_hi:[1,0]\n"
"v_pk_mul_f16 v102, v100, v94 op_sel:[0,0] op_sel_hi:[1,0]\n"
"v_pk_fma_f16 v88, v84, v94, v88 op_sel:[1,1,0] op_sel_hi:[0,1,1] neg_lo:[1,0,0]\n"
"v_pk_fma_f16 v102, v100, v94, v102 op_sel:[1,1,0] op_sel_hi:[0,1,1] neg_lo:[1,0,0]\n"
"ds_write2_b32 v153, v87, v103 offset0:136 offset1:204\n"
"v_pk_mul_f16 v89, v85, v94 op_sel:[0,0] op_sel_hi:[1,0]\n"
"v_pk_mul_f16 v103, v101, v94 op_sel:[0,0] op_sel_hi:[1,0]\n"
"v_pk_fma_f16 v89, v85, v94, v89 op_sel:[1,1,0] op_sel_hi:[0,1,1] neg_lo:[1,0,0]\n"
"v_pk_fma_f16 v103, v101, v94, v103 op_sel:[1,1,0] op_sel_hi:[0,1,1] neg_lo:[1,0,0]\n"
"ds_write2_b32 v154, v88, v102 offset0:0 offset1:68\n"
"v_pk_mul_f16 v90, v84, v95 op_sel:[0,0] op_sel_hi:[1,0]\n"
"v_pk_mul_f16 v102, v100, v95 op_sel:[0,0] op_sel_hi:[1,0]\n"
"v_pk_fma_f16 v90, v84, v95, v90 op_sel:[1,1,0] op_sel_hi:[0,1,1] neg_lo:[1,0,0]\n"
"v_pk_fma_f16 v102, v100, v95, v102 op_sel:[1,1,0] op_sel_hi:[0,1,1] neg_lo:[1,0,0]\n"
"ds_write2_b32 v154, v89, v103 offset0:136 offset1:204\n"
"v_pk_mul_f16 v91, v85, v95 op_sel:[0,0] op_sel_hi:[1,0]\n"
"v_pk_mul_f16 v103, v101, v95 op_sel:[0,0] op_sel_hi:[1,0]\n"
"v_pk_fma_f16 v91, v85, v95, v91 op_sel:[1,1,0] op_sel_hi:[0,1,1] neg_lo:[1,0,0]\n"
"v_pk_fma_f16 v103, v101, v95, v103 op_sel:[1,1,0] op_sel_hi:[0,1,1] neg_lo:[1,0,0]\n"
"ds_write2_b32 v155, v90, v102 offset0:0 offset1:68\n"
"ds_write2_b32 v155, v91, v103 offset0:136 offset1:204\n"
"ds_read_b128 v[52:55], v141 offset:0\n"
"ds_read_b128 v[56:59], v141 offset:64\n"
"ds_read_b128 v[60:63], v141 offset:128\n"
"ds_read_b128 v[64:67], v141 offset:192\n"
"v_mul_f32 v116, v113, v113\n"
"v_add_f32 v117, v112, v112\n"
"v_fma_f32 v116, v112, v112, -v116\n"
"v_mul_f32 v117, v117, v113\n"
"v_mul_f32 v120, v117, v117\n"
"v_add_f32 v121, v116, v116\n"
"v_fma_f32 v120, v116, v116, -v120\n"
"v_mul_f32 v121, v121, v117\n"
"v_mul_f32 v118, v117, v113\n"
"v_mul_f32 v119, v116, v113\n"
"v_fma_f32 v118, v116, v112, -v118\n"
"v_fmac_f32 v119, v117, v112\n"
"v_mul_f32 v122, v121, v121\n"
"v_add_f32 v123, v120, v120\n"
"v_fma_f32 v122, v120, v120, -v122\n"
"v_mul_f32 v123, v123, v121\n"
"v_cvt_pk_f16_f32 v84, v26, v27\n"
"v_mul_f32 v124, v27, v113\n"
"v_mul_f32 v125, v26, v113\n"
"v_fma_f32 v124, v26, v112, -v124\n"
"v_fmac_f32 v125, v27, v112\n"
"v_cvt_pk_f16_f32 v100, v124, v125\n"
"v_mul_f32 v124, v27, v117\n"
"v_mul_f32 v125, v26, v117\n"
"v_fma_f32 v124, v26, v116, -v124\n"
"v_fmac_f32 v125, v27, v116\n"
"v_cvt_pk_f16_f32 v85, v124, v125\n"
"v_mul_f32 v124, v27, v119\n"
"v_mul_f32 v125, v26, v119\n"
"v_fma_f32 v124, v26, v118, -v124\n"
"v_fmac_f32 v125, v27, v118\n"
"v_cvt_pk_f16_f32 v101, v124, v125\n"
"v_cvt_pk_f16_f32 v93, v120, v121\n"
"v_cvt_pk_f16_f32 v94, v122, v123\n"
"v_mul_f32 v124, v123, v121\n"
"v_mul_f32 v125, v122, v121\n"
"v_fma_f32 v124, v122, v120, -v124\n"
"v_fmac_f32 v125, v123, v120\n"
"v_cvt_pk_f16_f32 v95, v124, v125\n"
"ds_write2_b32 v152, v84, v100 offset0:0 offset1:68\n"
"v_pk_mul_f16 v86, v84, v93 op_sel:[0,0] op_sel_hi:[1,0]\n"
"v_pk_mul_f16 v102, v100, v93 op_sel:[0,0] op_sel_hi:[1,0]\n"
"v_pk_fma_f16 v86, v84, v93, v86 op_sel:[1,1,0] op_sel_hi:[0,1,1] neg_lo:[1,0,0]\n"
"v_pk_fma_f16 v102, v100, v93, v102 op_sel:[1,1,0] op_sel_hi:[0,1,1] neg_lo:[1,0,0]\n"
"ds_write2_b32 v152, v85, v101 offset0:136 offset1:204\n"
"v_pk_mul_f16 v87, v85, v93 op_sel:[0,0] op_sel_hi:[1,0]\n"
"v_pk_mul_f16 v103, v101, v93 op_sel:[0,0] op_sel_hi:[1,0]\n"
"v_pk_fma_f16 v87, v85, v93, v87 op_sel:[1,1,0] op_sel_hi:[0,1,1] neg_lo:[1,0,0]\n"
"v_pk_fma_f16 v103, v101, v93, v103 op_sel:[1,1,0] op_sel_hi:[0,1,1] neg_lo:[1,0,0]\n"
"ds_write2_b32 v153, v86, v102 offset0:0 offset1:68\n"
"v_pk_mul_f16 v88, v84, v94 op_sel:[0,0] op_sel_hi:[1,0]\n"
"v_pk_mul_f16 v102, v100, v94 op_sel:[0,0] op_sel_hi:[1,0]\n"
"v_pk_fma_f16 v88, v84, v94, v88 op_sel:[1,1,0] op_sel_hi:[0,1,1] neg_lo:[1,0,0]\n"
"v_pk_fma_f16 v102, v100, v94, v102 op_sel:[1,1,0] op_sel_hi:[0,1,1] neg_lo:[1,0,0]\n"
"ds_write2_b32 v153, v87, v103 offset0:136 offset1:204\n"
"v_pk_mul_f16 v89, v85, v94 op_sel:[0,0] op_sel_hi:[1,0]\n"
"v_pk_mul_f16 v103, v101, v94 op_sel:[0,0] op_sel_hi:[1,0]\n"
"v_pk_fma_f16 v89, v85, v94, v89 op_sel:[1,1,0] op_sel_hi:[0,1,1] neg_lo:[1,0,0]\n"
"v_pk_fma_f16 v103, v101, v94, v103 op_sel:[1,1,0] op_sel_hi:[0,1,1] neg_lo:[1,0,0]\n"
"ds_write2_b32 v154, v88, v102 offset0:0 offset1:68\n"
"v_pk_mul_f16 v90, v84, v95 op_sel:[0,0] op_sel_hi:[1,0]\n"
"v_pk_mul_f16 v102, v100, v95 op_sel:[0,0] op_sel_hi:[1,0]\n"
"v_pk_fma_f16 v90, v84, v95, v90 op_sel:[1,1,0] op_sel_hi:[0,1,1] neg_lo:[1,0,0]\n"
"v_pk_fma_f16 v102, v100, v95, v102 op_sel:[1,1,0] op_sel_hi:[0,1,1] neg_lo:[1,0,0]\n"
"ds_write2_b32 v154, v89, v103 offset0:136 offset1:204\n"
"v_pk_mul_f16 v91, v85, v95 op_sel:[0,0] op_sel_hi:[1,0]\n"
"v_pk_mul_f16 v103, v101, v95 op_sel:[0,0] op_sel_hi:[1,0]\n"
"v_pk_fma_f16 v91, v85, v95, v91 op_sel:[1,1,0] op_sel_hi:[0,1,1] neg_lo:[1,0,0]\n"
"v_pk_fma_f16 v103, v101, v95, v103 op_sel:[1,1,0] op_sel_hi:[0,1,1] neg_lo:[1,0,0]\n"
"ds_write2_b32 v155, v90, v102 offset0:0 offset1:68\n"
"ds_write2_b32 v155, v91, v103 offset0:136 offset1:204\n"
"ds_read_b128 v[68:71], v141 offset:0\n"
"ds_read_b128 v[72:75], v141 offset:64\n"
"ds_read_b128 v[76:79], v141 offset:128\n"
"ds_read_b128 v[80:83], v141 offset:192\n"
"v_mul_f32 v116, v115, v115\n"
"v_add_f32 v117, v114, v114\n"
"v_fma_f32 v116, v114, v114, -v116\n"
"v_mul_f32 v117, v117, v115\n"
"v_mul_f32 v120, v117, v117\n"
"v_add_f32 v121, v116, v116\n"
"v_fma_f32 v120, v116, v116, -v120\n"
"v_mul_f32 v121, v121, v117\n"
"v_mul_f32 v118, v117, v115\n"
"v_mul_f32 v119, v116, v115\n"
"v_fma_f32 v118, v116, v114, -v118\n"
"v_fmac_f32 v119, v117, v114\n"
"v_cvt_pk_f16_f32 v92, v114, v115\n"
"v_cvt_pk_f16_f32 v93, v116, v117\n"
"v_cvt_pk_f16_f32 v95, v120, v121\n"
"v_cvt_pk_f16_f32 v94, v118, v119\n"
"v_mul_f32 v124, v121, v115\n"
"v_mul_f32 v125, v120, v115\n"
"v_fma_f32 v124, v120, v114, -v124\n"
"v_fmac_f32 v125, v121, v114\n"
"v_cvt_pk_f16_f32 v96, v124, v125\n"
"v_mul_f32 v126, v121, v117\n"
"v_mul_f32 v127, v120, v117\n"
"v_fma_f32 v126, v120, v116, -v126\n"
"v_fmac_f32 v127, v121, v116\n"
"v_cvt_pk_f16_f32 v97, v126, v127\n"
"v_mul_f32 v124, v121, v119\n"
"v_mul_f32 v125, v120, v119\n"
"v_fma_f32 v124, v120, v118, -v124\n"
"v_fmac_f32 v125, v121, v118\n"
"v_cvt_pk_f16_f32 v98, v124, v125\n"
"v_mul_f32 v126, v121, v121\n"
"v_mul_f32 v127, v120, v121\n"
"v_fma_f32 v126, v120, v120, -v126\n"
"v_fmac_f32 v127, v121, v120\n"
"v_cvt_pk_f16_f32 v99, v126, v127\n"
"s_setprio 3\n"
"s_waitcnt lgkmcnt(3)\n"
"v_mfma_f32_16x16x32_f16 v[6:9], v[68:71], v[52:55], 0\n"
"v_pk_mul_f16 v100, v84, v92 op_sel:[0,0] op_sel_hi:[1,0]\n"
"v_pk_mul_f16 v101, v85, v92 op_sel:[0,0] op_sel_hi:[1,0]\n"
"s_waitcnt lgkmcnt(2)\n"
"v_mfma_f32_16x16x32_f16 v[6:9], v[72:75], v[56:59], v[6:9]\n"
"v_pk_fma_f16 v100, v84, v92, v100 op_sel:[1,1,0] op_sel_hi:[0,1,1] neg_lo:[1,0,0]\n"
"v_pk_fma_f16 v101, v85, v92, v101 op_sel:[1,1,0] op_sel_hi:[0,1,1] neg_lo:[1,0,0]\n"
"s_waitcnt lgkmcnt(1)\n"
"v_mfma_f32_16x16x32_f16 v[6:9], v[76:79], v[60:63], v[6:9]\n"
"v_pk_mul_f16 v102, v86, v92 op_sel:[0,0] op_sel_hi:[1,0]\n"
"v_pk_mul_f16 v103, v87, v92 op_sel:[0,0] op_sel_hi:[1,0]\n"
"s_waitcnt lgkmcnt(0)\n"
"v_mfma_f32_16x16x32_f16 v[6:9], v[80:83], v[64:67], v[6:9]\n"
"s_setprio 0\n"
"v_pk_fma_f16 v102, v86, v92, v102 op_sel:[1,1,0] op_sel_hi:[0,1,1] neg_lo:[1,0,0]\n"
"v_pk_fma_f16 v103, v87, v92, v103 op_sel:[1,1,0] op_sel_hi:[0,1,1] neg_lo:[1,0,0]\n"
"ds_write2_b32 v152, v100, v101 offset0:0 offset1:68\n"
"v_pk_mul_f16 v100, v88, v92 op_sel:[0,0] op_sel_hi:[1,0]\n"
"v_pk_mul_f16 v101, v89, v92 op_sel:[0,0] op_sel_hi:[1,0]\n"
"v_pk_fma_f16 v100, v88, v92, v100 op_sel:[1,1,0] op_sel_hi:[0,1,1] neg_lo:[1,0,0]\n"
"v_pk_fma_f16 v101, v89, v92, v101 op_sel:[1,1,0] op_sel_hi:[0,1,1] neg_lo:[1,0,0]\n"
"ds_write2_b32 v152, v102, v103 offset0:136 offset1:204\n"
"v_pk_mul_f16 v102, v90, v92 op_sel:[0,0] op_sel_hi:[1,0]\n"
"v_pk_mul_f16 v103, v91, v92 op_sel:[0,0] op_sel_hi:[1,0]\n"
"v_pk_fma_f16 v102, v90, v92, v102 op_sel:[1,1,0] op_sel_hi:[0,1,1] neg_lo:[1,0,0]\n"
"v_pk_fma_f16 v103, v91, v92, v103 op_sel:[1,1,0] op_sel_hi:[0,1,1] neg_lo:[1,0,0]\n"
"ds_write2_b32 v153, v100, v101 offset0:0 offset1:68\n"
"v_pk_mul_f16 v100, v84, v93 op_sel:[0,0] op_sel_hi:[1,0]\n"
"v_pk_mul_f16 v101, v86, v93 op_sel:[0,0] op_sel_hi:[1,0]\n"
"v_pk_fma_f16 v100, v84, v93, v100 op_sel:[1,1,0] op_sel_hi:[0,1,1] neg_lo:[1,0,0]\n"
"v_pk_fma_f16 v101, v86, v93, v101 op_sel:[1,1,0] op_sel_hi:[0,1,1] neg_lo:[1,0,0]\n"
"ds_write2_b32 v153, v102, v103 offset0:136 offset1:204\n"
"v_pk_mul_f16 v102, v88, v93 op_sel:[0,0] op_sel_hi:[1,0]\n"
"v_pk_mul_f16 v103, v90, v93 op_sel:[0,0] op_sel_hi:[1,0]\n"
"v_pk_fma_f16 v102, v88, v93, v102 op_sel:[1,1,0] op_sel_hi:[0,1,1] neg_lo:[1,0,0]\n"
"v_pk_fma_f16 v103, v90, v93, v103 op_sel:[1,1,0] op_sel_hi:[0,1,1] neg_lo:[1,0,0]\n"
"ds_write2_b32 v154, v100, v101 offset0:0 offset1:68\n"
"v_pk_mul_f16 v100, v84, v94 op_sel:[0,0] op_sel_hi:[1,0]\n"
"v_pk_mul_f16 v101, v86, v94 op_sel:[0,0] op_sel_hi:[1,0]\n"
"v_pk_fma_f16 v100, v84, v94, v100 op_sel:[1,1,0] op_sel_hi:[0,1,1] neg_lo:[1,0,0]\n"
"v_pk_fma_f16 v101, v86, v94, v101 op_sel:[1,1,0] op_sel_hi:[0,1,1] neg_lo:[1,0,0]\n"
"ds_write2_b32 v154, v102, v103 offset0:136 offset1:204\n"
"v_pk_mul_f16 v102, v88, v94 op_sel:[0,0] op_sel_hi:[1,0]\n"
"v_pk_mul_f16 v103, v90, v94 op_sel:[0,0] op_sel_hi:[1,0]\n"
"v_pk_fma_f16 v102, v88, v94, v102 op_sel:[1,1,0] op_sel_hi:[0,1,1] neg_lo:[1,0,0]\n"
"v_pk_fma_f16 v103, v90, v94, v103 op_sel:[1,1,0] op_sel_hi:[0,1,1] neg_lo:[1,0,0]\n"
"ds_write2_b32 v155, v100, v101 offset0:0 offset1:68\n"
"ds_write2_b32 v155, v102, v103 offset0:136 offset1:204\n"
"ds_read_b128 v[68:71], v141 offset:0\n"
"ds_read_b128 v[72:75], v141 offset:64\n"
"ds_read_b128 v[76:79], v141 offset:128\n"
"ds_read_b128 v[80:83], v141 offset:192\n"
"v_pk_mul_f16 v100, v84, v95 op_sel:[0,0] op_sel_hi:[1,0]\n"
"v_pk_mul_f16 v101, v88, v95 op_sel:[0,0] op_sel_hi:[1,0]\n"
"v_pk_fma_f16 v100, v84, v95, v100 op_sel:[1,1,0] op_sel_hi:[0,1,1] neg_lo:[1,0,0]\n"
"v_pk_fma_f16 v101, v88, v95, v101 op_sel:[1,1,0] op_sel_hi:[0,1,1] neg_lo:[1,0,0]\n"
"v_pk_mul_f16 v102, v84, v96 op_sel:[0,0] op_sel_hi:[1,0]\n"
"v_pk_mul_f16 v103, v88, v96 op_sel:[0,0] op_sel_hi:[1,0]\n"
"v_pk_fma_f16 v102, v84, v96, v102 op_sel:[1,1,0] op_sel_hi:[0,1,1] neg_lo:[1,0,0]\n"
"v_pk_fma_f16 v103, v88, v96, v103 op_sel:[1,1,0] op_sel_hi:[0,1,1] neg_lo:[1,0,0]\n"
"v_pk_mul_f16 v104, v84, v97 op_sel:[0,0] op_sel_hi:[1,0]\n"
"v_pk_mul_f16 v105, v88, v97 op_sel:[0,0] op_sel_hi:[1,0]\n"
"v_pk_fma_f16 v104, v84, v97, v104 op_sel:[1,1,0] op_sel_hi:[0,1,1] neg_lo:[1,0,0]\n"
"v_pk_fma_f16 v105, v88, v97, v105 op_sel:[1,1,0] op_sel_hi:[0,1,1] neg_lo:[1,0,0]\n"
"v_pk_mul_f16 v106, v84, v98 op_sel:[0,0] op_sel_hi:[1,0]\n"
"v_pk_mul_f16 v107, v88, v98 op_sel:[0,0] op_sel_hi:[1,0]\n"
"v_pk_fma_f16 v106, v84, v98, v106 op_sel:[1,1,0] op_sel_hi:[0,1,1] neg_lo:[1,0,0]\n"
"v_pk_fma_f16 v107, v88, v98, v107 op_sel:[1,1,0] op_sel_hi:[0,1,1] neg_lo:[1,0,0]\n"
"v_pk_mul_f16 v108, v84, v99 op_sel:[0,0] op_sel_hi:[1,0]\n"
"v_pk_mul_f16 v109, v88, v99 op_sel:[0,0] op_sel_hi:[1,0]\n"
"v_pk_fma_f16 v108, v84, v99, v108 op_sel:[1,1,0] op_sel_hi:[0,1,1] neg_lo:[1,0,0]\n"
"v_pk_fma_f16 v109, v88, v99, v109 op_sel:[1,1,0] op_sel_hi:[0,1,1] neg_lo:[1,0,0]\n"
"s_setprio 3\n"
"s_waitcnt lgkmcnt(3)\n"
"v_mfma_f32_16x16x32_f16 v[10:13], v[68:71], v[52:55], 0\n"
"ds_write2_b32 v152, v100, v101 offset0:0 offset1:68\n"
"s_waitcnt lgkmcnt(3)\n"
"v_mfma_f32_16x16x32_f16 v[10:13], v[72:75], v[56:59], v[10:13]\n"
"ds_write2_b32 v152, v102, v103 offset0:136 offset1:204\n"
"s_waitcnt lgkmcnt(3)\n"
"v_mfma_f32_16x16x32_f16 v[10:13], v[76:79], v[60:63], v[10:13]\n"
"ds_write2_b32 v153, v104, v105 offset0:0 offset1:68\n"
"s_waitcnt lgkmcnt(3)\n"
"v_mfma_f32_16x16x32_f16 v[10:13], v[80:83], v[64:67], v[10:13]\n"
"s_setprio 0\n"
"ds_write2_b32 v153, v106, v107 offset0:136 offset1:204\n"
"ds_write2_b32 v154, v108, v109 offset0:0 offset1:68\n"
"ds_read_b128 v[68:71], v141 offset:0\n"
"ds_read_b128 v[72:75], v141 offset:64\n"
"ds_read_b128 v[76:79], v141 offset:128\n"
"ds_read_b128 v[80:83], v141 offset:192\n"
"s_waitcnt vmcnt(0)\n"
"v_mov_b32 v40, v44\n"
"v_mov_b32 v41, v45\n"
"v_mov_b32 v42, v46\n"
"v_mov_b32 v43, v47\n"
"v_mov_b32 v44, v48\n"
"v_mov_b32 v45, v49\n"
"v_mov_b32 v46, v50\n"
"v_mov_b32 v47, v51\n"
"v_fmac_f32 v128, s50, v6\n"
"v_fmac_f32 v129, s50, v7\n"
"v_fmac_f32 v130, s50, v8\n"
"v_fmac_f32 v131, s50, v9\n"
"v_fmac_f32 v132, s50, v10\n"
"v_fmac_f32 v133, s50, v11\n"
"v_fmac_f32 v134, s50, v12\n"
"v_fmac_f32 v135, s50, v13\n"
"s_sub_u32 s55, s55, 1\n"
"s_setprio 3\n"
"s_waitcnt lgkmcnt(3)\n"
"v_mfma_f32_16x16x32_f16 v[14:17], v[68:71], v[52:55], 0\n"
"s_waitcnt lgkmcnt(2)\n"
"v_mfma_f32_16x16x32_f16 v[14:17], v[72:75], v[56:59], v[14:17]\n"
"s_waitcnt lgkmcnt(1)\n"
"v_mfma_f32_16x16x32_f16 v[14:17], v[76:79], v[60:63], v[14:17]\n"
"s_waitcnt lgkmcnt(0)\n"
"v_mfma_f32_16x16x32_f16 v[14:17], v[80:83], v[64:67], v[14:17]\n"
"s_setprio 0\n"
"s_nop 6\n"
"v_fmac_f32 v136, s50, v14\n"
"v_fmac_f32 v137, s50, v15\n"
"v_fmac_f32 v138, s50, v16\n"
"v_fmac_f32 v139, s50, v17\n"
"s_cmp_lg_u32 s55, 0\n"
"s_cbranch_scc1 .Lkg_305\n"
"v_lshlrev_b32 v40, 16, v40\n"
"v_lshlrev_b32 v41, 16, v41\n"
"v_lshlrev_b32 v42, 16, v42\n"
"v_lshlrev_b32 v43, 16, v43\n"
"v_mul_f32 v26, v142, v42\n"
"v_mul_f32 v27, v142, v43\n"
"v_max3_f32 v30, |v26|, |v27|, 0\n"
"s_nop 1\n"
"v_max_u32_dpp v30, v30, v30 row_ror:8 row_mask:0xf bank_mask:0xf\n"
"v_med3_f32 v40, v40, s52, v31\n"
"s_nop 0\n"
"v_max_u32_dpp v30, v30, v30 row_ror:4 row_mask:0xf bank_mask:0xf\n"
"v_mul_f32 v28, s53, v40\n"
"s_nop 0\n"
"v_max_u32_dpp v30, v30, v30 row_ror:2 row_mask:0xf bank_mask:0xf\n"
"v_exp_f32 v29, v28\n"
"s_nop 0\n"
"v_max_u32_dpp v30, v30, v30 row_ror:1 row_mask:0xf bank_mask:0xf\n"
"v_fma_f32 v28, v40, s53, -v28\n"
"v_fmac_f32 v28, 0x32a57060, v40\n"
"v_mov_b32 v42, v30\n"
"v_fma_f32 v28, v28, s54, 1.0\n"
"s_nop 0\n"
"v_permlane16_swap_b32 v30, v42\n"
"v_mul_f32 v40, v29, v28\n"
"v_max_u32 v30, v30, v42\n"
"v_cvt_pk_bf16_f32 v40, v40, v40\n"
"v_lshlrev_b32 v40, 16, v40\n"
"v_mov_b32 v42, v30\n"
"v_mul_f32 v25, v142, v41\n"
"s_nop 0\n"
"v_permlane32_swap_b32 v30, v42\n"
"v_mul_f32 v24, v142, -v40\n"
"v_max_u32 v30, v30, v42\n"
"v_mul_f32 v25, 0.15915494, v25\n"
"v_mul_f32 v24, s53, v24\n"
"v_readfirstlane_b32 s51, v30\n"
"s_nop 2\n"
"s_bfe_u32 s50, s51, 0x80017\n"
"s_max_i32 s50, s50, 37\n"
"s_min_i32 s50, s50, 217\n"
"s_cmp_gt_i32 s51, 0\n"
"s_cselect_b32 s50, s50, 127\n"
"s_sub_u32 s51, 267, s50\n"
"s_lshl_b32 s51, s51, 23\n"
"s_sub_u32 s50, s50, 27\n"
"s_lshl_b32 s50, s50, 23\n"
"v_mul_f32 v26, s51, v26\n"
"v_mul_f32 v27, s51, v27\n"
"v_mul_f32 v111, 0x41800000, v25\n"
"v_mul_f32 v100, 0x41800000, v24\n"
"v_fract_f32 v111, v111\n"
"v_exp_f32 v100, v100\n"
"v_cos_f32 v110, v111\n"
"v_sin_f32 v111, -v111\n"
"v_mul_f32 v110, v110, v100\n"
"v_mul_f32 v111, v111, v100\n"
"v_fract_f32 v113, v25\n"
"v_exp_f32 v100, v24\n"
"v_cos_f32 v112, v113\n"
"v_sin_f32 v113, v113\n"
"v_mul_f32 v112, v112, v100\n"
"v_mul_f32 v113, v113, v100\n"
"v_mul_f32 v115, 0x43800000, v25\n"
"v_mul_f32 v100, 0x43800000, v24\n"
"v_fract_f32 v115, v115\n"
"v_exp_f32 v100, v100\n"
"v_cos_f32 v114, v115\n"
"v_sin_f32 v115, v115\n"
"v_mul_f32 v114, v114, v100\n"
"v_mul_f32 v115, v115, v100\n"
"v_mul_f32 v116, v111, v111\n"
"v_add_f32 v117, v110, v110\n"
"v_fma_f32 v116, v110, v110, -v116\n"
"v_mul_f32 v117, v117, v111\n"
"v_mul_f32 v120, v117, v117\n"
"v_add_f32 v121, v116, v116\n"
"v_fma_f32 v120, v116, v116, -v120\n"
"v_mul_f32 v121, v121, v117\n"
"v_mul_f32 v118, v117, v111\n"
"v_mul_f32 v119, v116, v111\n"
"v_fma_f32 v118, v116, v110, -v118\n"
"v_fmac_f32 v119, v117, v110\n"
"v_mul_f32 v122, v121, v121\n"
"v_add_f32 v123, v120, v120\n"
"v_fma_f32 v122, v120, v120, -v122\n"
"v_mul_f32 v123, v123, v121\n"
"v_mov_b32 v84, 0x7400\n"
"v_mul_f32 v124, 0x46800000, v110\n"
"v_mul_f32 v125, 0x46800000, v111\n"
"v_cvt_pk_f16_f32 v100, v124, v125\n"
"v_mul_f32 v124, 0x46800000, v116\n"
"v_mul_f32 v125, 0x46800000, v117\n"
"v_cvt_pk_f16_f32 v85, v124, v125\n"
"v_mul_f32 v124, 0x46800000, v118\n"
"v_mul_f32 v125, 0x46800000, v119\n"
"v_cvt_pk_f16_f32 v101, v124, v125\n"
"v_cvt_pk_f16_f32 v93, v120, v121\n"
"v_cvt_pk_f16_f32 v94, v122, v123\n"
"v_mul_f32 v124, v123, v121\n"
"v_mul_f32 v125, v122, v121\n"
"v_fma_f32 v124, v122, v120, -v124\n"
"v_fmac_f32 v125, v123, v120\n"
"v_cvt_pk_f16_f32 v95, v124, v125\n"
"ds_write2_b32 v152, v84, v100 offset0:0 offset1:68\n"
"v_pk_mul_f16 v86, v84, v93 op_sel:[0,0] op_sel_hi:[1,0]\n"
"v_pk_mul_f16 v102, v100, v93 op_sel:[0,0] op_sel_hi:[1,0]\n"
"v_pk_fma_f16 v86, v84, v93, v86 op_sel:[1,1,0] op_sel_hi:[0,1,1] neg_lo:[1,0,0]\n"
"v_pk_fma_f16 v102, v100, v93, v102 op_sel:[1,1,0] op_sel_hi:[0,1,1] neg_lo:[1,0,0]\n"
"ds_write2_b32 v152, v85, v101 offset0:136 offset1:204\n"
"v_pk_mul_f16 v87, v85, v93 op_sel:[0,0] op_sel_hi:[1,0]\n"
"v_pk_mul_f16 v103, v101, v93 op_sel:[0,0] op_sel_hi:[1,0]\n"
"v_pk_fma_f16 v87, v85, v93, v87 op_sel:[1,1,0] op_sel_hi:[0,1,1] neg_lo:[1,0,0]\n"
"v_pk_fma_f16 v103, v101, v93, v103 op_sel:[1,1,0] op_sel_hi:[0,1,1] neg_lo:[1,0,0]\n"
"ds_write2_b32 v153, v86, v102 offset0:0 offset1:68\n"
"v_pk_mul_f16 v88, v84, v94 op_sel:[0,0] op_sel_hi:[1,0]\n"
"v_pk_mul_f16 v102, v100, v94 op_sel:[0,0] op_sel_hi:[1,0]\n"
"v_pk_fma_f16 v88, v84, v94, v88 op_sel:[1,1,0] op_sel_hi:[0,1,1] neg_lo:[1,0,0]\n"
"v_pk_fma_f16 v102, v100, v94, v102 op_sel:[1,1,0] op_sel_hi:[0,1,1] neg_lo:[1,0,0]\n"
"ds_write2_b32 v153, v87, v103 offset0:136 offset1:204\n"
"v_pk_mul_f16 v89, v85, v94 op_sel:[0,0] op_sel_hi:[1,0]\n"
"v_pk_mul_f16 v103, v101, v94 op_sel:[0,0] op_sel_hi:[1,0]\n"
"v_pk_fma_f16 v89, v85, v94, v89 op_sel:[1,1,0] op_sel_hi:[0,1,1] neg_lo:[1,0,0]\n"
"v_pk_fma_f16 v103, v101, v94, v103 op_sel:[1,1,0] op_sel_hi:[0,1,1] neg_lo:[1,0,0]\n"
"ds_write2_b32 v154, v88, v102 offset0:0 offset1:68\n"
"v_pk_mul_f16 v90, v84, v95 op_sel:[0,0] op_sel_hi:[1,0]\n"
"v_pk_mul_f16 v102, v100, v95 op_sel:[0,0] op_sel_hi:[1,0]\n"
"v_pk_fma_f16 v90, v84, v95, v90 op_sel:[1,1,0] op_sel_hi:[0,1,1] neg_lo:[1,0,0]\n"
"v_pk_fma_f16 v102, v100, v95, v102 op_sel:[1,1,0] op_sel_hi:[0,1,1] neg_lo:[1,0,0]\n"
"ds_write2_b32 v154, v89, v103 offset0:136 offset1:204\n"
"v_pk_mul_f16 v91, v85, v95 op_sel:[0,0] op_sel_hi:[1,0]\n"
"v_pk_mul_f16 v103, v101, v95 op_sel:[0,0] op_sel_hi:[1,0]\n"
"v_pk_fma_f16 v91, v85, v95, v91 op_sel:[1,1,0] op_sel_hi:[0,1,1] neg_lo:[1,0,0]\n"
"v_pk_fma_f16 v103, v101, v95, v103 op_sel:[1,1,0] op_sel_hi:[0,1,1] neg_lo:[1,0,0]\n"
"ds_write2_b32 v155, v90, v102 offset0:0 offset1:68\n"
"ds_write2_b32 v155, v91, v103 offset0:136 offset1:204\n"
"ds_read_b128 v[52:55], v141 offset:0\n"
"ds_read_b128 v[56:59], v141 offset:64\n"
"ds_read_b128 v[60:63], v141 offset:128\n"
"ds_read_b128 v[64:67], v141 offset:192\n"
"v_mul_f32 v116, v113, v113\n"
"v_add_f32 v117, v112, v112\n"
"v_fma_f32 v116, v112, v112, -v116\n"
"v_mul_f32 v117, v117, v113\n"
"v_mul_f32 v120, v117, v117\n"
"v_add_f32 v121, v116, v116\n"
"v_fma_f32 v120, v116, v116, -v120\n"
"v_mul_f32 v121, v121, v117\n"
"v_mul_f32 v118, v117, v113\n"
"s_setprio 3\n"
"buffer_load_dwordx4 v[44:47], v38, s[4:7], s33 offen sc1\n"
"s_setprio 0\n"
"v_mul_f32 v119, v116, v113\n"
"v_fma_f32 v118, v116, v112, -v118\n"
"v_fmac_f32 v119, v117, v112\n"
"v_mul_f32 v122, v121, v121\n"
"v_add_f32 v123, v120, v120\n"
"v_fma_f32 v122, v120, v120, -v122\n"
"v_mul_f32 v123, v123, v121\n"
"v_cvt_pk_f16_f32 v84, v26, v27\n"
"v_mul_f32 v124, v27, v113\n"
"v_mul_f32 v125, v26, v113\n"
"v_fma_f32 v124, v26, v112, -v124\n"
"v_fmac_f32 v125, v27, v112\n"
"v_cvt_pk_f16_f32 v100, v124, v125\n"
"v_mul_f32 v124, v27, v117\n"
"v_mul_f32 v125, v26, v117\n"
"v_fma_f32 v124, v26, v116, -v124\n"
"v_fmac_f32 v125, v27, v116\n"
"v_cvt_pk_f16_f32 v85, v124, v125\n"
"v_mul_f32 v124, v27, v119\n"
"v_mul_f32 v125, v26, v119\n"
"v_fma_f32 v124, v26, v118, -v124\n"
"v_fmac_f32 v125, v27, v118\n"
"v_cvt_pk_f16_f32 v101, v124, v125\n"
"v_cvt_pk_f16_f32 v93, v120, v121\n"
"v_cvt_pk_f16_f32 v94, v122, v123\n"
"v_mul_f32 v124, v123, v121\n"
"v_mul_f32 v125, v122, v121\n"
"v_fma_f32 v124, v122, v120, -v124\n"
"v_fmac_f32 v125, v123, v120\n"
"v_cvt_pk_f16_f32 v95, v124, v125\n"
"ds_write2_b32 v152, v84, v100 offset0:0 offset1:68\n"
"v_pk_mul_f16 v86, v84, v93 op_sel:[0,0] op_sel_hi:[1,0]\n"
"v_pk_mul_f16 v102, v100, v93 op_sel:[0,0] op_sel_hi:[1,0]\n"
"v_pk_fma_f16 v86, v84, v93, v86 op_sel:[1,1,0] op_sel_hi:[0,1,1] neg_lo:[1,0,0]\n"
"v_pk_fma_f16 v102, v100, v93, v102 op_sel:[1,1,0] op_sel_hi:[0,1,1] neg_lo:[1,0,0]\n"
"ds_write2_b32 v152, v85, v101 offset0:136 offset1:204\n"
"v_pk_mul_f16 v87, v85, v93 op_sel:[0,0] op_sel_hi:[1,0]\n"
"v_pk_mul_f16 v103, v101, v93 op_sel:[0,0] op_sel_hi:[1,0]\n"
"v_pk_fma_f16 v87, v85, v93, v87 op_sel:[1,1,0] op_sel_hi:[0,1,1] neg_lo:[1,0,0]\n"
"v_pk_fma_f16 v103, v101, v93, v103 op_sel:[1,1,0] op_sel_hi:[0,1,1] neg_lo:[1,0,0]\n"
"ds_write2_b32 v153, v86, v102 offset0:0 offset1:68\n"
"v_pk_mul_f16 v88, v84, v94 op_sel:[0,0] op_sel_hi:[1,0]\n"
"v_pk_mul_f16 v102, v100, v94 op_sel:[0,0] op_sel_hi:[1,0]\n"
"v_pk_fma_f16 v88, v84, v94, v88 op_sel:[1,1,0] op_sel_hi:[0,1,1] neg_lo:[1,0,0]\n"
"v_pk_fma_f16 v102, v100, v94, v102 op_sel:[1,1,0] op_sel_hi:[0,1,1] neg_lo:[1,0,0]\n"
"ds_write2_b32 v153, v87, v103 offset0:136 offset1:204\n"
"v_pk_mul_f16 v89, v85, v94 op_sel:[0,0] op_sel_hi:[1,0]\n"
"v_pk_mul_f16 v103, v101, v94 op_sel:[0,0] op_sel_hi:[1,0]\n"
"v_pk_fma_f16 v89, v85, v94, v89 op_sel:[1,1,0] op_sel_hi:[0,1,1] neg_lo:[1,0,0]\n"
"v_pk_fma_f16 v103, v101, v94, v103 op_sel:[1,1,0] op_sel_hi:[0,1,1] neg_lo:[1,0,0]\n"
"ds_write2_b32 v154, v88, v102 offset0:0 offset1:68\n"
"v_pk_mul_f16 v90, v84, v95 op_sel:[0,0] op_sel_hi:[1,0]\n"
"v_pk_mul_f16 v102, v100, v95 op_sel:[0,0] op_sel_hi:[1,0]\n"
"v_pk_fma_f16 v90, v84, v95, v90 op_sel:[1,1,0] op_sel_hi:[0,1,1] neg_lo:[1,0,0]\n"
"s_waitcnt vmcnt(0)\n"
"v_xor_b32 v44, s16, v44\n"
"v_xor_b32 v45, s16, v45\n"
"v_xor_b32 v46, s16, v46\n"
"v_xor_b32 v47, s16, v47\n"
"v_or_b32 v44, v44, v45\n"
"v_or3_b32 v44, v44, v46, v47\n"
"v_cmp_ne_u32 vcc, 0, v44\n"
"s_cbranch_vccnz .Lsl_301\n"
".Lbk_302:\n"
"v_pk_fma_f16 v102, v100, v95, v102 op_sel:[1,1,0] op_sel_hi:[0,1,1] neg_lo:[1,0,0]\n"
"ds_write2_b32 v154, v89, v103 offset0:136 offset1:204\n"
"v_pk_mul_f16 v91, v85, v95 op_sel:[0,0] op_sel_hi:[1,0]\n"
"v_pk_mul_f16 v103, v101, v95 op_sel:[0,0] op_sel_hi:[1,0]\n"
"v_pk_fma_f16 v91, v85, v95, v91 op_sel:[1,1,0] op_sel_hi:[0,1,1] neg_lo:[1,0,0]\n"
"v_pk_fma_f16 v103, v101, v95, v103 op_sel:[1,1,0] op_sel_hi:[0,1,1] neg_lo:[1,0,0]\n"
"buffer_load_dwordx4 v[156:159], v2, s[4:7], s32 offen offset:0 sc1\n"
"ds_write2_b32 v155, v90, v102 offset0:0 offset1:68\n"
"ds_write2_b32 v155, v91, v103 offset0:136 offset1:204\n"
"ds_read_b128 v[68:71], v141 offset:0\n"
"ds_read_b128 v[72:75], v141 offset:64\n"
"ds_read_b128 v[76:79], v141 offset:128\n"
"ds_read_b128 v[80:83], v141 offset:192\n"
"buffer_load_dwordx4 v[160:163], v2, s[4:7], s32 offen offset:1024 sc1\n"
"v_mul_f32 v116, v115, v115\n"
"v_add_f32 v117, v114, v114\n"
"v_fma_f32 v116, v114, v114, -v116\n"
"v_mul_f32 v117, v117, v115\n"
"v_mul_f32 v120, v117, v117\n"
"v_add_f32 v121, v116, v116\n"
"buffer_load_dwordx4 v[164:167], v2, s[4:7], s32 offen offset:2048 sc1\n"
"v_fma_f32 v120, v116, v116, -v120\n"
"v_mul_f32 v121, v121, v117\n"
"v_mul_f32 v118, v117, v115\n"
"v_mul_f32 v119, v116, v115\n"
"v_fma_f32 v118, v116, v114, -v118\n"
"v_fmac_f32 v119, v117, v114\n"
"buffer_load_dwordx4 v[168:171], v2, s[4:7], s32 offen offset:3072 sc1\n"
"v_cvt_pk_f16_f32 v92, v114, v115\n"
"v_cvt_pk_f16_f32 v93, v116, v117\n"
"v_cvt_pk_f16_f32 v95, v120, v121\n"
"v_cvt_pk_f16_f32 v94, v118, v119\n"
"v_mul_f32 v124, v121, v115\n"
"v_mul_f32 v125, v120, v115\n"
"s_add_u32 s32, s32, 4096\n"
"buffer_load_dwordx4 v[172:175], v2, s[4:7], s32 offen offset:0 sc1\n"
"v_fma_f32 v124, v120, v114, -v124\n"
"v_fmac_f32 v125, v121, v114\n"
"v_cvt_pk_f16_f32 v96, v124, v125\n"
"v_mul_f32 v126, v121, v117\n"
"v_mul_f32 v127, v120, v117\n"
"v_fma_f32 v126, v120, v116, -v126\n"
"buffer_load_dwordx4 v[176:179], v2, s[4:7], s32 offen offset:1024 sc1\n"
"v_fmac_f32 v127, v121, v116\n"
"v_cvt_pk_f16_f32 v97, v126, v127\n"
"v_mul_f32 v124, v121, v119\n"
"v_mul_f32 v125, v120, v119\n"
"v_fma_f32 v124, v120, v118, -v124\n"
"v_fmac_f32 v125, v121, v118\n"
"buffer_load_dwordx4 v[180:183], v2, s[4:7], s32 offen offset:2048 sc1\n"
"v_cvt_pk_f16_f32 v98, v124, v125\n"
"v_mul_f32 v126, v121, v121\n"
"v_mul_f32 v127, v120, v121\n"
"v_fma_f32 v126, v120, v120, -v126\n"
"v_fmac_f32 v127, v121, v120\n"
"v_cvt_pk_f16_f32 v99, v126, v127\n"
"buffer_load_dwordx4 v[184:187], v2, s[4:7], s32 offen offset:3072 sc1\n"
"s_setprio 3\n"
"s_waitcnt lgkmcnt(3)\n"
"v_mfma_f32_16x16x32_f16 v[6:9], v[68:71], v[52:55], 0\n"
"v_pk_mul_f16 v100, v84, v92 op_sel:[0,0] op_sel_hi:[1,0]\n"
"v_pk_mul_f16 v101, v85, v92 op_sel:[0,0] op_sel_hi:[1,0]\n"
"s_waitcnt lgkmcnt(2)\n"
"v_mfma_f32_16x16x32_f16 v[6:9], v[72:75], v[56:59], v[6:9]\n"
"v_pk_fma_f16 v100, v84, v92, v100 op_sel:[1,1,0] op_sel_hi:[0,1,1] neg_lo:[1,0,0]\n"
"s_add_u32 s32, s32, 4096\n"
"buffer_load_dwordx4 v[188:191], v2, s[4:7], s32 offen offset:0 sc1\n"
"v_pk_fma_f16 v101, v85, v92, v101 op_sel:[1,1,0] op_sel_hi:[0,1,1] neg_lo:[1,0,0]\n"
"s_waitcnt lgkmcnt(1)\n"
"v_mfma_f32_16x16x32_f16 v[6:9], v[76:79], v[60:63], v[6:9]\n"
"v_pk_mul_f16 v102, v86, v92 op_sel:[0,0] op_sel_hi:[1,0]\n"
"v_pk_mul_f16 v103, v87, v92 op_sel:[0,0] op_sel_hi:[1,0]\n"
"s_waitcnt lgkmcnt(0)\n"
"v_mfma_f32_16x16x32_f16 v[6:9], v[80:83], v[64:67], v[6:9]\n"
"s_setprio 0\n"
"buffer_load_dwordx4 v[192:195], v2, s[4:7], s32 offen offset:1024 sc1\n"
"v_pk_fma_f16 v102, v86, v92, v102 op_sel:[1,1,0] op_sel_hi:[0,1,1] neg_lo:[1,0,0]\n"
"v_pk_fma_f16 v103, v87, v92, v103 op_sel:[1,1,0] op_sel_hi:[0,1,1] neg_lo:[1,0,0]\n"
"ds_write2_b32 v152, v100, v101 offset0:0 offset1:68\n"
"v_pk_mul_f16 v100, v88, v92 op_sel:[0,0] op_sel_hi:[1,0]\n"
"v_pk_mul_f16 v101, v89, v92 op_sel:[0,0] op_sel_hi:[1,0]\n"
"v_pk_fma_f16 v100, v88, v92, v100 op_sel:[1,1,0] op_sel_hi:[0,1,1] neg_lo:[1,0,0]\n"
"buffer_load_dwordx4 v[196:199], v2, s[4:7], s32 offen offset:2048 sc1\n"
"v_pk_fma_f16 v101, v89, v92, v101 op_sel:[1,1,0] op_sel_hi:[0,1,1] neg_lo:[1,0,0]\n"
"ds_write2_b32 v152, v102, v103 offset0:136 offset1:204\n"
"v_pk_mul_f16 v102, v90, v92 op_sel:[0,0] op_sel_hi:[1,0]\n"
"v_pk_mul_f16 v103, v91, v92 op_sel:[0,0] op_sel_hi:[1,0]\n"
"v_pk_fma_f16 v102, v90, v92, v102 op_sel:[1,1,0] op_sel_hi:[0,1,1] neg_lo:[1,0,0]\n"
"v_pk_fma_f16 v103, v91, v92, v103 op_sel:[1,1,0] op_sel_hi:[0,1,1] neg_lo:[1,0,0]\n"
"buffer_load_dwordx4 v[200:203], v2, s[4:7], s32 offen offset:3072 sc1\n"
"ds_write2_b32 v153, v100, v101 offset0:0 offset1:68\n"
"v_pk_mul_f16 v100, v84, v93 op_sel:[0,0] op_sel_hi:[1,0]\n"
"v_pk_mul_f16 v101, v86, v93 op_sel:[0,0] op_sel_hi:[1,0]\n"
"v_pk_fma_f16 v100, v84, v93, v100 op_sel:[1,1,0] op_sel_hi:[0,1,1] neg_lo:[1,0,0]\n"
"v_pk_fma_f16 v101, v86, v93, v101 op_sel:[1,1,0] op_sel_hi:[0,1,1] neg_lo:[1,0,0]\n"
"ds_write2_b32 v153, v102, v103 offset0:136 offset1:204\n"
"s_add_u32 s32, s32, 4096\n"
"buffer_load_dwordx4 v[204:207], v2, s[4:7], s32 offen offset:0 sc1\n"
"v_pk_mul_f16 v102, v88, v93 op_sel:[0,0] op_sel_hi:[1,0]\n"
"v_pk_mul_f16 v103, v90, v93 op_sel:[0,0] op_sel_hi:[1,0]\n"
"v_pk_fma_f16 v102, v88, v93, v102 op_sel:[1,1,0] op_sel_hi:[0,1,1] neg_lo:[1,0,0]\n"
"v_pk_fma_f16 v103, v90, v93, v103 op_sel:[1,1,0] op_sel_hi:[0,1,1] neg_lo:[1,0,0]\n"
"ds_write2_b32 v154, v100, v101 offset0:0 offset1:68\n"
"v_pk_mul_f16 v100, v84, v94 op_sel:[0,0] op_sel_hi:[1,0]\n"
"buffer_load_dwordx4 v[208:211], v2, s[4:7], s32 offen offset:1024 sc1\n"
"v_pk_mul_f16 v101, v86, v94 op_sel:[0,0] op_sel_hi:[1,0]\n"
"v_pk_fma_f16 v100, v84, v94, v100 op_sel:[1,1,0] op_sel_hi:[0,1,1] neg_lo:[1,0,0]\n"
"v_pk_fma_f16 v101, v86, v94, v101 op_sel:[1,1,0] op_sel_hi:[0,1,1] neg_lo:[1,0,0]\n"
"ds_write2_b32 v154, v102, v103 offset0:136 offset1:204\n"
"v_pk_mul_f16 v102, v88, v94 op_sel:[0,0] op_sel_hi:[1,0]\n"
"v_pk_mul_f16 v103, v90, v94 op_sel:[0,0] op_sel_hi:[1,0]\n"
"buffer_load_dwordx4 v[212:215], v2, s[4:7], s32 offen offset:2048 sc1\n"
"v_pk_fma_f16 v102, v88, v94, v102 op_sel:[1,1,0] op_sel_hi:[0,1,1] neg_lo:[1,0,0]\n"
"v_pk_fma_f16 v103, v90, v94, v103 op_sel:[1,1,0] op_sel_hi:[0,1,1] neg_lo:[1,0,0]\n"
"ds_write2_b32 v155, v100, v101 offset0:0 offset1:68\n"
"ds_write2_b32 v155, v102, v103 offset0:136 offset1:204\n"
"ds_read_b128 v[68:71], v141 offset:0\n"
"ds_read_b128 v[72:75], v141 offset:64\n"
"buffer_load_dwordx4 v[216:219], v2, s[4:7], s32 offen offset:3072 sc1\n"
"ds_read_b128 v[76:79], v141 offset:128\n"
"ds_read_b128 v[80:83], v141 offset:192\n"
"v_pk_mul_f16 v100, v84, v95 op_sel:[0,0] op_sel_hi:[1,0]\n"
"v_pk_mul_f16 v101, v88, v95 op_sel:[0,0] op_sel_hi:[1,0]\n"
"v_pk_fma_f16 v100, v84, v95, v100 op_sel:[1,1,0] op_sel_hi:[0,1,1] neg_lo:[1,0,0]\n"
"v_pk_fma_f16 v101, v88, v95, v101 op_sel:[1,1,0] op_sel_hi:[0,1,1] neg_lo:[1,0,0]\n"
"s_add_u32 s32, s32, 4096\n"
"buffer_load_dwordx4 v[220:223], v2, s[4:7], s32 offen offset:0 sc1\n"
"v_pk_mul_f16 v102, v84, v96 op_sel:[0,0] op_sel_hi:[1,0]\n"
"v_pk_mul_f16 v103, v88, v96 op_sel:[0,0] op_sel_hi:[1,0]\n"
"v_pk_fma_f16 v102, v84, v96, v102 op_sel:[1,1,0] op_sel_hi:[0,1,1] neg_lo:[1,0,0]\n"
"v_pk_fma_f16 v103, v88, v96, v103 op_sel:[1,1,0] op_sel_hi:[0,1,1] neg_lo:[1,0,0]\n"
"v_pk_mul_f16 v104, v84, v97 op_sel:[0,0] op_sel_hi:[1,0]\n"
"v_pk_mul_f16 v105, v88, v97 op_sel:[0,0] op_sel_hi:[1,0]\n"
"buffer_load_dwordx4 v[224:227], v2, s[4:7], s32 offen offset:1024 sc1\n"
"v_pk_fma_f16 v104, v84, v97, v104 op_sel:[1,1,0] op_sel_hi:[0,1,1] neg_lo:[1,0,0]\n"
"v_pk_fma_f16 v105, v88, v97, v105 op_sel:[1,1,0] op_sel_hi:[0,1,1] neg_lo:[1,0,0]\n"
"v_pk_mul_f16 v106, v84, v98 op_sel:[0,0] op_sel_hi:[1,0]\n"
"v_pk_mul_f16 v107, v88, v98 op_sel:[0,0] op_sel_hi:[1,0]\n"
"v_pk_fma_f16 v106, v84, v98, v106 op_sel:[1,1,0] op_sel_hi:[0,1,1] neg_lo:[1,0,0]\n"
"v_pk_fma_f16 v107, v88, v98, v107 op_sel:[1,1,0] op_sel_hi:[0,1,1] neg_lo:[1,0,0]\n"
"buffer_load_dwordx4 v[228:231], v2, s[4:7], s32 offen offset:2048 sc1\n"
"v_pk_mul_f16 v108, v84, v99 op_sel:[0,0] op_sel_hi:[1,0]\n"
"v_pk_mul_f16 v109, v88, v99 op_sel:[0,0] op_sel_hi:[1,0]\n"
"v_pk_fma_f16 v108, v84, v99, v108 op_sel:[1,1,0] op_sel_hi:[0,1,1] neg_lo:[1,0,0]\n"
"v_pk_fma_f16 v109, v88, v99, v109 op_sel:[1,1,0] op_sel_hi:[0,1,1] neg_lo:[1,0,0]\n"
"s_setprio 3\n"
"s_waitcnt lgkmcnt(3)\n"
"v_mfma_f32_16x16x32_f16 v[10:13], v[68:71], v[52:55], 0\n"
"buffer_load_dwordx4 v[232:235], v2, s[4:7], s32 offen offset:3072 sc1\n"
"ds_write2_b32 v152, v100, v101 offset0:0 offset1:68\n"
"s_waitcnt lgkmcnt(3)\n"
"v_mfma_f32_16x16x32_f16 v[10:13], v[72:75], v[56:59], v[10:13]\n"
"ds_write2_b32 v152, v102, v103 offset0:136 offset1:204\n"
"s_waitcnt lgkmcnt(3)\n"
"v_mfma_f32_16x16x32_f16 v[10:13], v[76:79], v[60:63], v[10:13]\n"
"ds_write2_b32 v153, v104, v105 offset0:0 offset1:68\n"
"s_waitcnt lgkmcnt(3)\n"
"v_mfma_f32_16x16x32_f16 v[10:13], v[80:83], v[64:67], v[10:13]\n"
"s_add_u32 s32, s32, 4096\n"
"buffer_load_dwordx4 v[236:239], v2, s[4:7], s32 offen offset:0 sc1\n"
"s_setprio 0\n"
"ds_write2_b32 v153, v106, v107 offset0:136 offset1:204\n"
"ds_write2_b32 v154, v108, v109 offset0:0 offset1:68\n"
"ds_read_b128 v[68:71], v141 offset:0\n"
"ds_read_b128 v[72:75], v141 offset:64\n"
"ds_read_b128 v[76:79], v141 offset:128\n"
"buffer_load_dwordx4 v[240:243], v2, s[4:7], s32 offen offset:1024 sc1\n"
"ds_read_b128 v[80:83], v141 offset:192\n"
"v_fmac_f32 v128, s50, v6\n"
"v_fmac_f32 v129, s50, v7\n"
"v_fmac_f32 v130, s50, v8\n"
"v_fmac_f32 v131, s50, v9\n"
"v_fmac_f32 v132, s50, v10\n"
"buffer_load_dwordx4 v[244:247], v2, s[4:7], s32 offen offset:2048 sc1\n"
"v_fmac_f32 v133, s50, v11\n"
"v_fmac_f32 v134, s50, v12\n"
"v_fmac_f32 v135, s50, v13\n"
"s_setprio 3\n"
"s_waitcnt lgkmcnt(3)\n"
"v_mfma_f32_16x16x32_f16 v[14:17], v[68:71], v[52:55], 0\n"
"s_waitcnt lgkmcnt(2)\n"
"v_mfma_f32_16x16x32_f16 v[14:17], v[72:75], v[56:59], v[14:17]\n"
"buffer_load_dwordx4 v[248:251], v2, s[4:7], s32 offen offset:3072 sc1\n"
"s_waitcnt lgkmcnt(1)\n"
"v_mfma_f32_16x16x32_f16 v[14:17], v[76:79], v[60:63], v[14:17]\n"
"s_waitcnt lgkmcnt(0)\n"
"v_mfma_f32_16x16x32_f16 v[14:17], v[80:83], v[64:67], v[14:17]\n"
"s_setprio 0\n"
"s_nop 6\n"
"v_fmac_f32 v136, s50, v14\n"
"v_fmac_f32 v137, s50, v15\n"
"v_fmac_f32 v138, s50, v16\n"
"s_add_u32 s32, s32, 4096\n"
"buffer_load_dwordx4 v[252:255], v2, s[4:7], s32 offen offset:0 sc1\n"
"v_fmac_f32 v139, s50, v17\n"
"v_and_b32 v79, 15, v0\n"
"v_and_b32 v78, 1, v79\n"
"v_lshrrev_b32 v77, 4, v0\n"
"v_lshlrev_b32 v76, 3, v77\n"
"v_lshlrev_b32 v75, 8, v77\n"
"v_lshlrev_b32 v74, 7, v78\n"
"v_add_u32 v73, v75, v74\n"
"v_lshrrev_b32 v72, 1, v79\n"
"v_lshlrev_b32 v71, 4, v72\n"
"v_add_u32 v17, v73, v71\n"
"v_mul_i32_i24 v70, 0x500, v79\n"
"v_add_u32 v14, v70, v76\n"
"s_mov_b32 s79, 0x25400\n"
"s_cmp_eq_i32 s3, 0\n"
"s_cselect_b32 s79, s79, 0x21000\n"
"s_mov_b32 s78, 0x25c00\n"
"s_cmp_eq_i32 s3, 2\n"
"s_cselect_b32 s78, s78, 0x23400\n"
"s_cmp_lt_i32 s3, 2\n"
"s_cselect_b32 s79, s79, s78\n"
"v_add_u32 v6, s79, v2\n"
"s_mov_b32 s79, 0x40000\n"
"s_cmp_eq_i32 s3, 0\n"
"s_cselect_b32 s79, s79, 0x20c00\n"
"s_mov_b32 s78, 0x40000\n"
"s_cmp_eq_i32 s3, 2\n"
"s_cselect_b32 s78, s78, 0x23000\n"
"s_cmp_lt_i32 s3, 2\n"
"s_cselect_b32 s79, s79, s78\n"
"v_add_u32 v7, s79, v2\n"
"s_mov_b32 s79, 0x23c00\n"
"s_cmp_eq_i32 s3, 0\n"
"s_cselect_b32 s79, s79, 0x1f200\n"
"s_mov_b32 s78, 0x21800\n"
"s_cmp_eq_i32 s3, 2\n"
"s_cselect_b32 s78, s78, 0x1ce00\n"
"s_cmp_lt_i32 s3, 2\n"
"s_cselect_b32 s79, s79, s78\n"
"v_add_u32 v8, s79, v2\n"
"s_mov_b32 s79, 0x20400\n"
"s_cmp_eq_i32 s3, 0\n"
"s_cselect_b32 s79, s79, 0x24c00\n"
"s_mov_b32 s78, 0x1e000\n"
"s_cmp_eq_i32 s3, 2\n"
"s_cselect_b32 s78, s78, 0x22800\n"
"s_cmp_lt_i32 s3, 2\n"
"s_cselect_b32 s79, s79, s78\n"
"v_add_u32 v9, s79, v2\n"
"s_mov_b32 s79, 0x24400\n"
"s_cmp_eq_i32 s3, 0\n"
"s_cselect_b32 s79, s79, 0x24800\n"
"s_mov_b32 s78, 0x22000\n"
"s_cmp_eq_i32 s3, 2\n"
"s_cselect_b32 s78, s78, 0x22400\n"
"s_cmp_lt_i32 s3, 2\n"
"s_cselect_b32 s79, s79, s78\n"
"v_add_u32 v10, s79, v2\n"
"s_and_b32 s79, s3, 1\n"
"s_lshl_b32 s79, s79, 30\n"
"v_add_u32 v15, s79, v14\n"
"v_lshlrev_b32 v99, 3, v79\n"
"v_mov_b32 v98, 0x40000000\n"
"v_lshrrev_b32 v97, 2, v79\n"
"v_lshl_add_u32 v97, v78, 3, v97\n"
"v_cmp_eq_i32 vcc, v97, v77\n"
"s_nop 1\n"
"v_cndmask_b32 v16, v98, v99, vcc\n"
"s_and_b32 s79, s3, 1\n"
"s_lshl_b32 s79, s79, 30\n"
"v_add_u32 v18, s79, v17\n"
"s_and_b32 s79, s3, 1\n"
"s_sub_u32 s79, s79, 1\n"
"v_mov_b32 v19, s79\n"
"v_lshrrev_b32 v99, 1, v77\n"
"v_and_b32 v98, 1, v77\n"
"v_and_b32 v97, 4, v79\n"
"v_and_b32 v96, 3, v79\n"
"v_lshrrev_b32 v95, 3, v79\n"
"v_lshlrev_b32 v95, 6, v95\n"
"v_lshl_add_u32 v96, v96, 4, v95\n"
"v_lshl_add_u32 v97, v97, 1, v96\n"
"v_lshl_add_u32 v98, v98, 8, v97\n"
"v_lshl_add_u32 v99, v99, 7, v98\n"
"v_sub_u32 v39, v99, v14\n"
"s_mov_b32 s79, 0x20c00\n"
"s_cmp_eq_i32 s3, 0\n"
"s_cselect_b32 s79, s79, 0x25400\n"
"s_mov_b32 s78, 0x23000\n"
"s_cmp_eq_i32 s3, 2\n"
"s_cselect_b32 s78, s78, 0x25c00\n"
"s_cmp_lt_i32 s3, 2\n"
"s_cselect_b32 s79, s79, s78\n"
"v_add_u32 v11, s79, v2\n"
"s_mov_b32 s79, 0x21800\n"
"s_cmp_eq_i32 s3, 0\n"
"s_cselect_b32 s79, s79, 0x1ce00\n"
"s_mov_b32 s78, 0x23c00\n"
"s_cmp_eq_i32 s3, 2\n"
"s_cselect_b32 s78, s78, 0x1f200\n"
"s_cmp_lt_i32 s3, 2\n"
"s_cselect_b32 s79, s79, s78\n"
"v_add_u32 v12, s79, v2\n"
"s_mov_b32 s79, 0x22400\n"
"s_cmp_eq_i32 s3, 0\n"
"s_cselect_b32 s79, s79, 0x1e000\n"
"s_mov_b32 s78, 0x24800\n"
"s_cmp_eq_i32 s3, 2\n"
"s_cselect_b32 s78, s78, 0x20400\n"
"s_cmp_lt_i32 s3, 2\n"
"s_cselect_b32 s79, s79, s78\n"
"v_add_u32 v13, s79, v2\n"
"v_mov_b32 v20, 0x3f803f80\n"
"v_mov_b32 v21, 0x3f803f80\n"
"v_mov_b32 v22, 0x3f803f80\n"
"v_mov_b32 v23, 0x3f803f80\n"
"s_branch .Ljk_299\n"
".Lnk_298:\n"
"v_and_b32 v79, 15, v0\n"
"v_and_b32 v78, 1, v79\n"
"v_lshrrev_b32 v77, 4, v0\n"
"v_lshlrev_b32 v76, 3, v77\n"
"v_lshlrev_b32 v75, 8, v77\n"
"v_lshlrev_b32 v74, 7, v78\n"
"v_add_u32 v73, v75, v74\n"
"v_lshrrev_b32 v72, 1, v79\n"
"v_lshlrev_b32 v71, 4, v72\n"
"v_add_u32 v17, v73, v71\n"
"v_mul_i32_i24 v70, 0x500, v79\n"
"v_add_u32 v14, v70, v76\n"
"s_mov_b32 s79, 0x25400\n"
"s_cmp_eq_i32 s3, 0\n"
"s_cselect_b32 s79, s79, 0x21000\n"
"s_mov_b32 s78, 0x25c00\n"
"s_cmp_eq_i32 s3, 2\n"
"s_cselect_b32 s78, s78, 0x23400\n"
"s_cmp_lt_i32 s3, 2\n"
"s_cselect_b32 s79, s79, s78\n"
"v_add_u32 v6, s79, v2\n"
"s_mov_b32 s79, 0x40000\n"
"s_cmp_eq_i32 s3, 0\n"
"s_cselect_b32 s79, s79, 0x20c00\n"
"s_mov_b32 s78, 0x40000\n"
"s_cmp_eq_i32 s3, 2\n"
"s_cselect_b32 s78, s78, 0x23000\n"
"s_cmp_lt_i32 s3, 2\n"
"s_cselect_b32 s79, s79, s78\n"
"v_add_u32 v7, s79, v2\n"
"s_mov_b32 s79, 0x23c00\n"
"s_cmp_eq_i32 s3, 0\n"
"s_cselect_b32 s79, s79, 0x1f200\n"
"s_mov_b32 s78, 0x21800\n"
"s_cmp_eq_i32 s3, 2\n"
"s_cselect_b32 s78, s78, 0x1ce00\n"
"s_cmp_lt_i32 s3, 2\n"
"s_cselect_b32 s79, s79, s78\n"
"v_add_u32 v8, s79, v2\n"
"s_mov_b32 s79, 0x20400\n"
"s_cmp_eq_i32 s3, 0\n"
"s_cselect_b32 s79, s79, 0x24c00\n"
"s_mov_b32 s78, 0x1e000\n"
"s_cmp_eq_i32 s3, 2\n"
"s_cselect_b32 s78, s78, 0x22800\n"
"s_cmp_lt_i32 s3, 2\n"
"s_cselect_b32 s79, s79, s78\n"
"v_add_u32 v9, s79, v2\n"
"s_mov_b32 s79, 0x24400\n"
"s_cmp_eq_i32 s3, 0\n"
"s_cselect_b32 s79, s79, 0x24800\n"
"s_mov_b32 s78, 0x22000\n"
"s_cmp_eq_i32 s3, 2\n"
"s_cselect_b32 s78, s78, 0x22400\n"
"s_cmp_lt_i32 s3, 2\n"
"s_cselect_b32 s79, s79, s78\n"
"v_add_u32 v10, s79, v2\n"
"s_and_b32 s79, s3, 1\n"
"s_lshl_b32 s79, s79, 30\n"
"v_add_u32 v15, s79, v14\n"
"v_lshlrev_b32 v99, 3, v79\n"
"v_mov_b32 v98, 0x40000000\n"
"v_lshrrev_b32 v97, 2, v79\n"
"v_lshl_add_u32 v97, v78, 3, v97\n"
"v_cmp_eq_i32 vcc, v97, v77\n"
"s_nop 1\n"
"v_cndmask_b32 v16, v98, v99, vcc\n"
"s_and_b32 s79, s3, 1\n"
"s_lshl_b32 s79, s79, 30\n"
"v_add_u32 v18, s79, v17\n"
"s_and_b32 s79, s3, 1\n"
"s_sub_u32 s79, s79, 1\n"
"v_mov_b32 v19, s79\n"
"v_lshrrev_b32 v99, 1, v77\n"
"v_and_b32 v98, 1, v77\n"
"v_and_b32 v97, 4, v79\n"
"v_and_b32 v96, 3, v79\n"
"v_lshrrev_b32 v95, 3, v79\n"
"v_lshlrev_b32 v95, 6, v95\n"
"v_lshl_add_u32 v96, v96, 4, v95\n"
"v_lshl_add_u32 v97, v97, 1, v96\n"
"v_lshl_add_u32 v98, v98, 8, v97\n"
"v_lshl_add_u32 v99, v99, 7, v98\n"
"v_sub_u32 v39, v99, v14\n"
"s_mov_b32 s79, 0x20c00\n"
"s_cmp_eq_i32 s3, 0\n"
"s_cselect_b32 s79, s79, 0x25400\n"
"s_mov_b32 s78, 0x23000\n"
"s_cmp_eq_i32 s3, 2\n"
"s_cselect_b32 s78, s78, 0x25c00\n"
"s_cmp_lt_i32 s3, 2\n"
"s_cselect_b32 s79, s79, s78\n"
"v_add_u32 v11, s79, v2\n"
"s_mov_b32 s79, 0x21800\n"
"s_cmp_eq_i32 s3, 0\n"
"s_cselect_b32 s79, s79, 0x1ce00\n"
"s_mov_b32 s78, 0x23c00\n"
"s_cmp_eq_i32 s3, 2\n"
"s_cselect_b32 s78, s78, 0x1f200\n"
"s_cmp_lt_i32 s3, 2\n"
"s_cselect_b32 s79, s79, s78\n"
"v_add_u32 v12, s79, v2\n"
"s_mov_b32 s79, 0x22400\n"
"s_cmp_eq_i32 s3, 0\n"
"s_cselect_b32 s79, s79, 0x1e000\n"
"s_mov_b32 s78, 0x24800\n"
"s_cmp_eq_i32 s3, 2\n"
"s_cselect_b32 s78, s78, 0x20400\n"
"s_cmp_lt_i32 s3, 2\n"
"s_cselect_b32 s79, s79, s78\n"
"v_add_u32 v13, s79, v2\n"
"v_mov_b32 v20, 0x3f803f80\n"
"v_mov_b32 v21, 0x3f803f80\n"
"v_mov_b32 v22, 0x3f803f80\n"
"v_mov_b32 v23, 0x3f803f80\n"
"s_mov_b32 s17, 0x100000\n"
".Ltp_306:\n"
"s_sleep 2\n"
"ds_read_b32 v3, v5 offset:16\n"
"s_sub_u32 s17, s17, 1\n"
"s_cbranch_scc1 .Lou_307\n"
"s_waitcnt lgkmcnt(0)\n"
"v_cmp_eq_u32 vcc, 0, v3\n"
"s_cbranch_vccnz .Ltp_306\n"
".Lou_307:\n"
"s_waitcnt lgkmcnt(0)\n"
"s_nop 2\n"
"s_mov_b32 s17, 32768\n"
".Ltp_308:\n"
"buffer_load_dwordx4 v[40:43], v38, s[4:7], s33 offen sc1\n"
"s_sub_u32 s17, s17, 1\n"
"s_cbranch_scc1 .Lou_309\n"
"s_waitcnt vmcnt(0)\n"
"v_xor_b32 v40, s16, v40\n"
"v_xor_b32 v41, s16, v41\n"
"v_xor_b32 v42, s16, v42\n"
"v_xor_b32 v43, s16, v43\n"
"v_or_b32 v40, v40, v41\n"
"v_or3_b32 v40, v40, v42, v43\n"
"v_cmp_ne_u32 vcc, 0, v40\n"
"s_cbranch_vccz .Lou_309\n"
"s_sleep 1\n"
"s_branch .Ltp_308\n"
".Lou_309:\n"
"s_waitcnt vmcnt(0)\n"
".Lpl_300:\n"
"s_mov_b32 s17, 0x100000\n"
".Ltp_310:\n"
"s_sleep 2\n"
"ds_read_b32 v3, v5 offset:32\n"
"s_sub_u32 s17, s17, 1\n"
"s_cbranch_scc1 .Lou_311\n"
"s_waitcnt lgkmcnt(0)\n"
"v_cmp_eq_u32 vcc, 0, v3\n"
"s_cbranch_vccnz .Ltp_310\n"
".Lou_311:\n"
"s_waitcnt lgkmcnt(0)\n"
"buffer_load_dwordx4 v[156:159], v2, s[4:7], s32 offen offset:0 sc1\n"
"buffer_load_dwordx4 v[160:163], v2, s[4:7], s32 offen offset:1024 sc1\n"
"buffer_load_dwordx4 v[164:167], v2, s[4:7], s32 offen offset:2048 sc1\n"
"buffer_load_dwordx4 v[168:171], v2, s[4:7], s32 offen offset:3072 sc1\n"
"s_add_u32 s32, s32, 4096\n"
"buffer_load_dwordx4 v[172:175], v2, s[4:7], s32 offen offset:0 sc1\n"
"buffer_load_dwordx4 v[176:179], v2, s[4:7], s32 offen offset:1024 sc1\n"
"buffer_load_dwordx4 v[180:183], v2, s[4:7], s32 offen offset:2048 sc1\n"
"buffer_load_dwordx4 v[184:187], v2, s[4:7], s32 offen offset:3072 sc1\n"
"s_add_u32 s32, s32, 4096\n"
"buffer_load_dwordx4 v[188:191], v2, s[4:7], s32 offen offset:0 sc1\n"
"buffer_load_dwordx4 v[192:195], v2, s[4:7], s32 offen offset:1024 sc1\n"
"buffer_load_dwordx4 v[196:199], v2, s[4:7], s32 offen offset:2048 sc1\n"
"buffer_load_dwordx4 v[200:203], v2, s[4:7], s32 offen offset:3072 sc1\n"
"s_add_u32 s32, s32, 4096\n"
"buffer_load_dwordx4 v[204:207], v2, s[4:7], s32 offen offset:0 sc1\n"
"buffer_load_dwordx4 v[208:211], v2, s[4:7], s32 offen offset:1024 sc1\n"
"buffer_load_dwordx4 v[212:215], v2, s[4:7], s32 offen offset:2048 sc1\n"
"buffer_load_dwordx4 v[216:219], v2, s[4:7], s32 offen offset:3072 sc1\n"
"s_add_u32 s32, s32, 4096\n"
"buffer_load_dwordx4 v[220:223], v2, s[4:7], s32 offen offset:0 sc1\n"
"buffer_load_dwordx4 v[224:227], v2, s[4:7], s32 offen offset:1024 sc1\n"
"buffer_load_dwordx4 v[228:231], v2, s[4:7], s32 offen offset:2048 sc1\n"
"buffer_load_dwordx4 v[232:235], v2, s[4:7], s32 offen offset:3072 sc1\n"
"s_add_u32 s32, s32, 4096\n"
"buffer_load_dwordx4 v[236:239], v2, s[4:7], s32 offen offset:0 sc1\n"
"buffer_load_dwordx4 v[240:243], v2, s[4:7], s32 offen offset:1024 sc1\n"
"buffer_load_dwordx4 v[244:247], v2, s[4:7], s32 offen offset:2048 sc1\n"
"buffer_load_dwordx4 v[248:251], v2, s[4:7], s32 offen offset:3072 sc1\n"
"s_add_u32 s32, s32, 4096\n"
"buffer_load_dwordx4 v[252:255], v2, s[4:7], s32 offen offset:0 sc1\n"
".Ljk_299:\n"
"s_cmp_lt_i32 s18, 0\n"
"s_cselect_b32 s43, 3, 0\n"
"s_setprio 2\n"
"v_mov_b32 v40, -1\n"
"v_mov_b32 v41, -1\n"
"v_mov_b32 v42, -1\n"
"v_mov_b32 v43, -1\n"
"s_lshl_b32 s79, s2, 2\n"
"s_add_u32 s79, s79, s3\n"
"s_lshl_b32 s79, s79, 10\n"
"s_add_u32 s49, s79, 0xb48000\n"
"buffer_store_dwordx4 v[40:43], v2, s[4:7], s49 offen sc1\n"
"s_add_u32 s49, s49, 0x100000\n"
"buffer_store_dwordx4 v[40:43], v2, s[4:7], s49 offen sc1\n"
"s_add_u32 s49, s49, 0x100000\n"
"s_sub_u32 s79, 0xdc7fff, s49\n"
"s_and_b32 s79, s79, 0x80000000\n"
"s_or_b32 s49, s49, s79\n"
"buffer_store_dwordx4 v[40:43], v2, s[4:7], s49 offen sc1\n"
"s_nop 1\n"
"s_setprio 0\n"
"s_cmp_lg_u32 s43, 0\n"
"s_cbranch_scc1 .Lsk_329\n"
"ds_read_b32 v3, v5 offset:0\n"
"s_waitcnt lgkmcnt(0)\n"
"v_cmp_eq_u32 vcc, 0, v3\n"
"s_cbranch_vccz .Len_328\n"
"s_call_b64 s[46:47], .Lst_312\n"
".Lsk_329:\n"
"s_cmp_lg_u32 s43, 1\n"
"s_cbranch_scc1 .Lsk_330\n"
"ds_read_b32 v3, v5 offset:128\n"
"s_waitcnt lgkmcnt(0)\n"
"v_cmp_eq_u32 vcc, 0, v3\n"
"s_cbranch_vccz .Len_328\n"
"s_call_b64 s[46:47], .Lst_313\n"
".Lsk_330:\n"
"s_cmp_lg_u32 s43, 2\n"
"s_cbranch_scc1 .Lsk_331\n"
"ds_read_b32 v3, v5 offset:128\n"
"s_waitcnt lgkmcnt(0)\n"
"v_cmp_eq_u32 vcc, 0, v3\n"
"s_cbranch_vccz .Len_328\n"
"s_call_b64 s[46:47], .Lst_314\n"
".Lsk_331:\n"
".Len_328:\n"
"s_setprio 2\n"
"s_mul_i32 s79, s28, 0xa0\n"
"s_add_u32 s79, s30, s79\n"
"s_and_b32 s78, s3, 1\n"
"s_mul_i32 s78, s78, 0x60\n"
"s_add_u32 s36, s79, s78\n"
"s_mul_i32 s79, s3, 0x1400\n"
"s_add_u32 s79, s30, s79\n"
"s_add_u32 s32, s79, 0x368000\n"
"s_add_u32 s33, s32, 0x1000\n"
"s_lshl_b32 s79, s29, 1\n"
"s_lshr_b32 s78, s3, 1\n"
"s_xor_b32 s78, s78, 1\n"
"s_add_u32 s79, s79, s78\n"
"s_mul_i32 s79, s79, 0x5000\n"
"s_mul_i32 s78, s3, 0x1400\n"
"s_add_u32 s79, s79, s78\n"
"s_add_u32 s34, s79, 0x368000\n"
"s_add_u32 s35, s34, 0x1000\n"
"s_lshl_b32 s79, s30, 1\n"
"s_mul_i32 s78, s28, 0x1400\n"
"s_add_u32 s79, s79, s78\n"
"s_and_b32 s78, s3, 1\n"
"s_mul_i32 s78, s78, 0xc00\n"
"s_add_u32 s79, s79, s78\n"
"s_add_u32 s37, s79, 0x5e8000\n"
"s_lshl_b32 s79, s29, 1\n"
"s_lshr_b32 s78, s3, 1\n"
"s_add_u32 s79, s79, s78\n"
"s_lshl_b32 s79, s79, 11\n"
"s_lshl_b32 s78, s28, 8\n"
"s_add_u32 s79, s79, s78\n"
"s_and_b32 s78, s3, 1\n"
"s_lshl_b32 s78, s78, 7\n"
"s_add_u32 s79, s79, s78\n"
"s_add_u32 s38, s79, 0xae8000\n"
"buffer_load_dwordx2 v[40:41], v14, s[8:11], s36 offen offset:0\n"
"buffer_load_dwordx2 v[42:43], v14, s[8:11], s36 offen offset:32\n"
"buffer_load_dwordx2 v[44:45], v15, s[8:11], s36 offen offset:64\n"
"s_mov_b32 s17, 0x100000\n"
".Ltp_332:\n"
"s_sleep 2\n"
"ds_read_b32 v3, v5 offset:96\n"
"s_sub_u32 s17, s17, 1\n"
"s_cbranch_scc1 .Lou_333\n"
"s_waitcnt lgkmcnt(0)\n"
"v_cmp_eq_u32 vcc, 0, v3\n"
"s_cbranch_vccnz .Ltp_332\n"
".Lou_333:\n"
"s_waitcnt lgkmcnt(0)\n"
"s_cmp_eq_u32 s17, 0xfffff\n"
"s_cbranch_scc1 .Lsk_334\n"
"s_mov_b32 s89, 2\n"
".Ldl_336:\n"
"s_sub_u32 s89, s89, 1\n"
"s_cbranch_scc1 .Lde_337\n"
"s_sleep 1\n"
"s_branch .Ldl_336\n"
".Lde_337:\n"
".Lsk_334:\n"
"s_waitcnt vmcnt(0)\n"
"s_mov_b32 s17, 32768\n"
".Ltp_338:\n"
"buffer_load_dwordx4 v[88:91], v2, s[4:7], s48 offen sc1\n"
"s_sub_u32 s17, s17, 1\n"
"s_cbranch_scc1 .Lou_339\n"
"s_waitcnt vmcnt(0)\n"
"v_xor_b32 v88, s16, v88\n"
"v_xor_b32 v89, s16, v89\n"
"v_xor_b32 v90, s16, v90\n"
"v_xor_b32 v91, s16, v91\n"
"v_or_b32 v88, v88, v89\n"
"v_or3_b32 v88, v88, v90, v91\n"
"v_cmp_ne_u32 vcc, 0, v88\n"
"s_cbranch_vccz .Lou_339\n"
"s_mov_b32 s89, 6\n"
".Ldl_340:\n"
"s_sub_u32 s89, s89, 1\n"
"s_cbranch_scc1 .Lde_341\n"
"s_sleep 1\n"
"s_branch .Ldl_340\n"
".Lde_341:\n"
"s_branch .Ltp_338\n"
".Lou_339:\n"
"s_waitcnt vmcnt(0)\n"
"v_lshlrev_b32 v48, 16, v40\n"
"v_and_b32 v49, 0xffff0000, v40\n"
"v_lshlrev_b32 v50, 16, v41\n"
"v_and_b32 v51, 0xffff0000, v41\n"
"v_lshlrev_b32 v52, 16, v42\n"
"v_and_b32 v53, 0xffff0000, v42\n"
"v_lshlrev_b32 v54, 16, v43\n"
"v_and_b32 v55, 0xffff0000, v43\n"
"v_lshlrev_b32 v56, 16, v44\n"
"v_and_b32 v57, 0xffff0000, v44\n"
"v_lshlrev_b32 v58, 16, v45\n"
"v_and_b32 v59, 0xffff0000, v45\n"
"v_mov_b32 v46, 0\n"
"v_mov_b32 v47, 0\n"
"s_mov_b32 s17, 0x100000\n"
".Ltp_342:\n"
"s_sleep 1\n"
"ds_read_b32 v3, v5 offset:128\n"
"s_sub_u32 s17, s17, 1\n"
"s_cbranch_scc1 .Lou_343\n"
"s_waitcnt lgkmcnt(0)\n"
"v_cmp_eq_u32 vcc, 0, v3\n"
"s_cbranch_vccnz .Ltp_342\n"
".Lou_343:\n"
"s_waitcnt lgkmcnt(0)\n"
"s_cmp_eq_u32 s17, 0xfffff\n"
"s_cbranch_scc1 .Lsk_344\n"
"s_mov_b32 s89, 18\n"
".Ldl_346:\n"
"s_sub_u32 s89, s89, 1\n"
"s_cbranch_scc1 .Lde_347\n"
"s_sleep 1\n"
"s_branch .Ldl_346\n"
".Lde_347:\n"
"s_branch .Ljn_345\n"
".Lsk_344:\n"
".Ljn_345:\n"
"buffer_load_dwordx4 v[108:111], v2, s[4:7], s34 offen offset:0 sc1\n"
"s_nop 9\n"
"buffer_load_dwordx4 v[112:115], v2, s[4:7], s34 offen offset:1024 sc1\n"
"s_nop 9\n"
"buffer_load_dwordx4 v[116:119], v2, s[4:7], s34 offen offset:2048 sc1\n"
"s_nop 9\n"
"buffer_load_dwordx4 v[120:123], v2, s[4:7], s34 offen offset:3072 sc1\n"
"s_nop 9\n"
"buffer_load_dwordx4 v[124:127], v2, s[4:7], s35 offen offset:0 sc1\n"
"s_nop 9\n"
"buffer_load_dwordx4 v[88:91], v2, s[4:7], s32 offen offset:0 sc1\n"
"s_nop 9\n"
"buffer_load_dwordx4 v[92:95], v2, s[4:7], s32 offen offset:1024 sc1\n"
"s_nop 9\n"
"buffer_load_dwordx4 v[96:99], v2, s[4:7], s32 offen offset:2048 sc1\n"
"s_nop 9\n"
"buffer_load_dwordx4 v[100:103], v2, s[4:7], s32 offen offset:3072 sc1\n"
"s_nop 9\n"
"buffer_load_dwordx4 v[104:107], v2, s[4:7], s33 offen offset:0 sc1\n"
"s_waitcnt vmcnt(9)\n"
"v_max_u32 v38, v108, v109\n"
"v_max3_u32 v38, v110, v111, v38\n"
"v_cmp_eq_u32 vcc, -1, v38\n"
"s_cbranch_vccnz .Lsl_348\n"
".Lbk_349:\n"
"v_mfma_f32_16x16x32_bf16 v[68:71], v[156:159], v[108:111], 0\n"
"v_mfma_f32_16x16x32_bf16 v[72:75], v[160:163], v[108:111], 0\n"
"v_mfma_f32_16x16x32_bf16 v[76:79], v[164:167], v[108:111], 0\n"
"s_waitcnt vmcnt(8)\n"
"v_max_u32 v38, v112, v113\n"
"v_max3_u32 v38, v114, v115, v38\n"
"v_mfma_f32_16x16x32_bf16 v[80:83], v[168:171], v[108:111], 0\n"
"v_cmp_eq_u32 vcc, -1, v38\n"
"v_mfma_f32_16x16x32_bf16 v[84:87], v[172:175], v[108:111], 0\n"
"s_cbranch_vccnz .Lsl_350\n"
".Lbk_351:\n"
"v_mfma_f32_16x16x32_bf16 v[68:71], v[176:179], v[112:115], v[68:71]\n"
"v_mfma_f32_16x16x32_bf16 v[72:75], v[180:183], v[112:115], v[72:75]\n"
"v_mfma_f32_16x16x32_bf16 v[76:79], v[184:187], v[112:115], v[76:79]\n"
"s_waitcnt vmcnt(7)\n"
"v_max_u32 v38, v116, v117\n"
"v_max3_u32 v38, v118, v119, v38\n"
"v_mfma_f32_16x16x32_bf16 v[80:83], v[188:191], v[112:115], v[80:83]\n"
"v_cmp_eq_u32 vcc, -1, v38\n"
"v_mfma_f32_16x16x32_bf16 v[84:87], v[192:195], v[112:115], v[84:87]\n"
"s_cbranch_vccnz .Lsl_352\n"
".Lbk_353:\n"
"v_mfma_f32_16x16x32_bf16 v[68:71], v[196:199], v[116:119], v[68:71]\n"
"v_mfma_f32_16x16x32_bf16 v[72:75], v[200:203], v[116:119], v[72:75]\n"
"v_mfma_f32_16x16x32_bf16 v[76:79], v[204:207], v[116:119], v[76:79]\n"
"s_waitcnt vmcnt(6)\n"
"v_max_u32 v38, v120, v121\n"
"v_max3_u32 v38, v122, v123, v38\n"
"v_mfma_f32_16x16x32_bf16 v[80:83], v[208:211], v[116:119], v[80:83]\n"
"v_cmp_eq_u32 vcc, -1, v38\n"
"v_mfma_f32_16x16x32_bf16 v[84:87], v[212:215], v[116:119], v[84:87]\n"
"s_cbranch_vccnz .Lsl_354\n"
".Lbk_355:\n"
"v_mfma_f32_16x16x32_bf16 v[68:71], v[216:219], v[120:123], v[68:71]\n"
"v_mfma_f32_16x16x32_bf16 v[72:75], v[220:223], v[120:123], v[72:75]\n"
"v_mfma_f32_16x16x32_bf16 v[76:79], v[224:227], v[120:123], v[76:79]\n"
"s_waitcnt vmcnt(5)\n"
"v_max_u32 v38, v124, v125\n"
"v_max3_u32 v38, v126, v127, v38\n"
"v_mfma_f32_16x16x32_bf16 v[80:83], v[228:231], v[120:123], v[80:83]\n"
"v_cmp_eq_u32 vcc, -1, v38\n"
"v_mfma_f32_16x16x32_bf16 v[84:87], v[232:235], v[120:123], v[84:87]\n"
"s_cbranch_vccnz .Lsl_356\n"
".Lbk_357:\n"
"v_mfma_f32_16x16x32_bf16 v[68:71], v[236:239], v[124:127], v[68:71]\n"
"v_mfma_f32_16x16x32_bf16 v[72:75], v[240:243], v[124:127], v[72:75]\n"
"v_mfma_f32_16x16x32_bf16 v[76:79], v[244:247], v[124:127], v[76:79]\n"
"s_waitcnt vmcnt(4)\n"
"v_max_u32 v38, v88, v89\n"
"v_max3_u32 v38, v90, v91, v38\n"
"v_mfma_f32_16x16x32_bf16 v[80:83], v[248:251], v[124:127], v[80:83]\n"
"v_cmp_eq_u32 vcc, -1, v38\n"
"v_mfma_f32_16x16x32_bf16 v[84:87], v[252:255], v[124:127], v[84:87]\n"
"ds_write_b128 v8, v[68:71] offset:0\n"
"s_cbranch_vccnz .Lsl_358\n"
".Lbk_359:\n"
"v_mfma_f32_16x16x32_bf16 v[56:59], v[164:167], v[88:91], v[56:59]\n"
"s_waitcnt vmcnt(3)\n"
"v_max_u32 v38, v92, v93\n"
"v_max3_u32 v38, v94, v95, v38\n"
"ds_write_b128 v8, v[72:75] offset:1024\n"
"v_mfma_f32_16x16x32_bf16 v[60:63], v[168:171], v[88:91], 0\n"
"v_cmp_eq_u32 vcc, -1, v38\n"
"ds_write_b128 v10, v[76:79] offset:0\n"
"v_mfma_f32_16x16x32_bf16 v[64:67], v[172:175], v[88:91], 0\n"
"v_mov_b32 v117, s16\n"
"s_cbranch_vccnz .Lsl_360\n"
".Lbk_361:\n"
"v_mfma_f32_16x16x32_bf16 v[56:59], v[184:187], v[92:95], v[56:59]\n"
"s_waitcnt vmcnt(2)\n"
"v_max_u32 v38, v96, v97\n"
"v_max3_u32 v38, v98, v99, v38\n"
"v_mov_b32 v119, s16\n"
"v_mfma_f32_16x16x32_bf16 v[60:63], v[188:191], v[92:95], v[60:63]\n"
"v_cmp_eq_u32 vcc, -1, v38\n"
"ds_write_b128 v9, v[80:83] offset:0\n"
"v_mfma_f32_16x16x32_bf16 v[64:67], v[192:195], v[92:95], v[64:67]\n"
"ds_write_b128 v9, v[84:87] offset:1024\n"
"s_cbranch_vccnz .Lsl_362\n"
".Lbk_363:\n"
"v_mfma_f32_16x16x32_bf16 v[56:59], v[204:207], v[96:99], v[56:59]\n"
"s_waitcnt vmcnt(1)\n"
"v_max_u32 v38, v100, v101\n"
"v_max3_u32 v38, v102, v103, v38\n"
"ds_write_b32 v4, v4 offset:608\n"
"v_mfma_f32_16x16x32_bf16 v[60:63], v[208:211], v[96:99], v[60:63]\n"
"v_cmp_eq_u32 vcc, -1, v38\n"
"v_mov_b32 v121, s16\n"
"v_mov_b32 v123, s16\n"
"v_mfma_f32_16x16x32_bf16 v[64:67], v[212:215], v[96:99], v[64:67]\n"
"v_mov_b32 v125, s16\n"
"s_cbranch_vccnz .Lsl_364\n"
".Lbk_365:\n"
"v_mfma_f32_16x16x32_bf16 v[56:59], v[224:227], v[100:103], v[56:59]\n"
"s_waitcnt vmcnt(0)\n"
"v_max_u32 v38, v104, v105\n"
"v_max3_u32 v38, v106, v107, v38\n"
"v_mov_b32 v127, s16\n"
"v_mfma_f32_16x16x32_bf16 v[60:63], v[228:231], v[100:103], v[60:63]\n"
"v_cmp_eq_u32 vcc, -1, v38\n"
"ds_read_b32 v3, v5 offset:608\n"
"v_mfma_f32_16x16x32_bf16 v[64:67], v[232:235], v[100:103], v[64:67]\n"
"s_cbranch_vccnz .Lsl_366\n"
".Lbk_367:\n"
"v_mfma_f32_16x16x32_bf16 v[56:59], v[244:247], v[104:107], v[56:59]\n"
"ds_read_b128 v[76:79], v12 offset:2048\n"
"ds_read_b128 v[108:111], v13 offset:2048\n"
"v_mfma_f32_16x16x32_bf16 v[60:63], v[248:251], v[104:107], v[60:63]\n"
"ds_read_b128 v[68:71], v12 offset:0\n"
"ds_read_b128 v[80:83], v13 offset:0\n"
"v_mfma_f32_16x16x32_bf16 v[64:67], v[252:255], v[104:107], v[64:67]\n"
"ds_read_b128 v[72:75], v12 offset:1024\n"
"ds_read_b128 v[84:87], v13 offset:1024\n"
"v_mfma_f32_16x16x32_bf16 v[48:51], v[156:159], v[88:91], v[48:51]\n"
"ds_write_b128 v7, v[56:59] offset:0\n"
"v_mfma_f32_16x16x32_bf16 v[52:55], v[160:163], v[88:91], v[52:55]\n"
"ds_write_b128 v6, v[60:63] offset:0\n"
"v_mfma_f32_16x16x32_bf16 v[48:51], v[176:179], v[92:95], v[48:51]\n"
"ds_write_b128 v6, v[64:67] offset:1024\n"
"v_mfma_f32_16x16x32_bf16 v[52:55], v[180:183], v[92:95], v[52:55]\n"
"ds_write_b32 v4, v4 offset:624\n"
"v_mfma_f32_16x16x32_bf16 v[48:51], v[196:199], v[96:99], v[48:51]\n"
"s_mov_b32 s17, 0x100000\n"
"s_waitcnt lgkmcnt(10)\n"
"v_cmp_eq_u32 vcc, 0, v3\n"
"s_cbranch_vccz .Lok_369\n"
".Ltp_368:\n"
"ds_read_b32 v3, v5 offset:608\n"
"ds_read_b128 v[76:79], v12 offset:2048\n"
"ds_read_b128 v[108:111], v13 offset:2048\n"
"ds_read_b128 v[68:71], v12 offset:0\n"
"ds_read_b128 v[80:83], v13 offset:0\n"
"ds_read_b128 v[72:75], v12 offset:1024\n"
"ds_read_b128 v[84:87], v13 offset:1024\n"
"s_sub_u32 s17, s17, 1\n"
"s_cbranch_scc1 .Lok_369\n"
"s_waitcnt lgkmcnt(6)\n"
"v_cmp_eq_u32 vcc, 0, v3\n"
"s_cbranch_vccnz .Ltp_368\n"
"s_waitcnt lgkmcnt(0)\n"
".Lok_369:\n"
"v_mfma_f32_16x16x32_bf16 v[52:55], v[200:203], v[96:99], v[52:55]\n"
"s_waitcnt lgkmcnt(8)\n"
"v_add_f32 v76, v76, v108\n"
"v_add_f32 v77, v77, v109\n"
"v_add_f32 v78, v78, v110\n"
"v_mfma_f32_16x16x32_bf16 v[48:51], v[216:219], v[100:103], v[48:51]\n"
"v_add_f32 v79, v79, v111\n"
"s_waitcnt lgkmcnt(6)\n"
"v_add_f32 v68, v68, v80\n"
"v_add_f32 v69, v69, v81\n"
"v_mfma_f32_16x16x32_bf16 v[52:55], v[220:223], v[100:103], v[52:55]\n"
"v_add_f32 v70, v70, v82\n"
"v_add_f32 v71, v71, v83\n"
"s_waitcnt lgkmcnt(4)\n"
"v_add_f32 v72, v72, v84\n"
"v_mfma_f32_16x16x32_bf16 v[48:51], v[236:239], v[104:107], v[48:51]\n"
"v_add_f32 v73, v73, v85\n"
"v_add_f32 v74, v74, v86\n"
"v_add_f32 v75, v75, v87\n"
"v_mfma_f32_16x16x32_bf16 v[52:55], v[240:243], v[104:107], v[52:55]\n"
"ds_read_b32 v3, v5 offset:624\n"
"ds_read_b128 v[112:115], v11 offset:2048\n"
"ds_read_b128 v[60:63], v11 offset:0\n"
"ds_read_b128 v[64:67], v11 offset:1024\n"
"v_add_f32 v56, v56, v76\n"
"v_add_f32 v57, v57, v77\n"
"v_add_f32 v58, v58, v78\n"
"v_add_f32 v59, v59, v79\n"
"v_add_f32 v48, v48, v68\n"
"v_add_f32 v49, v49, v69\n"
"v_add_f32 v50, v50, v70\n"
"v_add_f32 v51, v51, v71\n"
"v_add_f32 v52, v52, v72\n"
"v_add_f32 v53, v53, v73\n"
"v_add_f32 v54, v54, v74\n"
"v_add_f32 v55, v55, v75\n"
"s_mov_b32 s17, 0x100000\n"
"s_waitcnt lgkmcnt(3)\n"
"v_cmp_eq_u32 vcc, 0, v3\n"
"s_cbranch_vccz .Lok_371\n"
".Ltp_370:\n"
"ds_read_b32 v3, v5 offset:624\n"
"ds_read_b128 v[112:115], v11 offset:2048\n"
"ds_read_b128 v[60:63], v11 offset:0\n"
"ds_read_b128 v[64:67], v11 offset:1024\n"
"s_sub_u32 s17, s17, 1\n"
"s_cbranch_scc1 .Lok_371\n"
"s_waitcnt lgkmcnt(3)\n"
"v_cmp_eq_u32 vcc, 0, v3\n"
"s_cbranch_vccnz .Ltp_370\n"
"s_waitcnt lgkmcnt(0)\n"
".Lok_371:\n"
"s_waitcnt lgkmcnt(2)\n"
"v_add_f32 v56, v56, v112\n"
"v_add_f32 v57, v57, v113\n"
"v_add_f32 v58, v58, v114\n"
"v_add_f32 v59, v59, v115\n"
"v_cvt_pk_bf16_f32 v44, v56, v57\n"
"v_cvt_pk_bf16_f32 v45, v58, v59\n"
"v_and_b32 v44, v19, v44\n"
"v_and_b32 v45, v19, v45\n"
"s_waitcnt lgkmcnt(1)\n"
"v_add_f32 v48, v48, v60\n"
"v_add_f32 v49, v49, v61\n"
"v_add_f32 v50, v50, v62\n"
"v_add_f32 v51, v51, v63\n"
"v_cvt_pk_bf16_f32 v40, v48, v49\n"
"v_cvt_pk_bf16_f32 v41, v50, v51\n"
"v_mfma_f32_16x16x32_bf16 v[88:91], v[44:47], v[44:47], 0\n"
"v_mfma_f32_16x16x32_bf16 v[92:95], v[20:23], v[44:47], 0\n"
"s_waitcnt lgkmcnt(0)\n"
"v_add_f32 v52, v52, v64\n"
"v_add_f32 v53, v53, v65\n"
"v_add_f32 v54, v54, v66\n"
"v_add_f32 v55, v55, v67\n"
"v_cvt_pk_bf16_f32 v42, v52, v53\n"
"v_cvt_pk_bf16_f32 v43, v54, v55\n"
"v_cvt_pk_bf16_f32 v116, v48, v49\n"
"v_cvt_pk_bf16_f32 v118, v50, v51\n"
"v_mfma_f32_16x16x32_bf16 v[88:91], v[40:43], v[40:43], v[88:91]\n"
"v_mfma_f32_16x16x32_bf16 v[92:95], v[20:23], v[40:43], v[92:95]\n"
"v_cvt_pk_bf16_f32 v120, v52, v53\n"
"v_cvt_pk_bf16_f32 v122, v54, v55\n"
"v_cvt_pk_bf16_f32 v124, v56, v57\n"
"v_cvt_pk_bf16_f32 v126, v58, v59\n"
"s_nop 0\n"
"v_cndmask_b32 v101, v88, v90, s[44:45]\n"
"v_cndmask_b32 v88, v89, v91, s[44:45]\n"
"s_nop 0\n"
"v_min_u32 v100, -2, v92\n"
"v_min_u32 v88, -2, v88\n"
"v_min_u32 v101, -2, v101\n"
"v_mov_b32_dpp v102, v100 quad_perm:[1,0,3,2] row_mask:0xf bank_mask:0xf\n"
"v_mov_b32_dpp v103, v88 quad_perm:[1,0,3,2] row_mask:0xf bank_mask:0xf\n"
"buffer_store_dwordx4 v[100:103], v16, s[4:7], s38 offen sc1\n"
"buffer_store_dwordx4 v[116:119], v17, s[4:7], s37 offen offset:0 sc1\n"
"buffer_store_dwordx4 v[120:123], v17, s[4:7], s37 offen offset:1024 sc1\n"
"buffer_store_dwordx4 v[124:127], v18, s[4:7], s37 offen offset:2048 sc1\n"
"ds_write_b32 v4, v4 offset:176\n"
"s_xor_b32 s79, s3, 1\n"
"s_lshl_b32 s79, s79, 8\n"
"s_add_u32 s32, s19, s79\n"
"s_cmp_ge_u32 s32, 64\n"
"s_cbranch_scc1 .Lnr_372\n"
"s_lshl_b32 s79, s19, 11\n"
"s_add_u32 s32, s79, 0xae8000\n"
"s_lshl_b32 s79, s19, 7\n"
"s_add_u32 s33, s79, 0xb28000\n"
"v_lshrrev_b32 v99, 3, v0\n"
"v_and_b32 v98, 7, v0\n"
"v_lshlrev_b32 v98, 4, v98\n"
"v_lshl_add_u32 v40, v99, 14, v98\n"
"v_mov_b32 v41, 0x3727c5ac\n"
"s_mov_b32 s89, 15\n"
".Ldl_373:\n"
"s_sub_u32 s89, s89, 1\n"
"s_cbranch_scc1 .Lde_374\n"
"s_sleep 1\n"
"s_branch .Ldl_373\n"
".Lde_374:\n"
"buffer_load_dwordx4 v[48:51], v2, s[4:7], s32 offen offset:0 sc1\n"
"buffer_load_dwordx4 v[52:55], v2, s[4:7], s32 offen offset:1024 sc1\n"
"s_mov_b32 s17, 32768\n"
".Ltp_375:\n"
"s_waitcnt vmcnt(0)\n"
"s_sub_u32 s17, s17, 1\n"
"s_cbranch_scc1 .Lou_376\n"
"v_max_u32 v38, v48, v49\n"
"v_max3_u32 v38, v50, v51, v38\n"
"v_max3_u32 v38, v52, v53, v38\n"
"v_max3_u32 v38, v54, v55, v38\n"
"v_cmp_eq_u32 vcc, -1, v38\n"
"s_cbranch_vccz .Lou_376\n"
"v_max_u32 v38, v50, v51\n"
"v_max3_u32 v38, v48, v49, v38\n"
"v_cmp_eq_u32 vcc, -1, v38\n"
"s_cbranch_vccz .Lsk_377\n"
"s_mov_b64 exec, vcc\n"
"buffer_load_dwordx4 v[48:51], v2, s[4:7], s32 offen offset:0 sc1\n"
"s_mov_b64 exec, -1\n"
".Lsk_377:\n"
"v_max_u32 v38, v54, v55\n"
"v_max3_u32 v38, v52, v53, v38\n"
"v_cmp_eq_u32 vcc, -1, v38\n"
"s_cbranch_vccz .Lsk_378\n"
"s_mov_b64 exec, vcc\n"
"buffer_load_dwordx4 v[52:55], v2, s[4:7], s32 offen offset:1024 sc1\n"
"s_mov_b64 exec, -1\n"
".Lsk_378:\n"
"s_branch .Ltp_375\n"
".Lou_376:\n"
"s_waitcnt vmcnt(0)\n"
"v_add_f32 v48, v48, v52\n"
"v_add_f32 v49, v49, v53\n"
"v_add_f32 v50, v50, v54\n"
"v_add_f32 v51, v51, v55\n"
"v_add_f32_dpp v52, v48, v48 row_ror:8 row_mask:0xf bank_mask:0xf\n"
"v_add_f32_dpp v53, v49, v49 row_ror:8 row_mask:0xf bank_mask:0xf\n"
"v_add_f32_dpp v54, v50, v50 row_ror:8 row_mask:0xf bank_mask:0xf\n"
"v_add_f32_dpp v55, v51, v51 row_ror:8 row_mask:0xf bank_mask:0xf\n"
"v_mov_b32 v48, v52\n"
"v_mov_b32 v49, v53\n"
"v_mov_b32 v50, v54\n"
"v_mov_b32 v51, v55\n"
"v_permlane16_swap_b32 v48, v52\n"
"v_permlane16_swap_b32 v49, v53\n"
"v_permlane16_swap_b32 v50, v54\n"
"v_permlane16_swap_b32 v51, v55\n"
"v_add_f32 v52, v48, v52\n"
"v_add_f32 v53, v49, v53\n"
"v_add_f32 v54, v50, v54\n"
"v_add_f32 v55, v51, v55\n"
"v_mov_b32 v48, v52\n"
"v_mov_b32 v49, v53\n"
"v_mov_b32 v50, v54\n"
"v_mov_b32 v51, v55\n"
"v_permlane32_swap_b32 v48, v52\n"
"v_permlane32_swap_b32 v49, v53\n"
"v_permlane32_swap_b32 v50, v54\n"
"v_permlane32_swap_b32 v51, v55\n"
"v_add_f32 v52, v48, v52\n"
"v_add_f32 v53, v49, v53\n"
"v_add_f32 v54, v50, v54\n"
"v_add_f32 v55, v51, v55\n"
"v_mul_f32 v48, 0x3acccccd, v52\n"
"v_mul_f32 v50, 0x3acccccd, v54\n"
"v_fma_f32 v53, -v52, v48, v53\n"
"v_fma_f32 v55, -v54, v50, v55\n"
"v_fmamk_f32 v53, v53, 0x3acccccd, v41\n"
"v_fmamk_f32 v55, v55, 0x3acccccd, v41\n"
"v_max_f32 v53, v53, v41\n"
"v_max_f32 v55, v55, v41\n"
"v_rsq_f32 v49, v53\n"
"v_rsq_f32 v51, v55\n"
"v_mul_f32 v48, -v48, v49\n"
"v_mul_f32 v50, -v50, v51\n"
"v_min_u32 v49, -2, v49\n"
"v_min_u32 v51, -2, v51\n"
"v_min_u32 v48, -2, v48\n"
"v_min_u32 v50, -2, v50\n"
"buffer_store_dwordx4 v[48:51], v40, s[4:7], s33 offen sc1\n"
"s_nop 3\n"
".Lnr_372:\n"
"v_lshlrev_b32 v99, 4, v0\n"
"v_mov_b32 v98, 0x40000000\n"
"v_cmp_gt_i32 vcc, 16, v0\n"
"s_nop 1\n"
"v_cndmask_b32 v43, v98, v99, vcc\n"
"v_mov_b32 v44, -1\n"
"v_mov_b32 v45, -1\n"
"v_mov_b32 v46, -1\n"
"v_mov_b32 v47, -1\n"
"s_add_u32 s49, s31, 0xa60000\n"
"buffer_store_dwordx4 v[44:47], v2, s[4:7], s49 offen offset:0 sc1\n"
"buffer_store_dwordx4 v[44:47], v43, s[4:7], s49 offen offset:1024 sc1\n"
"s_add_u32 s49, s31, 0xba0000\n"
"buffer_store_dwordx4 v[44:47], v2, s[4:7], s49 offen offset:0 sc1\n"
"buffer_store_dwordx4 v[44:47], v43, s[4:7], s49 offen offset:1024 sc1\n"
"s_lshl_b32 s79, s2, 2\n"
"s_add_u32 s79, s79, s3\n"
"s_lshl_b32 s79, s79, 10\n"
"s_add_u32 s49, s79, 0x1548000\n"
"s_sub_u32 s79, 0x15a7fff, s49\n"
"s_and_b32 s79, s79, 0x80000000\n"
"s_or_b32 s49, s49, s79\n"
"buffer_store_dwordx4 v[44:47], v2, s[4:7], s49 offen sc1\n"
"s_nop 1\n"
"s_setprio 0\n"
"s_cmp_lg_u32 s43, 0\n"
"s_cbranch_scc1 .Lsk_380\n"
"s_call_b64 s[46:47], .Lst_312\n"
".Lsk_380:\n"
"s_cmp_lg_u32 s43, 1\n"
"s_cbranch_scc1 .Lsk_381\n"
"s_call_b64 s[46:47], .Lst_313\n"
".Lsk_381:\n"
"s_cmp_lg_u32 s43, 2\n"
"s_cbranch_scc1 .Lsk_382\n"
"ds_read_b32 v3, v5 offset:144\n"
"s_waitcnt lgkmcnt(0)\n"
"v_cmp_eq_u32 vcc, 0, v3\n"
"s_cbranch_vccz .Len_379\n"
"s_call_b64 s[46:47], .Lst_314\n"
".Lsk_382:\n"
".Len_379:\n"
"s_setprio 2\n"
"s_mul_i32 s79, s28, 0xa0\n"
"s_add_u32 s79, s30, s79\n"
"s_and_b32 s78, s3, 1\n"
"s_mul_i32 s78, s78, 0x60\n"
"s_add_u32 s79, s79, s78\n"
"s_add_u32 s36, s79, 0x140000\n"
"s_mul_i32 s79, s3, 0x1400\n"
"s_add_u32 s79, s30, s79\n"
"s_add_u32 s32, s79, 0x4a8000\n"
"s_add_u32 s33, s32, 0x1000\n"
"s_lshl_b32 s79, s29, 1\n"
"s_lshr_b32 s78, s3, 1\n"
"s_xor_b32 s78, s78, 1\n"
"s_add_u32 s79, s79, s78\n"
"s_mul_i32 s79, s79, 0x5000\n"
"s_mul_i32 s78, s3, 0x1400\n"
"s_add_u32 s79, s79, s78\n"
"s_add_u32 s34, s79, 0x4a8000\n"
"s_add_u32 s35, s34, 0x1000\n"
"s_lshl_b32 s79, s30, 1\n"
"s_mul_i32 s78, s28, 0x1400\n"
"s_add_u32 s79, s79, s78\n"
"s_and_b32 s78, s3, 1\n"
"s_mul_i32 s78, s78, 0xc00\n"
"s_add_u32 s79, s79, s78\n"
"s_add_u32 s37, s79, 0x868000\n"
"s_lshl_b32 s79, s29, 1\n"
"s_lshr_b32 s78, s3, 1\n"
"s_add_u32 s79, s79, s78\n"
"s_lshl_b32 s79, s79, 11\n"
"s_lshl_b32 s78, s28, 8\n"
"s_add_u32 s79, s79, s78\n"
"s_and_b32 s78, s3, 1\n"
"s_lshl_b32 s78, s78, 7\n"
"s_add_u32 s79, s79, s78\n"
"s_add_u32 s38, s79, 0xb08000\n"
"buffer_load_dwordx2 v[40:41], v14, s[8:11], s36 offen offset:0\n"
"buffer_load_dwordx2 v[42:43], v14, s[8:11], s36 offen offset:32\n"
"buffer_load_dwordx2 v[44:45], v15, s[8:11], s36 offen offset:64\n"
"s_mov_b32 s17, 0x100000\n"
".Ltp_383:\n"
"ds_read_b32 v3, v5 offset:176\n"
"s_sub_u32 s17, s17, 1\n"
"s_cbranch_scc1 .Lou_384\n"
"s_waitcnt lgkmcnt(0)\n"
"v_cmp_eq_u32 vcc, 0, v3\n"
"s_cbranch_vccnz .Ltp_383\n"
".Lou_384:\n"
"s_waitcnt lgkmcnt(0)\n"
"s_waitcnt vmcnt(2)\n"
"v_lshlrev_b32 v48, 16, v40\n"
"v_and_b32 v49, 0xffff0000, v40\n"
"v_lshlrev_b32 v50, 16, v41\n"
"v_and_b32 v51, 0xffff0000, v41\n"
"s_waitcnt vmcnt(1)\n"
"v_lshlrev_b32 v52, 16, v42\n"
"v_and_b32 v53, 0xffff0000, v42\n"
"v_lshlrev_b32 v54, 16, v43\n"
"v_and_b32 v55, 0xffff0000, v43\n"
"s_waitcnt vmcnt(0)\n"
"v_lshlrev_b32 v56, 16, v44\n"
"v_and_b32 v57, 0xffff0000, v44\n"
"v_lshlrev_b32 v58, 16, v45\n"
"v_and_b32 v59, 0xffff0000, v45\n"
"v_mov_b32 v46, 0\n"
"v_mov_b32 v47, 0\n"
"s_mov_b32 s17, 0x100000\n"
".Ltp_385:\n"
"s_sleep 1\n"
"ds_read_b32 v3, v5 offset:160\n"
"s_sub_u32 s17, s17, 1\n"
"s_cbranch_scc1 .Lou_386\n"
"s_waitcnt lgkmcnt(0)\n"
"v_cmp_eq_u32 vcc, 0, v3\n"
"s_cbranch_vccnz .Ltp_385\n"
".Lou_386:\n"
"s_waitcnt lgkmcnt(0)\n"
"s_cmp_eq_u32 s17, 0xfffff\n"
"s_cbranch_scc1 .Lsk_387\n"
"s_mov_b32 s89, 24\n"
".Ldl_389:\n"
"s_sub_u32 s89, s89, 1\n"
"s_cbranch_scc1 .Lde_390\n"
"s_sleep 1\n"
"s_branch .Ldl_389\n"
".Lde_390:\n"
"s_branch .Ljn_388\n"
".Lsk_387:\n"
".Ljn_388:\n"
"buffer_load_dwordx4 v[108:111], v2, s[4:7], s34 offen offset:0 sc1\n"
"s_nop 9\n"
"buffer_load_dwordx4 v[112:115], v2, s[4:7], s34 offen offset:1024 sc1\n"
"s_nop 9\n"
"buffer_load_dwordx4 v[116:119], v2, s[4:7], s34 offen offset:2048 sc1\n"
"s_nop 9\n"
"buffer_load_dwordx4 v[120:123], v2, s[4:7], s34 offen offset:3072 sc1\n"
"s_nop 9\n"
"buffer_load_dwordx4 v[124:127], v2, s[4:7], s35 offen offset:0 sc1\n"
"s_nop 9\n"
"buffer_load_dwordx4 v[88:91], v2, s[4:7], s32 offen offset:0 sc1\n"
"s_nop 9\n"
"buffer_load_dwordx4 v[92:95], v2, s[4:7], s32 offen offset:1024 sc1\n"
"s_nop 9\n"
"buffer_load_dwordx4 v[96:99], v2, s[4:7], s32 offen offset:2048 sc1\n"
"s_nop 9\n"
"buffer_load_dwordx4 v[100:103], v2, s[4:7], s32 offen offset:3072 sc1\n"
"s_nop 9\n"
"buffer_load_dwordx4 v[104:107], v2, s[4:7], s33 offen offset:0 sc1\n"
"s_waitcnt vmcnt(9)\n"
"v_max_u32 v38, v108, v109\n"
"v_max3_u32 v38, v110, v111, v38\n"
"v_cmp_eq_u32 vcc, -1, v38\n"
"s_cbranch_vccnz .Lsl_391\n"
".Lbk_392:\n"
"v_mfma_f32_16x16x32_bf16 v[68:71], v[156:159], v[108:111], 0\n"
"v_mfma_f32_16x16x32_bf16 v[72:75], v[160:163], v[108:111], 0\n"
"v_mfma_f32_16x16x32_bf16 v[76:79], v[164:167], v[108:111], 0\n"
"s_waitcnt vmcnt(8)\n"
"v_max_u32 v38, v112, v113\n"
"v_max3_u32 v38, v114, v115, v38\n"
"v_mfma_f32_16x16x32_bf16 v[80:83], v[168:171], v[108:111], 0\n"
"v_cmp_eq_u32 vcc, -1, v38\n"
"v_mfma_f32_16x16x32_bf16 v[84:87], v[172:175], v[108:111], 0\n"
"s_cbranch_vccnz .Lsl_393\n"
".Lbk_394:\n"
"v_mfma_f32_16x16x32_bf16 v[68:71], v[176:179], v[112:115], v[68:71]\n"
"v_mfma_f32_16x16x32_bf16 v[72:75], v[180:183], v[112:115], v[72:75]\n"
"v_mfma_f32_16x16x32_bf16 v[76:79], v[184:187], v[112:115], v[76:79]\n"
"s_waitcnt vmcnt(7)\n"
"v_max_u32 v38, v116, v117\n"
"v_max3_u32 v38, v118, v119, v38\n"
"v_mfma_f32_16x16x32_bf16 v[80:83], v[188:191], v[112:115], v[80:83]\n"
"v_cmp_eq_u32 vcc, -1, v38\n"
"v_mfma_f32_16x16x32_bf16 v[84:87], v[192:195], v[112:115], v[84:87]\n"
"s_cbranch_vccnz .Lsl_395\n"
".Lbk_396:\n"
"v_mfma_f32_16x16x32_bf16 v[68:71], v[196:199], v[116:119], v[68:71]\n"
"v_mfma_f32_16x16x32_bf16 v[72:75], v[200:203], v[116:119], v[72:75]\n"
"v_mfma_f32_16x16x32_bf16 v[76:79], v[204:207], v[116:119], v[76:79]\n"
"s_waitcnt vmcnt(6)\n"
"v_max_u32 v38, v120, v121\n"
"v_max3_u32 v38, v122, v123, v38\n"
"v_mfma_f32_16x16x32_bf16 v[80:83], v[208:211], v[116:119], v[80:83]\n"
"v_cmp_eq_u32 vcc, -1, v38\n"
"v_mfma_f32_16x16x32_bf16 v[84:87], v[212:215], v[116:119], v[84:87]\n"
"s_cbranch_vccnz .Lsl_397\n"
".Lbk_398:\n"
"v_mfma_f32_16x16x32_bf16 v[68:71], v[216:219], v[120:123], v[68:71]\n"
"v_mfma_f32_16x16x32_bf16 v[72:75], v[220:223], v[120:123], v[72:75]\n"
"v_mfma_f32_16x16x32_bf16 v[76:79], v[224:227], v[120:123], v[76:79]\n"
"s_waitcnt vmcnt(5)\n"
"v_max_u32 v38, v124, v125\n"
"v_max3_u32 v38, v126, v127, v38\n"
"v_mfma_f32_16x16x32_bf16 v[80:83], v[228:231], v[120:123], v[80:83]\n"
"v_cmp_eq_u32 vcc, -1, v38\n"
"v_mfma_f32_16x16x32_bf16 v[84:87], v[232:235], v[120:123], v[84:87]\n"
"s_cbranch_vccnz .Lsl_399\n"
".Lbk_400:\n"
"v_mfma_f32_16x16x32_bf16 v[68:71], v[236:239], v[124:127], v[68:71]\n"
"v_mfma_f32_16x16x32_bf16 v[72:75], v[240:243], v[124:127], v[72:75]\n"
"v_mfma_f32_16x16x32_bf16 v[76:79], v[244:247], v[124:127], v[76:79]\n"
"s_waitcnt vmcnt(4)\n"
"v_max_u32 v38, v88, v89\n"
"v_max3_u32 v38, v90, v91, v38\n"
"v_mfma_f32_16x16x32_bf16 v[80:83], v[248:251], v[124:127], v[80:83]\n"
"v_cmp_eq_u32 vcc, -1, v38\n"
"v_mfma_f32_16x16x32_bf16 v[84:87], v[252:255], v[124:127], v[84:87]\n"
"ds_write_b128 v8, v[68:71] offset:0\n"
"s_cbranch_vccnz .Lsl_401\n"
".Lbk_402:\n"
"v_mfma_f32_16x16x32_bf16 v[56:59], v[164:167], v[88:91], v[56:59]\n"
"s_waitcnt vmcnt(3)\n"
"v_max_u32 v38, v92, v93\n"
"v_max3_u32 v38, v94, v95, v38\n"
"ds_write_b128 v8, v[72:75] offset:1024\n"
"v_mfma_f32_16x16x32_bf16 v[60:63], v[168:171], v[88:91], 0\n"
"v_cmp_eq_u32 vcc, -1, v38\n"
"ds_write_b128 v10, v[76:79] offset:0\n"
"v_mfma_f32_16x16x32_bf16 v[64:67], v[172:175], v[88:91], 0\n"
"v_mov_b32 v117, s16\n"
"s_cbranch_vccnz .Lsl_403\n"
".Lbk_404:\n"
"v_mfma_f32_16x16x32_bf16 v[56:59], v[184:187], v[92:95], v[56:59]\n"
"s_waitcnt vmcnt(2)\n"
"v_max_u32 v38, v96, v97\n"
"v_max3_u32 v38, v98, v99, v38\n"
"v_mov_b32 v119, s16\n"
"v_mfma_f32_16x16x32_bf16 v[60:63], v[188:191], v[92:95], v[60:63]\n"
"v_cmp_eq_u32 vcc, -1, v38\n"
"ds_write_b128 v9, v[80:83] offset:0\n"
"v_mfma_f32_16x16x32_bf16 v[64:67], v[192:195], v[92:95], v[64:67]\n"
"ds_write_b128 v9, v[84:87] offset:1024\n"
"s_cbranch_vccnz .Lsl_405\n"
".Lbk_406:\n"
"v_mfma_f32_16x16x32_bf16 v[56:59], v[204:207], v[96:99], v[56:59]\n"
"s_waitcnt vmcnt(1)\n"
"v_max_u32 v38, v100, v101\n"
"v_max3_u32 v38, v102, v103, v38\n"
"ds_write_b32 v4, v4 offset:640\n"
"v_mfma_f32_16x16x32_bf16 v[60:63], v[208:211], v[96:99], v[60:63]\n"
"v_cmp_eq_u32 vcc, -1, v38\n"
"v_mov_b32 v121, s16\n"
"v_mov_b32 v123, s16\n"
"v_mfma_f32_16x16x32_bf16 v[64:67], v[212:215], v[96:99], v[64:67]\n"
"v_mov_b32 v125, s16\n"
"s_cbranch_vccnz .Lsl_407\n"
".Lbk_408:\n"
"v_mfma_f32_16x16x32_bf16 v[56:59], v[224:227], v[100:103], v[56:59]\n"
"s_waitcnt vmcnt(0)\n"
"v_max_u32 v38, v104, v105\n"
"v_max3_u32 v38, v106, v107, v38\n"
"v_mov_b32 v127, s16\n"
"v_mfma_f32_16x16x32_bf16 v[60:63], v[228:231], v[100:103], v[60:63]\n"
"v_cmp_eq_u32 vcc, -1, v38\n"
"ds_read_b32 v3, v5 offset:640\n"
"v_mfma_f32_16x16x32_bf16 v[64:67], v[232:235], v[100:103], v[64:67]\n"
"s_cbranch_vccnz .Lsl_409\n"
".Lbk_410:\n"
"v_mfma_f32_16x16x32_bf16 v[56:59], v[244:247], v[104:107], v[56:59]\n"
"ds_read_b128 v[76:79], v12 offset:2048\n"
"ds_read_b128 v[108:111], v13 offset:2048\n"
"v_mfma_f32_16x16x32_bf16 v[60:63], v[248:251], v[104:107], v[60:63]\n"
"ds_read_b128 v[68:71], v12 offset:0\n"
"ds_read_b128 v[80:83], v13 offset:0\n"
"v_mfma_f32_16x16x32_bf16 v[64:67], v[252:255], v[104:107], v[64:67]\n"
"ds_read_b128 v[72:75], v12 offset:1024\n"
"ds_read_b128 v[84:87], v13 offset:1024\n"
"v_mfma_f32_16x16x32_bf16 v[48:51], v[156:159], v[88:91], v[48:51]\n"
"ds_write_b128 v7, v[56:59] offset:0\n"
"v_mfma_f32_16x16x32_bf16 v[52:55], v[160:163], v[88:91], v[52:55]\n"
"ds_write_b128 v6, v[60:63] offset:0\n"
"v_mfma_f32_16x16x32_bf16 v[48:51], v[176:179], v[92:95], v[48:51]\n"
"ds_write_b128 v6, v[64:67] offset:1024\n"
"v_mfma_f32_16x16x32_bf16 v[52:55], v[180:183], v[92:95], v[52:55]\n"
"ds_write_b32 v4, v4 offset:656\n"
"v_mfma_f32_16x16x32_bf16 v[48:51], v[196:199], v[96:99], v[48:51]\n"
"s_mov_b32 s17, 0x100000\n"
"s_waitcnt lgkmcnt(10)\n"
"v_cmp_eq_u32 vcc, 0, v3\n"
"s_cbranch_vccz .Lok_412\n"
".Ltp_411:\n"
"ds_read_b32 v3, v5 offset:640\n"
"ds_read_b128 v[76:79], v12 offset:2048\n"
"ds_read_b128 v[108:111], v13 offset:2048\n"
"ds_read_b128 v[68:71], v12 offset:0\n"
"ds_read_b128 v[80:83], v13 offset:0\n"
"ds_read_b128 v[72:75], v12 offset:1024\n"
"ds_read_b128 v[84:87], v13 offset:1024\n"
"s_sub_u32 s17, s17, 1\n"
"s_cbranch_scc1 .Lok_412\n"
"s_waitcnt lgkmcnt(6)\n"
"v_cmp_eq_u32 vcc, 0, v3\n"
"s_cbranch_vccnz .Ltp_411\n"
"s_waitcnt lgkmcnt(0)\n"
".Lok_412:\n"
"v_mfma_f32_16x16x32_bf16 v[52:55], v[200:203], v[96:99], v[52:55]\n"
"s_waitcnt lgkmcnt(8)\n"
"v_add_f32 v76, v76, v108\n"
"v_add_f32 v77, v77, v109\n"
"v_add_f32 v78, v78, v110\n"
"v_mfma_f32_16x16x32_bf16 v[48:51], v[216:219], v[100:103], v[48:51]\n"
"v_add_f32 v79, v79, v111\n"
"s_waitcnt lgkmcnt(6)\n"
"v_add_f32 v68, v68, v80\n"
"v_add_f32 v69, v69, v81\n"
"v_mfma_f32_16x16x32_bf16 v[52:55], v[220:223], v[100:103], v[52:55]\n"
"v_add_f32 v70, v70, v82\n"
"v_add_f32 v71, v71, v83\n"
"s_waitcnt lgkmcnt(4)\n"
"v_add_f32 v72, v72, v84\n"
"v_mfma_f32_16x16x32_bf16 v[48:51], v[236:239], v[104:107], v[48:51]\n"
"v_add_f32 v73, v73, v85\n"
"v_add_f32 v74, v74, v86\n"
"v_add_f32 v75, v75, v87\n"
"v_mfma_f32_16x16x32_bf16 v[52:55], v[240:243], v[104:107], v[52:55]\n"
"ds_read_b32 v3, v5 offset:656\n"
"ds_read_b128 v[112:115], v11 offset:2048\n"
"ds_read_b128 v[60:63], v11 offset:0\n"
"ds_read_b128 v[64:67], v11 offset:1024\n"
"v_add_f32 v56, v56, v76\n"
"v_add_f32 v57, v57, v77\n"
"v_add_f32 v58, v58, v78\n"
"v_add_f32 v59, v59, v79\n"
"v_add_f32 v48, v48, v68\n"
"v_add_f32 v49, v49, v69\n"
"v_add_f32 v50, v50, v70\n"
"v_add_f32 v51, v51, v71\n"
"v_add_f32 v52, v52, v72\n"
"v_add_f32 v53, v53, v73\n"
"v_add_f32 v54, v54, v74\n"
"v_add_f32 v55, v55, v75\n"
"s_mov_b32 s17, 0x100000\n"
"s_waitcnt lgkmcnt(3)\n"
"v_cmp_eq_u32 vcc, 0, v3\n"
"s_cbranch_vccz .Lok_414\n"
".Ltp_413:\n"
"ds_read_b32 v3, v5 offset:656\n"
"ds_read_b128 v[112:115], v11 offset:2048\n"
"ds_read_b128 v[60:63], v11 offset:0\n"
"ds_read_b128 v[64:67], v11 offset:1024\n"
"s_sub_u32 s17, s17, 1\n"
"s_cbranch_scc1 .Lok_414\n"
"s_waitcnt lgkmcnt(3)\n"
"v_cmp_eq_u32 vcc, 0, v3\n"
"s_cbranch_vccnz .Ltp_413\n"
"s_waitcnt lgkmcnt(0)\n"
".Lok_414:\n"
"s_waitcnt lgkmcnt(2)\n"
"v_add_f32 v56, v56, v112\n"
"v_add_f32 v57, v57, v113\n"
"v_add_f32 v58, v58, v114\n"
"v_add_f32 v59, v59, v115\n"
"v_cvt_pk_bf16_f32 v44, v56, v57\n"
"v_cvt_pk_bf16_f32 v45, v58, v59\n"
"v_and_b32 v44, v19, v44\n"
"v_and_b32 v45, v19, v45\n"
"s_waitcnt lgkmcnt(1)\n"
"v_add_f32 v48, v48, v60\n"
"v_add_f32 v49, v49, v61\n"
"v_add_f32 v50, v50, v62\n"
"v_add_f32 v51, v51, v63\n"
"v_cvt_pk_bf16_f32 v40, v48, v49\n"
"v_cvt_pk_bf16_f32 v41, v50, v51\n"
"v_mfma_f32_16x16x32_bf16 v[88:91], v[44:47], v[44:47], 0\n"
"v_mfma_f32_16x16x32_bf16 v[92:95], v[20:23], v[44:47], 0\n"
"s_waitcnt lgkmcnt(0)\n"
"v_add_f32 v52, v52, v64\n"
"v_add_f32 v53, v53, v65\n"
"v_add_f32 v54, v54, v66\n"
"v_add_f32 v55, v55, v67\n"
"v_cvt_pk_bf16_f32 v42, v52, v53\n"
"v_cvt_pk_bf16_f32 v43, v54, v55\n"
"v_cvt_pk_bf16_f32 v116, v48, v49\n"
"v_cvt_pk_bf16_f32 v118, v50, v51\n"
"v_mfma_f32_16x16x32_bf16 v[88:91], v[40:43], v[40:43], v[88:91]\n"
"v_mfma_f32_16x16x32_bf16 v[92:95], v[20:23], v[40:43], v[92:95]\n"
"v_cvt_pk_bf16_f32 v120, v52, v53\n"
"v_cvt_pk_bf16_f32 v122, v54, v55\n"
"v_cvt_pk_bf16_f32 v124, v56, v57\n"
"v_cvt_pk_bf16_f32 v126, v58, v59\n"
"s_nop 0\n"
"v_cndmask_b32 v101, v88, v90, s[44:45]\n"
"v_cndmask_b32 v88, v89, v91, s[44:45]\n"
"s_nop 0\n"
"v_min_u32 v100, -2, v92\n"
"v_min_u32 v88, -2, v88\n"
"v_min_u32 v101, -2, v101\n"
"v_mov_b32_dpp v102, v100 quad_perm:[1,0,3,2] row_mask:0xf bank_mask:0xf\n"
"v_mov_b32_dpp v103, v88 quad_perm:[1,0,3,2] row_mask:0xf bank_mask:0xf\n"
"buffer_store_dwordx4 v[100:103], v16, s[4:7], s38 offen sc1\n"
"buffer_store_dwordx4 v[116:119], v17, s[4:7], s37 offen offset:0 sc1\n"
"buffer_store_dwordx4 v[120:123], v17, s[4:7], s37 offen offset:1024 sc1\n"
"buffer_store_dwordx4 v[124:127], v18, s[4:7], s37 offen offset:2048 sc1\n"
"ds_write_b32 v4, v4 offset:256\n"
"s_xor_b32 s79, s3, 1\n"
"s_lshl_b32 s79, s79, 8\n"
"s_add_u32 s32, s19, s79\n"
"s_cmp_ge_u32 s32, 64\n"
"s_cbranch_scc1 .Lnr_415\n"
"s_lshl_b32 s79, s19, 11\n"
"s_add_u32 s32, s79, 0xb08000\n"
"s_lshl_b32 s79, s19, 7\n"
"s_add_u32 s33, s79, 0xb2a000\n"
"v_lshrrev_b32 v99, 3, v0\n"
"v_and_b32 v98, 7, v0\n"
"v_lshlrev_b32 v98, 4, v98\n"
"v_lshl_add_u32 v40, v99, 14, v98\n"
"v_mov_b32 v41, 0x3727c5ac\n"
"s_mov_b32 s89, 12\n"
".Ldl_416:\n"
"s_sub_u32 s89, s89, 1\n"
"s_cbranch_scc1 .Lde_417\n"
"s_sleep 1\n"
"s_branch .Ldl_416\n"
".Lde_417:\n"
"buffer_load_dwordx4 v[48:51], v2, s[4:7], s32 offen offset:0 sc1\n"
"buffer_load_dwordx4 v[52:55], v2, s[4:7], s32 offen offset:1024 sc1\n"
"s_mov_b32 s17, 32768\n"
".Ltp_418:\n"
"s_waitcnt vmcnt(0)\n"
"s_sub_u32 s17, s17, 1\n"
"s_cbranch_scc1 .Lou_419\n"
"v_max_u32 v38, v48, v49\n"
"v_max3_u32 v38, v50, v51, v38\n"
"v_max3_u32 v38, v52, v53, v38\n"
"v_max3_u32 v38, v54, v55, v38\n"
"v_cmp_eq_u32 vcc, -1, v38\n"
"s_cbranch_vccz .Lou_419\n"
"v_max_u32 v38, v50, v51\n"
"v_max3_u32 v38, v48, v49, v38\n"
"v_cmp_eq_u32 vcc, -1, v38\n"
"s_cbranch_vccz .Lsk_420\n"
"s_mov_b64 exec, vcc\n"
"buffer_load_dwordx4 v[48:51], v2, s[4:7], s32 offen offset:0 sc1\n"
"s_mov_b64 exec, -1\n"
".Lsk_420:\n"
"v_max_u32 v38, v54, v55\n"
"v_max3_u32 v38, v52, v53, v38\n"
"v_cmp_eq_u32 vcc, -1, v38\n"
"s_cbranch_vccz .Lsk_421\n"
"s_mov_b64 exec, vcc\n"
"buffer_load_dwordx4 v[52:55], v2, s[4:7], s32 offen offset:1024 sc1\n"
"s_mov_b64 exec, -1\n"
".Lsk_421:\n"
"s_branch .Ltp_418\n"
".Lou_419:\n"
"s_waitcnt vmcnt(0)\n"
"v_add_f32 v48, v48, v52\n"
"v_add_f32 v49, v49, v53\n"
"v_add_f32 v50, v50, v54\n"
"v_add_f32 v51, v51, v55\n"
"v_add_f32_dpp v52, v48, v48 row_ror:8 row_mask:0xf bank_mask:0xf\n"
"v_add_f32_dpp v53, v49, v49 row_ror:8 row_mask:0xf bank_mask:0xf\n"
"v_add_f32_dpp v54, v50, v50 row_ror:8 row_mask:0xf bank_mask:0xf\n"
"v_add_f32_dpp v55, v51, v51 row_ror:8 row_mask:0xf bank_mask:0xf\n"
"v_mov_b32 v48, v52\n"
"v_mov_b32 v49, v53\n"
"v_mov_b32 v50, v54\n"
"v_mov_b32 v51, v55\n"
"v_permlane16_swap_b32 v48, v52\n"
"v_permlane16_swap_b32 v49, v53\n"
"v_permlane16_swap_b32 v50, v54\n"
"v_permlane16_swap_b32 v51, v55\n"
"v_add_f32 v52, v48, v52\n"
"v_add_f32 v53, v49, v53\n"
"v_add_f32 v54, v50, v54\n"
"v_add_f32 v55, v51, v55\n"
"v_mov_b32 v48, v52\n"
"v_mov_b32 v49, v53\n"
"v_mov_b32 v50, v54\n"
"v_mov_b32 v51, v55\n"
"v_permlane32_swap_b32 v48, v52\n"
"v_permlane32_swap_b32 v49, v53\n"
"v_permlane32_swap_b32 v50, v54\n"
"v_permlane32_swap_b32 v51, v55\n"
"v_add_f32 v52, v48, v52\n"
"v_add_f32 v53, v49, v53\n"
"v_add_f32 v54, v50, v54\n"
"v_add_f32 v55, v51, v55\n"
"v_mul_f32 v48, 0x3acccccd, v52\n"
"v_mul_f32 v50, 0x3acccccd, v54\n"
"v_fma_f32 v53, -v52, v48, v53\n"
"v_fma_f32 v55, -v54, v50, v55\n"
"v_fmamk_f32 v53, v53, 0x3acccccd, v41\n"
"v_fmamk_f32 v55, v55, 0x3acccccd, v41\n"
"v_max_f32 v53, v53, v41\n"
"v_max_f32 v55, v55, v41\n"
"v_rsq_f32 v49, v53\n"
"v_rsq_f32 v51, v55\n"
"v_mul_f32 v48, -v48, v49\n"
"v_mul_f32 v50, -v50, v51\n"
"v_min_u32 v49, -2, v49\n"
"v_min_u32 v51, -2, v51\n"
"v_min_u32 v48, -2, v48\n"
"v_min_u32 v50, -2, v50\n"
"buffer_store_dwordx4 v[48:51], v40, s[4:7], s33 offen sc1\n"
"s_nop 3\n"
".Lnr_415:\n"
"s_cmp_lt_i32 s18, 0\n"
"s_cbranch_scc1 .Lnc_422\n"
"s_nop 0\n"
"s_setprio 0\n"
"s_cmp_lg_u32 s43, 0\n"
"s_cbranch_scc1 .Lsk_424\n"
"s_call_b64 s[46:47], .Lst_312\n"
".Lsk_424:\n"
"s_cmp_lg_u32 s43, 1\n"
"s_cbranch_scc1 .Lsk_425\n"
"s_call_b64 s[46:47], .Lst_313\n"
".Lsk_425:\n"
"s_cmp_lg_u32 s43, 2\n"
"s_cbranch_scc1 .Lsk_426\n"
"s_call_b64 s[46:47], .Lst_314\n"
".Lsk_426:\n"
".Len_423:\n"
"s_setprio 2\n"
"s_mov_b32 s17, 0x100000\n"
".Ltp_427:\n"
"s_sleep 1\n"
"ds_read_b32 v3, v5 offset:208\n"
"s_sub_u32 s17, s17, 1\n"
"s_cbranch_scc1 .Lou_428\n"
"s_waitcnt lgkmcnt(0)\n"
"v_cmp_eq_u32 vcc, 0, v3\n"
"s_cbranch_vccnz .Ltp_427\n"
".Lou_428:\n"
"s_waitcnt lgkmcnt(0)\n"
"s_setprio 0\n"
"s_lshl_b32 s79, s18, 8\n"
"s_add_u32 s32, s79, 0x868000\n"
"s_add_u32 s33, s32, 0x140000\n"
"s_and_b32 s79, s2, 7\n"
"s_lshl_b32 s79, s79, 14\n"
"s_add_u32 s34, s79, 0xb2a000\n"
"s_add_u32 s35, s34, 0x1000\n"
"v_and_b32 v126, 0xffffff80, v29\n"
"v_add_u32 v126, v126, v29\n"
"ds_read_b128 v[88:91], v25 offset:448\n"
"ds_read_b128 v[92:95], v25 offset:432\n"
"ds_read_b64 v[96:97], v24 offset:448\n"
"ds_read_b64 v[98:99], v24 offset:456\n"
"ds_read_b128 v[100:103], v25 offset:960\n"
"ds_read_b128 v[104:107], v25 offset:1472\n"
"s_waitcnt lgkmcnt(1)\n"
"v_mfma_f32_16x16x32_f16 v[64:67], v[88:91], v[144:147], 0\n"
"ds_read_b128 v[40:43], v25 offset:1984\n"
"ds_read_b64 v[44:45], v24 offset:960\n"
"v_mfma_f32_16x16x32_f16 v[68:71], v[92:95], v[148:151], 0\n"
"ds_read_b64 v[46:47], v24 offset:968\n"
"ds_read_b64 v[72:73], v24 offset:1472\n"
"v_mfma_f32_16x16x32_f16 v[48:51], v[152:155], v[88:91], 0\n"
"ds_read_b64 v[74:75], v24 offset:1480\n"
"ds_read_b64 v[76:77], v24 offset:384\n"
"v_mfma_f32_16x16x32_f16 v[48:51], v[96:99], v[100:103], v[48:51]\n"
"ds_read_b64 v[78:79], v24 offset:392\n"
"s_waitcnt lgkmcnt(4)\n"
"v_mfma_f32_16x16x32_f16 v[52:55], v[96:99], v[104:107], 0\n"
"ds_read_b128 v[84:87], v25 offset:896\n"
"ds_read_b128 v[92:95], v25 offset:1408\n"
"v_mfma_f32_16x16x32_f16 v[56:59], v[96:99], v[40:43], 0\n"
"ds_read_b128 v[88:91], v25 offset:1920\n"
"ds_read_b64 v[100:101], v24 offset:896\n"
"v_mfma_f32_16x16x32_f16 v[52:55], v[44:47], v[40:43], v[52:55]\n"
"ds_read_b64 v[102:103], v24 offset:904\n"
"ds_read_b64 v[96:97], v24 offset:1408\n"
"v_cvt_pk_f16_f32 v80, v64, v65\n"
"v_mfma_f32_16x16x32_f16 v[48:51], v[44:47], v[104:107], v[48:51]\n"
"ds_read_b64 v[98:99], v24 offset:1416\n"
"s_waitcnt lgkmcnt(4)\n"
"v_mfma_f32_16x16x32_f16 v[48:51], v[72:75], v[40:43], v[48:51]\n"
"ds_read_b128 v[104:107], v25 offset:960\n"
"v_cvt_pk_f16_f32 v81, v66, v67\n"
"v_mfma_f32_16x16x32_f16 v[48:51], v[76:79], v[84:87], v[48:51]\n"
"ds_read_b128 v[44:47], v25 offset:944\n"
"v_cvt_pk_f16_f32 v82, v68, v69\n"
"v_mfma_f32_16x16x32_f16 v[52:55], v[76:79], v[92:95], v[52:55]\n"
"ds_read_b64 v[40:41], v24 offset:320\n"
"ds_read_b64 v[42:43], v24 offset:328\n"
"v_mfma_f32_16x16x32_f16 v[56:59], v[76:79], v[88:91], v[56:59]\n"
"ds_read_b128 v[72:75], v25 offset:832\n"
"s_waitcnt lgkmcnt(5)\n"
"v_mfma_f32_16x16x32_f16 v[52:55], v[100:103], v[88:91], v[52:55]\n"
"ds_read_b128 v[84:87], v25 offset:1344\n"
"v_cvt_pk_f16_f32 v83, v70, v71\n"
"v_mfma_f32_16x16x32_f16 v[48:51], v[100:103], v[92:95], v[48:51]\n"
"ds_read_b128 v[76:79], v25 offset:1856\n"
"ds_read_b64 v[92:93], v24 offset:832\n"
"v_mfma_f32_16x16x32_f16 v[48:51], v[96:99], v[88:91], v[48:51]\n"
"ds_read_b64 v[94:95], v24 offset:840\n"
"v_mfma_f32_16x16x32_f16 v[48:51], v[80:83], v[128:131], v[48:51]\n"
"ds_read_b64 v[100:101], v24 offset:1344\n"
"s_waitcnt lgkmcnt(9)\n"
"v_mfma_f32_16x16x32_f16 v[52:55], v[80:83], v[132:135], v[52:55]\n"
"ds_read_b64 v[102:103], v24 offset:1352\n"
"ds_read_b64 v[88:89], v24 offset:256\n"
"v_mfma_f32_16x16x32_f16 v[56:59], v[80:83], v[136:139], v[56:59]\n"
"ds_read_b64 v[90:91], v24 offset:264\n"
"v_mfma_f32_16x16x32_f16 v[60:63], v[80:83], v[140:143], 0\n"
"ds_read_b128 v[96:99], v25 offset:768\n"
"v_mfma_f32_16x16x32_f16 v[64:67], v[104:107], v[144:147], 0\n"
"s_waitcnt lgkmcnt(8)\n"
"v_mfma_f32_16x16x32_f16 v[68:71], v[44:47], v[148:151], 0\n"
"ds_read_b128 v[44:47], v25 offset:1280\n"
"v_mfma_f32_16x16x32_f16 v[52:55], v[152:155], v[104:107], v[52:55]\n"
"ds_read_b128 v[104:107], v25 offset:1792\n"
"v_mfma_f32_16x16x32_f16 v[48:51], v[40:43], v[72:75], v[48:51]\n"
"ds_read_b64 v[72:73], v24 offset:768\n"
"v_mfma_f32_16x16x32_f16 v[52:55], v[40:43], v[84:87], v[52:55]\n"
"ds_read_b64 v[74:75], v24 offset:776\n"
"s_waitcnt lgkmcnt(7)\n"
"v_mfma_f32_16x16x32_f16 v[56:59], v[40:43], v[76:79], v[56:59]\n"
"ds_read_b64 v[40:41], v24 offset:1280\n"
"v_mfma_f32_16x16x32_f16 v[52:55], v[92:95], v[76:79], v[52:55]\n"
"ds_read_b64 v[42:43], v24 offset:1288\n"
"v_cvt_pk_f16_f32 v80, v64, v65\n"
"v_mfma_f32_16x16x32_f16 v[48:51], v[92:95], v[84:87], v[48:51]\n"
"ds_read_b128 v[84:87], v25 offset:1472\n"
"v_cvt_pk_f16_f32 v81, v66, v67\n"
"v_mfma_f32_16x16x32_f16 v[48:51], v[100:103], v[76:79], v[48:51]\n"
"ds_read_b128 v[92:95], v25 offset:1456\n"
"s_waitcnt lgkmcnt(4)\n"
"v_mfma_f32_16x16x32_f16 v[48:51], v[88:91], v[96:99], v[48:51]\n"
"ds_read_b64 v[76:77], v24 offset:192\n"
"ds_read_b64 v[78:79], v24 offset:200\n"
"v_mfma_f32_16x16x32_f16 v[52:55], v[88:91], v[44:47], v[52:55]\n"
"ds_read_b128 v[100:103], v25 offset:704\n"
"v_cvt_pk_f16_f32 v82, v68, v69\n"
"v_mfma_f32_16x16x32_f16 v[56:59], v[88:91], v[104:107], v[56:59]\n"
"ds_read_b128 v[96:99], v25 offset:1216\n"
"v_cvt_pk_f16_f32 v83, v70, v71\n"
"v_mfma_f32_16x16x32_f16 v[52:55], v[72:75], v[104:107], v[52:55]\n"
"ds_read_b128 v[88:91], v25 offset:1728\n"
"s_waitcnt lgkmcnt(7)\n"
"v_mfma_f32_16x16x32_f16 v[48:51], v[72:75], v[44:47], v[48:51]\n"
"ds_read_b64 v[44:45], v24 offset:704\n"
"v_mfma_f32_16x16x32_f16 v[48:51], v[40:43], v[104:107], v[48:51]\n"
"ds_read_b64 v[46:47], v24 offset:712\n"
"ds_read_b64 v[72:73], v24 offset:1216\n"
"v_mfma_f32_16x16x32_f16 v[52:55], v[80:83], v[128:131], v[52:55]\n"
"ds_read_b64 v[74:75], v24 offset:1224\n"
"ds_read_b64 v[104:105], v24 offset:128\n"
"v_mfma_f32_16x16x32_f16 v[56:59], v[80:83], v[132:135], v[56:59]\n"
"ds_read_b64 v[106:107], v24 offset:136\n"
"s_waitcnt lgkmcnt(11)\n"
"v_mfma_f32_16x16x32_f16 v[60:63], v[80:83], v[136:139], v[60:63]\n"
"ds_read_b128 v[40:43], v25 offset:640\n"
"v_mfma_f32_16x16x32_f16 v[64:67], v[84:87], v[144:147], 0\n"
"v_mfma_f32_16x16x32_f16 v[68:71], v[92:95], v[148:151], 0\n"
"ds_read_b128 v[92:95], v25 offset:1152\n"
"v_mfma_f32_16x16x32_f16 v[56:59], v[152:155], v[84:87], v[56:59]\n"
"ds_read_b128 v[84:87], v25 offset:1664\n"
"s_waitcnt lgkmcnt(7)\n"
"v_mfma_f32_16x16x32_f16 v[48:51], v[76:79], v[100:103], v[48:51]\n"
"ds_read_b64 v[100:101], v24 offset:640\n"
"v_mfma_f32_16x16x32_f16 v[52:55], v[76:79], v[96:99], v[52:55]\n"
"ds_read_b64 v[102:103], v24 offset:648\n"
"v_mfma_f32_16x16x32_f16 v[56:59], v[76:79], v[88:91], v[56:59]\n"
"ds_read_b64 v[76:77], v24 offset:1152\n"
"v_mfma_f32_16x16x32_f16 v[52:55], v[44:47], v[88:91], v[52:55]\n"
"ds_read_b64 v[78:79], v24 offset:1160\n"
"s_waitcnt lgkmcnt(5)\n"
"v_mfma_f32_16x16x32_f16 v[48:51], v[44:47], v[96:99], v[48:51]\n"
"ds_read_b128 v[96:99], v25 offset:1984\n"
"v_cvt_pk_f16_f32 v80, v64, v65\n"
"v_mfma_f32_16x16x32_f16 v[48:51], v[72:75], v[88:91], v[48:51]\n"
"ds_read_b128 v[44:47], v25 offset:1968\n"
"ds_read_b64 v[88:89], v24 offset:64\n"
"v_mfma_f32_16x16x32_f16 v[48:51], v[104:107], v[40:43], v[48:51]\n"
"ds_read_b64 v[90:91], v24 offset:72\n"
"ds_read_b128 v[72:75], v25 offset:576\n"
"v_mfma_f32_16x16x32_f16 v[52:55], v[104:107], v[92:95], v[52:55]\n"
"ds_read_b128 v[40:43], v25 offset:1088\n"
"s_waitcnt lgkmcnt(6)\n"
"v_mfma_f32_16x16x32_f16 v[56:59], v[104:107], v[84:87], v[56:59]\n"
"ds_read_b128 v[104:107], v25 offset:1600\n"
"v_cvt_pk_f16_f32 v81, v66, v67\n"
"v_mfma_f32_16x16x32_f16 v[52:55], v[100:103], v[84:87], v[52:55]\n"
"v_cvt_pk_f16_f32 v82, v68, v69\n"
"v_cvt_pk_f16_f32 v83, v70, v71\n"
"v_mfma_f32_16x16x32_f16 v[48:51], v[100:103], v[92:95], v[48:51]\n"
"ds_read_b64 v[92:93], v24 offset:576\n"
"ds_read_b64 v[94:95], v24 offset:584\n"
"v_mfma_f32_16x16x32_f16 v[48:51], v[76:79], v[84:87], v[48:51]\n"
"ds_read_b64 v[100:101], v24 offset:1088\n"
"s_waitcnt lgkmcnt(8)\n"
"v_mfma_f32_16x16x32_f16 v[56:59], v[80:83], v[128:131], v[56:59]\n"
"ds_read_b64 v[102:103], v24 offset:1096\n"
"ds_read_b64 v[84:85], v24 offset:0\n"
"v_mfma_f32_16x16x32_f16 v[60:63], v[80:83], v[132:135], v[60:63]\n"
"ds_read_b64 v[86:87], v24 offset:8\n"
"v_mfma_f32_16x16x32_f16 v[64:67], v[96:99], v[144:147], 0\n"
"ds_read_b128 v[76:79], v25 offset:512\n"
"v_mfma_f32_16x16x32_f16 v[68:71], v[44:47], v[148:151], 0\n"
"ds_read_b128 v[44:47], v25 offset:1024\n"
"s_waitcnt lgkmcnt(8)\n"
"v_mfma_f32_16x16x32_f16 v[60:63], v[152:155], v[96:99], v[60:63]\n"
"ds_read_b128 v[96:99], v25 offset:1536\n"
"v_mfma_f32_16x16x32_f16 v[48:51], v[88:91], v[72:75], v[48:51]\n"
"ds_read_b64 v[72:73], v24 offset:512\n"
"v_mfma_f32_16x16x32_f16 v[52:55], v[88:91], v[40:43], v[52:55]\n"
"ds_read_b64 v[74:75], v24 offset:520\n"
"v_mfma_f32_16x16x32_f16 v[56:59], v[88:91], v[104:107], v[56:59]\n"
"ds_read_b64 v[88:89], v24 offset:1024\n"
"s_waitcnt lgkmcnt(5)\n"
"v_mfma_f32_16x16x32_f16 v[52:55], v[92:95], v[104:107], v[52:55]\n"
"ds_read_b64 v[90:91], v24 offset:1032\n"
"v_cvt_pk_f16_f32 v80, v64, v65\n"
"v_mfma_f32_16x16x32_f16 v[48:51], v[92:95], v[40:43], v[48:51]\n"
"v_cvt_pk_f16_f32 v81, v66, v67\n"
"v_cvt_pk_f16_f32 v82, v68, v69\n"
"v_mfma_f32_16x16x32_f16 v[48:51], v[100:103], v[104:107], v[48:51]\n"
"v_cvt_pk_f16_f32 v83, v70, v71\n"
"v_mfma_f32_16x16x32_f16 v[48:51], v[84:87], v[76:79], v[48:51]\n"
"s_waitcnt lgkmcnt(2)\n"
"v_mfma_f32_16x16x32_f16 v[52:55], v[84:87], v[44:47], v[52:55]\n"
"v_mfma_f32_16x16x32_f16 v[56:59], v[84:87], v[96:99], v[56:59]\n"
"v_mfma_f32_16x16x32_f16 v[52:55], v[72:75], v[96:99], v[52:55]\n"
"v_mfma_f32_16x16x32_f16 v[48:51], v[72:75], v[44:47], v[48:51]\n"
"s_waitcnt lgkmcnt(0)\n"
"v_mfma_f32_16x16x32_f16 v[48:51], v[88:91], v[96:99], v[48:51]\n"
"v_mfma_f32_16x16x32_f16 v[60:63], v[80:83], v[128:131], v[60:63]\n"
"s_setprio 2\n"
"s_mov_b32 s17, 0x100000\n"
".Ltp_429:\n"
"ds_read_b32 v3, v5 offset:256\n"
"s_sub_u32 s17, s17, 1\n"
"s_cbranch_scc1 .Lou_430\n"
"s_waitcnt lgkmcnt(0)\n"
"v_cmp_eq_u32 vcc, 0, v3\n"
"s_cbranch_vccnz .Ltp_429\n"
".Lou_430:\n"
"s_waitcnt lgkmcnt(0)\n"
"s_mov_b32 s89, 2\n"
".Ldl_431:\n"
"s_sub_u32 s89, s89, 1\n"
"s_cbranch_scc1 .Lde_432\n"
"s_sleep 1\n"
"s_branch .Ldl_431\n"
".Lde_432:\n"
"buffer_load_dwordx4 v[110:113], v126, s[4:7], s32 offen offset:0 sc1\n"
"buffer_load_dwordx4 v[114:117], v126, s[4:7], s32 offen offset:128 sc1\n"
"buffer_load_dwordx4 v[118:121], v126, s[4:7], s33 offen offset:0 sc1\n"
"buffer_load_dwordx4 v[122:125], v126, s[4:7], s33 offen offset:128 sc1\n"
"s_mov_b32 s38, 0x05040100\n"
"s_mov_b32 s39, 0x07060302\n"
"s_mov_b32 s17, 32768\n"
".Ltp_433:\n"
"s_waitcnt vmcnt(0)\n"
"s_sub_u32 s17, s17, 1\n"
"s_cbranch_scc1 .Lou_434\n"
"v_bitop3_b32 v64, v111, v113, s16 bitop3:0x7e\n"
"v_bitop3_b32 v65, v115, v117, s16 bitop3:0x7e\n"
"v_bitop3_b32 v66, v119, v121, s16 bitop3:0x7e\n"
"v_bitop3_b32 v67, v123, v125, s16 bitop3:0x7e\n"
"v_or3_b32 v68, v64, v65, v66\n"
"v_or_b32 v68, v68, v67\n"
"v_cmp_ne_u32 vcc, 0, v68\n"
"s_cbranch_vccz .Lou_434\n"
"s_sleep 2\n"
"v_cmp_ne_u32 vcc, 0, v64\n"
"s_cbranch_vccz .Lsk_435\n"
"s_mov_b64 exec, vcc\n"
"buffer_load_dwordx4 v[110:113], v126, s[4:7], s32 offen offset:0 sc1\n"
"s_mov_b64 exec, -1\n"
".Lsk_435:\n"
"v_cmp_ne_u32 vcc, 0, v65\n"
"s_cbranch_vccz .Lsk_436\n"
"s_mov_b64 exec, vcc\n"
"buffer_load_dwordx4 v[114:117], v126, s[4:7], s32 offen offset:128 sc1\n"
"s_mov_b64 exec, -1\n"
".Lsk_436:\n"
"v_cmp_ne_u32 vcc, 0, v66\n"
"s_cbranch_vccz .Lsk_437\n"
"s_mov_b64 exec, vcc\n"
"buffer_load_dwordx4 v[118:121], v126, s[4:7], s33 offen offset:0 sc1\n"
"s_mov_b64 exec, -1\n"
".Lsk_437:\n"
"v_cmp_ne_u32 vcc, 0, v67\n"
"s_cbranch_vccz .Lsk_438\n"
"s_mov_b64 exec, vcc\n"
"buffer_load_dwordx4 v[122:125], v126, s[4:7], s33 offen offset:128 sc1\n"
"s_mov_b64 exec, -1\n"
".Lsk_438:\n"
"s_branch .Ltp_433\n"
".Lou_434:\n"
"s_waitcnt vmcnt(0)\n"
"s_mov_b32 s89, 2\n"
".Ldl_439:\n"
"s_sub_u32 s89, s89, 1\n"
"s_cbranch_scc1 .Lde_440\n"
"s_sleep 1\n"
"s_branch .Ldl_439\n"
".Lde_440:\n"
"buffer_load_dwordx4 v[88:91], v30, s[4:7], s34 offen sc1\n"
"buffer_load_dwordx4 v[92:95], v30, s[4:7], s35 offen sc1\n"
"v_perm_b32 v64, v114, v110, s38\n"
"v_perm_b32 v65, v114, v110, s39\n"
"v_perm_b32 v66, v116, v112, s38\n"
"v_perm_b32 v67, v116, v112, s39\n"
"ds_write_b32 v32, v64 offset:0\n"
"ds_write_b32 v33, v65 offset:2048\n"
"ds_write_b32 v32, v66 offset:4224\n"
"ds_write_b32 v33, v67 offset:6272\n"
"v_perm_b32 v64, v122, v118, s38\n"
"v_perm_b32 v65, v122, v118, s39\n"
"v_perm_b32 v66, v124, v120, s38\n"
"v_perm_b32 v67, v124, v120, s39\n"
"ds_write_b32 v32, v64 offset:1024\n"
"ds_write_b32 v33, v65 offset:3072\n"
"ds_write_b32 v32, v66 offset:5248\n"
"ds_write_b32 v33, v67 offset:7296\n"
"v_lshlrev_b32 v64, 16, v110\n"
"v_lshlrev_b32 v65, 16, v114\n"
"v_and_b32 v66, 0xffff0000, v110\n"
"v_and_b32 v67, 0xffff0000, v114\n"
"v_lshlrev_b32 v68, 16, v112\n"
"v_lshlrev_b32 v69, 16, v116\n"
"v_and_b32 v70, 0xffff0000, v112\n"
"v_and_b32 v71, 0xffff0000, v116\n"
"v_lshlrev_b32 v72, 16, v118\n"
"v_lshlrev_b32 v73, 16, v122\n"
"v_and_b32 v74, 0xffff0000, v118\n"
"v_and_b32 v75, 0xffff0000, v122\n"
"v_lshlrev_b32 v76, 16, v120\n"
"v_lshlrev_b32 v77, 16, v124\n"
"v_and_b32 v78, 0xffff0000, v120\n"
"v_and_b32 v79, 0xffff0000, v124\n"
"s_mov_b32 s17, 32768\n"
".Ltp_441:\n"
"s_waitcnt vmcnt(0)\n"
"s_sub_u32 s17, s17, 1\n"
"s_cbranch_scc1 .Lou_442\n"
"v_max_u32 v110, v88, v89\n"
"v_max3_u32 v110, v90, v91, v110\n"
"v_max3_u32 v110, v92, v93, v110\n"
"v_max3_u32 v110, v94, v95, v110\n"
"v_cmp_eq_u32 vcc, -1, v110\n"
"s_cbranch_vccz .Lou_442\n"
"v_max_u32 v110, v90, v91\n"
"v_max3_u32 v110, v88, v89, v110\n"
"v_cmp_eq_u32 vcc, -1, v110\n"
"s_cbranch_vccz .Lsk_443\n"
"s_mov_b64 exec, vcc\n"
"buffer_load_dwordx4 v[88:91], v30, s[4:7], s34 offen sc1\n"
"s_mov_b64 exec, -1\n"
".Lsk_443:\n"
"v_max_u32 v110, v94, v95\n"
"v_max3_u32 v110, v92, v93, v110\n"
"v_cmp_eq_u32 vcc, -1, v110\n"
"s_cbranch_vccz .Lsk_444\n"
"s_mov_b64 exec, vcc\n"
"buffer_load_dwordx4 v[92:95], v30, s[4:7], s35 offen sc1\n"
"s_mov_b64 exec, -1\n"
".Lsk_444:\n"
"s_branch .Ltp_441\n"
".Lou_442:\n"
"s_waitcnt vmcnt(0)\n"
"v_fma_f32 v64, v64, v89, v88\n"
"v_fma_f32 v65, v65, v91, v90\n"
"v_cvt_pk_f16_f32 v64, v64, v65\n"
"v_fma_f32 v66, v66, v89, v88\n"
"v_fma_f32 v67, v67, v91, v90\n"
"v_cvt_pk_f16_f32 v66, v66, v67\n"
"v_fma_f32 v68, v68, v89, v88\n"
"v_fma_f32 v69, v69, v91, v90\n"
"v_cvt_pk_f16_f32 v68, v68, v69\n"
"v_fma_f32 v70, v70, v89, v88\n"
"v_fma_f32 v71, v71, v91, v90\n"
"v_cvt_pk_f16_f32 v70, v70, v71\n"
"ds_write2st64_b32 v31, v64, v66 offset0:0 offset1:18\n"
"ds_write2st64_b32 v31, v68, v70 offset0:36 offset1:54\n"
"v_fma_f32 v72, v72, v93, v92\n"
"v_fma_f32 v73, v73, v95, v94\n"
"v_cvt_pk_f16_f32 v72, v72, v73\n"
"v_fma_f32 v74, v74, v93, v92\n"
"v_fma_f32 v75, v75, v95, v94\n"
"v_cvt_pk_f16_f32 v74, v74, v75\n"
"v_fma_f32 v76, v76, v93, v92\n"
"v_fma_f32 v77, v77, v95, v94\n"
"v_cvt_pk_f16_f32 v76, v76, v77\n"
"v_fma_f32 v78, v78, v93, v92\n"
"v_fma_f32 v79, v79, v95, v94\n"
"v_cvt_pk_f16_f32 v78, v78, v79\n"
"ds_write2st64_b32 v31, v72, v74 offset0:4 offset1:22\n"
"ds_write2st64_b32 v31, v76, v78 offset0:40 offset1:58\n"
"ds_write_b32 v4, v4 offset:672\n"
"s_mov_b32 s17, 0x100000\n"
".Ltp_445:\n"
"ds_read_b32 v3, v5 offset:672\n"
"s_sub_u32 s17, s17, 1\n"
"s_cbranch_scc1 .Lou_446\n"
"s_waitcnt lgkmcnt(0)\n"
"v_cmp_eq_u32 vcc, 0, v3\n"
"s_cbranch_vccnz .Ltp_445\n"
".Lou_446:\n"
"s_waitcnt lgkmcnt(0)\n"
"s_mov_b32 s36, 0xfffefffe\n"
"ds_read_b64 v[88:89], v24 offset:1984\n"
"ds_read_b64 v[90:91], v24 offset:1992\n"
"ds_read_b128 v[92:95], v25 offset:4032\n"
"ds_read_b128 v[96:99], v25 offset:3520\n"
"ds_read_b64 v[100:101], v24 offset:1472\n"
"ds_read_b64 v[102:103], v24 offset:1480\n"
"ds_read_b128 v[104:107], v25 offset:3008\n"
"s_waitcnt lgkmcnt(0)\n"
"v_mfma_f32_16x16x32_f16 v[60:63], v[88:91], v[92:95], v[60:63]\n"
"ds_read_b64 v[108:109], v24 offset:960\n"
"ds_read_b64 v[110:111], v24 offset:968\n"
"v_mfma_f32_16x16x32_f16 v[56:59], v[88:91], v[96:99], v[56:59]\n"
"ds_read_b128 v[112:115], v25 offset:2496\n"
"ds_read_b64 v[116:117], v24 offset:448\n"
"v_mfma_f32_16x16x32_f16 v[60:63], v[100:103], v[96:99], v[60:63]\n"
"ds_read_b64 v[118:119], v24 offset:456\n"
"ds_read_b64 v[120:121], v24 offset:1920\n"
"v_mfma_f32_16x16x32_f16 v[52:55], v[88:91], v[104:107], v[52:55]\n"
"ds_read_b64 v[122:123], v24 offset:1928\n"
"s_waitcnt lgkmcnt(4)\n"
"v_mfma_f32_16x16x32_f16 v[56:59], v[100:103], v[104:107], v[56:59]\n"
"ds_read_b128 v[124:127], v25 offset:3968\n"
"ds_read_b128 v[40:43], v25 offset:3456\n"
"v_mfma_f32_16x16x32_f16 v[60:63], v[108:111], v[104:107], v[60:63]\n"
"ds_read_b64 v[44:45], v24 offset:1408\n"
"ds_read_b64 v[46:47], v24 offset:1416\n"
"v_mfma_f32_16x16x32_f16 v[48:51], v[88:91], v[112:115], v[48:51]\n"
"ds_read_b128 v[92:95], v25 offset:2944\n"
"ds_read_b64 v[96:97], v24 offset:896\n"
"v_mfma_f32_16x16x32_f16 v[52:55], v[100:103], v[112:115], v[52:55]\n"
"ds_read_b64 v[98:99], v24 offset:904\n"
"s_waitcnt lgkmcnt(5)\n"
"v_mfma_f32_16x16x32_f16 v[56:59], v[108:111], v[112:115], v[56:59]\n"
"ds_read_b128 v[104:107], v25 offset:2432\n"
"ds_read_b64 v[88:89], v24 offset:384\n"
"v_mfma_f32_16x16x32_f16 v[60:63], v[116:119], v[112:115], v[60:63]\n"
"ds_read_b64 v[90:91], v24 offset:392\n"
"ds_read_b64 v[100:101], v24 offset:1856\n"
"v_mfma_f32_16x16x32_f16 v[60:63], v[120:123], v[124:127], v[60:63]\n"
"ds_read_b64 v[102:103], v24 offset:1864\n"
"ds_read_b128 v[108:111], v25 offset:3904\n"
"v_mfma_f32_16x16x32_f16 v[56:59], v[120:123], v[40:43], v[56:59]\n"
"ds_read_b128 v[112:115], v25 offset:3392\n"
"s_waitcnt lgkmcnt(7)\n"
"v_mfma_f32_16x16x32_f16 v[60:63], v[44:47], v[40:43], v[60:63]\n"
"ds_read_b64 v[116:117], v26 offset:0\n"
"ds_read_b64 v[124:125], v24 offset:1344\n"
"v_mfma_f32_16x16x32_f16 v[52:55], v[120:123], v[92:95], v[52:55]\n"
"ds_read_b64 v[126:127], v24 offset:1352\n"
"v_mfma_f32_16x16x32_f16 v[56:59], v[44:47], v[92:95], v[56:59]\n"
"ds_read_b128 v[40:43], v25 offset:2880\n"
"v_mfma_f32_16x16x32_f16 v[60:63], v[96:99], v[92:95], v[60:63]\n"
"ds_read_b64 v[92:93], v24 offset:832\n"
"s_waitcnt lgkmcnt(9)\n"
"v_mfma_f32_16x16x32_f16 v[48:51], v[120:123], v[104:107], v[48:51]\n"
"ds_read_b64 v[94:95], v24 offset:840\n"
"v_mfma_f32_16x16x32_f16 v[52:55], v[44:47], v[104:107], v[52:55]\n"
"ds_read_b128 v[120:123], v25 offset:2368\n"
"v_mfma_f32_16x16x32_f16 v[56:59], v[96:99], v[104:107], v[56:59]\n"
"ds_read_b64 v[44:45], v24 offset:320\n"
"v_mfma_f32_16x16x32_f16 v[60:63], v[88:91], v[104:107], v[60:63]\n"
"ds_read_b64 v[46:47], v24 offset:328\n"
"s_waitcnt lgkmcnt(6)\n"
"v_mfma_f32_16x16x32_f16 v[60:63], v[100:103], v[108:111], v[60:63]\n"
"ds_read_b64 v[96:97], v26 offset:512\n"
"ds_read_b64 v[104:105], v24 offset:1792\n"
"v_mfma_f32_16x16x32_f16 v[56:59], v[100:103], v[112:115], v[56:59]\n"
"ds_read_b64 v[106:107], v24 offset:1800\n"
"ds_read_b128 v[88:91], v25 offset:3840\n"
"v_mfma_f32_16x16x16_bf16 v[48:51], v[34:35], v[116:117], v[48:51]\n"
"ds_read_b128 v[108:111], v25 offset:3328\n"
"ds_read_b64 v[116:117], v24 offset:1280\n"
"v_mfma_f32_16x16x32_f16 v[60:63], v[124:127], v[112:115], v[60:63]\n"
"ds_read_b64 v[118:119], v24 offset:1288\n"
"s_waitcnt lgkmcnt(9)\n"
"v_mfma_f32_16x16x32_f16 v[52:55], v[100:103], v[40:43], v[52:55]\n"
"ds_read_b128 v[112:115], v25 offset:2816\n"
"s_and_b32 s79, s18, 0xfc\n"
"v_mfma_f32_16x16x32_f16 v[56:59], v[124:127], v[40:43], v[56:59]\n"
"s_and_b32 s78, s18, 1\n"
"s_lshl_b32 s78, s78, 1\n"
"v_mfma_f32_16x16x32_f16 v[60:63], v[92:95], v[40:43], v[60:63]\n"
"ds_read_b64 v[40:41], v24 offset:768\n"
"s_add_u32 s79, s79, s78\n"
"v_mfma_f32_16x16x32_f16 v[48:51], v[100:103], v[120:123], v[48:51]\n"
"ds_read_b64 v[42:43], v24 offset:776\n"
"s_waitcnt lgkmcnt(9)\n"
"v_mfma_f32_16x16x32_f16 v[52:55], v[124:127], v[120:123], v[52:55]\n"
"ds_read_b128 v[100:103], v25 offset:2304\n"
"s_lshr_b32 s78, s18, 1\n"
"v_mfma_f32_16x16x32_f16 v[56:59], v[92:95], v[120:123], v[56:59]\n"
"ds_read_b64 v[124:125], v24 offset:256\n"
"s_and_b32 s78, s78, 1\n"
"v_mfma_f32_16x16x32_f16 v[60:63], v[44:47], v[120:123], v[60:63]\n"
"ds_read_b64 v[126:127], v24 offset:264\n"
"ds_read_b64 v[92:93], v24 offset:1728\n"
"v_mfma_f32_16x16x16_bf16 v[52:55], v[34:35], v[96:97], v[52:55]\n"
"ds_read_b64 v[94:95], v24 offset:1736\n"
"s_waitcnt lgkmcnt(7)\n"
"v_mfma_f32_16x16x32_f16 v[60:63], v[104:107], v[88:91], v[60:63]\n"
"ds_read_b128 v[120:123], v25 offset:3776\n"
"ds_read_b128 v[44:47], v25 offset:3264\n"
"v_mfma_f32_16x16x32_f16 v[56:59], v[104:107], v[108:111], v[56:59]\n"
"ds_read_b64 v[96:97], v24 offset:1216\n"
"ds_read_b64 v[98:99], v24 offset:1224\n"
"v_mfma_f32_16x16x32_f16 v[60:63], v[116:119], v[108:111], v[60:63]\n"
"ds_read_b128 v[88:91], v25 offset:2752\n"
"s_add_u32 s79, s79, s78\n"
"v_mfma_f32_16x16x32_f16 v[52:55], v[104:107], v[112:115], v[52:55]\n"
"ds_read_b64 v[108:109], v26 offset:1024\n"
"s_waitcnt lgkmcnt(10)\n"
"v_mfma_f32_16x16x32_f16 v[56:59], v[116:119], v[112:115], v[56:59]\n"
"s_lshl_b32 s79, s79, 7\n"
"s_add_u32 s32, s79, 0xc88000\n"
"v_mfma_f32_16x16x32_f16 v[60:63], v[40:43], v[112:115], v[60:63]\n"
"ds_read_b64 v[112:113], v24 offset:704\n"
"v_mfma_f32_16x16x32_f16 v[48:51], v[104:107], v[100:103], v[48:51]\n"
"ds_read_b64 v[114:115], v24 offset:712\n"
"v_mfma_f32_16x16x32_f16 v[52:55], v[116:119], v[100:103], v[52:55]\n"
"ds_read_b128 v[104:107], v25 offset:2240\n"
"s_waitcnt lgkmcnt(7)\n"
"v_mfma_f32_16x16x32_f16 v[56:59], v[40:43], v[100:103], v[56:59]\n"
"ds_read_b64 v[116:117], v24 offset:192\n"
"v_mfma_f32_16x16x32_f16 v[60:63], v[124:127], v[100:103], v[60:63]\n"
"ds_read_b64 v[118:119], v24 offset:200\n"
"ds_read_b64 v[40:41], v24 offset:1664\n"
"v_mfma_f32_16x16x32_f16 v[60:63], v[92:95], v[120:123], v[60:63]\n"
"ds_read_b64 v[42:43], v24 offset:1672\n"
"ds_read_b128 v[100:103], v25 offset:3712\n"
"v_mfma_f32_16x16x32_f16 v[56:59], v[92:95], v[44:47], v[56:59]\n"
"ds_read_b128 v[124:127], v25 offset:3200\n"
"s_waitcnt lgkmcnt(9)\n"
"v_mfma_f32_16x16x32_f16 v[60:63], v[96:99], v[44:47], v[60:63]\n"
"ds_read_b64 v[120:121], v24 offset:1152\n"
"ds_read_b64 v[122:123], v24 offset:1160\n"
"v_mfma_f32_16x16x32_f16 v[52:55], v[92:95], v[88:91], v[52:55]\n"
"ds_read_b128 v[44:47], v25 offset:2688\n"
"v_mfma_f32_16x16x16_bf16 v[56:59], v[34:35], v[108:109], v[56:59]\n"
"ds_read_b64 v[108:109], v24 offset:640\n"
"s_nop 4\n"
"v_mfma_f32_16x16x32_f16 v[56:59], v[96:99], v[88:91], v[56:59]\n"
"ds_read_b64 v[110:111], v24 offset:648\n"
"s_waitcnt lgkmcnt(11)\n"
"v_mfma_f32_16x16x32_f16 v[60:63], v[112:115], v[88:91], v[60:63]\n"
"ds_read_b128 v[88:91], v25 offset:2176\n"
"v_mfma_f32_16x16x32_f16 v[48:51], v[92:95], v[104:107], v[48:51]\n"
"ds_read_b64 v[92:93], v26 offset:1536\n"
"v_mfma_f32_16x16x32_f16 v[52:55], v[96:99], v[104:107], v[52:55]\n"
"v_mfma_f32_16x16x32_f16 v[56:59], v[112:115], v[104:107], v[56:59]\n"
"ds_read_b64 v[96:97], v24 offset:128\n"
"s_waitcnt lgkmcnt(6)\n"
"v_mfma_f32_16x16x32_f16 v[60:63], v[116:119], v[104:107], v[60:63]\n"
"ds_read_b64 v[98:99], v24 offset:136\n"
"ds_read_b64 v[112:113], v24 offset:1600\n"
"v_mfma_f32_16x16x32_f16 v[60:63], v[40:43], v[100:103], v[60:63]\n"
"ds_read_b64 v[114:115], v24 offset:1608\n"
"ds_read_b128 v[104:107], v25 offset:3648\n"
"v_mfma_f32_16x16x32_f16 v[56:59], v[40:43], v[124:127], v[56:59]\n"
"ds_read_b128 v[116:119], v25 offset:3136\n"
"ds_read_b64 v[100:101], v24 offset:1088\n"
"v_mfma_f32_16x16x32_f16 v[60:63], v[120:123], v[124:127], v[60:63]\n"
"ds_read_b64 v[102:103], v24 offset:1096\n"
"s_waitcnt lgkmcnt(9)\n"
"v_mfma_f32_16x16x32_f16 v[52:55], v[40:43], v[44:47], v[52:55]\n"
"ds_read_b128 v[124:127], v25 offset:2624\n"
"v_mfma_f32_16x16x32_f16 v[56:59], v[120:123], v[44:47], v[56:59]\n"
"v_mfma_f32_16x16x32_f16 v[60:63], v[108:111], v[44:47], v[60:63]\n"
"ds_read_b64 v[44:45], v24 offset:576\n"
"v_mfma_f32_16x16x32_f16 v[48:51], v[40:43], v[88:91], v[48:51]\n"
"ds_read_b64 v[46:47], v24 offset:584\n"
"s_waitcnt lgkmcnt(9)\n"
"s_nop 0\n"
"v_mfma_f32_16x16x16_bf16 v[60:63], v[34:35], v[92:93], v[60:63]\n"
"ds_read_b128 v[40:43], v25 offset:2112\n"
"v_mfma_f32_16x16x32_f16 v[52:55], v[120:123], v[88:91], v[52:55]\n"
"ds_read_b64 v[92:93], v24 offset:64\n"
"v_mfma_f32_16x16x32_f16 v[56:59], v[108:111], v[88:91], v[56:59]\n"
"ds_read_b64 v[94:95], v24 offset:72\n"
"ds_read_b64 v[120:121], v24 offset:1536\n"
"v_mfma_f32_16x16x32_f16 v[60:63], v[96:99], v[88:91], v[60:63]\n"
"ds_read_b64 v[122:123], v24 offset:1544\n"
"s_waitcnt lgkmcnt(7)\n"
"v_mfma_f32_16x16x32_f16 v[60:63], v[112:115], v[104:107], v[60:63]\n"
"ds_read_b128 v[108:111], v25 offset:2048\n"
"ds_read_b128 v[88:91], v25 offset:2560\n"
"v_mfma_f32_16x16x32_f16 v[56:59], v[112:115], v[116:119], v[56:59]\n"
"ds_read_b64 v[96:97], v24 offset:1024\n"
"ds_read_b64 v[98:99], v24 offset:1032\n"
"v_mfma_f32_16x16x32_f16 v[60:63], v[100:103], v[116:119], v[60:63]\n"
"ds_read_b128 v[104:107], v25 offset:3072\n"
"v_mfma_f32_16x16x32_f16 v[52:55], v[112:115], v[124:127], v[52:55]\n"
"ds_read_b64 v[116:117], v24 offset:512\n"
"s_waitcnt lgkmcnt(10)\n"
"v_mfma_f32_16x16x32_f16 v[56:59], v[100:103], v[124:127], v[56:59]\n"
"ds_read_b64 v[118:119], v24 offset:520\n"
"v_mfma_f32_16x16x32_f16 v[60:63], v[44:47], v[124:127], v[60:63]\n"
"ds_read_b128 v[124:127], v25 offset:3584\n"
"v_mfma_f32_16x16x32_f16 v[48:51], v[112:115], v[40:43], v[48:51]\n"
"v_mfma_f32_16x16x32_f16 v[52:55], v[100:103], v[40:43], v[52:55]\n"
"ds_read_b64 v[112:113], v24 offset:0\n"
"s_waitcnt lgkmcnt(7)\n"
"v_mfma_f32_16x16x32_f16 v[56:59], v[44:47], v[40:43], v[56:59]\n"
"ds_read_b64 v[114:115], v24 offset:8\n"
"v_mfma_f32_16x16x32_f16 v[60:63], v[92:95], v[40:43], v[60:63]\n"
"v_mfma_f32_16x16x32_f16 v[48:51], v[120:123], v[108:111], v[48:51]\n"
"v_mfma_f32_16x16x32_f16 v[52:55], v[120:123], v[88:91], v[52:55]\n"
"s_waitcnt lgkmcnt(3)\n"
"v_mfma_f32_16x16x32_f16 v[52:55], v[96:99], v[108:111], v[52:55]\n"
"v_mfma_f32_16x16x32_f16 v[56:59], v[120:123], v[104:107], v[56:59]\n"
"v_mul_f32 v48, v36, v48\n"
"v_mul_f32 v49, v36, v49\n"
"v_mul_f32 v50, v36, v50\n"
"v_mfma_f32_16x16x32_f16 v[56:59], v[96:99], v[88:91], v[56:59]\n"
"v_mul_f32 v51, v36, v51\n"
"v_cvt_pk_bf16_f32 v48, v48, v49\n"
"v_cvt_pk_bf16_f32 v49, v50, v51\n"
"v_mfma_f32_16x16x32_f16 v[56:59], v[116:119], v[108:111], v[56:59]\n"
"v_pk_min_u16 v48, v48, s36\n"
"v_pk_min_u16 v49, v49, s36\n"
"s_waitcnt lgkmcnt(0)\n"
"v_mfma_f32_16x16x32_f16 v[60:63], v[120:123], v[124:127], v[60:63]\n"
"ds_write_b64 v26, v[48:49] offset:0\n"
"v_mfma_f32_16x16x32_f16 v[60:63], v[96:99], v[104:107], v[60:63]\n"
"v_mul_f32 v52, v36, v52\n"
"v_mul_f32 v53, v36, v53\n"
"v_mfma_f32_16x16x32_f16 v[60:63], v[116:119], v[88:91], v[60:63]\n"
"v_mul_f32 v54, v36, v54\n"
"v_mul_f32 v55, v36, v55\n"
"v_mfma_f32_16x16x32_f16 v[60:63], v[112:115], v[108:111], v[60:63]\n"
"v_cvt_pk_bf16_f32 v52, v52, v53\n"
"v_cvt_pk_bf16_f32 v53, v54, v55\n"
"v_pk_min_u16 v52, v52, s36\n"
"v_pk_min_u16 v53, v53, s36\n"
"ds_write_b64 v26, v[52:53] offset:512\n"
"v_mul_f32 v56, v36, v56\n"
"v_mul_f32 v57, v36, v57\n"
"v_mul_f32 v58, v36, v58\n"
"v_mul_f32 v59, v36, v59\n"
"v_cvt_pk_bf16_f32 v56, v56, v57\n"
"v_cvt_pk_bf16_f32 v57, v58, v59\n"
"v_pk_min_u16 v56, v56, s36\n"
"v_pk_min_u16 v57, v57, s36\n"
"ds_write_b64 v26, v[56:57] offset:1024\n"
"v_mul_f32 v60, v36, v60\n"
"v_mul_f32 v61, v36, v61\n"
"v_mul_f32 v62, v36, v62\n"
"v_mul_f32 v63, v36, v63\n"
"v_cvt_pk_bf16_f32 v60, v60, v61\n"
"v_cvt_pk_bf16_f32 v61, v62, v63\n"
"v_pk_min_u16 v60, v60, s36\n"
"v_pk_min_u16 v61, v61, s36\n"
"ds_write_b64 v26, v[60:61] offset:1536\n"
"ds_write_b32 v4, v4 offset:688\n"
"s_mov_b32 s17, 0x100000\n"
".Ltp_447:\n"
"ds_read_b32 v3, v5 offset:688\n"
"s_sub_u32 s17, s17, 1\n"
"s_cbranch_scc1 .Lou_448\n"
"s_waitcnt lgkmcnt(0)\n"
"v_cmp_eq_u32 vcc, 0, v3\n"
"s_cbranch_vccnz .Ltp_447\n"
".Lou_448:\n"
"s_waitcnt lgkmcnt(0)\n"
"ds_read_b64_tr_b16 v[64:65], v27 offset:0\n"
"ds_read_b64_tr_b16 v[66:67], v28 offset:0\n"
"ds_read_b64_tr_b16 v[68:69], v27 offset:1024\n"
"ds_read_b64_tr_b16 v[70:71], v28 offset:1024\n"
"s_add_u32 s33, s32, 0xa0000\n"
"s_waitcnt lgkmcnt(2)\n"
"buffer_store_dwordx4 v[64:67], v29, s[4:7], s32 offen sc1\n"
"s_waitcnt lgkmcnt(0)\n"
"buffer_store_dwordx4 v[68:71], v29, s[4:7], s33 offen sc1\n"
"ds_write_b32 v4, v4 offset:704\n"
"s_nop 0\n"
"s_branch .Ljc_449\n"
".Lnc_422:\n"
"s_mov_b32 s17, 0x100000\n"
".Ltp_450:\n"
"s_sleep 2\n"
"ds_read_b32 v3, v5 offset:272\n"
"s_sub_u32 s17, s17, 1\n"
"s_cbranch_scc1 .Lou_451\n"
"s_waitcnt lgkmcnt(0)\n"
"v_cmp_eq_u32 vcc, 0, v3\n"
"s_cbranch_vccnz .Ltp_450\n"
".Lou_451:\n"
"s_waitcnt lgkmcnt(0)\n"
"s_and_b32 s79, s2, 7\n"
"s_lshl_b32 s79, s79, 14\n"
"s_add_u32 s32, s79, 0xb2a000\n"
"s_add_u32 s33, s32, 0x1000\n"
"buffer_load_dwordx4 v[48:51], v2, s[4:7], s32 offen offset:0 sc1\n"
"buffer_load_dwordx4 v[52:55], v2, s[4:7], s32 offen offset:1024 sc1\n"
"buffer_load_dwordx4 v[56:59], v2, s[4:7], s32 offen offset:2048 sc1\n"
"buffer_load_dwordx4 v[60:63], v2, s[4:7], s32 offen offset:3072 sc1\n"
"buffer_load_dwordx4 v[64:67], v2, s[4:7], s33 offen offset:0 sc1\n"
"buffer_load_dwordx4 v[68:71], v2, s[4:7], s33 offen offset:1024 sc1\n"
"buffer_load_dwordx4 v[72:75], v2, s[4:7], s33 offen offset:2048 sc1\n"
"buffer_load_dwordx4 v[76:79], v2, s[4:7], s33 offen offset:3072 sc1\n"
"s_mov_b32 s17, 32768\n"
".Ltp_452:\n"
"s_waitcnt vmcnt(0)\n"
"s_sub_u32 s17, s17, 1\n"
"s_cbranch_scc1 .Lou_453\n"
"v_max_u32 v80, v48, v49\n"
"v_max3_u32 v80, v50, v51, v80\n"
"v_max3_u32 v80, v52, v53, v80\n"
"v_max3_u32 v80, v54, v55, v80\n"
"v_max3_u32 v80, v56, v57, v80\n"
"v_max3_u32 v80, v58, v59, v80\n"
"v_max3_u32 v80, v60, v61, v80\n"
"v_max3_u32 v80, v62, v63, v80\n"
"v_max3_u32 v80, v64, v65, v80\n"
"v_max3_u32 v80, v66, v67, v80\n"
"v_max3_u32 v80, v68, v69, v80\n"
"v_max3_u32 v80, v70, v71, v80\n"
"v_max3_u32 v80, v72, v73, v80\n"
"v_max3_u32 v80, v74, v75, v80\n"
"v_max3_u32 v80, v76, v77, v80\n"
"v_max3_u32 v80, v78, v79, v80\n"
"v_cmp_eq_u32 vcc, -1, v80\n"
"s_cbranch_vccz .Lou_453\n"
"s_sleep 4\n"
"v_max_u32 v80, v50, v51\n"
"v_max3_u32 v80, v48, v49, v80\n"
"v_cmp_eq_u32 vcc, -1, v80\n"
"s_cbranch_vccz .Lsk_454\n"
"s_mov_b64 exec, vcc\n"
"buffer_load_dwordx4 v[48:51], v2, s[4:7], s32 offen offset:0 sc1\n"
"s_mov_b64 exec, -1\n"
".Lsk_454:\n"
"v_max_u32 v80, v54, v55\n"
"v_max3_u32 v80, v52, v53, v80\n"
"v_cmp_eq_u32 vcc, -1, v80\n"
"s_cbranch_vccz .Lsk_455\n"
"s_mov_b64 exec, vcc\n"
"buffer_load_dwordx4 v[52:55], v2, s[4:7], s32 offen offset:1024 sc1\n"
"s_mov_b64 exec, -1\n"
".Lsk_455:\n"
"v_max_u32 v80, v58, v59\n"
"v_max3_u32 v80, v56, v57, v80\n"
"v_cmp_eq_u32 vcc, -1, v80\n"
"s_cbranch_vccz .Lsk_456\n"
"s_mov_b64 exec, vcc\n"
"buffer_load_dwordx4 v[56:59], v2, s[4:7], s32 offen offset:2048 sc1\n"
"s_mov_b64 exec, -1\n"
".Lsk_456:\n"
"v_max_u32 v80, v62, v63\n"
"v_max3_u32 v80, v60, v61, v80\n"
"v_cmp_eq_u32 vcc, -1, v80\n"
"s_cbranch_vccz .Lsk_457\n"
"s_mov_b64 exec, vcc\n"
"buffer_load_dwordx4 v[60:63], v2, s[4:7], s32 offen offset:3072 sc1\n"
"s_mov_b64 exec, -1\n"
".Lsk_457:\n"
"v_max_u32 v80, v66, v67\n"
"v_max3_u32 v80, v64, v65, v80\n"
"v_cmp_eq_u32 vcc, -1, v80\n"
"s_cbranch_vccz .Lsk_458\n"
"s_mov_b64 exec, vcc\n"
"buffer_load_dwordx4 v[64:67], v2, s[4:7], s33 offen offset:0 sc1\n"
"s_mov_b64 exec, -1\n"
".Lsk_458:\n"
"v_max_u32 v80, v70, v71\n"
"v_max3_u32 v80, v68, v69, v80\n"
"v_cmp_eq_u32 vcc, -1, v80\n"
"s_cbranch_vccz .Lsk_459\n"
"s_mov_b64 exec, vcc\n"
"buffer_load_dwordx4 v[68:71], v2, s[4:7], s33 offen offset:1024 sc1\n"
"s_mov_b64 exec, -1\n"
".Lsk_459:\n"
"v_max_u32 v80, v74, v75\n"
"v_max3_u32 v80, v72, v73, v80\n"
"v_cmp_eq_u32 vcc, -1, v80\n"
"s_cbranch_vccz .Lsk_460\n"
"s_mov_b64 exec, vcc\n"
"buffer_load_dwordx4 v[72:75], v2, s[4:7], s33 offen offset:2048 sc1\n"
"s_mov_b64 exec, -1\n"
".Lsk_460:\n"
"v_max_u32 v80, v78, v79\n"
"v_max3_u32 v80, v76, v77, v80\n"
"v_cmp_eq_u32 vcc, -1, v80\n"
"s_cbranch_vccz .Lsk_461\n"
"s_mov_b64 exec, vcc\n"
"buffer_load_dwordx4 v[76:79], v2, s[4:7], s33 offen offset:3072 sc1\n"
"s_mov_b64 exec, -1\n"
".Lsk_461:\n"
"s_branch .Ltp_452\n"
".Lou_453:\n"
"s_waitcnt vmcnt(0)\n"
".Ljc_449:\n"
"s_setprio 0\n"
"s_lshr_b32 s79, s2, 1\n"
"s_and_b32 s79, s79, 3\n"
"s_lshl_b32 s79, s79, 2\n"
"s_lshr_b32 s78, s2, 3\n"
"s_lshr_b32 s78, s78, 3\n"
"s_add_u32 s79, s79, s78\n"
"s_lshl_b32 s79, s79, 2\n"
"s_add_u32 s79, s79, 64\n"
"s_mul_i32 s79, s79, 0x5000\n"
"s_mul_i32 s78, s3, 0x1400\n"
"s_add_u32 s79, s79, s78\n"
"s_add_u32 s50, s79, 0xb48000\n"
"s_add_u32 s51, s50, 0x1000\n"
"v_min_i32 v99, 3, v0\n"
"v_mul_i32_i24 v48, 0x500, v99\n"
"s_mul_i32 s79, s3, 0x1400\n"
"s_add_u32 s79, s79, 0xa000\n"
"v_add_u32 v50, s79, v2\n"
"s_and_b32 s79, s2, 7\n"
"s_lshl_b32 s79, s79, 3\n"
"s_add_u32 s79, s79, 0x47\n"
"s_mul_i32 s79, s79, 0x5000\n"
"s_and_b32 s78, s2, 7\n"
"s_lshr_b32 s78, s78, 1\n"
"s_lshl_b32 s78, s78, 7\n"
"s_add_u32 s79, s79, s78\n"
"s_mul_i32 s78, s3, 0x1400\n"
"s_add_u32 s79, s79, s78\n"
"s_add_u32 s49, s79, 0xb48000\n"
"s_mov_b32 s17, 0x100000\n"
".Ltp_462:\n"
"s_sleep 2\n"
"ds_read_b32 v3, v5 offset:3264\n"
"s_sub_u32 s17, s17, 1\n"
"s_cbranch_scc1 .Lou_463\n"
"s_waitcnt lgkmcnt(0)\n"
"v_cmp_eq_u32 vcc, -1, v3\n"
"s_cbranch_vccnz .Ltp_462\n"
".Lou_463:\n"
"s_waitcnt lgkmcnt(0)\n"
"s_mov_b32 s89, 8\n"
".Ldl_464:\n"
"s_sub_u32 s89, s89, 1\n"
"s_cbranch_scc1 .Lde_465\n"
"s_sleep 1\n"
"s_branch .Ldl_464\n"
".Lde_465:\n"
"s_mov_b32 s17, 32768\n"
".Ltp_466:\n"
"buffer_load_dword v49, v48, s[4:7], s49 offen sc1\n"
"s_sub_u32 s17, s17, 1\n"
"s_cbranch_scc1 .Lou_467\n"
"s_waitcnt vmcnt(0)\n"
"v_cmp_eq_u32 vcc, -1, v49\n"
"s_cbranch_vccz .Lou_467\n"
"s_mov_b32 s89, 2\n"
".Ldl_468:\n"
"s_sub_u32 s89, s89, 1\n"
"s_cbranch_scc1 .Lde_469\n"
"s_sleep 1\n"
"s_branch .Ldl_468\n"
".Lde_469:\n"
"s_branch .Ltp_466\n"
".Lou_467:\n"
"s_waitcnt vmcnt(0)\n"
"buffer_load_dwordx4 v[52:55], v2, s[4:7], s50 offen offset:0 sc1\n"
"buffer_load_dwordx4 v[56:59], v2, s[4:7], s50 offen offset:1024 sc1\n"
"buffer_load_dwordx4 v[60:63], v2, s[4:7], s50 offen offset:2048 sc1\n"
"buffer_load_dwordx4 v[64:67], v2, s[4:7], s50 offen offset:3072 sc1\n"
"buffer_load_dwordx4 v[68:71], v2, s[4:7], s51 offen offset:0 sc1\n"
"s_mov_b32 s17, 32768\n"
".Ltp_470:\n"
"s_waitcnt vmcnt(0)\n"
"s_sub_u32 s17, s17, 1\n"
"s_cbranch_scc1 .Lou_471\n"
"v_max_u32 v38, v52, v53\n"
"v_max3_u32 v38, v54, v55, v38\n"
"v_max3_u32 v38, v56, v57, v38\n"
"v_max3_u32 v38, v58, v59, v38\n"
"v_max3_u32 v38, v60, v61, v38\n"
"v_max3_u32 v38, v62, v63, v38\n"
"v_max3_u32 v38, v64, v65, v38\n"
"v_max3_u32 v38, v66, v67, v38\n"
"v_max3_u32 v38, v68, v69, v38\n"
"v_max3_u32 v38, v70, v71, v38\n"
"v_cmp_eq_u32 vcc, -1, v38\n"
"s_cbranch_vccz .Lou_471\n"
"s_sleep 1\n"
"v_max_u32 v38, v54, v55\n"
"v_max3_u32 v38, v52, v53, v38\n"
"v_cmp_eq_u32 vcc, -1, v38\n"
"s_cbranch_vccz .Lsk_472\n"
"s_mov_b64 exec, vcc\n"
"buffer_load_dwordx4 v[52:55], v2, s[4:7], s50 offen offset:0 sc1\n"
"s_mov_b64 exec, -1\n"
".Lsk_472:\n"
"v_max_u32 v38, v58, v59\n"
"v_max3_u32 v38, v56, v57, v38\n"
"v_cmp_eq_u32 vcc, -1, v38\n"
"s_cbranch_vccz .Lsk_473\n"
"s_mov_b64 exec, vcc\n"
"buffer_load_dwordx4 v[56:59], v2, s[4:7], s50 offen offset:1024 sc1\n"
"s_mov_b64 exec, -1\n"
".Lsk_473:\n"
"v_max_u32 v38, v62, v63\n"
"v_max3_u32 v38, v60, v61, v38\n"
"v_cmp_eq_u32 vcc, -1, v38\n"
"s_cbranch_vccz .Lsk_474\n"
"s_mov_b64 exec, vcc\n"
"buffer_load_dwordx4 v[60:63], v2, s[4:7], s50 offen offset:2048 sc1\n"
"s_mov_b64 exec, -1\n"
".Lsk_474:\n"
"v_max_u32 v38, v66, v67\n"
"v_max3_u32 v38, v64, v65, v38\n"
"v_cmp_eq_u32 vcc, -1, v38\n"
"s_cbranch_vccz .Lsk_475\n"
"s_mov_b64 exec, vcc\n"
"buffer_load_dwordx4 v[64:67], v2, s[4:7], s50 offen offset:3072 sc1\n"
"s_mov_b64 exec, -1\n"
".Lsk_475:\n"
"v_max_u32 v38, v70, v71\n"
"v_max3_u32 v38, v68, v69, v38\n"
"v_cmp_eq_u32 vcc, -1, v38\n"
"s_cbranch_vccz .Lsk_476\n"
"s_mov_b64 exec, vcc\n"
"buffer_load_dwordx4 v[68:71], v2, s[4:7], s51 offen offset:0 sc1\n"
"s_mov_b64 exec, -1\n"
".Lsk_476:\n"
"s_branch .Ltp_470\n"
".Lou_471:\n"
"s_waitcnt vmcnt(0)\n"
"s_mov_b32 s17, 0x100000\n"
".Ltp_477:\n"
"s_sleep 2\n"
"ds_read_b32 v3, v5 offset:3584\n"
"s_sub_u32 s17, s17, 1\n"
"s_cbranch_scc1 .Lou_478\n"
"s_waitcnt lgkmcnt(0)\n"
"v_cmp_eq_u32 vcc, -1, v3\n"
"s_cbranch_vccnz .Ltp_477\n"
".Lou_478:\n"
"s_waitcnt lgkmcnt(0)\n"
"ds_write_b128 v50, v[52:55] offset:0\n"
"ds_write_b128 v50, v[56:59] offset:1024\n"
"ds_write_b128 v50, v[60:63] offset:2048\n"
"ds_write_b128 v50, v[64:67] offset:3072\n"
"ds_write_b128 v50, v[68:71] offset:4096\n"
"ds_write_b32 v4, v4 offset:368\n"
"s_mov_b32 s17, 0x100000\n"
".Ltp_479:\n"
"s_sleep 2\n"
"ds_read_b32 v3, v5 offset:3904\n"
"s_sub_u32 s17, s17, 1\n"
"s_cbranch_scc1 .Lou_480\n"
"s_waitcnt lgkmcnt(0)\n"
"v_cmp_eq_u32 vcc, -1, v3\n"
"s_cbranch_vccnz .Ltp_479\n"
".Lou_480:\n"
"s_waitcnt lgkmcnt(0)\n"
"s_mul_i32 s79, s3, 0x1400\n"
"s_add_u32 m0, s79, 0xf000\n"
"s_add_u32 s49, s50, 0x5000\n"
"buffer_load_dwordx4 v2, s[4:7], s49 offen offset:0 sc1 lds\n"
"buffer_load_dwordx4 v2, s[4:7], s49 offen offset:1024 sc1 lds\n"
"buffer_load_dwordx4 v2, s[4:7], s49 offen offset:2048 sc1 lds\n"
"buffer_load_dwordx4 v2, s[4:7], s49 offen offset:3072 sc1 lds\n"
"s_add_u32 m0, m0, 1024\n"
"s_add_u32 s49, s49, 1024\n"
"buffer_load_dwordx4 v2, s[4:7], s49 offen offset:3072 sc1 lds\n"
"s_setprio 2\n"
"v_add_u32 v14, v39, v14\n"
"v_add_u32 v15, v39, v15\n"
"s_mul_i32 s79, s28, 0xa00\n"
"s_add_u32 s79, s30, s79\n"
"s_and_b32 s78, s3, 1\n"
"s_mul_i32 s78, s78, 0x600\n"
"s_add_u32 s79, s79, s78\n"
"s_add_u32 s36, s79, 0xb48000\n"
"s_mul_i32 s79, s3, 0x1400\n"
"s_add_u32 s79, s30, s79\n"
"s_add_u32 s32, s79, 0xdc8000\n"
"s_add_u32 s33, s32, 0x1000\n"
"s_lshl_b32 s79, s29, 1\n"
"s_lshr_b32 s78, s3, 1\n"
"s_xor_b32 s78, s78, 1\n"
"s_add_u32 s79, s79, s78\n"
"s_mul_i32 s79, s79, 0x5000\n"
"s_mul_i32 s78, s3, 0x1400\n"
"s_add_u32 s79, s79, s78\n"
"s_add_u32 s34, s79, 0xdc8000\n"
"s_add_u32 s35, s34, 0x1000\n"
"s_lshl_b32 s79, s30, 1\n"
"s_mul_i32 s78, s28, 0x1400\n"
"s_add_u32 s79, s79, s78\n"
"s_and_b32 s78, s3, 1\n"
"s_mul_i32 s78, s78, 0xc00\n"
"s_add_u32 s79, s79, s78\n"
"s_add_u32 s37, s79, 0x1048000\n"
"s_lshl_b32 s79, s29, 1\n"
"s_lshr_b32 s78, s3, 1\n"
"s_add_u32 s79, s79, s78\n"
"s_lshl_b32 s79, s79, 11\n"
"s_lshl_b32 s78, s28, 8\n"
"s_add_u32 s79, s79, s78\n"
"s_and_b32 s78, s3, 1\n"
"s_lshl_b32 s78, s78, 7\n"
"s_add_u32 s79, s79, s78\n"
"s_add_u32 s38, s79, 0x1548000\n"
"s_mov_b32 s17, 0x100000\n"
".Ltp_481:\n"
"ds_read_b32 v3, v5 offset:256\n"
"s_sub_u32 s17, s17, 1\n"
"s_cbranch_scc1 .Lou_482\n"
"s_waitcnt lgkmcnt(0)\n"
"v_cmp_eq_u32 vcc, 0, v3\n"
"s_cbranch_vccnz .Ltp_481\n"
".Lou_482:\n"
"s_waitcnt lgkmcnt(0)\n"
"s_cmp_lt_i32 s18, 0\n"
"s_cbranch_scc1 .Lcw_483\n"
"s_mov_b32 s17, 0x100000\n"
".Ltp_484:\n"
"ds_read_b32 v3, v5 offset:704\n"
"s_sub_u32 s17, s17, 1\n"
"s_cbranch_scc1 .Lou_485\n"
"s_waitcnt lgkmcnt(0)\n"
"v_cmp_eq_u32 vcc, 0, v3\n"
"s_cbranch_vccnz .Ltp_484\n"
".Lou_485:\n"
"s_waitcnt lgkmcnt(0)\n"
".Lcw_483:\n"
"buffer_load_dwordx2 v[40:41], v14, s[4:7], s36 offen offset:0 sc1\n"
"buffer_load_dwordx2 v[42:43], v14, s[4:7], s36 offen offset:512 sc1\n"
"buffer_load_dwordx2 v[44:45], v15, s[4:7], s36 offen offset:1024 sc1\n"
"s_waitcnt vmcnt(3)\n"
"ds_write_b32 v4, v4 offset:304\n"
"s_mov_b32 s17, 0x100000\n"
".Ltp_489:\n"
"s_sleep 1\n"
"ds_read_b32 v3, v5 offset:352\n"
"s_sub_u32 s17, s17, 1\n"
"s_cbranch_scc1 .Lou_490\n"
"s_waitcnt lgkmcnt(0)\n"
"v_cmp_eq_u32 vcc, 0, v3\n"
"s_cbranch_vccnz .Ltp_489\n"
".Lou_490:\n"
"s_waitcnt lgkmcnt(0)\n"
"s_mul_i32 m0, s3, 0x1400\n"
"s_add_u32 s49, s50, 0xa000\n"
"buffer_load_dwordx4 v2, s[4:7], s49 offen offset:0 sc1 lds\n"
"buffer_load_dwordx4 v2, s[4:7], s49 offen offset:1024 sc1 lds\n"
"buffer_load_dwordx4 v2, s[4:7], s49 offen offset:2048 sc1 lds\n"
"buffer_load_dwordx4 v2, s[4:7], s49 offen offset:3072 sc1 lds\n"
"s_add_u32 m0, m0, 1024\n"
"s_add_u32 s49, s49, 1024\n"
"buffer_load_dwordx4 v2, s[4:7], s49 offen offset:3072 sc1 lds\n"
"s_waitcnt vmcnt(5)\n"
"s_mov_b32 s17, 32768\n"
"s_branch .Lmd_488\n"
".Ltp_486:\n"
"buffer_load_dwordx2 v[40:41], v14, s[4:7], s36 offen offset:0 sc1\n"
"buffer_load_dwordx2 v[42:43], v14, s[4:7], s36 offen offset:512 sc1\n"
"buffer_load_dwordx2 v[44:45], v15, s[4:7], s36 offen offset:1024 sc1\n"
"s_waitcnt vmcnt(0)\n"
".Lmd_488:\n"
"s_sub_u32 s17, s17, 1\n"
"s_cbranch_scc1 .Lou_487\n"
"v_max3_u32 v38, v40, v41, v42\n"
"v_max3_u32 v38, v43, v44, v38\n"
"v_max_u32 v38, v45, v38\n"
"v_cmp_eq_u32 vcc, -1, v38\n"
"s_cbranch_vccnz .Ltp_486\n"
".Lou_487:\n"
"v_lshlrev_b32 v48, 16, v40\n"
"v_and_b32 v49, 0xffff0000, v40\n"
"v_lshlrev_b32 v50, 16, v41\n"
"v_and_b32 v51, 0xffff0000, v41\n"
"v_lshlrev_b32 v52, 16, v42\n"
"v_and_b32 v53, 0xffff0000, v42\n"
"v_lshlrev_b32 v54, 16, v43\n"
"v_and_b32 v55, 0xffff0000, v43\n"
"v_lshlrev_b32 v56, 16, v44\n"
"v_and_b32 v57, 0xffff0000, v44\n"
"v_lshlrev_b32 v58, 16, v45\n"
"v_and_b32 v59, 0xffff0000, v45\n"
"v_mov_b32 v46, 0\n"
"v_mov_b32 v47, 0\n"
"s_mov_b32 s17, 0x100000\n"
".Ltp_491:\n"
"s_sleep 1\n"
"ds_read_b32 v3, v5 offset:384\n"
"s_sub_u32 s17, s17, 1\n"
"s_cbranch_scc1 .Lou_492\n"
"s_waitcnt lgkmcnt(0)\n"
"v_cmp_eq_u32 vcc, 0, v3\n"
"s_cbranch_vccnz .Ltp_491\n"
".Lou_492:\n"
"s_waitcnt lgkmcnt(0)\n"
"s_mul_i32 s79, s3, 0x1400\n"
"s_add_u32 m0, s79, 0x5000\n"
"s_add_u32 s49, s50, 0xf000\n"
"buffer_load_dwordx4 v2, s[4:7], s49 offen offset:0 sc1 lds\n"
"buffer_load_dwordx4 v2, s[4:7], s49 offen offset:1024 sc1 lds\n"
"buffer_load_dwordx4 v2, s[4:7], s49 offen offset:2048 sc1 lds\n"
"buffer_load_dwordx4 v2, s[4:7], s49 offen offset:3072 sc1 lds\n"
"s_add_u32 m0, m0, 1024\n"
"s_add_u32 s49, s49, 1024\n"
"buffer_load_dwordx4 v2, s[4:7], s49 offen offset:3072 sc1 lds\n"
"s_waitcnt vmcnt(5)\n"
"ds_write_b32 v4, v4 offset:416\n"
"s_waitcnt vmcnt(0)\n"
"ds_write_b32 v4, v4 offset:432\n"
"buffer_load_dwordx4 v[108:111], v2, s[4:7], s34 offen offset:0 sc1\n"
"s_nop 9\n"
"buffer_load_dwordx4 v[112:115], v2, s[4:7], s34 offen offset:1024 sc1\n"
"s_nop 9\n"
"buffer_load_dwordx4 v[116:119], v2, s[4:7], s34 offen offset:2048 sc1\n"
"s_nop 9\n"
"buffer_load_dwordx4 v[120:123], v2, s[4:7], s34 offen offset:3072 sc1\n"
"s_nop 9\n"
"buffer_load_dwordx4 v[124:127], v2, s[4:7], s35 offen offset:0 sc1\n"
"s_nop 9\n"
"buffer_load_dwordx4 v[88:91], v2, s[4:7], s32 offen offset:0 sc1\n"
"s_nop 9\n"
"buffer_load_dwordx4 v[92:95], v2, s[4:7], s32 offen offset:1024 sc1\n"
"s_nop 9\n"
"buffer_load_dwordx4 v[96:99], v2, s[4:7], s32 offen offset:2048 sc1\n"
"s_nop 9\n"
"buffer_load_dwordx4 v[100:103], v2, s[4:7], s32 offen offset:3072 sc1\n"
"s_nop 9\n"
"buffer_load_dwordx4 v[104:107], v2, s[4:7], s33 offen offset:0 sc1\n"
"s_waitcnt vmcnt(9)\n"
"v_max_u32 v38, v108, v109\n"
"v_max3_u32 v38, v110, v111, v38\n"
"v_cmp_eq_u32 vcc, -1, v38\n"
"s_cbranch_vccnz .Lsl_493\n"
".Lbk_494:\n"
"v_mfma_f32_16x16x32_bf16 v[68:71], v[156:159], v[108:111], 0\n"
"v_mfma_f32_16x16x32_bf16 v[72:75], v[160:163], v[108:111], 0\n"
"v_mfma_f32_16x16x32_bf16 v[76:79], v[164:167], v[108:111], 0\n"
"s_waitcnt vmcnt(8)\n"
"v_max_u32 v38, v112, v113\n"
"v_max3_u32 v38, v114, v115, v38\n"
"v_mfma_f32_16x16x32_bf16 v[80:83], v[168:171], v[108:111], 0\n"
"v_cmp_eq_u32 vcc, -1, v38\n"
"v_mfma_f32_16x16x32_bf16 v[84:87], v[172:175], v[108:111], 0\n"
"s_cbranch_vccnz .Lsl_495\n"
".Lbk_496:\n"
"v_mfma_f32_16x16x32_bf16 v[68:71], v[176:179], v[112:115], v[68:71]\n"
"v_mfma_f32_16x16x32_bf16 v[72:75], v[180:183], v[112:115], v[72:75]\n"
"v_mfma_f32_16x16x32_bf16 v[76:79], v[184:187], v[112:115], v[76:79]\n"
"s_waitcnt vmcnt(7)\n"
"v_max_u32 v38, v116, v117\n"
"v_max3_u32 v38, v118, v119, v38\n"
"v_mfma_f32_16x16x32_bf16 v[80:83], v[188:191], v[112:115], v[80:83]\n"
"v_cmp_eq_u32 vcc, -1, v38\n"
"v_mfma_f32_16x16x32_bf16 v[84:87], v[192:195], v[112:115], v[84:87]\n"
"s_cbranch_vccnz .Lsl_497\n"
".Lbk_498:\n"
"v_mfma_f32_16x16x32_bf16 v[68:71], v[196:199], v[116:119], v[68:71]\n"
"v_mfma_f32_16x16x32_bf16 v[72:75], v[200:203], v[116:119], v[72:75]\n"
"v_mfma_f32_16x16x32_bf16 v[76:79], v[204:207], v[116:119], v[76:79]\n"
"s_waitcnt vmcnt(6)\n"
"v_max_u32 v38, v120, v121\n"
"v_max3_u32 v38, v122, v123, v38\n"
"v_mfma_f32_16x16x32_bf16 v[80:83], v[208:211], v[116:119], v[80:83]\n"
"v_cmp_eq_u32 vcc, -1, v38\n"
"v_mfma_f32_16x16x32_bf16 v[84:87], v[212:215], v[116:119], v[84:87]\n"
"s_cbranch_vccnz .Lsl_499\n"
".Lbk_500:\n"
"v_mfma_f32_16x16x32_bf16 v[68:71], v[216:219], v[120:123], v[68:71]\n"
"v_mfma_f32_16x16x32_bf16 v[72:75], v[220:223], v[120:123], v[72:75]\n"
"v_mfma_f32_16x16x32_bf16 v[76:79], v[224:227], v[120:123], v[76:79]\n"
"s_waitcnt vmcnt(5)\n"
"v_max_u32 v38, v124, v125\n"
"v_max3_u32 v38, v126, v127, v38\n"
"v_mfma_f32_16x16x32_bf16 v[80:83], v[228:231], v[120:123], v[80:83]\n"
"v_cmp_eq_u32 vcc, -1, v38\n"
"v_mfma_f32_16x16x32_bf16 v[84:87], v[232:235], v[120:123], v[84:87]\n"
"s_cbranch_vccnz .Lsl_501\n"
".Lbk_502:\n"
"v_mfma_f32_16x16x32_bf16 v[68:71], v[236:239], v[124:127], v[68:71]\n"
"v_mfma_f32_16x16x32_bf16 v[72:75], v[240:243], v[124:127], v[72:75]\n"
"v_mfma_f32_16x16x32_bf16 v[76:79], v[244:247], v[124:127], v[76:79]\n"
"s_waitcnt vmcnt(4)\n"
"v_max_u32 v38, v88, v89\n"
"v_max3_u32 v38, v90, v91, v38\n"
"v_mfma_f32_16x16x32_bf16 v[80:83], v[248:251], v[124:127], v[80:83]\n"
"v_cmp_eq_u32 vcc, -1, v38\n"
"v_mfma_f32_16x16x32_bf16 v[84:87], v[252:255], v[124:127], v[84:87]\n"
"ds_write_b128 v8, v[68:71] offset:0\n"
"s_cbranch_vccnz .Lsl_503\n"
".Lbk_504:\n"
"v_mfma_f32_16x16x32_bf16 v[56:59], v[164:167], v[88:91], v[56:59]\n"
"s_waitcnt vmcnt(3)\n"
"v_max_u32 v38, v92, v93\n"
"v_max3_u32 v38, v94, v95, v38\n"
"ds_write_b128 v8, v[72:75] offset:1024\n"
"v_mfma_f32_16x16x32_bf16 v[60:63], v[168:171], v[88:91], 0\n"
"v_cmp_eq_u32 vcc, -1, v38\n"
"ds_write_b128 v10, v[76:79] offset:0\n"
"v_mfma_f32_16x16x32_bf16 v[64:67], v[172:175], v[88:91], 0\n"
"v_mov_b32 v117, s16\n"
"s_cbranch_vccnz .Lsl_505\n"
".Lbk_506:\n"
"v_mfma_f32_16x16x32_bf16 v[56:59], v[184:187], v[92:95], v[56:59]\n"
"s_waitcnt vmcnt(2)\n"
"v_max_u32 v38, v96, v97\n"
"v_max3_u32 v38, v98, v99, v38\n"
"v_mov_b32 v119, s16\n"
"v_mfma_f32_16x16x32_bf16 v[60:63], v[188:191], v[92:95], v[60:63]\n"
"v_cmp_eq_u32 vcc, -1, v38\n"
"ds_write_b128 v9, v[80:83] offset:0\n"
"v_mfma_f32_16x16x32_bf16 v[64:67], v[192:195], v[92:95], v[64:67]\n"
"ds_write_b128 v9, v[84:87] offset:1024\n"
"s_cbranch_vccnz .Lsl_507\n"
".Lbk_508:\n"
"v_mfma_f32_16x16x32_bf16 v[56:59], v[204:207], v[96:99], v[56:59]\n"
"s_waitcnt vmcnt(1)\n"
"v_max_u32 v38, v100, v101\n"
"v_max3_u32 v38, v102, v103, v38\n"
"ds_write_b32 v4, v4 offset:720\n"
"v_mfma_f32_16x16x32_bf16 v[60:63], v[208:211], v[96:99], v[60:63]\n"
"v_cmp_eq_u32 vcc, -1, v38\n"
"v_mov_b32 v121, s16\n"
"v_mov_b32 v123, s16\n"
"v_mfma_f32_16x16x32_bf16 v[64:67], v[212:215], v[96:99], v[64:67]\n"
"v_mov_b32 v125, s16\n"
"s_cbranch_vccnz .Lsl_509\n"
".Lbk_510:\n"
"v_mfma_f32_16x16x32_bf16 v[56:59], v[224:227], v[100:103], v[56:59]\n"
"s_waitcnt vmcnt(0)\n"
"v_max_u32 v38, v104, v105\n"
"v_max3_u32 v38, v106, v107, v38\n"
"v_mov_b32 v127, s16\n"
"v_mfma_f32_16x16x32_bf16 v[60:63], v[228:231], v[100:103], v[60:63]\n"
"v_cmp_eq_u32 vcc, -1, v38\n"
"ds_read_b32 v3, v5 offset:720\n"
"v_mfma_f32_16x16x32_bf16 v[64:67], v[232:235], v[100:103], v[64:67]\n"
"s_cbranch_vccnz .Lsl_511\n"
".Lbk_512:\n"
"v_mfma_f32_16x16x32_bf16 v[56:59], v[244:247], v[104:107], v[56:59]\n"
"ds_read_b128 v[76:79], v12 offset:2048\n"
"ds_read_b128 v[108:111], v13 offset:2048\n"
"v_mfma_f32_16x16x32_bf16 v[60:63], v[248:251], v[104:107], v[60:63]\n"
"ds_read_b128 v[68:71], v12 offset:0\n"
"ds_read_b128 v[80:83], v13 offset:0\n"
"v_mfma_f32_16x16x32_bf16 v[64:67], v[252:255], v[104:107], v[64:67]\n"
"ds_read_b128 v[72:75], v12 offset:1024\n"
"ds_read_b128 v[84:87], v13 offset:1024\n"
"v_mfma_f32_16x16x32_bf16 v[48:51], v[156:159], v[88:91], v[48:51]\n"
"ds_write_b128 v7, v[56:59] offset:0\n"
"v_mfma_f32_16x16x32_bf16 v[52:55], v[160:163], v[88:91], v[52:55]\n"
"ds_write_b128 v6, v[60:63] offset:0\n"
"v_mfma_f32_16x16x32_bf16 v[48:51], v[176:179], v[92:95], v[48:51]\n"
"ds_write_b128 v6, v[64:67] offset:1024\n"
"v_mfma_f32_16x16x32_bf16 v[52:55], v[180:183], v[92:95], v[52:55]\n"
"ds_write_b32 v4, v4 offset:736\n"
"v_mfma_f32_16x16x32_bf16 v[48:51], v[196:199], v[96:99], v[48:51]\n"
"s_mov_b32 s17, 0x100000\n"
"s_waitcnt lgkmcnt(10)\n"
"v_cmp_eq_u32 vcc, 0, v3\n"
"s_cbranch_vccz .Lok_514\n"
".Ltp_513:\n"
"ds_read_b32 v3, v5 offset:720\n"
"ds_read_b128 v[76:79], v12 offset:2048\n"
"ds_read_b128 v[108:111], v13 offset:2048\n"
"ds_read_b128 v[68:71], v12 offset:0\n"
"ds_read_b128 v[80:83], v13 offset:0\n"
"ds_read_b128 v[72:75], v12 offset:1024\n"
"ds_read_b128 v[84:87], v13 offset:1024\n"
"s_sub_u32 s17, s17, 1\n"
"s_cbranch_scc1 .Lok_514\n"
"s_waitcnt lgkmcnt(6)\n"
"v_cmp_eq_u32 vcc, 0, v3\n"
"s_cbranch_vccnz .Ltp_513\n"
"s_waitcnt lgkmcnt(0)\n"
".Lok_514:\n"
"v_mfma_f32_16x16x32_bf16 v[52:55], v[200:203], v[96:99], v[52:55]\n"
"s_waitcnt lgkmcnt(8)\n"
"v_add_f32 v76, v76, v108\n"
"v_add_f32 v77, v77, v109\n"
"v_add_f32 v78, v78, v110\n"
"v_mfma_f32_16x16x32_bf16 v[48:51], v[216:219], v[100:103], v[48:51]\n"
"v_add_f32 v79, v79, v111\n"
"s_waitcnt lgkmcnt(6)\n"
"v_add_f32 v68, v68, v80\n"
"v_add_f32 v69, v69, v81\n"
"v_mfma_f32_16x16x32_bf16 v[52:55], v[220:223], v[100:103], v[52:55]\n"
"v_add_f32 v70, v70, v82\n"
"v_add_f32 v71, v71, v83\n"
"s_waitcnt lgkmcnt(4)\n"
"v_add_f32 v72, v72, v84\n"
"v_mfma_f32_16x16x32_bf16 v[48:51], v[236:239], v[104:107], v[48:51]\n"
"v_add_f32 v73, v73, v85\n"
"v_add_f32 v74, v74, v86\n"
"v_add_f32 v75, v75, v87\n"
"v_mfma_f32_16x16x32_bf16 v[52:55], v[240:243], v[104:107], v[52:55]\n"
"ds_read_b32 v3, v5 offset:736\n"
"ds_read_b128 v[112:115], v11 offset:2048\n"
"ds_read_b128 v[60:63], v11 offset:0\n"
"ds_read_b128 v[64:67], v11 offset:1024\n"
"v_add_f32 v56, v56, v76\n"
"v_add_f32 v57, v57, v77\n"
"v_add_f32 v58, v58, v78\n"
"v_add_f32 v59, v59, v79\n"
"v_add_f32 v48, v48, v68\n"
"v_add_f32 v49, v49, v69\n"
"v_add_f32 v50, v50, v70\n"
"v_add_f32 v51, v51, v71\n"
"v_add_f32 v52, v52, v72\n"
"v_add_f32 v53, v53, v73\n"
"v_add_f32 v54, v54, v74\n"
"v_add_f32 v55, v55, v75\n"
"s_mov_b32 s17, 0x100000\n"
"s_waitcnt lgkmcnt(3)\n"
"v_cmp_eq_u32 vcc, 0, v3\n"
"s_cbranch_vccz .Lok_516\n"
".Ltp_515:\n"
"ds_read_b32 v3, v5 offset:736\n"
"ds_read_b128 v[112:115], v11 offset:2048\n"
"ds_read_b128 v[60:63], v11 offset:0\n"
"ds_read_b128 v[64:67], v11 offset:1024\n"
"s_sub_u32 s17, s17, 1\n"
"s_cbranch_scc1 .Lok_516\n"
"s_waitcnt lgkmcnt(3)\n"
"v_cmp_eq_u32 vcc, 0, v3\n"
"s_cbranch_vccnz .Ltp_515\n"
"s_waitcnt lgkmcnt(0)\n"
".Lok_516:\n"
"s_waitcnt lgkmcnt(2)\n"
"v_add_f32 v56, v56, v112\n"
"v_add_f32 v57, v57, v113\n"
"v_add_f32 v58, v58, v114\n"
"v_add_f32 v59, v59, v115\n"
"v_cvt_pk_bf16_f32 v44, v56, v57\n"
"v_cvt_pk_bf16_f32 v45, v58, v59\n"
"v_and_b32 v44, v19, v44\n"
"v_and_b32 v45, v19, v45\n"
"s_waitcnt lgkmcnt(1)\n"
"v_add_f32 v48, v48, v60\n"
"v_add_f32 v49, v49, v61\n"
"v_add_f32 v50, v50, v62\n"
"v_add_f32 v51, v51, v63\n"
"v_cvt_pk_bf16_f32 v40, v48, v49\n"
"v_cvt_pk_bf16_f32 v41, v50, v51\n"
"v_mfma_f32_16x16x32_bf16 v[88:91], v[44:47], v[44:47], 0\n"
"v_mfma_f32_16x16x32_bf16 v[92:95], v[20:23], v[44:47], 0\n"
"s_waitcnt lgkmcnt(0)\n"
"v_add_f32 v52, v52, v64\n"
"v_add_f32 v53, v53, v65\n"
"v_add_f32 v54, v54, v66\n"
"v_add_f32 v55, v55, v67\n"
"v_cvt_pk_bf16_f32 v42, v52, v53\n"
"v_cvt_pk_bf16_f32 v43, v54, v55\n"
"v_cvt_pk_bf16_f32 v116, v48, v49\n"
"v_cvt_pk_bf16_f32 v118, v50, v51\n"
"v_mfma_f32_16x16x32_bf16 v[88:91], v[40:43], v[40:43], v[88:91]\n"
"v_mfma_f32_16x16x32_bf16 v[92:95], v[20:23], v[40:43], v[92:95]\n"
"v_cvt_pk_bf16_f32 v120, v52, v53\n"
"v_cvt_pk_bf16_f32 v122, v54, v55\n"
"v_cvt_pk_bf16_f32 v124, v56, v57\n"
"v_cvt_pk_bf16_f32 v126, v58, v59\n"
"s_nop 0\n"
"v_cndmask_b32 v101, v88, v90, s[44:45]\n"
"v_cndmask_b32 v88, v89, v91, s[44:45]\n"
"s_nop 0\n"
"v_min_u32 v100, -2, v92\n"
"v_min_u32 v88, -2, v88\n"
"v_min_u32 v101, -2, v101\n"
"v_mov_b32_dpp v102, v100 quad_perm:[1,0,3,2] row_mask:0xf bank_mask:0xf\n"
"v_mov_b32_dpp v103, v88 quad_perm:[1,0,3,2] row_mask:0xf bank_mask:0xf\n"
"buffer_store_dwordx4 v[100:103], v16, s[4:7], s38 offen sc1\n"
"buffer_store_dwordx4 v[116:119], v17, s[4:7], s37 offen offset:0 sc1\n"
"buffer_store_dwordx4 v[120:123], v17, s[4:7], s37 offen offset:1024 sc1\n"
"buffer_store_dwordx4 v[124:127], v18, s[4:7], s37 offen offset:2048 sc1\n"
"ds_write_b32 v4, v4 offset:512\n"
"s_xor_b32 s79, s3, 1\n"
"s_lshl_b32 s79, s79, 8\n"
"s_add_u32 s32, s19, s79\n"
"s_cmp_ge_u32 s32, 64\n"
"s_cbranch_scc1 .Lnr_517\n"
"s_lshl_b32 s79, s19, 11\n"
"s_add_u32 s32, s79, 0x1548000\n"
"s_lshl_b32 s79, s19, 7\n"
"s_add_u32 s33, s79, 0x1588000\n"
"v_lshrrev_b32 v99, 3, v0\n"
"v_and_b32 v98, 7, v0\n"
"v_lshlrev_b32 v98, 4, v98\n"
"v_lshl_add_u32 v40, v99, 14, v98\n"
"v_mov_b32 v41, 0x3727c5ac\n"
"s_mov_b32 s89, 8\n"
".Ldl_518:\n"
"s_sub_u32 s89, s89, 1\n"
"s_cbranch_scc1 .Lde_519\n"
"s_sleep 1\n"
"s_branch .Ldl_518\n"
".Lde_519:\n"
"buffer_load_dwordx4 v[48:51], v2, s[4:7], s32 offen offset:0 sc1\n"
"buffer_load_dwordx4 v[52:55], v2, s[4:7], s32 offen offset:1024 sc1\n"
"s_mov_b32 s17, 32768\n"
".Ltp_520:\n"
"s_waitcnt vmcnt(0)\n"
"s_sub_u32 s17, s17, 1\n"
"s_cbranch_scc1 .Lou_521\n"
"v_max_u32 v38, v48, v49\n"
"v_max3_u32 v38, v50, v51, v38\n"
"v_max3_u32 v38, v52, v53, v38\n"
"v_max3_u32 v38, v54, v55, v38\n"
"v_cmp_eq_u32 vcc, -1, v38\n"
"s_cbranch_vccz .Lou_521\n"
"v_max_u32 v38, v50, v51\n"
"v_max3_u32 v38, v48, v49, v38\n"
"v_cmp_eq_u32 vcc, -1, v38\n"
"s_cbranch_vccz .Lsk_522\n"
"s_mov_b64 exec, vcc\n"
"buffer_load_dwordx4 v[48:51], v2, s[4:7], s32 offen offset:0 sc1\n"
"s_mov_b64 exec, -1\n"
".Lsk_522:\n"
"v_max_u32 v38, v54, v55\n"
"v_max3_u32 v38, v52, v53, v38\n"
"v_cmp_eq_u32 vcc, -1, v38\n"
"s_cbranch_vccz .Lsk_523\n"
"s_mov_b64 exec, vcc\n"
"buffer_load_dwordx4 v[52:55], v2, s[4:7], s32 offen offset:1024 sc1\n"
"s_mov_b64 exec, -1\n"
".Lsk_523:\n"
"s_branch .Ltp_520\n"
".Lou_521:\n"
"s_waitcnt vmcnt(0)\n"
"v_add_f32 v48, v48, v52\n"
"v_add_f32 v49, v49, v53\n"
"v_add_f32 v50, v50, v54\n"
"v_add_f32 v51, v51, v55\n"
"v_add_f32_dpp v52, v48, v48 row_ror:8 row_mask:0xf bank_mask:0xf\n"
"v_add_f32_dpp v53, v49, v49 row_ror:8 row_mask:0xf bank_mask:0xf\n"
"v_add_f32_dpp v54, v50, v50 row_ror:8 row_mask:0xf bank_mask:0xf\n"
"v_add_f32_dpp v55, v51, v51 row_ror:8 row_mask:0xf bank_mask:0xf\n"
"v_mov_b32 v48, v52\n"
"v_mov_b32 v49, v53\n"
"v_mov_b32 v50, v54\n"
"v_mov_b32 v51, v55\n"
"v_permlane16_swap_b32 v48, v52\n"
"v_permlane16_swap_b32 v49, v53\n"
"v_permlane16_swap_b32 v50, v54\n"
"v_permlane16_swap_b32 v51, v55\n"
"v_add_f32 v52, v48, v52\n"
"v_add_f32 v53, v49, v53\n"
"v_add_f32 v54, v50, v54\n"
"v_add_f32 v55, v51, v55\n"
"v_mov_b32 v48, v52\n"
"v_mov_b32 v49, v53\n"
"v_mov_b32 v50, v54\n"
"v_mov_b32 v51, v55\n"
"v_permlane32_swap_b32 v48, v52\n"
"v_permlane32_swap_b32 v49, v53\n"
"v_permlane32_swap_b32 v50, v54\n"
"v_permlane32_swap_b32 v51, v55\n"
"v_add_f32 v52, v48, v52\n"
"v_add_f32 v53, v49, v53\n"
"v_add_f32 v54, v50, v54\n"
"v_add_f32 v55, v51, v55\n"
"v_mul_f32 v48, 0x3acccccd, v52\n"
"v_mul_f32 v50, 0x3acccccd, v54\n"
"v_fma_f32 v53, -v52, v48, v53\n"
"v_fma_f32 v55, -v54, v50, v55\n"
"v_fmamk_f32 v53, v53, 0x3acccccd, v41\n"
"v_fmamk_f32 v55, v55, 0x3acccccd, v41\n"
"v_max_f32 v53, v53, v41\n"
"v_max_f32 v55, v55, v41\n"
"v_rsq_f32 v49, v53\n"
"v_rsq_f32 v51, v55\n"
"v_mul_f32 v48, -v48, v49\n"
"v_mul_f32 v50, -v50, v51\n"
"v_min_u32 v49, -2, v49\n"
"v_min_u32 v51, -2, v51\n"
"v_min_u32 v48, -2, v48\n"
"v_min_u32 v50, -2, v50\n"
"buffer_store_dwordx4 v[48:51], v40, s[4:7], s33 offen sc1\n"
"s_nop 3\n"
".Lnr_517:\n"
"s_mul_i32 s79, s28, 0xa00\n"
"s_add_u32 s79, s30, s79\n"
"s_and_b32 s78, s3, 1\n"
"s_mul_i32 s78, s78, 0x600\n"
"s_add_u32 s79, s79, s78\n"
"s_add_u32 s36, s79, 0xc88000\n"
"s_mul_i32 s79, s3, 0x1400\n"
"s_add_u32 s79, s30, s79\n"
"s_add_u32 s32, s79, 0xf08000\n"
"s_add_u32 s33, s32, 0x1000\n"
"s_lshl_b32 s79, s29, 1\n"
"s_lshr_b32 s78, s3, 1\n"
"s_xor_b32 s78, s78, 1\n"
"s_add_u32 s79, s79, s78\n"
"s_mul_i32 s79, s79, 0x5000\n"
"s_mul_i32 s78, s3, 0x1400\n"
"s_add_u32 s79, s79, s78\n"
"s_add_u32 s34, s79, 0xf08000\n"
"s_add_u32 s35, s34, 0x1000\n"
"s_lshl_b32 s79, s30, 1\n"
"s_mul_i32 s78, s28, 0x1400\n"
"s_add_u32 s79, s79, s78\n"
"s_and_b32 s78, s3, 1\n"
"s_mul_i32 s78, s78, 0xc00\n"
"s_add_u32 s79, s79, s78\n"
"s_add_u32 s37, s79, 0x12c8000\n"
"s_lshl_b32 s79, s29, 1\n"
"s_lshr_b32 s78, s3, 1\n"
"s_add_u32 s79, s79, s78\n"
"s_lshl_b32 s79, s79, 11\n"
"s_lshl_b32 s78, s28, 8\n"
"s_add_u32 s79, s79, s78\n"
"s_and_b32 s78, s3, 1\n"
"s_lshl_b32 s78, s78, 7\n"
"s_add_u32 s79, s79, s78\n"
"s_add_u32 s38, s79, 0x1568000\n"
"s_mov_b32 s17, 0x100000\n"
".Ltp_524:\n"
"ds_read_b32 v3, v5 offset:512\n"
"s_sub_u32 s17, s17, 1\n"
"s_cbranch_scc1 .Lou_525\n"
"s_waitcnt lgkmcnt(0)\n"
"v_cmp_eq_u32 vcc, 0, v3\n"
"s_cbranch_vccnz .Ltp_524\n"
".Lou_525:\n"
"s_waitcnt lgkmcnt(0)\n"
"s_mov_b32 s17, 0x100000\n"
".Ltp_529:\n"
"s_sleep 2\n"
"ds_read_b32 v3, v5 offset:4800\n"
"s_sub_u32 s17, s17, 1\n"
"s_cbranch_scc1 .Lou_530\n"
"s_waitcnt lgkmcnt(0)\n"
"v_cmp_eq_u32 vcc, -1, v3\n"
"s_cbranch_vccnz .Ltp_529\n"
".Lou_530:\n"
"s_waitcnt lgkmcnt(0)\n"
"s_mov_b32 s17, 32768\n"
".Ltp_526:\n"
"buffer_load_dwordx2 v[40:41], v14, s[4:7], s36 offen offset:0 sc1\n"
"buffer_load_dwordx2 v[42:43], v14, s[4:7], s36 offen offset:512 sc1\n"
"buffer_load_dwordx2 v[44:45], v15, s[4:7], s36 offen offset:1024 sc1\n"
"s_waitcnt vmcnt(0)\n"
".Lmd_528:\n"
"s_sub_u32 s17, s17, 1\n"
"s_cbranch_scc1 .Lou_527\n"
"v_max3_u32 v38, v40, v41, v42\n"
"v_max3_u32 v38, v43, v44, v38\n"
"v_max_u32 v38, v45, v38\n"
"v_cmp_eq_u32 vcc, -1, v38\n"
"s_cbranch_vccnz .Ltp_526\n"
".Lou_527:\n"
"v_lshlrev_b32 v48, 16, v40\n"
"v_and_b32 v49, 0xffff0000, v40\n"
"v_lshlrev_b32 v50, 16, v41\n"
"v_and_b32 v51, 0xffff0000, v41\n"
"v_lshlrev_b32 v52, 16, v42\n"
"v_and_b32 v53, 0xffff0000, v42\n"
"v_lshlrev_b32 v54, 16, v43\n"
"v_and_b32 v55, 0xffff0000, v43\n"
"v_lshlrev_b32 v56, 16, v44\n"
"v_and_b32 v57, 0xffff0000, v44\n"
"v_lshlrev_b32 v58, 16, v45\n"
"v_and_b32 v59, 0xffff0000, v45\n"
"v_mov_b32 v46, 0\n"
"v_mov_b32 v47, 0\n"
"s_mov_b32 s17, 0x100000\n"
".Ltp_531:\n"
"s_sleep 1\n"
"ds_read_b32 v3, v5 offset:496\n"
"s_sub_u32 s17, s17, 1\n"
"s_cbranch_scc1 .Lou_532\n"
"s_waitcnt lgkmcnt(0)\n"
"v_cmp_eq_u32 vcc, 0, v3\n"
"s_cbranch_vccnz .Ltp_531\n"
".Lou_532:\n"
"s_waitcnt lgkmcnt(0)\n"
"s_cmp_eq_u32 s17, 0xfffff\n"
"s_cbranch_scc1 .Lsk_533\n"
"s_mov_b32 s89, 20\n"
".Ldl_535:\n"
"s_sub_u32 s89, s89, 1\n"
"s_cbranch_scc1 .Lde_536\n"
"s_sleep 1\n"
"s_branch .Ldl_535\n"
".Lde_536:\n"
"s_branch .Ljn_534\n"
".Lsk_533:\n"
"s_mov_b32 s89, 12\n"
".Ldl_537:\n"
"s_sub_u32 s89, s89, 1\n"
"s_cbranch_scc1 .Lde_538\n"
"s_sleep 1\n"
"s_branch .Ldl_537\n"
".Lde_538:\n"
".Ljn_534:\n"
"buffer_load_dwordx4 v[108:111], v2, s[4:7], s34 offen offset:0 sc1\n"
"s_nop 9\n"
"buffer_load_dwordx4 v[112:115], v2, s[4:7], s34 offen offset:1024 sc1\n"
"s_nop 9\n"
"buffer_load_dwordx4 v[116:119], v2, s[4:7], s34 offen offset:2048 sc1\n"
"s_nop 9\n"
"buffer_load_dwordx4 v[120:123], v2, s[4:7], s34 offen offset:3072 sc1\n"
"s_nop 9\n"
"buffer_load_dwordx4 v[124:127], v2, s[4:7], s35 offen offset:0 sc1\n"
"s_nop 9\n"
"buffer_load_dwordx4 v[88:91], v2, s[4:7], s32 offen offset:0 sc1\n"
"s_nop 9\n"
"buffer_load_dwordx4 v[92:95], v2, s[4:7], s32 offen offset:1024 sc1\n"
"s_nop 9\n"
"buffer_load_dwordx4 v[96:99], v2, s[4:7], s32 offen offset:2048 sc1\n"
"s_nop 9\n"
"buffer_load_dwordx4 v[100:103], v2, s[4:7], s32 offen offset:3072 sc1\n"
"s_nop 9\n"
"buffer_load_dwordx4 v[104:107], v2, s[4:7], s33 offen offset:0 sc1\n"
"s_waitcnt vmcnt(9)\n"
"v_max_u32 v38, v108, v109\n"
"v_max3_u32 v38, v110, v111, v38\n"
"v_cmp_eq_u32 vcc, -1, v38\n"
"s_cbranch_vccnz .Lsl_539\n"
".Lbk_540:\n"
"v_mfma_f32_16x16x32_bf16 v[68:71], v[156:159], v[108:111], 0\n"
"v_mfma_f32_16x16x32_bf16 v[72:75], v[160:163], v[108:111], 0\n"
"v_mfma_f32_16x16x32_bf16 v[76:79], v[164:167], v[108:111], 0\n"
"s_waitcnt vmcnt(8)\n"
"v_max_u32 v38, v112, v113\n"
"v_max3_u32 v38, v114, v115, v38\n"
"v_mfma_f32_16x16x32_bf16 v[80:83], v[168:171], v[108:111], 0\n"
"v_cmp_eq_u32 vcc, -1, v38\n"
"v_mfma_f32_16x16x32_bf16 v[84:87], v[172:175], v[108:111], 0\n"
"s_cbranch_vccnz .Lsl_541\n"
".Lbk_542:\n"
"v_mfma_f32_16x16x32_bf16 v[68:71], v[176:179], v[112:115], v[68:71]\n"
"v_mfma_f32_16x16x32_bf16 v[72:75], v[180:183], v[112:115], v[72:75]\n"
"v_mfma_f32_16x16x32_bf16 v[76:79], v[184:187], v[112:115], v[76:79]\n"
"s_waitcnt vmcnt(7)\n"
"v_max_u32 v38, v116, v117\n"
"v_max3_u32 v38, v118, v119, v38\n"
"v_mfma_f32_16x16x32_bf16 v[80:83], v[188:191], v[112:115], v[80:83]\n"
"v_cmp_eq_u32 vcc, -1, v38\n"
"v_mfma_f32_16x16x32_bf16 v[84:87], v[192:195], v[112:115], v[84:87]\n"
"s_cbranch_vccnz .Lsl_543\n"
".Lbk_544:\n"
"v_mfma_f32_16x16x32_bf16 v[68:71], v[196:199], v[116:119], v[68:71]\n"
"v_mfma_f32_16x16x32_bf16 v[72:75], v[200:203], v[116:119], v[72:75]\n"
"v_mfma_f32_16x16x32_bf16 v[76:79], v[204:207], v[116:119], v[76:79]\n"
"s_waitcnt vmcnt(6)\n"
"v_max_u32 v38, v120, v121\n"
"v_max3_u32 v38, v122, v123, v38\n"
"v_mfma_f32_16x16x32_bf16 v[80:83], v[208:211], v[116:119], v[80:83]\n"
"v_cmp_eq_u32 vcc, -1, v38\n"
"v_mfma_f32_16x16x32_bf16 v[84:87], v[212:215], v[116:119], v[84:87]\n"
"s_cbranch_vccnz .Lsl_545\n"
".Lbk_546:\n"
"v_mfma_f32_16x16x32_bf16 v[68:71], v[216:219], v[120:123], v[68:71]\n"
"v_mfma_f32_16x16x32_bf16 v[72:75], v[220:223], v[120:123], v[72:75]\n"
"v_mfma_f32_16x16x32_bf16 v[76:79], v[224:227], v[120:123], v[76:79]\n"
"s_waitcnt vmcnt(5)\n"
"v_max_u32 v38, v124, v125\n"
"v_max3_u32 v38, v126, v127, v38\n"
"v_mfma_f32_16x16x32_bf16 v[80:83], v[228:231], v[120:123], v[80:83]\n"
"v_cmp_eq_u32 vcc, -1, v38\n"
"v_mfma_f32_16x16x32_bf16 v[84:87], v[232:235], v[120:123], v[84:87]\n"
"s_cbranch_vccnz .Lsl_547\n"
".Lbk_548:\n"
"v_mfma_f32_16x16x32_bf16 v[68:71], v[236:239], v[124:127], v[68:71]\n"
"v_mfma_f32_16x16x32_bf16 v[72:75], v[240:243], v[124:127], v[72:75]\n"
"v_mfma_f32_16x16x32_bf16 v[76:79], v[244:247], v[124:127], v[76:79]\n"
"s_waitcnt vmcnt(4)\n"
"v_max_u32 v38, v88, v89\n"
"v_max3_u32 v38, v90, v91, v38\n"
"v_mfma_f32_16x16x32_bf16 v[80:83], v[248:251], v[124:127], v[80:83]\n"
"v_cmp_eq_u32 vcc, -1, v38\n"
"v_mfma_f32_16x16x32_bf16 v[84:87], v[252:255], v[124:127], v[84:87]\n"
"ds_write_b128 v8, v[68:71] offset:0\n"
"s_cbranch_vccnz .Lsl_549\n"
".Lbk_550:\n"
"v_mfma_f32_16x16x32_bf16 v[56:59], v[164:167], v[88:91], v[56:59]\n"
"s_waitcnt vmcnt(3)\n"
"v_max_u32 v38, v92, v93\n"
"v_max3_u32 v38, v94, v95, v38\n"
"ds_write_b128 v8, v[72:75] offset:1024\n"
"v_mfma_f32_16x16x32_bf16 v[60:63], v[168:171], v[88:91], 0\n"
"v_cmp_eq_u32 vcc, -1, v38\n"
"ds_write_b128 v10, v[76:79] offset:0\n"
"v_mfma_f32_16x16x32_bf16 v[64:67], v[172:175], v[88:91], 0\n"
"v_mov_b32 v117, s16\n"
"s_cbranch_vccnz .Lsl_551\n"
".Lbk_552:\n"
"v_mfma_f32_16x16x32_bf16 v[56:59], v[184:187], v[92:95], v[56:59]\n"
"s_waitcnt vmcnt(2)\n"
"v_max_u32 v38, v96, v97\n"
"v_max3_u32 v38, v98, v99, v38\n"
"v_mov_b32 v119, s16\n"
"v_mfma_f32_16x16x32_bf16 v[60:63], v[188:191], v[92:95], v[60:63]\n"
"v_cmp_eq_u32 vcc, -1, v38\n"
"ds_write_b128 v9, v[80:83] offset:0\n"
"v_mfma_f32_16x16x32_bf16 v[64:67], v[192:195], v[92:95], v[64:67]\n"
"ds_write_b128 v9, v[84:87] offset:1024\n"
"s_cbranch_vccnz .Lsl_553\n"
".Lbk_554:\n"
"v_mfma_f32_16x16x32_bf16 v[56:59], v[204:207], v[96:99], v[56:59]\n"
"s_waitcnt vmcnt(1)\n"
"v_max_u32 v38, v100, v101\n"
"v_max3_u32 v38, v102, v103, v38\n"
"ds_write_b32 v4, v4 offset:752\n"
"v_mfma_f32_16x16x32_bf16 v[60:63], v[208:211], v[96:99], v[60:63]\n"
"v_cmp_eq_u32 vcc, -1, v38\n"
"v_mov_b32 v121, s16\n"
"v_mov_b32 v123, s16\n"
"v_mfma_f32_16x16x32_bf16 v[64:67], v[212:215], v[96:99], v[64:67]\n"
"v_mov_b32 v125, s16\n"
"s_cbranch_vccnz .Lsl_555\n"
".Lbk_556:\n"
"v_mfma_f32_16x16x32_bf16 v[56:59], v[224:227], v[100:103], v[56:59]\n"
"s_waitcnt vmcnt(0)\n"
"v_max_u32 v38, v104, v105\n"
"v_max3_u32 v38, v106, v107, v38\n"
"v_mov_b32 v127, s16\n"
"v_mfma_f32_16x16x32_bf16 v[60:63], v[228:231], v[100:103], v[60:63]\n"
"v_cmp_eq_u32 vcc, -1, v38\n"
"ds_read_b32 v3, v5 offset:752\n"
"v_mfma_f32_16x16x32_bf16 v[64:67], v[232:235], v[100:103], v[64:67]\n"
"s_cbranch_vccnz .Lsl_557\n"
".Lbk_558:\n"
"v_mfma_f32_16x16x32_bf16 v[56:59], v[244:247], v[104:107], v[56:59]\n"
"ds_read_b128 v[76:79], v12 offset:2048\n"
"ds_read_b128 v[108:111], v13 offset:2048\n"
"v_mfma_f32_16x16x32_bf16 v[60:63], v[248:251], v[104:107], v[60:63]\n"
"ds_read_b128 v[68:71], v12 offset:0\n"
"ds_read_b128 v[80:83], v13 offset:0\n"
"v_mfma_f32_16x16x32_bf16 v[64:67], v[252:255], v[104:107], v[64:67]\n"
"ds_read_b128 v[72:75], v12 offset:1024\n"
"ds_read_b128 v[84:87], v13 offset:1024\n"
"v_mfma_f32_16x16x32_bf16 v[48:51], v[156:159], v[88:91], v[48:51]\n"
"ds_write_b128 v7, v[56:59] offset:0\n"
"v_mfma_f32_16x16x32_bf16 v[52:55], v[160:163], v[88:91], v[52:55]\n"
"ds_write_b128 v6, v[60:63] offset:0\n"
"v_mfma_f32_16x16x32_bf16 v[48:51], v[176:179], v[92:95], v[48:51]\n"
"ds_write_b128 v6, v[64:67] offset:1024\n"
"v_mfma_f32_16x16x32_bf16 v[52:55], v[180:183], v[92:95], v[52:55]\n"
"ds_write_b32 v4, v4 offset:768\n"
"v_mfma_f32_16x16x32_bf16 v[48:51], v[196:199], v[96:99], v[48:51]\n"
"s_mov_b32 s17, 0x100000\n"
"s_waitcnt lgkmcnt(10)\n"
"v_cmp_eq_u32 vcc, 0, v3\n"
"s_cbranch_vccz .Lok_560\n"
".Ltp_559:\n"
"ds_read_b32 v3, v5 offset:752\n"
"ds_read_b128 v[76:79], v12 offset:2048\n"
"ds_read_b128 v[108:111], v13 offset:2048\n"
"ds_read_b128 v[68:71], v12 offset:0\n"
"ds_read_b128 v[80:83], v13 offset:0\n"
"ds_read_b128 v[72:75], v12 offset:1024\n"
"ds_read_b128 v[84:87], v13 offset:1024\n"
"s_sub_u32 s17, s17, 1\n"
"s_cbranch_scc1 .Lok_560\n"
"s_waitcnt lgkmcnt(6)\n"
"v_cmp_eq_u32 vcc, 0, v3\n"
"s_cbranch_vccnz .Ltp_559\n"
"s_waitcnt lgkmcnt(0)\n"
".Lok_560:\n"
"v_mfma_f32_16x16x32_bf16 v[52:55], v[200:203], v[96:99], v[52:55]\n"
"s_waitcnt lgkmcnt(8)\n"
"v_add_f32 v76, v76, v108\n"
"v_add_f32 v77, v77, v109\n"
"v_add_f32 v78, v78, v110\n"
"v_mfma_f32_16x16x32_bf16 v[48:51], v[216:219], v[100:103], v[48:51]\n"
"v_add_f32 v79, v79, v111\n"
"s_waitcnt lgkmcnt(6)\n"
"v_add_f32 v68, v68, v80\n"
"v_add_f32 v69, v69, v81\n"
"v_mfma_f32_16x16x32_bf16 v[52:55], v[220:223], v[100:103], v[52:55]\n"
"v_add_f32 v70, v70, v82\n"
"v_add_f32 v71, v71, v83\n"
"s_waitcnt lgkmcnt(4)\n"
"v_add_f32 v72, v72, v84\n"
"v_mfma_f32_16x16x32_bf16 v[48:51], v[236:239], v[104:107], v[48:51]\n"
"v_add_f32 v73, v73, v85\n"
"v_add_f32 v74, v74, v86\n"
"v_add_f32 v75, v75, v87\n"
"v_mfma_f32_16x16x32_bf16 v[52:55], v[240:243], v[104:107], v[52:55]\n"
"ds_read_b32 v3, v5 offset:768\n"
"ds_read_b128 v[112:115], v11 offset:2048\n"
"ds_read_b128 v[60:63], v11 offset:0\n"
"ds_read_b128 v[64:67], v11 offset:1024\n"
"v_add_f32 v56, v56, v76\n"
"v_add_f32 v57, v57, v77\n"
"v_add_f32 v58, v58, v78\n"
"v_add_f32 v59, v59, v79\n"
"v_add_f32 v48, v48, v68\n"
"v_add_f32 v49, v49, v69\n"
"v_add_f32 v50, v50, v70\n"
"v_add_f32 v51, v51, v71\n"
"v_add_f32 v52, v52, v72\n"
"v_add_f32 v53, v53, v73\n"
"v_add_f32 v54, v54, v74\n"
"v_add_f32 v55, v55, v75\n"
"s_mov_b32 s17, 0x100000\n"
"s_waitcnt lgkmcnt(3)\n"
"v_cmp_eq_u32 vcc, 0, v3\n"
"s_cbranch_vccz .Lok_562\n"
".Ltp_561:\n"
"ds_read_b32 v3, v5 offset:768\n"
"ds_read_b128 v[112:115], v11 offset:2048\n"
"ds_read_b128 v[60:63], v11 offset:0\n"
"ds_read_b128 v[64:67], v11 offset:1024\n"
"s_sub_u32 s17, s17, 1\n"
"s_cbranch_scc1 .Lok_562\n"
"s_waitcnt lgkmcnt(3)\n"
"v_cmp_eq_u32 vcc, 0, v3\n"
"s_cbranch_vccnz .Ltp_561\n"
"s_waitcnt lgkmcnt(0)\n"
".Lok_562:\n"
"s_waitcnt lgkmcnt(2)\n"
"v_add_f32 v56, v56, v112\n"
"v_add_f32 v57, v57, v113\n"
"v_add_f32 v58, v58, v114\n"
"v_add_f32 v59, v59, v115\n"
"v_cvt_pk_bf16_f32 v44, v56, v57\n"
"v_cvt_pk_bf16_f32 v45, v58, v59\n"
"v_and_b32 v44, v19, v44\n"
"v_and_b32 v45, v19, v45\n"
"s_waitcnt lgkmcnt(1)\n"
"v_add_f32 v48, v48, v60\n"
"v_add_f32 v49, v49, v61\n"
"v_add_f32 v50, v50, v62\n"
"v_add_f32 v51, v51, v63\n"
"v_cvt_pk_bf16_f32 v40, v48, v49\n"
"v_cvt_pk_bf16_f32 v41, v50, v51\n"
"v_mfma_f32_16x16x32_bf16 v[88:91], v[44:47], v[44:47], 0\n"
"v_mfma_f32_16x16x32_bf16 v[92:95], v[20:23], v[44:47], 0\n"
"s_waitcnt lgkmcnt(0)\n"
"v_add_f32 v52, v52, v64\n"
"v_add_f32 v53, v53, v65\n"
"v_add_f32 v54, v54, v66\n"
"v_add_f32 v55, v55, v67\n"
"v_cvt_pk_bf16_f32 v42, v52, v53\n"
"v_cvt_pk_bf16_f32 v43, v54, v55\n"
"v_cvt_pk_bf16_f32 v116, v48, v49\n"
"v_cvt_pk_bf16_f32 v118, v50, v51\n"
"v_mfma_f32_16x16x32_bf16 v[88:91], v[40:43], v[40:43], v[88:91]\n"
"v_mfma_f32_16x16x32_bf16 v[92:95], v[20:23], v[40:43], v[92:95]\n"
"v_cvt_pk_bf16_f32 v120, v52, v53\n"
"v_cvt_pk_bf16_f32 v122, v54, v55\n"
"v_cvt_pk_bf16_f32 v124, v56, v57\n"
"v_cvt_pk_bf16_f32 v126, v58, v59\n"
"s_nop 0\n"
"v_cndmask_b32 v101, v88, v90, s[44:45]\n"
"v_cndmask_b32 v88, v89, v91, s[44:45]\n"
"s_nop 0\n"
"v_min_u32 v100, -2, v92\n"
"v_min_u32 v88, -2, v88\n"
"v_min_u32 v101, -2, v101\n"
"v_mov_b32_dpp v102, v100 quad_perm:[1,0,3,2] row_mask:0xf bank_mask:0xf\n"
"v_mov_b32_dpp v103, v88 quad_perm:[1,0,3,2] row_mask:0xf bank_mask:0xf\n"
"buffer_store_dwordx4 v[100:103], v16, s[4:7], s38 offen sc1\n"
"buffer_store_dwordx4 v[116:119], v17, s[4:7], s37 offen offset:0 sc1\n"
"buffer_store_dwordx4 v[120:123], v17, s[4:7], s37 offen offset:1024 sc1\n"
"buffer_store_dwordx4 v[124:127], v18, s[4:7], s37 offen offset:2048 sc1\n"
"ds_write_b32 v4, v4 offset:784\n"
"s_xor_b32 s79, s3, 1\n"
"s_lshl_b32 s79, s79, 8\n"
"s_add_u32 s32, s19, s79\n"
"s_cmp_ge_u32 s32, 64\n"
"s_cbranch_scc1 .Lnr_563\n"
"s_lshl_b32 s79, s19, 11\n"
"s_add_u32 s32, s79, 0x1568000\n"
"s_lshl_b32 s79, s19, 7\n"
"s_add_u32 s33, s79, 0x158a000\n"
"v_lshrrev_b32 v99, 3, v0\n"
"v_and_b32 v98, 7, v0\n"
"v_lshlrev_b32 v98, 4, v98\n"
"v_lshl_add_u32 v40, v99, 14, v98\n"
"v_mov_b32 v41, 0x3727c5ac\n"
"s_mov_b32 s89, 16\n"
".Ldl_564:\n"
"s_sub_u32 s89, s89, 1\n"
"s_cbranch_scc1 .Lde_565\n"
"s_sleep 1\n"
"s_branch .Ldl_564\n"
".Lde_565:\n"
"buffer_load_dwordx4 v[48:51], v2, s[4:7], s32 offen offset:0 sc1\n"
"buffer_load_dwordx4 v[52:55], v2, s[4:7], s32 offen offset:1024 sc1\n"
"s_mov_b32 s17, 32768\n"
".Ltp_566:\n"
"s_waitcnt vmcnt(0)\n"
"s_sub_u32 s17, s17, 1\n"
"s_cbranch_scc1 .Lou_567\n"
"v_max_u32 v38, v48, v49\n"
"v_max3_u32 v38, v50, v51, v38\n"
"v_max3_u32 v38, v52, v53, v38\n"
"v_max3_u32 v38, v54, v55, v38\n"
"v_cmp_eq_u32 vcc, -1, v38\n"
"s_cbranch_vccz .Lou_567\n"
"v_max_u32 v38, v50, v51\n"
"v_max3_u32 v38, v48, v49, v38\n"
"v_cmp_eq_u32 vcc, -1, v38\n"
"s_cbranch_vccz .Lsk_568\n"
"s_mov_b64 exec, vcc\n"
"buffer_load_dwordx4 v[48:51], v2, s[4:7], s32 offen offset:0 sc1\n"
"s_mov_b64 exec, -1\n"
".Lsk_568:\n"
"v_max_u32 v38, v54, v55\n"
"v_max3_u32 v38, v52, v53, v38\n"
"v_cmp_eq_u32 vcc, -1, v38\n"
"s_cbranch_vccz .Lsk_569\n"
"s_mov_b64 exec, vcc\n"
"buffer_load_dwordx4 v[52:55], v2, s[4:7], s32 offen offset:1024 sc1\n"
"s_mov_b64 exec, -1\n"
".Lsk_569:\n"
"s_branch .Ltp_566\n"
".Lou_567:\n"
"s_waitcnt vmcnt(0)\n"
"v_add_f32 v48, v48, v52\n"
"v_add_f32 v49, v49, v53\n"
"v_add_f32 v50, v50, v54\n"
"v_add_f32 v51, v51, v55\n"
"v_add_f32_dpp v52, v48, v48 row_ror:8 row_mask:0xf bank_mask:0xf\n"
"v_add_f32_dpp v53, v49, v49 row_ror:8 row_mask:0xf bank_mask:0xf\n"
"v_add_f32_dpp v54, v50, v50 row_ror:8 row_mask:0xf bank_mask:0xf\n"
"v_add_f32_dpp v55, v51, v51 row_ror:8 row_mask:0xf bank_mask:0xf\n"
"v_mov_b32 v48, v52\n"
"v_mov_b32 v49, v53\n"
"v_mov_b32 v50, v54\n"
"v_mov_b32 v51, v55\n"
"v_permlane16_swap_b32 v48, v52\n"
"v_permlane16_swap_b32 v49, v53\n"
"v_permlane16_swap_b32 v50, v54\n"
"v_permlane16_swap_b32 v51, v55\n"
"v_add_f32 v52, v48, v52\n"
"v_add_f32 v53, v49, v53\n"
"v_add_f32 v54, v50, v54\n"
"v_add_f32 v55, v51, v55\n"
"v_mov_b32 v48, v52\n"
"v_mov_b32 v49, v53\n"
"v_mov_b32 v50, v54\n"
"v_mov_b32 v51, v55\n"
"v_permlane32_swap_b32 v48, v52\n"
"v_permlane32_swap_b32 v49, v53\n"
"v_permlane32_swap_b32 v50, v54\n"
"v_permlane32_swap_b32 v51, v55\n"
"v_add_f32 v52, v48, v52\n"
"v_add_f32 v53, v49, v53\n"
"v_add_f32 v54, v50, v54\n"
"v_add_f32 v55, v51, v55\n"
"v_mul_f32 v48, 0x3acccccd, v52\n"
"v_mul_f32 v50, 0x3acccccd, v54\n"
"v_fma_f32 v53, -v52, v48, v53\n"
"v_fma_f32 v55, -v54, v50, v55\n"
"v_fmamk_f32 v53, v53, 0x3acccccd, v41\n"
"v_fmamk_f32 v55, v55, 0x3acccccd, v41\n"
"v_max_f32 v53, v53, v41\n"
"v_max_f32 v55, v55, v41\n"
"v_rsq_f32 v49, v53\n"
"v_rsq_f32 v51, v55\n"
"v_mul_f32 v48, -v48, v49\n"
"v_mul_f32 v50, -v50, v51\n"
"v_min_u32 v49, -2, v49\n"
"v_min_u32 v51, -2, v51\n"
"v_min_u32 v48, -2, v48\n"
"v_min_u32 v50, -2, v50\n"
"buffer_store_dwordx4 v[48:51], v40, s[4:7], s33 offen sc1\n"
"s_nop 3\n"
".Lnr_563:\n"
"s_cmp_lt_i32 s18, 0\n"
"s_cbranch_scc1 .Lnc_570\n"
"s_mov_b32 s17, 0x100000\n"
".Ltp_571:\n"
"s_sleep 1\n"
"ds_read_b32 v3, v5 offset:528\n"
"s_sub_u32 s17, s17, 1\n"
"s_cbranch_scc1 .Lou_572\n"
"s_waitcnt lgkmcnt(0)\n"
"v_cmp_eq_u32 vcc, 0, v3\n"
"s_cbranch_vccnz .Ltp_571\n"
".Lou_572:\n"
"s_waitcnt lgkmcnt(0)\n"
"s_setprio 0\n"
"s_lshl_b32 s79, s18, 8\n"
"s_add_u32 s32, s79, 0x12c8000\n"
"s_add_u32 s33, s32, 0x140000\n"
"s_and_b32 s79, s2, 7\n"
"s_lshl_b32 s79, s79, 14\n"
"s_add_u32 s34, s79, 0x158a000\n"
"s_add_u32 s35, s34, 0x1000\n"
"v_and_b32 v126, 0xffffff80, v29\n"
"v_add_u32 v126, v126, v29\n"
"ds_read_b128 v[88:91], v25 offset:448\n"
"ds_read_b128 v[92:95], v25 offset:432\n"
"ds_read_b64 v[96:97], v24 offset:448\n"
"ds_read_b64 v[98:99], v24 offset:456\n"
"ds_read_b128 v[100:103], v25 offset:960\n"
"ds_read_b128 v[104:107], v25 offset:1472\n"
"s_waitcnt lgkmcnt(1)\n"
"v_mfma_f32_16x16x32_f16 v[64:67], v[88:91], v[144:147], 0\n"
"ds_read_b128 v[40:43], v25 offset:1984\n"
"ds_read_b64 v[44:45], v24 offset:960\n"
"v_mfma_f32_16x16x32_f16 v[68:71], v[92:95], v[148:151], 0\n"
"ds_read_b64 v[46:47], v24 offset:968\n"
"ds_read_b64 v[72:73], v24 offset:1472\n"
"v_mfma_f32_16x16x32_f16 v[48:51], v[152:155], v[88:91], 0\n"
"ds_read_b64 v[74:75], v24 offset:1480\n"
"ds_read_b64 v[76:77], v24 offset:384\n"
"v_mfma_f32_16x16x32_f16 v[48:51], v[96:99], v[100:103], v[48:51]\n"
"ds_read_b64 v[78:79], v24 offset:392\n"
"s_waitcnt lgkmcnt(4)\n"
"v_mfma_f32_16x16x32_f16 v[52:55], v[96:99], v[104:107], 0\n"
"ds_read_b128 v[84:87], v25 offset:896\n"
"ds_read_b128 v[92:95], v25 offset:1408\n"
"v_mfma_f32_16x16x32_f16 v[56:59], v[96:99], v[40:43], 0\n"
"ds_read_b128 v[88:91], v25 offset:1920\n"
"ds_read_b64 v[100:101], v24 offset:896\n"
"v_mfma_f32_16x16x32_f16 v[52:55], v[44:47], v[40:43], v[52:55]\n"
"ds_read_b64 v[102:103], v24 offset:904\n"
"ds_read_b64 v[96:97], v24 offset:1408\n"
"v_cvt_pk_f16_f32 v80, v64, v65\n"
"v_mfma_f32_16x16x32_f16 v[48:51], v[44:47], v[104:107], v[48:51]\n"
"ds_read_b64 v[98:99], v24 offset:1416\n"
"s_waitcnt lgkmcnt(4)\n"
"v_mfma_f32_16x16x32_f16 v[48:51], v[72:75], v[40:43], v[48:51]\n"
"ds_read_b128 v[104:107], v25 offset:960\n"
"v_cvt_pk_f16_f32 v81, v66, v67\n"
"v_mfma_f32_16x16x32_f16 v[48:51], v[76:79], v[84:87], v[48:51]\n"
"ds_read_b128 v[44:47], v25 offset:944\n"
"v_cvt_pk_f16_f32 v82, v68, v69\n"
"v_mfma_f32_16x16x32_f16 v[52:55], v[76:79], v[92:95], v[52:55]\n"
"ds_read_b64 v[40:41], v24 offset:320\n"
"ds_read_b64 v[42:43], v24 offset:328\n"
"v_mfma_f32_16x16x32_f16 v[56:59], v[76:79], v[88:91], v[56:59]\n"
"ds_read_b128 v[72:75], v25 offset:832\n"
"s_waitcnt lgkmcnt(5)\n"
"v_mfma_f32_16x16x32_f16 v[52:55], v[100:103], v[88:91], v[52:55]\n"
"ds_read_b128 v[84:87], v25 offset:1344\n"
"v_cvt_pk_f16_f32 v83, v70, v71\n"
"v_mfma_f32_16x16x32_f16 v[48:51], v[100:103], v[92:95], v[48:51]\n"
"ds_read_b128 v[76:79], v25 offset:1856\n"
"ds_read_b64 v[92:93], v24 offset:832\n"
"v_mfma_f32_16x16x32_f16 v[48:51], v[96:99], v[88:91], v[48:51]\n"
"ds_read_b64 v[94:95], v24 offset:840\n"
"v_mfma_f32_16x16x32_f16 v[48:51], v[80:83], v[128:131], v[48:51]\n"
"ds_read_b64 v[100:101], v24 offset:1344\n"
"s_waitcnt lgkmcnt(9)\n"
"v_mfma_f32_16x16x32_f16 v[52:55], v[80:83], v[132:135], v[52:55]\n"
"ds_read_b64 v[102:103], v24 offset:1352\n"
"ds_read_b64 v[88:89], v24 offset:256\n"
"v_mfma_f32_16x16x32_f16 v[56:59], v[80:83], v[136:139], v[56:59]\n"
"ds_read_b64 v[90:91], v24 offset:264\n"
"v_mfma_f32_16x16x32_f16 v[60:63], v[80:83], v[140:143], 0\n"
"ds_read_b128 v[96:99], v25 offset:768\n"
"v_mfma_f32_16x16x32_f16 v[64:67], v[104:107], v[144:147], 0\n"
"s_waitcnt lgkmcnt(8)\n"
"v_mfma_f32_16x16x32_f16 v[68:71], v[44:47], v[148:151], 0\n"
"ds_read_b128 v[44:47], v25 offset:1280\n"
"v_mfma_f32_16x16x32_f16 v[52:55], v[152:155], v[104:107], v[52:55]\n"
"ds_read_b128 v[104:107], v25 offset:1792\n"
"v_mfma_f32_16x16x32_f16 v[48:51], v[40:43], v[72:75], v[48:51]\n"
"ds_read_b64 v[72:73], v24 offset:768\n"
"v_mfma_f32_16x16x32_f16 v[52:55], v[40:43], v[84:87], v[52:55]\n"
"ds_read_b64 v[74:75], v24 offset:776\n"
"s_waitcnt lgkmcnt(7)\n"
"v_mfma_f32_16x16x32_f16 v[56:59], v[40:43], v[76:79], v[56:59]\n"
"ds_read_b64 v[40:41], v24 offset:1280\n"
"v_mfma_f32_16x16x32_f16 v[52:55], v[92:95], v[76:79], v[52:55]\n"
"ds_read_b64 v[42:43], v24 offset:1288\n"
"v_cvt_pk_f16_f32 v80, v64, v65\n"
"v_mfma_f32_16x16x32_f16 v[48:51], v[92:95], v[84:87], v[48:51]\n"
"ds_read_b128 v[84:87], v25 offset:1472\n"
"v_cvt_pk_f16_f32 v81, v66, v67\n"
"v_mfma_f32_16x16x32_f16 v[48:51], v[100:103], v[76:79], v[48:51]\n"
"ds_read_b128 v[92:95], v25 offset:1456\n"
"s_waitcnt lgkmcnt(4)\n"
"v_mfma_f32_16x16x32_f16 v[48:51], v[88:91], v[96:99], v[48:51]\n"
"ds_read_b64 v[76:77], v24 offset:192\n"
"ds_read_b64 v[78:79], v24 offset:200\n"
"v_mfma_f32_16x16x32_f16 v[52:55], v[88:91], v[44:47], v[52:55]\n"
"ds_read_b128 v[100:103], v25 offset:704\n"
"v_cvt_pk_f16_f32 v82, v68, v69\n"
"v_mfma_f32_16x16x32_f16 v[56:59], v[88:91], v[104:107], v[56:59]\n"
"ds_read_b128 v[96:99], v25 offset:1216\n"
"v_cvt_pk_f16_f32 v83, v70, v71\n"
"v_mfma_f32_16x16x32_f16 v[52:55], v[72:75], v[104:107], v[52:55]\n"
"ds_read_b128 v[88:91], v25 offset:1728\n"
"s_waitcnt lgkmcnt(7)\n"
"v_mfma_f32_16x16x32_f16 v[48:51], v[72:75], v[44:47], v[48:51]\n"
"ds_read_b64 v[44:45], v24 offset:704\n"
"v_mfma_f32_16x16x32_f16 v[48:51], v[40:43], v[104:107], v[48:51]\n"
"ds_read_b64 v[46:47], v24 offset:712\n"
"ds_read_b64 v[72:73], v24 offset:1216\n"
"v_mfma_f32_16x16x32_f16 v[52:55], v[80:83], v[128:131], v[52:55]\n"
"ds_read_b64 v[74:75], v24 offset:1224\n"
"ds_read_b64 v[104:105], v24 offset:128\n"
"v_mfma_f32_16x16x32_f16 v[56:59], v[80:83], v[132:135], v[56:59]\n"
"ds_read_b64 v[106:107], v24 offset:136\n"
"s_waitcnt lgkmcnt(11)\n"
"v_mfma_f32_16x16x32_f16 v[60:63], v[80:83], v[136:139], v[60:63]\n"
"ds_read_b128 v[40:43], v25 offset:640\n"
"v_mfma_f32_16x16x32_f16 v[64:67], v[84:87], v[144:147], 0\n"
"v_mfma_f32_16x16x32_f16 v[68:71], v[92:95], v[148:151], 0\n"
"ds_read_b128 v[92:95], v25 offset:1152\n"
"v_mfma_f32_16x16x32_f16 v[56:59], v[152:155], v[84:87], v[56:59]\n"
"ds_read_b128 v[84:87], v25 offset:1664\n"
"s_waitcnt lgkmcnt(7)\n"
"v_mfma_f32_16x16x32_f16 v[48:51], v[76:79], v[100:103], v[48:51]\n"
"ds_read_b64 v[100:101], v24 offset:640\n"
"v_mfma_f32_16x16x32_f16 v[52:55], v[76:79], v[96:99], v[52:55]\n"
"ds_read_b64 v[102:103], v24 offset:648\n"
"v_mfma_f32_16x16x32_f16 v[56:59], v[76:79], v[88:91], v[56:59]\n"
"ds_read_b64 v[76:77], v24 offset:1152\n"
"v_mfma_f32_16x16x32_f16 v[52:55], v[44:47], v[88:91], v[52:55]\n"
"ds_read_b64 v[78:79], v24 offset:1160\n"
"s_waitcnt lgkmcnt(5)\n"
"v_mfma_f32_16x16x32_f16 v[48:51], v[44:47], v[96:99], v[48:51]\n"
"ds_read_b128 v[96:99], v25 offset:1984\n"
"v_cvt_pk_f16_f32 v80, v64, v65\n"
"v_mfma_f32_16x16x32_f16 v[48:51], v[72:75], v[88:91], v[48:51]\n"
"ds_read_b128 v[44:47], v25 offset:1968\n"
"ds_read_b64 v[88:89], v24 offset:64\n"
"v_mfma_f32_16x16x32_f16 v[48:51], v[104:107], v[40:43], v[48:51]\n"
"ds_read_b64 v[90:91], v24 offset:72\n"
"ds_read_b128 v[72:75], v25 offset:576\n"
"v_mfma_f32_16x16x32_f16 v[52:55], v[104:107], v[92:95], v[52:55]\n"
"ds_read_b128 v[40:43], v25 offset:1088\n"
"s_waitcnt lgkmcnt(6)\n"
"v_mfma_f32_16x16x32_f16 v[56:59], v[104:107], v[84:87], v[56:59]\n"
"ds_read_b128 v[104:107], v25 offset:1600\n"
"v_cvt_pk_f16_f32 v81, v66, v67\n"
"v_mfma_f32_16x16x32_f16 v[52:55], v[100:103], v[84:87], v[52:55]\n"
"v_cvt_pk_f16_f32 v82, v68, v69\n"
"v_cvt_pk_f16_f32 v83, v70, v71\n"
"v_mfma_f32_16x16x32_f16 v[48:51], v[100:103], v[92:95], v[48:51]\n"
"ds_read_b64 v[92:93], v24 offset:576\n"
"ds_read_b64 v[94:95], v24 offset:584\n"
"v_mfma_f32_16x16x32_f16 v[48:51], v[76:79], v[84:87], v[48:51]\n"
"ds_read_b64 v[100:101], v24 offset:1088\n"
"s_waitcnt lgkmcnt(8)\n"
"v_mfma_f32_16x16x32_f16 v[56:59], v[80:83], v[128:131], v[56:59]\n"
"ds_read_b64 v[102:103], v24 offset:1096\n"
"ds_read_b64 v[84:85], v24 offset:0\n"
"v_mfma_f32_16x16x32_f16 v[60:63], v[80:83], v[132:135], v[60:63]\n"
"ds_read_b64 v[86:87], v24 offset:8\n"
"v_mfma_f32_16x16x32_f16 v[64:67], v[96:99], v[144:147], 0\n"
"ds_read_b128 v[76:79], v25 offset:512\n"
"v_mfma_f32_16x16x32_f16 v[68:71], v[44:47], v[148:151], 0\n"
"ds_read_b128 v[44:47], v25 offset:1024\n"
"s_waitcnt lgkmcnt(8)\n"
"v_mfma_f32_16x16x32_f16 v[60:63], v[152:155], v[96:99], v[60:63]\n"
"ds_read_b128 v[96:99], v25 offset:1536\n"
"v_mfma_f32_16x16x32_f16 v[48:51], v[88:91], v[72:75], v[48:51]\n"
"ds_read_b64 v[72:73], v24 offset:512\n"
"v_mfma_f32_16x16x32_f16 v[52:55], v[88:91], v[40:43], v[52:55]\n"
"ds_read_b64 v[74:75], v24 offset:520\n"
"v_mfma_f32_16x16x32_f16 v[56:59], v[88:91], v[104:107], v[56:59]\n"
"ds_read_b64 v[88:89], v24 offset:1024\n"
"s_waitcnt lgkmcnt(5)\n"
"v_mfma_f32_16x16x32_f16 v[52:55], v[92:95], v[104:107], v[52:55]\n"
"ds_read_b64 v[90:91], v24 offset:1032\n"
"v_cvt_pk_f16_f32 v80, v64, v65\n"
"v_mfma_f32_16x16x32_f16 v[48:51], v[92:95], v[40:43], v[48:51]\n"
"v_cvt_pk_f16_f32 v81, v66, v67\n"
"v_cvt_pk_f16_f32 v82, v68, v69\n"
"v_mfma_f32_16x16x32_f16 v[48:51], v[100:103], v[104:107], v[48:51]\n"
"v_cvt_pk_f16_f32 v83, v70, v71\n"
"v_mfma_f32_16x16x32_f16 v[48:51], v[84:87], v[76:79], v[48:51]\n"
"s_waitcnt lgkmcnt(2)\n"
"v_mfma_f32_16x16x32_f16 v[52:55], v[84:87], v[44:47], v[52:55]\n"
"v_mfma_f32_16x16x32_f16 v[56:59], v[84:87], v[96:99], v[56:59]\n"
"v_mfma_f32_16x16x32_f16 v[52:55], v[72:75], v[96:99], v[52:55]\n"
"v_mfma_f32_16x16x32_f16 v[48:51], v[72:75], v[44:47], v[48:51]\n"
"s_waitcnt lgkmcnt(0)\n"
"v_mfma_f32_16x16x32_f16 v[48:51], v[88:91], v[96:99], v[48:51]\n"
"v_mfma_f32_16x16x32_f16 v[60:63], v[80:83], v[128:131], v[60:63]\n"
"s_setprio 2\n"
"s_mov_b32 s17, 0x100000\n"
".Ltp_573:\n"
"ds_read_b32 v3, v5 offset:784\n"
"s_sub_u32 s17, s17, 1\n"
"s_cbranch_scc1 .Lou_574\n"
"s_waitcnt lgkmcnt(0)\n"
"v_cmp_eq_u32 vcc, 0, v3\n"
"s_cbranch_vccnz .Ltp_573\n"
".Lou_574:\n"
"s_waitcnt lgkmcnt(0)\n"
"buffer_load_dwordx4 v[110:113], v126, s[4:7], s32 offen offset:0 sc1\n"
"buffer_load_dwordx4 v[114:117], v126, s[4:7], s32 offen offset:128 sc1\n"
"buffer_load_dwordx4 v[118:121], v126, s[4:7], s33 offen offset:0 sc1\n"
"buffer_load_dwordx4 v[122:125], v126, s[4:7], s33 offen offset:128 sc1\n"
"ds_read_b64 v[128:129], v24 offset:1984\n"
"ds_read_b64 v[130:131], v24 offset:1992\n"
"ds_read_b64 v[132:133], v24 offset:1920\n"
"ds_read_b64 v[134:135], v24 offset:1928\n"
"ds_read_b64 v[136:137], v24 offset:1856\n"
"ds_read_b64 v[138:139], v24 offset:1864\n"
"ds_read_b64 v[140:141], v24 offset:1792\n"
"ds_read_b64 v[142:143], v24 offset:1800\n"
"ds_read_b64 v[144:145], v24 offset:1728\n"
"ds_read_b64 v[146:147], v24 offset:1736\n"
"ds_read_b64 v[148:149], v24 offset:1664\n"
"ds_read_b64 v[150:151], v24 offset:1672\n"
"ds_read_b64 v[152:153], v24 offset:1600\n"
"ds_read_b64 v[154:155], v24 offset:1608\n"
"ds_read_b64 v[156:157], v24 offset:1536\n"
"ds_read_b64 v[158:159], v24 offset:1544\n"
"ds_read_b64 v[160:161], v24 offset:1472\n"
"ds_read_b64 v[162:163], v24 offset:1480\n"
"ds_read_b64 v[164:165], v24 offset:1408\n"
"ds_read_b64 v[166:167], v24 offset:1416\n"
"ds_read_b64 v[168:169], v24 offset:1344\n"
"ds_read_b64 v[170:171], v24 offset:1352\n"
"ds_read_b64 v[172:173], v24 offset:1280\n"
"ds_read_b64 v[174:175], v24 offset:1288\n"
"ds_read_b64 v[176:177], v24 offset:1216\n"
"ds_read_b64 v[178:179], v24 offset:1224\n"
"ds_read_b64 v[180:181], v24 offset:1152\n"
"ds_read_b64 v[182:183], v24 offset:1160\n"
"ds_read_b64 v[184:185], v24 offset:1088\n"
"ds_read_b64 v[186:187], v24 offset:1096\n"
"ds_read_b64 v[188:189], v24 offset:1024\n"
"ds_read_b64 v[190:191], v24 offset:1032\n"
"ds_read_b64 v[192:193], v24 offset:960\n"
"ds_read_b64 v[194:195], v24 offset:968\n"
"ds_read_b64 v[196:197], v24 offset:896\n"
"ds_read_b64 v[198:199], v24 offset:904\n"
"ds_read_b64 v[200:201], v24 offset:832\n"
"ds_read_b64 v[202:203], v24 offset:840\n"
"ds_read_b64 v[204:205], v24 offset:768\n"
"ds_read_b64 v[206:207], v24 offset:776\n"
"ds_read_b64 v[208:209], v24 offset:704\n"
"ds_read_b64 v[210:211], v24 offset:712\n"
"ds_read_b64 v[212:213], v24 offset:640\n"
"ds_read_b64 v[214:215], v24 offset:648\n"
"ds_read_b64 v[216:217], v24 offset:576\n"
"ds_read_b64 v[218:219], v24 offset:584\n"
"ds_read_b64 v[220:221], v24 offset:512\n"
"ds_read_b64 v[222:223], v24 offset:520\n"
"ds_read_b64 v[224:225], v24 offset:448\n"
"ds_read_b64 v[226:227], v24 offset:456\n"
"ds_read_b64 v[228:229], v24 offset:384\n"
"ds_read_b64 v[230:231], v24 offset:392\n"
"ds_read_b64 v[232:233], v24 offset:320\n"
"ds_read_b64 v[234:235], v24 offset:328\n"
"ds_read_b64 v[236:237], v24 offset:256\n"
"ds_read_b64 v[238:239], v24 offset:264\n"
"ds_read_b64 v[240:241], v24 offset:192\n"
"ds_read_b64 v[242:243], v24 offset:200\n"
"ds_read_b64 v[244:245], v24 offset:128\n"
"ds_read_b64 v[246:247], v24 offset:136\n"
"ds_read_b64 v[248:249], v24 offset:64\n"
"ds_read_b64 v[250:251], v24 offset:72\n"
"ds_read_b64 v[252:253], v24 offset:0\n"
"ds_read_b64 v[254:255], v24 offset:8\n"
"s_mov_b32 s38, 0x05040100\n"
"s_mov_b32 s39, 0x07060302\n"
"s_mov_b32 s17, 32768\n"
".Ltp_575:\n"
"s_waitcnt vmcnt(0)\n"
"s_sub_u32 s17, s17, 1\n"
"s_cbranch_scc1 .Lou_576\n"
"v_bitop3_b32 v64, v111, v113, s16 bitop3:0x7e\n"
"v_bitop3_b32 v65, v115, v117, s16 bitop3:0x7e\n"
"v_bitop3_b32 v66, v119, v121, s16 bitop3:0x7e\n"
"v_bitop3_b32 v67, v123, v125, s16 bitop3:0x7e\n"
"v_or3_b32 v68, v64, v65, v66\n"
"v_or_b32 v68, v68, v67\n"
"v_cmp_ne_u32 vcc, 0, v68\n"
"s_cbranch_vccz .Lou_576\n"
"s_sleep 2\n"
"v_cmp_ne_u32 vcc, 0, v64\n"
"s_cbranch_vccz .Lsk_577\n"
"s_mov_b64 exec, vcc\n"
"buffer_load_dwordx4 v[110:113], v126, s[4:7], s32 offen offset:0 sc1\n"
"s_mov_b64 exec, -1\n"
".Lsk_577:\n"
"v_cmp_ne_u32 vcc, 0, v65\n"
"s_cbranch_vccz .Lsk_578\n"
"s_mov_b64 exec, vcc\n"
"buffer_load_dwordx4 v[114:117], v126, s[4:7], s32 offen offset:128 sc1\n"
"s_mov_b64 exec, -1\n"
".Lsk_578:\n"
"v_cmp_ne_u32 vcc, 0, v66\n"
"s_cbranch_vccz .Lsk_579\n"
"s_mov_b64 exec, vcc\n"
"buffer_load_dwordx4 v[118:121], v126, s[4:7], s33 offen offset:0 sc1\n"
"s_mov_b64 exec, -1\n"
".Lsk_579:\n"
"v_cmp_ne_u32 vcc, 0, v67\n"
"s_cbranch_vccz .Lsk_580\n"
"s_mov_b64 exec, vcc\n"
"buffer_load_dwordx4 v[122:125], v126, s[4:7], s33 offen offset:128 sc1\n"
"s_mov_b64 exec, -1\n"
".Lsk_580:\n"
"s_branch .Ltp_575\n"
".Lou_576:\n"
"s_waitcnt vmcnt(0)\n"
"buffer_load_dwordx4 v[88:91], v30, s[4:7], s34 offen sc1\n"
"buffer_load_dwordx4 v[92:95], v30, s[4:7], s35 offen sc1\n"
"v_perm_b32 v64, v114, v110, s38\n"
"v_perm_b32 v65, v114, v110, s39\n"
"v_perm_b32 v66, v116, v112, s38\n"
"v_perm_b32 v67, v116, v112, s39\n"
"ds_write_b32 v32, v64 offset:0\n"
"ds_write_b32 v33, v65 offset:2048\n"
"ds_write_b32 v32, v66 offset:4224\n"
"ds_write_b32 v33, v67 offset:6272\n"
"v_perm_b32 v64, v122, v118, s38\n"
"v_perm_b32 v65, v122, v118, s39\n"
"v_perm_b32 v66, v124, v120, s38\n"
"v_perm_b32 v67, v124, v120, s39\n"
"ds_write_b32 v32, v64 offset:1024\n"
"ds_write_b32 v33, v65 offset:3072\n"
"ds_write_b32 v32, v66 offset:5248\n"
"ds_write_b32 v33, v67 offset:7296\n"
"v_lshlrev_b32 v64, 16, v110\n"
"v_lshlrev_b32 v65, 16, v114\n"
"v_and_b32 v66, 0xffff0000, v110\n"
"v_and_b32 v67, 0xffff0000, v114\n"
"v_lshlrev_b32 v68, 16, v112\n"
"v_lshlrev_b32 v69, 16, v116\n"
"v_and_b32 v70, 0xffff0000, v112\n"
"v_and_b32 v71, 0xffff0000, v116\n"
"v_lshlrev_b32 v72, 16, v118\n"
"v_lshlrev_b32 v73, 16, v122\n"
"v_and_b32 v74, 0xffff0000, v118\n"
"v_and_b32 v75, 0xffff0000, v122\n"
"v_lshlrev_b32 v76, 16, v120\n"
"v_lshlrev_b32 v77, 16, v124\n"
"v_and_b32 v78, 0xffff0000, v120\n"
"v_and_b32 v79, 0xffff0000, v124\n"
"s_mov_b32 s17, 32768\n"
".Ltp_581:\n"
"s_waitcnt vmcnt(0)\n"
"s_sub_u32 s17, s17, 1\n"
"s_cbranch_scc1 .Lou_582\n"
"v_max_u32 v110, v88, v89\n"
"v_max3_u32 v110, v90, v91, v110\n"
"v_max3_u32 v110, v92, v93, v110\n"
"v_max3_u32 v110, v94, v95, v110\n"
"v_cmp_eq_u32 vcc, -1, v110\n"
"s_cbranch_vccz .Lou_582\n"
"v_max_u32 v110, v90, v91\n"
"v_max3_u32 v110, v88, v89, v110\n"
"v_cmp_eq_u32 vcc, -1, v110\n"
"s_cbranch_vccz .Lsk_583\n"
"s_mov_b64 exec, vcc\n"
"buffer_load_dwordx4 v[88:91], v30, s[4:7], s34 offen sc1\n"
"s_mov_b64 exec, -1\n"
".Lsk_583:\n"
"v_max_u32 v110, v94, v95\n"
"v_max3_u32 v110, v92, v93, v110\n"
"v_cmp_eq_u32 vcc, -1, v110\n"
"s_cbranch_vccz .Lsk_584\n"
"s_mov_b64 exec, vcc\n"
"buffer_load_dwordx4 v[92:95], v30, s[4:7], s35 offen sc1\n"
"s_mov_b64 exec, -1\n"
".Lsk_584:\n"
"s_branch .Ltp_581\n"
".Lou_582:\n"
"s_waitcnt vmcnt(0)\n"
"v_fma_f32 v64, v64, v89, v88\n"
"v_fma_f32 v65, v65, v91, v90\n"
"v_cvt_pk_f16_f32 v64, v64, v65\n"
"v_fma_f32 v66, v66, v89, v88\n"
"v_fma_f32 v67, v67, v91, v90\n"
"v_cvt_pk_f16_f32 v66, v66, v67\n"
"v_fma_f32 v68, v68, v89, v88\n"
"v_fma_f32 v69, v69, v91, v90\n"
"v_cvt_pk_f16_f32 v68, v68, v69\n"
"v_fma_f32 v70, v70, v89, v88\n"
"v_fma_f32 v71, v71, v91, v90\n"
"v_cvt_pk_f16_f32 v70, v70, v71\n"
"ds_write2st64_b32 v31, v64, v66 offset0:0 offset1:18\n"
"ds_write2st64_b32 v31, v68, v70 offset0:36 offset1:54\n"
"v_fma_f32 v72, v72, v93, v92\n"
"v_fma_f32 v73, v73, v95, v94\n"
"v_cvt_pk_f16_f32 v72, v72, v73\n"
"v_fma_f32 v74, v74, v93, v92\n"
"v_fma_f32 v75, v75, v95, v94\n"
"v_cvt_pk_f16_f32 v74, v74, v75\n"
"v_fma_f32 v76, v76, v93, v92\n"
"v_fma_f32 v77, v77, v95, v94\n"
"v_cvt_pk_f16_f32 v76, v76, v77\n"
"v_fma_f32 v78, v78, v93, v92\n"
"v_fma_f32 v79, v79, v95, v94\n"
"v_cvt_pk_f16_f32 v78, v78, v79\n"
"ds_write2st64_b32 v31, v72, v74 offset0:4 offset1:22\n"
"ds_write2st64_b32 v31, v76, v78 offset0:40 offset1:58\n"
"ds_write_b32 v4, v4 offset:800\n"
"s_mov_b32 s17, 0x100000\n"
".Ltp_585:\n"
"ds_read_b32 v3, v5 offset:800\n"
"s_sub_u32 s17, s17, 1\n"
"s_cbranch_scc1 .Lou_586\n"
"s_waitcnt lgkmcnt(0)\n"
"v_cmp_eq_u32 vcc, 0, v3\n"
"s_cbranch_vccnz .Ltp_585\n"
".Lou_586:\n"
"s_waitcnt lgkmcnt(0)\n"
"s_mov_b32 s36, 0xfffefffe\n"
"ds_read_b64 v[88:89], v26 offset:0\n"
"ds_read_b64 v[92:93], v26 offset:512\n"
"ds_read_b64 v[96:97], v26 offset:1024\n"
"ds_read_b64 v[100:101], v26 offset:1536\n"
"ds_read_b128 v[104:107], v25 offset:2496\n"
"s_waitcnt lgkmcnt(1)\n"
"v_mfma_f32_16x16x16_bf16 v[48:51], v[34:35], v[88:89], v[48:51]\n"
"ds_read_b128 v[108:111], v25 offset:3008\n"
"ds_read_b128 v[112:115], v25 offset:2432\n"
"v_mfma_f32_16x16x16_bf16 v[52:55], v[34:35], v[92:93], v[52:55]\n"
"ds_read_b128 v[116:119], v25 offset:2944\n"
"ds_read_b128 v[120:123], v25 offset:2368\n"
"v_mfma_f32_16x16x16_bf16 v[56:59], v[34:35], v[96:97], v[56:59]\n"
"ds_read_b128 v[124:127], v25 offset:2880\n"
"v_mfma_f32_16x16x16_bf16 v[60:63], v[34:35], v[100:101], v[60:63]\n"
"ds_read_b128 v[40:43], v25 offset:2304\n"
"s_waitcnt lgkmcnt(4)\n"
"v_mfma_f32_16x16x32_f16 v[48:51], v[128:131], v[104:107], v[48:51]\n"
"ds_read_b128 v[44:47], v25 offset:2816\n"
"v_mfma_f32_16x16x32_f16 v[52:55], v[160:163], v[104:107], v[52:55]\n"
"ds_read_b128 v[88:91], v25 offset:2240\n"
"v_mfma_f32_16x16x32_f16 v[52:55], v[128:131], v[108:111], v[52:55]\n"
"ds_read_b128 v[92:95], v25 offset:2752\n"
"v_mfma_f32_16x16x32_f16 v[48:51], v[132:135], v[112:115], v[48:51]\n"
"ds_read_b128 v[96:99], v25 offset:2176\n"
"s_waitcnt lgkmcnt(6)\n"
"v_mfma_f32_16x16x32_f16 v[52:55], v[164:167], v[112:115], v[52:55]\n"
"v_mfma_f32_16x16x32_f16 v[52:55], v[132:135], v[116:119], v[52:55]\n"
"ds_read_b128 v[100:103], v25 offset:2688\n"
"v_mfma_f32_16x16x32_f16 v[48:51], v[136:139], v[120:123], v[48:51]\n"
"ds_read_b128 v[104:107], v25 offset:2112\n"
"v_mfma_f32_16x16x32_f16 v[52:55], v[168:171], v[120:123], v[52:55]\n"
"s_waitcnt lgkmcnt(5)\n"
"v_mfma_f32_16x16x32_f16 v[52:55], v[136:139], v[124:127], v[52:55]\n"
"ds_read_b128 v[108:111], v25 offset:2624\n"
"v_mfma_f32_16x16x32_f16 v[48:51], v[140:143], v[40:43], v[48:51]\n"
"ds_read_b128 v[112:115], v25 offset:2048\n"
"v_mfma_f32_16x16x32_f16 v[52:55], v[172:175], v[40:43], v[52:55]\n"
"v_mfma_f32_16x16x32_f16 v[52:55], v[140:143], v[44:47], v[52:55]\n"
"ds_read_b128 v[116:119], v25 offset:2560\n"
"s_waitcnt lgkmcnt(5)\n"
"v_mfma_f32_16x16x32_f16 v[48:51], v[144:147], v[88:91], v[48:51]\n"
"ds_read_b128 v[120:123], v25 offset:2496\n"
"v_mfma_f32_16x16x32_f16 v[52:55], v[176:179], v[88:91], v[52:55]\n"
"ds_read_b128 v[124:127], v25 offset:3008\n"
"v_mfma_f32_16x16x32_f16 v[52:55], v[144:147], v[92:95], v[52:55]\n"
"ds_read_b128 v[40:43], v25 offset:3520\n"
"v_mfma_f32_16x16x32_f16 v[48:51], v[148:151], v[96:99], v[48:51]\n"
"ds_read_b128 v[44:47], v25 offset:2432\n"
"s_waitcnt lgkmcnt(7)\n"
"v_mfma_f32_16x16x32_f16 v[52:55], v[180:183], v[96:99], v[52:55]\n"
"ds_read_b128 v[88:91], v25 offset:2944\n"
"v_mfma_f32_16x16x32_f16 v[52:55], v[148:151], v[100:103], v[52:55]\n"
"ds_read_b128 v[92:95], v25 offset:3456\n"
"v_mfma_f32_16x16x32_f16 v[48:51], v[152:155], v[104:107], v[48:51]\n"
"ds_read_b128 v[96:99], v25 offset:2368\n"
"v_mfma_f32_16x16x32_f16 v[52:55], v[184:187], v[104:107], v[52:55]\n"
"ds_read_b128 v[100:103], v25 offset:2880\n"
"s_waitcnt lgkmcnt(8)\n"
"v_mfma_f32_16x16x32_f16 v[52:55], v[152:155], v[108:111], v[52:55]\n"
"ds_read_b128 v[104:107], v25 offset:3392\n"
"v_mfma_f32_16x16x32_f16 v[48:51], v[156:159], v[112:115], v[48:51]\n"
"ds_read_b128 v[108:111], v25 offset:2304\n"
"v_mfma_f32_16x16x32_f16 v[52:55], v[188:191], v[112:115], v[52:55]\n"
"ds_read_b128 v[112:115], v25 offset:2816\n"
"v_mfma_f32_16x16x32_f16 v[52:55], v[156:159], v[116:119], v[52:55]\n"
"ds_read_b128 v[116:119], v25 offset:3328\n"
"s_waitcnt lgkmcnt(8)\n"
"v_mfma_f32_16x16x32_f16 v[56:59], v[192:195], v[120:123], v[56:59]\n"
"ds_read_b128 v[120:123], v25 offset:2240\n"
"v_mfma_f32_16x16x32_f16 v[56:59], v[160:163], v[124:127], v[56:59]\n"
"ds_read_b128 v[124:127], v25 offset:2752\n"
"v_mfma_f32_16x16x32_f16 v[56:59], v[128:131], v[40:43], v[56:59]\n"
"ds_read_b128 v[40:43], v25 offset:3264\n"
"v_mul_f32 v48, v36, v48\n"
"v_mul_f32 v49, v36, v49\n"
"v_mul_f32 v50, v36, v50\n"
"v_mfma_f32_16x16x32_f16 v[56:59], v[196:199], v[44:47], v[56:59]\n"
"ds_read_b128 v[44:47], v25 offset:2176\n"
"v_mul_f32 v51, v36, v51\n"
"v_cvt_pk_bf16_f32 v48, v48, v49\n"
"s_waitcnt lgkmcnt(8)\n"
"v_mfma_f32_16x16x32_f16 v[56:59], v[164:167], v[88:91], v[56:59]\n"
"ds_read_b128 v[88:91], v25 offset:2688\n"
"v_cvt_pk_bf16_f32 v49, v50, v51\n"
"v_pk_min_u16 v48, v48, s36\n"
"v_pk_min_u16 v49, v49, s36\n"
"v_mfma_f32_16x16x32_f16 v[56:59], v[132:135], v[92:95], v[56:59]\n"
"ds_read_b128 v[92:95], v25 offset:3200\n"
"ds_write_b64 v26, v[48:49] offset:0\n"
"v_mfma_f32_16x16x32_f16 v[56:59], v[200:203], v[96:99], v[56:59]\n"
"ds_read_b128 v[96:99], v25 offset:2112\n"
"v_mul_f32 v52, v36, v52\n"
"v_mul_f32 v53, v36, v53\n"
"v_mul_f32 v54, v36, v54\n"
"v_mfma_f32_16x16x32_f16 v[56:59], v[168:171], v[100:103], v[56:59]\n"
"ds_read_b128 v[100:103], v25 offset:2624\n"
"v_mul_f32 v55, v36, v55\n"
"v_cvt_pk_bf16_f32 v52, v52, v53\n"
"s_waitcnt lgkmcnt(9)\n"
"v_mfma_f32_16x16x32_f16 v[56:59], v[136:139], v[104:107], v[56:59]\n"
"ds_read_b128 v[104:107], v25 offset:3136\n"
"v_cvt_pk_bf16_f32 v53, v54, v55\n"
"v_pk_min_u16 v52, v52, s36\n"
"v_pk_min_u16 v53, v53, s36\n"
"v_mfma_f32_16x16x32_f16 v[56:59], v[204:207], v[108:111], v[56:59]\n"
"ds_read_b128 v[108:111], v25 offset:2048\n"
"ds_write_b64 v26, v[52:53] offset:512\n"
"v_mfma_f32_16x16x32_f16 v[56:59], v[172:175], v[112:115], v[56:59]\n"
"ds_read_b128 v[112:115], v25 offset:2560\n"
"ds_write_b32 v4, v4 offset:592\n"
"v_mfma_f32_16x16x32_f16 v[56:59], v[140:143], v[116:119], v[56:59]\n"
"ds_read_b128 v[116:119], v25 offset:3072\n"
"s_waitcnt lgkmcnt(11)\n"
"v_mfma_f32_16x16x32_f16 v[56:59], v[208:211], v[120:123], v[56:59]\n"
"ds_read_b128 v[120:123], v25 offset:2496\n"
"v_mfma_f32_16x16x32_f16 v[56:59], v[176:179], v[124:127], v[56:59]\n"
"ds_read_b128 v[124:127], v25 offset:3008\n"
"v_mfma_f32_16x16x32_f16 v[56:59], v[144:147], v[40:43], v[56:59]\n"
"ds_read_b128 v[40:43], v25 offset:3520\n"
"v_mfma_f32_16x16x32_f16 v[56:59], v[212:215], v[44:47], v[56:59]\n"
"ds_read_b128 v[44:47], v25 offset:4032\n"
"s_waitcnt lgkmcnt(10)\n"
"v_mfma_f32_16x16x32_f16 v[56:59], v[180:183], v[88:91], v[56:59]\n"
"ds_read_b128 v[88:91], v25 offset:2432\n"
"v_mfma_f32_16x16x32_f16 v[56:59], v[148:151], v[92:95], v[56:59]\n"
"ds_read_b128 v[92:95], v25 offset:2944\n"
"v_mfma_f32_16x16x32_f16 v[56:59], v[216:219], v[96:99], v[56:59]\n"
"ds_read_b128 v[96:99], v25 offset:3456\n"
"v_mfma_f32_16x16x32_f16 v[56:59], v[184:187], v[100:103], v[56:59]\n"
"ds_read_b128 v[100:103], v25 offset:3968\n"
"s_waitcnt lgkmcnt(8)\n"
"v_mfma_f32_16x16x32_f16 v[56:59], v[152:155], v[104:107], v[56:59]\n"
"ds_read_b128 v[104:107], v25 offset:2368\n"
"v_mfma_f32_16x16x32_f16 v[56:59], v[220:223], v[108:111], v[56:59]\n"
"ds_read_b128 v[108:111], v25 offset:2880\n"
"v_mfma_f32_16x16x32_f16 v[56:59], v[188:191], v[112:115], v[56:59]\n"
"ds_read_b128 v[112:115], v25 offset:3392\n"
"v_mfma_f32_16x16x32_f16 v[56:59], v[156:159], v[116:119], v[56:59]\n"
"ds_read_b128 v[116:119], v25 offset:3904\n"
"s_waitcnt lgkmcnt(8)\n"
"v_mfma_f32_16x16x32_f16 v[60:63], v[224:227], v[120:123], v[60:63]\n"
"ds_read_b128 v[120:123], v25 offset:2304\n"
"v_mfma_f32_16x16x32_f16 v[60:63], v[192:195], v[124:127], v[60:63]\n"
"ds_read_b128 v[124:127], v25 offset:2816\n"
"v_mfma_f32_16x16x32_f16 v[60:63], v[160:163], v[40:43], v[60:63]\n"
"ds_read_b128 v[40:43], v25 offset:3328\n"
"v_mul_f32 v56, v36, v56\n"
"v_mul_f32 v57, v36, v57\n"
"v_mul_f32 v58, v36, v58\n"
"v_mul_f32 v59, v36, v59\n"
"v_mfma_f32_16x16x32_f16 v[60:63], v[128:131], v[44:47], v[60:63]\n"
"ds_read_b128 v[44:47], v25 offset:3840\n"
"v_cvt_pk_bf16_f32 v56, v56, v57\n"
"v_cvt_pk_bf16_f32 v57, v58, v59\n"
"v_pk_min_u16 v56, v56, s36\n"
"s_waitcnt lgkmcnt(8)\n"
"v_mfma_f32_16x16x32_f16 v[60:63], v[228:231], v[88:91], v[60:63]\n"
"ds_read_b128 v[88:91], v25 offset:2240\n"
"v_pk_min_u16 v57, v57, s36\n"
"ds_write_b64 v26, v[56:57] offset:1024\n"
"v_mfma_f32_16x16x32_f16 v[60:63], v[196:199], v[92:95], v[60:63]\n"
"ds_read_b128 v[92:95], v25 offset:2752\n"
"ds_write_b32 v4, v4 offset:576\n"
"v_mfma_f32_16x16x32_f16 v[60:63], v[164:167], v[96:99], v[60:63]\n"
"ds_read_b128 v[96:99], v25 offset:3264\n"
"v_mfma_f32_16x16x32_f16 v[60:63], v[132:135], v[100:103], v[60:63]\n"
"ds_read_b128 v[100:103], v25 offset:3776\n"
"s_waitcnt lgkmcnt(10)\n"
"v_mfma_f32_16x16x32_f16 v[60:63], v[232:235], v[104:107], v[60:63]\n"
"ds_read_b128 v[104:107], v25 offset:2176\n"
"v_mfma_f32_16x16x32_f16 v[60:63], v[200:203], v[108:111], v[60:63]\n"
"ds_read_b128 v[108:111], v25 offset:2688\n"
"v_mfma_f32_16x16x32_f16 v[60:63], v[168:171], v[112:115], v[60:63]\n"
"ds_read_b128 v[112:115], v25 offset:3200\n"
"v_mfma_f32_16x16x32_f16 v[60:63], v[136:139], v[116:119], v[60:63]\n"
"ds_read_b128 v[116:119], v25 offset:3712\n"
"s_waitcnt lgkmcnt(10)\n"
"v_mfma_f32_16x16x32_f16 v[60:63], v[236:239], v[120:123], v[60:63]\n"
"ds_read_b128 v[120:123], v25 offset:2112\n"
"v_mfma_f32_16x16x32_f16 v[60:63], v[204:207], v[124:127], v[60:63]\n"
"ds_read_b128 v[124:127], v25 offset:2624\n"
"v_mfma_f32_16x16x32_f16 v[60:63], v[172:175], v[40:43], v[60:63]\n"
"ds_read_b128 v[40:43], v25 offset:3136\n"
"v_mfma_f32_16x16x32_f16 v[60:63], v[140:143], v[44:47], v[60:63]\n"
"ds_read_b128 v[44:47], v25 offset:3648\n"
"s_waitcnt lgkmcnt(8)\n"
"v_mfma_f32_16x16x32_f16 v[60:63], v[240:243], v[88:91], v[60:63]\n"
"ds_read_b128 v[88:91], v25 offset:2048\n"
"v_mfma_f32_16x16x32_f16 v[60:63], v[208:211], v[92:95], v[60:63]\n"
"ds_read_b128 v[92:95], v25 offset:2560\n"
"v_mfma_f32_16x16x32_f16 v[60:63], v[176:179], v[96:99], v[60:63]\n"
"ds_read_b128 v[96:99], v25 offset:3072\n"
"v_mfma_f32_16x16x32_f16 v[60:63], v[144:147], v[100:103], v[60:63]\n"
"ds_read_b128 v[100:103], v25 offset:3584\n"
"s_waitcnt lgkmcnt(8)\n"
"v_mfma_f32_16x16x32_f16 v[60:63], v[244:247], v[104:107], v[60:63]\n"
"v_mfma_f32_16x16x32_f16 v[60:63], v[212:215], v[108:111], v[60:63]\n"
"v_mfma_f32_16x16x32_f16 v[60:63], v[180:183], v[112:115], v[60:63]\n"
"v_mfma_f32_16x16x32_f16 v[60:63], v[148:151], v[116:119], v[60:63]\n"
"s_waitcnt lgkmcnt(4)\n"
"v_mfma_f32_16x16x32_f16 v[60:63], v[248:251], v[120:123], v[60:63]\n"
"v_mfma_f32_16x16x32_f16 v[60:63], v[216:219], v[124:127], v[60:63]\n"
"v_mfma_f32_16x16x32_f16 v[60:63], v[184:187], v[40:43], v[60:63]\n"
"v_mfma_f32_16x16x32_f16 v[60:63], v[152:155], v[44:47], v[60:63]\n"
"s_waitcnt lgkmcnt(0)\n"
"v_mfma_f32_16x16x32_f16 v[60:63], v[252:255], v[88:91], v[60:63]\n"
"v_mfma_f32_16x16x32_f16 v[60:63], v[220:223], v[92:95], v[60:63]\n"
"v_mfma_f32_16x16x32_f16 v[60:63], v[188:191], v[96:99], v[60:63]\n"
"v_mfma_f32_16x16x32_f16 v[60:63], v[156:159], v[100:103], v[60:63]\n"
"s_nop 7\n"
"v_mul_f32 v60, v36, v60\n"
"v_mul_f32 v61, v36, v61\n"
"v_mul_f32 v62, v36, v62\n"
"v_mul_f32 v63, v36, v63\n"
"v_cvt_pk_bf16_f32 v60, v60, v61\n"
"v_cvt_pk_bf16_f32 v61, v62, v63\n"
"v_pk_min_u16 v60, v60, s36\n"
"v_pk_min_u16 v61, v61, s36\n"
"ds_write_b64 v26, v[60:61] offset:1536\n"
"ds_write_b32 v4, v4 offset:560\n"
"s_nop 2\n"
".Lnc_570:\n"
"s_branch .L_yend\n"
".Lsl_301:\n"
"s_nop 7\n"
"s_mov_b32 s17, 32768\n"
".Ltp_303:\n"
"buffer_load_dwordx4 v[44:47], v38, s[4:7], s33 offen sc1\n"
"s_sub_u32 s17, s17, 1\n"
"s_cbranch_scc1 .Lou_304\n"
"s_waitcnt vmcnt(0)\n"
"v_xor_b32 v44, s16, v44\n"
"v_xor_b32 v45, s16, v45\n"
"v_xor_b32 v46, s16, v46\n"
"v_xor_b32 v47, s16, v47\n"
"v_or_b32 v44, v44, v45\n"
"v_or3_b32 v44, v44, v46, v47\n"
"v_cmp_ne_u32 vcc, 0, v44\n"
"s_cbranch_vccz .Lou_304\n"
"s_sleep 16\n"
"s_branch .Ltp_303\n"
".Lou_304:\n"
"s_waitcnt vmcnt(0)\n"
"s_branch .Lbk_302\n"
".Lst_312:\n"
"s_nop 7\n"
"s_lshl_b32 s79, s3, 2\n"
"s_add_u32 s79, s79, 0x26800\n"
"v_mov_b32 v36, s79\n"
"v_and_b32 v79, 15, v0\n"
"v_lshrrev_b32 v78, 4, v0\n"
"v_lshlrev_b32 v77, 3, v78\n"
"v_sub_u32 v76, v77, v79\n"
"v_lshlrev_b32 v75, 4, v78\n"
"v_lshlrev_b32 v74, 8, v78\n"
"v_lshlrev_b32 v73, 3, v79\n"
"v_add_u32 v72, v74, v73\n"
"v_sub_u32 v71, 0x40a, v76\n"
"v_sub_u32 v70, 0x40b, v76\n"
"v_sub_u32 v69, 0x40c, v76\n"
"v_sub_u32 v68, 0x40d, v76\n"
"v_sub_u32 v67, 0x40e, v76\n"
"v_sub_u32 v66, 0x40f, v76\n"
"v_sub_u32 v65, 0x410, v76\n"
"s_mul_i32 s79, s3, 0x2100\n"
"s_add_u32 s79, s79, 0x15100\n"
"s_add_u32 s79, s79, 0x800\n"
"v_add_u32 v64, s79, v72\n"
"s_mul_i32 s79, s3, 0x2100\n"
"s_add_u32 s79, s79, 0x15100\n"
"v_lshl_add_u32 v99, v79, 6, v75\n"
"v_add_u32 v80, s79, v99\n"
"s_mul_i32 s79, s3, 0x2100\n"
"s_add_u32 s79, s79, 0x15100\n"
"s_add_u32 s79, s79, 0x400\n"
"v_lshl_add_u32 v99, v79, 5, v75\n"
"v_lshl_add_u32 v98, v79, 4, v74\n"
"v_cmp_gt_i32 vcc, 2, v78\n"
"s_nop 1\n"
"v_cndmask_b32 v99, v98, v99, vcc\n"
"v_add_u32 v81, s79, v99\n"
"v_mov_b32 v99, 0x40000\n"
"v_cmp_ge_i32 vcc, 32, v0\n"
"s_nop 1\n"
"v_cndmask_b32 v82, v99, v64, vcc\n"
"v_mov_b32 v99, 0x40000\n"
"v_cmp_gt_i32 vcc, 32, v0\n"
"s_nop 1\n"
"v_cndmask_b32 v83, v99, v64, vcc\n"
"s_mul_i32 s79, s3, 0x2100\n"
"s_add_u32 s79, s79, 0x15100\n"
"s_add_u32 s79, s79, 0x7ec\n"
"v_lshlrev_b32 v99, 5, v78\n"
"v_sub_u32 v99, v73, v99\n"
"v_add_u32 v84, s79, v99\n"
"s_mul_i32 s79, s3, 0x2100\n"
"s_add_u32 s79, s79, 0x15100\n"
"s_add_u32 s79, s79, 0x400\n"
"v_sub_u32 v99, 0x411, v76\n"
"v_bfe_u32 v98, v65, 2, 1\n"
"v_add_u32 v99, v99, v98\n"
"v_and_b32 v99, 0xffc, v99\n"
"v_min_i32 v99, 0x400, v99\n"
"v_add_u32 v100, s79, v99\n"
"s_mul_i32 s79, s3, 0x2100\n"
"s_add_u32 s79, s79, 0x15100\n"
"s_add_u32 s79, s79, 0x400\n"
"v_bfe_u32 v99, v66, 2, 1\n"
"v_add_u32 v99, v65, v99\n"
"v_and_b32 v99, 0xffc, v99\n"
"v_min_i32 v99, 0x400, v99\n"
"v_add_u32 v101, s79, v99\n"
"s_mul_i32 s79, s3, 0x2100\n"
"s_add_u32 s79, s79, 0x15100\n"
"s_add_u32 s79, s79, 0x400\n"
"v_bfe_u32 v99, v67, 2, 1\n"
"v_add_u32 v99, v66, v99\n"
"v_and_b32 v99, 0xffc, v99\n"
"v_min_i32 v99, 0x400, v99\n"
"v_add_u32 v102, s79, v99\n"
"s_mul_i32 s79, s3, 0x2100\n"
"s_add_u32 s79, s79, 0x15100\n"
"s_add_u32 s79, s79, 0x400\n"
"v_bfe_u32 v99, v68, 2, 1\n"
"v_add_u32 v99, v67, v99\n"
"v_and_b32 v99, 0xffc, v99\n"
"v_min_i32 v99, 0x400, v99\n"
"v_add_u32 v103, s79, v99\n"
"s_mul_i32 s79, s3, 0x2100\n"
"s_add_u32 s79, s79, 0x15100\n"
"s_add_u32 s79, s79, 0x400\n"
"v_bfe_u32 v99, v69, 2, 1\n"
"v_add_u32 v99, v68, v99\n"
"v_and_b32 v99, 0xffc, v99\n"
"v_min_i32 v99, 0x400, v99\n"
"v_add_u32 v104, s79, v99\n"
"s_mul_i32 s79, s3, 0x2100\n"
"s_add_u32 s79, s79, 0x15100\n"
"s_add_u32 s79, s79, 0x400\n"
"v_bfe_u32 v99, v70, 2, 1\n"
"v_add_u32 v99, v69, v99\n"
"v_and_b32 v99, 0xffc, v99\n"
"v_min_i32 v99, 0x400, v99\n"
"v_add_u32 v105, s79, v99\n"
"s_mul_i32 s79, s3, 0x2100\n"
"s_add_u32 s79, s79, 0x15100\n"
"s_add_u32 s79, s79, 0x400\n"
"v_bfe_u32 v99, v71, 2, 1\n"
"v_add_u32 v99, v70, v99\n"
"v_and_b32 v99, 0xffc, v99\n"
"v_min_i32 v99, 0x400, v99\n"
"v_add_u32 v106, s79, v99\n"
"s_mul_i32 s79, s3, 0x2100\n"
"s_add_u32 s79, s79, 0x15100\n"
"s_add_u32 s79, s79, 0x400\n"
"v_sub_u32 v99, 0x409, v76\n"
"v_bfe_u32 v99, v99, 2, 1\n"
"v_add_u32 v99, v71, v99\n"
"v_and_b32 v99, 0xffc, v99\n"
"v_min_i32 v99, 0x400, v99\n"
"v_add_u32 v107, s79, v99\n"
"v_cmp_eq_u32 vcc, 0, v0\n"
"v_lshlrev_b32 v140, 16, v140\n"
"v_mul_f32 v140, 0.5, v140\n"
"v_cndmask_b32 v140, 0, v140, vcc\n"
"v_add_f32 v128, v128, v140\n"
"v_cndmask_b32 v136, v136, 0, s[56:57]\n"
"v_cndmask_b32 v137, v137, 0, s[58:59]\n"
"v_cndmask_b32 v138, v138, 0, s[58:59]\n"
"v_cndmask_b32 v139, v139, 0, s[58:59]\n"
"v_max3_f32 v85, |v128|, |v129|, 0\n"
"v_max3_f32 v85, |v130|, |v131|, v85\n"
"v_max3_f32 v85, |v132|, |v133|, v85\n"
"v_max3_f32 v85, |v134|, |v135|, v85\n"
"v_max3_f32 v85, |v136|, |v137|, v85\n"
"v_max3_f32 v85, |v138|, |v139|, v85\n"
"s_nop 1\n"
"v_max_u32_dpp v85, v85, v85 row_ror:8 row_mask:0xf bank_mask:0xf\n"
"s_nop 1\n"
"v_max_u32_dpp v85, v85, v85 row_ror:4 row_mask:0xf bank_mask:0xf\n"
"s_nop 1\n"
"v_max_u32_dpp v85, v85, v85 row_ror:2 row_mask:0xf bank_mask:0xf\n"
"s_nop 1\n"
"v_max_u32_dpp v85, v85, v85 row_ror:1 row_mask:0xf bank_mask:0xf\n"
"v_mov_b32 v86, v85\n"
"s_nop 1\n"
"v_permlane16_swap_b32 v85, v86\n"
"v_max_u32 v85, v85, v86\n"
"v_mov_b32 v86, v85\n"
"s_nop 1\n"
"v_permlane32_swap_b32 v85, v86\n"
"v_max_u32 v85, v85, v86\n"
"s_nop 0\n"
"v_readfirstlane_b32 s51, v85\n"
"s_nop 2\n"
"s_bfe_u32 s50, s51, 0x80017\n"
"s_max_i32 s50, s50, 77\n"
"s_min_i32 s50, s50, 227\n"
"s_cmp_gt_i32 s51, 0\n"
"s_cselect_b32 s50, s50, 127\n"
"s_sub_u32 s51, 268, s50\n"
"s_lshl_b32 s51, s51, 23\n"
"s_sub_u32 s50, s50, 13\n"
"s_lshl_b32 s50, s50, 23\n"
"v_mul_f32 v128, s51, v128\n"
"v_mul_f32 v129, s51, v129\n"
"v_mul_f32 v130, s51, v130\n"
"v_mul_f32 v131, s51, v131\n"
"v_mul_f32 v132, s51, v132\n"
"v_mul_f32 v133, s51, v133\n"
"v_mul_f32 v134, s51, v134\n"
"v_mul_f32 v135, s51, v135\n"
"v_mul_f32 v136, s51, v136\n"
"v_mul_f32 v137, s51, v137\n"
"v_mul_f32 v138, s51, v138\n"
"v_mul_f32 v139, s51, v139\n"
"ds_write_b128 v80, v[128:131]\n"
"ds_write_b128 v81, v[132:135]\n"
"ds_write_b64 v82, v[136:137]\n"
"ds_write_b64 v83, v[138:139] offset:128\n"
"v_mov_b32 v87, s50\n"
"ds_write_b32 v36, v87\n"
"ds_read_b32 v100, v100\n"
"ds_read_b32 v101, v101\n"
"ds_read_b32 v102, v102\n"
"ds_read_b32 v103, v103\n"
"ds_read_b32 v104, v104\n"
"ds_read_b32 v105, v105\n"
"ds_read_b32 v106, v106\n"
"ds_read_b32 v107, v107\n"
"ds_read2_b32 v[48:49], v84 offset0:6 offset1:4\n"
"ds_read2_b32 v[50:51], v84 offset0:2 offset1:0\n"
"ds_read2_b32 v[52:53], v84 offset0:7 offset1:5\n"
"ds_read2_b32 v[54:55], v84 offset0:3 offset1:1\n"
"ds_read2_b32 v[56:57], v84 offset0:38 offset1:36\n"
"ds_read2_b32 v[58:59], v84 offset0:34 offset1:32\n"
"ds_read2_b32 v[60:61], v84 offset0:39 offset1:37\n"
"ds_read2_b32 v[62:63], v84 offset0:35 offset1:33\n"
"ds_read2_b32 v[64:65], v84 offset0:70 offset1:68\n"
"ds_read2_b32 v[66:67], v84 offset0:66 offset1:64\n"
"ds_read2_b32 v[68:69], v84 offset0:71 offset1:69\n"
"ds_read2_b32 v[70:71], v84 offset0:67 offset1:65\n"
"ds_read2_b32 v[72:73], v84 offset0:102 offset1:100\n"
"ds_read2_b32 v[74:75], v84 offset0:98 offset1:96\n"
"ds_read2_b32 v[76:77], v84 offset0:103 offset1:101\n"
"ds_read2_b32 v[78:79], v84 offset0:99 offset1:97\n"
"s_waitcnt lgkmcnt(15)\n"
"v_cvt_pk_f16_f32 v152, v100, v101\n"
"v_cvt_pk_f16_f32 v153, v102, v103\n"
"v_cvt_pk_f16_f32 v154, v104, v105\n"
"v_cvt_pk_f16_f32 v155, v106, v107\n"
"v_cvt_pk_f16_f32 v128, v48, v49\n"
"s_waitcnt lgkmcnt(14)\n"
"v_cvt_pk_f16_f32 v129, v50, v51\n"
"s_waitcnt lgkmcnt(13)\n"
"v_cvt_pk_f16_f32 v130, v52, v53\n"
"s_waitcnt lgkmcnt(12)\n"
"v_cvt_pk_f16_f32 v131, v54, v55\n"
"s_waitcnt lgkmcnt(11)\n"
"v_cvt_pk_f16_f32 v132, v56, v57\n"
"s_waitcnt lgkmcnt(10)\n"
"v_cvt_pk_f16_f32 v133, v58, v59\n"
"s_waitcnt lgkmcnt(9)\n"
"v_cvt_pk_f16_f32 v134, v60, v61\n"
"s_waitcnt lgkmcnt(8)\n"
"v_cvt_pk_f16_f32 v135, v62, v63\n"
"s_waitcnt lgkmcnt(7)\n"
"v_cvt_pk_f16_f32 v136, v64, v65\n"
"s_waitcnt lgkmcnt(6)\n"
"v_cvt_pk_f16_f32 v137, v66, v67\n"
"s_waitcnt lgkmcnt(5)\n"
"v_cvt_pk_f16_f32 v138, v68, v69\n"
"s_waitcnt lgkmcnt(4)\n"
"v_cvt_pk_f16_f32 v139, v70, v71\n"
"s_waitcnt lgkmcnt(3)\n"
"v_cvt_pk_f16_f32 v140, v72, v73\n"
"s_waitcnt lgkmcnt(2)\n"
"v_cvt_pk_f16_f32 v141, v74, v75\n"
"s_waitcnt lgkmcnt(1)\n"
"v_cvt_pk_f16_f32 v142, v76, v77\n"
"s_waitcnt lgkmcnt(0)\n"
"v_cvt_pk_f16_f32 v143, v78, v79\n"
"s_mov_b32 s43, 1\n"
"s_nop 3\n"
"s_setpc_b64 s[46:47]\n"
".Lst_313:\n"
"s_nop 7\n"
"v_lshlrev_b32 v79, 3, v0\n"
"v_lshlrev_b32 v78, 4, v0\n"
"v_lshlrev_b32 v77, 5, v0\n"
"v_lshrrev_b32 v76, 5, v0\n"
"v_sub_u32 v75, 0x5f0, v78\n"
"v_sub_u32 v74, 0x7e0, v77\n"
"v_cmp_le_i32 vcc, 32, v0\n"
"s_nop 1\n"
"v_cndmask_b32 v73, v75, v74, vcc\n"
"s_mul_i32 s79, s3, 0x2100\n"
"s_add_u32 s79, s79, 0x15100\n"
"s_sub_u32 s79, s79, 8\n"
"v_add_u32 v72, s79, v73\n"
"s_mul_i32 s79, s3, 0x2100\n"
"s_add_u32 s79, s79, 0x15100\n"
"s_add_u32 s79, s79, 0x7f4\n"
"v_sub_u32 v108, s79, v79\n"
"v_mov_b32 v99, 4\n"
"v_cmp_eq_i32 vcc, 63, v0\n"
"s_nop 1\n"
"v_cndmask_b32 v99, 0, v99, vcc\n"
"v_sub_u32 v109, v108, v99\n"
"v_mov_b32 v99, 4\n"
"v_cmp_le_i32 vcc, 31, v0\n"
"s_nop 1\n"
"v_cndmask_b32 v99, 0, v99, vcc\n"
"v_sub_u32 v110, v72, v99\n"
"s_mul_i32 s79, s3, 0x2100\n"
"s_add_u32 s79, s79, 0x14000\n"
"v_add_u32 v115, s79, v78\n"
"s_mul_i32 s79, s3, 0x2100\n"
"s_add_u32 s79, s79, 0x14000\n"
"s_add_u32 s79, s79, 0x800\n"
"v_and_b32 v99, 15, v0\n"
"v_lshrrev_b32 v98, 4, v0\n"
"v_mul_i32_i24 v98, 0x840, v98\n"
"v_lshl_add_u32 v99, v99, 2, v98\n"
"v_add_u32 v116, s79, v99\n"
"v_mov_b32 v117, 0\n"
"v_mad_i32_i24 v111, v76, 0, v72\n"
"v_lshl_add_u32 v112, v76, 2, v72\n"
"v_lshl_add_u32 v113, v76, 3, v72\n"
"v_mad_i32_i24 v114, v76, 12, v72\n"
"ds_read_b32 v48, v108 offset:12\n"
"ds_read_b32 v49, v108 offset:8\n"
"ds_read_b32 v50, v108 offset:4\n"
"ds_read_b32 v51, v109 offset:4\n"
"ds_read_b32 v52, v109 offset:0\n"
"ds_read_b32 v53, v114 offset:24\n"
"ds_read_b32 v54, v114 offset:20\n"
"ds_read_b32 v55, v114 offset:16\n"
"ds_read_b32 v56, v113 offset:16\n"
"ds_read_b32 v57, v112 offset:16\n"
"ds_read_b32 v58, v112 offset:12\n"
"ds_read_b32 v59, v112 offset:8\n"
"ds_read_b32 v60, v111 offset:8\n"
"ds_read_b32 v61, v110 offset:8\n"
"ds_read_b32 v62, v110 offset:4\n"
"ds_read_b32 v63, v110 offset:0\n"
"v_cmp_ne_u32 vcc, 63, v0\n"
"s_waitcnt lgkmcnt(2)\n"
"s_nop 0\n"
"v_cndmask_b32 v61, 0, v61, vcc\n"
"s_waitcnt lgkmcnt(1)\n"
"v_cndmask_b32 v62, 0, v62, vcc\n"
"s_waitcnt lgkmcnt(0)\n"
"v_cndmask_b32 v63, 0, v63, vcc\n"
"v_cvt_pk_f16_f32 v64, v48, v48\n"
"v_cvt_pk_f16_f32 v65, v49, v49\n"
"v_cvt_pk_f16_f32 v66, v49, v50\n"
"v_cvt_pk_f16_f32 v67, v50, v50\n"
"ds_write_b128 v115, v[64:67] offset:0\n"
"v_cvt_pk_f16_f32 v68, v48, v49\n"
"v_cvt_pk_f16_f32 v69, v49, v49\n"
"v_cvt_pk_f16_f32 v70, v50, v50\n"
"v_cvt_pk_f16_f32 v71, v50, v50\n"
"ds_write_b128 v115, v[68:71] offset:2112\n"
"v_cvt_pk_f16_f32 v64, v49, v49\n"
"v_cvt_pk_f16_f32 v65, v49, v50\n"
"v_cvt_pk_f16_f32 v66, v50, v50\n"
"v_cvt_pk_f16_f32 v67, v50, v51\n"
"ds_write_b128 v115, v[64:67] offset:4224\n"
"v_cvt_pk_f16_f32 v68, v49, v49\n"
"v_cvt_pk_f16_f32 v69, v50, v50\n"
"v_cvt_pk_f16_f32 v70, v50, v50\n"
"v_cvt_pk_f16_f32 v71, v51, v52\n"
"ds_write_b128 v115, v[68:71] offset:6336\n"
"v_cvt_pk_f16_f32 v64, v53, v54\n"
"v_cvt_pk_f16_f32 v65, v55, v56\n"
"v_cvt_pk_f16_f32 v66, v57, v58\n"
"v_cvt_pk_f16_f32 v67, v59, v60\n"
"ds_write_b128 v115, v[64:67] offset:1024\n"
"v_cvt_pk_f16_f32 v68, v54, v55\n"
"v_cvt_pk_f16_f32 v69, v56, v57\n"
"v_cvt_pk_f16_f32 v70, v58, v59\n"
"v_cvt_pk_f16_f32 v71, v60, v61\n"
"ds_write_b128 v115, v[68:71] offset:3136\n"
"v_cvt_pk_f16_f32 v64, v55, v56\n"
"v_cvt_pk_f16_f32 v65, v57, v58\n"
"v_cvt_pk_f16_f32 v66, v59, v60\n"
"v_cvt_pk_f16_f32 v67, v61, v62\n"
"ds_write_b128 v115, v[64:67] offset:5248\n"
"v_cvt_pk_f16_f32 v68, v56, v57\n"
"v_cvt_pk_f16_f32 v69, v58, v59\n"
"v_cvt_pk_f16_f32 v70, v60, v61\n"
"v_cvt_pk_f16_f32 v71, v62, v63\n"
"ds_write_b128 v115, v[68:71] offset:7360\n"
"ds_write_b32 v116, v117\n"
"ds_write_b32 v4, v4 offset:192\n"
"s_mov_b32 s43, 2\n"
"s_nop 0\n"
"s_setpc_b64 s[46:47]\n"
".Lst_314:\n"
"s_nop 7\n"
"v_and_b32 v79, 15, v0\n"
"v_and_b32 v78, 1, v79\n"
"v_and_b32 v77, 2, v79\n"
"v_lshlrev_b32 v76, 4, v0\n"
"v_lshrrev_b32 v75, 4, v0\n"
"v_xor_b32 v74, s3, v79\n"
"v_and_b32 v73, 1, v74\n"
"v_lshlrev_b32 v72, 1, v73\n"
"v_lshlrev_b32 v71, 1, v75\n"
"v_lshlrev_b32 v70, 3, v75\n"
"v_sub_u32 v69, v70, v79\n"
"v_lshlrev_b32 v68, 6, v75\n"
"v_lshlrev_b32 v67, 4, v77\n"
"v_lshlrev_b32 v66, 5, v79\n"
"v_lshrrev_b32 v65, 1, v75\n"
"v_lshrrev_b32 v64, 1, v79\n"
"v_lshrrev_b32 v63, 2, v79\n"
"v_lshrrev_b32 v62, 3, v79\n"
"v_lshlrev_b32 v61, 7, v62\n"
"v_xor_b32 v60, v63, v65\n"
"s_lshl_b32 s79, s3, 10\n"
"v_add_u32 v30, s79, v76\n"
"v_lshrrev_b32 v99, 2, v30\n"
"v_lshrrev_b32 v98, 6, v30\n"
"v_and_b32 v98, 24, v98\n"
"v_xor_b32 v99, v99, v98\n"
"v_add_u32 v32, 0x20c00, v99\n"
"s_lshl_b32 s79, s3, 8\n"
"s_add_u32 s79, s79, 0x20c00\n"
"v_and_b32 v99, 1, v60\n"
"v_add_u32 v99, v72, v99\n"
"v_lshl_add_u32 v98, v63, 11, v61\n"
"v_add_u32 v98, v98, v68\n"
"v_add_u32 v98, v98, v67\n"
"v_lshl_add_u32 v99, v99, 3, v98\n"
"v_add_u32 v27, s79, v99\n"
"s_mul_i32 s79, s3, 0x2100\n"
"s_add_u32 s79, s79, 0x13840\n"
"v_add_u32 v99, 0x3ef, v69\n"
"v_lshrrev_b32 v99, 2, v99\n"
"v_add_u32 v98, 0x3ef, v69\n"
"v_and_b32 v98, 3, v98\n"
"v_mul_i32_i24 v98, 0x840, v98\n"
"v_lshl_add_u32 v99, v99, 3, v98\n"
"v_add_u32 v24, s79, v99\n"
"s_mul_i32 s79, s3, 0x1200\n"
"s_add_u32 s79, s79, 0x1c420\n"
"v_lshl_add_u32 v99, v75, 4, v66\n"
"v_add_u32 v25, s79, v99\n"
"s_lshl_b32 s79, s3, 11\n"
"s_lshr_b32 s78, s3, 1\n"
"s_lshl_b32 s78, s78, 7\n"
"s_add_u32 s79, s79, s78\n"
"s_add_u32 s79, s79, 0x20c00\n"
"s_and_b32 s78, s3, 1\n"
"v_xor_b32 v99, v75, v63\n"
"v_xor_b32 v99, s78, v99\n"
"v_and_b32 v99, 3, v99\n"
"v_lshl_add_u32 v99, v99, 3, v66\n"
"v_add_u32 v26, s79, v99\n"
"v_xor_b32 v28, 8, v27\n"
"v_lshrrev_b32 v99, 7, v30\n"
"v_and_b32 v98, 0x70, v30\n"
"s_mov_b32 s79, 0x5000\n"
"v_mad_i32_i24 v29, v99, s79, v98\n"
"v_lshrrev_b32 v99, 2, v30\n"
"v_add_u32 v31, 0x1ce00, v99\n"
"v_xor_b32 v33, 8, v32\n"
"v_mov_b32 v99, 0xffff0000\n"
"v_mov_b32 v98, 0xffff\n"
"v_cmp_eq_i32 vcc, 1, v78\n"
"s_nop 1\n"
"v_cndmask_b32 v99, v98, v99, vcc\n"
"v_cmp_eq_i32 vcc, v64, v71\n"
"s_nop 1\n"
"v_cndmask_b32 v34, 0, v99, vcc\n"
"v_mov_b32 v99, 0xffff0000\n"
"v_mov_b32 v98, 0xffff\n"
"v_cmp_eq_i32 vcc, 1, v78\n"
"s_nop 1\n"
"v_cndmask_b32 v99, v98, v99, vcc\n"
"v_add_u32 v98, 1, v71\n"
"v_cmp_eq_i32 vcc, v64, v98\n"
"s_nop 1\n"
"v_cndmask_b32 v35, 0, v99, vcc\n"
"s_lshl_b32 s79, s3, 2\n"
"s_add_u32 s79, s79, 0x26800\n"
"v_mov_b32 v36, s79\n"
"v_lshrrev_b32 v99, 6, v30\n"
"v_and_b32 v98, 48, v30\n"
"v_mul_i32_i24 v98, 0x50, v98\n"
"s_mov_b32 s79, 0x2800\n"
"v_mad_i32_i24 v37, v99, s79, v98\n"
"v_lshrrev_b32 v99, 4, v0\n"
"v_lshlrev_b32 v99, 3, v99\n"
"v_and_b32 v98, 15, v0\n"
"v_sub_u32 v60, v99, v98\n"
"v_add_u32 v99, 15, v60\n"
"v_min_i32 v99, 31, v99\n"
"v_mov_b32 v98, 0x7f00000\n"
"v_lshrrev_b32 v61, v99, v98\n"
"v_add_u32 v99, 16, v60\n"
"v_min_i32 v99, 31, v99\n"
"v_mov_b32 v98, 0x1ff00000\n"
"v_lshrrev_b32 v60, v99, v98\n"
"v_bfe_u32 v99, v61, 1, 1\n"
"v_and_b32 v98, 1, v61\n"
"v_lshl_add_u32 v144, v99, 16, v98\n"
"v_mul_u32_u24 v144, 0x3c00, v144\n"
"v_bfe_u32 v99, v61, 3, 1\n"
"v_bfe_u32 v98, v61, 2, 1\n"
"v_lshl_add_u32 v145, v99, 16, v98\n"
"v_mul_u32_u24 v145, 0x3c00, v145\n"
"v_bfe_u32 v99, v61, 5, 1\n"
"v_bfe_u32 v98, v61, 4, 1\n"
"v_lshl_add_u32 v146, v99, 16, v98\n"
"v_mul_u32_u24 v146, 0x3c00, v146\n"
"v_bfe_u32 v99, v61, 7, 1\n"
"v_bfe_u32 v98, v61, 6, 1\n"
"v_lshl_add_u32 v147, v99, 16, v98\n"
"v_mul_u32_u24 v147, 0x3c00, v147\n"
"v_bfe_u32 v99, v60, 1, 1\n"
"v_and_b32 v98, 1, v60\n"
"v_lshl_add_u32 v148, v99, 16, v98\n"
"v_mul_u32_u24 v148, 0x3c00, v148\n"
"v_bfe_u32 v99, v60, 3, 1\n"
"v_bfe_u32 v98, v60, 2, 1\n"
"v_lshl_add_u32 v149, v99, 16, v98\n"
"v_mul_u32_u24 v149, 0x3c00, v149\n"
"v_bfe_u32 v99, v60, 5, 1\n"
"v_bfe_u32 v98, v60, 4, 1\n"
"v_lshl_add_u32 v150, v99, 16, v98\n"
"v_mul_u32_u24 v150, 0x3c00, v150\n"
"v_bfe_u32 v99, v60, 7, 1\n"
"v_bfe_u32 v98, v60, 6, 1\n"
"v_lshl_add_u32 v151, v99, 16, v98\n"
"v_mul_u32_u24 v151, 0x3c00, v151\n"
"v_lshrrev_b32 v99, 4, v0\n"
"v_lshlrev_b32 v99, 3, v99\n"
"v_and_b32 v98, 15, v0\n"
"v_sub_u32 v99, v99, v98\n"
"v_add_u32 v99, 15, v99\n"
"v_min_i32 v119, 31, v99\n"
"v_mov_b32 v121, 0xf8000000\n"
"v_ashrrev_i32 v121, v119, v121\n"
"v_lshrrev_b32 v99, 4, v0\n"
"v_and_b32 v98, 15, v0\n"
"v_sub_u32 v98, 15, v98\n"
"v_lshl_add_u32 v99, v99, 2, v98\n"
"v_mov_b32 v98, 0xffff\n"
"v_lshrrev_b32 v118, v99, v98\n"
"v_bfe_u32 v99, v121, 1, 1\n"
"v_and_b32 v98, 1, v121\n"
"v_lshl_add_u32 v122, v99, 16, v98\n"
"v_mul_u32_u24 v122, 0xffff, v122\n"
"v_bfe_u32 v99, v121, 3, 1\n"
"v_bfe_u32 v98, v121, 2, 1\n"
"v_lshl_add_u32 v123, v99, 16, v98\n"
"v_mul_u32_u24 v123, 0xffff, v123\n"
"v_bfe_u32 v99, v121, 5, 1\n"
"v_bfe_u32 v98, v121, 4, 1\n"
"v_lshl_add_u32 v124, v99, 16, v98\n"
"v_mul_u32_u24 v124, 0xffff, v124\n"
"v_bfe_u32 v99, v121, 7, 1\n"
"v_bfe_u32 v98, v121, 6, 1\n"
"v_lshl_add_u32 v125, v99, 16, v98\n"
"v_mul_u32_u24 v125, 0xffff, v125\n"
"v_bfe_u32 v99, v118, 1, 1\n"
"v_and_b32 v98, 1, v118\n"
"v_lshl_add_u32 v126, v99, 16, v98\n"
"v_mul_u32_u24 v126, 0xffff, v126\n"
"v_bfe_u32 v99, v118, 3, 1\n"
"v_bfe_u32 v98, v118, 2, 1\n"
"v_lshl_add_u32 v127, v99, 16, v98\n"
"v_mul_u32_u24 v127, 0xffff, v127\n"
"v_and_b32 v152, v122, v152\n"
"v_and_b32 v128, v126, v128\n"
"v_and_b32 v153, v123, v153\n"
"v_and_b32 v129, v127, v129\n"
"v_and_b32 v154, v124, v154\n"
"v_and_b32 v130, v126, v130\n"
"v_and_b32 v155, v125, v155\n"
"v_and_b32 v131, v127, v131\n"
"ds_read_b32 v36, v36\n"
"s_waitcnt lgkmcnt(0)\n"
"v_sub_u32 v76, 0x7f000000, v36\n"
"v_lshrrev_b32 v77, 16, v76\n"
"v_or_b32 v76, v76, v77\n"
"v_and_b32 v34, v76, v34\n"
"v_and_b32 v35, v76, v35\n"
"s_mov_b32 s43, 3\n"
"s_nop 3\n"
"s_setpc_b64 s[46:47]\n"
".Loa_315:\n"
"s_nop 7\n"
"s_mov_b32 s17, 32768\n"
".Ltp_316:\n"
"s_waitcnt vmcnt(0)\n"
"s_sub_u32 s17, s17, 1\n"
"s_cbranch_scc1 .Lou_317\n"
"v_max_u32 v38, v88, v89\n"
"v_max3_u32 v38, v90, v91, v38\n"
"v_max3_u32 v38, v92, v93, v38\n"
"v_max3_u32 v38, v94, v95, v38\n"
"v_max3_u32 v38, v96, v97, v38\n"
"v_max3_u32 v38, v98, v99, v38\n"
"v_max3_u32 v38, v100, v101, v38\n"
"v_max3_u32 v38, v102, v103, v38\n"
"v_max3_u32 v38, v104, v105, v38\n"
"v_max3_u32 v38, v106, v107, v38\n"
"v_max3_u32 v38, v108, v109, v38\n"
"v_max3_u32 v38, v110, v111, v38\n"
"v_max3_u32 v38, v112, v113, v38\n"
"v_max3_u32 v38, v114, v115, v38\n"
"v_max3_u32 v38, v116, v117, v38\n"
"v_max3_u32 v38, v118, v119, v38\n"
"v_max3_u32 v38, v120, v121, v38\n"
"v_max3_u32 v38, v122, v123, v38\n"
"v_max3_u32 v38, v124, v125, v38\n"
"v_max3_u32 v38, v126, v127, v38\n"
"v_cmp_eq_u32 vcc, -1, v38\n"
"s_cbranch_vccz .Lou_317\n"
"s_sleep 1\n"
"v_max_u32 v38, v90, v91\n"
"v_max3_u32 v38, v88, v89, v38\n"
"v_cmp_eq_u32 vcc, -1, v38\n"
"s_cbranch_vccz .Lsk_318\n"
"s_mov_b64 exec, vcc\n"
"buffer_load_dwordx4 v[88:91], v2, s[4:7], s32 offen offset:0 sc1\n"
"s_mov_b64 exec, -1\n"
".Lsk_318:\n"
"v_max_u32 v38, v94, v95\n"
"v_max3_u32 v38, v92, v93, v38\n"
"v_cmp_eq_u32 vcc, -1, v38\n"
"s_cbranch_vccz .Lsk_319\n"
"s_mov_b64 exec, vcc\n"
"buffer_load_dwordx4 v[92:95], v2, s[4:7], s32 offen offset:1024 sc1\n"
"s_mov_b64 exec, -1\n"
".Lsk_319:\n"
"v_max_u32 v38, v98, v99\n"
"v_max3_u32 v38, v96, v97, v38\n"
"v_cmp_eq_u32 vcc, -1, v38\n"
"s_cbranch_vccz .Lsk_320\n"
"s_mov_b64 exec, vcc\n"
"buffer_load_dwordx4 v[96:99], v2, s[4:7], s32 offen offset:2048 sc1\n"
"s_mov_b64 exec, -1\n"
".Lsk_320:\n"
"v_max_u32 v38, v102, v103\n"
"v_max3_u32 v38, v100, v101, v38\n"
"v_cmp_eq_u32 vcc, -1, v38\n"
"s_cbranch_vccz .Lsk_321\n"
"s_mov_b64 exec, vcc\n"
"buffer_load_dwordx4 v[100:103], v2, s[4:7], s32 offen offset:3072 sc1\n"
"s_mov_b64 exec, -1\n"
".Lsk_321:\n"
"v_max_u32 v38, v106, v107\n"
"v_max3_u32 v38, v104, v105, v38\n"
"v_cmp_eq_u32 vcc, -1, v38\n"
"s_cbranch_vccz .Lsk_322\n"
"s_mov_b64 exec, vcc\n"
"buffer_load_dwordx4 v[104:107], v2, s[4:7], s33 offen offset:0 sc1\n"
"s_mov_b64 exec, -1\n"
".Lsk_322:\n"
"v_max_u32 v38, v110, v111\n"
"v_max3_u32 v38, v108, v109, v38\n"
"v_cmp_eq_u32 vcc, -1, v38\n"
"s_cbranch_vccz .Lsk_323\n"
"s_mov_b64 exec, vcc\n"
"buffer_load_dwordx4 v[108:111], v2, s[4:7], s34 offen offset:0 sc1\n"
"s_mov_b64 exec, -1\n"
".Lsk_323:\n"
"v_max_u32 v38, v114, v115\n"
"v_max3_u32 v38, v112, v113, v38\n"
"v_cmp_eq_u32 vcc, -1, v38\n"
"s_cbranch_vccz .Lsk_324\n"
"s_mov_b64 exec, vcc\n"
"buffer_load_dwordx4 v[112:115], v2, s[4:7], s34 offen offset:1024 sc1\n"
"s_mov_b64 exec, -1\n"
".Lsk_324:\n"
"v_max_u32 v38, v118, v119\n"
"v_max3_u32 v38, v116, v117, v38\n"
"v_cmp_eq_u32 vcc, -1, v38\n"
"s_cbranch_vccz .Lsk_325\n"
"s_mov_b64 exec, vcc\n"
"buffer_load_dwordx4 v[116:119], v2, s[4:7], s34 offen offset:2048 sc1\n"
"s_mov_b64 exec, -1\n"
".Lsk_325:\n"
"v_max_u32 v38, v122, v123\n"
"v_max3_u32 v38, v120, v121, v38\n"
"v_cmp_eq_u32 vcc, -1, v38\n"
"s_cbranch_vccz .Lsk_326\n"
"s_mov_b64 exec, vcc\n"
"buffer_load_dwordx4 v[120:123], v2, s[4:7], s34 offen offset:3072 sc1\n"
"s_mov_b64 exec, -1\n"
".Lsk_326:\n"
"v_max_u32 v38, v126, v127\n"
"v_max3_u32 v38, v124, v125, v38\n"
"v_cmp_eq_u32 vcc, -1, v38\n"
"s_cbranch_vccz .Lsk_327\n"
"s_mov_b64 exec, vcc\n"
"buffer_load_dwordx4 v[124:127], v2, s[4:7], s35 offen offset:0 sc1\n"
"s_mov_b64 exec, -1\n"
".Lsk_327:\n"
"s_branch .Ltp_316\n"
".Lou_317:\n"
"s_waitcnt vmcnt(0)\n"
"s_setpc_b64 s[46:47]\n"
".Lsl_348:\n"
"s_nop 7\n"
"s_call_b64 s[46:47], .Loa_315\n"
"s_branch .Lbk_349\n"
".Lsl_350:\n"
"s_nop 7\n"
"s_call_b64 s[46:47], .Loa_315\n"
"s_branch .Lbk_351\n"
".Lsl_352:\n"
"s_nop 7\n"
"s_call_b64 s[46:47], .Loa_315\n"
"s_branch .Lbk_353\n"
".Lsl_354:\n"
"s_nop 7\n"
"s_call_b64 s[46:47], .Loa_315\n"
"s_branch .Lbk_355\n"
".Lsl_356:\n"
"s_nop 7\n"
"s_call_b64 s[46:47], .Loa_315\n"
"s_branch .Lbk_357\n"
".Lsl_358:\n"
"s_nop 7\n"
"s_call_b64 s[46:47], .Loa_315\n"
"s_branch .Lbk_359\n"
".Lsl_360:\n"
"s_nop 7\n"
"s_call_b64 s[46:47], .Loa_315\n"
"s_branch .Lbk_361\n"
".Lsl_362:\n"
"s_nop 7\n"
"s_call_b64 s[46:47], .Loa_315\n"
"s_branch .Lbk_363\n"
".Lsl_364:\n"
"s_nop 7\n"
"s_call_b64 s[46:47], .Loa_315\n"
"s_branch .Lbk_365\n"
".Lsl_366:\n"
"s_nop 7\n"
"s_call_b64 s[46:47], .Loa_315\n"
"s_branch .Lbk_367\n"
".Lsl_391:\n"
"s_nop 7\n"
"s_call_b64 s[46:47], .Loa_315\n"
"s_branch .Lbk_392\n"
".Lsl_393:\n"
"s_nop 7\n"
"s_call_b64 s[46:47], .Loa_315\n"
"s_branch .Lbk_394\n"
".Lsl_395:\n"
"s_nop 7\n"
"s_call_b64 s[46:47], .Loa_315\n"
"s_branch .Lbk_396\n"
".Lsl_397:\n"
"s_nop 7\n"
"s_call_b64 s[46:47], .Loa_315\n"
"s_branch .Lbk_398\n"
".Lsl_399:\n"
"s_nop 7\n"
"s_call_b64 s[46:47], .Loa_315\n"
"s_branch .Lbk_400\n"
".Lsl_401:\n"
"s_nop 7\n"
"s_call_b64 s[46:47], .Loa_315\n"
"s_branch .Lbk_402\n"
".Lsl_403:\n"
"s_nop 7\n"
"s_call_b64 s[46:47], .Loa_315\n"
"s_branch .Lbk_404\n"
".Lsl_405:\n"
"s_nop 7\n"
"s_call_b64 s[46:47], .Loa_315\n"
"s_branch .Lbk_406\n"
".Lsl_407:\n"
"s_nop 7\n"
"s_call_b64 s[46:47], .Loa_315\n"
"s_branch .Lbk_408\n"
".Lsl_409:\n"
"s_nop 7\n"
"s_call_b64 s[46:47], .Loa_315\n"
"s_branch .Lbk_410\n"
".Lsl_493:\n"
"s_nop 7\n"
"s_call_b64 s[46:47], .Loa_315\n"
"s_branch .Lbk_494\n"
".Lsl_495:\n"
"s_nop 7\n"
"s_call_b64 s[46:47], .Loa_315\n"
"s_branch .Lbk_496\n"
".Lsl_497:\n"
"s_nop 7\n"
"s_call_b64 s[46:47], .Loa_315\n"
"s_branch .Lbk_498\n"
".Lsl_499:\n"
"s_nop 7\n"
"s_call_b64 s[46:47], .Loa_315\n"
"s_branch .Lbk_500\n"
".Lsl_501:\n"
"s_nop 7\n"
"s_call_b64 s[46:47], .Loa_315\n"
"s_branch .Lbk_502\n"
".Lsl_503:\n"
"s_nop 7\n"
"s_call_b64 s[46:47], .Loa_315\n"
"s_branch .Lbk_504\n"
".Lsl_505:\n"
"s_nop 7\n"
"s_call_b64 s[46:47], .Loa_315\n"
"s_branch .Lbk_506\n"
".Lsl_507:\n"
"s_nop 7\n"
"s_call_b64 s[46:47], .Loa_315\n"
"s_branch .Lbk_508\n"
".Lsl_509:\n"
"s_nop 7\n"
"s_call_b64 s[46:47], .Loa_315\n"
"s_branch .Lbk_510\n"
".Lsl_511:\n"
"s_nop 7\n"
"s_call_b64 s[46:47], .Loa_315\n"
"s_branch .Lbk_512\n"
".Lsl_539:\n"
"s_nop 7\n"
"s_call_b64 s[46:47], .Loa_315\n"
"s_branch .Lbk_540\n"
".Lsl_541:\n"
"s_nop 7\n"
"s_call_b64 s[46:47], .Loa_315\n"
"s_branch .Lbk_542\n"
".Lsl_543:\n"
"s_nop 7\n"
"s_call_b64 s[46:47], .Loa_315\n"
"s_branch .Lbk_544\n"
".Lsl_545:\n"
"s_nop 7\n"
"s_call_b64 s[46:47], .Loa_315\n"
"s_branch .Lbk_546\n"
".Lsl_547:\n"
"s_nop 7\n"
"s_call_b64 s[46:47], .Loa_315\n"
"s_branch .Lbk_548\n"
".Lsl_549:\n"
"s_nop 7\n"
"s_call_b64 s[46:47], .Loa_315\n"
"s_branch .Lbk_550\n"
".Lsl_551:\n"
"s_nop 7\n"
"s_call_b64 s[46:47], .Loa_315\n"
"s_branch .Lbk_552\n"
".Lsl_553:\n"
"s_nop 7\n"
"s_call_b64 s[46:47], .Loa_315\n"
"s_branch .Lbk_554\n"
".Lsl_555:\n"
"s_nop 7\n"
"s_call_b64 s[46:47], .Loa_315\n"
"s_branch .Lbk_556\n"
".Lsl_557:\n"
"s_nop 7\n"
"s_call_b64 s[46:47], .Loa_315\n"
"s_branch .Lbk_558\n"
".L_yend:\n"
      :: "{s[0:1]}"(__builtin_amdgcn_kernarg_segment_ptr()), "{s2}"((int)__builtin_amdgcn_workgroup_id_x()),
         "{s3}"(__builtin_amdgcn_readfirstlane(tid >> 6)), "{v0}"(tid),
         "{v1}"((unsigned)(size_t)(__attribute__((address_space(3))) unsigned char*)smem)
      : "memory", "vcc", "scc", "m0", "v255", "s95");
}

extern "C" void kernel_launch(void* const* d_in, const int* in_sizes, int n_in,
                              void* d_out, int out_size, void* d_ws, size_t ws_size,
                              hipStream_t stream) {
  mega_kernel<<<256, 512, 0, stream>>>((const u16*)d_in[0], (const u16*)d_in[1], (const u16*)d_in[2],
                                       (const u16*)d_in[4], (const u16*)d_in[5], (const u16*)d_in[6],
                                       (const u16*)d_in[7], (const u16*)d_in[8], (const u16*)d_in[9],
                                       (u16*)d_out, (unsigned char*)d_ws);
}
```

```cpp
#include <hip/hip_runtime.h>
typedef unsigned short u16;

__global__ __launch_bounds__(512, 2) void mega_kernel(
    const u16* __restrict__ x_, const u16* __restrict__ wqkv_, const u16* __restrict__ wo_,
    const u16* __restrict__ log_A_real_, const u16* __restrict__ A_imag_,
    const u16* __restrict__ C_real_, const u16* __restrict__ C_imag_, const u16* __restrict__ log_dt_,
    const u16* __restrict__ D_skip, u16* __restrict__ out, unsigned char* __restrict__ ws_) {
  __shared__ __attribute__((aligned(16))) unsigned char smem[163840];
  const int tid = (int)__builtin_amdgcn_workitem_id_x();
  asm volatile(
"s_cmp_lt_u32 s3, 4\n"
"s_cbranch_scc1 .Lxs_1\n"
"s_load_dwordx8 s[8:15], s[0:1], 0x18\n"
"s_load_dwordx4 s[16:19], s[0:1], 0x38\n"
"s_load_dwordx2 s[22:23], s[0:1], 0x0\n"
"s_load_dwordx4 s[24:27], s[0:1], 0x48\n"
"s_getpc_b64 s[4:5]\n"
".Lmk_a:\n"
"v_lshrrev_b32 v79, 6, v0\n"
"v_and_b32 v78, 7, v0\n"
"v_lshl_add_u32 v79, v79, 3, v78\n"
"s_lshr_b32 s79, s2, 3\n"
"s_lshl_b32 s79, s79, 7\n"
"s_sub_u32 s79, s79, 0x20000\n"
"v_lshl_add_u32 v2, v79, 12, s79\n"
"v_min_u32 v2, .L_yend-.Lmk_a-4, v2\n"
"s_mov_b32 m0, 163328\n"
"v_mov_b32 v4, 0\n"
"global_load_lds_dword v2, s[4:5]\n"
"v_lshlrev_b32 v79, 2, v0\n"
"v_add_u32 v2, 0x26000, v79\n"
"ds_write_b32 v2, v4\n"
"v_lshlrev_b32 v79, 4, v0\n"
"v_add_u32 v2, 0x25840, v79\n"
"v_mov_b32 v8, -1\n"
"v_mov_b32 v9, -1\n"
"v_mov_b32 v10, -1\n"
"v_mov_b32 v11, -1\n"
"ds_write_b128 v2, v[8:11]\n"
"ds_write_b128 v2, v[8:11] offset:4096\n"
"s_and_b32 s79, s2, 7\n"
"s_lshl_b32 s79, s79, 4\n"
"s_lshr_b32 s78, s2, 3\n"
"s_add_u32 s79, s79, s78\n"
"s_and_b32 s78, s2, 3\n"
"s_lshl_b32 s78, s78, 3\n"
"s_and_b32 s77, s2, 4\n"
"s_cmp_eq_i32 s77, 4\n"
"s_cselect_b32 s78, 32, s78\n"
"s_add_u32 s79, s79, s78\n"
"s_lshl_b32 s79, s79, 2\n"
"s_and_b32 s78, s3, 3\n"
"s_add_u32 s6, s79, s78\n"
"v_and_b32 v79, 63, v0\n"
"s_mul_i32 s79, s6, 0x180\n"
"v_lshl_add_u32 v2, v79, 1, s79\n"
"s_lshl_b32 s79, s6, 1\n"
"s_and_b32 s20, s79, 0xffc\n"
"s_and_b32 s79, s2, 4\n"
"s_lshl_b32 s79, s79, 4\n"
"s_add_u32 s7, s2, s79\n"
"s_waitcnt lgkmcnt(0)\n"
"s_barrier\n"
"s_cmp_ge_u32 s7, 192\n"
"s_cbranch_scc1 .Lnl_2\n"
"s_load_dword s40, s[16:17], s20\n"
"global_load_ushort v32, v2, s[8:9] offset:0\n"
"global_load_ushort v33, v2, s[10:11] offset:0\n"
"global_load_ushort v34, v2, s[12:13] offset:0\n"
"global_load_ushort v35, v2, s[14:15] offset:0\n"
"global_load_ushort v40, v2, s[8:9] offset:128\n"
"global_load_ushort v41, v2, s[10:11] offset:128\n"
"global_load_ushort v42, v2, s[12:13] offset:128\n"
"global_load_ushort v43, v2, s[14:15] offset:128\n"
"global_load_ushort v44, v2, s[8:9] offset:256\n"
"global_load_ushort v45, v2, s[10:11] offset:256\n"
"global_load_ushort v46, v2, s[12:13] offset:256\n"
"global_load_ushort v47, v2, s[14:15] offset:256\n"
"s_load_dword s41, s[18:19], s20\n"
".Lnl_2:\n"
"s_load_dword s16, s[26:27], 0x0\n"
"s_branch .L_xend\n"
".Lxs_1:\n"
"s_load_dwordx4 s[20:23], s[0:1], 0x0\n"
"s_load_dwordx2 s[56:57], s[0:1], 0x10\n"
"s_load_dwordx4 s[24:27], s[0:1], 0x48\n"
"v_and_b32 v0, 63, v0\n"
"s_barrier\n"
"s_lshl_b32 s79, s2, 2\n"
"s_add_u32 s31, s79, s3\n"
"s_mul_i32 s79, s31, 0xcd\n"
"s_lshr_b32 s46, s79, 11\n"
"s_mul_i32 s79, s46, 10\n"
"s_sub_u32 s47, s31, s79\n"
"s_lshl_b32 s79, s47, 6\n"
"v_add_u32 v68, s79, v0\n"
"v_lshlrev_b32 v80, 1, v68\n"
"s_mul_i32 s48, s46, 0x7800\n"
"s_mov_b32 s14, 0x258000\n"
"s_mov_b32 s15, 0x20000\n"
"s_waitcnt lgkmcnt(0)\n"
"s_mov_b64 s[12:13], s[22:23]\n"
"s_cmp_ge_u32 s2, 200\n"
"s_cbranch_scc1 .Lnorep_3\n"
"buffer_load_ushort v28, v80, s[12:15], s48 offen offset:0\n"
"buffer_load_ushort v36, v80, s[12:15], s48 offen offset:1280\n"
"buffer_load_ushort v44, v80, s[12:15], s48 offen offset:2560\n"
"s_add_u32 s48, s48, 3840\n"
"buffer_load_ushort v29, v80, s[12:15], s48 offen offset:0\n"
"buffer_load_ushort v37, v80, s[12:15], s48 offen offset:1280\n"
"buffer_load_ushort v45, v80, s[12:15], s48 offen offset:2560\n"
"s_add_u32 s48, s48, 3840\n"
"buffer_load_ushort v30, v80, s[12:15], s48 offen offset:0\n"
"buffer_load_ushort v38, v80, s[12:15], s48 offen offset:1280\n"
"buffer_load_ushort v46, v80, s[12:15], s48 offen offset:2560\n"
"s_add_u32 s48, s48, 3840\n"
"buffer_load_ushort v31, v80, s[12:15], s48 offen offset:0\n"
"buffer_load_ushort v39, v80, s[12:15], s48 offen offset:1280\n"
"buffer_load_ushort v47, v80, s[12:15], s48 offen offset:2560\n"
"s_add_u32 s48, s48, 3840\n"
"buffer_load_ushort v32, v80, s[12:15], s48 offen offset:0\n"
"buffer_load_ushort v40, v80, s[12:15], s48 offen offset:1280\n"
"buffer_load_ushort v48, v80, s[12:15], s48 offen offset:2560\n"
"s_add_u32 s48, s48, 3840\n"
"buffer_load_ushort v33, v80, s[12:15], s48 offen offset:0\n"
"buffer_load_ushort v41, v80, s[12:15], s48 offen offset:1280\n"
"buffer_load_ushort v49, v80, s[12:15], s48 offen offset:2560\n"
"s_add_u32 s48, s48, 3840\n"
"buffer_load_ushort v34, v80, s[12:15], s48 offen offset:0\n"
"buffer_load_ushort v42, v80, s[12:15], s48 offen offset:1280\n"
"buffer_load_ushort v50, v80, s[12:15], s48 offen offset:2560\n"
"s_add_u32 s48, s48, 3840\n"
"buffer_load_ushort v35, v80, s[12:15], s48 offen offset:0\n"
"buffer_load_ushort v43, v80, s[12:15], s48 offen offset:1280\n"
"buffer_load_ushort v51, v80, s[12:15], s48 offen offset:2560\n"
"s_branch .Lrep_4\n"
".Lnorep_3:\n"
"s_sleep 20\n"
"s_mov_b64 s[12:13], s[56:57]\n"
"s_cmp_lt_i32 s31, 0x3e8\n"
"s_cselect_b32 s14, 0xc8000, 0\n"
"v_lshlrev_b32 v80, 1, v0\n"
"s_lshl_b32 s79, s31, 2\n"
"s_add_u32 s79, s79, 0xfffff380\n"
"s_mul_i32 s79, s79, 0xcd\n"
"s_lshr_b32 s80, s79, 11\n"
"s_lshl_b32 s79, s31, 2\n"
"s_add_u32 s79, s79, 0xfffff380\n"
"s_mul_i32 s78, s80, 10\n"
"s_sub_u32 s84, s79, s78\n"
"s_mul_i32 s79, s80, 0x67\n"
"s_lshr_b32 s79, s79, 9\n"
"s_mul_i32 s79, s79, 20\n"
"s_lshl_b32 s78, s80, 2\n"
"s_add_u32 s79, s79, s78\n"
"s_mul_i32 s79, s79, 0x500\n"
"s_lshl_b32 s78, s84, 7\n"
"s_add_u32 s48, s79, s78\n"
"s_add_u32 s49, s48, 0x6400\n"
"buffer_load_ushort v100, v80, s[12:15], s48 offen offset:0\n"
"buffer_load_ushort v101, v80, s[12:15], s48 offen offset:1280\n"
"buffer_load_ushort v102, v80, s[12:15], s48 offen offset:2560\n"
"buffer_load_ushort v103, v80, s[12:15], s48 offen offset:3840\n"
"buffer_load_ushort v104, v80, s[12:15], s49 offen offset:0\n"
"buffer_load_ushort v105, v80, s[12:15], s49 offen offset:1280\n"
"buffer_load_ushort v106, v80, s[12:15], s49 offen offset:2560\n"
"buffer_load_ushort v107, v80, s[12:15], s49 offen offset:3840\n"
"s_lshl_b32 s79, s31, 2\n"
"s_add_u32 s79, s79, 0xfffff381\n"
"s_mul_i32 s79, s79, 0xcd\n"
"s_lshr_b32 s81, s79, 11\n"
"s_lshl_b32 s79, s31, 2\n"
"s_add_u32 s79, s79, 0xfffff381\n"
"s_mul_i32 s78, s81, 10\n"
"s_sub_u32 s85, s79, s78\n"
"s_mul_i32 s79, s81, 0x67\n"
"s_lshr_b32 s79, s79, 9\n"
"s_mul_i32 s79, s79, 20\n"
"s_lshl_b32 s78, s81, 2\n"
"s_add_u32 s79, s79, s78\n"
"s_mul_i32 s79, s79, 0x500\n"
"s_lshl_b32 s78, s85, 7\n"
"s_add_u32 s48, s79, s78\n"
"s_add_u32 s49, s48, 0x6400\n"
"buffer_load_ushort v108, v80, s[12:15], s48 offen offset:0\n"
"buffer_load_ushort v109, v80, s[12:15], s48 offen offset:1280\n"
"buffer_load_ushort v110, v80, s[12:15], s48 offen offset:2560\n"
"buffer_load_ushort v111, v80, s[12:15], s48 offen offset:3840\n"
"buffer_load_ushort v112, v80, s[12:15], s49 offen offset:0\n"
"buffer_load_ushort v113, v80, s[12:15], s49 offen offset:1280\n"
"buffer_load_ushort v114, v80, s[12:15], s49 offen offset:2560\n"
"buffer_load_ushort v115, v80, s[12:15], s49 offen offset:3840\n"
"s_lshl_b32 s79, s31, 2\n"
"s_add_u32 s79, s79, 0xfffff382\n"
"s_mul_i32 s79, s79, 0xcd\n"
"s_lshr_b32 s82, s79, 11\n"
"s_lshl_b32 s79, s31, 2\n"
"s_add_u32 s79, s79, 0xfffff382\n"
"s_mul_i32 s78, s82, 10\n"
"s_sub_u32 s86, s79, s78\n"
"s_mul_i32 s79, s82, 0x67\n"
"s_lshr_b32 s79, s79, 9\n"
"s_mul_i32 s79, s79, 20\n"
"s_lshl_b32 s78, s82, 2\n"
"s_add_u32 s79, s79, s78\n"
"s_mul_i32 s79, s79, 0x500\n"
"s_lshl_b32 s78, s86, 7\n"
"s_add_u32 s48, s79, s78\n"
"s_add_u32 s49, s48, 0x6400\n"
"buffer_load_ushort v116, v80, s[12:15], s48 offen offset:0\n"
"buffer_load_ushort v117, v80, s[12:15], s48 offen offset:1280\n"
"buffer_load_ushort v118, v80, s[12:15], s48 offen offset:2560\n"
"buffer_load_ushort v119, v80, s[12:15], s48 offen offset:3840\n"
"buffer_load_ushort v120, v80, s[12:15], s49 offen offset:0\n"
"buffer_load_ushort v121, v80, s[12:15], s49 offen offset:1280\n"
"buffer_load_ushort v122, v80, s[12:15], s49 offen offset:2560\n"
"buffer_load_ushort v123, v80, s[12:15], s49 offen offset:3840\n"
"s_lshl_b32 s79, s31, 2\n"
"s_add_u32 s79, s79, 0xfffff383\n"
"s_mul_i32 s79, s79, 0xcd\n"
"s_lshr_b32 s83, s79, 11\n"
"s_lshl_b32 s79, s31, 2\n"
"s_add_u32 s79, s79, 0xfffff383\n"
"s_mul_i32 s78, s83, 10\n"
"s_sub_u32 s87, s79, s78\n"
"s_mul_i32 s79, s83, 0x67\n"
"s_lshr_b32 s79, s79, 9\n"
"s_mul_i32 s79, s79, 20\n"
"s_lshl_b32 s78, s83, 2\n"
"s_add_u32 s79, s79, s78\n"
"s_mul_i32 s79, s79, 0x500\n"
"s_lshl_b32 s78, s87, 7\n"
"s_add_u32 s48, s79, s78\n"
"s_add_u32 s49, s48, 0x6400\n"
"buffer_load_ushort v124, v80, s[12:15], s48 offen offset:0\n"
"buffer_load_ushort v125, v80, s[12:15], s48 offen offset:1280\n"
"buffer_load_ushort v126, v80, s[12:15], s48 offen offset:2560\n"
"buffer_load_ushort v127, v80, s[12:15], s48 offen offset:3840\n"
"buffer_load_ushort v128, v80, s[12:15], s49 offen offset:0\n"
"buffer_load_ushort v129, v80, s[12:15], s49 offen offset:1280\n"
"buffer_load_ushort v130, v80, s[12:15], s49 offen offset:2560\n"
"buffer_load_ushort v131, v80, s[12:15], s49 offen offset:3840\n"
".Lrep_4:\n"
"s_mov_b32 s4, s26\n"
"s_mov_b32 s5, s27\n"
"s_mov_b32 s6, 0x1688000\n"
"s_mov_b32 s7, 0x20000\n"
"s_load_dword s16, s[26:27], 0x0\n"
"v_lshlrev_b32 v1, 4, v0\n"
"s_and_b32 s79, s2, 7\n"
"s_lshl_b32 s79, s79, 4\n"
"s_lshr_b32 s78, s2, 3\n"
"s_add_u32 s79, s79, s78\n"
"s_and_b32 s78, s2, 3\n"
"s_lshl_b32 s78, s78, 3\n"
"s_and_b32 s77, s2, 4\n"
"s_cmp_eq_i32 s77, 4\n"
"s_cselect_b32 s78, 32, s78\n"
"s_add_u32 s79, s79, s78\n"
"s_and_b32 s78, s2, 4\n"
"s_lshl_b32 s78, s78, 4\n"
"s_add_u32 s78, s2, s78\n"
"s_cmp_lt_i32 s78, 0xc0\n"
"s_cselect_b32 s45, s79, -1\n"
"s_mov_b32 s8, s20\n"
"s_mov_b32 s9, s21\n"
"s_mov_b32 s10, 0x280000\n"
"s_mov_b32 s11, 0x20000\n"
"s_mov_b32 s12, s24\n"
"s_mov_b32 s13, s25\n"
"s_mov_b32 s14, 0x280000\n"
"s_mov_b32 s15, 0x20000\n"
"s_and_b32 s79, s2, 1\n"
"s_lshl_b32 s79, s79, 3\n"
"s_lshr_b32 s78, s2, 3\n"
"s_and_b32 s78, s78, 7\n"
"s_add_u32 s18, s79, s78\n"
"s_lshr_b32 s79, s2, 1\n"
"s_and_b32 s79, s79, 3\n"
"s_lshl_b32 s79, s79, 2\n"
"s_lshr_b32 s78, s2, 3\n"
"s_lshr_b32 s78, s78, 3\n"
"s_add_u32 s19, s79, s78\n"
"s_mul_i32 s79, s19, 0\n"
"s_add_u32 s79, s3, s79\n"
"s_and_b32 s43, s79, 3\n"
"v_mul_i32_i24 v79, 0xcd, v68\n"
"v_lshrrev_b32 v84, 13, v79\n"
"v_mul_i32_i24 v79, 40, v84\n"
"v_sub_u32 v85, v68, v79\n"
"v_mul_i32_i24 v79, 0xa0, v84\n"
"v_lshlrev_b32 v84, 10, v79\n"
"v_subrev_u32 v79, 24, v85\n"
"v_and_b32 v78, 15, v85\n"
"v_cmp_le_i32 vcc, 32, v85\n"
"s_nop 1\n"
"v_cndmask_b32 v79, v78, v79, vcc\n"
"v_lshrrev_b32 v78, 4, v85\n"
"s_mov_b32 s79, 0xa000\n"
"v_mad_i32_i24 v78, v78, s79, v84\n"
"v_lshl_add_u32 v64, v79, 4, v78\n"
"v_mov_b32 v86, v85\n"
"v_and_b32 v79, 15, v86\n"
"v_lshrrev_b32 v78, 4, v86\n"
"v_min_i32 v78, 3, v78\n"
"s_mov_b32 s79, 0xa000\n"
"v_mad_i32_i24 v78, v78, s79, v84\n"
"v_mov_b32 v77, 0\n"
"v_mov_b32 v76, 0x400\n"
"v_lshrrev_b32 v75, 4, v86\n"
"v_cmp_eq_i32 vcc, 3, v75\n"
"s_nop 1\n"
"v_cndmask_b32 v77, v76, v77, vcc\n"
"v_add_u32 v78, v78, v77\n"
"v_lshl_add_u32 v65, v79, 4, v78\n"
"v_add_u32 v86, 40, v85\n"
"v_and_b32 v79, 15, v86\n"
"v_lshrrev_b32 v78, 4, v86\n"
"v_min_i32 v78, 3, v78\n"
"s_mov_b32 s79, 0xa000\n"
"v_mad_i32_i24 v78, v78, s79, v84\n"
"v_mov_b32 v77, 0\n"
"v_mov_b32 v76, 0x400\n"
"v_lshrrev_b32 v75, 4, v86\n"
"v_cmp_eq_i32 vcc, 3, v75\n"
"s_nop 1\n"
"v_cndmask_b32 v77, v76, v77, vcc\n"
"v_add_u32 v78, v78, v77\n"
"v_lshl_add_u32 v66, v79, 4, v78\n"
"v_subrev_u32 v79, 0x80, v64\n"
"v_mov_b32 v78, 0x40000000\n"
"v_cmp_le_i32 vcc, 32, v85\n"
"s_nop 1\n"
"v_cndmask_b32 v67, v78, v79, vcc\n"
"s_lshr_b32 s79, s46, 2\n"
"s_lshl_b32 s79, s79, 11\n"
"s_and_b32 s78, s46, 3\n"
"s_lshl_b32 s78, s78, 8\n"
"s_add_u32 s79, s79, s78\n"
"s_add_u32 s48, s79, 0x100000\n"
"s_lshl_b32 s79, s47, 10\n"
"s_lshr_b32 s78, s46, 2\n"
"s_lshl_b32 s78, s78, 5\n"
"s_add_u32 s79, s79, s78\n"
"s_and_b32 s78, s46, 3\n"
"s_lshl_b32 s78, s78, 2\n"
"s_add_u32 s79, s79, s78\n"
"s_lshl_b32 s78, s2, 5\n"
"s_lshl_b32 s77, s3, 2\n"
"s_add_u32 s78, s78, s77\n"
"s_add_u32 s78, s78, 0xf00\n"
"s_cmp_lt_i32 s31, 0x320\n"
"s_cselect_b32 s79, s79, s78\n"
"s_add_u32 s32, s79, 0x1000\n"
"v_mov_b32 v20, 0x3f803f80\n"
"v_mov_b32 v21, 0x3f803f80\n"
"v_mov_b32 v22, 0x3f803f80\n"
"v_mov_b32 v23, 0x3f803f80\n"
"s_mov_b32 s79, 0x55\n"
"s_cmp_lt_i32 s47, 5\n"
"s_cselect_b32 s79, s79, 0xaa\n"
"s_cmp_lt_i32 s31, 0x320\n"
"s_cselect_b32 s58, s79, 0xff\n"
"s_mov_b32 s59, 0\n"
"s_mul_i32 s79, s19, 0x14000\n"
"s_mul_i32 s78, s43, 0x140\n"
"s_add_u32 s28, s79, s78\n"
"s_mul_i32 s29, s43, 0x1400\n"
"s_lshr_b32 s79, s2, 3\n"
"s_and_b32 s31, s79, 7\n"
"s_mul_i32 s33, s43, 0x140\n"
"s_sub_u32 s79, s31, 5\n"
"s_cmp_ge_i32 s31, 5\n"
"s_cselect_b32 s31, s79, s31\n"
"s_mul_i32 s79, s31, 0x5000\n"
"s_add_u32 s79, s79, s28\n"
"s_sub_u32 s79, s79, 0x5000\n"
"s_max_i32 s60, s79, s33\n"
"s_mul_i32 s79, s31, 0x5000\n"
"s_mov_b32 s78, 0x12400\n"
"s_cmp_ge_i32 s31, 2\n"
"s_cselect_b32 s78, s78, 0xa000\n"
"s_add_u32 s79, s79, s78\n"
"s_cmp_eq_i32 s31, 4\n"
"s_cselect_b32 s79, 0, s79\n"
"s_add_u32 s61, s29, s79\n"
"s_sub_u32 s79, s31, 4\n"
"s_add_u32 s78, s31, 1\n"
"s_cmp_ge_i32 s31, 4\n"
"s_cselect_b32 s31, s79, s78\n"
"s_mul_i32 s79, s31, 0x5000\n"
"s_add_u32 s79, s79, s28\n"
"s_sub_u32 s79, s79, 0x5000\n"
"s_max_i32 s62, s79, s33\n"
"s_mul_i32 s79, s31, 0x5000\n"
"s_mov_b32 s78, 0x12400\n"
"s_cmp_ge_i32 s31, 2\n"
"s_cselect_b32 s78, s78, 0xa000\n"
"s_add_u32 s79, s79, s78\n"
"s_cmp_eq_i32 s31, 4\n"
"s_cselect_b32 s79, 0, s79\n"
"s_add_u32 s63, s29, s79\n"
"s_sub_u32 s79, s31, 4\n"
"s_add_u32 s78, s31, 1\n"
"s_cmp_ge_i32 s31, 4\n"
"s_cselect_b32 s31, s79, s78\n"
"s_mul_i32 s79, s31, 0x5000\n"
"s_add_u32 s79, s79, s28\n"
"s_sub_u32 s79, s79, 0x5000\n"
"s_max_i32 s64, s79, s33\n"
"s_mul_i32 s79, s31, 0x5000\n"
"s_mov_b32 s78, 0x12400\n"
"s_cmp_ge_i32 s31, 2\n"
"s_cselect_b32 s78, s78, 0xa000\n"
"s_add_u32 s79, s79, s78\n"
"s_cmp_eq_i32 s31, 4\n"
"s_cselect_b32 s79, 0, s79\n"
"s_add_u32 s65, s29, s79\n"
"s_sub_u32 s79, s31, 4\n"
"s_add_u32 s78, s31, 1\n"
"s_cmp_ge_i32 s31, 4\n"
"s_cselect_b32 s31, s79, s78\n"
"s_mul_i32 s79, s31, 0x5000\n"
"s_add_u32 s79, s79, s28\n"
"s_sub_u32 s79, s79, 0x5000\n"
"s_max_i32 s66, s79, s33\n"
"s_mul_i32 s79, s31, 0x5000\n"
"s_mov_b32 s78, 0x12400\n"
"s_cmp_ge_i32 s31, 2\n"
"s_cselect_b32 s78, s78, 0xa000\n"
"s_add_u32 s79, s79, s78\n"
"s_cmp_eq_i32 s31, 4\n"
"s_cselect_b32 s79, 0, s79\n"
"s_add_u32 s67, s29, s79\n"
"s_sub_u32 s79, s31, 4\n"
"s_add_u32 s78, s31, 1\n"
"s_cmp_ge_i32 s31, 4\n"
"s_cselect_b32 s31, s79, s78\n"
"s_mul_i32 s79, s31, 0x5000\n"
"s_add_u32 s79, s79, s28\n"
"s_sub_u32 s79, s79, 0x5000\n"
"s_max_i32 s68, s79, s33\n"
"s_mul_i32 s79, s31, 0x5000\n"
"s_mov_b32 s78, 0x12400\n"
"s_cmp_ge_i32 s31, 2\n"
"s_cselect_b32 s78, s78, 0xa000\n"
"s_add_u32 s79, s79, s78\n"
"s_cmp_eq_i32 s31, 4\n"
"s_cselect_b32 s79, 0, s79\n"
"s_add_u32 s69, s29, s79\n"
"v_and_b32 v79, 3, v0\n"
"v_lshrrev_b32 v78, 5, v0\n"
"v_lshlrev_b32 v78, 1, v78\n"
"v_xor_b32 v79, v79, v78\n"
"v_lshrrev_b32 v78, 2, v0\n"
"v_mul_i32_i24 v78, 0x500, v78\n"
"v_lshl_add_u32 v2, v79, 4, v78\n"
"v_min_i32 v79, 24, v0\n"
"s_lshr_b32 s79, s2, 3\n"
"s_and_b32 s79, s79, 7\n"
"s_lshl_b32 s79, s79, 2\n"
"s_add_u32 s79, s79, s3\n"
"s_mul_i32 s79, s79, 0xc80\n"
"s_lshl_b32 s78, s19, 6\n"
"s_add_u32 s78, s78, 0x3f0\n"
"s_mul_i32 s78, s78, 0x500\n"
"s_add_u32 s79, s79, s78\n"
"v_lshl_add_u32 v89, v79, 7, s79\n"
"s_mov_b32 s31, 0x05040100\n"
"s_cmp_ge_u32 s2, 200\n"
"s_cbranch_scc1 .Lnorep_5\n"
"buffer_store_dwordx4 v[20:23], v67, s[4:7], s48 offen sc1\n"
"s_waitcnt vmcnt(1)\n"
"v_perm_b32 v52, v29, v28, s31\n"
"v_perm_b32 v53, v31, v30, s31\n"
"v_perm_b32 v54, v33, v32, s31\n"
"v_perm_b32 v55, v35, v34, s31\n"
"buffer_store_dwordx4 v[52:55], v64, s[4:7], s48 offen sc1\n"
"v_perm_b32 v56, v37, v36, s31\n"
"v_perm_b32 v57, v39, v38, s31\n"
"v_perm_b32 v58, v41, v40, s31\n"
"v_perm_b32 v59, v43, v42, s31\n"
"buffer_store_dwordx4 v[56:59], v65, s[4:7], s48 offen sc1\n"
"v_perm_b32 v60, v45, v44, s31\n"
"v_perm_b32 v61, v47, v46, s31\n"
"v_perm_b32 v62, v49, v48, s31\n"
"v_perm_b32 v63, v51, v50, s31\n"
"buffer_store_dwordx4 v[60:63], v66, s[4:7], s48 offen sc1\n"
"s_branch .Lrep_6\n"
".Lnorep_5:\n"
"s_lshl_b32 s79, s84, 6\n"
"v_add_u32 v68, s79, v0\n"
"v_mul_i32_i24 v79, 0xcd, v68\n"
"v_lshrrev_b32 v81, 14, v79\n"
"s_lshr_b32 s79, s80, 2\n"
"s_mul_i32 s79, s79, 13\n"
"s_lshr_b32 s79, s79, 6\n"
"s_and_b32 s79, s79, 1\n"
"s_lshl_b32 s79, s79, 1\n"
"v_mul_i32_i24 v79, 0x50, v81\n"
"v_sub_u32 v79, v68, v79\n"
"v_lshrrev_b32 v79, 4, v79\n"
"v_add_u32 v82, s79, v79\n"
"v_subrev_u32 v79, 5, v82\n"
"v_cmp_le_i32 vcc, 5, v82\n"
"s_nop 1\n"
"v_cndmask_b32 v79, v82, v79, vcc\n"
"s_mov_b32 s79, 0x64\n"
"v_mad_i32_i24 v79, v81, s79, v79\n"
"v_and_b32 v78, 15, v68\n"
"v_lshlrev_b32 v78, 4, v78\n"
"v_lshl_add_u32 v64, v79, 10, v78\n"
"s_lshr_b32 s79, s80, 2\n"
"s_mul_i32 s79, s79, 0x1400\n"
"s_and_b32 s78, s80, 3\n"
"s_lshl_b32 s78, s78, 8\n"
"s_add_u32 s79, s79, s78\n"
"s_add_u32 s79, s79, 0x380000\n"
"s_cmp_lt_i32 s80, 0x50\n"
"s_cselect_b32 s80, s79, 0x40000000\n"
"s_lshl_b32 s79, s85, 6\n"
"v_add_u32 v68, s79, v0\n"
"v_mul_i32_i24 v79, 0xcd, v68\n"
"v_lshrrev_b32 v81, 14, v79\n"
"s_lshr_b32 s79, s81, 2\n"
"s_mul_i32 s79, s79, 13\n"
"s_lshr_b32 s79, s79, 6\n"
"s_and_b32 s79, s79, 1\n"
"s_lshl_b32 s79, s79, 1\n"
"v_mul_i32_i24 v79, 0x50, v81\n"
"v_sub_u32 v79, v68, v79\n"
"v_lshrrev_b32 v79, 4, v79\n"
"v_add_u32 v82, s79, v79\n"
"v_subrev_u32 v79, 5, v82\n"
"v_cmp_le_i32 vcc, 5, v82\n"
"s_nop 1\n"
"v_cndmask_b32 v79, v82, v79, vcc\n"
"s_mov_b32 s79, 0x64\n"
"v_mad_i32_i24 v79, v81, s79, v79\n"
"v_and_b32 v78, 15, v68\n"
"v_lshlrev_b32 v78, 4, v78\n"
"v_lshl_add_u32 v65, v79, 10, v78\n"
"s_lshr_b32 s79, s81, 2\n"
"s_mul_i32 s79, s79, 0x1400\n"
"s_and_b32 s78, s81, 3\n"
"s_lshl_b32 s78, s78, 8\n"
"s_add_u32 s79, s79, s78\n"
"s_add_u32 s79, s79, 0x380000\n"
"s_cmp_lt_i32 s81, 0x50\n"
"s_cselect_b32 s81, s79, 0x40000000\n"
"s_lshl_b32 s79, s86, 6\n"
"v_add_u32 v68, s79, v0\n"
"v_mul_i32_i24 v79, 0xcd, v68\n"
"v_lshrrev_b32 v81, 14, v79\n"
"s_lshr_b32 s79, s82, 2\n"
"s_mul_i32 s79, s79, 13\n"
"s_lshr_b32 s79, s79, 6\n"
"s_and_b32 s79, s79, 1\n"
"s_lshl_b32 s79, s79, 1\n"
"v_mul_i32_i24 v79, 0x50, v81\n"
"v_sub_u32 v79, v68, v79\n"
"v_lshrrev_b32 v79, 4, v79\n"
"v_add_u32 v82, s79, v79\n"
"v_subrev_u32 v79, 5, v82\n"
"v_cmp_le_i32 vcc, 5, v82\n"
"s_nop 1\n"
"v_cndmask_b32 v79, v82, v79, vcc\n"
"s_mov_b32 s79, 0x64\n"
"v_mad_i32_i24 v79, v81, s79, v79\n"
"v_and_b32 v78, 15, v68\n"
"v_lshlrev_b32 v78, 4, v78\n"
"v_lshl_add_u32 v66, v79, 10, v78\n"
"s_lshr_b32 s79, s82, 2\n"
"s_mul_i32 s79, s79, 0x1400\n"
"s_and_b32 s78, s82, 3\n"
"s_lshl_b32 s78, s78, 8\n"
"s_add_u32 s79, s79, s78\n"
"s_add_u32 s79, s79, 0x380000\n"
"s_cmp_lt_i32 s82, 0x50\n"
"s_cselect_b32 s82, s79, 0x40000000\n"
"s_lshl_b32 s79, s87, 6\n"
"v_add_u32 v68, s79, v0\n"
"v_mul_i32_i24 v79, 0xcd, v68\n"
"v_lshrrev_b32 v81, 14, v79\n"
"s_lshr_b32 s79, s83, 2\n"
"s_mul_i32 s79, s79, 13\n"
"s_lshr_b32 s79, s79, 6\n"
"s_and_b32 s79, s79, 1\n"
"s_lshl_b32 s79, s79, 1\n"
"v_mul_i32_i24 v79, 0x50, v81\n"
"v_sub_u32 v79, v68, v79\n"
"v_lshrrev_b32 v79, 4, v79\n"
"v_add_u32 v82, s79, v79\n"
"v_subrev_u32 v79, 5, v82\n"
"v_cmp_le_i32 vcc, 5, v82\n"
"s_nop 1\n"
"v_cndmask_b32 v79, v82, v79, vcc\n"
"s_mov_b32 s79, 0x64\n"
"v_mad_i32_i24 v79, v81, s79, v79\n"
"v_and_b32 v78, 15, v68\n"
"v_lshlrev_b32 v78, 4, v78\n"
"v_lshl_add_u32 v67, v79, 10, v78\n"
"s_lshr_b32 s79, s83, 2\n"
"s_mul_i32 s79, s79, 0x1400\n"
"s_and_b32 s78, s83, 3\n"
"s_lshl_b32 s78, s78, 8\n"
"s_add_u32 s79, s79, s78\n"
"s_add_u32 s79, s79, 0x380000\n"
"s_cmp_lt_i32 s83, 0x50\n"
"s_cselect_b32 s83, s79, 0x40000000\n"
"s_waitcnt vmcnt(0)\n"
"v_perm_b32 v28, v101, v100, s31\n"
"v_perm_b32 v29, v103, v102, s31\n"
"v_perm_b32 v30, v105, v104, s31\n"
"v_perm_b32 v31, v107, v106, s31\n"
"buffer_store_dwordx4 v[28:31], v64, s[4:7], s80 offen sc1\n"
"v_perm_b32 v32, v109, v108, s31\n"
"v_perm_b32 v33, v111, v110, s31\n"
"v_perm_b32 v34, v113, v112, s31\n"
"v_perm_b32 v35, v115, v114, s31\n"
"buffer_store_dwordx4 v[32:35], v65, s[4:7], s81 offen sc1\n"
"v_perm_b32 v36, v117, v116, s31\n"
"v_perm_b32 v37, v119, v118, s31\n"
"v_perm_b32 v38, v121, v120, s31\n"
"v_perm_b32 v39, v123, v122, s31\n"
"buffer_store_dwordx4 v[36:39], v66, s[4:7], s82 offen sc1\n"
"v_perm_b32 v40, v125, v124, s31\n"
"v_perm_b32 v41, v127, v126, s31\n"
"v_perm_b32 v42, v129, v128, s31\n"
"v_perm_b32 v43, v131, v130, s31\n"
"buffer_store_dwordx4 v[40:43], v67, s[4:7], s83 offen sc1\n"
".Lrep_6:\n"
"v_lshlrev_b32 v80, 14, v0\n"
"s_mov_b32 s34, 0xaaaaaaaa\n"
"s_mov_b32 s35, 0xaaaaaaaa\n"
"s_mov_b32 s40, 0xffff\n"
"s_mov_b32 s41, 0\n"
"s_mov_b32 s36, 0xfff0ffff\n"
"s_mov_b32 s37, 0xf000ff00\n"
"s_mov_b32 s38, 0xffc0fffc\n"
"s_mov_b32 s39, 0xc000fc00\n"
"s_lshl_b32 s79, s19, 2\n"
"s_add_u32 s79, s79, s3\n"
"s_mul_i32 s79, s79, 0x5000\n"
"s_mul_i32 s78, s18, 0x500\n"
"s_add_u32 s79, s79, s78\n"
"s_add_u32 s30, s79, 0x448000\n"
"s_mul_i32 s79, s18, 40\n"
"s_lshr_b32 s79, s79, 6\n"
"s_lshl_b32 s79, s79, 10\n"
"v_lshl_add_u32 v79, v0, 5, s79\n"
"v_min_i32 v78, 39, v0\n"
"v_subrev_u32 v78, 20, v78\n"
"s_mul_i32 s79, s18, 40\n"
"s_add_u32 s79, s79, 39\n"
"s_lshr_b32 s79, s79, 6\n"
"s_lshl_b32 s79, s79, 10\n"
"v_lshl_add_u32 v78, v78, 5, s79\n"
"v_cmp_gt_i32 vcc, 20, v0\n"
"s_nop 1\n"
"v_cndmask_b32 v19, v78, v79, vcc\n"
"s_and_b32 s79, s2, 7\n"
"s_lshl_b32 s79, s79, 14\n"
"s_add_u32 s50, s79, 0x1000\n"
"s_mul_i32 s79, s19, 0x14000\n"
"s_mul_i32 s78, s43, 0x1400\n"
"s_add_u32 s79, s79, s78\n"
"s_add_u32 s44, s79, 0xc28000\n"
"s_add_u32 s79, s19, s3\n"
"s_cmp_eq_i32 s79, 0\n"
"s_cselect_b32 s42, 0xfe967699, 0\n"
"s_lshl_b32 s79, s18, 2\n"
"s_add_u32 s79, s79, s3\n"
"s_mul_i32 s79, s79, 0xa000\n"
"s_add_u32 s31, s79, 0x100000\n"
"s_lshl_b32 s79, s43, 11\n"
"s_add_u32 s52, s31, s79\n"
"s_mov_b32 s79, 0xffffe800\n"
"s_cmp_ge_i32 s43, 3\n"
"s_cselect_b32 s79, s79, 0x8800\n"
"s_add_u32 s53, s52, s79\n"
"s_mov_b32 s79, 0xfffff000\n"
"s_cmp_ge_i32 s43, 2\n"
"s_cselect_b32 s79, s79, 0x9000\n"
"s_add_u32 s54, s52, s79\n"
"s_mov_b32 s79, 0xfffff800\n"
"s_cmp_ge_i32 s43, 1\n"
"s_cselect_b32 s79, s79, 0x9800\n"
"s_add_u32 s55, s52, s79\n"
"s_mov_b32 s20, 0xcccccccc\n"
"s_mov_b32 s21, 0xcccccccc\n"
"s_mov_b32 s22, 0xf0000f\n"
"s_mov_b32 s23, 0xf0000f00\n"
"v_and_b32 v169, 3, v0\n"
"v_and_b32 v168, 15, v0\n"
"v_lshrrev_b32 v167, 4, v0\n"
"v_and_b32 v166, 3, v168\n"
"v_lshlrev_b32 v165, 2, v166\n"
"v_lshlrev_b32 v164, 3, v167\n"
"v_lshlrev_b32 v163, 8, v167\n"
"v_lshlrev_b32 v162, 2, v168\n"
"v_lshlrev_b32 v161, 4, v168\n"
"v_lshlrev_b32 v160, 6, v168\n"
"v_lshrrev_b32 v159, 2, v168\n"
"v_lshrrev_b32 v158, 3, v168\n"
"v_lshlrev_b32 v157, 1, v158\n"
"v_mul_i32_i24 v156, 0xf8, v168\n"
"v_add_u32 v155, v161, v165\n"
"v_add_u32 v154, v155, v163\n"
"v_xor_b32 v153, v167, v157\n"
"v_lshlrev_b32 v152, 4, v153\n"
"v_add_u32 v151, v160, v152\n"
"v_add_u32 v17, 0x5000, v154\n"
"v_add_u32 v12, 0x26940, v162\n"
"s_lshl_b32 s79, s3, 4\n"
"v_add_u32 v150, s79, v168\n"
"v_mul_i32_i24 v79, 0xf8, v150\n"
"v_add_u32 v24, 0x5000, v79\n"
"s_lshl_b32 s79, s3, 11\n"
"s_add_u32 s79, s79, 0x5000\n"
"v_add_u32 v79, s79, v161\n"
"v_mov_b32 v78, 0x40000\n"
"v_cmp_eq_i32 vcc, v167, v159\n"
"s_nop 1\n"
"v_cndmask_b32 v92, v78, v79, vcc\n"
"v_mov_b32 v79, 0xf0\n"
"v_add_u32 v78, 48, v164\n"
"v_cmp_gt_i32 vcc, 2, v167\n"
"s_nop 1\n"
"v_cndmask_b32 v79, v78, v79, vcc\n"
"s_lshl_b32 s79, s3, 5\n"
"s_cmp_eq_i32 s3, 3\n"
"s_cselect_b32 s79, 0xb0, s79\n"
"v_add_u32 v78, s79, v164\n"
"v_mov_b32 v77, s3\n"
"v_cmp_eq_i32 vcc, 2, v77\n"
"s_nop 1\n"
"v_cndmask_b32 v79, v78, v79, vcc\n"
"s_mov_b32 s79, 0x5000\n"
"v_add3_u32 v7, v156, v79, s79\n"
"s_lshl_b32 s79, s3, 5\n"
"s_add_u32 s79, s79, 0x50\n"
"s_cmp_eq_i32 s3, 3\n"
"s_cselect_b32 s79, 0xd0, s79\n"
"s_add_u32 s79, s79, 0x5000\n"
"v_add3_u32 v8, v156, v164, s79\n"
"s_lshl_b32 s79, s3, 2\n"
"s_add_u32 s79, s79, 0x26840\n"
"v_add_u32 v79, s79, v161\n"
"v_mov_b32 v78, 0x40000\n"
"s_xor_b32 s79, s3, 2\n"
"s_lshl_b32 s79, s79, 2\n"
"v_add_u32 v77, s79, v167\n"
"v_mov_b32 v76, 1\n"
"v_cmp_gt_i32 vcc, 4, v168\n"
"s_nop 1\n"
"v_cndmask_b32 v76, 0, v76, vcc\n"
"v_cmp_eq_i32 vcc, v77, v76\n"
"s_nop 1\n"
"v_cndmask_b32 v78, v78, v12, vcc\n"
"v_cmp_eq_i32 vcc, v167, v159\n"
"s_nop 1\n"
"v_cndmask_b32 v9, v78, v79, vcc\n"
"v_add_u32 v11, 0x26840, v161\n"
"s_lshl_b32 s79, s3, 2\n"
"s_add_u32 s79, s79, 0x26840\n"
"v_add_u32 v79, s79, v161\n"
"v_mov_b32 v78, 0x40000\n"
"v_cmp_eq_i32 vcc, 0, v167\n"
"s_nop 1\n"
"v_cndmask_b32 v10, v78, v79, vcc\n"
"s_lshl_b32 s79, s3, 2\n"
"s_add_u32 s79, s79, 0x26400\n"
"v_mov_b32 v79, s79\n"
"v_mov_b32 v78, 0x40000\n"
"v_cmp_eq_i32 vcc, 0, v0\n"
"s_nop 1\n"
"v_cndmask_b32 v13, v78, v79, vcc\n"
"v_lshlrev_b32 v79, 2, v169\n"
"v_add_u32 v14, 0x26400, v79\n"
"v_add_u32 v25, v24, v164\n"
"s_lshl_b32 s79, s3, 4\n"
"v_lshl_add_u32 v79, v167, 2, s79\n"
"v_add_u32 v79, v79, v159\n"
"v_mul_i32_i24 v79, 0xf8, v79\n"
"v_lshl_add_u32 v79, v166, 3, v79\n"
"v_add_u32 v26, 0x50a0, v79\n"
"v_mov_b32 v27, 0x3727c5ac\n"
"v_xor_b32 v18, 4, v17\n"
"v_xor_b32 v79, 1, v166\n"
"v_lshl_add_u32 v91, v79, 2, v92\n"
"s_mul_i32 s79, s43, 0x1400\n"
"v_lshrrev_b32 v79, 2, v0\n"
"v_lshlrev_b32 v78, 4, v169\n"
"v_lshl_add_u32 v79, v79, 10, v78\n"
"v_add_u32 v79, s79, v79\n"
"v_mov_b32 v78, 0x40000\n"
"v_cmp_gt_i32 vcc, 20, v0\n"
"s_nop 1\n"
"v_cndmask_b32 v93, v78, v79, vcc\n"
"v_lshlrev_b32 v79, 3, v168\n"
"v_add_u32 v94, 0x9d80, v79\n"
"v_lshlrev_b32 v79, 3, v0\n"
"v_add_u32 v16, 0x9d80, v79\n"
"s_mul_i32 s79, s43, 0x1400\n"
"v_add_u32 v88, s79, v151\n"
"s_lshl_b32 s79, s43, 10\n"
"v_add_u32 v3, s79, v151\n"
"s_lshl_b32 s79, s43, 10\n"
"s_add_u32 s79, s79, 0xfffff400\n"
"s_lshl_b32 s78, s43, 10\n"
"s_add_u32 s78, s78, 0x4400\n"
"s_cmp_ge_i32 s43, 3\n"
"s_cselect_b32 s79, s79, s78\n"
"v_add_u32 v4, s79, v151\n"
"s_lshl_b32 s79, s43, 10\n"
"s_add_u32 s79, s79, 0xfffff800\n"
"s_lshl_b32 s78, s43, 10\n"
"s_add_u32 s78, s78, 0x4800\n"
"s_cmp_ge_i32 s43, 2\n"
"s_cselect_b32 s79, s79, s78\n"
"v_add_u32 v5, s79, v151\n"
"s_lshl_b32 s79, s43, 10\n"
"s_add_u32 s79, s79, 0xfffffc00\n"
"s_lshl_b32 s78, s43, 10\n"
"s_add_u32 s78, s78, 0x4c00\n"
"s_cmp_ge_i32 s43, 1\n"
"s_cselect_b32 s79, s79, s78\n"
"v_add_u32 v6, s79, v151\n"
"s_waitcnt lgkmcnt(0)\n"
"s_mul_i32 s16, s16, 0x2c9277b5\n"
"s_add_u32 s16, s16, 0xac564b05\n"
"v_mov_b32 v82, s16\n"
"s_waitcnt vmcnt(0)\n"
"s_mov_b64 exec, s[58:59]\n"
"buffer_store_dword v82, v80, s[4:7], s32 offen sc1\n"
"s_mov_b64 exec, -1\n"
"s_setprio 1\n"
"s_add_u32 m0, s61, 0\n"
"s_nop 0\n"
"buffer_load_dwordx4 v2, s[8:11], s60 offen offset:0 lds\n"
"s_add_u32 m0, s61, 0x3c0\n"
"s_nop 0\n"
"buffer_load_dwordx4 v2, s[8:11], s60 offen offset:64 lds\n"
"s_add_u32 m0, s61, 0x780\n"
"s_nop 0\n"
"buffer_load_dwordx4 v2, s[8:11], s60 offen offset:128 lds\n"
"s_add_u32 m0, s61, 0xb40\n"
"s_nop 0\n"
"buffer_load_dwordx4 v2, s[8:11], s60 offen offset:192 lds\n"
"s_add_u32 m0, s61, 0xf00\n"
"s_nop 0\n"
"buffer_load_dwordx4 v2, s[8:11], s60 offen offset:256 lds\n"
"s_add_u32 m0, s63, 0\n"
"s_nop 0\n"
"buffer_load_dwordx4 v2, s[8:11], s62 offen offset:0 lds\n"
"s_add_u32 m0, s63, 0x3c0\n"
"s_nop 0\n"
"buffer_load_dwordx4 v2, s[8:11], s62 offen offset:64 lds\n"
"s_add_u32 m0, s63, 0x780\n"
"s_nop 0\n"
"buffer_load_dwordx4 v2, s[8:11], s62 offen offset:128 lds\n"
"s_add_u32 m0, s63, 0xb40\n"
"s_nop 0\n"
"buffer_load_dwordx4 v2, s[8:11], s62 offen offset:192 lds\n"
"s_add_u32 m0, s63, 0xf00\n"
"s_nop 0\n"
"buffer_load_dwordx4 v2, s[8:11], s62 offen offset:256 lds\n"
"s_add_u32 m0, s65, 0\n"
"s_nop 0\n"
"buffer_load_dwordx4 v2, s[8:11], s64 offen offset:0 lds\n"
"s_add_u32 m0, s65, 0x3c0\n"
"s_nop 0\n"
"buffer_load_dwordx4 v2, s[8:11], s64 offen offset:64 lds\n"
"s_add_u32 m0, s65, 0x780\n"
"s_nop 0\n"
"buffer_load_dwordx4 v2, s[8:11], s64 offen offset:128 lds\n"
"s_add_u32 m0, s65, 0xb40\n"
"s_nop 0\n"
"buffer_load_dwordx4 v2, s[8:11], s64 offen offset:192 lds\n"
"s_add_u32 m0, s65, 0xf00\n"
"s_nop 0\n"
"buffer_load_dwordx4 v2, s[8:11], s64 offen offset:256 lds\n"
"s_add_u32 m0, s67, 0\n"
"s_nop 0\n"
"buffer_load_dwordx4 v2, s[8:11], s66 offen offset:0 lds\n"
"s_add_u32 m0, s67, 0x3c0\n"
"s_nop 0\n"
"buffer_load_dwordx4 v2, s[8:11], s66 offen offset:64 lds\n"
"s_add_u32 m0, s67, 0x780\n"
"s_nop 0\n"
"buffer_load_dwordx4 v2, s[8:11], s66 offen offset:128 lds\n"
"s_add_u32 m0, s67, 0xb40\n"
"s_nop 0\n"
"buffer_load_dwordx4 v2, s[8:11], s66 offen offset:192 lds\n"
"s_add_u32 m0, s67, 0xf00\n"
"s_nop 0\n"
"buffer_load_dwordx4 v2, s[8:11], s66 offen offset:256 lds\n"
"s_add_u32 m0, s69, 0\n"
"s_nop 0\n"
"buffer_load_dwordx4 v2, s[8:11], s68 offen offset:0 lds\n"
"s_add_u32 m0, s69, 0x3c0\n"
"s_nop 0\n"
"buffer_load_dwordx4 v2, s[8:11], s68 offen offset:64 lds\n"
"s_add_u32 m0, s69, 0x780\n"
"s_nop 0\n"
"buffer_load_dwordx4 v2, s[8:11], s68 offen offset:128 lds\n"
"s_add_u32 m0, s69, 0xb40\n"
"s_nop 0\n"
"buffer_load_dwordx4 v2, s[8:11], s68 offen offset:192 lds\n"
"s_add_u32 m0, s69, 0xf00\n"
"s_nop 0\n"
"buffer_load_dwordx4 v2, s[8:11], s68 offen offset:256 lds\n"
"buffer_load_dwordx4 v[96:99], v19, s[4:7], s50 offen sc1\n"
"s_waitcnt vmcnt(1)\n"
"ds_write_b128 v93, v[20:23] offset:40960\n"
"ds_write_b32 v13, v13 offset:0\n"
"v_add_u32 v88, 0xa000, v88\n"
"ds_read_b128 v[68:71], v88 offset:0\n"
"ds_read_b128 v[72:75], v88 offset:1024\n"
"ds_read_b128 v[76:79], v88 offset:2048\n"
"ds_read_b128 v[80:83], v88 offset:3072\n"
"s_waitcnt vmcnt(0)\n"
"v_xor_b32 v96, s16, v96\n"
"v_xor_b32 v97, s16, v97\n"
"v_xor_b32 v98, s16, v98\n"
"v_xor_b32 v99, s16, v99\n"
"v_or_b32 v96, v96, v97\n"
"v_or3_b32 v96, v96, v98, v99\n"
"v_cmp_ne_u32 vcc, 0, v96\n"
"s_cbranch_vccnz .Lwt_11\n"
"buffer_load_dwordx4 v[96:99], v1, s[4:7], s52 offen offset:0 sc1\n"
"s_waitcnt lgkmcnt(3)\n"
"v_mfma_f32_16x16x32_bf16 v[28:31], v[68:71], v[68:71], 0\n"
"ds_read_b128 v[84:87], v88 offset:4096\n"
"v_mfma_f32_16x16x32_bf16 v[32:35], v[20:23], v[68:71], 0\n"
"s_waitcnt lgkmcnt(3)\n"
"v_mfma_f32_16x16x32_bf16 v[28:31], v[72:75], v[72:75], v[28:31]\n"
"buffer_load_dwordx4 v[100:103], v1, s[4:7], s52 offen offset:1024 sc1\n"
"ds_read_b128 v[68:71], v88 offset:20480\n"
"v_mfma_f32_16x16x32_bf16 v[32:35], v[20:23], v[72:75], v[32:35]\n"
"s_waitcnt lgkmcnt(3)\n"
"v_mfma_f32_16x16x32_bf16 v[28:31], v[76:79], v[76:79], v[28:31]\n"
"ds_read_b128 v[72:75], v88 offset:21504\n"
"v_mfma_f32_16x16x32_bf16 v[32:35], v[20:23], v[76:79], v[32:35]\n"
"buffer_load_dwordx4 v[104:107], v1, s[4:7], s52 offen offset:2048 sc1\n"
"s_waitcnt lgkmcnt(3)\n"
"v_mfma_f32_16x16x32_bf16 v[28:31], v[80:83], v[80:83], v[28:31]\n"
"ds_read_b128 v[76:79], v88 offset:22528\n"
"v_mfma_f32_16x16x32_bf16 v[32:35], v[20:23], v[80:83], v[32:35]\n"
"s_waitcnt lgkmcnt(3)\n"
"v_mfma_f32_16x16x32_bf16 v[28:31], v[84:87], v[84:87], v[28:31]\n"
"ds_read_b128 v[80:83], v88 offset:23552\n"
"buffer_load_dwordx4 v[108:111], v1, s[4:7], s52 offen offset:3072 sc1\n"
"v_mfma_f32_16x16x32_bf16 v[32:35], v[20:23], v[84:87], v[32:35]\n"
"s_waitcnt lgkmcnt(3)\n"
"v_mfma_f32_16x16x32_bf16 v[36:39], v[68:71], v[68:71], 0\n"
"ds_read_b128 v[84:87], v88 offset:24576\n"
"v_mfma_f32_16x16x32_bf16 v[40:43], v[20:23], v[68:71], 0\n"
"s_waitcnt lgkmcnt(3)\n"
"v_mfma_f32_16x16x32_bf16 v[36:39], v[72:75], v[72:75], v[36:39]\n"
"s_add_u32 s52, s52, 4096\n"
"buffer_load_dwordx4 v[112:115], v1, s[4:7], s52 offen offset:0 sc1\n"
"v_add_u32 v88, 0x12400, v88\n"
"ds_read_b128 v[68:71], v88 offset:0\n"
"v_mfma_f32_16x16x32_bf16 v[40:43], v[20:23], v[72:75], v[40:43]\n"
"ds_write_b128 v92, v[28:31] offset:0\n"
"buffer_load_dwordx4 v[116:119], v1, s[4:7], s52 offen offset:1024 sc1\n"
"s_waitcnt lgkmcnt(4)\n"
"v_mfma_f32_16x16x32_bf16 v[36:39], v[76:79], v[76:79], v[36:39]\n"
"ds_read_b128 v[72:75], v88 offset:1024\n"
"v_mfma_f32_16x16x32_bf16 v[40:43], v[20:23], v[76:79], v[40:43]\n"
"ds_write_b32 v91, v32 offset:0\n"
"s_waitcnt lgkmcnt(5)\n"
"v_mfma_f32_16x16x32_bf16 v[36:39], v[80:83], v[80:83], v[36:39]\n"
"buffer_load_dwordx4 v[120:123], v1, s[4:7], s52 offen offset:2048 sc1\n"
"ds_read_b128 v[76:79], v88 offset:2048\n"
"v_mfma_f32_16x16x32_bf16 v[40:43], v[20:23], v[80:83], v[40:43]\n"
"s_waitcnt lgkmcnt(5)\n"
"v_mfma_f32_16x16x32_bf16 v[36:39], v[84:87], v[84:87], v[36:39]\n"
"ds_read_b128 v[80:83], v88 offset:3072\n"
"v_mfma_f32_16x16x32_bf16 v[40:43], v[20:23], v[84:87], v[40:43]\n"
"buffer_load_dwordx4 v[124:127], v1, s[4:7], s52 offen offset:3072 sc1\n"
"s_waitcnt lgkmcnt(5)\n"
"v_mfma_f32_16x16x32_bf16 v[44:47], v[68:71], v[68:71], 0\n"
"ds_read_b128 v[84:87], v88 offset:4096\n"
"v_mfma_f32_16x16x32_bf16 v[48:51], v[20:23], v[68:71], 0\n"
"s_waitcnt lgkmcnt(4)\n"
"v_mfma_f32_16x16x32_bf16 v[44:47], v[72:75], v[72:75], v[44:47]\n"
"ds_read_b128 v[68:71], v88 offset:20480\n"
"s_add_u32 s52, s52, 4096\n"
"buffer_load_dwordx4 v[128:131], v1, s[4:7], s52 offen offset:0 sc1\n"
"v_mfma_f32_16x16x32_bf16 v[48:51], v[20:23], v[72:75], v[48:51]\n"
"ds_write_b128 v92, v[36:39] offset:256\n"
"s_waitcnt lgkmcnt(4)\n"
"v_mfma_f32_16x16x32_bf16 v[44:47], v[76:79], v[76:79], v[44:47]\n"
"ds_read_b128 v[72:75], v88 offset:21504\n"
"buffer_load_dwordx4 v[132:135], v1, s[4:7], s52 offen offset:1024 sc1\n"
"v_mfma_f32_16x16x32_bf16 v[48:51], v[20:23], v[76:79], v[48:51]\n"
"ds_write_b32 v91, v40 offset:256\n"
"s_waitcnt lgkmcnt(5)\n"
"v_mfma_f32_16x16x32_bf16 v[44:47], v[80:83], v[80:83], v[44:47]\n"
"ds_read_b128 v[76:79], v88 offset:22528\n"
"v_mfma_f32_16x16x32_bf16 v[48:51], v[20:23], v[80:83], v[48:51]\n"
"buffer_load_dwordx4 v[136:139], v1, s[4:7], s52 offen offset:2048 sc1\n"
"s_waitcnt lgkmcnt(5)\n"
"v_mfma_f32_16x16x32_bf16 v[44:47], v[84:87], v[84:87], v[44:47]\n"
"ds_read_b128 v[80:83], v88 offset:23552\n"
"v_mfma_f32_16x16x32_bf16 v[48:51], v[20:23], v[84:87], v[48:51]\n"
"s_waitcnt lgkmcnt(5)\n"
"v_mfma_f32_16x16x32_bf16 v[52:55], v[68:71], v[68:71], 0\n"
"ds_read_b128 v[84:87], v88 offset:24576\n"
"buffer_load_dwordx4 v[140:143], v1, s[4:7], s52 offen offset:3072 sc1\n"
"v_mfma_f32_16x16x32_bf16 v[56:59], v[20:23], v[68:71], 0\n"
"s_waitcnt lgkmcnt(4)\n"
"v_mfma_f32_16x16x32_bf16 v[52:55], v[72:75], v[72:75], v[52:55]\n"
"v_add_u32 v88, 0xfffe3c00, v88\n"
"ds_read_b128 v[68:71], v88 offset:0\n"
"v_mfma_f32_16x16x32_bf16 v[56:59], v[20:23], v[72:75], v[56:59]\n"
"s_add_u32 s52, s52, 4096\n"
"buffer_load_dwordx4 v[144:147], v1, s[4:7], s52 offen offset:0 sc1\n"
"ds_write_b128 v92, v[44:47] offset:512\n"
"s_waitcnt lgkmcnt(4)\n"
"v_mfma_f32_16x16x32_bf16 v[52:55], v[76:79], v[76:79], v[52:55]\n"
"ds_read_b128 v[72:75], v88 offset:1024\n"
"v_mfma_f32_16x16x32_bf16 v[56:59], v[20:23], v[76:79], v[56:59]\n"
"buffer_load_dwordx4 v[148:151], v1, s[4:7], s52 offen offset:1024 sc1\n"
"ds_write_b32 v91, v48 offset:512\n"
"ds_read_b32 v90, v14 offset:0\n"
"s_waitcnt lgkmcnt(6)\n"
"v_mfma_f32_16x16x32_bf16 v[52:55], v[80:83], v[80:83], v[52:55]\n"
"ds_read_b128 v[76:79], v88 offset:2048\n"
"v_mfma_f32_16x16x32_bf16 v[56:59], v[20:23], v[80:83], v[56:59]\n"
"buffer_load_dwordx4 v[152:155], v1, s[4:7], s52 offen offset:2048 sc1\n"
"s_waitcnt lgkmcnt(6)\n"
"v_mfma_f32_16x16x32_bf16 v[52:55], v[84:87], v[84:87], v[52:55]\n"
"ds_read_b128 v[80:83], v88 offset:3072\n"
"v_mfma_f32_16x16x32_bf16 v[56:59], v[20:23], v[84:87], v[56:59]\n"
"s_waitcnt lgkmcnt(6)\n"
"v_mfma_f32_16x16x32_bf16 v[60:63], v[68:71], v[68:71], 0\n"
"ds_read_b128 v[84:87], v88 offset:4096\n"
"buffer_load_dwordx4 v[156:159], v1, s[4:7], s52 offen offset:3072 sc1\n"
"v_mfma_f32_16x16x32_bf16 v[64:67], v[20:23], v[68:71], 0\n"
"s_waitcnt lgkmcnt(3)\n"
"v_cmp_eq_u32 vcc, 0, v90\n"
"v_mfma_f32_16x16x32_bf16 v[60:63], v[72:75], v[72:75], v[60:63]\n"
"v_mfma_f32_16x16x32_bf16 v[64:67], v[20:23], v[72:75], v[64:67]\n"
"ds_write_b128 v92, v[52:55] offset:768\n"
"s_cbranch_vccnz .Lsl_7\n"
".Lbk_8:\n"
"s_waitcnt lgkmcnt(3)\n"
"v_mfma_f32_16x16x32_bf16 v[60:63], v[76:79], v[76:79], v[60:63]\n"
"v_add_u32 v3, 0x5000, v3\n"
"v_mfma_f32_16x16x32_bf16 v[64:67], v[20:23], v[76:79], v[64:67]\n"
"ds_read_b128 v[68:71], v3 offset:20480\n"
"ds_write_b32 v91, v56 offset:768\n"
"ds_read_b128 v[72:75], v3 offset:40960\n"
"s_waitcnt lgkmcnt(5)\n"
"v_mfma_f32_16x16x32_bf16 v[60:63], v[80:83], v[80:83], v[60:63]\n"
"v_add_u32 v95, 0x17400, v3\n"
"v_mfma_f32_16x16x32_bf16 v[64:67], v[20:23], v[80:83], v[64:67]\n"
"ds_read_b128 v[76:79], v95 offset:0\n"
"s_waitcnt lgkmcnt(5)\n"
"v_mfma_f32_16x16x32_bf16 v[60:63], v[84:87], v[84:87], v[60:63]\n"
"ds_read_b128 v[80:83], v95 offset:20480\n"
"v_mfma_f32_16x16x32_bf16 v[64:67], v[20:23], v[84:87], v[64:67]\n"
"s_nop 4\n"
"ds_write_b128 v92, v[60:63] offset:1024\n"
"s_nop 1\n"
"ds_write_b32 v91, v64 offset:1024\n"
"s_branch .Ljn_12\n"
".Lwt_11:\n"
"s_waitcnt lgkmcnt(0)\n"
"v_mfma_f32_16x16x32_bf16 v[28:31], v[68:71], v[68:71], 0\n"
"ds_read_b128 v[84:87], v88 offset:4096\n"
"v_mfma_f32_16x16x32_bf16 v[32:35], v[20:23], v[68:71], 0\n"
"v_mfma_f32_16x16x32_bf16 v[28:31], v[72:75], v[72:75], v[28:31]\n"
"ds_read_b128 v[68:71], v88 offset:20480\n"
"v_mfma_f32_16x16x32_bf16 v[32:35], v[20:23], v[72:75], v[32:35]\n"
"v_mfma_f32_16x16x32_bf16 v[28:31], v[76:79], v[76:79], v[28:31]\n"
"ds_read_b128 v[72:75], v88 offset:21504\n"
"v_mfma_f32_16x16x32_bf16 v[32:35], v[20:23], v[76:79], v[32:35]\n"
"v_mfma_f32_16x16x32_bf16 v[28:31], v[80:83], v[80:83], v[28:31]\n"
"ds_read_b128 v[76:79], v88 offset:22528\n"
"v_mfma_f32_16x16x32_bf16 v[32:35], v[20:23], v[80:83], v[32:35]\n"
"s_waitcnt lgkmcnt(3)\n"
"v_mfma_f32_16x16x32_bf16 v[28:31], v[84:87], v[84:87], v[28:31]\n"
"ds_read_b128 v[80:83], v88 offset:23552\n"
"v_mfma_f32_16x16x32_bf16 v[32:35], v[20:23], v[84:87], v[32:35]\n"
"s_waitcnt lgkmcnt(3)\n"
"v_mfma_f32_16x16x32_bf16 v[36:39], v[68:71], v[68:71], 0\n"
"ds_read_b128 v[84:87], v88 offset:24576\n"
"v_mfma_f32_16x16x32_bf16 v[40:43], v[20:23], v[68:71], 0\n"
"s_waitcnt lgkmcnt(3)\n"
"v_mfma_f32_16x16x32_bf16 v[36:39], v[72:75], v[72:75], v[36:39]\n"
"v_add_u32 v88, 0x12400, v88\n"
"ds_read_b128 v[68:71], v88 offset:0\n"
"v_mfma_f32_16x16x32_bf16 v[40:43], v[20:23], v[72:75], v[40:43]\n"
"ds_write_b128 v92, v[28:31] offset:0\n"
"s_waitcnt lgkmcnt(4)\n"
"v_mfma_f32_16x16x32_bf16 v[36:39], v[76:79], v[76:79], v[36:39]\n"
"ds_read_b128 v[72:75], v88 offset:1024\n"
"v_mfma_f32_16x16x32_bf16 v[40:43], v[20:23], v[76:79], v[40:43]\n"
"ds_write_b32 v91, v32 offset:0\n"
"s_waitcnt lgkmcnt(5)\n"
"v_mfma_f32_16x16x32_bf16 v[36:39], v[80:83], v[80:83], v[36:39]\n"
"ds_read_b128 v[76:79], v88 offset:2048\n"
"v_mfma_f32_16x16x32_bf16 v[40:43], v[20:23], v[80:83], v[40:43]\n"
"s_waitcnt lgkmcnt(5)\n"
"v_mfma_f32_16x16x32_bf16 v[36:39], v[84:87], v[84:87], v[36:39]\n"
"ds_read_b128 v[80:83], v88 offset:3072\n"
"v_mfma_f32_16x16x32_bf16 v[40:43], v[20:23], v[84:87], v[40:43]\n"
"s_waitcnt lgkmcnt(5)\n"
"v_mfma_f32_16x16x32_bf16 v[44:47], v[68:71], v[68:71], 0\n"
"ds_read_b128 v[84:87], v88 offset:4096\n"
"v_mfma_f32_16x16x32_bf16 v[48:51], v[20:23], v[68:71], 0\n"
"s_waitcnt lgkmcnt(4)\n"
"v_mfma_f32_16x16x32_bf16 v[44:47], v[72:75], v[72:75], v[44:47]\n"
"ds_read_b128 v[68:71], v88 offset:20480\n"
"v_mfma_f32_16x16x32_bf16 v[48:51], v[20:23], v[72:75], v[48:51]\n"
"ds_write_b128 v92, v[36:39] offset:256\n"
"s_waitcnt lgkmcnt(4)\n"
"v_mfma_f32_16x16x32_bf16 v[44:47], v[76:79], v[76:79], v[44:47]\n"
"ds_read_b128 v[72:75], v88 offset:21504\n"
"v_mfma_f32_16x16x32_bf16 v[48:51], v[20:23], v[76:79], v[48:51]\n"
"ds_write_b32 v91, v40 offset:256\n"
"s_waitcnt lgkmcnt(5)\n"
"v_mfma_f32_16x16x32_bf16 v[44:47], v[80:83], v[80:83], v[44:47]\n"
"ds_read_b128 v[76:79], v88 offset:22528\n"
"v_mfma_f32_16x16x32_bf16 v[48:51], v[20:23], v[80:83], v[48:51]\n"
"s_waitcnt lgkmcnt(5)\n"
"v_mfma_f32_16x16x32_bf16 v[44:47], v[84:87], v[84:87], v[44:47]\n"
"ds_read_b128 v[80:83], v88 offset:23552\n"
"v_mfma_f32_16x16x32_bf16 v[48:51], v[20:23], v[84:87], v[48:51]\n"
"s_waitcnt lgkmcnt(5)\n"
"v_mfma_f32_16x16x32_bf16 v[52:55], v[68:71], v[68:71], 0\n"
"ds_read_b128 v[84:87], v88 offset:24576\n"
"v_mfma_f32_16x16x32_bf16 v[56:59], v[20:23], v[68:71], 0\n"
"s_waitcnt lgkmcnt(4)\n"
"v_mfma_f32_16x16x32_bf16 v[52:55], v[72:75], v[72:75], v[52:55]\n"
"v_add_u32 v88, 0xfffe3c00, v88\n"
"ds_read_b128 v[68:71], v88 offset:0\n"
"v_mfma_f32_16x16x32_bf16 v[56:59], v[20:23], v[72:75], v[56:59]\n"
"ds_write_b128 v92, v[44:47] offset:512\n"
"s_waitcnt lgkmcnt(4)\n"
"v_mfma_f32_16x16x32_bf16 v[52:55], v[76:79], v[76:79], v[52:55]\n"
"ds_read_b128 v[72:75], v88 offset:1024\n"
"v_mfma_f32_16x16x32_bf16 v[56:59], v[20:23], v[76:79], v[56:59]\n"
"ds_write_b32 v91, v48 offset:512\n"
"ds_read_b32 v90, v14 offset:0\n"
"s_waitcnt lgkmcnt(6)\n"
"v_mfma_f32_16x16x32_bf16 v[52:55], v[80:83], v[80:83], v[52:55]\n"
"ds_read_b128 v[76:79], v88 offset:2048\n"
"v_mfma_f32_16x16x32_bf16 v[56:59], v[20:23], v[80:83], v[56:59]\n"
"s_waitcnt lgkmcnt(6)\n"
"v_mfma_f32_16x16x32_bf16 v[52:55], v[84:87], v[84:87], v[52:55]\n"
"ds_read_b128 v[80:83], v88 offset:3072\n"
"v_mfma_f32_16x16x32_bf16 v[56:59], v[20:23], v[84:87], v[56:59]\n"
"s_waitcnt lgkmcnt(6)\n"
"v_mfma_f32_16x16x32_bf16 v[60:63], v[68:71], v[68:71], 0\n"
"ds_read_b128 v[84:87], v88 offset:4096\n"
"v_mfma_f32_16x16x32_bf16 v[64:67], v[20:23], v[68:71], 0\n"
"s_waitcnt lgkmcnt(3)\n"
"v_cmp_eq_u32 vcc, 0, v90\n"
"v_mfma_f32_16x16x32_bf16 v[60:63], v[72:75], v[72:75], v[60:63]\n"
"v_mfma_f32_16x16x32_bf16 v[64:67], v[20:23], v[72:75], v[64:67]\n"
"ds_write_b128 v92, v[52:55] offset:768\n"
"s_waitcnt lgkmcnt(3)\n"
"v_mfma_f32_16x16x32_bf16 v[60:63], v[76:79], v[76:79], v[60:63]\n"
"v_mfma_f32_16x16x32_bf16 v[64:67], v[20:23], v[76:79], v[64:67]\n"
"ds_write_b32 v91, v56 offset:768\n"
"s_waitcnt lgkmcnt(3)\n"
"v_mfma_f32_16x16x32_bf16 v[60:63], v[80:83], v[80:83], v[60:63]\n"
"v_mfma_f32_16x16x32_bf16 v[64:67], v[20:23], v[80:83], v[64:67]\n"
"s_waitcnt lgkmcnt(2)\n"
"v_mfma_f32_16x16x32_bf16 v[60:63], v[84:87], v[84:87], v[60:63]\n"
"v_mfma_f32_16x16x32_bf16 v[64:67], v[20:23], v[84:87], v[64:67]\n"
"buffer_load_dwordx4 v[96:99], v19, s[4:7], s50 offen sc1\n"
"s_mov_b32 s17, 0x100000\n"
".Ltp_13:\n"
"ds_read_b32 v90, v14 offset:0\n"
"s_sub_u32 s17, s17, 1\n"
"s_cbranch_scc1 .Lou_14\n"
"s_waitcnt lgkmcnt(0)\n"
"v_cmp_eq_u32 vcc, 0, v90\n"
"s_cbranch_vccnz .Ltp_13\n"
".Lou_14:\n"
"s_waitcnt lgkmcnt(0)\n"
"v_add_u32 v3, 0x5000, v3\n"
"ds_read_b128 v[68:71], v3 offset:20480\n"
"ds_read_b128 v[72:75], v3 offset:40960\n"
"v_add_u32 v95, 0x17400, v3\n"
"ds_read_b128 v[76:79], v95 offset:0\n"
"ds_read_b128 v[80:83], v95 offset:20480\n"
"ds_write_b128 v92, v[60:63] offset:1024\n"
"ds_write_b32 v91, v64 offset:1024\n"
"s_waitcnt vmcnt(0) lgkmcnt(2)\n"
"s_mov_b32 s17, 32768\n"
"s_branch .Lmd_17\n"
".Ltp_15:\n"
"buffer_load_dwordx4 v[96:99], v19, s[4:7], s50 offen sc1\n"
".Lmd_17:\n"
"s_sub_u32 s17, s17, 1\n"
"s_cbranch_scc1 .Lou_16\n"
"s_waitcnt vmcnt(0)\n"
"v_xor_b32 v96, s16, v96\n"
"v_xor_b32 v97, s16, v97\n"
"v_xor_b32 v98, s16, v98\n"
"v_xor_b32 v99, s16, v99\n"
"v_or_b32 v96, v96, v97\n"
"v_or3_b32 v96, v96, v98, v99\n"
"v_cmp_ne_u32 vcc, 0, v96\n"
"s_cbranch_vccz .Lou_16\n"
"s_sleep 8\n"
"s_branch .Ltp_15\n"
".Lou_16:\n"
"s_waitcnt vmcnt(0)\n"
"buffer_load_dwordx4 v[96:99], v1, s[4:7], s52 offen offset:0 sc1\n"
"buffer_load_dwordx4 v[100:103], v1, s[4:7], s52 offen offset:1024 sc1\n"
"buffer_load_dwordx4 v[104:107], v1, s[4:7], s52 offen offset:2048 sc1\n"
"buffer_load_dwordx4 v[108:111], v1, s[4:7], s52 offen offset:3072 sc1\n"
"s_add_u32 s52, s52, 4096\n"
"buffer_load_dwordx4 v[112:115], v1, s[4:7], s52 offen offset:0 sc1\n"
"buffer_load_dwordx4 v[116:119], v1, s[4:7], s52 offen offset:1024 sc1\n"
"buffer_load_dwordx4 v[120:123], v1, s[4:7], s52 offen offset:2048 sc1\n"
"buffer_load_dwordx4 v[124:127], v1, s[4:7], s52 offen offset:3072 sc1\n"
"s_add_u32 s52, s52, 4096\n"
"buffer_load_dwordx4 v[128:131], v1, s[4:7], s52 offen offset:0 sc1\n"
"buffer_load_dwordx4 v[132:135], v1, s[4:7], s52 offen offset:1024 sc1\n"
"buffer_load_dwordx4 v[136:139], v1, s[4:7], s52 offen offset:2048 sc1\n"
"buffer_load_dwordx4 v[140:143], v1, s[4:7], s52 offen offset:3072 sc1\n"
"s_add_u32 s52, s52, 4096\n"
"buffer_load_dwordx4 v[144:147], v1, s[4:7], s52 offen offset:0 sc1\n"
"buffer_load_dwordx4 v[148:151], v1, s[4:7], s52 offen offset:1024 sc1\n"
"buffer_load_dwordx4 v[152:155], v1, s[4:7], s52 offen offset:2048 sc1\n"
"buffer_load_dwordx4 v[156:159], v1, s[4:7], s52 offen offset:3072 sc1\n"
".Ljn_12:\n"
"s_waitcnt lgkmcnt(2)\n"
"ds_write_b32 v13, v13 offset:16\n"
"s_waitcnt vmcnt(14)\n"
"v_mfma_f32_16x16x32_bf16 v[28:31], v[96:99], v[68:71], 0\n"
"ds_read_b128 v[84:87], v3 offset:21504\n"
"s_add_u32 s52, s52, 4096\n"
"v_mfma_f32_16x16x32_bf16 v[32:35], v[100:103], v[68:71], 0\n"
"v_mfma_f32_16x16x32_bf16 v[36:39], v[96:99], v[72:75], 0\n"
"ds_read_b128 v[68:71], v3 offset:41984\n"
"buffer_load_dwordx4 v[160:163], v1, s[4:7], s52 offen offset:0 sc1\n"
"v_mfma_f32_16x16x32_bf16 v[40:43], v[100:103], v[72:75], 0\n"
"v_mfma_f32_16x16x32_bf16 v[44:47], v[96:99], v[76:79], 0\n"
"ds_read_b128 v[72:75], v95 offset:1024\n"
"v_mfma_f32_16x16x32_bf16 v[48:51], v[100:103], v[76:79], 0\n"
"buffer_load_dwordx4 v[164:167], v1, s[4:7], s52 offen offset:1024 sc1\n"
"v_mfma_f32_16x16x32_bf16 v[52:55], v[96:99], v[80:83], 0\n"
"ds_read_b128 v[76:79], v95 offset:21504\n"
"v_mfma_f32_16x16x32_bf16 v[56:59], v[100:103], v[80:83], 0\n"
"s_waitcnt vmcnt(14) lgkmcnt(2)\n"
"v_mfma_f32_16x16x32_bf16 v[28:31], v[104:107], v[84:87], v[28:31]\n"
"ds_read_b128 v[80:83], v3 offset:22528\n"
"buffer_load_dwordx4 v[168:171], v1, s[4:7], s52 offen offset:2048 sc1\n"
"v_mfma_f32_16x16x32_bf16 v[32:35], v[108:111], v[84:87], v[32:35]\n"
"v_mfma_f32_16x16x32_bf16 v[36:39], v[104:107], v[68:71], v[36:39]\n"
"ds_read_b128 v[84:87], v3 offset:43008\n"
"v_mfma_f32_16x16x32_bf16 v[40:43], v[108:111], v[68:71], v[40:43]\n"
"buffer_load_dwordx4 v[172:175], v1, s[4:7], s52 offen offset:3072 sc1\n"
"v_mov_b32 v20, -1\n"
"s_waitcnt lgkmcnt(2)\n"
"v_mfma_f32_16x16x32_bf16 v[44:47], v[104:107], v[72:75], v[44:47]\n"
"ds_read_b128 v[68:71], v95 offset:2048\n"
"s_add_u32 s52, s52, 4096\n"
"v_mov_b32 v21, -1\n"
"v_mfma_f32_16x16x32_bf16 v[48:51], v[108:111], v[72:75], v[48:51]\n"
"v_mov_b32 v22, -1\n"
"v_mov_b32 v23, -1\n"
"v_cndmask_b32 v91, 2.0, v1, s[40:41]\n"
"v_mfma_f32_16x16x32_bf16 v[52:55], v[104:107], v[76:79], v[52:55]\n"
"ds_read_b128 v[72:75], v95 offset:22528\n"
"buffer_load_dwordx4 v[176:179], v1, s[4:7], s52 offen offset:0 sc1\n"
"s_mov_b32 s33, s30\n"
"v_mfma_f32_16x16x32_bf16 v[56:59], v[108:111], v[76:79], v[56:59]\n"
"buffer_store_dwordx4 v[20:23], v1, s[4:7], s33 offen offset:0 sc1\n"
"s_waitcnt vmcnt(16) lgkmcnt(2)\n"
"v_mfma_f32_16x16x32_bf16 v[28:31], v[112:115], v[80:83], v[28:31]\n"
"ds_read_b128 v[76:79], v3 offset:23552\n"
"v_mfma_f32_16x16x32_bf16 v[32:35], v[116:119], v[80:83], v[32:35]\n"
"buffer_load_dwordx4 v[180:183], v1, s[4:7], s52 offen offset:1024 sc1\n"
"v_mfma_f32_16x16x32_bf16 v[36:39], v[112:115], v[84:87], v[36:39]\n"
"ds_read_b128 v[80:83], v3 offset:44032\n"
"v_mfma_f32_16x16x32_bf16 v[40:43], v[116:119], v[84:87], v[40:43]\n"
"s_waitcnt lgkmcnt(2)\n"
"v_mfma_f32_16x16x32_bf16 v[44:47], v[112:115], v[68:71], v[44:47]\n"
"ds_read_b128 v[84:87], v95 offset:3072\n"
"buffer_load_dwordx4 v[184:187], v1, s[4:7], s52 offen offset:2048 sc1\n"
"buffer_store_dwordx4 v[20:23], v91, s[4:7], s33 offen offset:1024 sc1\n"
"v_mfma_f32_16x16x32_bf16 v[48:51], v[116:119], v[68:71], v[48:51]\n"
"s_add_u32 s33, s30, 0x140000\n"
"ds_read_b32 v90, v14 offset:16\n"
"v_mfma_f32_16x16x32_bf16 v[52:55], v[112:115], v[72:75], v[52:55]\n"
"ds_read_b128 v[68:71], v95 offset:23552\n"
"v_mfma_f32_16x16x32_bf16 v[56:59], v[116:119], v[72:75], v[56:59]\n"
"buffer_load_dwordx4 v[188:191], v1, s[4:7], s52 offen offset:3072 sc1\n"
"s_waitcnt vmcnt(18) lgkmcnt(3)\n"
"v_mfma_f32_16x16x32_bf16 v[28:31], v[120:123], v[76:79], v[28:31]\n"
"ds_read_b128 v[72:75], v3 offset:24576\n"
"s_add_u32 s52, s52, 4096\n"
"v_mfma_f32_16x16x32_bf16 v[32:35], v[124:127], v[76:79], v[32:35]\n"
"buffer_store_dwordx4 v[20:23], v1, s[4:7], s33 offen offset:0 sc1\n"
"v_mfma_f32_16x16x32_bf16 v[36:39], v[120:123], v[80:83], v[36:39]\n"
"ds_read_b128 v[76:79], v3 offset:45056\n"
"buffer_load_dwordx4 v[192:195], v1, s[4:7], s52 offen offset:0 sc1\n"
"v_mfma_f32_16x16x32_bf16 v[40:43], v[124:127], v[80:83], v[40:43]\n"
"s_waitcnt lgkmcnt(2)\n"
"v_mfma_f32_16x16x32_bf16 v[44:47], v[120:123], v[84:87], v[44:47]\n"
"ds_read_b128 v[80:83], v95 offset:4096\n"
"v_mfma_f32_16x16x32_bf16 v[48:51], v[124:127], v[84:87], v[48:51]\n"
"buffer_load_dwordx4 v[196:199], v1, s[4:7], s52 offen offset:1024 sc1\n"
"v_mfma_f32_16x16x32_bf16 v[52:55], v[120:123], v[68:71], v[52:55]\n"
"ds_read_b128 v[84:87], v95 offset:24576\n"
"buffer_store_dwordx4 v[20:23], v91, s[4:7], s33 offen offset:1024 sc1\n"
"v_mfma_f32_16x16x32_bf16 v[56:59], v[124:127], v[68:71], v[56:59]\n"
"s_lshl_b32 s79, s2, 2\n"
"s_add_u32 s79, s79, s3\n"
"v_cmp_eq_u32 vcc, 0, v90\n"
"s_waitcnt vmcnt(20) lgkmcnt(2)\n"
"v_mfma_f32_16x16x32_bf16 v[28:31], v[128:131], v[72:75], v[28:31]\n"
"ds_read_b128 v[68:71], v3 offset:25600\n"
"buffer_load_dwordx4 v[200:203], v1, s[4:7], s52 offen offset:2048 sc1\n"
"s_lshl_b32 s79, s79, 10\n"
"v_mfma_f32_16x16x32_bf16 v[32:35], v[132:135], v[72:75], v[32:35]\n"
"s_add_u32 s33, s79, 0xbc8000\n"
"s_sub_u32 s79, 0xc27fff, s33\n"
"s_and_b32 s79, s79, 0x80000000\n"
"s_cbranch_vccnz .Lsl_26\n"
".Lbk_27:\n"
"v_mfma_f32_16x16x32_bf16 v[36:39], v[128:131], v[76:79], v[36:39]\n"
"ds_read_b128 v[72:75], v3 offset:46080\n"
"s_or_b32 s33, s33, s79\n"
"v_mfma_f32_16x16x32_bf16 v[40:43], v[132:135], v[76:79], v[40:43]\n"
"buffer_load_dwordx4 v[204:207], v1, s[4:7], s52 offen offset:3072 sc1\n"
"buffer_store_dwordx4 v[20:23], v1, s[4:7], s33 offen sc1\n"
"ds_read2st64_b32 v[60:61], v17 offset0:0 offset1:8\n"
"s_waitcnt lgkmcnt(3)\n"
"v_mfma_f32_16x16x32_bf16 v[44:47], v[128:131], v[80:83], v[44:47]\n"
"ds_read_b128 v[76:79], v95 offset:5120\n"
"s_add_u32 s52, s52, 4096\n"
"v_mfma_f32_16x16x32_bf16 v[48:51], v[132:135], v[80:83], v[48:51]\n"
"ds_read2st64_b32 v[62:63], v17 offset0:16 offset1:24\n"
"v_mfma_f32_16x16x32_bf16 v[52:55], v[128:131], v[84:87], v[52:55]\n"
"ds_read_b128 v[80:83], v95 offset:25600\n"
"buffer_load_dwordx4 v[208:211], v1, s[4:7], s52 offen offset:0 sc1\n"
"v_mfma_f32_16x16x32_bf16 v[56:59], v[132:135], v[84:87], v[56:59]\n"
"ds_read2st64_b32 v[64:65], v18 offset0:0 offset1:8\n"
"s_waitcnt vmcnt(22) lgkmcnt(5)\n"
"v_mfma_f32_16x16x32_bf16 v[28:31], v[136:139], v[68:71], v[28:31]\n"
"ds_read_b128 v[84:87], v3 offset:26624\n"
"v_mfma_f32_16x16x32_bf16 v[32:35], v[140:143], v[68:71], v[32:35]\n"
"buffer_load_dwordx4 v[212:215], v1, s[4:7], s52 offen offset:1024 sc1\n"
"ds_read2st64_b32 v[66:67], v18 offset0:16 offset1:24\n"
"v_mfma_f32_16x16x32_bf16 v[36:39], v[136:139], v[72:75], v[36:39]\n"
"ds_read_b128 v[68:71], v3 offset:47104\n"
"v_mfma_f32_16x16x32_bf16 v[40:43], v[140:143], v[72:75], v[40:43]\n"
"s_waitcnt lgkmcnt(4)\n"
"v_mfma_f32_16x16x32_bf16 v[44:47], v[136:139], v[76:79], v[44:47]\n"
"ds_read_b128 v[72:75], v95 offset:6144\n"
"buffer_load_dwordx4 v[216:219], v1, s[4:7], s52 offen offset:2048 sc1\n"
"v_mfma_f32_16x16x32_bf16 v[48:51], v[140:143], v[76:79], v[48:51]\n"
"v_mfma_f32_16x16x32_bf16 v[52:55], v[136:139], v[80:83], v[52:55]\n"
"ds_read_b128 v[76:79], v95 offset:26624\n"
"v_mfma_f32_16x16x32_bf16 v[56:59], v[140:143], v[80:83], v[56:59]\n"
"buffer_load_dwordx4 v[220:223], v1, s[4:7], s52 offen offset:3072 sc1\n"
"s_waitcnt vmcnt(23) lgkmcnt(2)\n"
"v_mfma_f32_16x16x32_bf16 v[28:31], v[144:147], v[84:87], v[28:31]\n"
"ds_read_b128 v[80:83], v3 offset:27648\n"
"s_add_u32 s52, s52, 4096\n"
"v_mfma_f32_16x16x32_bf16 v[32:35], v[148:151], v[84:87], v[32:35]\n"
"v_mfma_f32_16x16x32_bf16 v[36:39], v[144:147], v[68:71], v[36:39]\n"
"ds_read_b128 v[84:87], v3 offset:48128\n"
"buffer_load_dwordx4 v[224:227], v1, s[4:7], s52 offen offset:0 sc1\n"
"v_mfma_f32_16x16x32_bf16 v[40:43], v[148:151], v[68:71], v[40:43]\n"
"v_add_f32 v60, v60, v61\n"
"s_waitcnt lgkmcnt(2)\n"
"v_mfma_f32_16x16x32_bf16 v[44:47], v[144:147], v[72:75], v[44:47]\n"
"ds_read_b128 v[68:71], v95 offset:7168\n"
"v_add_f32 v62, v62, v63\n"
"v_mfma_f32_16x16x32_bf16 v[48:51], v[148:151], v[72:75], v[48:51]\n"
"buffer_load_dwordx4 v[228:231], v1, s[4:7], s52 offen offset:1024 sc1\n"
"v_add_f32 v64, v64, v65\n"
"v_mfma_f32_16x16x32_bf16 v[52:55], v[144:147], v[76:79], v[52:55]\n"
"ds_read_b128 v[72:75], v95 offset:27648\n"
"v_add_f32 v66, v66, v67\n"
"v_mfma_f32_16x16x32_bf16 v[56:59], v[148:151], v[76:79], v[56:59]\n"
"v_add_f32 v64, v64, v66\n"
"s_waitcnt vmcnt(23) lgkmcnt(2)\n"
"v_mfma_f32_16x16x32_bf16 v[28:31], v[152:155], v[80:83], v[28:31]\n"
"ds_read_b128 v[76:79], v3 offset:28672\n"
"buffer_load_dwordx4 v[232:235], v1, s[4:7], s53 offen offset:0 sc1\n"
"v_mfma_f32_16x16x32_bf16 v[32:35], v[156:159], v[80:83], v[32:35]\n"
"v_add_f32 v60, v60, v62\n"
"v_mul_f32 v63, 0x3acccccd, v64\n"
"v_mfma_f32_16x16x32_bf16 v[36:39], v[152:155], v[84:87], v[36:39]\n"
"ds_read_b128 v[80:83], v3 offset:49152\n"
"v_fma_f32 v60, -v64, v63, v60\n"
"v_mfma_f32_16x16x32_bf16 v[40:43], v[156:159], v[84:87], v[40:43]\n"
"buffer_load_dwordx4 v[236:239], v1, s[4:7], s53 offen offset:1024 sc1\n"
"s_waitcnt lgkmcnt(2)\n"
"v_mfma_f32_16x16x32_bf16 v[44:47], v[152:155], v[68:71], v[44:47]\n"
"ds_read_b128 v[84:87], v95 offset:8192\n"
"v_fmamk_f32 v60, v60, 0x3acccccd, v27\n"
"v_mfma_f32_16x16x32_bf16 v[48:51], v[156:159], v[68:71], v[48:51]\n"
"v_max_f32 v60, v60, v27\n"
"v_rsq_f32 v62, v60\n"
"v_mfma_f32_16x16x32_bf16 v[52:55], v[152:155], v[72:75], v[52:55]\n"
"ds_read_b128 v[68:71], v95 offset:28672\n"
"buffer_load_dwordx4 v[240:243], v1, s[4:7], s54 offen offset:0 sc1\n"
"v_mfma_f32_16x16x32_bf16 v[56:59], v[156:159], v[72:75], v[56:59]\n"
"v_mul_f32 v63, -v63, v62\n"
"s_waitcnt vmcnt(24) lgkmcnt(2)\n"
"v_mfma_f32_16x16x32_bf16 v[28:31], v[160:163], v[76:79], v[28:31]\n"
"ds_read_b128 v[72:75], v3 offset:29696\n"
"v_mfma_f32_16x16x32_bf16 v[32:35], v[164:167], v[76:79], v[32:35]\n"
"buffer_load_dwordx4 v[244:247], v1, s[4:7], s54 offen offset:1024 sc1\n"
"v_mfma_f32_16x16x32_bf16 v[36:39], v[160:163], v[80:83], v[36:39]\n"
"ds_read_b128 v[76:79], v3 offset:50176\n"
"v_mfma_f32_16x16x32_bf16 v[40:43], v[164:167], v[80:83], v[40:43]\n"
"ds_write_b64 v16, v[62:63] offset:0\n"
"s_waitcnt lgkmcnt(3)\n"
"v_mfma_f32_16x16x32_bf16 v[44:47], v[160:163], v[84:87], v[44:47]\n"
"ds_read_b128 v[80:83], v95 offset:9216\n"
"buffer_load_dwordx4 v[248:251], v1, s[4:7], s55 offen offset:0 sc1\n"
"v_mfma_f32_16x16x32_bf16 v[48:51], v[164:167], v[84:87], v[48:51]\n"
"v_mfma_f32_16x16x32_bf16 v[52:55], v[160:163], v[68:71], v[52:55]\n"
"ds_read_b128 v[84:87], v95 offset:29696\n"
"v_mfma_f32_16x16x32_bf16 v[56:59], v[164:167], v[68:71], v[56:59]\n"
"buffer_load_dwordx4 v[252:255], v1, s[4:7], s55 offen offset:1024 sc1\n"
"ds_read2st64_b32 v[60:61], v17 offset0:4 offset1:12\n"
"s_waitcnt vmcnt(25) lgkmcnt(4)\n"
"v_mfma_f32_16x16x32_bf16 v[28:31], v[168:171], v[72:75], v[28:31]\n"
"ds_read_b128 v[68:71], v3 offset:30720\n"
"s_mov_b32 m0, 163328\n"
"v_mfma_f32_16x16x32_bf16 v[32:35], v[172:175], v[72:75], v[32:35]\n"
"ds_read2st64_b32 v[62:63], v17 offset0:20 offset1:28\n"
"v_mfma_f32_16x16x32_bf16 v[36:39], v[168:171], v[76:79], v[36:39]\n"
"ds_read_b128 v[72:75], v3 offset:51200\n"
"buffer_load_dword v89, s[8:11], 0 offen lds\n"
"v_mfma_f32_16x16x32_bf16 v[40:43], v[172:175], v[76:79], v[40:43]\n"
"ds_read2st64_b32 v[64:65], v18 offset0:4 offset1:12\n"
"s_waitcnt lgkmcnt(5)\n"
"v_mfma_f32_16x16x32_bf16 v[44:47], v[168:171], v[80:83], v[44:47]\n"
"ds_read_b128 v[76:79], v95 offset:10240\n"
"ds_write_b32 v13, v13 offset:32\n"
"v_mfma_f32_16x16x32_bf16 v[48:51], v[172:175], v[80:83], v[48:51]\n"
"ds_read2st64_b32 v[66:67], v18 offset0:20 offset1:28\n"
"v_mfma_f32_16x16x32_bf16 v[52:55], v[168:171], v[84:87], v[52:55]\n"
"ds_read_b128 v[80:83], v95 offset:30720\n"
"v_mfma_f32_16x16x32_bf16 v[56:59], v[172:175], v[84:87], v[56:59]\n"
"s_waitcnt vmcnt(23) lgkmcnt(5)\n"
"v_mfma_f32_16x16x32_bf16 v[28:31], v[176:179], v[68:71], v[28:31]\n"
"ds_read_b128 v[84:87], v3 offset:31744\n"
"v_mfma_f32_16x16x32_bf16 v[32:35], v[180:183], v[68:71], v[32:35]\n"
"v_mfma_f32_16x16x32_bf16 v[36:39], v[176:179], v[72:75], v[36:39]\n"
"ds_read_b128 v[68:71], v3 offset:52224\n"
"v_mfma_f32_16x16x32_bf16 v[40:43], v[180:183], v[72:75], v[40:43]\n"
"s_waitcnt lgkmcnt(2)\n"
"v_mfma_f32_16x16x32_bf16 v[44:47], v[176:179], v[76:79], v[44:47]\n"
"ds_read_b128 v[72:75], v95 offset:11264\n"
"v_mfma_f32_16x16x32_bf16 v[48:51], v[180:183], v[76:79], v[48:51]\n"
"s_waitcnt vmcnt(13)\n"
"ds_write_b32 v13, v13 offset:48\n"
"v_mfma_f32_16x16x32_bf16 v[52:55], v[176:179], v[80:83], v[52:55]\n"
"ds_read_b128 v[76:79], v95 offset:31744\n"
"v_mfma_f32_16x16x32_bf16 v[56:59], v[180:183], v[80:83], v[56:59]\n"
"v_add_f32 v60, v60, v61\n"
"s_waitcnt lgkmcnt(3)\n"
"v_mfma_f32_16x16x32_bf16 v[28:31], v[184:187], v[84:87], v[28:31]\n"
"ds_read_b128 v[80:83], v3 offset:32768\n"
"v_add_f32 v62, v62, v63\n"
"v_mfma_f32_16x16x32_bf16 v[32:35], v[188:191], v[84:87], v[32:35]\n"
"v_add_f32 v64, v64, v65\n"
"v_add_f32 v66, v66, v67\n"
"v_mfma_f32_16x16x32_bf16 v[36:39], v[184:187], v[68:71], v[36:39]\n"
"ds_read_b128 v[84:87], v3 offset:53248\n"
"v_add_f32 v64, v64, v66\n"
"v_mfma_f32_16x16x32_bf16 v[40:43], v[188:191], v[68:71], v[40:43]\n"
"v_add_f32 v60, v60, v62\n"
"s_waitcnt lgkmcnt(2)\n"
"v_mfma_f32_16x16x32_bf16 v[44:47], v[184:187], v[72:75], v[44:47]\n"
"ds_read_b128 v[68:71], v95 offset:12288\n"
"v_mul_f32 v63, 0x3acccccd, v64\n"
"v_mfma_f32_16x16x32_bf16 v[48:51], v[188:191], v[72:75], v[48:51]\n"
"v_fma_f32 v60, -v64, v63, v60\n"
"v_fmamk_f32 v60, v60, 0x3acccccd, v27\n"
"v_mfma_f32_16x16x32_bf16 v[52:55], v[184:187], v[76:79], v[52:55]\n"
"ds_read_b128 v[72:75], v95 offset:32768\n"
"v_max_f32 v60, v60, v27\n"
"v_mfma_f32_16x16x32_bf16 v[56:59], v[188:191], v[76:79], v[56:59]\n"
"ds_read_b32 v90, v14 offset:48\n"
"s_waitcnt lgkmcnt(3)\n"
"v_mfma_f32_16x16x32_bf16 v[28:31], v[192:195], v[80:83], v[28:31]\n"
"ds_read_b128 v[76:79], v3 offset:33792\n"
"v_rsq_f32 v62, v60\n"
"v_mfma_f32_16x16x32_bf16 v[32:35], v[196:199], v[80:83], v[32:35]\n"
"v_mul_f32 v63, -v63, v62\n"
"v_cndmask_b32 v16, 2.0, v16, s[40:41]\n"
"v_mfma_f32_16x16x32_bf16 v[36:39], v[192:195], v[84:87], v[36:39]\n"
"ds_read_b128 v[80:83], v3 offset:54272\n"
"v_mfma_f32_16x16x32_bf16 v[40:43], v[196:199], v[84:87], v[40:43]\n"
"s_waitcnt lgkmcnt(3)\n"
"v_mfma_f32_16x16x32_bf16 v[44:47], v[192:195], v[68:71], v[44:47]\n"
"ds_read_b128 v[84:87], v95 offset:13312\n"
"v_mfma_f32_16x16x32_bf16 v[48:51], v[196:199], v[68:71], v[48:51]\n"
"v_mfma_f32_16x16x32_bf16 v[52:55], v[192:195], v[72:75], v[52:55]\n"
"ds_read_b128 v[68:71], v95 offset:33792\n"
"v_mfma_f32_16x16x32_bf16 v[56:59], v[196:199], v[72:75], v[56:59]\n"
"s_waitcnt lgkmcnt(4)\n"
"v_cmp_eq_u32 vcc, 0, v90\n"
"ds_write_b64 v16, v[62:63] offset:512\n"
"s_waitcnt lgkmcnt(3)\n"
"v_mfma_f32_16x16x32_bf16 v[28:31], v[200:203], v[76:79], v[28:31]\n"
"ds_read_b128 v[72:75], v3 offset:34816\n"
"v_mfma_f32_16x16x32_bf16 v[32:35], v[204:207], v[76:79], v[32:35]\n"
"s_cbranch_vccnz .Lsl_18\n"
".Lbk_19:\n"
"ds_write_b32 v13, v13 offset:80\n"
"v_mfma_f32_16x16x32_bf16 v[36:39], v[200:203], v[80:83], v[36:39]\n"
"ds_read_b128 v[76:79], v3 offset:55296\n"
"v_lshrrev_b32 v19, 3, v0\n"
"v_mfma_f32_16x16x32_bf16 v[40:43], v[204:207], v[80:83], v[40:43]\n"
"v_add_u32 v19, s3, v19\n"
"s_waitcnt lgkmcnt(4)\n"
"v_mfma_f32_16x16x32_bf16 v[44:47], v[200:203], v[84:87], v[44:47]\n"
"ds_read_b128 v[80:83], v95 offset:14336\n"
"v_lshlrev_b32 v91, 27, v19\n"
"v_mfma_f32_16x16x32_bf16 v[48:51], v[204:207], v[84:87], v[48:51]\n"
"v_lshl_add_u32 v91, v0, 14, v91\n"
"v_mov_b32 v19, s16\n"
"v_mfma_f32_16x16x32_bf16 v[52:55], v[200:203], v[68:71], v[52:55]\n"
"ds_read_b128 v[84:87], v95 offset:34816\n"
"s_lshr_b32 s79, s2, 2\n"
"v_mfma_f32_16x16x32_bf16 v[56:59], v[204:207], v[68:71], v[56:59]\n"
"s_lshl_b32 s79, s79, 5\n"
"s_waitcnt vmcnt(11) lgkmcnt(2)\n"
"v_mfma_f32_16x16x32_bf16 v[28:31], v[208:211], v[72:75], v[28:31]\n"
"ds_read_b128 v[68:71], v3 offset:35840\n"
"s_and_b32 s78, s2, 3\n"
"v_mfma_f32_16x16x32_bf16 v[32:35], v[212:215], v[72:75], v[32:35]\n"
"s_lshl_b32 s78, s78, 2\n"
"s_add_u32 s79, s79, s78\n"
"v_mfma_f32_16x16x32_bf16 v[36:39], v[208:211], v[76:79], v[36:39]\n"
"ds_read_b128 v[72:75], v3 offset:56320\n"
"s_add_u32 s32, s79, 0x4000\n"
"v_mfma_f32_16x16x32_bf16 v[40:43], v[212:215], v[76:79], v[40:43]\n"
"buffer_store_dword v19, v91, s[4:7], s32 offen sc1\n"
"s_waitcnt lgkmcnt(2)\n"
"v_mfma_f32_16x16x32_bf16 v[44:47], v[208:211], v[80:83], v[44:47]\n"
"ds_read_b128 v[76:79], v95 offset:15360\n"
"v_mfma_f32_16x16x32_bf16 v[48:51], v[212:215], v[80:83], v[48:51]\n"
"v_mfma_f32_16x16x32_bf16 v[52:55], v[208:211], v[84:87], v[52:55]\n"
"ds_read_b128 v[80:83], v95 offset:35840\n"
"v_mfma_f32_16x16x32_bf16 v[56:59], v[212:215], v[84:87], v[56:59]\n"
"s_waitcnt vmcnt(10) lgkmcnt(2)\n"
"v_mfma_f32_16x16x32_bf16 v[28:31], v[216:219], v[68:71], v[28:31]\n"
"ds_read_b128 v[84:87], v3 offset:36864\n"
"v_mfma_f32_16x16x32_bf16 v[32:35], v[220:223], v[68:71], v[32:35]\n"
"ds_read_b32 v90, v14 offset:80\n"
"v_mfma_f32_16x16x32_bf16 v[36:39], v[216:219], v[72:75], v[36:39]\n"
"ds_read_b128 v[68:71], v4 offset:40960\n"
"v_mfma_f32_16x16x32_bf16 v[40:43], v[220:223], v[72:75], v[40:43]\n"
"s_waitcnt lgkmcnt(3)\n"
"v_mfma_f32_16x16x32_bf16 v[44:47], v[216:219], v[76:79], v[44:47]\n"
"ds_read_b128 v[72:75], v5 offset:40960\n"
"v_mfma_f32_16x16x32_bf16 v[48:51], v[220:223], v[76:79], v[48:51]\n"
"ds_read_b64 v[88:89], v94 offset:0\n"
"v_mfma_f32_16x16x32_bf16 v[52:55], v[216:219], v[80:83], v[52:55]\n"
"ds_read_b128 v[76:79], v6 offset:40960\n"
"v_mfma_f32_16x16x32_bf16 v[56:59], v[220:223], v[80:83], v[56:59]\n"
"s_waitcnt vmcnt(6) lgkmcnt(3)\n"
"v_mfma_f32_16x16x32_bf16 v[28:31], v[224:227], v[84:87], v[28:31]\n"
"ds_read_b128 v[80:83], v3 offset:57344\n"
"v_mfma_f32_16x16x32_bf16 v[32:35], v[228:231], v[84:87], v[32:35]\n"
"ds_read_b64 v[92:93], v94 offset:128\n"
"v_mfma_f32_16x16x32_bf16 v[28:31], v[232:235], v[68:71], v[28:31]\n"
"ds_read_b128 v[84:87], v4 offset:61440\n"
"v_mfma_f32_16x16x32_bf16 v[32:35], v[236:239], v[68:71], v[32:35]\n"
"v_cmp_eq_u32 vcc, 0, v90\n"
"s_waitcnt vmcnt(2) lgkmcnt(3)\n"
"v_mfma_f32_16x16x32_bf16 v[28:31], v[240:243], v[72:75], v[28:31]\n"
"ds_read_b128 v[68:71], v5 offset:61440\n"
"v_mfma_f32_16x16x32_bf16 v[32:35], v[244:247], v[72:75], v[32:35]\n"
"s_cbranch_vccnz .Lsl_30\n"
".Lbk_31:\n"
"v_mfma_f32_16x16x32_bf16 v[28:31], v[248:251], v[76:79], v[28:31]\n"
"ds_read_b128 v[72:75], v6 offset:61440\n"
"v_mfma_f32_16x16x32_bf16 v[32:35], v[252:255], v[76:79], v[32:35]\n"
"s_waitcnt lgkmcnt(2)\n"
"v_mfma_f32_16x16x32_bf16 v[36:39], v[224:227], v[80:83], v[36:39]\n"
"ds_read_b128 v[76:79], v95 offset:16384\n"
"v_mfma_f32_16x16x32_bf16 v[40:43], v[228:231], v[80:83], v[40:43]\n"
"v_mov_b32_dpp v16, v28 row_newbcast:0 row_mask:0xf bank_mask:0xf\n"
"v_mov_b32_dpp v17, v29 row_newbcast:0 row_mask:0xf bank_mask:0xf\n"
"v_mov_b32_dpp v18, v30 row_newbcast:0 row_mask:0xf bank_mask:0xf\n"
"v_mfma_f32_16x16x32_bf16 v[36:39], v[232:235], v[84:87], v[36:39]\n"
"v_add_u32 v4, 0x1c400, v4\n"
"ds_read_b128 v[80:83], v4 offset:0\n"
"v_mov_b32_dpp v19, v31 row_newbcast:0 row_mask:0xf bank_mask:0xf\n"
"v_mfma_f32_16x16x32_bf16 v[40:43], v[236:239], v[84:87], v[40:43]\n"
"v_mov_b32_dpp v20, v32 row_newbcast:0 row_mask:0xf bank_mask:0xf\n"
"v_mov_b32_dpp v21, v33 row_newbcast:0 row_mask:0xf bank_mask:0xf\n"
"s_waitcnt lgkmcnt(2)\n"
"v_mfma_f32_16x16x32_bf16 v[36:39], v[240:243], v[68:71], v[36:39]\n"
"v_add_u32 v5, 0x1c400, v5\n"
"ds_read_b128 v[84:87], v5 offset:0\n"
"v_mfma_f32_16x16x32_bf16 v[40:43], v[244:247], v[68:71], v[40:43]\n"
"v_mov_b32_dpp v22, v34 row_newbcast:0 row_mask:0xf bank_mask:0xf\n"
"v_mov_b32_dpp v23, v35 row_newbcast:0 row_mask:0xf bank_mask:0xf\n"
"v_mul_f32 v28, v88, v28\n"
"v_mfma_f32_16x16x32_bf16 v[36:39], v[248:251], v[72:75], v[36:39]\n"
"v_add_u32 v6, 0x1c400, v6\n"
"ds_read_b128 v[68:71], v6 offset:0\n"
"v_mul_f32 v29, v88, v29\n"
"v_mfma_f32_16x16x32_bf16 v[40:43], v[252:255], v[72:75], v[40:43]\n"
"v_mul_f32 v30, v88, v30\n"
"v_mul_f32 v31, v88, v31\n"
"s_waitcnt lgkmcnt(2)\n"
"v_mfma_f32_16x16x32_bf16 v[44:47], v[224:227], v[76:79], v[44:47]\n"
"ds_read_b128 v[72:75], v95 offset:36864\n"
"v_fmac_f32 v28, v89, v16\n"
"v_fmac_f32 v29, v89, v17\n"
"v_mfma_f32_16x16x32_bf16 v[48:51], v[228:231], v[76:79], v[48:51]\n"
"ds_write_b32 v13, v13 offset:64\n"
"v_fmac_f32 v30, v89, v18\n"
"v_mfma_f32_16x16x32_bf16 v[44:47], v[232:235], v[80:83], v[44:47]\n"
"ds_read_b128 v[76:79], v4 offset:20480\n"
"v_fmac_f32 v31, v89, v19\n"
"v_cvt_pk_bf16_f32 v28, v28, v29\n"
"v_mfma_f32_16x16x32_bf16 v[48:51], v[236:239], v[80:83], v[48:51]\n"
"v_cvt_pk_bf16_f32 v29, v30, v31\n"
"ds_write_b64 v7, v[28:29] offset:0\n"
"s_waitcnt lgkmcnt(4)\n"
"v_mfma_f32_16x16x32_bf16 v[44:47], v[240:243], v[84:87], v[44:47]\n"
"ds_read_b128 v[80:83], v5 offset:20480\n"
"v_mul_f32 v32, v88, v32\n"
"v_mul_f32 v33, v88, v33\n"
"v_mfma_f32_16x16x32_bf16 v[48:51], v[244:247], v[84:87], v[48:51]\n"
"v_mul_f32 v34, v88, v34\n"
"v_mul_f32 v35, v88, v35\n"
"v_fmac_f32 v32, v89, v20\n"
"v_mfma_f32_16x16x32_bf16 v[44:47], v[248:251], v[68:71], v[44:47]\n"
"ds_read_b128 v[84:87], v6 offset:20480\n"
"v_fmac_f32 v33, v89, v21\n"
"v_fmac_f32 v34, v89, v22\n"
"v_mfma_f32_16x16x32_bf16 v[48:51], v[252:255], v[68:71], v[48:51]\n"
"v_fmac_f32 v35, v89, v23\n"
"v_cvt_pk_bf16_f32 v32, v32, v33\n"
"s_waitcnt lgkmcnt(3)\n"
"v_mfma_f32_16x16x32_bf16 v[52:55], v[224:227], v[72:75], v[52:55]\n"
"v_add_u32 v3, 0xffffb000, v3\n"
"ds_read_b128 v[68:71], v3 offset:0\n"
"v_cvt_pk_bf16_f32 v33, v34, v35\n"
"v_mfma_f32_16x16x32_bf16 v[56:59], v[228:231], v[72:75], v[56:59]\n"
"ds_read_b32 v90, v14 offset:64\n"
"ds_write_b64 v8, v[32:33] offset:0\n"
"v_mfma_f32_16x16x32_bf16 v[52:55], v[232:235], v[76:79], v[52:55]\n"
"ds_read_b128 v[72:75], v3 offset:1024\n"
"ds_read_b64 v[88:89], v94 offset:256\n"
"v_mul_f32 v36, v92, v36\n"
"v_mfma_f32_16x16x32_bf16 v[56:59], v[236:239], v[76:79], v[56:59]\n"
"v_mul_f32 v37, v92, v37\n"
"v_mul_f32 v38, v92, v38\n"
"s_waitcnt lgkmcnt(5)\n"
"v_mfma_f32_16x16x32_bf16 v[52:55], v[240:243], v[80:83], v[52:55]\n"
"ds_read_b128 v[76:79], v3 offset:2048\n"
"v_mul_f32 v39, v92, v39\n"
"v_fmac_f32 v36, v93, v16\n"
"v_mfma_f32_16x16x32_bf16 v[56:59], v[244:247], v[80:83], v[56:59]\n"
"v_fmac_f32 v37, v93, v17\n"
"v_fmac_f32 v38, v93, v18\n"
"v_fmac_f32 v39, v93, v19\n"
"v_mfma_f32_16x16x32_bf16 v[52:55], v[248:251], v[84:87], v[52:55]\n"
"ds_read_b128 v[80:83], v3 offset:3072\n"
"v_cvt_pk_bf16_f32 v36, v36, v37\n"
"v_cvt_pk_bf16_f32 v37, v38, v39\n"
"v_mfma_f32_16x16x32_bf16 v[56:59], v[252:255], v[84:87], v[56:59]\n"
"ds_write_b64 v7, v[36:37] offset:3968\n"
"s_waitcnt lgkmcnt(4)\n"
"v_mfma_f32_16x16x32_bf16 v[60:63], v[96:99], v[68:71], 0\n"
"ds_read_b128 v[84:87], v3 offset:4096\n"
"v_mul_f32 v40, v92, v40\n"
"v_mul_f32 v41, v92, v41\n"
"v_mfma_f32_16x16x32_bf16 v[64:67], v[100:103], v[68:71], 0\n"
"v_cmp_eq_u32 vcc, 0, v90\n"
"v_mul_f32 v42, v92, v42\n"
"v_mul_f32 v43, v92, v43\n"
"v_mfma_f32_16x16x32_bf16 v[60:63], v[104:107], v[72:75], v[60:63]\n"
"ds_read_b128 v[68:71], v3 offset:5120\n"
"v_fmac_f32 v40, v93, v20\n"
"v_fmac_f32 v41, v93, v21\n"
"v_mfma_f32_16x16x32_bf16 v[64:67], v[108:111], v[72:75], v[64:67]\n"
"s_cbranch_vccnz .Lsl_22\n"
".Lbk_23:\n"
"s_waitcnt lgkmcnt(3)\n"
"v_mfma_f32_16x16x32_bf16 v[60:63], v[112:115], v[76:79], v[60:63]\n"
"ds_read_b128 v[72:75], v3 offset:6144\n"
"v_fmac_f32 v42, v93, v22\n"
"v_fmac_f32 v43, v93, v23\n"
"v_mfma_f32_16x16x32_bf16 v[64:67], v[116:119], v[76:79], v[64:67]\n"
"v_cvt_pk_bf16_f32 v40, v40, v41\n"
"v_cvt_pk_bf16_f32 v41, v42, v43\n"
"ds_write_b64 v8, v[40:41] offset:3968\n"
"v_mfma_f32_16x16x32_bf16 v[60:63], v[120:123], v[80:83], v[60:63]\n"
"ds_read_b128 v[76:79], v3 offset:7168\n"
"ds_read_b64 v[92:93], v94 offset:384\n"
"v_mul_f32 v44, v88, v44\n"
"v_mfma_f32_16x16x32_bf16 v[64:67], v[124:127], v[80:83], v[64:67]\n"
"s_add_u32 s31, s28, 0x140000\n"
"v_mul_f32 v45, v88, v45\n"
"s_waitcnt lgkmcnt(4)\n"
"v_mfma_f32_16x16x32_bf16 v[60:63], v[128:131], v[84:87], v[60:63]\n"
"ds_read_b128 v[80:83], v3 offset:8192\n"
"s_add_u32 m0, s29, 0xa000\n"
"v_mul_f32 v46, v88, v46\n"
"v_mfma_f32_16x16x32_bf16 v[64:67], v[132:135], v[84:87], v[64:67]\n"
"buffer_load_dwordx4 v2, s[8:11], s31 offen offset:0 lds\n"
"v_mul_f32 v47, v88, v47\n"
"v_fmac_f32 v44, v89, v16\n"
"v_mfma_f32_16x16x32_bf16 v[60:63], v[136:139], v[68:71], v[60:63]\n"
"ds_read_b128 v[84:87], v3 offset:9216\n"
"s_add_u32 m0, s29, 0xa3c0\n"
"v_fmac_f32 v45, v89, v17\n"
"v_mfma_f32_16x16x32_bf16 v[64:67], v[140:143], v[68:71], v[64:67]\n"
"v_fmac_f32 v46, v89, v18\n"
"v_fmac_f32 v47, v89, v19\n"
"s_waitcnt lgkmcnt(3)\n"
"v_mfma_f32_16x16x32_bf16 v[60:63], v[144:147], v[72:75], v[60:63]\n"
"ds_read_b128 v[68:71], v3 offset:10240\n"
"buffer_load_dwordx4 v2, s[8:11], s31 offen offset:64 lds\n"
"v_cvt_pk_bf16_f32 v44, v44, v45\n"
"v_mfma_f32_16x16x32_bf16 v[64:67], v[148:151], v[72:75], v[64:67]\n"
"s_add_u32 m0, s29, 0xa780\n"
"v_cvt_pk_bf16_f32 v45, v46, v47\n"
"ds_write_b64 v7, v[44:45] offset:7936\n"
"v_mfma_f32_16x16x32_bf16 v[60:63], v[152:155], v[76:79], v[60:63]\n"
"ds_read_b128 v[72:75], v3 offset:11264\n"
"v_mul_f32 v48, v88, v48\n"
"v_mul_f32 v49, v88, v49\n"
"v_mfma_f32_16x16x32_bf16 v[64:67], v[156:159], v[76:79], v[64:67]\n"
"buffer_load_dwordx4 v2, s[8:11], s31 offen offset:128 lds\n"
"v_mul_f32 v50, v88, v50\n"
"s_waitcnt lgkmcnt(3)\n"
"v_mfma_f32_16x16x32_bf16 v[60:63], v[160:163], v[80:83], v[60:63]\n"
"ds_read_b128 v[76:79], v3 offset:12288\n"
"s_add_u32 m0, s29, 0xab40\n"
"v_mul_f32 v51, v88, v51\n"
"v_mfma_f32_16x16x32_bf16 v[64:67], v[164:167], v[80:83], v[64:67]\n"
"v_fmac_f32 v48, v89, v20\n"
"v_fmac_f32 v49, v89, v21\n"
"v_fmac_f32 v50, v89, v22\n"
"v_mfma_f32_16x16x32_bf16 v[60:63], v[168:171], v[84:87], v[60:63]\n"
"ds_read_b128 v[80:83], v3 offset:13312\n"
"buffer_load_dwordx4 v2, s[8:11], s31 offen offset:192 lds\n"
"v_fmac_f32 v51, v89, v23\n"
"v_mfma_f32_16x16x32_bf16 v[64:67], v[172:175], v[84:87], v[64:67]\n"
"s_add_u32 m0, s29, 0xaf00\n"
"v_cvt_pk_bf16_f32 v48, v48, v49\n"
"s_waitcnt lgkmcnt(2)\n"
"v_mfma_f32_16x16x32_bf16 v[60:63], v[176:179], v[68:71], v[60:63]\n"
"ds_read_b128 v[84:87], v3 offset:14336\n"
"v_cvt_pk_bf16_f32 v49, v50, v51\n"
"ds_write_b64 v8, v[48:49] offset:7936\n"
"v_mfma_f32_16x16x32_bf16 v[64:67], v[180:183], v[68:71], v[64:67]\n"
"buffer_load_dwordx4 v2, s[8:11], s31 offen offset:256 lds\n"
"ds_read_b64 v[88:89], v94 offset:512\n"
"v_mul_f32 v52, v92, v52\n"
"v_mfma_f32_16x16x32_bf16 v[60:63], v[184:187], v[72:75], v[60:63]\n"
"ds_read_b128 v[68:71], v3 offset:15360\n"
"s_add_u32 s31, s28, 0x145000\n"
"v_mul_f32 v53, v92, v53\n"
"v_mfma_f32_16x16x32_bf16 v[64:67], v[188:191], v[72:75], v[64:67]\n"
"s_add_u32 m0, s29, 0xf000\n"
"v_mul_f32 v54, v92, v54\n"
"s_waitcnt lgkmcnt(4)\n"
"v_mfma_f32_16x16x32_bf16 v[60:63], v[192:195], v[76:79], v[60:63]\n"
"ds_read_b128 v[72:75], v3 offset:16384\n"
"buffer_load_dwordx4 v2, s[8:11], s31 offen offset:0 lds\n"
"v_mul_f32 v55, v92, v55\n"
"v_mfma_f32_16x16x32_bf16 v[64:67], v[196:199], v[76:79], v[64:67]\n"
"s_add_u32 m0, s29, 0xf3c0\n"
"v_fmac_f32 v52, v93, v16\n"
"v_fmac_f32 v53, v93, v17\n"
"v_mfma_f32_16x16x32_bf16 v[60:63], v[200:203], v[80:83], v[60:63]\n"
"v_add_u32 v4, 0xfffe3c00, v4\n"
"ds_read_b128 v[76:79], v4 offset:0\n"
"v_fmac_f32 v54, v93, v18\n"
"v_mfma_f32_16x16x32_bf16 v[64:67], v[204:207], v[80:83], v[64:67]\n"
"buffer_load_dwordx4 v2, s[8:11], s31 offen offset:64 lds\n"
"v_fmac_f32 v55, v93, v19\n"
"s_waitcnt lgkmcnt(2)\n"
"v_mfma_f32_16x16x32_bf16 v[60:63], v[208:211], v[84:87], v[60:63]\n"
"v_add_u32 v5, 0xfffe3c00, v5\n"
"ds_read_b128 v[80:83], v5 offset:0\n"
"v_cvt_pk_bf16_f32 v52, v52, v53\n"
"v_mfma_f32_16x16x32_bf16 v[64:67], v[212:215], v[84:87], v[64:67]\n"
"s_add_u32 m0, s29, 0xf780\n"
"v_cvt_pk_bf16_f32 v53, v54, v55\n"
"ds_write_b64 v7, v[52:53] offset:11904\n"
"v_mfma_f32_16x16x32_bf16 v[60:63], v[216:219], v[68:71], v[60:63]\n"
"v_add_u32 v6, 0xfffe3c00, v6\n"
"ds_read_b128 v[84:87], v6 offset:0\n"
"buffer_load_dwordx4 v2, s[8:11], s31 offen offset:128 lds\n"
"v_mfma_f32_16x16x32_bf16 v[64:67], v[220:223], v[68:71], v[64:67]\n"
"s_add_u32 m0, s29, 0xfb40\n"
"v_mul_f32 v56, v92, v56\n"
"s_waitcnt lgkmcnt(3)\n"
"v_mfma_f32_16x16x32_bf16 v[60:63], v[224:227], v[72:75], v[60:63]\n"
"v_mul_f32 v57, v92, v57\n"
"v_mul_f32 v58, v92, v58\n"
"v_mul_f32 v59, v92, v59\n"
"v_mfma_f32_16x16x32_bf16 v[64:67], v[228:231], v[72:75], v[64:67]\n"
"buffer_load_dwordx4 v2, s[8:11], s31 offen offset:192 lds\n"
"v_fmac_f32 v56, v93, v20\n"
"v_fmac_f32 v57, v93, v21\n"
"v_mfma_f32_16x16x32_bf16 v[60:63], v[232:235], v[76:79], v[60:63]\n"
"s_add_u32 m0, s29, 0xff00\n"
"v_fmac_f32 v58, v93, v22\n"
"v_mfma_f32_16x16x32_bf16 v[64:67], v[236:239], v[76:79], v[64:67]\n"
"v_fmac_f32 v59, v93, v23\n"
"s_waitcnt lgkmcnt(0)\n"
"v_mfma_f32_16x16x32_bf16 v[60:63], v[240:243], v[80:83], v[60:63]\n"
"buffer_load_dwordx4 v2, s[8:11], s31 offen offset:256 lds\n"
"v_cvt_pk_bf16_f32 v56, v56, v57\n"
"v_mfma_f32_16x16x32_bf16 v[64:67], v[244:247], v[80:83], v[64:67]\n"
"v_cvt_pk_bf16_f32 v57, v58, v59\n"
"ds_write_b64 v8, v[56:57] offset:11904\n"
"v_mfma_f32_16x16x32_bf16 v[60:63], v[248:251], v[84:87], v[60:63]\n"
"v_mfma_f32_16x16x32_bf16 v[64:67], v[252:255], v[84:87], v[64:67]\n"
"s_nop 4\n"
"v_mul_f32 v60, v88, v60\n"
"v_mul_f32 v61, v88, v61\n"
"v_mul_f32 v62, v88, v62\n"
"v_mul_f32 v63, v88, v63\n"
"v_fmac_f32 v60, v89, v16\n"
"v_fmac_f32 v61, v89, v17\n"
"v_fmac_f32 v62, v89, v18\n"
"v_fmac_f32 v63, v89, v19\n"
"v_cvt_pk_bf16_f32 v60, v60, v61\n"
"v_cvt_pk_bf16_f32 v61, v62, v63\n"
"ds_write_b64 v7, v[60:61] offset:15872\n"
"v_mul_f32 v64, v88, v64\n"
"v_mul_f32 v65, v88, v65\n"
"v_mul_f32 v66, v88, v66\n"
"v_mul_f32 v67, v88, v67\n"
"v_fmac_f32 v64, v89, v20\n"
"v_fmac_f32 v65, v89, v21\n"
"v_fmac_f32 v66, v89, v22\n"
"v_fmac_f32 v67, v89, v23\n"
"v_cvt_pk_bf16_f32 v64, v64, v65\n"
"v_cvt_pk_bf16_f32 v65, v66, v67\n"
"ds_write_b64 v8, v[64:65] offset:15872\n"
"ds_write_b32 v13, v13 offset:96\n"
"s_cmp_lt_i32 s3, 2\n"
"s_cselect_b32 s33, 1, 0\n"
"v_mov_b32 v46, 0\n"
"v_mov_b32 v47, 0\n"
"v_mov_b32 v50, 0\n"
"v_mov_b32 v51, 0\n"
"v_mov_b32 v72, s42\n"
"v_mov_b32 v73, s42\n"
"v_mov_b32 v74, s42\n"
"v_mov_b32 v75, s42\n"
"s_mov_b32 s17, 0x100000\n"
".Ltp_34:\n"
"ds_read_b32 v90, v14 offset:96\n"
"s_sub_u32 s17, s17, 1\n"
"s_cbranch_scc1 .Lou_35\n"
"s_waitcnt lgkmcnt(0)\n"
"v_cmp_eq_u32 vcc, 0, v90\n"
"s_cbranch_vccnz .Ltp_34\n"
".Lou_35:\n"
"s_waitcnt lgkmcnt(0)\n"
"s_mov_b64 exec, 0xffff\n"
"ds_read_b64 v[46:47], v24 offset:4000\n"
"ds_read_b64 v[50:51], v24 offset:4040\n"
"s_mov_b64 exec, -1\n"
"ds_read_b64 v[44:45], v25 offset:3968\n"
"ds_read_b64 v[56:57], v25 offset:80\n"
"ds_read_b64 v[58:59], v24 offset:112\n"
"ds_read_b64 v[64:65], v25 offset:4048\n"
"ds_read_b64 v[66:67], v24 offset:4080\n"
"ds_read_b64 v[48:49], v25 offset:4008\n"
"ds_read_b64 v[60:61], v25 offset:120\n"
"ds_read_b64 v[62:63], v24 offset:152\n"
"ds_read_b64 v[68:69], v25 offset:4088\n"
"ds_read_b64 v[70:71], v24 offset:4120\n"
"s_add_u32 s31, s28, 0x14a000\n"
"s_add_u32 m0, s29, 0\n"
"s_nop 0\n"
"buffer_load_dwordx4 v2, s[8:11], s31 offen offset:0 lds\n"
"ds_read_b64_tr_b16 v[28:29], v26 offset:0\n"
"ds_read_b64_tr_b16 v[30:31], v26 offset:3968\n"
"ds_read_b64_tr_b16 v[32:33], v26 offset:40\n"
"v_mov_b32 v52, 0x0c0c0c0c\n"
"v_mov_b32 v53, 0xc0c0100\n"
"v_mov_b32 v54, 0x1000c0c\n"
"v_cndmask_b32 v53, v53, v54, s[34:35]\n"
"v_cndmask_b32 v80, v53, v52, s[36:37]\n"
"v_cndmask_b32 v82, v52, v53, s[36:37]\n"
"v_mov_b32 v53, 0xc0c0302\n"
"v_mov_b32 v54, 0x3020c0c\n"
"v_cndmask_b32 v53, v53, v54, s[34:35]\n"
"v_cndmask_b32 v81, v53, v52, s[38:39]\n"
"v_cndmask_b32 v83, v52, v53, s[38:39]\n"
"s_waitcnt lgkmcnt(10)\n"
"v_mfma_f32_16x16x32_bf16 v[56:59], v[56:59], v[44:47], v[72:75]\n"
"s_waitcnt lgkmcnt(8)\n"
"v_mfma_f32_16x16x32_bf16 v[64:67], v[64:67], v[44:47], 0\n"
"ds_read_b64_tr_b16 v[34:35], v26 offset:4008\n"
"s_add_u32 m0, s29, 0x3c0\n"
"s_nop 0\n"
"buffer_load_dwordx4 v2, s[8:11], s31 offen offset:64 lds\n"
"ds_read_b64_tr_b16 v[36:37], v26 offset:32\n"
"ds_read_b64_tr_b16 v[38:39], v26 offset:4000\n"
"s_waitcnt lgkmcnt(8)\n"
"v_mfma_f32_16x16x32_bf16 v[60:63], v[60:63], v[48:51], v[72:75]\n"
"s_waitcnt lgkmcnt(6)\n"
"v_mfma_f32_16x16x32_bf16 v[68:71], v[68:71], v[48:51], 0\n"
"ds_read_b64_tr_b16 v[40:41], v26 offset:72\n"
"ds_read_b64_tr_b16 v[42:43], v26 offset:4040\n"
"v_mov_b32 v84, 0x3f803f80\n"
"v_mov_b32 v85, 0x3f803f80\n"
"v_mov_b32 v86, 0x3f803f80\n"
"v_mov_b32 v87, 0x3f803f80\n"
"v_cndmask_b32 v76, v56, v57, s[34:35]\n"
"v_cndmask_b32 v72, v64, v65, s[34:35]\n"
"v_cndmask_b32 v76, v76, v72, s[36:37]\n"
"v_cndmask_b32 v77, v58, v59, s[34:35]\n"
"v_cndmask_b32 v72, v66, v67, s[34:35]\n"
"v_cndmask_b32 v77, v77, v72, s[38:39]\n"
"v_max_f32 v52, v76, v77\n"
"s_add_u32 m0, s29, 0x780\n"
"s_nop 0\n"
"buffer_load_dwordx4 v2, s[8:11], s31 offen offset:128 lds\n"
"v_max_f32 v54, v76, v77\n"
"v_cndmask_b32 v78, v60, v61, s[34:35]\n"
"v_cndmask_b32 v72, v68, v69, s[34:35]\n"
"v_cndmask_b32 v78, v78, v72, s[36:37]\n"
"v_cndmask_b32 v79, v62, v63, s[34:35]\n"
"v_cndmask_b32 v72, v70, v71, s[34:35]\n"
"v_cndmask_b32 v79, v79, v72, s[38:39]\n"
"v_max_f32 v53, v78, v79\n"
"v_max_f32 v55, v78, v79\n"
"v_permlane16_swap_b32 v52, v54\n"
"s_nop 0\n"
"v_permlane16_swap_b32 v53, v55\n"
"v_max_f32 v72, v52, v54\n"
"v_max_f32 v52, v52, v54\n"
"v_max_f32 v73, v53, v55\n"
"v_max_f32 v53, v53, v55\n"
"v_permlane32_swap_b32 v52, v72\n"
"s_nop 0\n"
"v_permlane32_swap_b32 v53, v73\n"
"s_add_u32 m0, s29, 0xb40\n"
"s_nop 0\n"
"buffer_load_dwordx4 v2, s[8:11], s31 offen offset:192 lds\n"
"v_max_f32 v52, v52, v72\n"
"v_max_f32 v53, v53, v73\n"
"v_mul_f32 v52, 0xbea52b5b, v52\n"
"v_mul_f32 v53, 0xbea52b5b, v53\n"
"v_fmamk_f32 v76, v76, 0x3ea52b5b, v52\n"
"v_fmamk_f32 v77, v77, 0x3ea52b5b, v52\n"
"v_fmamk_f32 v78, v78, 0x3ea52b5b, v53\n"
"v_fmamk_f32 v79, v79, 0x3ea52b5b, v53\n"
"v_exp_f32 v76, v76\n"
"v_exp_f32 v77, v77\n"
"v_exp_f32 v78, v78\n"
"v_exp_f32 v79, v79\n"
"v_cvt_pk_bf16_f32 v52, v76, v77\n"
"v_cvt_pk_bf16_f32 v53, v78, v79\n"
"v_perm_b32 v44, v52, v52, v80\n"
"v_perm_b32 v45, v52, v52, v81\n"
"v_perm_b32 v46, v52, v52, v82\n"
"s_add_u32 m0, s29, 0xf00\n"
"s_nop 0\n"
"buffer_load_dwordx4 v2, s[8:11], s31 offen offset:256 lds\n"
"v_perm_b32 v47, v52, v52, v83\n"
"v_perm_b32 v48, v53, v53, v80\n"
"v_perm_b32 v49, v53, v53, v81\n"
"v_perm_b32 v50, v53, v53, v82\n"
"v_perm_b32 v51, v53, v53, v83\n"
"s_waitcnt lgkmcnt(0)\n"
"s_waitcnt vmcnt(5)\n"
"ds_write_b32 v13, v13 offset:112\n"
"v_mfma_f32_16x16x32_bf16 v[56:59], v[84:87], v[44:47], 0\n"
"v_mfma_f32_16x16x32_bf16 v[60:63], v[84:87], v[48:51], 0\n"
"v_mfma_f32_16x16x32_bf16 v[28:31], v[28:31], v[44:47], 0\n"
"v_mfma_f32_16x16x32_bf16 v[32:35], v[32:35], v[48:51], 0\n"
"v_mfma_f32_16x16x32_bf16 v[36:39], v[36:39], v[44:47], 0\n"
"v_mfma_f32_16x16x32_bf16 v[40:43], v[40:43], v[48:51], 0\n"
"v_cndmask_b32 v88, 2.0, v1, s[40:41]\n"
"v_rcp_f32 v52, v56\n"
"v_rcp_f32 v53, v60\n"
"s_mov_b32 s17, 0x100000\n"
".Ltp_36:\n"
"ds_read_b32 v90, v14 offset:112\n"
"s_sub_u32 s17, s17, 1\n"
"s_cbranch_scc1 .Lou_37\n"
"s_waitcnt lgkmcnt(0)\n"
"v_cmp_eq_u32 vcc, 0, v90\n"
"s_cbranch_vccnz .Ltp_36\n"
".Lou_37:\n"
"s_waitcnt lgkmcnt(0)\n"
"ds_read_b128 v[56:59], v3 offset:40960\n"
"s_mov_b32 s31, s30\n"
"v_mul_f32 v28, v52, v28\n"
"v_mul_f32 v29, v52, v29\n"
"v_mul_f32 v30, v52, v30\n"
"v_mul_f32 v31, v52, v31\n"
"v_mul_f32 v32, v53, v32\n"
"ds_read_b128 v[60:63], v3 offset:41984\n"
"v_mul_f32 v33, v53, v33\n"
"v_mul_f32 v34, v53, v34\n"
"v_mul_f32 v35, v53, v35\n"
"v_cvt_pk_bf16_f32 v28, v28, v29\n"
"v_cvt_pk_bf16_f32 v29, v30, v31\n"
"v_cvt_pk_bf16_f32 v30, v32, v33\n"
"ds_read_b128 v[64:67], v3 offset:43008\n"
"v_cvt_pk_bf16_f32 v31, v34, v35\n"
"v_min_u32 v28, -2, v28\n"
"v_min_u32 v29, -2, v29\n"
"v_min_u32 v30, -2, v30\n"
"v_min_u32 v31, -2, v31\n"
"buffer_store_dwordx4 v[28:31], v1, s[4:7], s31 offen offset:0 sc1\n"
"ds_read_b128 v[68:71], v3 offset:44032\n"
"v_mul_f32 v36, v52, v36\n"
"v_mul_f32 v37, v52, v37\n"
"v_mul_f32 v38, v52, v38\n"
"v_mul_f32 v39, v52, v39\n"
"v_mul_f32 v40, v53, v40\n"
"v_mul_f32 v41, v53, v41\n"
"ds_read_b128 v[72:75], v3 offset:45056\n"
"v_mul_f32 v42, v53, v42\n"
"v_mul_f32 v43, v53, v43\n"
"v_cvt_pk_bf16_f32 v36, v36, v37\n"
"v_cvt_pk_bf16_f32 v37, v38, v39\n"
"v_cvt_pk_bf16_f32 v38, v40, v41\n"
"v_cvt_pk_bf16_f32 v39, v42, v43\n"
"v_min_u32 v36, -2, v36\n"
"v_min_u32 v37, -2, v37\n"
"v_min_u32 v38, -2, v38\n"
"v_min_u32 v39, -2, v39\n"
"buffer_store_dwordx4 v[36:39], v88, s[4:7], s31 offen offset:1024 sc1\n"
"ds_write_b32 v13, v13 offset:128\n"
"s_mul_i32 s79, s43, 0x1400\n"
"v_lshrrev_b32 v92, 2, v0\n"
"v_and_b32 v91, 3, v0\n"
"v_lshlrev_b32 v91, 4, v91\n"
"v_lshl_add_u32 v92, v92, 10, v91\n"
"v_add_u32 v92, s79, v92\n"
"v_mov_b32 v91, 0x40000\n"
"v_cmp_gt_i32 vcc, 20, v0\n"
"s_nop 1\n"
"v_cndmask_b32 v93, v91, v92, vcc\n"
"s_waitcnt lgkmcnt(4)\n"
"v_mfma_f32_16x16x32_bf16 v[28:31], v[96:99], v[56:59], 0\n"
"ds_read_b128 v[76:79], v3 offset:46080\n"
"s_add_u32 s31, s28, 0x14f000\n"
"v_mfma_f32_16x16x32_bf16 v[32:35], v[100:103], v[56:59], 0\n"
"s_add_u32 m0, s29, 0x5000\n"
"v_mfma_f32_16x16x32_bf16 v[52:55], v[56:59], v[56:59], 0\n"
"v_mfma_f32_16x16x32_bf16 v[28:31], v[104:107], v[60:63], v[28:31]\n"
"ds_read_b128 v[56:59], v3 offset:47104\n"
"buffer_load_dwordx4 v2, s[8:11], s31 offen offset:0 lds\n"
"v_mfma_f32_16x16x32_bf16 v[32:35], v[108:111], v[60:63], v[32:35]\n"
"s_add_u32 m0, s29, 0x53c0\n"
"s_waitcnt lgkmcnt(4)\n"
"v_mfma_f32_16x16x32_bf16 v[28:31], v[112:115], v[64:67], v[28:31]\n"
"ds_read_b128 v[60:63], v3 offset:48128\n"
"v_mfma_f32_16x16x32_bf16 v[32:35], v[116:119], v[64:67], v[32:35]\n"
"v_mfma_f32_16x16x32_bf16 v[28:31], v[120:123], v[68:71], v[28:31]\n"
"ds_read_b128 v[64:67], v3 offset:49152\n"
"buffer_load_dwordx4 v2, s[8:11], s31 offen offset:64 lds\n"
"v_mfma_f32_16x16x32_bf16 v[32:35], v[124:127], v[68:71], v[32:35]\n"
"s_add_u32 m0, s29, 0x5780\n"
"s_waitcnt lgkmcnt(3)\n"
"v_mfma_f32_16x16x32_bf16 v[28:31], v[128:131], v[72:75], v[28:31]\n"
"ds_read_b128 v[68:71], v3 offset:50176\n"
"v_mfma_f32_16x16x32_bf16 v[32:35], v[132:135], v[72:75], v[32:35]\n"
"v_mfma_f32_16x16x32_bf16 v[52:55], v[72:75], v[72:75], v[52:55]\n"
"buffer_load_dwordx4 v2, s[8:11], s31 offen offset:128 lds\n"
"v_mfma_f32_16x16x32_bf16 v[28:31], v[136:139], v[76:79], v[28:31]\n"
"ds_read_b128 v[72:75], v3 offset:51200\n"
"s_add_u32 m0, s29, 0x5b40\n"
"v_mfma_f32_16x16x32_bf16 v[32:35], v[140:143], v[76:79], v[32:35]\n"
"s_waitcnt lgkmcnt(3)\n"
"v_mfma_f32_16x16x32_bf16 v[28:31], v[144:147], v[56:59], v[28:31]\n"
"ds_read_b128 v[76:79], v3 offset:52224\n"
"v_mfma_f32_16x16x32_bf16 v[32:35], v[148:151], v[56:59], v[32:35]\n"
"buffer_load_dwordx4 v2, s[8:11], s31 offen offset:192 lds\n"
"v_mfma_f32_16x16x32_bf16 v[28:31], v[152:155], v[60:63], v[28:31]\n"
"ds_read_b128 v[56:59], v3 offset:53248\n"
"s_add_u32 m0, s29, 0x5f00\n"
"v_mfma_f32_16x16x32_bf16 v[32:35], v[156:159], v[60:63], v[32:35]\n"
"s_waitcnt lgkmcnt(3)\n"
"v_mfma_f32_16x16x32_bf16 v[28:31], v[160:163], v[64:67], v[28:31]\n"
"ds_read_b128 v[60:63], v3 offset:54272\n"
"v_mfma_f32_16x16x32_bf16 v[32:35], v[164:167], v[64:67], v[32:35]\n"
"buffer_load_dwordx4 v2, s[8:11], s31 offen offset:256 lds\n"
"v_mfma_f32_16x16x32_bf16 v[52:55], v[64:67], v[64:67], v[52:55]\n"
"v_mfma_f32_16x16x32_bf16 v[28:31], v[168:171], v[68:71], v[28:31]\n"
"ds_read_b128 v[64:67], v3 offset:55296\n"
"v_mfma_f32_16x16x32_bf16 v[32:35], v[172:175], v[68:71], v[32:35]\n"
"s_waitcnt lgkmcnt(3)\n"
"v_mfma_f32_16x16x32_bf16 v[28:31], v[176:179], v[72:75], v[28:31]\n"
"ds_read_b128 v[68:71], v3 offset:56320\n"
"v_mfma_f32_16x16x32_bf16 v[32:35], v[180:183], v[72:75], v[32:35]\n"
"v_mfma_f32_16x16x32_bf16 v[28:31], v[184:187], v[76:79], v[28:31]\n"
"ds_read_b128 v[72:75], v3 offset:57344\n"
"v_mfma_f32_16x16x32_bf16 v[32:35], v[188:191], v[76:79], v[32:35]\n"
"s_waitcnt lgkmcnt(3)\n"
"v_mfma_f32_16x16x32_bf16 v[28:31], v[192:195], v[56:59], v[28:31]\n"
"ds_read_b128 v[76:79], v4 offset:40960\n"
"v_mfma_f32_16x16x32_bf16 v[32:35], v[196:199], v[56:59], v[32:35]\n"
"v_mfma_f32_16x16x32_bf16 v[52:55], v[56:59], v[56:59], v[52:55]\n"
"v_mfma_f32_16x16x32_bf16 v[28:31], v[200:203], v[60:63], v[28:31]\n"
"ds_read_b128 v[56:59], v5 offset:40960\n"
"v_mfma_f32_16x16x32_bf16 v[32:35], v[204:207], v[60:63], v[32:35]\n"
"s_waitcnt lgkmcnt(3)\n"
"v_mfma_f32_16x16x32_bf16 v[28:31], v[208:211], v[64:67], v[28:31]\n"
"ds_read_b128 v[60:63], v6 offset:40960\n"
"v_mfma_f32_16x16x32_bf16 v[32:35], v[212:215], v[64:67], v[32:35]\n"
"v_mfma_f32_16x16x32_bf16 v[28:31], v[216:219], v[68:71], v[28:31]\n"
"v_add_u32 v3, 0x5000, v3\n"
"ds_read_b128 v[64:67], v3 offset:40960\n"
"v_mfma_f32_16x16x32_bf16 v[32:35], v[220:223], v[68:71], v[32:35]\n"
"s_waitcnt lgkmcnt(3)\n"
"v_mfma_f32_16x16x32_bf16 v[28:31], v[224:227], v[72:75], v[28:31]\n"
"ds_read_b128 v[68:71], v3 offset:41984\n"
"v_mfma_f32_16x16x32_bf16 v[32:35], v[228:231], v[72:75], v[32:35]\n"
"v_mfma_f32_16x16x32_bf16 v[52:55], v[72:75], v[72:75], v[52:55]\n"
"v_mfma_f32_16x16x32_bf16 v[28:31], v[232:235], v[76:79], v[28:31]\n"
"ds_read_b128 v[72:75], v3 offset:43008\n"
"v_mfma_f32_16x16x32_bf16 v[32:35], v[236:239], v[76:79], v[32:35]\n"
"s_waitcnt lgkmcnt(3)\n"
"v_mfma_f32_16x16x32_bf16 v[28:31], v[240:243], v[56:59], v[28:31]\n"
"ds_read_b128 v[76:79], v3 offset:44032\n"
"v_cndmask_b32 v94, v52, v53, s[34:35]\n"
"v_mfma_f32_16x16x32_bf16 v[32:35], v[244:247], v[56:59], v[32:35]\n"
"v_cndmask_b32 v54, v54, v55, s[34:35]\n"
"v_cndmask_b32 v94, v94, v54, s[20:21]\n"
"v_mfma_f32_16x16x32_bf16 v[28:31], v[248:251], v[60:63], v[28:31]\n"
"ds_read_b128 v[56:59], v3 offset:45056\n"
"v_mfma_f32_16x16x32_bf16 v[32:35], v[252:255], v[60:63], v[32:35]\n"
"s_waitcnt lgkmcnt(3)\n"
"v_mfma_f32_16x16x32_bf16 v[36:39], v[96:99], v[64:67], 0\n"
"ds_read_b128 v[60:63], v3 offset:46080\n"
"v_mfma_f32_16x16x32_bf16 v[40:43], v[100:103], v[64:67], 0\n"
"v_cndmask_b32 v94, v28, v94, s[22:23]\n"
"s_waitcnt vmcnt(7)\n"
"v_mfma_f32_16x16x32_bf16 v[52:55], v[64:67], v[64:67], 0\n"
"ds_write_b32 v9, v94 offset:0\n"
"v_mfma_f32_16x16x32_bf16 v[36:39], v[104:107], v[68:71], v[36:39]\n"
"ds_read_b128 v[64:67], v3 offset:47104\n"
"v_mfma_f32_16x16x32_bf16 v[40:43], v[108:111], v[68:71], v[40:43]\n"
"s_waitcnt lgkmcnt(4)\n"
"v_mfma_f32_16x16x32_bf16 v[36:39], v[112:115], v[72:75], v[36:39]\n"
"ds_read_b128 v[68:71], v3 offset:48128\n"
"v_mfma_f32_16x16x32_bf16 v[40:43], v[116:119], v[72:75], v[40:43]\n"
"v_mfma_f32_16x16x32_bf16 v[36:39], v[120:123], v[76:79], v[36:39]\n"
"ds_read_b128 v[72:75], v3 offset:49152\n"
"v_mfma_f32_16x16x32_bf16 v[40:43], v[124:127], v[76:79], v[40:43]\n"
"s_waitcnt lgkmcnt(4)\n"
"v_mfma_f32_16x16x32_bf16 v[36:39], v[128:131], v[56:59], v[36:39]\n"
"ds_read_b128 v[76:79], v3 offset:50176\n"
"v_mfma_f32_16x16x32_bf16 v[40:43], v[132:135], v[56:59], v[40:43]\n"
"v_mfma_f32_16x16x32_bf16 v[52:55], v[56:59], v[56:59], v[52:55]\n"
"v_mfma_f32_16x16x32_bf16 v[36:39], v[136:139], v[60:63], v[36:39]\n"
"ds_read_b128 v[56:59], v3 offset:51200\n"
"v_mfma_f32_16x16x32_bf16 v[40:43], v[140:143], v[60:63], v[40:43]\n"
"s_waitcnt lgkmcnt(3)\n"
"v_mfma_f32_16x16x32_bf16 v[36:39], v[144:147], v[64:67], v[36:39]\n"
"ds_read_b128 v[60:63], v3 offset:52224\n"
"ds_read_b128 v[84:87], v11 offset:0\n"
"ds_read_b32 v91, v12 offset:0\n"
"v_mfma_f32_16x16x32_bf16 v[40:43], v[148:151], v[64:67], v[40:43]\n"
"v_mfma_f32_16x16x32_bf16 v[36:39], v[152:155], v[68:71], v[36:39]\n"
"ds_read_b128 v[64:67], v3 offset:53248\n"
"v_mfma_f32_16x16x32_bf16 v[40:43], v[156:159], v[68:71], v[40:43]\n"
"s_waitcnt lgkmcnt(5)\n"
"v_mfma_f32_16x16x32_bf16 v[36:39], v[160:163], v[72:75], v[36:39]\n"
"ds_read_b128 v[68:71], v3 offset:54272\n"
"v_mfma_f32_16x16x32_bf16 v[40:43], v[164:167], v[72:75], v[40:43]\n"
"v_mfma_f32_16x16x32_bf16 v[52:55], v[72:75], v[72:75], v[52:55]\n"
"v_mfma_f32_16x16x32_bf16 v[36:39], v[168:171], v[76:79], v[36:39]\n"
"ds_read_b128 v[72:75], v3 offset:55296\n"
"s_waitcnt lgkmcnt(4)\n"
"v_add_f32 v84, v84, v85\n"
"v_mfma_f32_16x16x32_bf16 v[40:43], v[172:175], v[76:79], v[40:43]\n"
"v_add_f32 v86, v86, v87\n"
"v_mfma_f32_16x16x32_bf16 v[36:39], v[176:179], v[56:59], v[36:39]\n"
"ds_read_b128 v[76:79], v3 offset:56320\n"
"v_add_f32 v84, v84, v86\n"
"v_mfma_f32_16x16x32_bf16 v[40:43], v[180:183], v[56:59], v[40:43]\n"
"s_waitcnt lgkmcnt(4)\n"
"v_mul_f32 v89, 0x3acccccd, v91\n"
"v_fma_f32 v84, -v91, v89, v84\n"
"v_mfma_f32_16x16x32_bf16 v[36:39], v[184:187], v[60:63], v[36:39]\n"
"ds_read_b128 v[56:59], v3 offset:57344\n"
"v_fmamk_f32 v84, v84, 0x3acccccd, v27\n"
"v_mfma_f32_16x16x32_bf16 v[40:43], v[188:191], v[60:63], v[40:43]\n"
"v_cmp_u_f32 vcc, v84, v84\n"
"s_waitcnt lgkmcnt(3)\n"
"v_mfma_f32_16x16x32_bf16 v[36:39], v[192:195], v[64:67], v[36:39]\n"
"ds_read_b128 v[60:63], v4 offset:61440\n"
"v_max_f32 v84, v84, v27\n"
"v_mfma_f32_16x16x32_bf16 v[40:43], v[196:199], v[64:67], v[40:43]\n"
"v_rsq_f32 v88, v84\n"
"v_mfma_f32_16x16x32_bf16 v[52:55], v[64:67], v[64:67], v[52:55]\n"
"v_mul_f32 v89, -v89, v88\n"
"v_mfma_f32_16x16x32_bf16 v[36:39], v[200:203], v[68:71], v[36:39]\n"
"ds_read_b128 v[64:67], v5 offset:61440\n"
"s_cbranch_vccnz .Lsl_38\n"
".Lbk_39:\n"
"v_mfma_f32_16x16x32_bf16 v[40:43], v[204:207], v[68:71], v[40:43]\n"
"s_add_u32 s31, s28, 0x13b000\n"
"s_waitcnt lgkmcnt(3)\n"
"v_mfma_f32_16x16x32_bf16 v[36:39], v[208:211], v[72:75], v[36:39]\n"
"ds_read_b128 v[68:71], v6 offset:61440\n"
"s_add_u32 m0, s29, 0xa000\n"
"v_mfma_f32_16x16x32_bf16 v[40:43], v[212:215], v[72:75], v[40:43]\n"
"v_mul_f32 v28, v88, v28\n"
"v_mul_f32 v29, v88, v29\n"
"v_mfma_f32_16x16x32_bf16 v[36:39], v[216:219], v[76:79], v[36:39]\n"
"v_add_u32 v3, 0xffffb000, v3\n"
"ds_read_b128 v[72:75], v3 offset:0\n"
"buffer_load_dwordx4 v2, s[8:11], s31 offen offset:0 lds\n"
"v_mfma_f32_16x16x32_bf16 v[40:43], v[220:223], v[76:79], v[40:43]\n"
"s_add_u32 m0, s29, 0xa3c0\n"
"s_waitcnt lgkmcnt(3)\n"
"v_mfma_f32_16x16x32_bf16 v[36:39], v[224:227], v[56:59], v[36:39]\n"
"ds_read_b128 v[76:79], v3 offset:1024\n"
"v_mul_f32 v30, v88, v30\n"
"v_mfma_f32_16x16x32_bf16 v[40:43], v[228:231], v[56:59], v[40:43]\n"
"v_mul_f32 v31, v88, v31\n"
"v_fmac_f32 v28, v89, v16\n"
"v_mfma_f32_16x16x32_bf16 v[52:55], v[56:59], v[56:59], v[52:55]\n"
"buffer_load_dwordx4 v2, s[8:11], s31 offen offset:64 lds\n"
"v_fmac_f32 v29, v89, v17\n"
"v_mfma_f32_16x16x32_bf16 v[36:39], v[232:235], v[60:63], v[36:39]\n"
"ds_read_b128 v[56:59], v3 offset:2048\n"
"s_add_u32 m0, s29, 0xa780\n"
"v_mfma_f32_16x16x32_bf16 v[40:43], v[236:239], v[60:63], v[40:43]\n"
"v_fmac_f32 v30, v89, v18\n"
"s_waitcnt lgkmcnt(3)\n"
"v_mfma_f32_16x16x32_bf16 v[36:39], v[240:243], v[64:67], v[36:39]\n"
"ds_read_b128 v[60:63], v3 offset:3072\n"
"v_cndmask_b32 v94, v52, v53, s[34:35]\n"
"v_mfma_f32_16x16x32_bf16 v[40:43], v[244:247], v[64:67], v[40:43]\n"
"v_cndmask_b32 v54, v54, v55, s[34:35]\n"
"v_cndmask_b32 v94, v94, v54, s[20:21]\n"
"buffer_load_dwordx4 v2, s[8:11], s31 offen offset:128 lds\n"
"v_mfma_f32_16x16x32_bf16 v[36:39], v[248:251], v[68:71], v[36:39]\n"
"ds_read_b128 v[64:67], v3 offset:4096\n"
"s_add_u32 m0, s29, 0xab40\n"
"v_mfma_f32_16x16x32_bf16 v[40:43], v[252:255], v[68:71], v[40:43]\n"
"v_fmac_f32 v31, v89, v19\n"
"s_waitcnt lgkmcnt(3)\n"
"v_mfma_f32_16x16x32_bf16 v[44:47], v[96:99], v[72:75], 0\n"
"ds_read_b128 v[68:71], v3 offset:5120\n"
"v_cvt_pk_bf16_f32 v28, v28, v29\n"
"v_mfma_f32_16x16x32_bf16 v[48:51], v[100:103], v[72:75], 0\n"
"v_cndmask_b32 v94, v36, v94, s[22:23]\n"
"s_waitcnt vmcnt(3)\n"
"buffer_load_dwordx4 v2, s[8:11], s31 offen offset:192 lds\n"
"v_mfma_f32_16x16x32_bf16 v[52:55], v[72:75], v[72:75], 0\n"
"ds_write_b32 v9, v94 offset:320\n"
"v_mfma_f32_16x16x32_bf16 v[44:47], v[104:107], v[76:79], v[44:47]\n"
"ds_read_b128 v[72:75], v3 offset:6144\n"
"s_add_u32 m0, s29, 0xaf00\n"
"v_mfma_f32_16x16x32_bf16 v[48:51], v[108:111], v[76:79], v[48:51]\n"
"v_cvt_pk_bf16_f32 v29, v30, v31\n"
"s_waitcnt lgkmcnt(4)\n"
"v_mfma_f32_16x16x32_bf16 v[44:47], v[112:115], v[56:59], v[44:47]\n"
"ds_read_b128 v[76:79], v3 offset:7168\n"
"buffer_load_dwordx4 v2, s[8:11], s31 offen offset:256 lds\n"
"v_mfma_f32_16x16x32_bf16 v[48:51], v[116:119], v[56:59], v[48:51]\n"
"v_mul_f32 v32, v88, v32\n"
"v_mul_f32 v33, v88, v33\n"
"v_mfma_f32_16x16x32_bf16 v[44:47], v[120:123], v[60:63], v[44:47]\n"
"ds_read_b128 v[56:59], v3 offset:8192\n"
"v_mul_f32 v34, v88, v34\n"
"v_mfma_f32_16x16x32_bf16 v[48:51], v[124:127], v[60:63], v[48:51]\n"
"v_mul_f32 v35, v88, v35\n"
"s_waitcnt lgkmcnt(4)\n"
"v_mfma_f32_16x16x32_bf16 v[44:47], v[128:131], v[64:67], v[44:47]\n"
"ds_read_b128 v[60:63], v3 offset:9216\n"
"v_fmac_f32 v32, v89, v20\n"
"v_mfma_f32_16x16x32_bf16 v[48:51], v[132:135], v[64:67], v[48:51]\n"
"v_fmac_f32 v33, v89, v21\n"
"v_fmac_f32 v34, v89, v22\n"
"v_mfma_f32_16x16x32_bf16 v[52:55], v[64:67], v[64:67], v[52:55]\n"
"v_fmac_f32 v35, v89, v23\n"
"v_cvt_pk_bf16_f32 v32, v32, v33\n"
"v_mfma_f32_16x16x32_bf16 v[44:47], v[136:139], v[68:71], v[44:47]\n"
"ds_read_b128 v[64:67], v3 offset:10240\n"
"v_cvt_pk_bf16_f32 v33, v34, v35\n"
"v_mfma_f32_16x16x32_bf16 v[48:51], v[140:143], v[68:71], v[48:51]\n"
"s_waitcnt lgkmcnt(3)\n"
"v_mfma_f32_16x16x32_bf16 v[44:47], v[144:147], v[72:75], v[44:47]\n"
"ds_read_b128 v[68:71], v3 offset:11264\n"
"ds_read_b128 v[84:87], v11 offset:320\n"
"ds_read_b32 v91, v12 offset:320\n"
"v_mfma_f32_16x16x32_bf16 v[48:51], v[148:151], v[72:75], v[48:51]\n"
"v_mfma_f32_16x16x32_bf16 v[44:47], v[152:155], v[76:79], v[44:47]\n"
"ds_read_b128 v[72:75], v3 offset:12288\n"
"v_mfma_f32_16x16x32_bf16 v[48:51], v[156:159], v[76:79], v[48:51]\n"
"s_waitcnt lgkmcnt(5)\n"
"v_mfma_f32_16x16x32_bf16 v[44:47], v[160:163], v[56:59], v[44:47]\n"
"ds_read_b128 v[76:79], v3 offset:13312\n"
"v_mfma_f32_16x16x32_bf16 v[48:51], v[164:167], v[56:59], v[48:51]\n"
"v_mfma_f32_16x16x32_bf16 v[52:55], v[56:59], v[56:59], v[52:55]\n"
"v_mfma_f32_16x16x32_bf16 v[44:47], v[168:171], v[60:63], v[44:47]\n"
"ds_read_b128 v[56:59], v3 offset:14336\n"
"s_waitcnt lgkmcnt(4)\n"
"v_add_f32 v84, v84, v85\n"
"v_mfma_f32_16x16x32_bf16 v[48:51], v[172:175], v[60:63], v[48:51]\n"
"v_add_f32 v86, v86, v87\n"
"v_mfma_f32_16x16x32_bf16 v[44:47], v[176:179], v[64:67], v[44:47]\n"
"ds_read_b128 v[60:63], v3 offset:15360\n"
"v_add_f32 v84, v84, v86\n"
"v_mfma_f32_16x16x32_bf16 v[48:51], v[180:183], v[64:67], v[48:51]\n"
"s_waitcnt lgkmcnt(4)\n"
"v_mul_f32 v89, 0x3acccccd, v91\n"
"v_fma_f32 v84, -v91, v89, v84\n"
"v_mfma_f32_16x16x32_bf16 v[44:47], v[184:187], v[68:71], v[44:47]\n"
"ds_read_b128 v[64:67], v3 offset:16384\n"
"v_fmamk_f32 v84, v84, 0x3acccccd, v27\n"
"v_mfma_f32_16x16x32_bf16 v[48:51], v[188:191], v[68:71], v[48:51]\n"
"v_cmp_u_f32 vcc, v84, v84\n"
"s_waitcnt lgkmcnt(3)\n"
"v_mfma_f32_16x16x32_bf16 v[44:47], v[192:195], v[72:75], v[44:47]\n"
"ds_read_b128 v[68:71], v4 offset:0\n"
"v_max_f32 v84, v84, v27\n"
"v_mfma_f32_16x16x32_bf16 v[48:51], v[196:199], v[72:75], v[48:51]\n"
"v_rsq_f32 v88, v84\n"
"v_mfma_f32_16x16x32_bf16 v[52:55], v[72:75], v[72:75], v[52:55]\n"
"v_mul_f32 v89, -v89, v88\n"
"v_mfma_f32_16x16x32_bf16 v[44:47], v[200:203], v[76:79], v[44:47]\n"
"ds_read_b128 v[72:75], v5 offset:0\n"
"s_cbranch_vccnz .Lsl_42\n"
".Lbk_43:\n"
"v_mfma_f32_16x16x32_bf16 v[48:51], v[204:207], v[76:79], v[48:51]\n"
"ds_write_b64 v7, v[28:29] offset:44928\n"
"s_waitcnt lgkmcnt(4)\n"
"v_mfma_f32_16x16x32_bf16 v[44:47], v[208:211], v[56:59], v[44:47]\n"
"ds_read_b128 v[76:79], v6 offset:0\n"
"v_mul_f32 v36, v88, v36\n"
"v_mfma_f32_16x16x32_bf16 v[48:51], v[212:215], v[56:59], v[48:51]\n"
"v_mul_f32 v37, v88, v37\n"
"v_mul_f32 v38, v88, v38\n"
"v_mfma_f32_16x16x32_bf16 v[44:47], v[216:219], v[60:63], v[44:47]\n"
"ds_read_b128 v[56:59], v3 offset:20480\n"
"v_mul_f32 v39, v88, v39\n"
"v_mfma_f32_16x16x32_bf16 v[48:51], v[220:223], v[60:63], v[48:51]\n"
"v_fmac_f32 v36, v89, v16\n"
"s_waitcnt lgkmcnt(4)\n"
"v_mfma_f32_16x16x32_bf16 v[44:47], v[224:227], v[64:67], v[44:47]\n"
"ds_read_b128 v[60:63], v3 offset:21504\n"
"v_fmac_f32 v37, v89, v17\n"
"v_mfma_f32_16x16x32_bf16 v[48:51], v[228:231], v[64:67], v[48:51]\n"
"v_mov_b32 v84, 0x3f803f80\n"
"v_mov_b32 v85, 0x3f803f80\n"
"v_mfma_f32_16x16x32_bf16 v[52:55], v[64:67], v[64:67], v[52:55]\n"
"v_mov_b32 v86, 0x3f803f80\n"
"v_mov_b32 v87, 0x3f803f80\n"
"ds_write_b64 v8, v[32:33] offset:44928\n"
"v_mfma_f32_16x16x32_bf16 v[44:47], v[232:235], v[68:71], v[44:47]\n"
"ds_read_b128 v[64:67], v3 offset:22528\n"
"v_fmac_f32 v38, v89, v18\n"
"v_mfma_f32_16x16x32_bf16 v[48:51], v[236:239], v[68:71], v[48:51]\n"
"v_fmac_f32 v39, v89, v19\n"
"s_waitcnt lgkmcnt(4)\n"
"v_mfma_f32_16x16x32_bf16 v[44:47], v[240:243], v[72:75], v[44:47]\n"
"ds_read_b128 v[68:71], v3 offset:23552\n"
"v_cndmask_b32 v94, v52, v53, s[34:35]\n"
"v_mfma_f32_16x16x32_bf16 v[48:51], v[244:247], v[72:75], v[48:51]\n"
"v_cndmask_b32 v54, v54, v55, s[34:35]\n"
"v_cndmask_b32 v94, v94, v54, s[20:21]\n"
"v_mfma_f32_16x16x32_bf16 v[44:47], v[248:251], v[76:79], v[44:47]\n"
"ds_read_b128 v[72:75], v3 offset:24576\n"
"v_cvt_pk_bf16_f32 v36, v36, v37\n"
"v_mfma_f32_16x16x32_bf16 v[48:51], v[252:255], v[76:79], v[48:51]\n"
"v_cvt_pk_bf16_f32 v37, v38, v39\n"
"s_waitcnt lgkmcnt(4)\n"
"v_mfma_f32_16x16x32_bf16 v[28:31], v[96:99], v[56:59], 0\n"
"ds_read_b128 v[76:79], v3 offset:25600\n"
"v_mul_f32 v40, v88, v40\n"
"v_mfma_f32_16x16x32_bf16 v[32:35], v[100:103], v[56:59], 0\n"
"v_cndmask_b32 v94, v44, v94, s[22:23]\n"
"s_waitcnt vmcnt(0)\n"
"ds_write_b128 v93, v[84:87] offset:40960\n"
"v_mfma_f32_16x16x32_bf16 v[52:55], v[56:59], v[56:59], 0\n"
"ds_write_b32 v9, v94 offset:640\n"
"v_mfma_f32_16x16x32_bf16 v[28:31], v[104:107], v[60:63], v[28:31]\n"
"ds_read_b128 v[56:59], v3 offset:26624\n"
"v_mul_f32 v41, v88, v41\n"
"v_mfma_f32_16x16x32_bf16 v[32:35], v[108:111], v[60:63], v[32:35]\n"
"v_mul_f32 v42, v88, v42\n"
"s_waitcnt lgkmcnt(5)\n"
"v_mfma_f32_16x16x32_bf16 v[28:31], v[112:115], v[64:67], v[28:31]\n"
"ds_read_b128 v[60:63], v3 offset:27648\n"
"v_mul_f32 v43, v88, v43\n"
"v_mfma_f32_16x16x32_bf16 v[32:35], v[116:119], v[64:67], v[32:35]\n"
"v_fmac_f32 v40, v89, v20\n"
"v_fmac_f32 v41, v89, v21\n"
"v_mfma_f32_16x16x32_bf16 v[28:31], v[120:123], v[68:71], v[28:31]\n"
"ds_read_b128 v[64:67], v3 offset:28672\n"
"v_fmac_f32 v42, v89, v22\n"
"v_mfma_f32_16x16x32_bf16 v[32:35], v[124:127], v[68:71], v[32:35]\n"
"v_fmac_f32 v43, v89, v23\n"
"s_waitcnt lgkmcnt(5)\n"
"v_mfma_f32_16x16x32_bf16 v[28:31], v[128:131], v[72:75], v[28:31]\n"
"ds_read_b128 v[68:71], v3 offset:29696\n"
"v_cvt_pk_bf16_f32 v40, v40, v41\n"
"v_mfma_f32_16x16x32_bf16 v[32:35], v[132:135], v[72:75], v[32:35]\n"
"v_cvt_pk_bf16_f32 v41, v42, v43\n"
"v_mfma_f32_16x16x32_bf16 v[52:55], v[72:75], v[72:75], v[52:55]\n"
"v_mfma_f32_16x16x32_bf16 v[28:31], v[136:139], v[76:79], v[28:31]\n"
"ds_read_b128 v[72:75], v3 offset:30720\n"
"v_mfma_f32_16x16x32_bf16 v[32:35], v[140:143], v[76:79], v[32:35]\n"
"s_waitcnt lgkmcnt(3)\n"
"v_mfma_f32_16x16x32_bf16 v[28:31], v[144:147], v[56:59], v[28:31]\n"
"ds_read_b128 v[76:79], v3 offset:31744\n"
"ds_read_b128 v[84:87], v11 offset:640\n"
"ds_read_b32 v91, v12 offset:640\n"
"v_mfma_f32_16x16x32_bf16 v[32:35], v[148:151], v[56:59], v[32:35]\n"
"v_mfma_f32_16x16x32_bf16 v[28:31], v[152:155], v[60:63], v[28:31]\n"
"ds_read_b128 v[56:59], v3 offset:32768\n"
"v_mfma_f32_16x16x32_bf16 v[32:35], v[156:159], v[60:63], v[32:35]\n"
"s_waitcnt lgkmcnt(5)\n"
"v_mfma_f32_16x16x32_bf16 v[28:31], v[160:163], v[64:67], v[28:31]\n"
"ds_read_b128 v[60:63], v3 offset:33792\n"
"v_mfma_f32_16x16x32_bf16 v[32:35], v[164:167], v[64:67], v[32:35]\n"
"v_mfma_f32_16x16x32_bf16 v[52:55], v[64:67], v[64:67], v[52:55]\n"
"v_mfma_f32_16x16x32_bf16 v[28:31], v[168:171], v[68:71], v[28:31]\n"
"ds_read_b128 v[64:67], v3 offset:34816\n"
"s_waitcnt lgkmcnt(4)\n"
"v_add_f32 v84, v84, v85\n"
"v_mfma_f32_16x16x32_bf16 v[32:35], v[172:175], v[68:71], v[32:35]\n"
"v_add_f32 v86, v86, v87\n"
"v_mfma_f32_16x16x32_bf16 v[28:31], v[176:179], v[72:75], v[28:31]\n"
"ds_read_b128 v[68:71], v3 offset:35840\n"
"v_add_f32 v84, v84, v86\n"
"v_mfma_f32_16x16x32_bf16 v[32:35], v[180:183], v[72:75], v[32:35]\n"
"s_waitcnt lgkmcnt(4)\n"
"v_mul_f32 v89, 0x3acccccd, v91\n"
"v_fma_f32 v84, -v91, v89, v84\n"
"v_mfma_f32_16x16x32_bf16 v[28:31], v[184:187], v[76:79], v[28:31]\n"
"ds_read_b128 v[72:75], v3 offset:36864\n"
"v_fmamk_f32 v84, v84, 0x3acccccd, v27\n"
"v_mfma_f32_16x16x32_bf16 v[32:35], v[188:191], v[76:79], v[32:35]\n"
"v_cmp_u_f32 vcc, v84, v84\n"
"s_waitcnt lgkmcnt(3)\n"
"v_mfma_f32_16x16x32_bf16 v[28:31], v[192:195], v[56:59], v[28:31]\n"
"ds_read_b128 v[76:79], v4 offset:20480\n"
"v_max_f32 v84, v84, v27\n"
"v_mfma_f32_16x16x32_bf16 v[32:35], v[196:199], v[56:59], v[32:35]\n"
"v_rsq_f32 v88, v84\n"
"v_mfma_f32_16x16x32_bf16 v[52:55], v[56:59], v[56:59], v[52:55]\n"
"v_mul_f32 v89, -v89, v88\n"
"v_mfma_f32_16x16x32_bf16 v[28:31], v[200:203], v[60:63], v[28:31]\n"
"ds_read_b128 v[56:59], v5 offset:20480\n"
"s_cbranch_vccnz .Lsl_46\n"
".Lbk_47:\n"
"v_mfma_f32_16x16x32_bf16 v[32:35], v[204:207], v[60:63], v[32:35]\n"
"ds_write_b64 v7, v[36:37] offset:48896\n"
"s_waitcnt lgkmcnt(4)\n"
"v_mfma_f32_16x16x32_bf16 v[28:31], v[208:211], v[64:67], v[28:31]\n"
"ds_read_b128 v[60:63], v6 offset:20480\n"
"v_mul_f32 v44, v88, v44\n"
"v_mul_f32 v45, v88, v45\n"
"v_mfma_f32_16x16x32_bf16 v[32:35], v[212:215], v[64:67], v[32:35]\n"
"v_mul_f32 v46, v88, v46\n"
"v_mul_f32 v47, v88, v47\n"
"v_fmac_f32 v44, v89, v16\n"
"v_mfma_f32_16x16x32_bf16 v[28:31], v[216:219], v[68:71], v[28:31]\n"
"ds_read_b128 v[64:67], v3 offset:40960\n"
"v_fmac_f32 v45, v89, v17\n"
"v_fmac_f32 v46, v89, v18\n"
"v_mfma_f32_16x16x32_bf16 v[32:35], v[220:223], v[68:71], v[32:35]\n"
"v_fmac_f32 v47, v89, v19\n"
"s_waitcnt lgkmcnt(4)\n"
"v_mfma_f32_16x16x32_bf16 v[28:31], v[224:227], v[72:75], v[28:31]\n"
"ds_read_b128 v[68:71], v3 offset:45056\n"
"v_cvt_pk_bf16_f32 v44, v44, v45\n"
"v_mfma_f32_16x16x32_bf16 v[32:35], v[228:231], v[72:75], v[32:35]\n"
"v_cvt_pk_bf16_f32 v45, v46, v47\n"
"v_mul_f32 v48, v88, v48\n"
"v_mfma_f32_16x16x32_bf16 v[52:55], v[72:75], v[72:75], v[52:55]\n"
"ds_write_b64 v8, v[40:41] offset:48896\n"
"v_mfma_f32_16x16x32_bf16 v[28:31], v[232:235], v[76:79], v[28:31]\n"
"ds_read_b128 v[72:75], v3 offset:49152\n"
"v_mul_f32 v49, v88, v49\n"
"v_mfma_f32_16x16x32_bf16 v[32:35], v[236:239], v[76:79], v[32:35]\n"
"v_mul_f32 v50, v88, v50\n"
"s_waitcnt lgkmcnt(4)\n"
"v_mfma_f32_16x16x32_bf16 v[28:31], v[240:243], v[56:59], v[28:31]\n"
"ds_read_b128 v[76:79], v3 offset:53248\n"
"v_cndmask_b32 v94, v52, v53, s[34:35]\n"
"v_mfma_f32_16x16x32_bf16 v[32:35], v[244:247], v[56:59], v[32:35]\n"
"v_cndmask_b32 v54, v54, v55, s[34:35]\n"
"v_cndmask_b32 v94, v94, v54, s[20:21]\n"
"v_mfma_f32_16x16x32_bf16 v[28:31], v[248:251], v[60:63], v[28:31]\n"
"ds_read_b128 v[56:59], v3 offset:57344\n"
"v_mul_f32 v51, v88, v51\n"
"v_mfma_f32_16x16x32_bf16 v[32:35], v[252:255], v[60:63], v[32:35]\n"
"v_fmac_f32 v48, v89, v20\n"
"s_waitcnt lgkmcnt(4)\n"
"s_cmp_eq_u32 s3, 3\n"
"s_cbranch_scc1 .Lfu_58\n"
"ds_read_b128 v[60:63], v3 offset:41984\n"
"v_fmac_f32 v49, v89, v21\n"
"v_mfma_f32_16x16x32_bf16 v[40:43], v[100:103], v[64:67], 0\n"
"v_cndmask_b32 v94, v28, v94, s[22:23]\n"
"v_fmac_f32 v50, v89, v22\n"
"v_mfma_f32_16x16x32_bf16 v[52:55], v[64:67], v[64:67], 0\n"
"ds_write_b32 v9, v94 offset:960\n"
"ds_read_b128 v[64:67], v3 offset:43008\n"
"v_fmac_f32 v51, v89, v23\n"
"v_mfma_f32_16x16x32_bf16 v[40:43], v[132:135], v[68:71], v[40:43]\n"
"v_cvt_pk_bf16_f32 v48, v48, v49\n"
"v_cvt_pk_bf16_f32 v49, v50, v51\n"
"v_mfma_f32_16x16x32_bf16 v[52:55], v[68:71], v[68:71], v[52:55]\n"
"ds_write_b64 v7, v[44:45] offset:52864\n"
"s_waitcnt lgkmcnt(5)\n"
"ds_read_b128 v[68:71], v3 offset:44032\n"
"v_mfma_f32_16x16x32_bf16 v[40:43], v[164:167], v[72:75], v[40:43]\n"
"v_mfma_f32_16x16x32_bf16 v[52:55], v[72:75], v[72:75], v[52:55]\n"
"ds_write_b64 v8, v[48:49] offset:52864\n"
"ds_read_b128 v[72:75], v3 offset:46080\n"
"v_mfma_f32_16x16x32_bf16 v[40:43], v[196:199], v[76:79], v[40:43]\n"
"v_mfma_f32_16x16x32_bf16 v[52:55], v[76:79], v[76:79], v[52:55]\n"
"ds_read_b128 v[84:87], v11 offset:960\n"
"ds_read_b32 v91, v12 offset:960\n"
"s_waitcnt lgkmcnt(8)\n"
"ds_read_b128 v[76:79], v3 offset:47104\n"
"v_mfma_f32_16x16x32_bf16 v[40:43], v[228:231], v[56:59], v[40:43]\n"
"v_mfma_f32_16x16x32_bf16 v[52:55], v[56:59], v[56:59], v[52:55]\n"
"ds_read_b128 v[56:59], v3 offset:48128\n"
"v_mfma_f32_16x16x32_bf16 v[40:43], v[108:111], v[60:63], v[40:43]\n"
"s_waitcnt lgkmcnt(6)\n"
"ds_read_b128 v[60:63], v3 offset:50176\n"
"s_nop 2\n"
"ds_write_b32 v10, v52 offset:1600\n"
"v_cndmask_b32 v94, v52, v53, s[34:35]\n"
"v_cndmask_b32 v54, v54, v55, s[34:35]\n"
"v_mfma_f32_16x16x32_bf16 v[40:43], v[116:119], v[64:67], v[40:43]\n"
"s_waitcnt lgkmcnt(5)\n"
"v_add_f32 v84, v84, v85\n"
"v_add_f32 v86, v86, v87\n"
"v_add_f32 v84, v84, v86\n"
"v_cndmask_b32 v94, v94, v54, s[20:21]\n"
"ds_write_b32 v9, v94 offset:1280\n"
"ds_read_b128 v[64:67], v3 offset:51200\n"
"s_waitcnt lgkmcnt(6)\n"
"v_mul_f32 v89, 0x3acccccd, v91\n"
"v_fma_f32 v84, -v91, v89, v84\n"
"v_mfma_f32_16x16x32_bf16 v[40:43], v[124:127], v[68:71], v[40:43]\n"
"v_fmamk_f32 v84, v84, 0x3acccccd, v27\n"
"s_waitcnt lgkmcnt(5)\n"
"ds_read_b128 v[68:71], v3 offset:52224\n"
"v_cmp_u_f32 vcc, v84, v84\n"
"v_mfma_f32_16x16x32_bf16 v[40:43], v[140:143], v[72:75], v[40:43]\n"
"v_max_f32 v84, v84, v27\n"
"v_rsq_f32 v88, v84\n"
"ds_read_b128 v[72:75], v3 offset:54272\n"
"v_mul_f32 v89, -v89, v88\n"
"v_mfma_f32_16x16x32_bf16 v[40:43], v[148:151], v[76:79], v[40:43]\n"
"s_cbranch_vccnz .Lsl_50c\n"
".Lbk_51c:\n"
"s_waitcnt lgkmcnt(5)\n"
"ds_read_b128 v[76:79], v3 offset:55296\n"
"v_mul_f32 v28, v88, v28\n"
"v_mul_f32 v29, v88, v29\n"
"v_mfma_f32_16x16x32_bf16 v[40:43], v[156:159], v[56:59], v[40:43]\n"
"ds_read_b128 v[44:47], v11 offset:1280\n"
"ds_read_b128 v[48:51], v11 offset:1600\n"
"v_mul_f32 v30, v88, v30\n"
"ds_read_b128 v[56:59], v3 offset:56320\n"
"v_mul_f32 v31, v88, v31\n"
"v_fmac_f32 v28, v89, v16\n"
"v_mfma_f32_16x16x32_bf16 v[40:43], v[172:175], v[60:63], v[40:43]\n"
"v_fmac_f32 v29, v89, v17\n"
"v_fmac_f32 v30, v89, v18\n"
"s_waitcnt lgkmcnt(5)\n"
"ds_read_b128 v[60:63], v4 offset:40960\n"
"v_fmac_f32 v31, v89, v19\n"
"v_cvt_pk_bf16_f32 v28, v28, v29\n"
"v_mfma_f32_16x16x32_bf16 v[40:43], v[180:183], v[64:67], v[40:43]\n"
"v_cvt_pk_bf16_f32 v29, v30, v31\n"
"ds_write_b64 v7, v[28:29] offset:56832\n"
"ds_read_b128 v[64:67], v5 offset:40960\n"
"v_mul_f32 v32, v88, v32\n"
"v_mul_f32 v33, v88, v33\n"
"v_mfma_f32_16x16x32_bf16 v[40:43], v[188:191], v[68:71], v[40:43]\n"
"v_mul_f32 v34, v88, v34\n"
"v_mul_f32 v35, v88, v35\n"
"s_waitcnt lgkmcnt(6)\n"
"ds_read_b128 v[68:71], v6 offset:40960\n"
"s_waitcnt lgkmcnt(6)\n"
"v_add_f32 v44, v44, v45\n"
"v_fmac_f32 v32, v89, v20\n"
"v_mfma_f32_16x16x32_bf16 v[40:43], v[204:207], v[72:75], v[40:43]\n"
"v_add_f32 v46, v46, v47\n"
"s_waitcnt lgkmcnt(5)\n"
"v_add_f32 v48, v48, v49\n"
"v_fmac_f32 v33, v89, v21\n"
"v_add_f32 v50, v50, v51\n"
"v_add_f32 v48, v48, v50\n"
"v_fmac_f32 v34, v89, v22\n"
"v_mfma_f32_16x16x32_bf16 v[40:43], v[212:215], v[76:79], v[40:43]\n"
"v_add_f32 v44, v44, v46\n"
"v_fmac_f32 v35, v89, v23\n"
"s_waitcnt lgkmcnt(3)\n"
"v_mul_f32 v53, 0x3acccccd, v48\n"
"v_fma_f32 v44, -v48, v53, v44\n"
"v_cvt_pk_bf16_f32 v32, v32, v33\n"
"v_mfma_f32_16x16x32_bf16 v[40:43], v[220:223], v[56:59], v[40:43]\n"
"v_fmamk_f32 v44, v44, 0x3acccccd, v27\n"
"v_cmp_u_f32 vcc, v44, v44\n"
"v_max_f32 v44, v44, v27\n"
"v_rsq_f32 v52, v44\n"
"v_mfma_f32_16x16x32_bf16 v[40:43], v[236:239], v[60:63], v[40:43]\n"
"v_mul_f32 v53, -v53, v52\n"
"s_waitcnt lgkmcnt(0)\n"
"s_cbranch_vccnz .Lsl_54c\n"
".Lbk_55c:\n"
"v_mfma_f32_16x16x32_bf16 v[40:43], v[244:247], v[64:67], v[40:43]\n"
"v_cvt_pk_bf16_f32 v33, v34, v35\n"
"ds_write_b64 v8, v[32:33] offset:56832\n"
"v_mfma_f32_16x16x32_bf16 v[40:43], v[252:255], v[68:71], v[40:43]\n"
"s_nop 7\n"
"v_mul_f32 v40, v52, v40\n"
"v_mul_f32 v41, v52, v41\n"
"v_mul_f32 v42, v52, v42\n"
"v_mul_f32 v43, v52, v43\n"
"v_fmac_f32 v40, v53, v20\n"
"v_fmac_f32 v41, v53, v21\n"
"v_fmac_f32 v42, v53, v22\n"
"v_fmac_f32 v43, v53, v23\n"
"v_cvt_pk_bf16_f32 v40, v40, v41\n"
"v_cvt_pk_bf16_f32 v41, v42, v43\n"
"ds_write_b64 v8, v[40:41] offset:40960\n"
"ds_write_b32 v13, v13 offset:144\n"
"s_branch .Ljn_59\n"
".Lfu_58:\n"
"s_nop 0\n"
"v_mfma_f32_16x16x32_bf16 v[36:39], v[96:99], v[64:67], 0\n"
"ds_read_b128 v[60:63], v3 offset:41984\n"
"v_fmac_f32 v49, v89, v21\n"
"v_mfma_f32_16x16x32_bf16 v[40:43], v[100:103], v[64:67], 0\n"
"v_cndmask_b32 v94, v28, v94, s[22:23]\n"
"v_fmac_f32 v50, v89, v22\n"
"v_mfma_f32_16x16x32_bf16 v[52:55], v[64:67], v[64:67], 0\n"
"ds_write_b32 v9, v94 offset:960\n"
"v_mfma_f32_16x16x32_bf16 v[36:39], v[128:131], v[68:71], v[36:39]\n"
"ds_read_b128 v[64:67], v3 offset:43008\n"
"v_fmac_f32 v51, v89, v23\n"
"v_mfma_f32_16x16x32_bf16 v[40:43], v[132:135], v[68:71], v[40:43]\n"
"v_cvt_pk_bf16_f32 v48, v48, v49\n"
"v_cvt_pk_bf16_f32 v49, v50, v51\n"
"v_mfma_f32_16x16x32_bf16 v[52:55], v[68:71], v[68:71], v[52:55]\n"
"ds_write_b64 v7, v[44:45] offset:52864\n"
"s_waitcnt lgkmcnt(5)\n"
"v_mfma_f32_16x16x32_bf16 v[36:39], v[160:163], v[72:75], v[36:39]\n"
"ds_read_b128 v[68:71], v3 offset:44032\n"
"v_mfma_f32_16x16x32_bf16 v[40:43], v[164:167], v[72:75], v[40:43]\n"
"v_mfma_f32_16x16x32_bf16 v[52:55], v[72:75], v[72:75], v[52:55]\n"
"ds_write_b64 v8, v[48:49] offset:52864\n"
"v_mfma_f32_16x16x32_bf16 v[36:39], v[192:195], v[76:79], v[36:39]\n"
"ds_read_b128 v[72:75], v3 offset:46080\n"
"v_mfma_f32_16x16x32_bf16 v[40:43], v[196:199], v[76:79], v[40:43]\n"
"v_mfma_f32_16x16x32_bf16 v[52:55], v[76:79], v[76:79], v[52:55]\n"
"ds_read_b128 v[84:87], v11 offset:960\n"
"ds_read_b32 v91, v12 offset:960\n"
"s_waitcnt lgkmcnt(8)\n"
"v_mfma_f32_16x16x32_bf16 v[36:39], v[224:227], v[56:59], v[36:39]\n"
"ds_read_b128 v[76:79], v3 offset:47104\n"
"v_mfma_f32_16x16x32_bf16 v[40:43], v[228:231], v[56:59], v[40:43]\n"
"v_mfma_f32_16x16x32_bf16 v[52:55], v[56:59], v[56:59], v[52:55]\n"
"v_mfma_f32_16x16x32_bf16 v[36:39], v[104:107], v[60:63], v[36:39]\n"
"ds_read_b128 v[56:59], v3 offset:48128\n"
"v_mfma_f32_16x16x32_bf16 v[40:43], v[108:111], v[60:63], v[40:43]\n"
"s_waitcnt lgkmcnt(6)\n"
"v_mfma_f32_16x16x32_bf16 v[36:39], v[112:115], v[64:67], v[36:39]\n"
"ds_read_b128 v[60:63], v3 offset:50176\n"
"ds_write_b32 v10, v52 offset:1600\n"
"v_cndmask_b32 v94, v52, v53, s[34:35]\n"
"v_cndmask_b32 v54, v54, v55, s[34:35]\n"
"v_mfma_f32_16x16x32_bf16 v[40:43], v[116:119], v[64:67], v[40:43]\n"
"s_waitcnt lgkmcnt(5)\n"
"v_add_f32 v84, v84, v85\n"
"v_add_f32 v86, v86, v87\n"
"v_add_f32 v84, v84, v86\n"
"v_cndmask_b32 v94, v94, v54, s[20:21]\n"
"ds_write_b32 v9, v94 offset:1280\n"
"v_mfma_f32_16x16x32_bf16 v[36:39], v[120:123], v[68:71], v[36:39]\n"
"ds_read_b128 v[64:67], v3 offset:51200\n"
"s_waitcnt lgkmcnt(6)\n"
"v_mul_f32 v89, 0x3acccccd, v91\n"
"v_fma_f32 v84, -v91, v89, v84\n"
"v_mfma_f32_16x16x32_bf16 v[40:43], v[124:127], v[68:71], v[40:43]\n"
"v_fmamk_f32 v84, v84, 0x3acccccd, v27\n"
"s_waitcnt lgkmcnt(5)\n"
"v_mfma_f32_16x16x32_bf16 v[36:39], v[136:139], v[72:75], v[36:39]\n"
"ds_read_b128 v[68:71], v3 offset:52224\n"
"v_cmp_u_f32 vcc, v84, v84\n"
"v_mfma_f32_16x16x32_bf16 v[40:43], v[140:143], v[72:75], v[40:43]\n"
"v_max_f32 v84, v84, v27\n"
"v_rsq_f32 v88, v84\n"
"v_mfma_f32_16x16x32_bf16 v[36:39], v[144:147], v[76:79], v[36:39]\n"
"ds_read_b128 v[72:75], v3 offset:54272\n"
"v_mul_f32 v89, -v89, v88\n"
"v_mfma_f32_16x16x32_bf16 v[40:43], v[148:151], v[76:79], v[40:43]\n"
"s_cbranch_vccnz .Lsl_50\n"
".Lbk_51:\n"
"s_waitcnt lgkmcnt(5)\n"
"v_mfma_f32_16x16x32_bf16 v[36:39], v[152:155], v[56:59], v[36:39]\n"
"ds_read_b128 v[76:79], v3 offset:55296\n"
"v_mul_f32 v28, v88, v28\n"
"v_mul_f32 v29, v88, v29\n"
"v_mfma_f32_16x16x32_bf16 v[40:43], v[156:159], v[56:59], v[40:43]\n"
"ds_read_b128 v[44:47], v11 offset:1280\n"
"ds_read_b128 v[48:51], v11 offset:1600\n"
"v_mul_f32 v30, v88, v30\n"
"v_mfma_f32_16x16x32_bf16 v[36:39], v[168:171], v[60:63], v[36:39]\n"
"ds_read_b128 v[56:59], v3 offset:56320\n"
"v_mul_f32 v31, v88, v31\n"
"v_fmac_f32 v28, v89, v16\n"
"v_mfma_f32_16x16x32_bf16 v[40:43], v[172:175], v[60:63], v[40:43]\n"
"v_fmac_f32 v29, v89, v17\n"
"v_fmac_f32 v30, v89, v18\n"
"s_waitcnt lgkmcnt(5)\n"
"v_mfma_f32_16x16x32_bf16 v[36:39], v[176:179], v[64:67], v[36:39]\n"
"ds_read_b128 v[60:63], v4 offset:40960\n"
"v_fmac_f32 v31, v89, v19\n"
"v_cvt_pk_bf16_f32 v28, v28, v29\n"
"v_mfma_f32_16x16x32_bf16 v[40:43], v[180:183], v[64:67], v[40:43]\n"
"v_cvt_pk_bf16_f32 v29, v30, v31\n"
"ds_write_b64 v7, v[28:29] offset:56832\n"
"v_mfma_f32_16x16x32_bf16 v[36:39], v[184:187], v[68:71], v[36:39]\n"
"ds_read_b128 v[64:67], v5 offset:40960\n"
"v_mul_f32 v32, v88, v32\n"
"v_mul_f32 v33, v88, v33\n"
"v_mfma_f32_16x16x32_bf16 v[40:43], v[188:191], v[68:71], v[40:43]\n"
"v_mul_f32 v34, v88, v34\n"
"v_mul_f32 v35, v88, v35\n"
"s_waitcnt lgkmcnt(6)\n"
"v_mfma_f32_16x16x32_bf16 v[36:39], v[200:203], v[72:75], v[36:39]\n"
"ds_read_b128 v[68:71], v6 offset:40960\n"
"s_waitcnt lgkmcnt(6)\n"
"v_add_f32 v44, v44, v45\n"
"v_fmac_f32 v32, v89, v20\n"
"v_mfma_f32_16x16x32_bf16 v[40:43], v[204:207], v[72:75], v[40:43]\n"
"v_add_f32 v46, v46, v47\n"
"s_waitcnt lgkmcnt(5)\n"
"v_add_f32 v48, v48, v49\n"
"v_fmac_f32 v33, v89, v21\n"
"v_mfma_f32_16x16x32_bf16 v[36:39], v[208:211], v[76:79], v[36:39]\n"
"v_add_f32 v50, v50, v51\n"
"v_add_f32 v48, v48, v50\n"
"v_fmac_f32 v34, v89, v22\n"
"v_mfma_f32_16x16x32_bf16 v[40:43], v[212:215], v[76:79], v[40:43]\n"
"v_add_f32 v44, v44, v46\n"
"v_fmac_f32 v35, v89, v23\n"
"s_waitcnt lgkmcnt(3)\n"
"v_mfma_f32_16x16x32_bf16 v[36:39], v[216:219], v[56:59], v[36:39]\n"
"v_mul_f32 v53, 0x3acccccd, v48\n"
"v_fma_f32 v44, -v48, v53, v44\n"
"v_cvt_pk_bf16_f32 v32, v32, v33\n"
"v_mfma_f32_16x16x32_bf16 v[40:43], v[220:223], v[56:59], v[40:43]\n"
"v_fmamk_f32 v44, v44, 0x3acccccd, v27\n"
"v_cmp_u_f32 vcc, v44, v44\n"
"v_mfma_f32_16x16x32_bf16 v[36:39], v[232:235], v[60:63], v[36:39]\n"
"v_max_f32 v44, v44, v27\n"
"v_rsq_f32 v52, v44\n"
"v_mfma_f32_16x16x32_bf16 v[40:43], v[236:239], v[60:63], v[40:43]\n"
"v_mul_f32 v53, -v53, v52\n"
"s_waitcnt lgkmcnt(0)\n"
"v_mfma_f32_16x16x32_bf16 v[36:39], v[240:243], v[64:67], v[36:39]\n"
"s_cbranch_vccnz .Lsl_54\n"
".Lbk_55:\n"
"v_mfma_f32_16x16x32_bf16 v[40:43], v[244:247], v[64:67], v[40:43]\n"
"v_cvt_pk_bf16_f32 v33, v34, v35\n"
"ds_write_b64 v8, v[32:33] offset:56832\n"
"v_mfma_f32_16x16x32_bf16 v[36:39], v[248:251], v[68:71], v[36:39]\n"
"v_mfma_f32_16x16x32_bf16 v[40:43], v[252:255], v[68:71], v[40:43]\n"
"s_nop 4\n"
"v_mul_f32 v36, v52, v36\n"
"v_mul_f32 v37, v52, v37\n"
"v_mul_f32 v38, v52, v38\n"
"v_mul_f32 v39, v52, v39\n"
"v_fmac_f32 v36, v53, v16\n"
"v_fmac_f32 v37, v53, v17\n"
"v_fmac_f32 v38, v53, v18\n"
"v_fmac_f32 v39, v53, v19\n"
"v_cvt_pk_bf16_f32 v36, v36, v37\n"
"v_cvt_pk_bf16_f32 v37, v38, v39\n"
"ds_write_b64 v7, v[36:37] offset:40960\n"
"v_mul_f32 v40, v52, v40\n"
"v_mul_f32 v41, v52, v41\n"
"v_mul_f32 v42, v52, v42\n"
"v_mul_f32 v43, v52, v43\n"
"v_fmac_f32 v40, v53, v20\n"
"v_fmac_f32 v41, v53, v21\n"
"v_fmac_f32 v42, v53, v22\n"
"v_fmac_f32 v43, v53, v23\n"
"v_cvt_pk_bf16_f32 v40, v40, v41\n"
"v_cvt_pk_bf16_f32 v41, v42, v43\n"
"ds_write_b64 v8, v[40:41] offset:40960\n"
"ds_write_b32 v13, v13 offset:144\n"
".Ljn_59:\n"
"v_mov_b32 v46, 0\n"
"v_mov_b32 v47, 0\n"
"v_mov_b32 v50, 0\n"
"v_mov_b32 v51, 0\n"
"s_mov_b32 s17, 0x100000\n"
".Ltp_60:\n"
"ds_read_b32 v90, v14 offset:144\n"
"s_sub_u32 s17, s17, 1\n"
"s_cbranch_scc1 .Lou_61\n"
"s_waitcnt lgkmcnt(0)\n"
"v_cmp_eq_u32 vcc, 0, v90\n"
"s_cbranch_vccnz .Ltp_60\n"
".Lou_61:\n"
"s_waitcnt lgkmcnt(0)\n"
"s_mov_b64 exec, 0xffff\n"
"ds_read_b64 v[46:47], v24 offset:44960\n"
"ds_read_b64 v[50:51], v24 offset:45000\n"
"s_mov_b64 exec, -1\n"
"ds_read_b64 v[44:45], v25 offset:44928\n"
"ds_read_b64 v[56:57], v25 offset:41040\n"
"ds_read_b64 v[58:59], v24 offset:41072\n"
"ds_read_b64 v[64:65], v25 offset:45008\n"
"ds_read_b64 v[66:67], v24 offset:45040\n"
"ds_read_b64 v[48:49], v25 offset:44968\n"
"ds_read_b64 v[60:61], v25 offset:41080\n"
"ds_read_b64 v[62:63], v24 offset:41112\n"
"ds_read_b64 v[68:69], v25 offset:45048\n"
"ds_read_b64 v[70:71], v24 offset:45080\n"
"ds_read_b64_tr_b16 v[28:29], v26 offset:40960\n"
"ds_read_b64_tr_b16 v[30:31], v26 offset:44928\n"
"ds_read_b64_tr_b16 v[32:33], v26 offset:41000\n"
"s_waitcnt lgkmcnt(10)\n"
"v_mfma_f32_16x16x32_bf16 v[56:59], v[56:59], v[44:47], 0\n"
"s_waitcnt lgkmcnt(8)\n"
"v_mfma_f32_16x16x32_bf16 v[64:67], v[64:67], v[44:47], 0\n"
"ds_read_b64_tr_b16 v[34:35], v26 offset:44968\n"
"ds_read_b64_tr_b16 v[36:37], v26 offset:40992\n"
"ds_read_b64_tr_b16 v[38:39], v26 offset:44960\n"
"s_waitcnt lgkmcnt(8)\n"
"v_mfma_f32_16x16x32_bf16 v[60:63], v[60:63], v[48:51], 0\n"
"s_waitcnt lgkmcnt(6)\n"
"v_mfma_f32_16x16x32_bf16 v[68:71], v[68:71], v[48:51], 0\n"
"ds_read_b64_tr_b16 v[40:41], v26 offset:41032\n"
"ds_read_b64_tr_b16 v[42:43], v26 offset:45000\n"
"v_mov_b32 v84, 0x3f803f80\n"
"v_mov_b32 v85, 0x3f803f80\n"
"v_mov_b32 v86, 0x3f803f80\n"
"v_mov_b32 v87, 0x3f803f80\n"
"v_cndmask_b32 v76, v56, v57, s[34:35]\n"
"v_cndmask_b32 v72, v64, v65, s[34:35]\n"
"v_cndmask_b32 v76, v76, v72, s[36:37]\n"
"v_cndmask_b32 v77, v58, v59, s[34:35]\n"
"v_cndmask_b32 v72, v66, v67, s[34:35]\n"
"v_cndmask_b32 v77, v77, v72, s[38:39]\n"
"v_max_f32 v52, v76, v77\n"
"v_max_f32 v54, v76, v77\n"
"v_cndmask_b32 v78, v60, v61, s[34:35]\n"
"v_cndmask_b32 v72, v68, v69, s[34:35]\n"
"v_cndmask_b32 v78, v78, v72, s[36:37]\n"
"v_cndmask_b32 v79, v62, v63, s[34:35]\n"
"v_cndmask_b32 v72, v70, v71, s[34:35]\n"
"v_cndmask_b32 v79, v79, v72, s[38:39]\n"
"v_max_f32 v53, v78, v79\n"
"v_max_f32 v55, v78, v79\n"
"v_permlane16_swap_b32 v52, v54\n"
"s_nop 0\n"
"v_permlane16_swap_b32 v53, v55\n"
"v_max_f32 v72, v52, v54\n"
"v_max_f32 v52, v52, v54\n"
"v_max_f32 v73, v53, v55\n"
"v_max_f32 v53, v53, v55\n"
"v_permlane32_swap_b32 v52, v72\n"
"s_nop 0\n"
"v_permlane32_swap_b32 v53, v73\n"
"v_max_f32 v52, v52, v72\n"
"v_max_f32 v53, v53, v73\n"
"v_mul_f32 v52, 0xbea52b5b, v52\n"
"v_mul_f32 v53, 0xbea52b5b, v53\n"
"v_fmamk_f32 v76, v76, 0x3ea52b5b, v52\n"
"v_fmamk_f32 v77, v77, 0x3ea52b5b, v52\n"
"v_fmamk_f32 v78, v78, 0x3ea52b5b, v53\n"
"v_fmamk_f32 v79, v79, 0x3ea52b5b, v53\n"
"v_exp_f32 v76, v76\n"
"v_exp_f32 v77, v77\n"
"v_exp_f32 v78, v78\n"
"v_exp_f32 v79, v79\n"
"v_cvt_pk_bf16_f32 v52, v76, v77\n"
"v_cvt_pk_bf16_f32 v53, v78, v79\n"
"v_perm_b32 v44, v52, v52, v80\n"
"v_perm_b32 v45, v52, v52, v81\n"
"v_perm_b32 v46, v52, v52, v82\n"
"v_perm_b32 v47, v52, v52, v83\n"
"v_perm_b32 v48, v53, v53, v80\n"
"v_perm_b32 v49, v53, v53, v81\n"
"v_perm_b32 v50, v53, v53, v82\n"
"v_perm_b32 v51, v53, v53, v83\n"
"v_mfma_f32_16x16x32_bf16 v[56:59], v[84:87], v[44:47], 0\n"
"s_nop 0\n"
"v_mfma_f32_16x16x32_bf16 v[60:63], v[84:87], v[48:51], 0\n"
"s_waitcnt lgkmcnt(6)\n"
"v_mfma_f32_16x16x32_bf16 v[28:31], v[28:31], v[44:47], 0\n"
"s_waitcnt lgkmcnt(4)\n"
"v_mfma_f32_16x16x32_bf16 v[32:35], v[32:35], v[48:51], 0\n"
"s_waitcnt lgkmcnt(2)\n"
"v_mfma_f32_16x16x32_bf16 v[36:39], v[36:39], v[44:47], 0\n"
"s_waitcnt lgkmcnt(0)\n"
"v_mfma_f32_16x16x32_bf16 v[40:43], v[40:43], v[48:51], 0\n"
"v_cndmask_b32 v88, 2.0, v1, s[40:41]\n"
"v_rcp_f32 v52, v56\n"
"v_rcp_f32 v53, v60\n"
"s_add_u32 s31, s30, 0x140000\n"
"v_mul_f32 v28, v52, v28\n"
"v_mul_f32 v29, v52, v29\n"
"v_mul_f32 v30, v52, v30\n"
"v_mul_f32 v31, v52, v31\n"
"v_mul_f32 v32, v53, v32\n"
"v_mul_f32 v33, v53, v33\n"
"v_mul_f32 v34, v53, v34\n"
"v_mul_f32 v35, v53, v35\n"
"v_cvt_pk_bf16_f32 v28, v28, v29\n"
"v_cvt_pk_bf16_f32 v29, v30, v31\n"
"v_cvt_pk_bf16_f32 v30, v32, v33\n"
"v_cvt_pk_bf16_f32 v31, v34, v35\n"
"v_min_u32 v28, -2, v28\n"
"v_min_u32 v29, -2, v29\n"
"v_min_u32 v30, -2, v30\n"
"v_min_u32 v31, -2, v31\n"
"buffer_store_dwordx4 v[28:31], v1, s[4:7], s31 offen offset:0 sc1\n"
"v_mul_f32 v36, v52, v36\n"
"v_mul_f32 v37, v52, v37\n"
"v_mul_f32 v38, v52, v38\n"
"v_mul_f32 v39, v52, v39\n"
"v_mul_f32 v40, v53, v40\n"
"v_mul_f32 v41, v53, v41\n"
"v_mul_f32 v42, v53, v42\n"
"v_mul_f32 v43, v53, v43\n"
"v_cvt_pk_bf16_f32 v36, v36, v37\n"
"v_cvt_pk_bf16_f32 v37, v38, v39\n"
"v_cvt_pk_bf16_f32 v38, v40, v41\n"
"v_cvt_pk_bf16_f32 v39, v42, v43\n"
"v_min_u32 v36, -2, v36\n"
"v_min_u32 v37, -2, v37\n"
"v_min_u32 v38, -2, v38\n"
"v_min_u32 v39, -2, v39\n"
"buffer_store_dwordx4 v[36:39], v88, s[4:7], s31 offen offset:1024 sc1\n"
"ds_write_b32 v13, v13 offset:160\n"
"s_nop 2\n"
"s_cmp_lt_i32 s45, 0\n"
"s_cbranch_scc1 .Lnc_62\n"
"s_setprio 1\n"
"s_lshl_b32 s79, s3, 10\n"
"v_lshl_add_u32 v34, v0, 4, s79\n"
"v_lshrrev_b32 v59, 7, v34\n"
"v_and_b32 v58, 0x70, v34\n"
"s_mov_b32 s79, 0x5000\n"
"v_mad_i32_i24 v33, v59, s79, v58\n"
"s_mov_b32 s17, 0x100000\n"
".Ltp_63:\n"
"s_sleep 1\n"
"ds_read_b32 v42, v14 offset:176\n"
"s_sub_u32 s17, s17, 1\n"
"s_cbranch_scc1 .Lou_64\n"
"s_waitcnt lgkmcnt(0)\n"
"v_cmp_eq_u32 vcc, 0, v42\n"
"s_cbranch_vccnz .Ltp_63\n"
".Lou_64:\n"
"s_waitcnt lgkmcnt(0)\n"
"s_lshl_b32 s79, s45, 8\n"
"s_add_u32 s46, s79, 0x6c8000\n"
"s_add_u32 s47, s46, 0x140000\n"
"s_and_b32 s79, s2, 7\n"
"s_lshl_b32 s79, s79, 14\n"
"s_add_u32 s48, s79, 0xc08000\n"
"s_add_u32 s49, s48, 0x1000\n"
"v_and_b32 v94, 0xffffff80, v33\n"
"v_add_u32 v94, v94, v33\n"
"buffer_load_dwordx4 v[78:81], v94, s[4:7], s46 offen offset:0 sc1\n"
"buffer_load_dwordx4 v[82:85], v94, s[4:7], s46 offen offset:128 sc1\n"
"buffer_load_dwordx4 v[86:89], v94, s[4:7], s47 offen offset:0 sc1\n"
"buffer_load_dwordx4 v[90:93], v94, s[4:7], s47 offen offset:128 sc1\n"
"s_mul_i32 s79, s3, 0x1200\n"
"s_add_u32 s79, s79, 0x1c400\n"
"v_lshl_add_u32 v44, v0, 3, s79\n"
"v_mov_b32 v46, 0\n"
"v_mov_b32 v47, 0\n"
"ds_write_b64 v44, v[46:47]\n"
"v_and_b32 v51, 15, v0\n"
"v_and_b32 v50, 1, v51\n"
"v_and_b32 v49, 2, v51\n"
"v_lshlrev_b32 v48, 4, v0\n"
"v_lshrrev_b32 v47, 4, v0\n"
"v_xor_b32 v46, s3, v51\n"
"v_and_b32 v45, 1, v46\n"
"v_lshlrev_b32 v44, 1, v45\n"
"s_lshl_b32 s79, s3, 10\n"
"v_add_u32 v59, s79, v48\n"
"v_lshrrev_b32 v59, 2, v59\n"
"s_lshl_b32 s79, s3, 10\n"
"v_add_u32 v58, s79, v48\n"
"v_lshrrev_b32 v58, 6, v58\n"
"v_and_b32 v58, 24, v58\n"
"v_xor_b32 v59, v59, v58\n"
"v_add_u32 v36, 0xa000, v59\n"
"s_lshl_b32 s79, s3, 8\n"
"s_add_u32 s79, s79, 0xa000\n"
"v_lshrrev_b32 v59, 2, v51\n"
"v_lshrrev_b32 v58, 1, v47\n"
"v_xor_b32 v59, v59, v58\n"
"v_and_b32 v59, 1, v59\n"
"v_add_u32 v59, v44, v59\n"
"v_lshrrev_b32 v58, 2, v51\n"
"v_lshrrev_b32 v57, 3, v51\n"
"v_lshlrev_b32 v57, 7, v57\n"
"v_lshl_add_u32 v58, v58, 11, v57\n"
"v_lshl_add_u32 v58, v47, 6, v58\n"
"v_lshl_add_u32 v58, v49, 4, v58\n"
"v_lshl_add_u32 v59, v59, 3, v58\n"
"v_add_u32 v31, s79, v59\n"
"s_mul_i32 s79, s3, 0x2100\n"
"s_add_u32 s79, s79, 0x13840\n"
"v_lshlrev_b32 v59, 3, v47\n"
"v_sub_u32 v59, v59, v51\n"
"v_add_u32 v59, 0x3ef, v59\n"
"v_lshrrev_b32 v59, 2, v59\n"
"v_lshlrev_b32 v58, 3, v47\n"
"v_sub_u32 v58, v58, v51\n"
"v_add_u32 v58, 0x3ef, v58\n"
"v_and_b32 v58, 3, v58\n"
"v_mul_i32_i24 v58, 0x840, v58\n"
"v_lshl_add_u32 v59, v59, 3, v58\n"
"v_add_u32 v28, s79, v59\n"
"s_mul_i32 s79, s3, 0x1200\n"
"s_add_u32 s79, s79, 0x1c420\n"
"v_lshlrev_b32 v59, 4, v47\n"
"v_lshl_add_u32 v59, v51, 5, v59\n"
"v_add_u32 v29, s79, v59\n"
"s_lshl_b32 s79, s3, 11\n"
"s_lshr_b32 s78, s3, 1\n"
"s_lshl_b32 s78, s78, 7\n"
"s_add_u32 s79, s79, s78\n"
"s_add_u32 s79, s79, 0xa000\n"
"s_and_b32 s78, s3, 1\n"
"v_lshrrev_b32 v59, 2, v51\n"
"v_xor_b32 v59, v47, v59\n"
"v_xor_b32 v59, s78, v59\n"
"v_and_b32 v59, 3, v59\n"
"v_lshlrev_b32 v59, 3, v59\n"
"v_lshl_add_u32 v59, v51, 5, v59\n"
"v_add_u32 v30, s79, v59\n"
"v_xor_b32 v32, 8, v31\n"
"s_lshl_b32 s79, s3, 10\n"
"v_add_u32 v59, s79, v48\n"
"v_lshrrev_b32 v59, 2, v59\n"
"v_add_u32 v35, 0x1c600, v59\n"
"v_xor_b32 v37, 8, v36\n"
"v_mov_b32 v59, 0xffff0000\n"
"v_mov_b32 v58, 0xffff\n"
"v_cmp_eq_i32 vcc, 1, v50\n"
"s_nop 1\n"
"v_cndmask_b32 v59, v58, v59, vcc\n"
"v_lshrrev_b32 v58, 1, v51\n"
"v_lshlrev_b32 v57, 1, v47\n"
"v_cmp_eq_i32 vcc, v58, v57\n"
"s_nop 1\n"
"v_cndmask_b32 v38, 0, v59, vcc\n"
"v_mov_b32 v59, 0xffff0000\n"
"v_mov_b32 v58, 0xffff\n"
"v_cmp_eq_i32 vcc, 1, v50\n"
"s_nop 1\n"
"v_cndmask_b32 v59, v58, v59, vcc\n"
"v_lshrrev_b32 v58, 1, v51\n"
"v_lshl_add_u32 v57, v47, 1, 1\n"
"v_cmp_eq_i32 vcc, v58, v57\n"
"s_nop 1\n"
"v_cndmask_b32 v39, 0, v59, vcc\n"
"s_lshl_b32 s79, s3, 2\n"
"s_add_u32 s79, s79, 0x26800\n"
"v_mov_b32 v40, s79\n"
"s_lshl_b32 s79, s3, 10\n"
"v_add_u32 v59, s79, v48\n"
"v_lshrrev_b32 v59, 6, v59\n"
"s_lshl_b32 s79, s3, 10\n"
"v_add_u32 v58, s79, v48\n"
"v_and_b32 v58, 48, v58\n"
"v_mul_i32_i24 v58, 0x50, v58\n"
"s_mov_b32 s79, 0x2800\n"
"v_mad_i32_i24 v41, v59, s79, v58\n"
"v_lshlrev_b32 v49, 3, v0\n"
"v_lshlrev_b32 v48, 4, v0\n"
"v_lshlrev_b32 v47, 5, v0\n"
"v_lshrrev_b32 v46, 5, v0\n"
"v_sub_u32 v45, 0x5f0, v48\n"
"v_sub_u32 v44, 0x7e0, v47\n"
"s_mul_i32 s79, s3, 0x2100\n"
"s_add_u32 s79, s79, 0x15100\n"
"s_add_u32 s79, s79, 0x7f4\n"
"v_sub_u32 v60, s79, v49\n"
"v_mov_b32 v57, 4\n"
"v_cmp_eq_i32 vcc, 63, v0\n"
"s_nop 1\n"
"v_cndmask_b32 v57, 0, v57, vcc\n"
"v_sub_u32 v61, v60, v57\n"
"s_mul_i32 s79, s3, 0x2100\n"
"s_add_u32 s79, s79, 0x15100\n"
"s_sub_u32 s79, s79, 8\n"
"v_cmp_le_i32 vcc, 32, v0\n"
"s_nop 1\n"
"v_cndmask_b32 v57, v45, v44, vcc\n"
"v_add_u32 v57, s79, v57\n"
"v_mov_b32 v56, 4\n"
"v_cmp_le_i32 vcc, 31, v0\n"
"s_nop 1\n"
"v_cndmask_b32 v56, 0, v56, vcc\n"
"v_sub_u32 v62, v57, v56\n"
"s_mul_i32 s79, s3, 0x2100\n"
"s_add_u32 s79, s79, 0x14000\n"
"v_add_u32 v67, s79, v48\n"
"s_mul_i32 s79, s3, 0x2100\n"
"s_add_u32 s79, s79, 0x14000\n"
"s_add_u32 s79, s79, 0x800\n"
"v_and_b32 v57, 15, v0\n"
"v_lshrrev_b32 v56, 4, v0\n"
"v_mul_i32_i24 v56, 0x840, v56\n"
"v_lshl_add_u32 v57, v57, 2, v56\n"
"v_add_u32 v68, s79, v57\n"
"v_mov_b32 v69, 0\n"
"s_mul_i32 s79, s3, 0x2100\n"
"s_add_u32 s79, s79, 0x15100\n"
"s_sub_u32 s79, s79, 8\n"
"v_cmp_le_i32 vcc, 32, v0\n"
"s_nop 1\n"
"v_cndmask_b32 v57, v45, v44, vcc\n"
"v_add_u32 v57, s79, v57\n"
"v_mad_i32_i24 v63, v46, 0, v57\n"
"s_mul_i32 s79, s3, 0x2100\n"
"s_add_u32 s79, s79, 0x15100\n"
"s_sub_u32 s79, s79, 8\n"
"v_cmp_le_i32 vcc, 32, v0\n"
"s_nop 1\n"
"v_cndmask_b32 v57, v45, v44, vcc\n"
"v_add_u32 v57, s79, v57\n"
"v_lshl_add_u32 v64, v46, 2, v57\n"
"s_mul_i32 s79, s3, 0x2100\n"
"s_add_u32 s79, s79, 0x15100\n"
"s_sub_u32 s79, s79, 8\n"
"v_cmp_le_i32 vcc, 32, v0\n"
"s_nop 1\n"
"v_cndmask_b32 v57, v45, v44, vcc\n"
"v_add_u32 v57, s79, v57\n"
"v_lshl_add_u32 v65, v46, 3, v57\n"
"s_mul_i32 s79, s3, 0x2100\n"
"s_add_u32 s79, s79, 0x15100\n"
"s_sub_u32 s79, s79, 8\n"
"v_cmp_le_i32 vcc, 32, v0\n"
"s_nop 1\n"
"v_cndmask_b32 v57, v45, v44, vcc\n"
"v_add_u32 v57, s79, v57\n"
"v_mad_i32_i24 v66, v46, 12, v57\n"
"s_mov_b32 s17, 0x100000\n"
".Ltp_65:\n"
"s_sleep 1\n"
"ds_read_b32 v42, v14 offset:192\n"
"s_sub_u32 s17, s17, 1\n"
"s_cbranch_scc1 .Lou_66\n"
"s_waitcnt lgkmcnt(0)\n"
"v_cmp_eq_u32 vcc, 0, v42\n"
"s_cbranch_vccnz .Ltp_65\n"
".Lou_66:\n"
"s_waitcnt lgkmcnt(0)\n"
"ds_read_b32 v44, v60 offset:12\n"
"ds_read_b32 v45, v60 offset:8\n"
"ds_read_b32 v46, v60 offset:4\n"
"ds_read_b32 v47, v61 offset:4\n"
"ds_read_b32 v48, v61 offset:0\n"
"ds_read_b32 v49, v66 offset:24\n"
"ds_read_b32 v50, v66 offset:20\n"
"ds_read_b32 v51, v66 offset:16\n"
"ds_read_b32 v52, v65 offset:16\n"
"ds_read_b32 v53, v64 offset:16\n"
"ds_read_b32 v54, v64 offset:12\n"
"ds_read_b32 v55, v64 offset:8\n"
"ds_read_b32 v56, v63 offset:8\n"
"ds_read_b32 v57, v62 offset:8\n"
"ds_read_b32 v58, v62 offset:4\n"
"ds_read_b32 v59, v62 offset:0\n"
"v_cmp_ne_u32 vcc, 63, v0\n"
"s_waitcnt lgkmcnt(2)\n"
"s_nop 0\n"
"v_cndmask_b32 v57, 0, v57, vcc\n"
"s_waitcnt lgkmcnt(1)\n"
"v_cndmask_b32 v58, 0, v58, vcc\n"
"s_waitcnt lgkmcnt(0)\n"
"v_cndmask_b32 v59, 0, v59, vcc\n"
"v_cvt_pk_f16_f32 v70, v44, v44\n"
"v_cvt_pk_f16_f32 v71, v45, v45\n"
"v_cvt_pk_f16_f32 v72, v45, v46\n"
"v_cvt_pk_f16_f32 v73, v46, v46\n"
"ds_write_b128 v67, v[70:73] offset:0\n"
"v_cvt_pk_f16_f32 v74, v44, v45\n"
"v_cvt_pk_f16_f32 v75, v45, v45\n"
"v_cvt_pk_f16_f32 v76, v46, v46\n"
"v_cvt_pk_f16_f32 v77, v46, v46\n"
"ds_write_b128 v67, v[74:77] offset:2112\n"
"v_cvt_pk_f16_f32 v70, v45, v45\n"
"v_cvt_pk_f16_f32 v71, v45, v46\n"
"v_cvt_pk_f16_f32 v72, v46, v46\n"
"v_cvt_pk_f16_f32 v73, v46, v47\n"
"ds_write_b128 v67, v[70:73] offset:4224\n"
"v_cvt_pk_f16_f32 v74, v45, v45\n"
"v_cvt_pk_f16_f32 v75, v46, v46\n"
"v_cvt_pk_f16_f32 v76, v46, v46\n"
"v_cvt_pk_f16_f32 v77, v47, v48\n"
"ds_write_b128 v67, v[74:77] offset:6336\n"
"v_cvt_pk_f16_f32 v70, v49, v50\n"
"v_cvt_pk_f16_f32 v71, v51, v52\n"
"v_cvt_pk_f16_f32 v72, v53, v54\n"
"v_cvt_pk_f16_f32 v73, v55, v56\n"
"ds_write_b128 v67, v[70:73] offset:1024\n"
"v_cvt_pk_f16_f32 v74, v50, v51\n"
"v_cvt_pk_f16_f32 v75, v52, v53\n"
"v_cvt_pk_f16_f32 v76, v54, v55\n"
"v_cvt_pk_f16_f32 v77, v56, v57\n"
"ds_write_b128 v67, v[74:77] offset:3136\n"
"v_cvt_pk_f16_f32 v70, v51, v52\n"
"v_cvt_pk_f16_f32 v71, v53, v54\n"
"v_cvt_pk_f16_f32 v72, v55, v56\n"
"v_cvt_pk_f16_f32 v73, v57, v58\n"
"ds_write_b128 v67, v[70:73] offset:5248\n"
"v_cvt_pk_f16_f32 v74, v52, v53\n"
"v_cvt_pk_f16_f32 v75, v54, v55\n"
"v_cvt_pk_f16_f32 v76, v56, v57\n"
"v_cvt_pk_f16_f32 v77, v58, v59\n"
"ds_write_b128 v67, v[74:77] offset:7360\n"
"ds_write_b32 v68, v69\n"
"ds_read_b32 v40, v40\n"
"s_waitcnt lgkmcnt(0)\n"
"v_sub_u32 v44, 0x7f000000, v40\n"
"v_lshrrev_b32 v45, 16, v44\n"
"v_or_b32 v44, v44, v45\n"
"v_and_b32 v38, v44, v38\n"
"v_and_b32 v39, v44, v39\n"
"s_mov_b32 s52, 0x05040100\n"
"s_mov_b32 s53, 0x07060302\n"
"s_mov_b32 s17, 32768\n"
".Ltp_67:\n"
"s_waitcnt vmcnt(0)\n"
"s_sub_u32 s17, s17, 1\n"
"s_cbranch_scc1 .Lou_68\n"
"v_bitop3_b32 v54, v79, v81, s16 bitop3:0x7e\n"
"v_bitop3_b32 v55, v83, v85, s16 bitop3:0x7e\n"
"v_bitop3_b32 v56, v87, v89, s16 bitop3:0x7e\n"
"v_bitop3_b32 v57, v91, v93, s16 bitop3:0x7e\n"
"v_or3_b32 v58, v54, v55, v56\n"
"v_or_b32 v58, v58, v57\n"
"v_cmp_ne_u32 vcc, 0, v58\n"
"s_cbranch_vccz .Lou_68\n"
"s_sleep 2\n"
"v_cmp_ne_u32 vcc, 0, v54\n"
"s_cbranch_vccz .Lsk_69\n"
"s_mov_b64 exec, vcc\n"
"buffer_load_dwordx4 v[78:81], v94, s[4:7], s46 offen offset:0 sc1\n"
"s_mov_b64 exec, -1\n"
".Lsk_69:\n"
"v_cmp_ne_u32 vcc, 0, v55\n"
"s_cbranch_vccz .Lsk_70\n"
"s_mov_b64 exec, vcc\n"
"buffer_load_dwordx4 v[82:85], v94, s[4:7], s46 offen offset:128 sc1\n"
"s_mov_b64 exec, -1\n"
".Lsk_70:\n"
"v_cmp_ne_u32 vcc, 0, v56\n"
"s_cbranch_vccz .Lsk_71\n"
"s_mov_b64 exec, vcc\n"
"buffer_load_dwordx4 v[86:89], v94, s[4:7], s47 offen offset:0 sc1\n"
"s_mov_b64 exec, -1\n"
".Lsk_71:\n"
"v_cmp_ne_u32 vcc, 0, v57\n"
"s_cbranch_vccz .Lsk_72\n"
"s_mov_b64 exec, vcc\n"
"buffer_load_dwordx4 v[90:93], v94, s[4:7], s47 offen offset:128 sc1\n"
"s_mov_b64 exec, -1\n"
".Lsk_72:\n"
"s_branch .Ltp_67\n"
".Lou_68:\n"
"s_waitcnt vmcnt(0)\n"
"buffer_load_dwordx4 v[70:73], v34, s[4:7], s48 offen sc1\n"
"buffer_load_dwordx4 v[74:77], v34, s[4:7], s49 offen sc1\n"
"v_perm_b32 v54, v82, v78, s52\n"
"v_perm_b32 v55, v82, v78, s53\n"
"v_perm_b32 v56, v84, v80, s52\n"
"v_perm_b32 v57, v84, v80, s53\n"
"ds_write_b32 v36, v54 offset:0\n"
"ds_write_b32 v37, v55 offset:2048\n"
"ds_write_b32 v36, v56 offset:4224\n"
"ds_write_b32 v37, v57 offset:6272\n"
"v_perm_b32 v54, v90, v86, s52\n"
"v_perm_b32 v55, v90, v86, s53\n"
"v_perm_b32 v56, v92, v88, s52\n"
"v_perm_b32 v57, v92, v88, s53\n"
"ds_write_b32 v36, v54 offset:1024\n"
"ds_write_b32 v37, v55 offset:3072\n"
"ds_write_b32 v36, v56 offset:5248\n"
"ds_write_b32 v37, v57 offset:7296\n"
"v_lshlrev_b32 v54, 16, v78\n"
"v_lshlrev_b32 v55, 16, v82\n"
"v_and_b32 v56, 0xffff0000, v78\n"
"v_and_b32 v57, 0xffff0000, v82\n"
"v_lshlrev_b32 v58, 16, v80\n"
"v_lshlrev_b32 v59, 16, v84\n"
"v_and_b32 v60, 0xffff0000, v80\n"
"v_and_b32 v61, 0xffff0000, v84\n"
"v_lshlrev_b32 v62, 16, v86\n"
"v_lshlrev_b32 v63, 16, v90\n"
"v_and_b32 v64, 0xffff0000, v86\n"
"v_and_b32 v65, 0xffff0000, v90\n"
"v_lshlrev_b32 v66, 16, v88\n"
"v_lshlrev_b32 v67, 16, v92\n"
"v_and_b32 v68, 0xffff0000, v88\n"
"v_and_b32 v69, 0xffff0000, v92\n"
"s_mov_b32 s17, 32768\n"
".Ltp_73:\n"
"s_waitcnt vmcnt(0)\n"
"s_sub_u32 s17, s17, 1\n"
"s_cbranch_scc1 .Lou_74\n"
"v_max_u32 v78, v70, v71\n"
"v_max3_u32 v78, v72, v73, v78\n"
"v_max3_u32 v78, v74, v75, v78\n"
"v_max3_u32 v78, v76, v77, v78\n"
"v_cmp_eq_u32 vcc, -1, v78\n"
"s_cbranch_vccz .Lou_74\n"
"v_max_u32 v78, v72, v73\n"
"v_max3_u32 v78, v70, v71, v78\n"
"v_cmp_eq_u32 vcc, -1, v78\n"
"s_cbranch_vccz .Lsk_75\n"
"s_mov_b64 exec, vcc\n"
"buffer_load_dwordx4 v[70:73], v34, s[4:7], s48 offen sc1\n"
"s_mov_b64 exec, -1\n"
".Lsk_75:\n"
"v_max_u32 v78, v76, v77\n"
"v_max3_u32 v78, v74, v75, v78\n"
"v_cmp_eq_u32 vcc, -1, v78\n"
"s_cbranch_vccz .Lsk_76\n"
"s_mov_b64 exec, vcc\n"
"buffer_load_dwordx4 v[74:77], v34, s[4:7], s49 offen sc1\n"
"s_mov_b64 exec, -1\n"
".Lsk_76:\n"
"s_branch .Ltp_73\n"
".Lou_74:\n"
"s_waitcnt vmcnt(0)\n"
"v_fma_f32 v54, v54, v71, v70\n"
"v_fma_f32 v55, v55, v73, v72\n"
"v_cvt_pk_f16_f32 v54, v54, v55\n"
"v_fma_f32 v56, v56, v71, v70\n"
"v_fma_f32 v57, v57, v73, v72\n"
"v_cvt_pk_f16_f32 v56, v56, v57\n"
"v_fma_f32 v58, v58, v71, v70\n"
"v_fma_f32 v59, v59, v73, v72\n"
"v_cvt_pk_f16_f32 v58, v58, v59\n"
"v_fma_f32 v60, v60, v71, v70\n"
"v_fma_f32 v61, v61, v73, v72\n"
"v_cvt_pk_f16_f32 v60, v60, v61\n"
"ds_write2st64_b32 v35, v54, v56 offset0:0 offset1:18\n"
"ds_write2st64_b32 v35, v58, v60 offset0:36 offset1:54\n"
"v_fma_f32 v62, v62, v75, v74\n"
"v_fma_f32 v63, v63, v77, v76\n"
"v_cvt_pk_f16_f32 v62, v62, v63\n"
"v_fma_f32 v64, v64, v75, v74\n"
"v_fma_f32 v65, v65, v77, v76\n"
"v_cvt_pk_f16_f32 v64, v64, v65\n"
"v_fma_f32 v66, v66, v75, v74\n"
"v_fma_f32 v67, v67, v77, v76\n"
"v_cvt_pk_f16_f32 v66, v66, v67\n"
"v_fma_f32 v68, v68, v75, v74\n"
"v_fma_f32 v69, v69, v77, v76\n"
"v_cvt_pk_f16_f32 v68, v68, v69\n"
"ds_write2st64_b32 v35, v62, v64 offset0:4 offset1:22\n"
"ds_write2st64_b32 v35, v66, v68 offset0:40 offset1:58\n"
"ds_write_b32 v13, v13 offset:208\n"
"s_mov_b32 s17, 0x100000\n"
".Ltp_77:\n"
"ds_read_b32 v42, v14 offset:208\n"
"s_sub_u32 s17, s17, 1\n"
"s_cbranch_scc1 .Lou_78\n"
"s_waitcnt lgkmcnt(0)\n"
"v_cmp_eq_u32 vcc, 0, v42\n"
"s_cbranch_vccnz .Ltp_77\n"
".Lou_78:\n"
"s_waitcnt lgkmcnt(0)\n"
"s_mov_b32 s50, 0xfffefffe\n"
"ds_read_b64 v[60:61], v28 offset:1984\n"
"ds_read_b64 v[62:63], v28 offset:1992\n"
"ds_read_b128 v[64:67], v29 offset:1984\n"
"ds_read_b128 v[68:71], v29 offset:1472\n"
"ds_read_b64 v[72:73], v28 offset:1472\n"
"ds_read_b64 v[74:75], v28 offset:1480\n"
"ds_read_b128 v[76:79], v29 offset:960\n"
"s_waitcnt lgkmcnt(0)\n"
"v_mfma_f32_16x16x32_f16 v[56:59], v[60:63], v[64:67], 0\n"
"ds_read_b64 v[80:81], v28 offset:960\n"
"ds_read_b64 v[82:83], v28 offset:968\n"
"v_mfma_f32_16x16x32_f16 v[52:55], v[60:63], v[68:71], 0\n"
"ds_read_b128 v[84:87], v29 offset:448\n"
"ds_read_b64 v[88:89], v28 offset:448\n"
"v_mfma_f32_16x16x32_f16 v[56:59], v[72:75], v[68:71], v[56:59]\n"
"ds_read_b64 v[90:91], v28 offset:456\n"
"ds_read_b64 v[92:93], v28 offset:1920\n"
"v_mfma_f32_16x16x32_f16 v[48:51], v[60:63], v[76:79], 0\n"
"ds_read_b64 v[94:95], v28 offset:1928\n"
"s_waitcnt lgkmcnt(4)\n"
"v_mfma_f32_16x16x32_f16 v[52:55], v[72:75], v[76:79], v[52:55]\n"
"ds_read_b128 v[2:5], v29 offset:1920\n"
"ds_read_b128 v[34:37], v29 offset:1408\n"
"v_mfma_f32_16x16x32_f16 v[56:59], v[80:83], v[76:79], v[56:59]\n"
"ds_read_b64 v[64:65], v28 offset:1408\n"
"ds_read_b64 v[66:67], v28 offset:1416\n"
"v_mfma_f32_16x16x32_f16 v[44:47], v[60:63], v[84:87], 0\n"
"ds_read_b128 v[68:71], v29 offset:896\n"
"ds_read_b64 v[76:77], v28 offset:896\n"
"v_mfma_f32_16x16x32_f16 v[48:51], v[72:75], v[84:87], v[48:51]\n"
"ds_read_b64 v[78:79], v28 offset:904\n"
"s_waitcnt lgkmcnt(5)\n"
"v_mfma_f32_16x16x32_f16 v[52:55], v[80:83], v[84:87], v[52:55]\n"
"ds_read_b128 v[60:63], v29 offset:384\n"
"ds_read_b64 v[72:73], v28 offset:384\n"
"v_mfma_f32_16x16x32_f16 v[56:59], v[88:91], v[84:87], v[56:59]\n"
"ds_read_b64 v[74:75], v28 offset:392\n"
"ds_read_b64 v[80:81], v28 offset:1856\n"
"v_mfma_f32_16x16x32_f16 v[56:59], v[92:95], v[2:5], v[56:59]\n"
"ds_read_b64 v[82:83], v28 offset:1864\n"
"ds_read_b128 v[84:87], v29 offset:1856\n"
"v_mfma_f32_16x16x32_f16 v[52:55], v[92:95], v[34:37], v[52:55]\n"
"ds_read_b128 v[88:91], v29 offset:1344\n"
"s_waitcnt lgkmcnt(7)\n"
"v_mfma_f32_16x16x32_f16 v[56:59], v[64:67], v[34:37], v[56:59]\n"
"ds_read_b64 v[2:3], v30 offset:0\n"
"ds_read_b64 v[34:35], v28 offset:1344\n"
"v_mfma_f32_16x16x32_f16 v[48:51], v[92:95], v[68:71], v[48:51]\n"
"ds_read_b64 v[36:37], v28 offset:1352\n"
"v_mfma_f32_16x16x32_f16 v[52:55], v[64:67], v[68:71], v[52:55]\n"
"v_mfma_f32_16x16x32_f16 v[56:59], v[76:79], v[68:71], v[56:59]\n"
"ds_read_b128 v[68:71], v29 offset:832\n"
"s_waitcnt lgkmcnt(8)\n"
"v_mfma_f32_16x16x32_f16 v[44:47], v[92:95], v[60:63], v[44:47]\n"
"ds_read_b64 v[92:93], v28 offset:832\n"
"v_mfma_f32_16x16x32_f16 v[48:51], v[64:67], v[60:63], v[48:51]\n"
"ds_read_b64 v[94:95], v28 offset:840\n"
"v_mfma_f32_16x16x32_f16 v[52:55], v[76:79], v[60:63], v[52:55]\n"
"ds_read_b128 v[64:67], v29 offset:320\n"
"ds_read_b64 v[76:77], v28 offset:320\n"
"v_mfma_f32_16x16x32_f16 v[56:59], v[72:75], v[60:63], v[56:59]\n"
"ds_read_b64 v[78:79], v28 offset:328\n"
"s_waitcnt lgkmcnt(6)\n"
"v_mfma_f32_16x16x32_f16 v[56:59], v[80:83], v[84:87], v[56:59]\n"
"ds_read_b64 v[60:61], v30 offset:512\n"
"ds_read_b64 v[72:73], v28 offset:1792\n"
"v_mfma_f32_16x16x32_f16 v[52:55], v[80:83], v[88:91], v[52:55]\n"
"ds_read_b64 v[74:75], v28 offset:1800\n"
"s_and_b32 s79, s45, 0xfc\n"
"v_mfma_f32_16x16x16_bf16 v[44:47], v[38:39], v[2:3], v[44:47]\n"
"ds_read_b128 v[84:87], v29 offset:1792\n"
"s_and_b32 s78, s45, 1\n"
"v_mfma_f32_16x16x32_f16 v[56:59], v[34:37], v[88:91], v[56:59]\n"
"ds_read_b128 v[2:5], v29 offset:1280\n"
"s_waitcnt lgkmcnt(7)\n"
"v_mfma_f32_16x16x32_f16 v[48:51], v[80:83], v[68:71], v[48:51]\n"
"ds_read_b64 v[88:89], v28 offset:1280\n"
"s_lshl_b32 s78, s78, 1\n"
"v_mfma_f32_16x16x32_f16 v[52:55], v[34:37], v[68:71], v[52:55]\n"
"ds_read_b64 v[90:91], v28 offset:1288\n"
"s_add_u32 s79, s79, s78\n"
"v_mfma_f32_16x16x32_f16 v[56:59], v[92:95], v[68:71], v[56:59]\n"
"ds_read_b128 v[68:71], v29 offset:768\n"
"s_lshr_b32 s78, s45, 1\n"
"v_mfma_f32_16x16x32_f16 v[44:47], v[80:83], v[64:67], v[44:47]\n"
"ds_read_b64 v[80:81], v28 offset:768\n"
"s_waitcnt lgkmcnt(8)\n"
"v_mfma_f32_16x16x32_f16 v[48:51], v[34:37], v[64:67], v[48:51]\n"
"ds_read_b64 v[82:83], v28 offset:776\n"
"s_and_b32 s78, s78, 1\n"
"v_mfma_f32_16x16x32_f16 v[52:55], v[92:95], v[64:67], v[52:55]\n"
"ds_read_b128 v[34:37], v29 offset:256\n"
"ds_read_b64 v[92:93], v28 offset:256\n"
"v_mfma_f32_16x16x32_f16 v[56:59], v[76:79], v[64:67], v[56:59]\n"
"ds_read_b64 v[94:95], v28 offset:264\n"
"ds_read_b64 v[64:65], v28 offset:1728\n"
"v_mfma_f32_16x16x16_bf16 v[48:51], v[38:39], v[60:61], v[48:51]\n"
"ds_read_b64 v[66:67], v28 offset:1736\n"
"s_waitcnt lgkmcnt(7)\n"
"v_mfma_f32_16x16x32_f16 v[56:59], v[72:75], v[84:87], v[56:59]\n"
"ds_read_b128 v[76:79], v29 offset:1728\n"
"ds_read_b128 v[60:63], v29 offset:1216\n"
"v_mfma_f32_16x16x32_f16 v[52:55], v[72:75], v[2:5], v[52:55]\n"
"ds_read_b64 v[84:85], v28 offset:1216\n"
"s_add_u32 s79, s79, s78\n"
"v_mfma_f32_16x16x32_f16 v[56:59], v[88:91], v[2:5], v[56:59]\n"
"ds_read_b64 v[86:87], v28 offset:1224\n"
"s_lshl_b32 s79, s79, 7\n"
"v_mfma_f32_16x16x32_f16 v[48:51], v[72:75], v[68:71], v[48:51]\n"
"ds_read_b128 v[2:5], v29 offset:704\n"
"s_waitcnt lgkmcnt(9)\n"
"v_mfma_f32_16x16x32_f16 v[52:55], v[88:91], v[68:71], v[52:55]\n"
"s_add_u32 s46, s79, 0xc28000\n"
"v_mfma_f32_16x16x32_f16 v[56:59], v[80:83], v[68:71], v[56:59]\n"
"ds_read_b64 v[68:69], v30 offset:1024\n"
"v_mfma_f32_16x16x32_f16 v[44:47], v[72:75], v[34:37], v[44:47]\n"
"ds_read_b64 v[72:73], v28 offset:704\n"
"v_mfma_f32_16x16x32_f16 v[48:51], v[88:91], v[34:37], v[48:51]\n"
"ds_read_b64 v[74:75], v28 offset:712\n"
"s_waitcnt lgkmcnt(6)\n"
"v_mfma_f32_16x16x32_f16 v[52:55], v[80:83], v[34:37], v[52:55]\n"
"ds_read_b128 v[88:91], v29 offset:192\n"
"ds_read_b64 v[80:81], v28 offset:192\n"
"v_mfma_f32_16x16x32_f16 v[56:59], v[92:95], v[34:37], v[56:59]\n"
"ds_read_b64 v[82:83], v28 offset:200\n"
"ds_read_b64 v[34:35], v28 offset:1664\n"
"v_mfma_f32_16x16x32_f16 v[56:59], v[64:67], v[76:79], v[56:59]\n"
"ds_read_b64 v[36:37], v28 offset:1672\n"
"ds_read_b128 v[92:95], v29 offset:1664\n"
"v_mfma_f32_16x16x32_f16 v[52:55], v[64:67], v[60:63], v[52:55]\n"
"ds_read_b128 v[76:79], v29 offset:1152\n"
"s_waitcnt lgkmcnt(9)\n"
"v_mfma_f32_16x16x32_f16 v[56:59], v[84:87], v[60:63], v[56:59]\n"
"ds_read_b64 v[60:61], v28 offset:1152\n"
"v_mfma_f32_16x16x32_f16 v[48:51], v[64:67], v[2:5], v[48:51]\n"
"ds_read_b64 v[62:63], v28 offset:1160\n"
"v_mfma_f32_16x16x16_bf16 v[52:55], v[38:39], v[68:69], v[52:55]\n"
"ds_read_b128 v[68:71], v29 offset:640\n"
"s_nop 4\n"
"v_mfma_f32_16x16x32_f16 v[52:55], v[84:87], v[2:5], v[52:55]\n"
"s_waitcnt lgkmcnt(9)\n"
"v_mfma_f32_16x16x32_f16 v[56:59], v[72:75], v[2:5], v[56:59]\n"
"ds_read_b64 v[2:3], v28 offset:640\n"
"v_mfma_f32_16x16x32_f16 v[44:47], v[64:67], v[88:91], v[44:47]\n"
"ds_read_b64 v[4:5], v28 offset:648\n"
"v_mfma_f32_16x16x32_f16 v[48:51], v[84:87], v[88:91], v[48:51]\n"
"ds_read_b128 v[64:67], v29 offset:128\n"
"v_mfma_f32_16x16x32_f16 v[52:55], v[72:75], v[88:91], v[52:55]\n"
"ds_read_b64 v[84:85], v30 offset:1536\n"
"s_waitcnt lgkmcnt(5)\n"
"v_mfma_f32_16x16x32_f16 v[56:59], v[80:83], v[88:91], v[56:59]\n"
"ds_read_b64 v[72:73], v28 offset:128\n"
"ds_read_b64 v[74:75], v28 offset:136\n"
"v_mfma_f32_16x16x32_f16 v[56:59], v[34:37], v[92:95], v[56:59]\n"
"ds_read_b64 v[88:89], v28 offset:1600\n"
"ds_read_b64 v[90:91], v28 offset:1608\n"
"v_mfma_f32_16x16x32_f16 v[52:55], v[34:37], v[76:79], v[52:55]\n"
"ds_read_b128 v[80:83], v29 offset:1600\n"
"v_mfma_f32_16x16x32_f16 v[56:59], v[60:63], v[76:79], v[56:59]\n"
"ds_read_b128 v[92:95], v29 offset:1088\n"
"s_waitcnt lgkmcnt(7)\n"
"v_mfma_f32_16x16x32_f16 v[48:51], v[34:37], v[68:71], v[48:51]\n"
"ds_read_b64 v[76:77], v28 offset:1088\n"
"v_mfma_f32_16x16x32_f16 v[52:55], v[60:63], v[68:71], v[52:55]\n"
"ds_read_b64 v[78:79], v28 offset:1096\n"
"v_mfma_f32_16x16x32_f16 v[56:59], v[2:5], v[68:71], v[56:59]\n"
"ds_read_b128 v[68:71], v29 offset:576\n"
"v_mfma_f32_16x16x32_f16 v[44:47], v[34:37], v[64:67], v[44:47]\n"
"ds_read_b64 v[34:35], v28 offset:576\n"
"s_waitcnt lgkmcnt(8)\n"
"s_nop 0\n"
"v_mfma_f32_16x16x16_bf16 v[56:59], v[38:39], v[84:85], v[56:59]\n"
"ds_read_b64 v[36:37], v28 offset:584\n"
"v_mfma_f32_16x16x32_f16 v[48:51], v[60:63], v[64:67], v[48:51]\n"
"ds_read_b128 v[84:87], v29 offset:64\n"
"ds_read_b64 v[60:61], v28 offset:64\n"
"v_mfma_f32_16x16x32_f16 v[52:55], v[2:5], v[64:67], v[52:55]\n"
"ds_read_b64 v[62:63], v28 offset:72\n"
"ds_read_b64 v[2:3], v28 offset:1536\n"
"v_mfma_f32_16x16x32_f16 v[56:59], v[72:75], v[64:67], v[56:59]\n"
"ds_read_b64 v[4:5], v28 offset:1544\n"
"s_waitcnt lgkmcnt(7)\n"
"v_mfma_f32_16x16x32_f16 v[56:59], v[88:91], v[80:83], v[56:59]\n"
"ds_read_b128 v[64:67], v29 offset:0\n"
"ds_read_b128 v[72:75], v29 offset:512\n"
"v_mfma_f32_16x16x32_f16 v[52:55], v[88:91], v[92:95], v[52:55]\n"
"ds_read_b64 v[80:81], v28 offset:1024\n"
"v_mfma_f32_16x16x32_f16 v[56:59], v[76:79], v[92:95], v[56:59]\n"
"ds_read_b64 v[82:83], v28 offset:1032\n"
"v_mfma_f32_16x16x32_f16 v[48:51], v[88:91], v[68:71], v[48:51]\n"
"ds_read_b128 v[92:95], v29 offset:1024\n"
"s_waitcnt lgkmcnt(9)\n"
"v_mfma_f32_16x16x32_f16 v[52:55], v[76:79], v[68:71], v[52:55]\n"
"v_mfma_f32_16x16x32_f16 v[56:59], v[34:37], v[68:71], v[56:59]\n"
"ds_read_b64 v[68:69], v28 offset:512\n"
"v_mfma_f32_16x16x32_f16 v[44:47], v[88:91], v[84:87], v[44:47]\n"
"ds_read_b64 v[70:71], v28 offset:520\n"
"v_mfma_f32_16x16x32_f16 v[48:51], v[76:79], v[84:87], v[48:51]\n"
"ds_read_b128 v[88:91], v29 offset:1536\n"
"s_waitcnt lgkmcnt(6)\n"
"v_mfma_f32_16x16x32_f16 v[52:55], v[34:37], v[84:87], v[52:55]\n"
"ds_read_b64 v[76:77], v28 offset:0\n"
"v_mfma_f32_16x16x32_f16 v[56:59], v[60:63], v[84:87], v[56:59]\n"
"ds_read_b64 v[78:79], v28 offset:8\n"
"v_mfma_f32_16x16x32_f16 v[44:47], v[2:5], v[64:67], v[44:47]\n"
"v_mfma_f32_16x16x32_f16 v[48:51], v[2:5], v[72:75], v[48:51]\n"
"s_waitcnt lgkmcnt(3)\n"
"v_mfma_f32_16x16x32_f16 v[48:51], v[80:83], v[64:67], v[48:51]\n"
"v_mfma_f32_16x16x32_f16 v[52:55], v[2:5], v[92:95], v[52:55]\n"
"v_mul_f32 v44, v40, v44\n"
"v_mul_f32 v45, v40, v45\n"
"v_mul_f32 v46, v40, v46\n"
"v_mfma_f32_16x16x32_f16 v[52:55], v[80:83], v[72:75], v[52:55]\n"
"v_mul_f32 v47, v40, v47\n"
"v_cvt_pk_bf16_f32 v44, v44, v45\n"
"v_cvt_pk_bf16_f32 v45, v46, v47\n"
"v_mfma_f32_16x16x32_f16 v[52:55], v[68:71], v[64:67], v[52:55]\n"
"v_pk_min_u16 v44, v44, s50\n"
"v_pk_min_u16 v45, v45, s50\n"
"s_waitcnt lgkmcnt(0)\n"
"v_mfma_f32_16x16x32_f16 v[56:59], v[2:5], v[88:91], v[56:59]\n"
"ds_write_b64 v30, v[44:45] offset:0\n"
"v_mfma_f32_16x16x32_f16 v[56:59], v[80:83], v[92:95], v[56:59]\n"
"v_mul_f32 v48, v40, v48\n"
"v_mul_f32 v49, v40, v49\n"
"v_mfma_f32_16x16x32_f16 v[56:59], v[68:71], v[72:75], v[56:59]\n"
"v_mul_f32 v50, v40, v50\n"
"v_mul_f32 v51, v40, v51\n"
"v_mfma_f32_16x16x32_f16 v[56:59], v[76:79], v[64:67], v[56:59]\n"
"v_cvt_pk_bf16_f32 v48, v48, v49\n"
"v_cvt_pk_bf16_f32 v49, v50, v51\n"
"v_pk_min_u16 v48, v48, s50\n"
"v_pk_min_u16 v49, v49, s50\n"
"ds_write_b64 v30, v[48:49] offset:512\n"
"v_mul_f32 v52, v40, v52\n"
"v_mul_f32 v53, v40, v53\n"
"v_mul_f32 v54, v40, v54\n"
"v_mul_f32 v55, v40, v55\n"
"v_cvt_pk_bf16_f32 v52, v52, v53\n"
"v_cvt_pk_bf16_f32 v53, v54, v55\n"
"v_pk_min_u16 v52, v52, s50\n"
"v_pk_min_u16 v53, v53, s50\n"
"ds_write_b64 v30, v[52:53] offset:1024\n"
"v_mul_f32 v56, v40, v56\n"
"v_mul_f32 v57, v40, v57\n"
"v_mul_f32 v58, v40, v58\n"
"v_mul_f32 v59, v40, v59\n"
"v_cvt_pk_bf16_f32 v56, v56, v57\n"
"v_cvt_pk_bf16_f32 v57, v58, v59\n"
"v_pk_min_u16 v56, v56, s50\n"
"v_pk_min_u16 v57, v57, s50\n"
"ds_write_b64 v30, v[56:57] offset:1536\n"
"ds_write_b32 v13, v13 offset:224\n"
"s_mov_b32 s17, 0x100000\n"
".Ltp_79:\n"
"ds_read_b32 v42, v14 offset:224\n"
"s_sub_u32 s17, s17, 1\n"
"s_cbranch_scc1 .Lou_80\n"
"s_waitcnt lgkmcnt(0)\n"
"v_cmp_eq_u32 vcc, 0, v42\n"
"s_cbranch_vccnz .Ltp_79\n"
".Lou_80:\n"
"s_waitcnt lgkmcnt(0)\n"
"ds_read_b64_tr_b16 v[60:61], v31 offset:0\n"
"ds_read_b64_tr_b16 v[62:63], v32 offset:0\n"
"ds_read_b64_tr_b16 v[64:65], v31 offset:1024\n"
"ds_read_b64_tr_b16 v[66:67], v32 offset:1024\n"
"s_add_u32 s47, s46, 0xa0000\n"
"s_waitcnt lgkmcnt(2)\n"
"buffer_store_dwordx4 v[60:63], v33, s[4:7], s46 offen sc1\n"
"s_waitcnt lgkmcnt(0)\n"
"buffer_store_dwordx4 v[64:67], v33, s[4:7], s47 offen sc1\n"
"ds_write_b32 v13, v13 offset:240\n"
"s_setprio 1\n"
"s_mov_b32 s17, 0x100000\n"
".Ltp_81:\n"
"ds_read_b32 v42, v14 offset:240\n"
"s_sub_u32 s17, s17, 1\n"
"s_cbranch_scc1 .Lou_82\n"
"s_waitcnt lgkmcnt(0)\n"
"v_cmp_eq_u32 vcc, 0, v42\n"
"s_cbranch_vccnz .Ltp_81\n"
".Lou_82:\n"
"s_waitcnt lgkmcnt(0)\n"
"s_nop 2\n"
".Lnc_62:\n"
"v_and_b32 v35, 15, v0\n"
"v_and_b32 v34, 3, v35\n"
"v_and_b32 v33, 4, v35\n"
"v_lshrrev_b32 v32, 4, v0\n"
"v_and_b32 v31, 1, v32\n"
"v_lshlrev_b32 v30, 7, v31\n"
"v_lshlrev_b32 v29, 1, v33\n"
"v_lshlrev_b32 v28, 4, v34\n"
"v_lshrrev_b32 v79, 3, v35\n"
"v_lshrrev_b32 v78, 1, v32\n"
"v_lshl_add_u32 v78, v78, 9, v30\n"
"v_add_u32 v78, v78, v28\n"
"v_lshl_add_u32 v79, v79, 6, v78\n"
"s_lshl_b32 s79, s43, 10\n"
"v_add3_u32 v3, v79, v29, s79\n"
"v_lshrrev_b32 v79, 3, v35\n"
"v_lshrrev_b32 v78, 1, v32\n"
"v_lshl_add_u32 v78, v78, 9, v30\n"
"v_add_u32 v78, v78, v28\n"
"v_lshl_add_u32 v79, v79, 6, v78\n"
"s_lshl_b32 s79, s43, 10\n"
"s_add_u32 s79, s79, 0xfffff400\n"
"s_lshl_b32 s78, s43, 10\n"
"s_add_u32 s78, s78, 0x4400\n"
"s_cmp_ge_i32 s43, 3\n"
"s_cselect_b32 s79, s79, s78\n"
"v_add3_u32 v4, v79, v29, s79\n"
"v_lshrrev_b32 v79, 3, v35\n"
"v_lshrrev_b32 v78, 1, v32\n"
"v_lshl_add_u32 v78, v78, 9, v30\n"
"v_add_u32 v78, v78, v28\n"
"v_lshl_add_u32 v79, v79, 6, v78\n"
"s_lshl_b32 s79, s43, 10\n"
"s_add_u32 s79, s79, 0xfffff800\n"
"s_lshl_b32 s78, s43, 10\n"
"s_add_u32 s78, s78, 0x4800\n"
"s_cmp_ge_i32 s43, 2\n"
"s_cselect_b32 s79, s79, s78\n"
"v_add3_u32 v5, v79, v29, s79\n"
"v_lshrrev_b32 v79, 3, v35\n"
"v_lshrrev_b32 v78, 1, v32\n"
"v_lshl_add_u32 v78, v78, 9, v30\n"
"v_add_u32 v78, v78, v28\n"
"v_lshl_add_u32 v79, v79, 6, v78\n"
"s_lshl_b32 s79, s43, 10\n"
"s_add_u32 s79, s79, 0xfffffc00\n"
"s_lshl_b32 s78, s43, 10\n"
"s_add_u32 s78, s78, 0x4c00\n"
"s_cmp_ge_i32 s43, 1\n"
"s_cselect_b32 s79, s79, s78\n"
"v_add3_u32 v6, v79, v29, s79\n"
"s_mul_i32 s79, s43, 0x1400\n"
"v_lshl_add_u32 v79, v0, 7, s79\n"
"v_mov_b32 v78, 0x40000\n"
"v_cmp_gt_i32 vcc, 40, v0\n"
"s_nop 1\n"
"v_cndmask_b32 v93, v78, v79, vcc\n"
"v_mov_b32 v28, 0x0c0c0c0c\n"
"v_mov_b32 v29, 0xc0c0100\n"
"v_mov_b32 v30, 0x1000c0c\n"
"v_cndmask_b32 v29, v29, v30, s[34:35]\n"
"v_cndmask_b32 v80, v29, v28, s[36:37]\n"
"v_cndmask_b32 v82, v28, v29, s[36:37]\n"
"v_mov_b32 v29, 0xc0c0302\n"
"v_mov_b32 v30, 0x3020c0c\n"
"v_cndmask_b32 v29, v29, v30, s[34:35]\n"
"v_cndmask_b32 v81, v29, v28, s[38:39]\n"
"v_cndmask_b32 v83, v28, v29, s[38:39]\n"
"s_mov_b32 s17, 0x100000\n"
".Ltp_83:\n"
"ds_read_b32 v90, v14 offset:160\n"
"s_sub_u32 s17, s17, 1\n"
"s_cbranch_scc1 .Lou_84\n"
"s_waitcnt lgkmcnt(0)\n"
"v_cmp_eq_u32 vcc, 0, v90\n"
"s_cbranch_vccnz .Ltp_83\n"
".Lou_84:\n"
"s_waitcnt lgkmcnt(0)\n"
"s_cmp_ge_i32 s45, 0\n"
"s_cbranch_scc1 .Lnw_85\n"
"s_mov_b32 s17, 0x100000\n"
".Ltp_86:\n"
"s_sleep 1\n"
"ds_read_b32 v90, v14 offset:176\n"
"s_sub_u32 s17, s17, 1\n"
"s_cbranch_scc1 .Lou_87\n"
"s_waitcnt lgkmcnt(0)\n"
"v_cmp_eq_u32 vcc, 0, v90\n"
"s_cbranch_vccnz .Ltp_86\n"
".Lou_87:\n"
"s_waitcnt lgkmcnt(0)\n"
"s_mov_b32 s89, 30\n"
".Ldl_88:\n"
"s_sub_u32 s89, s89, 1\n"
"s_cbranch_scc1 .Lde_89\n"
"s_sleep 1\n"
"s_branch .Ldl_88\n"
".Lde_89:\n"
"s_and_b32 s79, s2, 7\n"
"s_lshl_b32 s79, s79, 14\n"
"s_add_u32 s46, s79, 0xc08000\n"
"s_add_u32 s47, s46, 0x1000\n"
"buffer_load_dwordx4 v[28:31], v1, s[4:7], s46 offen offset:0 sc1\n"
"buffer_load_dwordx4 v[32:35], v1, s[4:7], s46 offen offset:1024 sc1\n"
"buffer_load_dwordx4 v[36:39], v1, s[4:7], s46 offen offset:2048 sc1\n"
"buffer_load_dwordx4 v[40:43], v1, s[4:7], s46 offen offset:3072 sc1\n"
"buffer_load_dwordx4 v[44:47], v1, s[4:7], s47 offen offset:0 sc1\n"
"buffer_load_dwordx4 v[48:51], v1, s[4:7], s47 offen offset:1024 sc1\n"
"buffer_load_dwordx4 v[52:55], v1, s[4:7], s47 offen offset:2048 sc1\n"
"buffer_load_dwordx4 v[56:59], v1, s[4:7], s47 offen offset:3072 sc1\n"
"s_mov_b32 s17, 32768\n"
".Ltp_90:\n"
"s_waitcnt vmcnt(0)\n"
"s_sub_u32 s17, s17, 1\n"
"s_cbranch_scc1 .Lou_91\n"
"v_max_u32 v60, v28, v29\n"
"v_max3_u32 v60, v30, v31, v60\n"
"v_max3_u32 v60, v32, v33, v60\n"
"v_max3_u32 v60, v34, v35, v60\n"
"v_max3_u32 v60, v36, v37, v60\n"
"v_max3_u32 v60, v38, v39, v60\n"
"v_max3_u32 v60, v40, v41, v60\n"
"v_max3_u32 v60, v42, v43, v60\n"
"v_max3_u32 v60, v44, v45, v60\n"
"v_max3_u32 v60, v46, v47, v60\n"
"v_max3_u32 v60, v48, v49, v60\n"
"v_max3_u32 v60, v50, v51, v60\n"
"v_max3_u32 v60, v52, v53, v60\n"
"v_max3_u32 v60, v54, v55, v60\n"
"v_max3_u32 v60, v56, v57, v60\n"
"v_max3_u32 v60, v58, v59, v60\n"
"v_cmp_eq_u32 vcc, -1, v60\n"
"s_cbranch_vccz .Lou_91\n"
"s_sleep 4\n"
"v_max_u32 v60, v30, v31\n"
"v_max3_u32 v60, v28, v29, v60\n"
"v_cmp_eq_u32 vcc, -1, v60\n"
"s_cbranch_vccz .Lsk_92\n"
"s_mov_b64 exec, vcc\n"
"buffer_load_dwordx4 v[28:31], v1, s[4:7], s46 offen offset:0 sc1\n"
"s_mov_b64 exec, -1\n"
".Lsk_92:\n"
"v_max_u32 v60, v34, v35\n"
"v_max3_u32 v60, v32, v33, v60\n"
"v_cmp_eq_u32 vcc, -1, v60\n"
"s_cbranch_vccz .Lsk_93\n"
"s_mov_b64 exec, vcc\n"
"buffer_load_dwordx4 v[32:35], v1, s[4:7], s46 offen offset:1024 sc1\n"
"s_mov_b64 exec, -1\n"
".Lsk_93:\n"
"v_max_u32 v60, v38, v39\n"
"v_max3_u32 v60, v36, v37, v60\n"
"v_cmp_eq_u32 vcc, -1, v60\n"
"s_cbranch_vccz .Lsk_94\n"
"s_mov_b64 exec, vcc\n"
"buffer_load_dwordx4 v[36:39], v1, s[4:7], s46 offen offset:2048 sc1\n"
"s_mov_b64 exec, -1\n"
".Lsk_94:\n"
"v_max_u32 v60, v42, v43\n"
"v_max3_u32 v60, v40, v41, v60\n"
"v_cmp_eq_u32 vcc, -1, v60\n"
"s_cbranch_vccz .Lsk_95\n"
"s_mov_b64 exec, vcc\n"
"buffer_load_dwordx4 v[40:43], v1, s[4:7], s46 offen offset:3072 sc1\n"
"s_mov_b64 exec, -1\n"
".Lsk_95:\n"
"v_max_u32 v60, v46, v47\n"
"v_max3_u32 v60, v44, v45, v60\n"
"v_cmp_eq_u32 vcc, -1, v60\n"
"s_cbranch_vccz .Lsk_96\n"
"s_mov_b64 exec, vcc\n"
"buffer_load_dwordx4 v[44:47], v1, s[4:7], s47 offen offset:0 sc1\n"
"s_mov_b64 exec, -1\n"
".Lsk_96:\n"
"v_max_u32 v60, v50, v51\n"
"v_max3_u32 v60, v48, v49, v60\n"
"v_cmp_eq_u32 vcc, -1, v60\n"
"s_cbranch_vccz .Lsk_97\n"
"s_mov_b64 exec, vcc\n"
"buffer_load_dwordx4 v[48:51], v1, s[4:7], s47 offen offset:1024 sc1\n"
"s_mov_b64 exec, -1\n"
".Lsk_97:\n"
"v_max_u32 v60, v54, v55\n"
"v_max3_u32 v60, v52, v53, v60\n"
"v_cmp_eq_u32 vcc, -1, v60\n"
"s_cbranch_vccz .Lsk_98\n"
"s_mov_b64 exec, vcc\n"
"buffer_load_dwordx4 v[52:55], v1, s[4:7], s47 offen offset:2048 sc1\n"
"s_mov_b64 exec, -1\n"
".Lsk_98:\n"
"v_max_u32 v60, v58, v59\n"
"v_max3_u32 v60, v56, v57, v60\n"
"v_cmp_eq_u32 vcc, -1, v60\n"
"s_cbranch_vccz .Lsk_99\n"
"s_mov_b64 exec, vcc\n"
"buffer_load_dwordx4 v[56:59], v1, s[4:7], s47 offen offset:3072 sc1\n"
"s_mov_b64 exec, -1\n"
".Lsk_99:\n"
"s_branch .Ltp_90\n"
".Lou_91:\n"
"s_waitcnt vmcnt(0)\n"
"s_mov_b32 s89, 30\n"
".Ldl_100:\n"
"s_sub_u32 s89, s89, 1\n"
"s_cbranch_scc1 .Lde_101\n"
"s_sleep 1\n"
"s_branch .Ldl_100\n"
".Lde_101:\n"
".Lnw_85:\n"
"s_mov_b32 s89, 8\n"
".Ldl_102:\n"
"s_sub_u32 s89, s89, 1\n"
"s_cbranch_scc1 .Lde_103\n"
"s_sleep 1\n"
"s_branch .Ldl_102\n"
".Lde_103:\n"
"s_cmp_ge_i32 s45, 0\n"
"s_cbranch_scc1 .Lnw_104\n"
"v_min_i32 v79, 3, v0\n"
"v_mul_i32_i24 v28, 0x500, v79\n"
"s_and_b32 s79, s2, 7\n"
"s_lshl_b32 s79, s79, 3\n"
"s_add_u32 s79, s79, 7\n"
"s_mul_i32 s79, s79, 0x5000\n"
"s_and_b32 s78, s2, 7\n"
"s_lshr_b32 s78, s78, 1\n"
"s_lshl_b32 s78, s78, 7\n"
"s_add_u32 s79, s79, s78\n"
"s_add_u32 s79, s79, s29\n"
"s_add_u32 s46, s79, 0xc28000\n"
"s_mov_b32 s17, 32768\n"
".Ltp_105:\n"
"buffer_load_dword v29, v28, s[4:7], s46 offen sc1\n"
"s_sub_u32 s17, s17, 1\n"
"s_cbranch_scc1 .Lou_106\n"
"s_waitcnt vmcnt(0)\n"
"v_cmp_eq_u32 vcc, -1, v29\n"
"s_cbranch_vccz .Lou_106\n"
"s_mov_b32 s89, 2\n"
".Ldl_107:\n"
"s_sub_u32 s89, s89, 1\n"
"s_cbranch_scc1 .Lde_108\n"
"s_sleep 1\n"
"s_branch .Ldl_107\n"
".Lde_108:\n"
"s_branch .Ltp_105\n"
".Lou_106:\n"
"s_waitcnt vmcnt(0)\n"
".Lnw_104:\n"
"s_mov_b32 s17, 0x100000\n"
".Ltp_109:\n"
"ds_read_b32 v90, v14 offset:256\n"
"s_sub_u32 s17, s17, 1\n"
"s_cbranch_scc1 .Lou_110\n"
"s_waitcnt lgkmcnt(0)\n"
"v_cmp_eq_u32 vcc, 0, v90\n"
"s_cbranch_vccnz .Ltp_109\n"
".Lou_110:\n"
"s_waitcnt lgkmcnt(0)\n"
"s_mov_b32 s54, 0\n"
"s_nop 1\n"
".Lag_111:\n"
"v_mov_b32 v92, 0\n"
"s_mov_b32 s46, s44\n"
"s_add_u32 s47, s46, 0x1000\n"
"v_add_u32 v2, s29, v1\n"
"buffer_load_dwordx4 v[28:31], v1, s[4:7], s46 offen offset:0 sc1\n"
"buffer_load_dwordx4 v[32:35], v1, s[4:7], s46 offen offset:1024 sc1\n"
"buffer_load_dwordx4 v[36:39], v1, s[4:7], s46 offen offset:2048 sc1\n"
"buffer_load_dwordx4 v[40:43], v1, s[4:7], s46 offen offset:3072 sc1\n"
"buffer_load_dwordx4 v[44:47], v1, s[4:7], s47 offen offset:0 sc1\n"
"s_mov_b32 s17, 32768\n"
".Ltp_114:\n"
"s_waitcnt vmcnt(0)\n"
"s_sub_u32 s17, s17, 1\n"
"s_cbranch_scc1 .Lou_115\n"
"v_max_u32 v48, v28, v29\n"
"v_max3_u32 v48, v30, v31, v48\n"
"v_max3_u32 v48, v32, v33, v48\n"
"v_max3_u32 v48, v34, v35, v48\n"
"v_max3_u32 v48, v36, v37, v48\n"
"v_max3_u32 v48, v38, v39, v48\n"
"v_max3_u32 v48, v40, v41, v48\n"
"v_max3_u32 v48, v42, v43, v48\n"
"v_max3_u32 v48, v44, v45, v48\n"
"v_max3_u32 v48, v46, v47, v48\n"
"v_cmp_eq_u32 vcc, -1, v48\n"
"s_cbranch_vccz .Lou_115\n"
"s_sleep 1\n"
"v_max_u32 v48, v30, v31\n"
"v_max3_u32 v48, v28, v29, v48\n"
"v_cmp_eq_u32 vcc, -1, v48\n"
"s_cbranch_vccz .Lsk_116\n"
"s_mov_b64 exec, vcc\n"
"buffer_load_dwordx4 v[28:31], v1, s[4:7], s46 offen offset:0 sc1\n"
"s_mov_b64 exec, -1\n"
".Lsk_116:\n"
"v_max_u32 v48, v34, v35\n"
"v_max3_u32 v48, v32, v33, v48\n"
"v_cmp_eq_u32 vcc, -1, v48\n"
"s_cbranch_vccz .Lsk_117\n"
"s_mov_b64 exec, vcc\n"
"buffer_load_dwordx4 v[32:35], v1, s[4:7], s46 offen offset:1024 sc1\n"
"s_mov_b64 exec, -1\n"
".Lsk_117:\n"
"v_max_u32 v48, v38, v39\n"
"v_max3_u32 v48, v36, v37, v48\n"
"v_cmp_eq_u32 vcc, -1, v48\n"
"s_cbranch_vccz .Lsk_118\n"
"s_mov_b64 exec, vcc\n"
"buffer_load_dwordx4 v[36:39], v1, s[4:7], s46 offen offset:2048 sc1\n"
"s_mov_b64 exec, -1\n"
".Lsk_118:\n"
"v_max_u32 v48, v42, v43\n"
"v_max3_u32 v48, v40, v41, v48\n"
"v_cmp_eq_u32 vcc, -1, v48\n"
"s_cbranch_vccz .Lsk_119\n"
"s_mov_b64 exec, vcc\n"
"buffer_load_dwordx4 v[40:43], v1, s[4:7], s46 offen offset:3072 sc1\n"
"s_mov_b64 exec, -1\n"
".Lsk_119:\n"
"v_max_u32 v48, v46, v47\n"
"v_max3_u32 v48, v44, v45, v48\n"
"v_cmp_eq_u32 vcc, -1, v48\n"
"s_cbranch_vccz .Lsk_120\n"
"s_mov_b64 exec, vcc\n"
"buffer_load_dwordx4 v[44:47], v1, s[4:7], s47 offen offset:0 sc1\n"
"s_mov_b64 exec, -1\n"
".Lsk_120:\n"
"s_branch .Ltp_114\n"
".Lou_115:\n"
"s_waitcnt vmcnt(0)\n"
"s_add_u32 m0, s29, 0x5000\n"
"s_add_u32 s31, s44, 0x5000\n"
"buffer_load_dwordx4 v1, s[4:7], s31 offen offset:0 sc1 lds\n"
"ds_write_b128 v2, v[28:31] offset:0\n"
"buffer_load_dwordx4 v1, s[4:7], s31 offen offset:1024 sc1 lds\n"
"ds_write_b128 v2, v[32:35] offset:1024\n"
"buffer_load_dwordx4 v1, s[4:7], s31 offen offset:2048 sc1 lds\n"
"ds_write_b128 v2, v[36:39] offset:2048\n"
"buffer_load_dwordx4 v1, s[4:7], s31 offen offset:3072 sc1 lds\n"
"ds_write_b128 v2, v[40:43] offset:3072\n"
"s_add_u32 m0, m0, 1024\n"
"s_add_u32 s31, s31, 1024\n"
"buffer_load_dwordx4 v1, s[4:7], s31 offen offset:3072 sc1 lds\n"
"ds_write_b128 v2, v[44:47] offset:4096\n"
"ds_write_b32 v13, v13 offset:272\n"
"s_mov_b32 s17, 0x100000\n"
".Ltp_121:\n"
"ds_read_b32 v90, v14 offset:272\n"
"s_sub_u32 s17, s17, 1\n"
"s_cbranch_scc1 .Lou_122\n"
"s_waitcnt lgkmcnt(0)\n"
"v_cmp_eq_u32 vcc, 0, v90\n"
"s_cbranch_vccnz .Ltp_121\n"
".Lou_122:\n"
"s_waitcnt lgkmcnt(0)\n"
"ds_read_b64 v[56:57], v3 offset:0\n"
"ds_read_b64 v[58:59], v3 offset:256\n"
"ds_read_b64 v[60:61], v3 offset:1024\n"
"ds_read_b64 v[62:63], v3 offset:1280\n"
"ds_read_b64 v[64:65], v3 offset:2048\n"
"ds_read_b64 v[66:67], v3 offset:2304\n"
"ds_read_b64 v[68:69], v3 offset:3072\n"
"ds_read_b64 v[70:71], v3 offset:3328\n"
"ds_read_b64 v[72:73], v3 offset:4096\n"
"ds_read_b64 v[74:75], v3 offset:4352\n"
"s_waitcnt lgkmcnt(6)\n"
"v_mfma_f32_16x16x32_bf16 v[28:31], v[96:99], v[56:59], 0\n"
"ds_read_b64 v[76:77], v3 offset:5120\n"
"s_add_u32 m0, s29, 0xa000\n"
"v_mfma_f32_16x16x32_bf16 v[32:35], v[100:103], v[56:59], 0\n"
"ds_read_b64 v[78:79], v3 offset:5376\n"
"s_add_u32 s31, s44, 0xa000\n"
"v_mfma_f32_16x16x32_bf16 v[52:55], v[56:59], v[56:59], 0\n"
"v_mfma_f32_16x16x32_bf16 v[28:31], v[104:107], v[60:63], v[28:31]\n"
"ds_read_b64 v[56:57], v3 offset:6144\n"
"buffer_load_dwordx4 v1, s[4:7], s31 offen offset:0 sc1 lds\n"
"v_mfma_f32_16x16x32_bf16 v[32:35], v[108:111], v[60:63], v[32:35]\n"
"ds_read_b64 v[58:59], v3 offset:6400\n"
"s_waitcnt lgkmcnt(6)\n"
"v_mfma_f32_16x16x32_bf16 v[28:31], v[112:115], v[64:67], v[28:31]\n"
"ds_read_b64 v[60:61], v3 offset:7168\n"
"v_mfma_f32_16x16x32_bf16 v[32:35], v[116:119], v[64:67], v[32:35]\n"
"ds_read_b64 v[62:63], v3 offset:7424\n"
"v_mfma_f32_16x16x32_bf16 v[28:31], v[120:123], v[68:71], v[28:31]\n"
"ds_read_b64 v[64:65], v3 offset:8192\n"
"buffer_load_dwordx4 v1, s[4:7], s31 offen offset:1024 sc1 lds\n"
"v_mfma_f32_16x16x32_bf16 v[32:35], v[124:127], v[68:71], v[32:35]\n"
"ds_read_b64 v[66:67], v3 offset:8448\n"
"s_waitcnt lgkmcnt(6)\n"
"v_mfma_f32_16x16x32_bf16 v[28:31], v[128:131], v[72:75], v[28:31]\n"
"ds_read_b64 v[68:69], v3 offset:9216\n"
"v_mfma_f32_16x16x32_bf16 v[32:35], v[132:135], v[72:75], v[32:35]\n"
"ds_read_b64 v[70:71], v3 offset:9472\n"
"v_mfma_f32_16x16x32_bf16 v[52:55], v[72:75], v[72:75], v[52:55]\n"
"buffer_load_dwordx4 v1, s[4:7], s31 offen offset:2048 sc1 lds\n"
"v_mfma_f32_16x16x32_bf16 v[28:31], v[136:139], v[76:79], v[28:31]\n"
"ds_read_b64 v[72:73], v3 offset:10240\n"
"v_mfma_f32_16x16x32_bf16 v[32:35], v[140:143], v[76:79], v[32:35]\n"
"ds_read_b64 v[74:75], v3 offset:10496\n"
"s_waitcnt lgkmcnt(6)\n"
"v_mfma_f32_16x16x32_bf16 v[28:31], v[144:147], v[56:59], v[28:31]\n"
"ds_read_b64 v[76:77], v3 offset:11264\n"
"v_mfma_f32_16x16x32_bf16 v[32:35], v[148:151], v[56:59], v[32:35]\n"
"ds_read_b64 v[78:79], v3 offset:11520\n"
"buffer_load_dwordx4 v1, s[4:7], s31 offen offset:3072 sc1 lds\n"
"v_mfma_f32_16x16x32_bf16 v[28:31], v[152:155], v[60:63], v[28:31]\n"
"ds_read_b64 v[56:57], v3 offset:12288\n"
"s_add_u32 m0, m0, 1024\n"
"v_mfma_f32_16x16x32_bf16 v[32:35], v[156:159], v[60:63], v[32:35]\n"
"ds_read_b64 v[58:59], v3 offset:12544\n"
"s_waitcnt lgkmcnt(6)\n"
"v_mfma_f32_16x16x32_bf16 v[28:31], v[160:163], v[64:67], v[28:31]\n"
"ds_read_b64 v[60:61], v3 offset:13312\n"
"s_add_u32 s31, s31, 1024\n"
"v_mfma_f32_16x16x32_bf16 v[32:35], v[164:167], v[64:67], v[32:35]\n"
"ds_read_b64 v[62:63], v3 offset:13568\n"
"buffer_load_dwordx4 v1, s[4:7], s31 offen offset:3072 sc1 lds\n"
"v_mfma_f32_16x16x32_bf16 v[52:55], v[64:67], v[64:67], v[52:55]\n"
"s_waitcnt vmcnt(5)\n"
"ds_write_b32 v13, v13 offset:288\n"
"v_mfma_f32_16x16x32_bf16 v[28:31], v[168:171], v[68:71], v[28:31]\n"
"ds_read_b64 v[64:65], v3 offset:14336\n"
"s_add_u32 m0, s29, 0xf000\n"
"s_add_u32 s31, s44, 0xf000\n"
"v_mfma_f32_16x16x32_bf16 v[32:35], v[172:175], v[68:71], v[32:35]\n"
"ds_read_b64 v[66:67], v3 offset:14592\n"
"s_waitcnt lgkmcnt(7)\n"
"v_mfma_f32_16x16x32_bf16 v[28:31], v[176:179], v[72:75], v[28:31]\n"
"ds_read_b64 v[68:69], v3 offset:15360\n"
"buffer_load_dwordx4 v1, s[4:7], s31 offen offset:0 sc1 lds\n"
"v_mfma_f32_16x16x32_bf16 v[32:35], v[180:183], v[72:75], v[32:35]\n"
"ds_read_b64 v[70:71], v3 offset:15616\n"
"v_mfma_f32_16x16x32_bf16 v[28:31], v[184:187], v[76:79], v[28:31]\n"
"ds_read_b64 v[72:73], v3 offset:16384\n"
"v_mfma_f32_16x16x32_bf16 v[32:35], v[188:191], v[76:79], v[32:35]\n"
"ds_read_b64 v[74:75], v3 offset:16640\n"
"s_waitcnt lgkmcnt(7)\n"
"v_mfma_f32_16x16x32_bf16 v[28:31], v[192:195], v[56:59], v[28:31]\n"
"ds_read_b64 v[76:77], v4 offset:0\n"
"buffer_load_dwordx4 v1, s[4:7], s31 offen offset:1024 sc1 lds\n"
"v_mfma_f32_16x16x32_bf16 v[32:35], v[196:199], v[56:59], v[32:35]\n"
"ds_read_b64 v[78:79], v4 offset:256\n"
"ds_read_b32 v90, v14 offset:288\n"
"v_mfma_f32_16x16x32_bf16 v[52:55], v[56:59], v[56:59], v[52:55]\n"
"v_mfma_f32_16x16x32_bf16 v[28:31], v[200:203], v[60:63], v[28:31]\n"
"ds_read_b64 v[56:57], v5 offset:0\n"
"v_mfma_f32_16x16x32_bf16 v[32:35], v[204:207], v[60:63], v[32:35]\n"
"ds_read_b64 v[58:59], v5 offset:256\n"
"buffer_load_dwordx4 v1, s[4:7], s31 offen offset:2048 sc1 lds\n"
"s_waitcnt lgkmcnt(7)\n"
"v_mfma_f32_16x16x32_bf16 v[28:31], v[208:211], v[64:67], v[28:31]\n"
"ds_read_b64 v[60:61], v6 offset:0\n"
"s_waitcnt lgkmcnt(3)\n"
"v_cmp_eq_u32 vcc, 0, v90\n"
"v_mfma_f32_16x16x32_bf16 v[32:35], v[212:215], v[64:67], v[32:35]\n"
"ds_read_b64 v[62:63], v6 offset:256\n"
"v_mfma_f32_16x16x32_bf16 v[28:31], v[216:219], v[68:71], v[28:31]\n"
"s_cbranch_vccnz .Lsl_123\n"
".Lbk_124:\n"
"ds_read_b64 v[64:65], v3 offset:20480\n"
"v_mfma_f32_16x16x32_bf16 v[32:35], v[220:223], v[68:71], v[32:35]\n"
"ds_read_b64 v[66:67], v3 offset:20736\n"
"buffer_load_dwordx4 v1, s[4:7], s31 offen offset:3072 sc1 lds\n"
"v_mfma_f32_16x16x32_bf16 v[28:31], v[224:227], v[72:75], v[28:31]\n"
"ds_read_b64 v[68:69], v3 offset:21504\n"
"s_add_u32 m0, m0, 1024\n"
"v_mfma_f32_16x16x32_bf16 v[32:35], v[228:231], v[72:75], v[32:35]\n"
"ds_read_b64 v[70:71], v3 offset:21760\n"
"s_add_u32 s31, s31, 1024\n"
"v_mfma_f32_16x16x32_bf16 v[52:55], v[72:75], v[72:75], v[52:55]\n"
"v_mfma_f32_16x16x32_bf16 v[28:31], v[232:235], v[76:79], v[28:31]\n"
"ds_read_b64 v[72:73], v3 offset:22528\n"
"buffer_load_dwordx4 v1, s[4:7], s31 offen offset:3072 sc1 lds\n"
"v_mfma_f32_16x16x32_bf16 v[32:35], v[236:239], v[76:79], v[32:35]\n"
"ds_read_b64 v[74:75], v3 offset:22784\n"
"s_waitcnt lgkmcnt(6)\n"
"v_mfma_f32_16x16x32_bf16 v[28:31], v[240:243], v[56:59], v[28:31]\n"
"ds_read_b64 v[76:77], v3 offset:23552\n"
"v_cndmask_b32 v94, v52, v53, s[34:35]\n"
"v_mfma_f32_16x16x32_bf16 v[32:35], v[244:247], v[56:59], v[32:35]\n"
"ds_read_b64 v[78:79], v3 offset:23808\n"
"v_cndmask_b32 v54, v54, v55, s[34:35]\n"
"v_mfma_f32_16x16x32_bf16 v[28:31], v[248:251], v[60:63], v[28:31]\n"
"ds_read_b64 v[56:57], v3 offset:24576\n"
"v_cndmask_b32 v94, v94, v54, s[20:21]\n"
"v_mfma_f32_16x16x32_bf16 v[32:35], v[252:255], v[60:63], v[32:35]\n"
"ds_read_b64 v[58:59], v3 offset:24832\n"
"s_waitcnt lgkmcnt(6)\n"
"v_mfma_f32_16x16x32_bf16 v[36:39], v[96:99], v[64:67], 0\n"
"ds_read_b64 v[60:61], v3 offset:25600\n"
"v_mfma_f32_16x16x32_bf16 v[40:43], v[100:103], v[64:67], 0\n"
"ds_read_b64 v[62:63], v3 offset:25856\n"
"v_cndmask_b32 v94, v28, v94, s[22:23]\n"
"s_waitcnt vmcnt(5)\n"
"v_mfma_f32_16x16x32_bf16 v[52:55], v[64:67], v[64:67], 0\n"
"ds_write_b32 v9, v94 offset:1856\n"
"v_mfma_f32_16x16x32_bf16 v[36:39], v[104:107], v[68:71], v[36:39]\n"
"ds_read_b64 v[64:65], v3 offset:26624\n"
"v_mfma_f32_16x16x32_bf16 v[40:43], v[108:111], v[68:71], v[40:43]\n"
"ds_read_b64 v[66:67], v3 offset:26880\n"
"s_waitcnt lgkmcnt(7)\n"
"v_mfma_f32_16x16x32_bf16 v[36:39], v[112:115], v[72:75], v[36:39]\n"
"ds_read_b64 v[68:69], v3 offset:27648\n"
"v_mfma_f32_16x16x32_bf16 v[40:43], v[116:119], v[72:75], v[40:43]\n"
"ds_read_b64 v[70:71], v3 offset:27904\n"
"v_mfma_f32_16x16x32_bf16 v[36:39], v[120:123], v[76:79], v[36:39]\n"
"ds_read_b64 v[72:73], v3 offset:28672\n"
"v_mfma_f32_16x16x32_bf16 v[40:43], v[124:127], v[76:79], v[40:43]\n"
"ds_read_b64 v[74:75], v3 offset:28928\n"
"s_waitcnt lgkmcnt(7)\n"
"v_mfma_f32_16x16x32_bf16 v[36:39], v[128:131], v[56:59], v[36:39]\n"
"ds_read_b64 v[76:77], v3 offset:29696\n"
"v_mfma_f32_16x16x32_bf16 v[40:43], v[132:135], v[56:59], v[40:43]\n"
"ds_read_b64 v[78:79], v3 offset:29952\n"
"v_mfma_f32_16x16x32_bf16 v[52:55], v[56:59], v[56:59], v[52:55]\n"
"v_mfma_f32_16x16x32_bf16 v[36:39], v[136:139], v[60:63], v[36:39]\n"
"ds_read_b64 v[56:57], v3 offset:30720\n"
"v_mfma_f32_16x16x32_bf16 v[40:43], v[140:143], v[60:63], v[40:43]\n"
"ds_read_b64 v[58:59], v3 offset:30976\n"
"s_waitcnt lgkmcnt(6)\n"
"v_mfma_f32_16x16x32_bf16 v[36:39], v[144:147], v[64:67], v[36:39]\n"
"ds_read_b64 v[60:61], v3 offset:31744\n"
"ds_read_b128 v[84:87], v11 offset:1856\n"
"ds_read_b32 v91, v12 offset:1856\n"
"v_mfma_f32_16x16x32_bf16 v[40:43], v[148:151], v[64:67], v[40:43]\n"
"ds_read_b64 v[62:63], v3 offset:32000\n"
"v_mfma_f32_16x16x32_bf16 v[36:39], v[152:155], v[68:71], v[36:39]\n"
"ds_read_b64 v[64:65], v3 offset:32768\n"
"v_mfma_f32_16x16x32_bf16 v[40:43], v[156:159], v[68:71], v[40:43]\n"
"ds_read_b64 v[66:67], v3 offset:33024\n"
"s_waitcnt lgkmcnt(8)\n"
"v_mfma_f32_16x16x32_bf16 v[36:39], v[160:163], v[72:75], v[36:39]\n"
"ds_read_b64 v[68:69], v3 offset:33792\n"
"v_mfma_f32_16x16x32_bf16 v[40:43], v[164:167], v[72:75], v[40:43]\n"
"ds_read_b64 v[70:71], v3 offset:34048\n"
"v_mfma_f32_16x16x32_bf16 v[52:55], v[72:75], v[72:75], v[52:55]\n"
"v_mfma_f32_16x16x32_bf16 v[36:39], v[168:171], v[76:79], v[36:39]\n"
"ds_read_b64 v[72:73], v3 offset:34816\n"
"s_waitcnt lgkmcnt(7)\n"
"v_add_f32 v84, v84, v85\n"
"v_add_f32 v86, v86, v87\n"
"v_mfma_f32_16x16x32_bf16 v[40:43], v[172:175], v[76:79], v[40:43]\n"
"ds_read_b64 v[74:75], v3 offset:35072\n"
"v_add_f32 v84, v84, v86\n"
"s_waitcnt lgkmcnt(6)\n"
"v_mfma_f32_16x16x32_bf16 v[36:39], v[176:179], v[56:59], v[36:39]\n"
"ds_read_b64 v[76:77], v3 offset:35840\n"
"v_mul_f32 v89, 0x3acccccd, v91\n"
"v_fma_f32 v84, -v91, v89, v84\n"
"v_mfma_f32_16x16x32_bf16 v[40:43], v[180:183], v[56:59], v[40:43]\n"
"ds_read_b64 v[78:79], v3 offset:36096\n"
"v_fmamk_f32 v84, v84, 0x3acccccd, v27\n"
"v_mfma_f32_16x16x32_bf16 v[36:39], v[184:187], v[60:63], v[36:39]\n"
"ds_read_b64 v[56:57], v3 offset:36864\n"
"v_cmp_u_f32 vcc, v84, v84\n"
"v_mfma_f32_16x16x32_bf16 v[40:43], v[188:191], v[60:63], v[40:43]\n"
"ds_read_b64 v[58:59], v3 offset:37120\n"
"s_waitcnt lgkmcnt(6)\n"
"v_mfma_f32_16x16x32_bf16 v[36:39], v[192:195], v[64:67], v[36:39]\n"
"ds_read_b64 v[60:61], v4 offset:20480\n"
"v_max_f32 v84, v84, v27\n"
"v_mfma_f32_16x16x32_bf16 v[40:43], v[196:199], v[64:67], v[40:43]\n"
"ds_read_b64 v[62:63], v4 offset:20736\n"
"v_rsq_f32 v88, v84\n"
"v_mfma_f32_16x16x32_bf16 v[52:55], v[64:67], v[64:67], v[52:55]\n"
"v_mul_f32 v89, -v89, v88\n"
"v_mfma_f32_16x16x32_bf16 v[36:39], v[200:203], v[68:71], v[36:39]\n"
"ds_read_b64 v[64:65], v5 offset:20480\n"
"s_cbranch_vccnz .Lsl_127\n"
".Lbk_128:\n"
"v_mfma_f32_16x16x32_bf16 v[40:43], v[204:207], v[68:71], v[40:43]\n"
"ds_read_b64 v[66:67], v5 offset:20736\n"
"s_add_u32 m0, s29, 0\n"
"s_waitcnt lgkmcnt(6)\n"
"v_mfma_f32_16x16x32_bf16 v[36:39], v[208:211], v[72:75], v[36:39]\n"
"ds_read_b64 v[68:69], v6 offset:20480\n"
"s_sub_u32 s79, s44, 0x5000\n"
"s_add_u32 s78, s29, 0xc28000\n"
"v_mfma_f32_16x16x32_bf16 v[40:43], v[212:215], v[72:75], v[40:43]\n"
"ds_read_b64 v[70:71], v6 offset:20736\n"
"s_max_i32 s31, s79, s78\n"
"v_mul_f32 v28, v88, v28\n"
"v_mfma_f32_16x16x32_bf16 v[36:39], v[216:219], v[76:79], v[36:39]\n"
"ds_read_b64 v[72:73], v3 offset:40960\n"
"buffer_load_dwordx4 v1, s[4:7], s31 offen offset:0 sc1 lds\n"
"v_mul_f32 v29, v88, v29\n"
"v_mfma_f32_16x16x32_bf16 v[40:43], v[220:223], v[76:79], v[40:43]\n"
"ds_read_b64 v[74:75], v3 offset:41216\n"
"s_waitcnt lgkmcnt(6)\n"
"v_mfma_f32_16x16x32_bf16 v[36:39], v[224:227], v[56:59], v[36:39]\n"
"ds_read_b64 v[76:77], v3 offset:41984\n"
"v_mul_f32 v30, v88, v30\n"
"v_mfma_f32_16x16x32_bf16 v[40:43], v[228:231], v[56:59], v[40:43]\n"
"ds_read_b64 v[78:79], v3 offset:42240\n"
"v_mul_f32 v31, v88, v31\n"
"v_mfma_f32_16x16x32_bf16 v[52:55], v[56:59], v[56:59], v[52:55]\n"
"buffer_load_dwordx4 v1, s[4:7], s31 offen offset:1024 sc1 lds\n"
"v_fmac_f32 v28, v89, v16\n"
"v_mfma_f32_16x16x32_bf16 v[36:39], v[232:235], v[60:63], v[36:39]\n"
"ds_read_b64 v[56:57], v3 offset:43008\n"
"v_fmac_f32 v29, v89, v17\n"
"v_mfma_f32_16x16x32_bf16 v[40:43], v[236:239], v[60:63], v[40:43]\n"
"ds_read_b64 v[58:59], v3 offset:43264\n"
"s_waitcnt lgkmcnt(6)\n"
"v_mfma_f32_16x16x32_bf16 v[36:39], v[240:243], v[64:67], v[36:39]\n"
"ds_read_b64 v[60:61], v3 offset:44032\n"
"v_cndmask_b32 v94, v52, v53, s[34:35]\n"
"v_mfma_f32_16x16x32_bf16 v[40:43], v[244:247], v[64:67], v[40:43]\n"
"ds_read_b64 v[62:63], v3 offset:44288\n"
"v_cndmask_b32 v54, v54, v55, s[34:35]\n"
"buffer_load_dwordx4 v1, s[4:7], s31 offen offset:2048 sc1 lds\n"
"v_mfma_f32_16x16x32_bf16 v[36:39], v[248:251], v[68:71], v[36:39]\n"
"ds_read_b64 v[64:65], v3 offset:45056\n"
"v_cndmask_b32 v94, v94, v54, s[20:21]\n"
"v_mfma_f32_16x16x32_bf16 v[40:43], v[252:255], v[68:71], v[40:43]\n"
"ds_read_b64 v[66:67], v3 offset:45312\n"
"s_waitcnt lgkmcnt(6)\n"
"v_mfma_f32_16x16x32_bf16 v[44:47], v[96:99], v[72:75], 0\n"
"ds_read_b64 v[68:69], v3 offset:46080\n"
"v_fmac_f32 v30, v89, v18\n"
"v_mfma_f32_16x16x32_bf16 v[48:51], v[100:103], v[72:75], 0\n"
"ds_read_b64 v[70:71], v3 offset:46336\n"
"v_cndmask_b32 v94, v36, v94, s[22:23]\n"
"s_waitcnt vmcnt(3)\n"
"buffer_load_dwordx4 v1, s[4:7], s31 offen offset:3072 sc1 lds\n"
"v_mfma_f32_16x16x32_bf16 v[52:55], v[72:75], v[72:75], 0\n"
"ds_write_b32 v9, v94 offset:2176\n"
"s_add_u32 m0, m0, 1024\n"
"v_mfma_f32_16x16x32_bf16 v[44:47], v[104:107], v[76:79], v[44:47]\n"
"ds_read_b64 v[72:73], v3 offset:47104\n"
"s_add_u32 s31, s31, 1024\n"
"v_mfma_f32_16x16x32_bf16 v[48:51], v[108:111], v[76:79], v[48:51]\n"
"ds_read_b64 v[74:75], v3 offset:47360\n"
"s_waitcnt lgkmcnt(7)\n"
"v_mfma_f32_16x16x32_bf16 v[44:47], v[112:115], v[56:59], v[44:47]\n"
"ds_read_b64 v[76:77], v3 offset:48128\n"
"buffer_load_dwordx4 v1, s[4:7], s31 offen offset:3072 sc1 lds\n"
"v_mfma_f32_16x16x32_bf16 v[48:51], v[116:119], v[56:59], v[48:51]\n"
"ds_read_b64 v[78:79], v3 offset:48384\n"
"v_fmac_f32 v31, v89, v19\n"
"v_mfma_f32_16x16x32_bf16 v[44:47], v[120:123], v[60:63], v[44:47]\n"
"ds_read_b64 v[56:57], v3 offset:49152\n"
"v_cvt_pk_bf16_f32 v28, v28, v29\n"
"v_mfma_f32_16x16x32_bf16 v[48:51], v[124:127], v[60:63], v[48:51]\n"
"ds_read_b64 v[58:59], v3 offset:49408\n"
"s_waitcnt lgkmcnt(7)\n"
"v_mfma_f32_16x16x32_bf16 v[44:47], v[128:131], v[64:67], v[44:47]\n"
"ds_read_b64 v[60:61], v3 offset:50176\n"
"v_cvt_pk_bf16_f32 v29, v30, v31\n"
"v_mfma_f32_16x16x32_bf16 v[48:51], v[132:135], v[64:67], v[48:51]\n"
"ds_read_b64 v[62:63], v3 offset:50432\n"
"v_mul_f32 v32, v88, v32\n"
"v_mfma_f32_16x16x32_bf16 v[52:55], v[64:67], v[64:67], v[52:55]\n"
"v_mul_f32 v33, v88, v33\n"
"v_mul_f32 v34, v88, v34\n"
"v_mfma_f32_16x16x32_bf16 v[44:47], v[136:139], v[68:71], v[44:47]\n"
"ds_read_b64 v[64:65], v3 offset:51200\n"
"v_mul_f32 v35, v88, v35\n"
"v_mfma_f32_16x16x32_bf16 v[48:51], v[140:143], v[68:71], v[48:51]\n"
"ds_read_b64 v[66:67], v3 offset:51456\n"
"s_waitcnt lgkmcnt(6)\n"
"v_mfma_f32_16x16x32_bf16 v[44:47], v[144:147], v[72:75], v[44:47]\n"
"ds_read_b64 v[68:69], v3 offset:52224\n"
"ds_read_b128 v[84:87], v11 offset:2176\n"
"ds_read_b32 v91, v12 offset:2176\n"
"v_mfma_f32_16x16x32_bf16 v[48:51], v[148:151], v[72:75], v[48:51]\n"
"ds_read_b64 v[70:71], v3 offset:52480\n"
"v_fmac_f32 v32, v89, v20\n"
"v_mfma_f32_16x16x32_bf16 v[44:47], v[152:155], v[76:79], v[44:47]\n"
"ds_read_b64 v[72:73], v3 offset:53248\n"
"v_fmac_f32 v33, v89, v21\n"
"v_mfma_f32_16x16x32_bf16 v[48:51], v[156:159], v[76:79], v[48:51]\n"
"ds_read_b64 v[74:75], v3 offset:53504\n"
"s_waitcnt lgkmcnt(8)\n"
"v_mfma_f32_16x16x32_bf16 v[44:47], v[160:163], v[56:59], v[44:47]\n"
"ds_read_b64 v[76:77], v3 offset:54272\n"
"v_fmac_f32 v34, v89, v22\n"
"v_mfma_f32_16x16x32_bf16 v[48:51], v[164:167], v[56:59], v[48:51]\n"
"ds_read_b64 v[78:79], v3 offset:54528\n"
"v_fmac_f32 v35, v89, v23\n"
"v_mfma_f32_16x16x32_bf16 v[52:55], v[56:59], v[56:59], v[52:55]\n"
"v_cvt_pk_bf16_f32 v32, v32, v33\n"
"v_cvt_pk_bf16_f32 v33, v34, v35\n"
"v_mfma_f32_16x16x32_bf16 v[44:47], v[168:171], v[60:63], v[44:47]\n"
"ds_read_b64 v[56:57], v3 offset:55296\n"
"s_waitcnt lgkmcnt(7)\n"
"v_add_f32 v84, v84, v85\n"
"v_add_f32 v86, v86, v87\n"
"v_mfma_f32_16x16x32_bf16 v[48:51], v[172:175], v[60:63], v[48:51]\n"
"ds_read_b64 v[58:59], v3 offset:55552\n"
"v_add_f32 v84, v84, v86\n"
"s_waitcnt lgkmcnt(6)\n"
"v_mfma_f32_16x16x32_bf16 v[44:47], v[176:179], v[64:67], v[44:47]\n"
"ds_read_b64 v[60:61], v3 offset:56320\n"
"v_mul_f32 v89, 0x3acccccd, v91\n"
"v_fma_f32 v84, -v91, v89, v84\n"
"v_mfma_f32_16x16x32_bf16 v[48:51], v[180:183], v[64:67], v[48:51]\n"
"ds_read_b64 v[62:63], v3 offset:56576\n"
"v_fmamk_f32 v84, v84, 0x3acccccd, v27\n"
"v_mfma_f32_16x16x32_bf16 v[44:47], v[184:187], v[68:71], v[44:47]\n"
"ds_read_b64 v[64:65], v3 offset:57344\n"
"v_cmp_u_f32 vcc, v84, v84\n"
"v_mfma_f32_16x16x32_bf16 v[48:51], v[188:191], v[68:71], v[48:51]\n"
"ds_read_b64 v[66:67], v3 offset:57600\n"
"s_waitcnt lgkmcnt(6)\n"
"v_mfma_f32_16x16x32_bf16 v[44:47], v[192:195], v[72:75], v[44:47]\n"
"ds_read_b64 v[68:69], v4 offset:40960\n"
"v_max_f32 v84, v84, v27\n"
"v_mfma_f32_16x16x32_bf16 v[48:51], v[196:199], v[72:75], v[48:51]\n"
"ds_read_b64 v[70:71], v4 offset:41216\n"
"v_rsq_f32 v88, v84\n"
"v_mfma_f32_16x16x32_bf16 v[52:55], v[72:75], v[72:75], v[52:55]\n"
"v_mul_f32 v89, -v89, v88\n"
"v_mfma_f32_16x16x32_bf16 v[44:47], v[200:203], v[76:79], v[44:47]\n"
"ds_read_b64 v[72:73], v5 offset:40960\n"
"s_cbranch_vccnz .Lsl_131\n"
".Lbk_132:\n"
"v_mfma_f32_16x16x32_bf16 v[48:51], v[204:207], v[76:79], v[48:51]\n"
"ds_read_b64 v[74:75], v5 offset:41216\n"
"ds_write_b64 v7, v[28:29] offset:3968\n"
"s_waitcnt lgkmcnt(7)\n"
"v_mfma_f32_16x16x32_bf16 v[44:47], v[208:211], v[56:59], v[44:47]\n"
"ds_read_b64 v[76:77], v6 offset:40960\n"
"v_mul_f32 v36, v88, v36\n"
"v_mfma_f32_16x16x32_bf16 v[48:51], v[212:215], v[56:59], v[48:51]\n"
"ds_read_b64 v[78:79], v6 offset:41216\n"
"v_mul_f32 v37, v88, v37\n"
"v_mfma_f32_16x16x32_bf16 v[44:47], v[216:219], v[60:63], v[44:47]\n"
"v_add_u32 v3, 0x5000, v3\n"
"ds_read_b64 v[56:57], v3 offset:40960\n"
"v_mfma_f32_16x16x32_bf16 v[48:51], v[220:223], v[60:63], v[48:51]\n"
"ds_read_b64 v[58:59], v3 offset:41216\n"
"s_waitcnt lgkmcnt(7)\n"
"v_mfma_f32_16x16x32_bf16 v[44:47], v[224:227], v[64:67], v[44:47]\n"
"ds_read_b64 v[60:61], v3 offset:41984\n"
"v_mul_f32 v38, v88, v38\n"
"v_mfma_f32_16x16x32_bf16 v[48:51], v[228:231], v[64:67], v[48:51]\n"
"ds_read_b64 v[62:63], v3 offset:42240\n"
"v_mov_b32 v84, 0x3f803f80\n"
"v_mfma_f32_16x16x32_bf16 v[52:55], v[64:67], v[64:67], v[52:55]\n"
"v_mov_b32 v85, 0x3f803f80\n"
"ds_write_b64 v8, v[32:33] offset:3968\n"
"v_mfma_f32_16x16x32_bf16 v[44:47], v[232:235], v[68:71], v[44:47]\n"
"ds_read_b64 v[64:65], v3 offset:43008\n"
"v_mul_f32 v39, v88, v39\n"
"v_mfma_f32_16x16x32_bf16 v[48:51], v[236:239], v[68:71], v[48:51]\n"
"ds_read_b64 v[66:67], v3 offset:43264\n"
"s_waitcnt lgkmcnt(7)\n"
"v_mfma_f32_16x16x32_bf16 v[44:47], v[240:243], v[72:75], v[44:47]\n"
"ds_read_b64 v[68:69], v3 offset:44032\n"
"v_cndmask_b32 v94, v52, v53, s[34:35]\n"
"v_mfma_f32_16x16x32_bf16 v[48:51], v[244:247], v[72:75], v[48:51]\n"
"ds_read_b64 v[70:71], v3 offset:44288\n"
"v_cndmask_b32 v54, v54, v55, s[34:35]\n"
"v_mfma_f32_16x16x32_bf16 v[44:47], v[248:251], v[76:79], v[44:47]\n"
"ds_read_b64 v[72:73], v3 offset:45056\n"
"v_cndmask_b32 v94, v94, v54, s[20:21]\n"
"v_mfma_f32_16x16x32_bf16 v[48:51], v[252:255], v[76:79], v[48:51]\n"
"ds_read_b64 v[74:75], v3 offset:45312\n"
"s_waitcnt lgkmcnt(7)\n"
"v_mfma_f32_16x16x32_bf16 v[28:31], v[96:99], v[56:59], 0\n"
"ds_read_b64 v[76:77], v3 offset:46080\n"
"v_fmac_f32 v36, v89, v16\n"
"v_mfma_f32_16x16x32_bf16 v[32:35], v[100:103], v[56:59], 0\n"
"ds_read_b64 v[78:79], v3 offset:46336\n"
"v_cndmask_b32 v94, v44, v94, s[22:23]\n"
"s_waitcnt vmcnt(0)\n"
"ds_write_b64 v93, v[84:85] offset:0\n"
"v_mfma_f32_16x16x32_bf16 v[52:55], v[56:59], v[56:59], 0\n"
"ds_write_b32 v9, v94 offset:2496\n"
"v_mfma_f32_16x16x32_bf16 v[28:31], v[104:107], v[60:63], v[28:31]\n"
"ds_read_b64 v[56:57], v3 offset:47104\n"
"v_fmac_f32 v37, v89, v17\n"
"v_mfma_f32_16x16x32_bf16 v[32:35], v[108:111], v[60:63], v[32:35]\n"
"ds_read_b64 v[58:59], v3 offset:47360\n"
"s_waitcnt lgkmcnt(8)\n"
"v_mfma_f32_16x16x32_bf16 v[28:31], v[112:115], v[64:67], v[28:31]\n"
"ds_read_b64 v[60:61], v3 offset:48128\n"
"v_fmac_f32 v38, v89, v18\n"
"v_mfma_f32_16x16x32_bf16 v[32:35], v[116:119], v[64:67], v[32:35]\n"
"ds_read_b64 v[62:63], v3 offset:48384\n"
"v_fmac_f32 v39, v89, v19\n"
"v_mfma_f32_16x16x32_bf16 v[28:31], v[120:123], v[68:71], v[28:31]\n"
"ds_read_b64 v[64:65], v3 offset:49152\n"
"v_cvt_pk_bf16_f32 v36, v36, v37\n"
"v_mfma_f32_16x16x32_bf16 v[32:35], v[124:127], v[68:71], v[32:35]\n"
"ds_read_b64 v[66:67], v3 offset:49408\n"
"s_waitcnt lgkmcnt(8)\n"
"v_mfma_f32_16x16x32_bf16 v[28:31], v[128:131], v[72:75], v[28:31]\n"
"ds_read_b64 v[68:69], v3 offset:50176\n"
"v_cvt_pk_bf16_f32 v37, v38, v39\n"
"v_mfma_f32_16x16x32_bf16 v[32:35], v[132:135], v[72:75], v[32:35]\n"
"ds_read_b64 v[70:71], v3 offset:50432\n"
"v_mul_f32 v40, v88, v40\n"
"v_mfma_f32_16x16x32_bf16 v[52:55], v[72:75], v[72:75], v[52:55]\n"
"v_mul_f32 v41, v88, v41\n"
"v_mul_f32 v42, v88, v42\n"
"v_mfma_f32_16x16x32_bf16 v[28:31], v[136:139], v[76:79], v[28:31]\n"
"ds_read_b64 v[72:73], v3 offset:51200\n"
"v_mul_f32 v43, v88, v43\n"
"v_mfma_f32_16x16x32_bf16 v[32:35], v[140:143], v[76:79], v[32:35]\n"
"ds_read_b64 v[74:75], v3 offset:51456\n"
"s_waitcnt lgkmcnt(6)\n"
"v_mfma_f32_16x16x32_bf16 v[28:31], v[144:147], v[56:59], v[28:31]\n"
"ds_read_b64 v[76:77], v3 offset:52224\n"
"ds_read_b128 v[84:87], v11 offset:2496\n"
"ds_read_b32 v91, v12 offset:2496\n"
"v_mfma_f32_16x16x32_bf16 v[32:35], v[148:151], v[56:59], v[32:35]\n"
"ds_read_b64 v[78:79], v3 offset:52480\n"
"v_fmac_f32 v40, v89, v20\n"
"v_mfma_f32_16x16x32_bf16 v[28:31], v[152:155], v[60:63], v[28:31]\n"
"ds_read_b64 v[56:57], v3 offset:53248\n"
"v_fmac_f32 v41, v89, v21\n"
"v_mfma_f32_16x16x32_bf16 v[32:35], v[156:159], v[60:63], v[32:35]\n"
"ds_read_b64 v[58:59], v3 offset:53504\n"
"s_waitcnt lgkmcnt(8)\n"
"v_mfma_f32_16x16x32_bf16 v[28:31], v[160:163], v[64:67], v[28:31]\n"
"ds_read_b64 v[60:61], v3 offset:54272\n"
"v_fmac_f32 v42, v89, v22\n"
"v_mfma_f32_16x16x32_bf16 v[32:35], v[164:167], v[64:67], v[32:35]\n"
"ds_read_b64 v[62:63], v3 offset:54528\n"
"v_fmac_f32 v43, v89, v23\n"
"v_mfma_f32_16x16x32_bf16 v[52:55], v[64:67], v[64:67], v[52:55]\n"
"v_cvt_pk_bf16_f32 v40, v40, v41\n"
"v_cvt_pk_bf16_f32 v41, v42, v43\n"
"v_mfma_f32_16x16x32_bf16 v[28:31], v[168:171], v[68:71], v[28:31]\n"
"ds_read_b64 v[64:65], v3 offset:55296\n"
"s_waitcnt lgkmcnt(7)\n"
"v_add_f32 v84, v84, v85\n"
"v_add_f32 v86, v86, v87\n"
"v_mfma_f32_16x16x32_bf16 v[32:35], v[172:175], v[68:71], v[32:35]\n"
"ds_read_b64 v[66:67], v3 offset:55552\n"
"v_add_f32 v84, v84, v86\n"
"s_waitcnt lgkmcnt(6)\n"
"v_mfma_f32_16x16x32_bf16 v[28:31], v[176:179], v[72:75], v[28:31]\n"
"ds_read_b64 v[68:69], v3 offset:56320\n"
"v_mul_f32 v89, 0x3acccccd, v91\n"
"v_fma_f32 v84, -v91, v89, v84\n"
"v_mfma_f32_16x16x32_bf16 v[32:35], v[180:183], v[72:75], v[32:35]\n"
"ds_read_b64 v[70:71], v3 offset:56576\n"
"v_fmamk_f32 v84, v84, 0x3acccccd, v27\n"
"v_mfma_f32_16x16x32_bf16 v[28:31], v[184:187], v[76:79], v[28:31]\n"
"ds_read_b64 v[72:73], v3 offset:57344\n"
"v_cmp_u_f32 vcc, v84, v84\n"
"v_mfma_f32_16x16x32_bf16 v[32:35], v[188:191], v[76:79], v[32:35]\n"
"ds_read_b64 v[74:75], v3 offset:57600\n"
"s_waitcnt lgkmcnt(6)\n"
"v_mfma_f32_16x16x32_bf16 v[28:31], v[192:195], v[56:59], v[28:31]\n"
"ds_read_b64 v[76:77], v4 offset:61440\n"
"v_max_f32 v84, v84, v27\n"
"v_mfma_f32_16x16x32_bf16 v[32:35], v[196:199], v[56:59], v[32:35]\n"
"ds_read_b64 v[78:79], v4 offset:61696\n"
"v_rsq_f32 v88, v84\n"
"v_mfma_f32_16x16x32_bf16 v[52:55], v[56:59], v[56:59], v[52:55]\n"
"v_mul_f32 v89, -v89, v88\n"
"v_mfma_f32_16x16x32_bf16 v[28:31], v[200:203], v[60:63], v[28:31]\n"
"ds_read_b64 v[56:57], v5 offset:61440\n"
"s_cbranch_vccnz .Lsl_135\n"
".Lbk_136:\n"
"v_mfma_f32_16x16x32_bf16 v[32:35], v[204:207], v[60:63], v[32:35]\n"
"ds_read_b64 v[58:59], v5 offset:61696\n"
"ds_write_b64 v7, v[36:37] offset:7936\n"
"s_waitcnt lgkmcnt(7)\n"
"v_mfma_f32_16x16x32_bf16 v[28:31], v[208:211], v[64:67], v[28:31]\n"
"ds_read_b64 v[60:61], v6 offset:61440\n"
"v_mul_f32 v44, v88, v44\n"
"v_mul_f32 v45, v88, v45\n"
"v_mfma_f32_16x16x32_bf16 v[32:35], v[212:215], v[64:67], v[32:35]\n"
"ds_read_b64 v[62:63], v6 offset:61696\n"
"v_mul_f32 v46, v88, v46\n"
"v_mul_f32 v47, v88, v47\n"
"v_mfma_f32_16x16x32_bf16 v[28:31], v[216:219], v[68:71], v[28:31]\n"
"v_add_u32 v3, 0xffffb000, v3\n"
"ds_read_b64 v[64:65], v3 offset:0\n"
"v_fmac_f32 v44, v89, v16\n"
"v_mfma_f32_16x16x32_bf16 v[32:35], v[220:223], v[68:71], v[32:35]\n"
"ds_read_b64 v[66:67], v3 offset:256\n"
"v_fmac_f32 v45, v89, v17\n"
"s_waitcnt lgkmcnt(7)\n"
"v_mfma_f32_16x16x32_bf16 v[28:31], v[224:227], v[72:75], v[28:31]\n"
"ds_read_b64 v[68:69], v3 offset:4096\n"
"v_fmac_f32 v46, v89, v18\n"
"v_fmac_f32 v47, v89, v19\n"
"v_mfma_f32_16x16x32_bf16 v[32:35], v[228:231], v[72:75], v[32:35]\n"
"ds_read_b64 v[70:71], v3 offset:4352\n"
"v_cvt_pk_bf16_f32 v44, v44, v45\n"
"v_cvt_pk_bf16_f32 v45, v46, v47\n"
"v_mfma_f32_16x16x32_bf16 v[52:55], v[72:75], v[72:75], v[52:55]\n"
"ds_write_b64 v8, v[40:41] offset:7936\n"
"v_mfma_f32_16x16x32_bf16 v[28:31], v[232:235], v[76:79], v[28:31]\n"
"ds_read_b64 v[72:73], v3 offset:8192\n"
"v_mul_f32 v48, v88, v48\n"
"v_mul_f32 v49, v88, v49\n"
"v_mfma_f32_16x16x32_bf16 v[32:35], v[236:239], v[76:79], v[32:35]\n"
"ds_read_b64 v[74:75], v3 offset:8448\n"
"v_mul_f32 v50, v88, v50\n"
"s_waitcnt lgkmcnt(7)\n"
"v_mfma_f32_16x16x32_bf16 v[28:31], v[240:243], v[56:59], v[28:31]\n"
"ds_read_b64 v[76:77], v3 offset:12288\n"
"v_cndmask_b32 v94, v52, v53, s[34:35]\n"
"v_mul_f32 v51, v88, v51\n"
"v_mfma_f32_16x16x32_bf16 v[32:35], v[244:247], v[56:59], v[32:35]\n"
"ds_read_b64 v[78:79], v3 offset:12544\n"
"v_cndmask_b32 v54, v54, v55, s[34:35]\n"
"v_fmac_f32 v48, v89, v20\n"
"v_mfma_f32_16x16x32_bf16 v[28:31], v[248:251], v[60:63], v[28:31]\n"
"ds_read_b64 v[56:57], v3 offset:16384\n"
"v_cndmask_b32 v94, v94, v54, s[20:21]\n"
"v_fmac_f32 v49, v89, v21\n"
"v_mfma_f32_16x16x32_bf16 v[32:35], v[252:255], v[60:63], v[32:35]\n"
"ds_read_b64 v[58:59], v3 offset:16640\n"
"v_fmac_f32 v50, v89, v22\n"
"s_waitcnt lgkmcnt(7)\n"
"s_cmp_eq_u32 s3, 3\n"
"s_cbranch_scc1 .Lfu_147\n"
"ds_read_b64 v[60:61], v3 offset:1024\n"
"v_fmac_f32 v51, v89, v23\n"
"v_mfma_f32_16x16x32_bf16 v[40:43], v[100:103], v[64:67], 0\n"
"ds_read_b64 v[62:63], v3 offset:1280\n"
"v_cndmask_b32 v94, v28, v94, s[22:23]\n"
"v_mfma_f32_16x16x32_bf16 v[52:55], v[64:67], v[64:67], 0\n"
"ds_write_b32 v9, v94 offset:2816\n"
"ds_read_b64 v[64:65], v3 offset:2048\n"
"v_cvt_pk_bf16_f32 v48, v48, v49\n"
"v_mfma_f32_16x16x32_bf16 v[40:43], v[132:135], v[68:71], v[40:43]\n"
"ds_read_b64 v[66:67], v3 offset:2304\n"
"v_cvt_pk_bf16_f32 v49, v50, v51\n"
"v_mfma_f32_16x16x32_bf16 v[52:55], v[68:71], v[68:71], v[52:55]\n"
"ds_write_b64 v7, v[44:45] offset:11904\n"
"s_waitcnt lgkmcnt(8)\n"
"ds_read_b64 v[68:69], v3 offset:3072\n"
"v_mfma_f32_16x16x32_bf16 v[40:43], v[164:167], v[72:75], v[40:43]\n"
"ds_read_b64 v[70:71], v3 offset:3328\n"
"v_mfma_f32_16x16x32_bf16 v[52:55], v[72:75], v[72:75], v[52:55]\n"
"ds_write_b64 v8, v[48:49] offset:11904\n"
"ds_read_b64 v[72:73], v3 offset:5120\n"
"v_mfma_f32_16x16x32_bf16 v[40:43], v[196:199], v[76:79], v[40:43]\n"
"ds_read_b64 v[74:75], v3 offset:5376\n"
"v_mfma_f32_16x16x32_bf16 v[52:55], v[76:79], v[76:79], v[52:55]\n"
"ds_read_b128 v[84:87], v11 offset:2816\n"
"ds_read_b32 v91, v12 offset:2816\n"
"s_waitcnt lgkmcnt(11)\n"
"ds_read_b64 v[76:77], v3 offset:6144\n"
"v_mfma_f32_16x16x32_bf16 v[40:43], v[228:231], v[56:59], v[40:43]\n"
"ds_read_b64 v[78:79], v3 offset:6400\n"
"v_mfma_f32_16x16x32_bf16 v[52:55], v[56:59], v[56:59], v[52:55]\n"
"ds_read_b64 v[56:57], v3 offset:7168\n"
"v_mfma_f32_16x16x32_bf16 v[40:43], v[108:111], v[60:63], v[40:43]\n"
"ds_read_b64 v[58:59], v3 offset:7424\n"
"s_waitcnt lgkmcnt(9)\n"
"ds_read_b64 v[60:61], v3 offset:9216\n"
"s_nop 1\n"
"ds_write_b32 v10, v52 offset:3456\n"
"v_cndmask_b32 v94, v52, v53, s[34:35]\n"
"v_cndmask_b32 v54, v54, v55, s[34:35]\n"
"v_mfma_f32_16x16x32_bf16 v[40:43], v[116:119], v[64:67], v[40:43]\n"
"ds_read_b64 v[62:63], v3 offset:9472\n"
"s_waitcnt lgkmcnt(8)\n"
"v_add_f32 v84, v84, v85\n"
"v_add_f32 v86, v86, v87\n"
"v_cndmask_b32 v94, v94, v54, s[20:21]\n"
"ds_write_b32 v9, v94 offset:3136\n"
"ds_read_b64 v[64:65], v3 offset:10240\n"
"v_add_f32 v84, v84, v86\n"
"s_waitcnt lgkmcnt(9)\n"
"v_mul_f32 v89, 0x3acccccd, v91\n"
"v_mfma_f32_16x16x32_bf16 v[40:43], v[124:127], v[68:71], v[40:43]\n"
"ds_read_b64 v[66:67], v3 offset:10496\n"
"v_fma_f32 v84, -v91, v89, v84\n"
"s_waitcnt lgkmcnt(8)\n"
"ds_read_b64 v[68:69], v3 offset:11264\n"
"v_fmamk_f32 v84, v84, 0x3acccccd, v27\n"
"v_cmp_u_f32 vcc, v84, v84\n"
"v_mfma_f32_16x16x32_bf16 v[40:43], v[140:143], v[72:75], v[40:43]\n"
"ds_read_b64 v[70:71], v3 offset:11520\n"
"v_max_f32 v84, v84, v27\n"
"v_rsq_f32 v88, v84\n"
"ds_read_b64 v[72:73], v3 offset:13312\n"
"v_mul_f32 v89, -v89, v88\n"
"v_mfma_f32_16x16x32_bf16 v[40:43], v[148:151], v[76:79], v[40:43]\n"
"ds_read_b64 v[74:75], v3 offset:13568\n"
"s_cbranch_vccnz .Lsl_139c\n"
".Lbk_140c:\n"
"s_waitcnt lgkmcnt(7)\n"
"ds_read_b64 v[76:77], v3 offset:14336\n"
"v_mul_f32 v28, v88, v28\n"
"v_mul_f32 v29, v88, v29\n"
"v_mfma_f32_16x16x32_bf16 v[40:43], v[156:159], v[56:59], v[40:43]\n"
"ds_read_b64 v[78:79], v3 offset:14592\n"
"ds_read_b128 v[44:47], v11 offset:3136\n"
"ds_read_b128 v[48:51], v11 offset:3456\n"
"ds_read_b64 v[56:57], v3 offset:15360\n"
"v_mul_f32 v30, v88, v30\n"
"v_mul_f32 v31, v88, v31\n"
"v_mfma_f32_16x16x32_bf16 v[40:43], v[172:175], v[60:63], v[40:43]\n"
"ds_read_b64 v[58:59], v3 offset:15616\n"
"v_fmac_f32 v28, v89, v16\n"
"s_waitcnt lgkmcnt(8)\n"
"ds_read_b64 v[60:61], v4 offset:0\n"
"v_fmac_f32 v29, v89, v17\n"
"v_fmac_f32 v30, v89, v18\n"
"v_mfma_f32_16x16x32_bf16 v[40:43], v[180:183], v[64:67], v[40:43]\n"
"ds_read_b64 v[62:63], v4 offset:256\n"
"v_fmac_f32 v31, v89, v19\n"
"v_cvt_pk_bf16_f32 v28, v28, v29\n"
"ds_read_b64 v[64:65], v5 offset:0\n"
"v_cvt_pk_bf16_f32 v29, v30, v31\n"
"ds_write_b64 v7, v[28:29] offset:15872\n"
"v_mfma_f32_16x16x32_bf16 v[40:43], v[188:191], v[68:71], v[40:43]\n"
"ds_read_b64 v[66:67], v5 offset:256\n"
"v_mul_f32 v32, v88, v32\n"
"s_waitcnt lgkmcnt(9)\n"
"ds_read_b64 v[68:69], v6 offset:0\n"
"s_waitcnt lgkmcnt(9)\n"
"v_add_f32 v44, v44, v45\n"
"v_mul_f32 v33, v88, v33\n"
"v_mfma_f32_16x16x32_bf16 v[40:43], v[204:207], v[72:75], v[40:43]\n"
"ds_read_b64 v[70:71], v6 offset:256\n"
"v_add_f32 v46, v46, v47\n"
"v_mul_f32 v34, v88, v34\n"
"s_waitcnt lgkmcnt(9)\n"
"v_add_f32 v48, v48, v49\n"
"v_add_f32 v50, v50, v51\n"
"v_mul_f32 v35, v88, v35\n"
"v_mfma_f32_16x16x32_bf16 v[40:43], v[212:215], v[76:79], v[40:43]\n"
"v_add_f32 v48, v48, v50\n"
"v_fmac_f32 v32, v89, v20\n"
"s_waitcnt lgkmcnt(5)\n"
"v_add_f32 v44, v44, v46\n"
"v_mul_f32 v53, 0x3acccccd, v48\n"
"v_fmac_f32 v33, v89, v21\n"
"v_mfma_f32_16x16x32_bf16 v[40:43], v[220:223], v[56:59], v[40:43]\n"
"v_fma_f32 v44, -v48, v53, v44\n"
"v_fmamk_f32 v44, v44, 0x3acccccd, v27\n"
"v_fmac_f32 v34, v89, v22\n"
"v_cmp_u_f32 vcc, v44, v44\n"
"v_max_f32 v44, v44, v27\n"
"v_fmac_f32 v35, v89, v23\n"
"v_mfma_f32_16x16x32_bf16 v[40:43], v[236:239], v[60:63], v[40:43]\n"
"v_rsq_f32 v52, v44\n"
"s_waitcnt lgkmcnt(0)\n"
"v_mul_f32 v53, -v53, v52\n"
"v_cvt_pk_bf16_f32 v32, v32, v33\n"
"v_cvt_pk_bf16_f32 v33, v34, v35\n"
"v_mfma_f32_16x16x32_bf16 v[40:43], v[244:247], v[64:67], v[40:43]\n"
"s_cbranch_vccnz .Lsl_143c\n"
".Lbk_144c:\n"
"ds_write_b64 v8, v[32:33] offset:15872\n"
"v_mfma_f32_16x16x32_bf16 v[40:43], v[252:255], v[68:71], v[40:43]\n"
"v_cmp_u_f32 vcc, v92, v92\n"
"s_nop 6\n"
"v_mul_f32 v40, v52, v40\n"
"v_mul_f32 v41, v52, v41\n"
"v_mul_f32 v42, v52, v42\n"
"v_mul_f32 v43, v52, v43\n"
"v_fmac_f32 v40, v53, v20\n"
"v_fmac_f32 v41, v53, v21\n"
"v_fmac_f32 v42, v53, v22\n"
"v_fmac_f32 v43, v53, v23\n"
"v_cvt_pk_bf16_f32 v40, v40, v41\n"
"v_cvt_pk_bf16_f32 v41, v42, v43\n"
"ds_write_b64 v8, v[40:41] offset:0\n"
"s_cbranch_vccnz .Lrd_113\n"
".Lgo_112c:\n"
"ds_write_b32 v13, v13 offset:304\n"
"s_branch .Ljn_148\n"
".Lfu_147:\n"
"s_nop 0\n"
"v_mfma_f32_16x16x32_bf16 v[36:39], v[96:99], v[64:67], 0\n"
"ds_read_b64 v[60:61], v3 offset:1024\n"
"v_fmac_f32 v51, v89, v23\n"
"v_mfma_f32_16x16x32_bf16 v[40:43], v[100:103], v[64:67], 0\n"
"ds_read_b64 v[62:63], v3 offset:1280\n"
"v_cndmask_b32 v94, v28, v94, s[22:23]\n"
"v_mfma_f32_16x16x32_bf16 v[52:55], v[64:67], v[64:67], 0\n"
"ds_write_b32 v9, v94 offset:2816\n"
"v_mfma_f32_16x16x32_bf16 v[36:39], v[128:131], v[68:71], v[36:39]\n"
"ds_read_b64 v[64:65], v3 offset:2048\n"
"v_cvt_pk_bf16_f32 v48, v48, v49\n"
"v_mfma_f32_16x16x32_bf16 v[40:43], v[132:135], v[68:71], v[40:43]\n"
"ds_read_b64 v[66:67], v3 offset:2304\n"
"v_cvt_pk_bf16_f32 v49, v50, v51\n"
"v_mfma_f32_16x16x32_bf16 v[52:55], v[68:71], v[68:71], v[52:55]\n"
"ds_write_b64 v7, v[44:45] offset:11904\n"
"s_waitcnt lgkmcnt(8)\n"
"v_mfma_f32_16x16x32_bf16 v[36:39], v[160:163], v[72:75], v[36:39]\n"
"ds_read_b64 v[68:69], v3 offset:3072\n"
"v_mfma_f32_16x16x32_bf16 v[40:43], v[164:167], v[72:75], v[40:43]\n"
"ds_read_b64 v[70:71], v3 offset:3328\n"
"v_mfma_f32_16x16x32_bf16 v[52:55], v[72:75], v[72:75], v[52:55]\n"
"ds_write_b64 v8, v[48:49] offset:11904\n"
"v_mfma_f32_16x16x32_bf16 v[36:39], v[192:195], v[76:79], v[36:39]\n"
"ds_read_b64 v[72:73], v3 offset:5120\n"
"v_mfma_f32_16x16x32_bf16 v[40:43], v[196:199], v[76:79], v[40:43]\n"
"ds_read_b64 v[74:75], v3 offset:5376\n"
"v_mfma_f32_16x16x32_bf16 v[52:55], v[76:79], v[76:79], v[52:55]\n"
"ds_read_b128 v[84:87], v11 offset:2816\n"
"ds_read_b32 v91, v12 offset:2816\n"
"s_waitcnt lgkmcnt(11)\n"
"v_mfma_f32_16x16x32_bf16 v[36:39], v[224:227], v[56:59], v[36:39]\n"
"ds_read_b64 v[76:77], v3 offset:6144\n"
"v_mfma_f32_16x16x32_bf16 v[40:43], v[228:231], v[56:59], v[40:43]\n"
"ds_read_b64 v[78:79], v3 offset:6400\n"
"v_mfma_f32_16x16x32_bf16 v[52:55], v[56:59], v[56:59], v[52:55]\n"
"v_mfma_f32_16x16x32_bf16 v[36:39], v[104:107], v[60:63], v[36:39]\n"
"ds_read_b64 v[56:57], v3 offset:7168\n"
"v_mfma_f32_16x16x32_bf16 v[40:43], v[108:111], v[60:63], v[40:43]\n"
"ds_read_b64 v[58:59], v3 offset:7424\n"
"s_waitcnt lgkmcnt(9)\n"
"v_mfma_f32_16x16x32_bf16 v[36:39], v[112:115], v[64:67], v[36:39]\n"
"ds_read_b64 v[60:61], v3 offset:9216\n"
"ds_write_b32 v10, v52 offset:3456\n"
"v_cndmask_b32 v94, v52, v53, s[34:35]\n"
"v_cndmask_b32 v54, v54, v55, s[34:35]\n"
"v_mfma_f32_16x16x32_bf16 v[40:43], v[116:119], v[64:67], v[40:43]\n"
"ds_read_b64 v[62:63], v3 offset:9472\n"
"s_waitcnt lgkmcnt(8)\n"
"v_add_f32 v84, v84, v85\n"
"v_add_f32 v86, v86, v87\n"
"v_cndmask_b32 v94, v94, v54, s[20:21]\n"
"ds_write_b32 v9, v94 offset:3136\n"
"v_mfma_f32_16x16x32_bf16 v[36:39], v[120:123], v[68:71], v[36:39]\n"
"ds_read_b64 v[64:65], v3 offset:10240\n"
"v_add_f32 v84, v84, v86\n"
"s_waitcnt lgkmcnt(9)\n"
"v_mul_f32 v89, 0x3acccccd, v91\n"
"v_mfma_f32_16x16x32_bf16 v[40:43], v[124:127], v[68:71], v[40:43]\n"
"ds_read_b64 v[66:67], v3 offset:10496\n"
"v_fma_f32 v84, -v91, v89, v84\n"
"s_waitcnt lgkmcnt(8)\n"
"v_mfma_f32_16x16x32_bf16 v[36:39], v[136:139], v[72:75], v[36:39]\n"
"ds_read_b64 v[68:69], v3 offset:11264\n"
"v_fmamk_f32 v84, v84, 0x3acccccd, v27\n"
"v_cmp_u_f32 vcc, v84, v84\n"
"v_mfma_f32_16x16x32_bf16 v[40:43], v[140:143], v[72:75], v[40:43]\n"
"ds_read_b64 v[70:71], v3 offset:11520\n"
"v_max_f32 v84, v84, v27\n"
"v_rsq_f32 v88, v84\n"
"v_mfma_f32_16x16x32_bf16 v[36:39], v[144:147], v[76:79], v[36:39]\n"
"ds_read_b64 v[72:73], v3 offset:13312\n"
"v_mul_f32 v89, -v89, v88\n"
"v_mfma_f32_16x16x32_bf16 v[40:43], v[148:151], v[76:79], v[40:43]\n"
"ds_read_b64 v[74:75], v3 offset:13568\n"
"s_cbranch_vccnz .Lsl_139\n"
".Lbk_140:\n"
"s_waitcnt lgkmcnt(7)\n"
"v_mfma_f32_16x16x32_bf16 v[36:39], v[152:155], v[56:59], v[36:39]\n"
"ds_read_b64 v[76:77], v3 offset:14336\n"
"v_mul_f32 v28, v88, v28\n"
"v_mul_f32 v29, v88, v29\n"
"v_mfma_f32_16x16x32_bf16 v[40:43], v[156:159], v[56:59], v[40:43]\n"
"ds_read_b64 v[78:79], v3 offset:14592\n"
"ds_read_b128 v[44:47], v11 offset:3136\n"
"ds_read_b128 v[48:51], v11 offset:3456\n"
"v_mfma_f32_16x16x32_bf16 v[36:39], v[168:171], v[60:63], v[36:39]\n"
"ds_read_b64 v[56:57], v3 offset:15360\n"
"v_mul_f32 v30, v88, v30\n"
"v_mul_f32 v31, v88, v31\n"
"v_mfma_f32_16x16x32_bf16 v[40:43], v[172:175], v[60:63], v[40:43]\n"
"ds_read_b64 v[58:59], v3 offset:15616\n"
"v_fmac_f32 v28, v89, v16\n"
"s_waitcnt lgkmcnt(8)\n"
"v_mfma_f32_16x16x32_bf16 v[36:39], v[176:179], v[64:67], v[36:39]\n"
"ds_read_b64 v[60:61], v4 offset:0\n"
"v_fmac_f32 v29, v89, v17\n"
"v_fmac_f32 v30, v89, v18\n"
"v_mfma_f32_16x16x32_bf16 v[40:43], v[180:183], v[64:67], v[40:43]\n"
"ds_read_b64 v[62:63], v4 offset:256\n"
"v_fmac_f32 v31, v89, v19\n"
"v_cvt_pk_bf16_f32 v28, v28, v29\n"
"v_mfma_f32_16x16x32_bf16 v[36:39], v[184:187], v[68:71], v[36:39]\n"
"ds_read_b64 v[64:65], v5 offset:0\n"
"v_cvt_pk_bf16_f32 v29, v30, v31\n"
"ds_write_b64 v7, v[28:29] offset:15872\n"
"v_mfma_f32_16x16x32_bf16 v[40:43], v[188:191], v[68:71], v[40:43]\n"
"ds_read_b64 v[66:67], v5 offset:256\n"
"v_mul_f32 v32, v88, v32\n"
"s_waitcnt lgkmcnt(9)\n"
"v_mfma_f32_16x16x32_bf16 v[36:39], v[200:203], v[72:75], v[36:39]\n"
"ds_read_b64 v[68:69], v6 offset:0\n"
"s_waitcnt lgkmcnt(9)\n"
"v_add_f32 v44, v44, v45\n"
"v_mul_f32 v33, v88, v33\n"
"v_mfma_f32_16x16x32_bf16 v[40:43], v[204:207], v[72:75], v[40:43]\n"
"ds_read_b64 v[70:71], v6 offset:256\n"
"v_add_f32 v46, v46, v47\n"
"v_mul_f32 v34, v88, v34\n"
"v_mfma_f32_16x16x32_bf16 v[36:39], v[208:211], v[76:79], v[36:39]\n"
"s_waitcnt lgkmcnt(9)\n"
"v_add_f32 v48, v48, v49\n"
"v_add_f32 v50, v50, v51\n"
"v_mul_f32 v35, v88, v35\n"
"v_mfma_f32_16x16x32_bf16 v[40:43], v[212:215], v[76:79], v[40:43]\n"
"v_add_f32 v48, v48, v50\n"
"v_fmac_f32 v32, v89, v20\n"
"s_waitcnt lgkmcnt(5)\n"
"v_mfma_f32_16x16x32_bf16 v[36:39], v[216:219], v[56:59], v[36:39]\n"
"v_add_f32 v44, v44, v46\n"
"v_mul_f32 v53, 0x3acccccd, v48\n"
"v_fmac_f32 v33, v89, v21\n"
"v_mfma_f32_16x16x32_bf16 v[40:43], v[220:223], v[56:59], v[40:43]\n"
"v_fma_f32 v44, -v48, v53, v44\n"
"v_fmamk_f32 v44, v44, 0x3acccccd, v27\n"
"v_fmac_f32 v34, v89, v22\n"
"v_mfma_f32_16x16x32_bf16 v[36:39], v[232:235], v[60:63], v[36:39]\n"
"v_cmp_u_f32 vcc, v44, v44\n"
"v_max_f32 v44, v44, v27\n"
"v_fmac_f32 v35, v89, v23\n"
"v_mfma_f32_16x16x32_bf16 v[40:43], v[236:239], v[60:63], v[40:43]\n"
"v_rsq_f32 v52, v44\n"
"s_waitcnt lgkmcnt(0)\n"
"v_mfma_f32_16x16x32_bf16 v[36:39], v[240:243], v[64:67], v[36:39]\n"
"v_mul_f32 v53, -v53, v52\n"
"v_cvt_pk_bf16_f32 v32, v32, v33\n"
"v_cvt_pk_bf16_f32 v33, v34, v35\n"
"v_mfma_f32_16x16x32_bf16 v[40:43], v[244:247], v[64:67], v[40:43]\n"
"s_cbranch_vccnz .Lsl_143\n"
".Lbk_144:\n"
"ds_write_b64 v8, v[32:33] offset:15872\n"
"v_mfma_f32_16x16x32_bf16 v[36:39], v[248:251], v[68:71], v[36:39]\n"
"v_mfma_f32_16x16x32_bf16 v[40:43], v[252:255], v[68:71], v[40:43]\n"
"v_cmp_u_f32 vcc, v92, v92\n"
"s_nop 3\n"
"v_mul_f32 v36, v52, v36\n"
"v_mul_f32 v37, v52, v37\n"
"v_mul_f32 v38, v52, v38\n"
"v_mul_f32 v39, v52, v39\n"
"v_fmac_f32 v36, v53, v16\n"
"v_fmac_f32 v37, v53, v17\n"
"v_fmac_f32 v38, v53, v18\n"
"v_fmac_f32 v39, v53, v19\n"
"v_cvt_pk_bf16_f32 v36, v36, v37\n"
"v_cvt_pk_bf16_f32 v37, v38, v39\n"
"ds_write_b64 v7, v[36:37] offset:0\n"
"v_mul_f32 v40, v52, v40\n"
"v_mul_f32 v41, v52, v41\n"
"v_mul_f32 v42, v52, v42\n"
"v_mul_f32 v43, v52, v43\n"
"v_fmac_f32 v40, v53, v20\n"
"v_fmac_f32 v41, v53, v21\n"
"v_fmac_f32 v42, v53, v22\n"
"v_fmac_f32 v43, v53, v23\n"
"v_cvt_pk_bf16_f32 v40, v40, v41\n"
"v_cvt_pk_bf16_f32 v41, v42, v43\n"
"ds_write_b64 v8, v[40:41] offset:0\n"
"s_cbranch_vccnz .Lrd_113\n"
".Lgo_112:\n"
"ds_write_b32 v13, v13 offset:304\n"
".Ljn_148:\n"
"v_mov_b32 v46, 0\n"
"v_mov_b32 v47, 0\n"
"v_mov_b32 v50, 0\n"
"v_mov_b32 v51, 0\n"
"v_mov_b32 v72, s42\n"
"v_mov_b32 v73, s42\n"
"v_mov_b32 v74, s42\n"
"v_mov_b32 v75, s42\n"
"s_mov_b32 s17, 0x100000\n"
".Ltp_191:\n"
"ds_read_b32 v90, v14 offset:304\n"
"s_sub_u32 s17, s17, 1\n"
"s_cbranch_scc1 .Lou_192\n"
"s_waitcnt lgkmcnt(0)\n"
"v_cmp_eq_u32 vcc, 0, v90\n"
"s_cbranch_vccnz .Ltp_191\n"
".Lou_192:\n"
"s_waitcnt lgkmcnt(0)\n"
"s_mov_b64 exec, 0xffff\n"
"ds_read_b64 v[46:47], v24 offset:4000\n"
"ds_read_b64 v[50:51], v24 offset:4040\n"
"s_mov_b64 exec, -1\n"
"ds_read_b64 v[44:45], v25 offset:3968\n"
"ds_read_b64 v[56:57], v25 offset:80\n"
"ds_read_b64 v[58:59], v24 offset:112\n"
"ds_read_b64 v[64:65], v25 offset:4048\n"
"ds_read_b64 v[66:67], v24 offset:4080\n"
"ds_read_b64 v[48:49], v25 offset:4008\n"
"ds_read_b64 v[60:61], v25 offset:120\n"
"ds_read_b64 v[62:63], v24 offset:152\n"
"ds_read_b64 v[68:69], v25 offset:4088\n"
"ds_read_b64 v[70:71], v24 offset:4120\n"
"ds_read_b64_tr_b16 v[28:29], v26 offset:0\n"
"ds_read_b64_tr_b16 v[30:31], v26 offset:3968\n"
"ds_read_b64_tr_b16 v[32:33], v26 offset:40\n"
"s_waitcnt lgkmcnt(10)\n"
"v_mfma_f32_16x16x32_bf16 v[56:59], v[56:59], v[44:47], v[72:75]\n"
"s_waitcnt lgkmcnt(8)\n"
"v_mfma_f32_16x16x32_bf16 v[64:67], v[64:67], v[44:47], 0\n"
"ds_read_b64_tr_b16 v[34:35], v26 offset:4008\n"
"ds_read_b64_tr_b16 v[36:37], v26 offset:32\n"
"ds_read_b64_tr_b16 v[38:39], v26 offset:4000\n"
"s_waitcnt lgkmcnt(8)\n"
"v_mfma_f32_16x16x32_bf16 v[60:63], v[60:63], v[48:51], v[72:75]\n"
"s_waitcnt lgkmcnt(6)\n"
"v_mfma_f32_16x16x32_bf16 v[68:71], v[68:71], v[48:51], 0\n"
"ds_read_b64_tr_b16 v[40:41], v26 offset:72\n"
"ds_read_b64_tr_b16 v[42:43], v26 offset:4040\n"
"v_mov_b32 v84, 0x3f803f80\n"
"v_mov_b32 v85, 0x3f803f80\n"
"v_mov_b32 v86, 0x3f803f80\n"
"v_mov_b32 v87, 0x3f803f80\n"
"v_cndmask_b32 v76, v56, v57, s[34:35]\n"
"v_cndmask_b32 v72, v64, v65, s[34:35]\n"
"v_cndmask_b32 v76, v76, v72, s[36:37]\n"
"v_cndmask_b32 v77, v58, v59, s[34:35]\n"
"v_cndmask_b32 v72, v66, v67, s[34:35]\n"
"v_cndmask_b32 v77, v77, v72, s[38:39]\n"
"v_max_f32 v52, v76, v77\n"
"v_max_f32 v54, v76, v77\n"
"v_cndmask_b32 v78, v60, v61, s[34:35]\n"
"v_cndmask_b32 v72, v68, v69, s[34:35]\n"
"v_cndmask_b32 v78, v78, v72, s[36:37]\n"
"v_cndmask_b32 v79, v62, v63, s[34:35]\n"
"v_cndmask_b32 v72, v70, v71, s[34:35]\n"
"v_cndmask_b32 v79, v79, v72, s[38:39]\n"
"v_max_f32 v53, v78, v79\n"
"v_max_f32 v55, v78, v79\n"
"v_permlane16_swap_b32 v52, v54\n"
"s_nop 0\n"
"v_permlane16_swap_b32 v53, v55\n"
"v_max_f32 v72, v52, v54\n"
"v_max_f32 v52, v52, v54\n"
"v_max_f32 v73, v53, v55\n"
"v_max_f32 v53, v53, v55\n"
"v_permlane32_swap_b32 v52, v72\n"
"s_nop 0\n"
"v_permlane32_swap_b32 v53, v73\n"
"v_max_f32 v52, v52, v72\n"
"v_max_f32 v53, v53, v73\n"
"v_mul_f32 v52, 0xbea52b5b, v52\n"
"v_mul_f32 v53, 0xbea52b5b, v53\n"
"v_fmamk_f32 v76, v76, 0x3ea52b5b, v52\n"
"v_fmamk_f32 v77, v77, 0x3ea52b5b, v52\n"
"v_fmamk_f32 v78, v78, 0x3ea52b5b, v53\n"
"v_fmamk_f32 v79, v79, 0x3ea52b5b, v53\n"
"v_exp_f32 v76, v76\n"
"v_exp_f32 v77, v77\n"
"v_exp_f32 v78, v78\n"
"v_exp_f32 v79, v79\n"
"v_cvt_pk_bf16_f32 v52, v76, v77\n"
"v_cvt_pk_bf16_f32 v53, v78, v79\n"
"v_perm_b32 v44, v52, v52, v80\n"
"v_perm_b32 v45, v52, v52, v81\n"
"v_perm_b32 v46, v52, v52, v82\n"
"v_perm_b32 v47, v52, v52, v83\n"
"v_perm_b32 v48, v53, v53, v80\n"
"v_perm_b32 v49, v53, v53, v81\n"
"v_perm_b32 v50, v53, v53, v82\n"
"v_perm_b32 v51, v53, v53, v83\n"
"v_mfma_f32_16x16x32_bf16 v[56:59], v[84:87], v[44:47], 0\n"
"s_nop 0\n"
"v_mfma_f32_16x16x32_bf16 v[60:63], v[84:87], v[48:51], 0\n"
"s_waitcnt lgkmcnt(6)\n"
"v_mfma_f32_16x16x32_bf16 v[28:31], v[28:31], v[44:47], 0\n"
"s_waitcnt lgkmcnt(4)\n"
"v_mfma_f32_16x16x32_bf16 v[32:35], v[32:35], v[48:51], 0\n"
"s_waitcnt lgkmcnt(2)\n"
"v_mfma_f32_16x16x32_bf16 v[36:39], v[36:39], v[44:47], 0\n"
"s_waitcnt lgkmcnt(0)\n"
"v_mfma_f32_16x16x32_bf16 v[40:43], v[40:43], v[48:51], 0\n"
"v_cndmask_b32 v88, 2.0, v1, s[40:41]\n"
"v_rcp_f32 v52, v56\n"
"v_rcp_f32 v53, v60\n"
"ds_read_b32 v90, v14 offset:320\n"
"ds_read_b64 v[56:57], v3 offset:40960\n"
"ds_read_b64 v[58:59], v3 offset:41216\n"
"s_add_u32 s31, s30, 0xa60000\n"
"v_mul_f32 v28, v52, v28\n"
"v_mul_f32 v29, v52, v29\n"
"v_mul_f32 v30, v52, v30\n"
"v_mul_f32 v31, v52, v31\n"
"v_mul_f32 v32, v53, v32\n"
"ds_read_b64 v[60:61], v3 offset:41984\n"
"ds_read_b64 v[62:63], v3 offset:42240\n"
"v_mul_f32 v33, v53, v33\n"
"v_mul_f32 v34, v53, v34\n"
"v_mul_f32 v35, v53, v35\n"
"v_cvt_pk_bf16_f32 v28, v28, v29\n"
"v_cvt_pk_bf16_f32 v29, v30, v31\n"
"v_cvt_pk_bf16_f32 v30, v32, v33\n"
"ds_read_b64 v[64:65], v3 offset:43008\n"
"ds_read_b64 v[66:67], v3 offset:43264\n"
"v_cvt_pk_bf16_f32 v31, v34, v35\n"
"v_min_u32 v28, -2, v28\n"
"v_min_u32 v29, -2, v29\n"
"v_min_u32 v30, -2, v30\n"
"v_min_u32 v31, -2, v31\n"
"buffer_store_dwordx4 v[28:31], v1, s[4:7], s31 offen offset:0 sc1\n"
"ds_read_b64 v[68:69], v3 offset:44032\n"
"ds_read_b64 v[70:71], v3 offset:44288\n"
"v_mul_f32 v36, v52, v36\n"
"v_mul_f32 v37, v52, v37\n"
"v_mul_f32 v38, v52, v38\n"
"v_mul_f32 v39, v52, v39\n"
"v_mul_f32 v40, v53, v40\n"
"v_mul_f32 v41, v53, v41\n"
"ds_read_b64 v[72:73], v3 offset:45056\n"
"ds_read_b64 v[74:75], v3 offset:45312\n"
"v_mul_f32 v42, v53, v42\n"
"v_mul_f32 v43, v53, v43\n"
"v_cvt_pk_bf16_f32 v36, v36, v37\n"
"v_cvt_pk_bf16_f32 v37, v38, v39\n"
"v_cvt_pk_bf16_f32 v38, v40, v41\n"
"v_cvt_pk_bf16_f32 v39, v42, v43\n"
"v_min_u32 v36, -2, v36\n"
"v_min_u32 v37, -2, v37\n"
"v_min_u32 v38, -2, v38\n"
"v_min_u32 v39, -2, v39\n"
"buffer_store_dwordx4 v[36:39], v88, s[4:7], s31 offen offset:1024 sc1\n"
"ds_write_b32 v13, v13 offset:368\n"
"s_cmp_lg_u32 s54, 0\n"
"s_cbranch_scc1 .Lfx_196\n"
".Lbk_197:\n"
"s_mov_b32 s54, 0\n"
".Lag_193:\n"
"v_mov_b32 v92, 0\n"
"s_waitcnt lgkmcnt(11)\n"
"v_cmp_eq_u32 vcc, 0, v90\n"
"s_cbranch_vccnz .Lsl_230\n"
".Lbk_231:\n"
"s_waitcnt lgkmcnt(7)\n"
"v_mfma_f32_16x16x32_bf16 v[28:31], v[96:99], v[56:59], 0\n"
"ds_read_b64 v[76:77], v3 offset:46080\n"
"v_mfma_f32_16x16x32_bf16 v[32:35], v[100:103], v[56:59], 0\n"
"ds_read_b64 v[78:79], v3 offset:46336\n"
"v_mfma_f32_16x16x32_bf16 v[52:55], v[56:59], v[56:59], 0\n"
"v_mfma_f32_16x16x32_bf16 v[28:31], v[104:107], v[60:63], v[28:31]\n"
"ds_read_b64 v[56:57], v3 offset:47104\n"
"v_mfma_f32_16x16x32_bf16 v[32:35], v[108:111], v[60:63], v[32:35]\n"
"ds_read_b64 v[58:59], v3 offset:47360\n"
"s_waitcnt lgkmcnt(7)\n"
"v_mfma_f32_16x16x32_bf16 v[28:31], v[112:115], v[64:67], v[28:31]\n"
"ds_read_b64 v[60:61], v3 offset:48128\n"
"v_mfma_f32_16x16x32_bf16 v[32:35], v[116:119], v[64:67], v[32:35]\n"
"ds_read_b64 v[62:63], v3 offset:48384\n"
"v_mfma_f32_16x16x32_bf16 v[28:31], v[120:123], v[68:71], v[28:31]\n"
"ds_read_b64 v[64:65], v3 offset:49152\n"
"v_mfma_f32_16x16x32_bf16 v[32:35], v[124:127], v[68:71], v[32:35]\n"
"ds_read_b64 v[66:67], v3 offset:49408\n"
"s_waitcnt lgkmcnt(6)\n"
"v_mfma_f32_16x16x32_bf16 v[28:31], v[128:131], v[72:75], v[28:31]\n"
"ds_read_b64 v[68:69], v3 offset:50176\n"
"v_mfma_f32_16x16x32_bf16 v[32:35], v[132:135], v[72:75], v[32:35]\n"
"ds_read_b64 v[70:71], v3 offset:50432\n"
"v_mfma_f32_16x16x32_bf16 v[52:55], v[72:75], v[72:75], v[52:55]\n"
"v_mfma_f32_16x16x32_bf16 v[28:31], v[136:139], v[76:79], v[28:31]\n"
"ds_read_b64 v[72:73], v3 offset:51200\n"
"v_mfma_f32_16x16x32_bf16 v[32:35], v[140:143], v[76:79], v[32:35]\n"
"ds_read_b64 v[74:75], v3 offset:51456\n"
"s_waitcnt lgkmcnt(6)\n"
"v_mfma_f32_16x16x32_bf16 v[28:31], v[144:147], v[56:59], v[28:31]\n"
"ds_read_b64 v[76:77], v3 offset:52224\n"
"v_mfma_f32_16x16x32_bf16 v[32:35], v[148:151], v[56:59], v[32:35]\n"
"ds_read_b64 v[78:79], v3 offset:52480\n"
"v_mfma_f32_16x16x32_bf16 v[28:31], v[152:155], v[60:63], v[28:31]\n"
"ds_read_b64 v[56:57], v3 offset:53248\n"
"v_mfma_f32_16x16x32_bf16 v[32:35], v[156:159], v[60:63], v[32:35]\n"
"ds_read_b64 v[58:59], v3 offset:53504\n"
"s_waitcnt lgkmcnt(6)\n"
"v_mfma_f32_16x16x32_bf16 v[28:31], v[160:163], v[64:67], v[28:31]\n"
"ds_read_b64 v[60:61], v3 offset:54272\n"
"v_mfma_f32_16x16x32_bf16 v[32:35], v[164:167], v[64:67], v[32:35]\n"
"ds_read_b64 v[62:63], v3 offset:54528\n"
"v_mfma_f32_16x16x32_bf16 v[52:55], v[64:67], v[64:67], v[52:55]\n"
"v_mfma_f32_16x16x32_bf16 v[28:31], v[168:171], v[68:71], v[28:31]\n"
"ds_read_b64 v[64:65], v3 offset:55296\n"
"v_mfma_f32_16x16x32_bf16 v[32:35], v[172:175], v[68:71], v[32:35]\n"
"ds_read_b64 v[66:67], v3 offset:55552\n"
"s_waitcnt lgkmcnt(6)\n"
"v_mfma_f32_16x16x32_bf16 v[28:31], v[176:179], v[72:75], v[28:31]\n"
"ds_read_b64 v[68:69], v3 offset:56320\n"
"v_mfma_f32_16x16x32_bf16 v[32:35], v[180:183], v[72:75], v[32:35]\n"
"ds_read_b64 v[70:71], v3 offset:56576\n"
"v_mfma_f32_16x16x32_bf16 v[28:31], v[184:187], v[76:79], v[28:31]\n"
"ds_read_b64 v[72:73], v3 offset:57344\n"
"v_mfma_f32_16x16x32_bf16 v[32:35], v[188:191], v[76:79], v[32:35]\n"
"ds_read_b64 v[74:75], v3 offset:57600\n"
"s_waitcnt lgkmcnt(6)\n"
"v_mfma_f32_16x16x32_bf16 v[28:31], v[192:195], v[56:59], v[28:31]\n"
"ds_read_b64 v[76:77], v4 offset:40960\n"
"v_mfma_f32_16x16x32_bf16 v[32:35], v[196:199], v[56:59], v[32:35]\n"
"ds_read_b64 v[78:79], v4 offset:41216\n"
"v_mfma_f32_16x16x32_bf16 v[52:55], v[56:59], v[56:59], v[52:55]\n"
"v_mfma_f32_16x16x32_bf16 v[28:31], v[200:203], v[60:63], v[28:31]\n"
"ds_read_b64 v[56:57], v5 offset:40960\n"
"v_mfma_f32_16x16x32_bf16 v[32:35], v[204:207], v[60:63], v[32:35]\n"
"ds_read_b64 v[58:59], v5 offset:41216\n"
"s_waitcnt lgkmcnt(6)\n"
"v_mfma_f32_16x16x32_bf16 v[28:31], v[208:211], v[64:67], v[28:31]\n"
"ds_read_b64 v[60:61], v6 offset:40960\n"
"v_mfma_f32_16x16x32_bf16 v[32:35], v[212:215], v[64:67], v[32:35]\n"
"ds_read_b64 v[62:63], v6 offset:41216\n"
"v_mfma_f32_16x16x32_bf16 v[28:31], v[216:219], v[68:71], v[28:31]\n"
"v_add_u32 v3, 0x5000, v3\n"
"ds_read_b64 v[64:65], v3 offset:40960\n"
"v_mfma_f32_16x16x32_bf16 v[32:35], v[220:223], v[68:71], v[32:35]\n"
"ds_read_b64 v[66:67], v3 offset:41216\n"
"s_waitcnt lgkmcnt(6)\n"
"v_mfma_f32_16x16x32_bf16 v[28:31], v[224:227], v[72:75], v[28:31]\n"
"ds_read_b64 v[68:69], v3 offset:41984\n"
"v_mfma_f32_16x16x32_bf16 v[32:35], v[228:231], v[72:75], v[32:35]\n"
"ds_read_b64 v[70:71], v3 offset:42240\n"
"v_mfma_f32_16x16x32_bf16 v[52:55], v[72:75], v[72:75], v[52:55]\n"
"v_mfma_f32_16x16x32_bf16 v[28:31], v[232:235], v[76:79], v[28:31]\n"
"ds_read_b64 v[72:73], v3 offset:43008\n"
"v_mfma_f32_16x16x32_bf16 v[32:35], v[236:239], v[76:79], v[32:35]\n"
"ds_read_b64 v[74:75], v3 offset:43264\n"
"s_waitcnt lgkmcnt(6)\n"
"v_mfma_f32_16x16x32_bf16 v[28:31], v[240:243], v[56:59], v[28:31]\n"
"ds_read_b64 v[76:77], v3 offset:44032\n"
"v_cndmask_b32 v94, v52, v53, s[34:35]\n"
"v_mfma_f32_16x16x32_bf16 v[32:35], v[244:247], v[56:59], v[32:35]\n"
"ds_read_b64 v[78:79], v3 offset:44288\n"
"v_cndmask_b32 v54, v54, v55, s[34:35]\n"
"v_mfma_f32_16x16x32_bf16 v[28:31], v[248:251], v[60:63], v[28:31]\n"
"ds_read_b64 v[56:57], v3 offset:45056\n"
"v_cndmask_b32 v94, v94, v54, s[20:21]\n"
"v_mfma_f32_16x16x32_bf16 v[32:35], v[252:255], v[60:63], v[32:35]\n"
"ds_read_b64 v[58:59], v3 offset:45312\n"
"s_waitcnt lgkmcnt(6)\n"
"v_mfma_f32_16x16x32_bf16 v[36:39], v[96:99], v[64:67], 0\n"
"ds_read_b64 v[60:61], v3 offset:46080\n"
"v_mfma_f32_16x16x32_bf16 v[40:43], v[100:103], v[64:67], 0\n"
"ds_read_b64 v[62:63], v3 offset:46336\n"
"v_cndmask_b32 v94, v28, v94, s[22:23]\n"
"v_mfma_f32_16x16x32_bf16 v[52:55], v[64:67], v[64:67], 0\n"
"ds_write_b32 v9, v94 offset:3712\n"
"v_mfma_f32_16x16x32_bf16 v[36:39], v[104:107], v[68:71], v[36:39]\n"
"ds_read_b64 v[64:65], v3 offset:47104\n"
"v_mfma_f32_16x16x32_bf16 v[40:43], v[108:111], v[68:71], v[40:43]\n"
"ds_read_b64 v[66:67], v3 offset:47360\n"
"s_waitcnt lgkmcnt(7)\n"
"v_mfma_f32_16x16x32_bf16 v[36:39], v[112:115], v[72:75], v[36:39]\n"
"ds_read_b64 v[68:69], v3 offset:48128\n"
"v_mfma_f32_16x16x32_bf16 v[40:43], v[116:119], v[72:75], v[40:43]\n"
"ds_read_b64 v[70:71], v3 offset:48384\n"
"v_mfma_f32_16x16x32_bf16 v[36:39], v[120:123], v[76:79], v[36:39]\n"
"ds_read_b64 v[72:73], v3 offset:49152\n"
"v_mfma_f32_16x16x32_bf16 v[40:43], v[124:127], v[76:79], v[40:43]\n"
"ds_read_b64 v[74:75], v3 offset:49408\n"
"s_waitcnt lgkmcnt(7)\n"
"v_mfma_f32_16x16x32_bf16 v[36:39], v[128:131], v[56:59], v[36:39]\n"
"ds_read_b64 v[76:77], v3 offset:50176\n"
"v_mfma_f32_16x16x32_bf16 v[40:43], v[132:135], v[56:59], v[40:43]\n"
"ds_read_b64 v[78:79], v3 offset:50432\n"
"v_mfma_f32_16x16x32_bf16 v[52:55], v[56:59], v[56:59], v[52:55]\n"
"v_mfma_f32_16x16x32_bf16 v[36:39], v[136:139], v[60:63], v[36:39]\n"
"ds_read_b64 v[56:57], v3 offset:51200\n"
"v_mfma_f32_16x16x32_bf16 v[40:43], v[140:143], v[60:63], v[40:43]\n"
"ds_read_b64 v[58:59], v3 offset:51456\n"
"s_waitcnt lgkmcnt(6)\n"
"v_mfma_f32_16x16x32_bf16 v[36:39], v[144:147], v[64:67], v[36:39]\n"
"ds_read_b64 v[60:61], v3 offset:52224\n"
"ds_read_b128 v[84:87], v11 offset:3712\n"
"ds_read_b32 v91, v12 offset:3712\n"
"v_mfma_f32_16x16x32_bf16 v[40:43], v[148:151], v[64:67], v[40:43]\n"
"ds_read_b64 v[62:63], v3 offset:52480\n"
"v_mfma_f32_16x16x32_bf16 v[36:39], v[152:155], v[68:71], v[36:39]\n"
"ds_read_b64 v[64:65], v3 offset:53248\n"
"v_mfma_f32_16x16x32_bf16 v[40:43], v[156:159], v[68:71], v[40:43]\n"
"ds_read_b64 v[66:67], v3 offset:53504\n"
"s_waitcnt lgkmcnt(8)\n"
"v_mfma_f32_16x16x32_bf16 v[36:39], v[160:163], v[72:75], v[36:39]\n"
"ds_read_b64 v[68:69], v3 offset:54272\n"
"v_mfma_f32_16x16x32_bf16 v[40:43], v[164:167], v[72:75], v[40:43]\n"
"ds_read_b64 v[70:71], v3 offset:54528\n"
"v_mfma_f32_16x16x32_bf16 v[52:55], v[72:75], v[72:75], v[52:55]\n"
"v_mfma_f32_16x16x32_bf16 v[36:39], v[168:171], v[76:79], v[36:39]\n"
"ds_read_b64 v[72:73], v3 offset:55296\n"
"s_waitcnt lgkmcnt(7)\n"
"v_add_f32 v84, v84, v85\n"
"v_add_f32 v86, v86, v87\n"
"v_mfma_f32_16x16x32_bf16 v[40:43], v[172:175], v[76:79], v[40:43]\n"
"ds_read_b64 v[74:75], v3 offset:55552\n"
"v_add_f32 v84, v84, v86\n"
"s_waitcnt lgkmcnt(6)\n"
"v_mfma_f32_16x16x32_bf16 v[36:39], v[176:179], v[56:59], v[36:39]\n"
"ds_read_b64 v[76:77], v3 offset:56320\n"
"v_mul_f32 v89, 0x3acccccd, v91\n"
"v_fma_f32 v84, -v91, v89, v84\n"
"v_mfma_f32_16x16x32_bf16 v[40:43], v[180:183], v[56:59], v[40:43]\n"
"ds_read_b64 v[78:79], v3 offset:56576\n"
"v_fmamk_f32 v84, v84, 0x3acccccd, v27\n"
"v_cmp_u_f32 vcc, v84, v84\n"
"v_mfma_f32_16x16x32_bf16 v[36:39], v[184:187], v[60:63], v[36:39]\n"
"ds_read_b64 v[56:57], v3 offset:57344\n"
"v_max_f32 v84, v84, v27\n"
"v_mfma_f32_16x16x32_bf16 v[40:43], v[188:191], v[60:63], v[40:43]\n"
"ds_read_b64 v[58:59], v3 offset:57600\n"
"s_waitcnt lgkmcnt(6)\n"
"v_mfma_f32_16x16x32_bf16 v[36:39], v[192:195], v[64:67], v[36:39]\n"
"ds_read_b64 v[60:61], v4 offset:61440\n"
"v_rsq_f32 v88, v84\n"
"v_mfma_f32_16x16x32_bf16 v[40:43], v[196:199], v[64:67], v[40:43]\n"
"ds_read_b64 v[62:63], v4 offset:61696\n"
"ds_read_b32 v90, v14 offset:400\n"
"v_mfma_f32_16x16x32_bf16 v[52:55], v[64:67], v[64:67], v[52:55]\n"
"v_mul_f32 v89, -v89, v88\n"
"v_mfma_f32_16x16x32_bf16 v[36:39], v[200:203], v[68:71], v[36:39]\n"
"ds_read_b64 v[64:65], v5 offset:61440\n"
"s_cbranch_vccnz .Lsl_208\n"
".Lbk_209:\n"
"v_mfma_f32_16x16x32_bf16 v[40:43], v[204:207], v[68:71], v[40:43]\n"
"ds_read_b64 v[66:67], v5 offset:61696\n"
"s_add_u32 m0, s29, 0xa000\n"
"s_waitcnt lgkmcnt(7)\n"
"v_mfma_f32_16x16x32_bf16 v[36:39], v[208:211], v[72:75], v[36:39]\n"
"ds_read_b64 v[68:69], v6 offset:61440\n"
"s_waitcnt lgkmcnt(3)\n"
"v_cmp_eq_u32 vcc, 0, v90\n"
"v_mul_f32 v28, v88, v28\n"
"v_mfma_f32_16x16x32_bf16 v[40:43], v[212:215], v[72:75], v[40:43]\n"
"ds_read_b64 v[70:71], v6 offset:61696\n"
"s_add_u32 s31, s44, 0x13b000\n"
"v_mul_f32 v29, v88, v29\n"
"v_mfma_f32_16x16x32_bf16 v[36:39], v[216:219], v[76:79], v[36:39]\n"
"s_cbranch_vccnz .Lsl_200\n"
".Lbk_201:\n"
"v_add_u32 v3, 0xffffb000, v3\n"
"ds_read_b64 v[72:73], v3 offset:0\n"
"buffer_load_dwordx4 v1, s[4:7], s31 offen offset:0 sc1 lds\n"
"v_mfma_f32_16x16x32_bf16 v[40:43], v[220:223], v[76:79], v[40:43]\n"
"ds_read_b64 v[74:75], v3 offset:256\n"
"v_mfma_f32_16x16x32_bf16 v[36:39], v[224:227], v[56:59], v[36:39]\n"
"ds_read_b64 v[76:77], v3 offset:1024\n"
"v_mul_f32 v30, v88, v30\n"
"v_mfma_f32_16x16x32_bf16 v[40:43], v[228:231], v[56:59], v[40:43]\n"
"ds_read_b64 v[78:79], v3 offset:1280\n"
"v_mul_f32 v31, v88, v31\n"
"v_mfma_f32_16x16x32_bf16 v[52:55], v[56:59], v[56:59], v[52:55]\n"
"buffer_load_dwordx4 v1, s[4:7], s31 offen offset:1024 sc1 lds\n"
"v_fmac_f32 v28, v89, v16\n"
"v_mfma_f32_16x16x32_bf16 v[36:39], v[232:235], v[60:63], v[36:39]\n"
"ds_read_b64 v[56:57], v3 offset:2048\n"
"v_fmac_f32 v29, v89, v17\n"
"v_mfma_f32_16x16x32_bf16 v[40:43], v[236:239], v[60:63], v[40:43]\n"
"ds_read_b64 v[58:59], v3 offset:2304\n"
"s_waitcnt lgkmcnt(6)\n"
"v_mfma_f32_16x16x32_bf16 v[36:39], v[240:243], v[64:67], v[36:39]\n"
"ds_read_b64 v[60:61], v3 offset:3072\n"
"v_cndmask_b32 v94, v52, v53, s[34:35]\n"
"v_mfma_f32_16x16x32_bf16 v[40:43], v[244:247], v[64:67], v[40:43]\n"
"ds_read_b64 v[62:63], v3 offset:3328\n"
"v_cndmask_b32 v54, v54, v55, s[34:35]\n"
"buffer_load_dwordx4 v1, s[4:7], s31 offen offset:2048 sc1 lds\n"
"v_mfma_f32_16x16x32_bf16 v[36:39], v[248:251], v[68:71], v[36:39]\n"
"ds_read_b64 v[64:65], v3 offset:4096\n"
"v_cndmask_b32 v94, v94, v54, s[20:21]\n"
"v_mfma_f32_16x16x32_bf16 v[40:43], v[252:255], v[68:71], v[40:43]\n"
"ds_read_b64 v[66:67], v3 offset:4352\n"
"s_waitcnt lgkmcnt(6)\n"
"v_mfma_f32_16x16x32_bf16 v[44:47], v[96:99], v[72:75], 0\n"
"ds_read_b64 v[68:69], v3 offset:5120\n"
"v_fmac_f32 v30, v89, v18\n"
"v_mfma_f32_16x16x32_bf16 v[48:51], v[100:103], v[72:75], 0\n"
"ds_read_b64 v[70:71], v3 offset:5376\n"
"v_cndmask_b32 v94, v36, v94, s[22:23]\n"
"buffer_load_dwordx4 v1, s[4:7], s31 offen offset:3072 sc1 lds\n"
"v_mfma_f32_16x16x32_bf16 v[52:55], v[72:75], v[72:75], 0\n"
"ds_write_b32 v9, v94 offset:4032\n"
"s_add_u32 m0, m0, 1024\n"
"v_mfma_f32_16x16x32_bf16 v[44:47], v[104:107], v[76:79], v[44:47]\n"
"ds_read_b64 v[72:73], v3 offset:6144\n"
"s_add_u32 s31, s31, 1024\n"
"v_mfma_f32_16x16x32_bf16 v[48:51], v[108:111], v[76:79], v[48:51]\n"
"ds_read_b64 v[74:75], v3 offset:6400\n"
"s_waitcnt lgkmcnt(7)\n"
"v_mfma_f32_16x16x32_bf16 v[44:47], v[112:115], v[56:59], v[44:47]\n"
"ds_read_b64 v[76:77], v3 offset:7168\n"
"buffer_load_dwordx4 v1, s[4:7], s31 offen offset:3072 sc1 lds\n"
"v_mfma_f32_16x16x32_bf16 v[48:51], v[116:119], v[56:59], v[48:51]\n"
"ds_read_b64 v[78:79], v3 offset:7424\n"
"v_fmac_f32 v31, v89, v19\n"
"v_mfma_f32_16x16x32_bf16 v[44:47], v[120:123], v[60:63], v[44:47]\n"
"ds_read_b64 v[56:57], v3 offset:8192\n"
"v_cvt_pk_bf16_f32 v28, v28, v29\n"
"v_mfma_f32_16x16x32_bf16 v[48:51], v[124:127], v[60:63], v[48:51]\n"
"ds_read_b64 v[58:59], v3 offset:8448\n"
"s_waitcnt lgkmcnt(7)\n"
"v_mfma_f32_16x16x32_bf16 v[44:47], v[128:131], v[64:67], v[44:47]\n"
"ds_read_b64 v[60:61], v3 offset:9216\n"
"v_cvt_pk_bf16_f32 v29, v30, v31\n"
"v_mfma_f32_16x16x32_bf16 v[48:51], v[132:135], v[64:67], v[48:51]\n"
"ds_read_b64 v[62:63], v3 offset:9472\n"
"v_mul_f32 v32, v88, v32\n"
"v_mfma_f32_16x16x32_bf16 v[52:55], v[64:67], v[64:67], v[52:55]\n"
"v_mul_f32 v33, v88, v33\n"
"v_mul_f32 v34, v88, v34\n"
"v_mfma_f32_16x16x32_bf16 v[44:47], v[136:139], v[68:71], v[44:47]\n"
"ds_read_b64 v[64:65], v3 offset:10240\n"
"v_mul_f32 v35, v88, v35\n"
"v_mfma_f32_16x16x32_bf16 v[48:51], v[140:143], v[68:71], v[48:51]\n"
"ds_read_b64 v[66:67], v3 offset:10496\n"
"s_waitcnt lgkmcnt(6)\n"
"v_mfma_f32_16x16x32_bf16 v[44:47], v[144:147], v[72:75], v[44:47]\n"
"ds_read_b64 v[68:69], v3 offset:11264\n"
"ds_read_b128 v[84:87], v11 offset:4032\n"
"ds_read_b32 v91, v12 offset:4032\n"
"v_mfma_f32_16x16x32_bf16 v[48:51], v[148:151], v[72:75], v[48:51]\n"
"ds_read_b64 v[70:71], v3 offset:11520\n"
"v_fmac_f32 v32, v89, v20\n"
"v_mfma_f32_16x16x32_bf16 v[44:47], v[152:155], v[76:79], v[44:47]\n"
"ds_read_b64 v[72:73], v3 offset:12288\n"
"v_fmac_f32 v33, v89, v21\n"
"v_mfma_f32_16x16x32_bf16 v[48:51], v[156:159], v[76:79], v[48:51]\n"
"ds_read_b64 v[74:75], v3 offset:12544\n"
"s_waitcnt lgkmcnt(8)\n"
"v_mfma_f32_16x16x32_bf16 v[44:47], v[160:163], v[56:59], v[44:47]\n"
"ds_read_b64 v[76:77], v3 offset:13312\n"
"v_fmac_f32 v34, v89, v22\n"
"v_mfma_f32_16x16x32_bf16 v[48:51], v[164:167], v[56:59], v[48:51]\n"
"ds_read_b64 v[78:79], v3 offset:13568\n"
"v_fmac_f32 v35, v89, v23\n"
"v_mfma_f32_16x16x32_bf16 v[52:55], v[56:59], v[56:59], v[52:55]\n"
"v_cvt_pk_bf16_f32 v32, v32, v33\n"
"v_cvt_pk_bf16_f32 v33, v34, v35\n"
"v_mfma_f32_16x16x32_bf16 v[44:47], v[168:171], v[60:63], v[44:47]\n"
"ds_read_b64 v[56:57], v3 offset:14336\n"
"s_waitcnt lgkmcnt(7)\n"
"v_add_f32 v84, v84, v85\n"
"v_add_f32 v86, v86, v87\n"
"v_mfma_f32_16x16x32_bf16 v[48:51], v[172:175], v[60:63], v[48:51]\n"
"ds_read_b64 v[58:59], v3 offset:14592\n"
"v_add_f32 v84, v84, v86\n"
"s_waitcnt lgkmcnt(6)\n"
"v_mfma_f32_16x16x32_bf16 v[44:47], v[176:179], v[64:67], v[44:47]\n"
"ds_read_b64 v[60:61], v3 offset:15360\n"
"v_mul_f32 v89, 0x3acccccd, v91\n"
"v_fma_f32 v84, -v91, v89, v84\n"
"v_mfma_f32_16x16x32_bf16 v[48:51], v[180:183], v[64:67], v[48:51]\n"
"ds_read_b64 v[62:63], v3 offset:15616\n"
"v_fmamk_f32 v84, v84, 0x3acccccd, v27\n"
"v_cmp_u_f32 vcc, v84, v84\n"
"v_mfma_f32_16x16x32_bf16 v[44:47], v[184:187], v[68:71], v[44:47]\n"
"ds_read_b64 v[64:65], v3 offset:16384\n"
"v_max_f32 v84, v84, v27\n"
"v_mfma_f32_16x16x32_bf16 v[48:51], v[188:191], v[68:71], v[48:51]\n"
"ds_read_b64 v[66:67], v3 offset:16640\n"
"s_waitcnt lgkmcnt(6)\n"
"v_mfma_f32_16x16x32_bf16 v[44:47], v[192:195], v[72:75], v[44:47]\n"
"ds_read_b64 v[68:69], v4 offset:0\n"
"v_rsq_f32 v88, v84\n"
"v_mfma_f32_16x16x32_bf16 v[48:51], v[196:199], v[72:75], v[48:51]\n"
"ds_read_b64 v[70:71], v4 offset:256\n"
"ds_read_b32 v90, v14 offset:416\n"
"v_mfma_f32_16x16x32_bf16 v[52:55], v[72:75], v[72:75], v[52:55]\n"
"v_mul_f32 v89, -v89, v88\n"
"v_mfma_f32_16x16x32_bf16 v[44:47], v[200:203], v[76:79], v[44:47]\n"
"ds_read_b64 v[72:73], v5 offset:0\n"
"s_cbranch_vccnz .Lsl_212\n"
".Lbk_213:\n"
"v_mfma_f32_16x16x32_bf16 v[48:51], v[204:207], v[76:79], v[48:51]\n"
"ds_read_b64 v[74:75], v5 offset:256\n"
"ds_write_b64 v7, v[28:29] offset:44928\n"
"s_waitcnt lgkmcnt(8)\n"
"v_mfma_f32_16x16x32_bf16 v[44:47], v[208:211], v[56:59], v[44:47]\n"
"ds_read_b64 v[76:77], v6 offset:0\n"
"s_waitcnt lgkmcnt(4)\n"
"v_cmp_eq_u32 vcc, 0, v90\n"
"v_mul_f32 v36, v88, v36\n"
"v_mfma_f32_16x16x32_bf16 v[48:51], v[212:215], v[56:59], v[48:51]\n"
"ds_read_b64 v[78:79], v6 offset:256\n"
"v_mul_f32 v37, v88, v37\n"
"v_mfma_f32_16x16x32_bf16 v[44:47], v[216:219], v[60:63], v[44:47]\n"
"s_cbranch_vccnz .Lsl_204\n"
".Lbk_205:\n"
"ds_read_b64 v[56:57], v3 offset:20480\n"
"v_mfma_f32_16x16x32_bf16 v[48:51], v[220:223], v[60:63], v[48:51]\n"
"ds_read_b64 v[58:59], v3 offset:20736\n"
"v_mfma_f32_16x16x32_bf16 v[44:47], v[224:227], v[64:67], v[44:47]\n"
"ds_read_b64 v[60:61], v3 offset:21504\n"
"v_mul_f32 v38, v88, v38\n"
"v_mfma_f32_16x16x32_bf16 v[48:51], v[228:231], v[64:67], v[48:51]\n"
"ds_read_b64 v[62:63], v3 offset:21760\n"
"v_mov_b32 v84, 0x3f803f80\n"
"v_mfma_f32_16x16x32_bf16 v[52:55], v[64:67], v[64:67], v[52:55]\n"
"v_mov_b32 v85, 0x3f803f80\n"
"ds_write_b64 v8, v[32:33] offset:44928\n"
"v_mfma_f32_16x16x32_bf16 v[44:47], v[232:235], v[68:71], v[44:47]\n"
"ds_read_b64 v[64:65], v3 offset:22528\n"
"v_mul_f32 v39, v88, v39\n"
"v_mfma_f32_16x16x32_bf16 v[48:51], v[236:239], v[68:71], v[48:51]\n"
"ds_read_b64 v[66:67], v3 offset:22784\n"
"s_waitcnt lgkmcnt(7)\n"
"v_mfma_f32_16x16x32_bf16 v[44:47], v[240:243], v[72:75], v[44:47]\n"
"ds_read_b64 v[68:69], v3 offset:23552\n"
"v_cndmask_b32 v94, v52, v53, s[34:35]\n"
"v_mfma_f32_16x16x32_bf16 v[48:51], v[244:247], v[72:75], v[48:51]\n"
"ds_read_b64 v[70:71], v3 offset:23808\n"
"v_cndmask_b32 v54, v54, v55, s[34:35]\n"
"v_mfma_f32_16x16x32_bf16 v[44:47], v[248:251], v[76:79], v[44:47]\n"
"ds_read_b64 v[72:73], v3 offset:24576\n"
"v_cndmask_b32 v94, v94, v54, s[20:21]\n"
"v_mfma_f32_16x16x32_bf16 v[48:51], v[252:255], v[76:79], v[48:51]\n"
"ds_read_b64 v[74:75], v3 offset:24832\n"
"s_waitcnt lgkmcnt(7)\n"
"v_mfma_f32_16x16x32_bf16 v[28:31], v[96:99], v[56:59], 0\n"
"ds_read_b64 v[76:77], v3 offset:25600\n"
"v_fmac_f32 v36, v89, v16\n"
"v_mfma_f32_16x16x32_bf16 v[32:35], v[100:103], v[56:59], 0\n"
"ds_read_b64 v[78:79], v3 offset:25856\n"
"v_cndmask_b32 v94, v44, v94, s[22:23]\n"
"s_waitcnt vmcnt(0)\n"
"ds_write_b64 v93, v[84:85] offset:40960\n"
"v_mfma_f32_16x16x32_bf16 v[52:55], v[56:59], v[56:59], 0\n"
"ds_write_b32 v9, v94 offset:4352\n"
"v_mfma_f32_16x16x32_bf16 v[28:31], v[104:107], v[60:63], v[28:31]\n"
"ds_read_b64 v[56:57], v3 offset:26624\n"
"v_fmac_f32 v37, v89, v17\n"
"v_mfma_f32_16x16x32_bf16 v[32:35], v[108:111], v[60:63], v[32:35]\n"
"ds_read_b64 v[58:59], v3 offset:26880\n"
"s_waitcnt lgkmcnt(8)\n"
"v_mfma_f32_16x16x32_bf16 v[28:31], v[112:115], v[64:67], v[28:31]\n"
"ds_read_b64 v[60:61], v3 offset:27648\n"
"v_fmac_f32 v38, v89, v18\n"
"v_mfma_f32_16x16x32_bf16 v[32:35], v[116:119], v[64:67], v[32:35]\n"
"ds_read_b64 v[62:63], v3 offset:27904\n"
"v_fmac_f32 v39, v89, v19\n"
"v_mfma_f32_16x16x32_bf16 v[28:31], v[120:123], v[68:71], v[28:31]\n"
"ds_read_b64 v[64:65], v3 offset:28672\n"
"v_cvt_pk_bf16_f32 v36, v36, v37\n"
"v_mfma_f32_16x16x32_bf16 v[32:35], v[124:127], v[68:71], v[32:35]\n"
"ds_read_b64 v[66:67], v3 offset:28928\n"
"s_waitcnt lgkmcnt(8)\n"
"v_mfma_f32_16x16x32_bf16 v[28:31], v[128:131], v[72:75], v[28:31]\n"
"ds_read_b64 v[68:69], v3 offset:29696\n"
"v_cvt_pk_bf16_f32 v37, v38, v39\n"
"v_mfma_f32_16x16x32_bf16 v[32:35], v[132:135], v[72:75], v[32:35]\n"
"ds_read_b64 v[70:71], v3 offset:29952\n"
"v_mul_f32 v40, v88, v40\n"
"v_mfma_f32_16x16x32_bf16 v[52:55], v[72:75], v[72:75], v[52:55]\n"
"v_mul_f32 v41, v88, v41\n"
"v_mul_f32 v42, v88, v42\n"
"v_mfma_f32_16x16x32_bf16 v[28:31], v[136:139], v[76:79], v[28:31]\n"
"ds_read_b64 v[72:73], v3 offset:30720\n"
"v_mul_f32 v43, v88, v43\n"
"v_mfma_f32_16x16x32_bf16 v[32:35], v[140:143], v[76:79], v[32:35]\n"
"ds_read_b64 v[74:75], v3 offset:30976\n"
"s_waitcnt lgkmcnt(6)\n"
"v_mfma_f32_16x16x32_bf16 v[28:31], v[144:147], v[56:59], v[28:31]\n"
"ds_read_b64 v[76:77], v3 offset:31744\n"
"ds_read_b128 v[84:87], v11 offset:4352\n"
"ds_read_b32 v91, v12 offset:4352\n"
"v_mfma_f32_16x16x32_bf16 v[32:35], v[148:151], v[56:59], v[32:35]\n"
"ds_read_b64 v[78:79], v3 offset:32000\n"
"v_fmac_f32 v40, v89, v20\n"
"v_mfma_f32_16x16x32_bf16 v[28:31], v[152:155], v[60:63], v[28:31]\n"
"ds_read_b64 v[56:57], v3 offset:32768\n"
"v_fmac_f32 v41, v89, v21\n"
"v_mfma_f32_16x16x32_bf16 v[32:35], v[156:159], v[60:63], v[32:35]\n"
"ds_read_b64 v[58:59], v3 offset:33024\n"
"s_waitcnt lgkmcnt(8)\n"
"v_mfma_f32_16x16x32_bf16 v[28:31], v[160:163], v[64:67], v[28:31]\n"
"ds_read_b64 v[60:61], v3 offset:33792\n"
"v_fmac_f32 v42, v89, v22\n"
"v_mfma_f32_16x16x32_bf16 v[32:35], v[164:167], v[64:67], v[32:35]\n"
"ds_read_b64 v[62:63], v3 offset:34048\n"
"v_fmac_f32 v43, v89, v23\n"
"v_mfma_f32_16x16x32_bf16 v[52:55], v[64:67], v[64:67], v[52:55]\n"
"v_cvt_pk_bf16_f32 v40, v40, v41\n"
"v_cvt_pk_bf16_f32 v41, v42, v43\n"
"v_mfma_f32_16x16x32_bf16 v[28:31], v[168:171], v[68:71], v[28:31]\n"
"ds_read_b64 v[64:65], v3 offset:34816\n"
"s_waitcnt lgkmcnt(7)\n"
"v_add_f32 v84, v84, v85\n"
"v_add_f32 v86, v86, v87\n"
"v_mfma_f32_16x16x32_bf16 v[32:35], v[172:175], v[68:71], v[32:35]\n"
"ds_read_b64 v[66:67], v3 offset:35072\n"
"v_add_f32 v84, v84, v86\n"
"s_waitcnt lgkmcnt(6)\n"
"v_mfma_f32_16x16x32_bf16 v[28:31], v[176:179], v[72:75], v[28:31]\n"
"ds_read_b64 v[68:69], v3 offset:35840\n"
"v_mul_f32 v89, 0x3acccccd, v91\n"
"v_fma_f32 v84, -v91, v89, v84\n"
"v_mfma_f32_16x16x32_bf16 v[32:35], v[180:183], v[72:75], v[32:35]\n"
"ds_read_b64 v[70:71], v3 offset:36096\n"
"v_fmamk_f32 v84, v84, 0x3acccccd, v27\n"
"v_mfma_f32_16x16x32_bf16 v[28:31], v[184:187], v[76:79], v[28:31]\n"
"ds_read_b64 v[72:73], v3 offset:36864\n"
"v_cmp_u_f32 vcc, v84, v84\n"
"v_mfma_f32_16x16x32_bf16 v[32:35], v[188:191], v[76:79], v[32:35]\n"
"ds_read_b64 v[74:75], v3 offset:37120\n"
"s_waitcnt lgkmcnt(6)\n"
"v_mfma_f32_16x16x32_bf16 v[28:31], v[192:195], v[56:59], v[28:31]\n"
"ds_read_b64 v[76:77], v4 offset:20480\n"
"v_max_f32 v84, v84, v27\n"
"v_mfma_f32_16x16x32_bf16 v[32:35], v[196:199], v[56:59], v[32:35]\n"
"ds_read_b64 v[78:79], v4 offset:20736\n"
"v_rsq_f32 v88, v84\n"
"v_mfma_f32_16x16x32_bf16 v[52:55], v[56:59], v[56:59], v[52:55]\n"
"v_mul_f32 v89, -v89, v88\n"
"v_mfma_f32_16x16x32_bf16 v[28:31], v[200:203], v[60:63], v[28:31]\n"
"ds_read_b64 v[56:57], v5 offset:20480\n"
"s_cbranch_vccnz .Lsl_216\n"
".Lbk_217:\n"
"v_mfma_f32_16x16x32_bf16 v[32:35], v[204:207], v[60:63], v[32:35]\n"
"ds_read_b64 v[58:59], v5 offset:20736\n"
"ds_write_b64 v7, v[36:37] offset:48896\n"
"s_waitcnt lgkmcnt(7)\n"
"v_mfma_f32_16x16x32_bf16 v[28:31], v[208:211], v[64:67], v[28:31]\n"
"ds_read_b64 v[60:61], v6 offset:20480\n"
"v_mul_f32 v44, v88, v44\n"
"v_mul_f32 v45, v88, v45\n"
"v_mfma_f32_16x16x32_bf16 v[32:35], v[212:215], v[64:67], v[32:35]\n"
"ds_read_b64 v[62:63], v6 offset:20736\n"
"v_mul_f32 v46, v88, v46\n"
"v_mul_f32 v47, v88, v47\n"
"v_mfma_f32_16x16x32_bf16 v[28:31], v[216:219], v[68:71], v[28:31]\n"
"ds_read_b64 v[64:65], v3 offset:40960\n"
"v_fmac_f32 v44, v89, v16\n"
"v_fmac_f32 v45, v89, v17\n"
"v_mfma_f32_16x16x32_bf16 v[32:35], v[220:223], v[68:71], v[32:35]\n"
"ds_read_b64 v[66:67], v3 offset:41216\n"
"v_fmac_f32 v46, v89, v18\n"
"s_waitcnt lgkmcnt(7)\n"
"v_mfma_f32_16x16x32_bf16 v[28:31], v[224:227], v[72:75], v[28:31]\n"
"ds_read_b64 v[68:69], v3 offset:45056\n"
"v_fmac_f32 v47, v89, v19\n"
"v_cvt_pk_bf16_f32 v44, v44, v45\n"
"v_mfma_f32_16x16x32_bf16 v[32:35], v[228:231], v[72:75], v[32:35]\n"
"ds_read_b64 v[70:71], v3 offset:45312\n"
"v_cvt_pk_bf16_f32 v45, v46, v47\n"
"v_mul_f32 v48, v88, v48\n"
"v_mfma_f32_16x16x32_bf16 v[52:55], v[72:75], v[72:75], v[52:55]\n"
"ds_write_b64 v8, v[40:41] offset:48896\n"
"v_mfma_f32_16x16x32_bf16 v[28:31], v[232:235], v[76:79], v[28:31]\n"
"ds_read_b64 v[72:73], v3 offset:49152\n"
"v_mul_f32 v49, v88, v49\n"
"v_mul_f32 v50, v88, v50\n"
"v_mfma_f32_16x16x32_bf16 v[32:35], v[236:239], v[76:79], v[32:35]\n"
"ds_read_b64 v[74:75], v3 offset:49408\n"
"v_mul_f32 v51, v88, v51\n"
"s_waitcnt lgkmcnt(7)\n"
"v_mfma_f32_16x16x32_bf16 v[28:31], v[240:243], v[56:59], v[28:31]\n"
"ds_read_b64 v[76:77], v3 offset:53248\n"
"v_cndmask_b32 v94, v52, v53, s[34:35]\n"
"v_fmac_f32 v48, v89, v20\n"
"v_mfma_f32_16x16x32_bf16 v[32:35], v[244:247], v[56:59], v[32:35]\n"
"ds_read_b64 v[78:79], v3 offset:53504\n"
"v_cndmask_b32 v54, v54, v55, s[34:35]\n"
"v_fmac_f32 v49, v89, v21\n"
"v_mfma_f32_16x16x32_bf16 v[28:31], v[248:251], v[60:63], v[28:31]\n"
"ds_read_b64 v[56:57], v3 offset:57344\n"
"v_cndmask_b32 v94, v94, v54, s[20:21]\n"
"v_fmac_f32 v50, v89, v22\n"
"v_mfma_f32_16x16x32_bf16 v[32:35], v[252:255], v[60:63], v[32:35]\n"
"ds_read_b64 v[58:59], v3 offset:57600\n"
"s_waitcnt lgkmcnt(7)\n"
"s_cmp_eq_u32 s3, 3\n"
"s_cbranch_scc1 .Lfu_228\n"
"ds_read_b64 v[60:61], v3 offset:41984\n"
"v_fmac_f32 v51, v89, v23\n"
"v_mfma_f32_16x16x32_bf16 v[40:43], v[100:103], v[64:67], 0\n"
"ds_read_b64 v[62:63], v3 offset:42240\n"
"v_cndmask_b32 v94, v28, v94, s[22:23]\n"
"v_mfma_f32_16x16x32_bf16 v[52:55], v[64:67], v[64:67], 0\n"
"ds_write_b32 v9, v94 offset:4672\n"
"ds_read_b64 v[64:65], v3 offset:43008\n"
"v_cvt_pk_bf16_f32 v48, v48, v49\n"
"v_mfma_f32_16x16x32_bf16 v[40:43], v[132:135], v[68:71], v[40:43]\n"
"ds_read_b64 v[66:67], v3 offset:43264\n"
"v_cvt_pk_bf16_f32 v49, v50, v51\n"
"v_mfma_f32_16x16x32_bf16 v[52:55], v[68:71], v[68:71], v[52:55]\n"
"ds_write_b64 v7, v[44:45] offset:52864\n"
"s_waitcnt lgkmcnt(8)\n"
"ds_read_b64 v[68:69], v3 offset:44032\n"
"v_mfma_f32_16x16x32_bf16 v[40:43], v[164:167], v[72:75], v[40:43]\n"
"ds_read_b64 v[70:71], v3 offset:44288\n"
"v_mfma_f32_16x16x32_bf16 v[52:55], v[72:75], v[72:75], v[52:55]\n"
"ds_write_b64 v8, v[48:49] offset:52864\n"
"ds_read_b64 v[72:73], v3 offset:46080\n"
"v_mfma_f32_16x16x32_bf16 v[40:43], v[196:199], v[76:79], v[40:43]\n"
"ds_read_b64 v[74:75], v3 offset:46336\n"
"v_mfma_f32_16x16x32_bf16 v[52:55], v[76:79], v[76:79], v[52:55]\n"
"ds_read_b128 v[84:87], v11 offset:4672\n"
"ds_read_b32 v91, v12 offset:4672\n"
"s_waitcnt lgkmcnt(11)\n"
"ds_read_b64 v[76:77], v3 offset:47104\n"
"v_mfma_f32_16x16x32_bf16 v[40:43], v[228:231], v[56:59], v[40:43]\n"
"ds_read_b64 v[78:79], v3 offset:47360\n"
"v_mfma_f32_16x16x32_bf16 v[52:55], v[56:59], v[56:59], v[52:55]\n"
"ds_read_b64 v[56:57], v3 offset:48128\n"
"v_mfma_f32_16x16x32_bf16 v[40:43], v[108:111], v[60:63], v[40:43]\n"
"ds_read_b64 v[58:59], v3 offset:48384\n"
"s_waitcnt lgkmcnt(9)\n"
"ds_read_b64 v[60:61], v3 offset:50176\n"
"s_nop 1\n"
"ds_write_b32 v10, v52 offset:5312\n"
"v_cndmask_b32 v94, v52, v53, s[34:35]\n"
"v_cndmask_b32 v54, v54, v55, s[34:35]\n"
"v_mfma_f32_16x16x32_bf16 v[40:43], v[116:119], v[64:67], v[40:43]\n"
"ds_read_b64 v[62:63], v3 offset:50432\n"
"s_waitcnt lgkmcnt(8)\n"
"v_add_f32 v84, v84, v85\n"
"v_add_f32 v86, v86, v87\n"
"v_cndmask_b32 v94, v94, v54, s[20:21]\n"
"ds_write_b32 v9, v94 offset:4992\n"
"ds_read_b64 v[64:65], v3 offset:51200\n"
"v_add_f32 v84, v84, v86\n"
"s_waitcnt lgkmcnt(9)\n"
"v_mul_f32 v89, 0x3acccccd, v91\n"
"v_mfma_f32_16x16x32_bf16 v[40:43], v[124:127], v[68:71], v[40:43]\n"
"ds_read_b64 v[66:67], v3 offset:51456\n"
"v_fma_f32 v84, -v91, v89, v84\n"
"s_waitcnt lgkmcnt(8)\n"
"ds_read_b64 v[68:69], v3 offset:52224\n"
"v_fmamk_f32 v84, v84, 0x3acccccd, v27\n"
"v_cmp_u_f32 vcc, v84, v84\n"
"v_mfma_f32_16x16x32_bf16 v[40:43], v[140:143], v[72:75], v[40:43]\n"
"ds_read_b64 v[70:71], v3 offset:52480\n"
"v_max_f32 v84, v84, v27\n"
"v_rsq_f32 v88, v84\n"
"ds_read_b64 v[72:73], v3 offset:54272\n"
"v_mul_f32 v89, -v89, v88\n"
"v_mfma_f32_16x16x32_bf16 v[40:43], v[148:151], v[76:79], v[40:43]\n"
"ds_read_b64 v[74:75], v3 offset:54528\n"
"s_cbranch_vccnz .Lsl_220c\n"
".Lbk_221c:\n"
"s_waitcnt lgkmcnt(7)\n"
"ds_read_b64 v[76:77], v3 offset:55296\n"
"v_mul_f32 v28, v88, v28\n"
"v_mul_f32 v29, v88, v29\n"
"v_mfma_f32_16x16x32_bf16 v[40:43], v[156:159], v[56:59], v[40:43]\n"
"ds_read_b64 v[78:79], v3 offset:55552\n"
"ds_read_b128 v[44:47], v11 offset:4992\n"
"ds_read_b128 v[48:51], v11 offset:5312\n"
"ds_read_b64 v[56:57], v3 offset:56320\n"
"v_mul_f32 v30, v88, v30\n"
"v_mul_f32 v31, v88, v31\n"
"v_mfma_f32_16x16x32_bf16 v[40:43], v[172:175], v[60:63], v[40:43]\n"
"ds_read_b64 v[58:59], v3 offset:56576\n"
"v_fmac_f32 v28, v89, v16\n"
"s_waitcnt lgkmcnt(8)\n"
"ds_read_b64 v[60:61], v4 offset:40960\n"
"v_fmac_f32 v29, v89, v17\n"
"v_fmac_f32 v30, v89, v18\n"
"v_mfma_f32_16x16x32_bf16 v[40:43], v[180:183], v[64:67], v[40:43]\n"
"ds_read_b64 v[62:63], v4 offset:41216\n"
"v_fmac_f32 v31, v89, v19\n"
"v_cvt_pk_bf16_f32 v28, v28, v29\n"
"ds_read_b64 v[64:65], v5 offset:40960\n"
"v_cvt_pk_bf16_f32 v29, v30, v31\n"
"ds_write_b64 v7, v[28:29] offset:56832\n"
"v_mfma_f32_16x16x32_bf16 v[40:43], v[188:191], v[68:71], v[40:43]\n"
"ds_read_b64 v[66:67], v5 offset:41216\n"
"v_mul_f32 v32, v88, v32\n"
"s_waitcnt lgkmcnt(9)\n"
"ds_read_b64 v[68:69], v6 offset:40960\n"
"s_waitcnt lgkmcnt(9)\n"
"v_add_f32 v44, v44, v45\n"
"v_mul_f32 v33, v88, v33\n"
"v_mfma_f32_16x16x32_bf16 v[40:43], v[204:207], v[72:75], v[40:43]\n"
"ds_read_b64 v[70:71], v6 offset:41216\n"
"v_add_f32 v46, v46, v47\n"
"v_mul_f32 v34, v88, v34\n"
"s_waitcnt lgkmcnt(9)\n"
"v_add_f32 v48, v48, v49\n"
"v_add_f32 v50, v50, v51\n"
"v_mul_f32 v35, v88, v35\n"
"v_mfma_f32_16x16x32_bf16 v[40:43], v[212:215], v[76:79], v[40:43]\n"
"v_add_f32 v48, v48, v50\n"
"v_fmac_f32 v32, v89, v20\n"
"s_waitcnt lgkmcnt(5)\n"
"v_add_f32 v44, v44, v46\n"
"v_mul_f32 v53, 0x3acccccd, v48\n"
"v_fmac_f32 v33, v89, v21\n"
"v_mfma_f32_16x16x32_bf16 v[40:43], v[220:223], v[56:59], v[40:43]\n"
"v_fma_f32 v44, -v48, v53, v44\n"
"v_fmamk_f32 v44, v44, 0x3acccccd, v27\n"
"v_fmac_f32 v34, v89, v22\n"
"v_cmp_u_f32 vcc, v44, v44\n"
"v_max_f32 v44, v44, v27\n"
"v_fmac_f32 v35, v89, v23\n"
"v_mfma_f32_16x16x32_bf16 v[40:43], v[236:239], v[60:63], v[40:43]\n"
"v_rsq_f32 v52, v44\n"
"s_waitcnt lgkmcnt(0)\n"
"v_mul_f32 v53, -v53, v52\n"
"v_cvt_pk_bf16_f32 v32, v32, v33\n"
"v_cvt_pk_bf16_f32 v33, v34, v35\n"
"v_mfma_f32_16x16x32_bf16 v[40:43], v[244:247], v[64:67], v[40:43]\n"
"s_cbranch_vccnz .Lsl_224c\n"
".Lbk_225c:\n"
"ds_write_b64 v8, v[32:33] offset:56832\n"
"v_mfma_f32_16x16x32_bf16 v[40:43], v[252:255], v[68:71], v[40:43]\n"
"v_cmp_u_f32 vcc, v92, v92\n"
"s_nop 6\n"
"v_mul_f32 v40, v52, v40\n"
"v_mul_f32 v41, v52, v41\n"
"v_mul_f32 v42, v52, v42\n"
"v_mul_f32 v43, v52, v43\n"
"v_fmac_f32 v40, v53, v20\n"
"v_fmac_f32 v41, v53, v21\n"
"v_fmac_f32 v42, v53, v22\n"
"v_fmac_f32 v43, v53, v23\n"
"v_cvt_pk_bf16_f32 v40, v40, v41\n"
"v_cvt_pk_bf16_f32 v41, v42, v43\n"
"ds_write_b64 v8, v[40:41] offset:40960\n"
"s_cbranch_vccnz .Lrd_195\n"
".Lgo_194c:\n"
"ds_write_b32 v13, v13 offset:432\n"
"s_branch .Ljn_229\n"
".Lfu_228:\n"
"s_nop 0\n"
"v_mfma_f32_16x16x32_bf16 v[36:39], v[96:99], v[64:67], 0\n"
"ds_read_b64 v[60:61], v3 offset:41984\n"
"v_fmac_f32 v51, v89, v23\n"
"v_mfma_f32_16x16x32_bf16 v[40:43], v[100:103], v[64:67], 0\n"
"ds_read_b64 v[62:63], v3 offset:42240\n"
"v_cndmask_b32 v94, v28, v94, s[22:23]\n"
"v_mfma_f32_16x16x32_bf16 v[52:55], v[64:67], v[64:67], 0\n"
"ds_write_b32 v9, v94 offset:4672\n"
"v_mfma_f32_16x16x32_bf16 v[36:39], v[128:131], v[68:71], v[36:39]\n"
"ds_read_b64 v[64:65], v3 offset:43008\n"
"v_cvt_pk_bf16_f32 v48, v48, v49\n"
"v_mfma_f32_16x16x32_bf16 v[40:43], v[132:135], v[68:71], v[40:43]\n"
"ds_read_b64 v[66:67], v3 offset:43264\n"
"v_cvt_pk_bf16_f32 v49, v50, v51\n"
"v_mfma_f32_16x16x32_bf16 v[52:55], v[68:71], v[68:71], v[52:55]\n"
"ds_write_b64 v7, v[44:45] offset:52864\n"
"s_waitcnt lgkmcnt(8)\n"
"v_mfma_f32_16x16x32_bf16 v[36:39], v[160:163], v[72:75], v[36:39]\n"
"ds_read_b64 v[68:69], v3 offset:44032\n"
"v_mfma_f32_16x16x32_bf16 v[40:43], v[164:167], v[72:75], v[40:43]\n"
"ds_read_b64 v[70:71], v3 offset:44288\n"
"v_mfma_f32_16x16x32_bf16 v[52:55], v[72:75], v[72:75], v[52:55]\n"
"ds_write_b64 v8, v[48:49] offset:52864\n"
"v_mfma_f32_16x16x32_bf16 v[36:39], v[192:195], v[76:79], v[36:39]\n"
"ds_read_b64 v[72:73], v3 offset:46080\n"
"v_mfma_f32_16x16x32_bf16 v[40:43], v[196:199], v[76:79], v[40:43]\n"
"ds_read_b64 v[74:75], v3 offset:46336\n"
"v_mfma_f32_16x16x32_bf16 v[52:55], v[76:79], v[76:79], v[52:55]\n"
"ds_read_b128 v[84:87], v11 offset:4672\n"
"ds_read_b32 v91, v12 offset:4672\n"
"s_waitcnt lgkmcnt(11)\n"
"v_mfma_f32_16x16x32_bf16 v[36:39], v[224:227], v[56:59], v[36:39]\n"
"ds_read_b64 v[76:77], v3 offset:47104\n"
"v_mfma_f32_16x16x32_bf16 v[40:43], v[228:231], v[56:59], v[40:43]\n"
"ds_read_b64 v[78:79], v3 offset:47360\n"
"v_mfma_f32_16x16x32_bf16 v[52:55], v[56:59], v[56:59], v[52:55]\n"
"v_mfma_f32_16x16x32_bf16 v[36:39], v[104:107], v[60:63], v[36:39]\n"
"ds_read_b64 v[56:57], v3 offset:48128\n"
"v_mfma_f32_16x16x32_bf16 v[40:43], v[108:111], v[60:63], v[40:43]\n"
"ds_read_b64 v[58:59], v3 offset:48384\n"
"s_waitcnt lgkmcnt(9)\n"
"v_mfma_f32_16x16x32_bf16 v[36:39], v[112:115], v[64:67], v[36:39]\n"
"ds_read_b64 v[60:61], v3 offset:50176\n"
"ds_write_b32 v10, v52 offset:5312\n"
"v_cndmask_b32 v94, v52, v53, s[34:35]\n"
"v_cndmask_b32 v54, v54, v55, s[34:35]\n"
"v_mfma_f32_16x16x32_bf16 v[40:43], v[116:119], v[64:67], v[40:43]\n"
"ds_read_b64 v[62:63], v3 offset:50432\n"
"s_waitcnt lgkmcnt(8)\n"
"v_add_f32 v84, v84, v85\n"
"v_add_f32 v86, v86, v87\n"
"v_cndmask_b32 v94, v94, v54, s[20:21]\n"
"ds_write_b32 v9, v94 offset:4992\n"
"v_mfma_f32_16x16x32_bf16 v[36:39], v[120:123], v[68:71], v[36:39]\n"
"ds_read_b64 v[64:65], v3 offset:51200\n"
"v_add_f32 v84, v84, v86\n"
"s_waitcnt lgkmcnt(9)\n"
"v_mul_f32 v89, 0x3acccccd, v91\n"
"v_mfma_f32_16x16x32_bf16 v[40:43], v[124:127], v[68:71], v[40:43]\n"
"ds_read_b64 v[66:67], v3 offset:51456\n"
"v_fma_f32 v84, -v91, v89, v84\n"
"s_waitcnt lgkmcnt(8)\n"
"v_mfma_f32_16x16x32_bf16 v[36:39], v[136:139], v[72:75], v[36:39]\n"
"ds_read_b64 v[68:69], v3 offset:52224\n"
"v_fmamk_f32 v84, v84, 0x3acccccd, v27\n"
"v_cmp_u_f32 vcc, v84, v84\n"
"v_mfma_f32_16x16x32_bf16 v[40:43], v[140:143], v[72:75], v[40:43]\n"
"ds_read_b64 v[70:71], v3 offset:52480\n"
"v_max_f32 v84, v84, v27\n"
"v_rsq_f32 v88, v84\n"
"v_mfma_f32_16x16x32_bf16 v[36:39], v[144:147], v[76:79], v[36:39]\n"
"ds_read_b64 v[72:73], v3 offset:54272\n"
"v_mul_f32 v89, -v89, v88\n"
"v_mfma_f32_16x16x32_bf16 v[40:43], v[148:151], v[76:79], v[40:43]\n"
"ds_read_b64 v[74:75], v3 offset:54528\n"
"s_cbranch_vccnz .Lsl_220\n"
".Lbk_221:\n"
"s_waitcnt lgkmcnt(7)\n"
"v_mfma_f32_16x16x32_bf16 v[36:39], v[152:155], v[56:59], v[36:39]\n"
"ds_read_b64 v[76:77], v3 offset:55296\n"
"v_mul_f32 v28, v88, v28\n"
"v_mul_f32 v29, v88, v29\n"
"v_mfma_f32_16x16x32_bf16 v[40:43], v[156:159], v[56:59], v[40:43]\n"
"ds_read_b64 v[78:79], v3 offset:55552\n"
"ds_read_b128 v[44:47], v11 offset:4992\n"
"ds_read_b128 v[48:51], v11 offset:5312\n"
"v_mfma_f32_16x16x32_bf16 v[36:39], v[168:171], v[60:63], v[36:39]\n"
"ds_read_b64 v[56:57], v3 offset:56320\n"
"v_mul_f32 v30, v88, v30\n"
"v_mul_f32 v31, v88, v31\n"
"v_mfma_f32_16x16x32_bf16 v[40:43], v[172:175], v[60:63], v[40:43]\n"
"ds_read_b64 v[58:59], v3 offset:56576\n"
"v_fmac_f32 v28, v89, v16\n"
"s_waitcnt lgkmcnt(8)\n"
"v_mfma_f32_16x16x32_bf16 v[36:39], v[176:179], v[64:67], v[36:39]\n"
"ds_read_b64 v[60:61], v4 offset:40960\n"
"v_fmac_f32 v29, v89, v17\n"
"v_fmac_f32 v30, v89, v18\n"
"v_mfma_f32_16x16x32_bf16 v[40:43], v[180:183], v[64:67], v[40:43]\n"
"ds_read_b64 v[62:63], v4 offset:41216\n"
"v_fmac_f32 v31, v89, v19\n"
"v_cvt_pk_bf16_f32 v28, v28, v29\n"
"v_mfma_f32_16x16x32_bf16 v[36:39], v[184:187], v[68:71], v[36:39]\n"
"ds_read_b64 v[64:65], v5 offset:40960\n"
"v_cvt_pk_bf16_f32 v29, v30, v31\n"
"ds_write_b64 v7, v[28:29] offset:56832\n"
"v_mfma_f32_16x16x32_bf16 v[40:43], v[188:191], v[68:71], v[40:43]\n"
"ds_read_b64 v[66:67], v5 offset:41216\n"
"v_mul_f32 v32, v88, v32\n"
"s_waitcnt lgkmcnt(9)\n"
"v_mfma_f32_16x16x32_bf16 v[36:39], v[200:203], v[72:75], v[36:39]\n"
"ds_read_b64 v[68:69], v6 offset:40960\n"
"s_waitcnt lgkmcnt(9)\n"
"v_add_f32 v44, v44, v45\n"
"v_mul_f32 v33, v88, v33\n"
"v_mfma_f32_16x16x32_bf16 v[40:43], v[204:207], v[72:75], v[40:43]\n"
"ds_read_b64 v[70:71], v6 offset:41216\n"
"v_add_f32 v46, v46, v47\n"
"v_mul_f32 v34, v88, v34\n"
"v_mfma_f32_16x16x32_bf16 v[36:39], v[208:211], v[76:79], v[36:39]\n"
"s_waitcnt lgkmcnt(9)\n"
"v_add_f32 v48, v48, v49\n"
"v_add_f32 v50, v50, v51\n"
"v_mul_f32 v35, v88, v35\n"
"v_mfma_f32_16x16x32_bf16 v[40:43], v[212:215], v[76:79], v[40:43]\n"
"v_add_f32 v48, v48, v50\n"
"v_fmac_f32 v32, v89, v20\n"
"s_waitcnt lgkmcnt(5)\n"
"v_mfma_f32_16x16x32_bf16 v[36:39], v[216:219], v[56:59], v[36:39]\n"
"v_add_f32 v44, v44, v46\n"
"v_mul_f32 v53, 0x3acccccd, v48\n"
"v_fmac_f32 v33, v89, v21\n"
"v_mfma_f32_16x16x32_bf16 v[40:43], v[220:223], v[56:59], v[40:43]\n"
"v_fma_f32 v44, -v48, v53, v44\n"
"v_fmamk_f32 v44, v44, 0x3acccccd, v27\n"
"v_fmac_f32 v34, v89, v22\n"
"v_mfma_f32_16x16x32_bf16 v[36:39], v[232:235], v[60:63], v[36:39]\n"
"v_cmp_u_f32 vcc, v44, v44\n"
"v_max_f32 v44, v44, v27\n"
"v_fmac_f32 v35, v89, v23\n"
"v_mfma_f32_16x16x32_bf16 v[40:43], v[236:239], v[60:63], v[40:43]\n"
"v_rsq_f32 v52, v44\n"
"s_waitcnt lgkmcnt(0)\n"
"v_mfma_f32_16x16x32_bf16 v[36:39], v[240:243], v[64:67], v[36:39]\n"
"v_mul_f32 v53, -v53, v52\n"
"v_cvt_pk_bf16_f32 v32, v32, v33\n"
"v_cvt_pk_bf16_f32 v33, v34, v35\n"
"v_mfma_f32_16x16x32_bf16 v[40:43], v[244:247], v[64:67], v[40:43]\n"
"s_cbranch_vccnz .Lsl_224\n"
".Lbk_225:\n"
"ds_write_b64 v8, v[32:33] offset:56832\n"
"v_mfma_f32_16x16x32_bf16 v[36:39], v[248:251], v[68:71], v[36:39]\n"
"v_mfma_f32_16x16x32_bf16 v[40:43], v[252:255], v[68:71], v[40:43]\n"
"v_cmp_u_f32 vcc, v92, v92\n"
"s_nop 3\n"
"v_mul_f32 v36, v52, v36\n"
"v_mul_f32 v37, v52, v37\n"
"v_mul_f32 v38, v52, v38\n"
"v_mul_f32 v39, v52, v39\n"
"v_fmac_f32 v36, v53, v16\n"
"v_fmac_f32 v37, v53, v17\n"
"v_fmac_f32 v38, v53, v18\n"
"v_fmac_f32 v39, v53, v19\n"
"v_cvt_pk_bf16_f32 v36, v36, v37\n"
"v_cvt_pk_bf16_f32 v37, v38, v39\n"
"ds_write_b64 v7, v[36:37] offset:40960\n"
"v_mul_f32 v40, v52, v40\n"
"v_mul_f32 v41, v52, v41\n"
"v_mul_f32 v42, v52, v42\n"
"v_mul_f32 v43, v52, v43\n"
"v_fmac_f32 v40, v53, v20\n"
"v_fmac_f32 v41, v53, v21\n"
"v_fmac_f32 v42, v53, v22\n"
"v_fmac_f32 v43, v53, v23\n"
"v_cvt_pk_bf16_f32 v40, v40, v41\n"
"v_cvt_pk_bf16_f32 v41, v42, v43\n"
"ds_write_b64 v8, v[40:41] offset:40960\n"
"s_cbranch_vccnz .Lrd_195\n"
".Lgo_194:\n"
"ds_write_b32 v13, v13 offset:432\n"
".Ljn_229:\n"
"v_mov_b32 v46, 0\n"
"v_mov_b32 v47, 0\n"
"v_mov_b32 v50, 0\n"
"v_mov_b32 v51, 0\n"
"s_mov_b32 s17, 0x100000\n"
".Ltp_274:\n"
"ds_read_b32 v90, v14 offset:432\n"
"s_sub_u32 s17, s17, 1\n"
"s_cbranch_scc1 .Lou_275\n"
"s_waitcnt lgkmcnt(0)\n"
"v_cmp_eq_u32 vcc, 0, v90\n"
"s_cbranch_vccnz .Ltp_274\n"
".Lou_275:\n"
"s_waitcnt lgkmcnt(0)\n"
"s_mov_b64 exec, 0xffff\n"
"ds_read_b64 v[46:47], v24 offset:44960\n"
"ds_read_b64 v[50:51], v24 offset:45000\n"
"s_mov_b64 exec, -1\n"
"ds_read_b64 v[44:45], v25 offset:44928\n"
"ds_read_b64 v[56:57], v25 offset:41040\n"
"ds_read_b64 v[58:59], v24 offset:41072\n"
"ds_read_b64 v[64:65], v25 offset:45008\n"
"ds_read_b64 v[66:67], v24 offset:45040\n"
"ds_read_b64 v[48:49], v25 offset:44968\n"
"ds_read_b64 v[60:61], v25 offset:41080\n"
"ds_read_b64 v[62:63], v24 offset:41112\n"
"ds_read_b64 v[68:69], v25 offset:45048\n"
"ds_read_b64 v[70:71], v24 offset:45080\n"
"ds_read_b64_tr_b16 v[28:29], v26 offset:40960\n"
"ds_read_b64_tr_b16 v[30:31], v26 offset:44928\n"
"ds_read_b64_tr_b16 v[32:33], v26 offset:41000\n"
"s_waitcnt lgkmcnt(10)\n"
"v_mfma_f32_16x16x32_bf16 v[56:59], v[56:59], v[44:47], 0\n"
"s_waitcnt lgkmcnt(8)\n"
"v_mfma_f32_16x16x32_bf16 v[64:67], v[64:67], v[44:47], 0\n"
"ds_read_b64_tr_b16 v[34:35], v26 offset:44968\n"
"ds_read_b64_tr_b16 v[36:37], v26 offset:40992\n"
"ds_read_b64_tr_b16 v[38:39], v26 offset:44960\n"
"s_waitcnt lgkmcnt(8)\n"
"v_mfma_f32_16x16x32_bf16 v[60:63], v[60:63], v[48:51], 0\n"
"s_waitcnt lgkmcnt(6)\n"
"v_mfma_f32_16x16x32_bf16 v[68:71], v[68:71], v[48:51], 0\n"
"ds_read_b64_tr_b16 v[40:41], v26 offset:41032\n"
"ds_read_b64_tr_b16 v[42:43], v26 offset:45000\n"
"v_mov_b32 v84, 0x3f803f80\n"
"v_mov_b32 v85, 0x3f803f80\n"
"v_mov_b32 v86, 0x3f803f80\n"
"v_mov_b32 v87, 0x3f803f80\n"
"v_cndmask_b32 v76, v56, v57, s[34:35]\n"
"v_cndmask_b32 v72, v64, v65, s[34:35]\n"
"v_cndmask_b32 v76, v76, v72, s[36:37]\n"
"v_cndmask_b32 v77, v58, v59, s[34:35]\n"
"v_cndmask_b32 v72, v66, v67, s[34:35]\n"
"v_cndmask_b32 v77, v77, v72, s[38:39]\n"
"v_max_f32 v52, v76, v77\n"
"v_max_f32 v54, v76, v77\n"
"v_cndmask_b32 v78, v60, v61, s[34:35]\n"
"v_cndmask_b32 v72, v68, v69, s[34:35]\n"
"v_cndmask_b32 v78, v78, v72, s[36:37]\n"
"v_cndmask_b32 v79, v62, v63, s[34:35]\n"
"v_cndmask_b32 v72, v70, v71, s[34:35]\n"
"v_cndmask_b32 v79, v79, v72, s[38:39]\n"
"v_max_f32 v53, v78, v79\n"
"v_max_f32 v55, v78, v79\n"
"v_permlane16_swap_b32 v52, v54\n"
"s_nop 0\n"
"v_permlane16_swap_b32 v53, v55\n"
"v_max_f32 v72, v52, v54\n"
"v_max_f32 v52, v52, v54\n"
"v_max_f32 v73, v53, v55\n"
"v_max_f32 v53, v53, v55\n"
"v_permlane32_swap_b32 v52, v72\n"
"s_nop 0\n"
"v_permlane32_swap_b32 v53, v73\n"
"v_max_f32 v52, v52, v72\n"
"v_max_f32 v53, v53, v73\n"
"v_mul_f32 v52, 0xbea52b5b, v52\n"
"v_mul_f32 v53, 0xbea52b5b, v53\n"
"v_fmamk_f32 v76, v76, 0x3ea52b5b, v52\n"
"v_fmamk_f32 v77, v77, 0x3ea52b5b, v52\n"
"v_fmamk_f32 v78, v78, 0x3ea52b5b, v53\n"
"v_fmamk_f32 v79, v79, 0x3ea52b5b, v53\n"
"v_exp_f32 v76, v76\n"
"v_exp_f32 v77, v77\n"
"v_exp_f32 v78, v78\n"
"v_exp_f32 v79, v79\n"
"v_cvt_pk_bf16_f32 v52, v76, v77\n"
"v_cvt_pk_bf16_f32 v53, v78, v79\n"
"v_perm_b32 v44, v52, v52, v80\n"
"v_perm_b32 v45, v52, v52, v81\n"
"v_perm_b32 v46, v52, v52, v82\n"
"v_perm_b32 v47, v52, v52, v83\n"
"v_perm_b32 v48, v53, v53, v80\n"
"v_perm_b32 v49, v53, v53, v81\n"
"v_perm_b32 v50, v53, v53, v82\n"
"v_perm_b32 v51, v53, v53, v83\n"
"v_mfma_f32_16x16x32_bf16 v[56:59], v[84:87], v[44:47], 0\n"
"s_nop 0\n"
"v_mfma_f32_16x16x32_bf16 v[60:63], v[84:87], v[48:51], 0\n"
"s_waitcnt lgkmcnt(6)\n"
"v_mfma_f32_16x16x32_bf16 v[28:31], v[28:31], v[44:47], 0\n"
"s_waitcnt lgkmcnt(4)\n"
"v_mfma_f32_16x16x32_bf16 v[32:35], v[32:35], v[48:51], 0\n"
"s_waitcnt lgkmcnt(2)\n"
"v_mfma_f32_16x16x32_bf16 v[36:39], v[36:39], v[44:47], 0\n"
"s_waitcnt lgkmcnt(0)\n"
"v_mfma_f32_16x16x32_bf16 v[40:43], v[40:43], v[48:51], 0\n"
"v_cndmask_b32 v88, 2.0, v1, s[40:41]\n"
"v_rcp_f32 v52, v56\n"
"v_rcp_f32 v53, v60\n"
"s_add_u32 s31, s30, 0xba0000\n"
"v_mul_f32 v28, v52, v28\n"
"v_mul_f32 v29, v52, v29\n"
"v_mul_f32 v30, v52, v30\n"
"v_mul_f32 v31, v52, v31\n"
"v_mul_f32 v32, v53, v32\n"
"v_mul_f32 v33, v53, v33\n"
"v_mul_f32 v34, v53, v34\n"
"v_mul_f32 v35, v53, v35\n"
"v_cvt_pk_bf16_f32 v28, v28, v29\n"
"v_cvt_pk_bf16_f32 v29, v30, v31\n"
"v_cvt_pk_bf16_f32 v30, v32, v33\n"
"v_cvt_pk_bf16_f32 v31, v34, v35\n"
"v_min_u32 v28, -2, v28\n"
"v_min_u32 v29, -2, v29\n"
"v_min_u32 v30, -2, v30\n"
"v_min_u32 v31, -2, v31\n"
"buffer_store_dwordx4 v[28:31], v1, s[4:7], s31 offen offset:0 sc1\n"
"v_mul_f32 v36, v52, v36\n"
"v_mul_f32 v37, v52, v37\n"
"v_mul_f32 v38, v52, v38\n"
"v_mul_f32 v39, v52, v39\n"
"v_mul_f32 v40, v53, v40\n"
"v_mul_f32 v41, v53, v41\n"
"v_mul_f32 v42, v53, v42\n"
"v_mul_f32 v43, v53, v43\n"
"v_cvt_pk_bf16_f32 v36, v36, v37\n"
"v_cvt_pk_bf16_f32 v37, v38, v39\n"
"v_cvt_pk_bf16_f32 v38, v40, v41\n"
"v_cvt_pk_bf16_f32 v39, v42, v43\n"
"v_min_u32 v36, -2, v36\n"
"v_min_u32 v37, -2, v37\n"
"v_min_u32 v38, -2, v38\n"
"v_min_u32 v39, -2, v39\n"
"buffer_store_dwordx4 v[36:39], v88, s[4:7], s31 offen offset:1024 sc1\n"
"ds_write_b32 v13, v13 offset:480\n"
"s_nop 2\n"
"s_cmp_lt_i32 s45, 0\n"
"s_cbranch_scc1 .Lnc_276\n"
"s_setprio 0\n"
"s_lshl_b32 s79, s3, 10\n"
"v_lshl_add_u32 v34, v0, 4, s79\n"
"v_lshrrev_b32 v59, 7, v34\n"
"v_and_b32 v58, 0x70, v34\n"
"s_mov_b32 s79, 0x5000\n"
"v_mad_i32_i24 v33, v59, s79, v58\n"
"s_mov_b32 s17, 0x100000\n"
".Ltp_277:\n"
"s_sleep 1\n"
"ds_read_b32 v42, v14 offset:496\n"
"s_sub_u32 s17, s17, 1\n"
"s_cbranch_scc1 .Lou_278\n"
"s_waitcnt lgkmcnt(0)\n"
"v_cmp_eq_u32 vcc, 0, v42\n"
"s_cbranch_vccnz .Ltp_277\n"
".Lou_278:\n"
"s_waitcnt lgkmcnt(0)\n"
"s_lshl_b32 s79, s45, 8\n"
"s_add_u32 s46, s79, 0x1128000\n"
"s_add_u32 s47, s46, 0x140000\n"
"s_and_b32 s79, s2, 7\n"
"s_lshl_b32 s79, s79, 14\n"
"s_add_u32 s48, s79, 0x1668000\n"
"s_add_u32 s49, s48, 0x1000\n"
"v_and_b32 v94, 0xffffff80, v33\n"
"v_add_u32 v94, v94, v33\n"
"buffer_load_dwordx4 v[78:81], v94, s[4:7], s46 offen offset:0 sc1\n"
"buffer_load_dwordx4 v[82:85], v94, s[4:7], s46 offen offset:128 sc1\n"
"buffer_load_dwordx4 v[86:89], v94, s[4:7], s47 offen offset:0 sc1\n"
"buffer_load_dwordx4 v[90:93], v94, s[4:7], s47 offen offset:128 sc1\n"
"v_and_b32 v51, 15, v0\n"
"v_and_b32 v50, 1, v51\n"
"v_and_b32 v49, 2, v51\n"
"v_lshlrev_b32 v48, 4, v0\n"
"v_lshrrev_b32 v47, 4, v0\n"
"v_xor_b32 v46, s3, v51\n"
"v_and_b32 v45, 1, v46\n"
"v_lshlrev_b32 v44, 1, v45\n"
"s_lshl_b32 s79, s3, 10\n"
"v_add_u32 v59, s79, v48\n"
"v_lshrrev_b32 v59, 2, v59\n"
"s_lshl_b32 s79, s3, 10\n"
"v_add_u32 v58, s79, v48\n"
"v_lshrrev_b32 v58, 6, v58\n"
"v_and_b32 v58, 24, v58\n"
"v_xor_b32 v59, v59, v58\n"
"v_add_u32 v36, 0xa000, v59\n"
"s_lshl_b32 s79, s3, 8\n"
"s_add_u32 s79, s79, 0xa000\n"
"v_lshrrev_b32 v59, 2, v51\n"
"v_lshrrev_b32 v58, 1, v47\n"
"v_xor_b32 v59, v59, v58\n"
"v_and_b32 v59, 1, v59\n"
"v_add_u32 v59, v44, v59\n"
"v_lshrrev_b32 v58, 2, v51\n"
"v_lshrrev_b32 v57, 3, v51\n"
"v_lshlrev_b32 v57, 7, v57\n"
"v_lshl_add_u32 v58, v58, 11, v57\n"
"v_lshl_add_u32 v58, v47, 6, v58\n"
"v_lshl_add_u32 v58, v49, 4, v58\n"
"v_lshl_add_u32 v59, v59, 3, v58\n"
"v_add_u32 v31, s79, v59\n"
"s_mul_i32 s79, s3, 0x2100\n"
"s_add_u32 s79, s79, 0x13840\n"
"v_lshlrev_b32 v59, 3, v47\n"
"v_sub_u32 v59, v59, v51\n"
"v_add_u32 v59, 0x3ef, v59\n"
"v_lshrrev_b32 v59, 2, v59\n"
"v_lshlrev_b32 v58, 3, v47\n"
"v_sub_u32 v58, v58, v51\n"
"v_add_u32 v58, 0x3ef, v58\n"
"v_and_b32 v58, 3, v58\n"
"v_mul_i32_i24 v58, 0x840, v58\n"
"v_lshl_add_u32 v59, v59, 3, v58\n"
"v_add_u32 v28, s79, v59\n"
"s_mul_i32 s79, s3, 0x1200\n"
"s_add_u32 s79, s79, 0x1c420\n"
"v_lshlrev_b32 v59, 4, v47\n"
"v_lshl_add_u32 v59, v51, 5, v59\n"
"v_add_u32 v29, s79, v59\n"
"s_lshl_b32 s79, s3, 11\n"
"s_lshr_b32 s78, s3, 1\n"
"s_lshl_b32 s78, s78, 7\n"
"s_add_u32 s79, s79, s78\n"
"s_add_u32 s79, s79, 0xa000\n"
"s_and_b32 s78, s3, 1\n"
"v_lshrrev_b32 v59, 2, v51\n"
"v_xor_b32 v59, v47, v59\n"
"v_xor_b32 v59, s78, v59\n"
"v_and_b32 v59, 3, v59\n"
"v_lshlrev_b32 v59, 3, v59\n"
"v_lshl_add_u32 v59, v51, 5, v59\n"
"v_add_u32 v30, s79, v59\n"
"v_xor_b32 v32, 8, v31\n"
"s_lshl_b32 s79, s3, 10\n"
"v_add_u32 v59, s79, v48\n"
"v_lshrrev_b32 v59, 2, v59\n"
"v_add_u32 v35, 0x1c600, v59\n"
"v_xor_b32 v37, 8, v36\n"
"v_mov_b32 v59, 0xffff0000\n"
"v_mov_b32 v58, 0xffff\n"
"v_cmp_eq_i32 vcc, 1, v50\n"
"s_nop 1\n"
"v_cndmask_b32 v59, v58, v59, vcc\n"
"v_lshrrev_b32 v58, 1, v51\n"
"v_lshlrev_b32 v57, 1, v47\n"
"v_cmp_eq_i32 vcc, v58, v57\n"
"s_nop 1\n"
"v_cndmask_b32 v38, 0, v59, vcc\n"
"v_mov_b32 v59, 0xffff0000\n"
"v_mov_b32 v58, 0xffff\n"
"v_cmp_eq_i32 vcc, 1, v50\n"
"s_nop 1\n"
"v_cndmask_b32 v59, v58, v59, vcc\n"
"v_lshrrev_b32 v58, 1, v51\n"
"v_lshl_add_u32 v57, v47, 1, 1\n"
"v_cmp_eq_i32 vcc, v58, v57\n"
"s_nop 1\n"
"v_cndmask_b32 v39, 0, v59, vcc\n"
"s_lshl_b32 s79, s3, 2\n"
"s_add_u32 s79, s79, 0x26800\n"
"v_mov_b32 v40, s79\n"
"s_lshl_b32 s79, s3, 10\n"
"v_add_u32 v59, s79, v48\n"
"v_lshrrev_b32 v59, 6, v59\n"
"s_lshl_b32 s79, s3, 10\n"
"v_add_u32 v58, s79, v48\n"
"v_and_b32 v58, 48, v58\n"
"v_mul_i32_i24 v58, 0x50, v58\n"
"s_mov_b32 s79, 0x2800\n"
"v_mad_i32_i24 v41, v59, s79, v58\n"
"ds_read_b32 v40, v40\n"
"s_waitcnt lgkmcnt(0)\n"
"v_sub_u32 v44, 0x7f000000, v40\n"
"v_lshrrev_b32 v45, 16, v44\n"
"v_or_b32 v44, v44, v45\n"
"v_and_b32 v38, v44, v38\n"
"v_and_b32 v39, v44, v39\n"
"s_mov_b32 s52, 0x05040100\n"
"s_mov_b32 s53, 0x07060302\n"
"s_mov_b32 s17, 32768\n"
".Ltp_279:\n"
"s_waitcnt vmcnt(0)\n"
"s_sub_u32 s17, s17, 1\n"
"s_cbranch_scc1 .Lou_280\n"
"v_bitop3_b32 v54, v79, v81, s16 bitop3:0x7e\n"
"v_bitop3_b32 v55, v83, v85, s16 bitop3:0x7e\n"
"v_bitop3_b32 v56, v87, v89, s16 bitop3:0x7e\n"
"v_bitop3_b32 v57, v91, v93, s16 bitop3:0x7e\n"
"v_or3_b32 v58, v54, v55, v56\n"
"v_or_b32 v58, v58, v57\n"
"v_cmp_ne_u32 vcc, 0, v58\n"
"s_cbranch_vccz .Lou_280\n"
"s_sleep 2\n"
"v_cmp_ne_u32 vcc, 0, v54\n"
"s_cbranch_vccz .Lsk_281\n"
"s_mov_b64 exec, vcc\n"
"buffer_load_dwordx4 v[78:81], v94, s[4:7], s46 offen offset:0 sc1\n"
"s_mov_b64 exec, -1\n"
".Lsk_281:\n"
"v_cmp_ne_u32 vcc, 0, v55\n"
"s_cbranch_vccz .Lsk_282\n"
"s_mov_b64 exec, vcc\n"
"buffer_load_dwordx4 v[82:85], v94, s[4:7], s46 offen offset:128 sc1\n"
"s_mov_b64 exec, -1\n"
".Lsk_282:\n"
"v_cmp_ne_u32 vcc, 0, v56\n"
"s_cbranch_vccz .Lsk_283\n"
"s_mov_b64 exec, vcc\n"
"buffer_load_dwordx4 v[86:89], v94, s[4:7], s47 offen offset:0 sc1\n"
"s_mov_b64 exec, -1\n"
".Lsk_283:\n"
"v_cmp_ne_u32 vcc, 0, v57\n"
"s_cbranch_vccz .Lsk_284\n"
"s_mov_b64 exec, vcc\n"
"buffer_load_dwordx4 v[90:93], v94, s[4:7], s47 offen offset:128 sc1\n"
"s_mov_b64 exec, -1\n"
".Lsk_284:\n"
"s_branch .Ltp_279\n"
".Lou_280:\n"
"s_waitcnt vmcnt(0)\n"
"buffer_load_dwordx4 v[70:73], v34, s[4:7], s48 offen sc1\n"
"buffer_load_dwordx4 v[74:77], v34, s[4:7], s49 offen sc1\n"
"v_perm_b32 v54, v82, v78, s52\n"
"v_perm_b32 v55, v82, v78, s53\n"
"v_perm_b32 v56, v84, v80, s52\n"
"v_perm_b32 v57, v84, v80, s53\n"
"ds_write_b32 v36, v54 offset:0\n"
"ds_write_b32 v37, v55 offset:2048\n"
"ds_write_b32 v36, v56 offset:4224\n"
"ds_write_b32 v37, v57 offset:6272\n"
"v_perm_b32 v54, v90, v86, s52\n"
"v_perm_b32 v55, v90, v86, s53\n"
"v_perm_b32 v56, v92, v88, s52\n"
"v_perm_b32 v57, v92, v88, s53\n"
"ds_write_b32 v36, v54 offset:1024\n"
"ds_write_b32 v37, v55 offset:3072\n"
"ds_write_b32 v36, v56 offset:5248\n"
"ds_write_b32 v37, v57 offset:7296\n"
"v_lshlrev_b32 v54, 16, v78\n"
"v_lshlrev_b32 v55, 16, v82\n"
"v_and_b32 v56, 0xffff0000, v78\n"
"v_and_b32 v57, 0xffff0000, v82\n"
"v_lshlrev_b32 v58, 16, v80\n"
"v_lshlrev_b32 v59, 16, v84\n"
"v_and_b32 v60, 0xffff0000, v80\n"
"v_and_b32 v61, 0xffff0000, v84\n"
"v_lshlrev_b32 v62, 16, v86\n"
"v_lshlrev_b32 v63, 16, v90\n"
"v_and_b32 v64, 0xffff0000, v86\n"
"v_and_b32 v65, 0xffff0000, v90\n"
"v_lshlrev_b32 v66, 16, v88\n"
"v_lshlrev_b32 v67, 16, v92\n"
"v_and_b32 v68, 0xffff0000, v88\n"
"v_and_b32 v69, 0xffff0000, v92\n"
"s_mov_b32 s17, 32768\n"
".Ltp_285:\n"
"s_waitcnt vmcnt(0)\n"
"s_sub_u32 s17, s17, 1\n"
"s_cbranch_scc1 .Lou_286\n"
"v_max_u32 v78, v70, v71\n"
"v_max3_u32 v78, v72, v73, v78\n"
"v_max3_u32 v78, v74, v75, v78\n"
"v_max3_u32 v78, v76, v77, v78\n"
"v_cmp_eq_u32 vcc, -1, v78\n"
"s_cbranch_vccz .Lou_286\n"
"v_max_u32 v78, v72, v73\n"
"v_max3_u32 v78, v70, v71, v78\n"
"v_cmp_eq_u32 vcc, -1, v78\n"
"s_cbranch_vccz .Lsk_287\n"
"s_mov_b64 exec, vcc\n"
"buffer_load_dwordx4 v[70:73], v34, s[4:7], s48 offen sc1\n"
"s_mov_b64 exec, -1\n"
".Lsk_287:\n"
"v_max_u32 v78, v76, v77\n"
"v_max3_u32 v78, v74, v75, v78\n"
"v_cmp_eq_u32 vcc, -1, v78\n"
"s_cbranch_vccz .Lsk_288\n"
"s_mov_b64 exec, vcc\n"
"buffer_load_dwordx4 v[74:77], v34, s[4:7], s49 offen sc1\n"
"s_mov_b64 exec, -1\n"
".Lsk_288:\n"
"s_branch .Ltp_285\n"
".Lou_286:\n"
"s_waitcnt vmcnt(0)\n"
"v_fma_f32 v54, v54, v71, v70\n"
"v_fma_f32 v55, v55, v73, v72\n"
"v_cvt_pk_f16_f32 v54, v54, v55\n"
"v_fma_f32 v56, v56, v71, v70\n"
"v_fma_f32 v57, v57, v73, v72\n"
"v_cvt_pk_f16_f32 v56, v56, v57\n"
"v_fma_f32 v58, v58, v71, v70\n"
"v_fma_f32 v59, v59, v73, v72\n"
"v_cvt_pk_f16_f32 v58, v58, v59\n"
"v_fma_f32 v60, v60, v71, v70\n"
"v_fma_f32 v61, v61, v73, v72\n"
"v_cvt_pk_f16_f32 v60, v60, v61\n"
"ds_write2st64_b32 v35, v54, v56 offset0:0 offset1:18\n"
"ds_write2st64_b32 v35, v58, v60 offset0:36 offset1:54\n"
"v_fma_f32 v62, v62, v75, v74\n"
"v_fma_f32 v63, v63, v77, v76\n"
"v_cvt_pk_f16_f32 v62, v62, v63\n"
"v_fma_f32 v64, v64, v75, v74\n"
"v_fma_f32 v65, v65, v77, v76\n"
"v_cvt_pk_f16_f32 v64, v64, v65\n"
"v_fma_f32 v66, v66, v75, v74\n"
"v_fma_f32 v67, v67, v77, v76\n"
"v_cvt_pk_f16_f32 v66, v66, v67\n"
"v_fma_f32 v68, v68, v75, v74\n"
"v_fma_f32 v69, v69, v77, v76\n"
"v_cvt_pk_f16_f32 v68, v68, v69\n"
"ds_write2st64_b32 v35, v62, v64 offset0:4 offset1:22\n"
"ds_write2st64_b32 v35, v66, v68 offset0:40 offset1:58\n"
"ds_write_b32 v13, v13 offset:512\n"
"s_mov_b32 s17, 0x100000\n"
".Ltp_289:\n"
"ds_read_b32 v42, v14 offset:512\n"
"s_sub_u32 s17, s17, 1\n"
"s_cbranch_scc1 .Lou_290\n"
"s_waitcnt lgkmcnt(0)\n"
"v_cmp_eq_u32 vcc, 0, v42\n"
"s_cbranch_vccnz .Ltp_289\n"
".Lou_290:\n"
"s_waitcnt lgkmcnt(0)\n"
"s_mov_b32 s50, 0xfffefffe\n"
"ds_read_b64 v[60:61], v28 offset:1984\n"
"ds_read_b64 v[62:63], v28 offset:1992\n"
"ds_read_b128 v[64:67], v29 offset:1984\n"
"ds_read_b128 v[68:71], v29 offset:1472\n"
"ds_read_b64 v[72:73], v28 offset:1472\n"
"ds_read_b64 v[74:75], v28 offset:1480\n"
"ds_read_b128 v[76:79], v29 offset:960\n"
"s_waitcnt lgkmcnt(0)\n"
"v_mfma_f32_16x16x32_f16 v[56:59], v[60:63], v[64:67], 0\n"
"ds_read_b64 v[80:81], v28 offset:960\n"
"ds_read_b64 v[82:83], v28 offset:968\n"
"v_mfma_f32_16x16x32_f16 v[52:55], v[60:63], v[68:71], 0\n"
"ds_read_b128 v[84:87], v29 offset:448\n"
"ds_read_b64 v[88:89], v28 offset:448\n"
"v_mfma_f32_16x16x32_f16 v[56:59], v[72:75], v[68:71], v[56:59]\n"
"ds_read_b64 v[90:91], v28 offset:456\n"
"ds_read_b64 v[92:93], v28 offset:1920\n"
"v_mfma_f32_16x16x32_f16 v[48:51], v[60:63], v[76:79], 0\n"
"ds_read_b64 v[94:95], v28 offset:1928\n"
"s_waitcnt lgkmcnt(4)\n"
"v_mfma_f32_16x16x32_f16 v[52:55], v[72:75], v[76:79], v[52:55]\n"
"ds_read_b128 v[2:5], v29 offset:1920\n"
"ds_read_b128 v[34:37], v29 offset:1408\n"
"v_mfma_f32_16x16x32_f16 v[56:59], v[80:83], v[76:79], v[56:59]\n"
"ds_read_b64 v[64:65], v28 offset:1408\n"
"ds_read_b64 v[66:67], v28 offset:1416\n"
"v_mfma_f32_16x16x32_f16 v[44:47], v[60:63], v[84:87], 0\n"
"ds_read_b128 v[68:71], v29 offset:896\n"
"ds_read_b64 v[76:77], v28 offset:896\n"
"v_mfma_f32_16x16x32_f16 v[48:51], v[72:75], v[84:87], v[48:51]\n"
"ds_read_b64 v[78:79], v28 offset:904\n"
"s_waitcnt lgkmcnt(5)\n"
"v_mfma_f32_16x16x32_f16 v[52:55], v[80:83], v[84:87], v[52:55]\n"
"ds_read_b128 v[60:63], v29 offset:384\n"
"ds_read_b64 v[72:73], v28 offset:384\n"
"v_mfma_f32_16x16x32_f16 v[56:59], v[88:91], v[84:87], v[56:59]\n"
"ds_read_b64 v[74:75], v28 offset:392\n"
"ds_read_b64 v[80:81], v28 offset:1856\n"
"v_mfma_f32_16x16x32_f16 v[56:59], v[92:95], v[2:5], v[56:59]\n"
"ds_read_b64 v[82:83], v28 offset:1864\n"
"ds_read_b128 v[84:87], v29 offset:1856\n"
"v_mfma_f32_16x16x32_f16 v[52:55], v[92:95], v[34:37], v[52:55]\n"
"ds_read_b128 v[88:91], v29 offset:1344\n"
"s_waitcnt lgkmcnt(7)\n"
"v_mfma_f32_16x16x32_f16 v[56:59], v[64:67], v[34:37], v[56:59]\n"
"ds_read_b64 v[2:3], v30 offset:0\n"
"ds_read_b64 v[34:35], v28 offset:1344\n"
"v_mfma_f32_16x16x32_f16 v[48:51], v[92:95], v[68:71], v[48:51]\n"
"ds_read_b64 v[36:37], v28 offset:1352\n"
"v_mfma_f32_16x16x32_f16 v[52:55], v[64:67], v[68:71], v[52:55]\n"
"v_mfma_f32_16x16x32_f16 v[56:59], v[76:79], v[68:71], v[56:59]\n"
"ds_read_b128 v[68:71], v29 offset:832\n"
"s_waitcnt lgkmcnt(8)\n"
"v_mfma_f32_16x16x32_f16 v[44:47], v[92:95], v[60:63], v[44:47]\n"
"ds_read_b64 v[92:93], v28 offset:832\n"
"v_mfma_f32_16x16x32_f16 v[48:51], v[64:67], v[60:63], v[48:51]\n"
"ds_read_b64 v[94:95], v28 offset:840\n"
"v_mfma_f32_16x16x32_f16 v[52:55], v[76:79], v[60:63], v[52:55]\n"
"ds_read_b128 v[64:67], v29 offset:320\n"
"ds_read_b64 v[76:77], v28 offset:320\n"
"v_mfma_f32_16x16x32_f16 v[56:59], v[72:75], v[60:63], v[56:59]\n"
"ds_read_b64 v[78:79], v28 offset:328\n"
"s_waitcnt lgkmcnt(6)\n"
"v_mfma_f32_16x16x32_f16 v[56:59], v[80:83], v[84:87], v[56:59]\n"
"ds_read_b64 v[60:61], v30 offset:512\n"
"ds_read_b64 v[72:73], v28 offset:1792\n"
"v_mfma_f32_16x16x32_f16 v[52:55], v[80:83], v[88:91], v[52:55]\n"
"ds_read_b64 v[74:75], v28 offset:1800\n"
"s_lshl_b32 s46, s45, 3\n"
"v_mfma_f32_16x16x16_bf16 v[44:47], v[38:39], v[2:3], v[44:47]\n"
"ds_read_b128 v[84:87], v29 offset:1792\n"
"v_mfma_f32_16x16x32_f16 v[56:59], v[34:37], v[88:91], v[56:59]\n"
"ds_read_b128 v[2:5], v29 offset:1280\n"
"s_waitcnt lgkmcnt(7)\n"
"v_mfma_f32_16x16x32_f16 v[48:51], v[80:83], v[68:71], v[48:51]\n"
"ds_read_b64 v[88:89], v28 offset:1280\n"
"v_mfma_f32_16x16x32_f16 v[52:55], v[34:37], v[68:71], v[52:55]\n"
"ds_read_b64 v[90:91], v28 offset:1288\n"
"v_mfma_f32_16x16x32_f16 v[56:59], v[92:95], v[68:71], v[56:59]\n"
"ds_read_b128 v[68:71], v29 offset:768\n"
"v_mfma_f32_16x16x32_f16 v[44:47], v[80:83], v[64:67], v[44:47]\n"
"ds_read_b64 v[80:81], v28 offset:768\n"
"s_waitcnt lgkmcnt(8)\n"
"v_mfma_f32_16x16x32_f16 v[48:51], v[34:37], v[64:67], v[48:51]\n"
"ds_read_b64 v[82:83], v28 offset:776\n"
"v_mfma_f32_16x16x32_f16 v[52:55], v[92:95], v[64:67], v[52:55]\n"
"ds_read_b128 v[34:37], v29 offset:256\n"
"ds_read_b64 v[92:93], v28 offset:256\n"
"v_mfma_f32_16x16x32_f16 v[56:59], v[76:79], v[64:67], v[56:59]\n"
"ds_read_b64 v[94:95], v28 offset:264\n"
"ds_read_b64 v[64:65], v28 offset:1728\n"
"v_mfma_f32_16x16x16_bf16 v[48:51], v[38:39], v[60:61], v[48:51]\n"
"ds_read_b64 v[66:67], v28 offset:1736\n"
"s_waitcnt lgkmcnt(7)\n"
"v_mfma_f32_16x16x32_f16 v[56:59], v[72:75], v[84:87], v[56:59]\n"
"ds_read_b128 v[76:79], v29 offset:1728\n"
"ds_read_b128 v[60:63], v29 offset:1216\n"
"v_mfma_f32_16x16x32_f16 v[52:55], v[72:75], v[2:5], v[52:55]\n"
"ds_read_b64 v[84:85], v28 offset:1216\n"
"v_mfma_f32_16x16x32_f16 v[56:59], v[88:91], v[2:5], v[56:59]\n"
"ds_read_b64 v[86:87], v28 offset:1224\n"
"v_mfma_f32_16x16x32_f16 v[48:51], v[72:75], v[68:71], v[48:51]\n"
"ds_read_b128 v[2:5], v29 offset:704\n"
"s_waitcnt lgkmcnt(9)\n"
"v_mfma_f32_16x16x32_f16 v[52:55], v[88:91], v[68:71], v[52:55]\n"
"v_mfma_f32_16x16x32_f16 v[56:59], v[80:83], v[68:71], v[56:59]\n"
"ds_read_b64 v[68:69], v30 offset:1024\n"
"v_mfma_f32_16x16x32_f16 v[44:47], v[72:75], v[34:37], v[44:47]\n"
"ds_read_b64 v[72:73], v28 offset:704\n"
"v_mfma_f32_16x16x32_f16 v[48:51], v[88:91], v[34:37], v[48:51]\n"
"ds_read_b64 v[74:75], v28 offset:712\n"
"s_waitcnt lgkmcnt(6)\n"
"v_mfma_f32_16x16x32_f16 v[52:55], v[80:83], v[34:37], v[52:55]\n"
"ds_read_b128 v[88:91], v29 offset:192\n"
"ds_read_b64 v[80:81], v28 offset:192\n"
"v_mfma_f32_16x16x32_f16 v[56:59], v[92:95], v[34:37], v[56:59]\n"
"ds_read_b64 v[82:83], v28 offset:200\n"
"ds_read_b64 v[34:35], v28 offset:1664\n"
"v_mfma_f32_16x16x32_f16 v[56:59], v[64:67], v[76:79], v[56:59]\n"
"ds_read_b64 v[36:37], v28 offset:1672\n"
"ds_read_b128 v[92:95], v29 offset:1664\n"
"v_mfma_f32_16x16x32_f16 v[52:55], v[64:67], v[60:63], v[52:55]\n"
"ds_read_b128 v[76:79], v29 offset:1152\n"
"s_waitcnt lgkmcnt(9)\n"
"v_mfma_f32_16x16x32_f16 v[56:59], v[84:87], v[60:63], v[56:59]\n"
"ds_read_b64 v[60:61], v28 offset:1152\n"
"v_mfma_f32_16x16x32_f16 v[48:51], v[64:67], v[2:5], v[48:51]\n"
"ds_read_b64 v[62:63], v28 offset:1160\n"
"v_mfma_f32_16x16x16_bf16 v[52:55], v[38:39], v[68:69], v[52:55]\n"
"ds_read_b128 v[68:71], v29 offset:640\n"
"s_nop 4\n"
"v_mfma_f32_16x16x32_f16 v[52:55], v[84:87], v[2:5], v[52:55]\n"
"s_waitcnt lgkmcnt(9)\n"
"v_mfma_f32_16x16x32_f16 v[56:59], v[72:75], v[2:5], v[56:59]\n"
"ds_read_b64 v[2:3], v28 offset:640\n"
"v_mfma_f32_16x16x32_f16 v[44:47], v[64:67], v[88:91], v[44:47]\n"
"ds_read_b64 v[4:5], v28 offset:648\n"
"v_mfma_f32_16x16x32_f16 v[48:51], v[84:87], v[88:91], v[48:51]\n"
"ds_read_b128 v[64:67], v29 offset:128\n"
"v_mfma_f32_16x16x32_f16 v[52:55], v[72:75], v[88:91], v[52:55]\n"
"ds_read_b64 v[84:85], v30 offset:1536\n"
"s_waitcnt lgkmcnt(5)\n"
"v_mfma_f32_16x16x32_f16 v[56:59], v[80:83], v[88:91], v[56:59]\n"
"ds_read_b64 v[72:73], v28 offset:128\n"
"ds_read_b64 v[74:75], v28 offset:136\n"
"v_mfma_f32_16x16x32_f16 v[56:59], v[34:37], v[92:95], v[56:59]\n"
"ds_read_b64 v[88:89], v28 offset:1600\n"
"ds_read_b64 v[90:91], v28 offset:1608\n"
"v_mfma_f32_16x16x32_f16 v[52:55], v[34:37], v[76:79], v[52:55]\n"
"ds_read_b128 v[80:83], v29 offset:1600\n"
"v_mfma_f32_16x16x32_f16 v[56:59], v[60:63], v[76:79], v[56:59]\n"
"ds_read_b128 v[92:95], v29 offset:1088\n"
"s_waitcnt lgkmcnt(7)\n"
"v_mfma_f32_16x16x32_f16 v[48:51], v[34:37], v[68:71], v[48:51]\n"
"ds_read_b64 v[76:77], v28 offset:1088\n"
"v_mfma_f32_16x16x32_f16 v[52:55], v[60:63], v[68:71], v[52:55]\n"
"ds_read_b64 v[78:79], v28 offset:1096\n"
"v_mfma_f32_16x16x32_f16 v[56:59], v[2:5], v[68:71], v[56:59]\n"
"ds_read_b128 v[68:71], v29 offset:576\n"
"v_mfma_f32_16x16x32_f16 v[44:47], v[34:37], v[64:67], v[44:47]\n"
"ds_read_b64 v[34:35], v28 offset:576\n"
"s_waitcnt lgkmcnt(8)\n"
"s_nop 0\n"
"v_mfma_f32_16x16x16_bf16 v[56:59], v[38:39], v[84:85], v[56:59]\n"
"ds_read_b64 v[36:37], v28 offset:584\n"
"v_mfma_f32_16x16x32_f16 v[48:51], v[60:63], v[64:67], v[48:51]\n"
"ds_read_b128 v[84:87], v29 offset:64\n"
"ds_read_b64 v[60:61], v28 offset:64\n"
"v_mfma_f32_16x16x32_f16 v[52:55], v[2:5], v[64:67], v[52:55]\n"
"ds_read_b64 v[62:63], v28 offset:72\n"
"ds_read_b64 v[2:3], v28 offset:1536\n"
"v_mfma_f32_16x16x32_f16 v[56:59], v[72:75], v[64:67], v[56:59]\n"
"ds_read_b64 v[4:5], v28 offset:1544\n"
"s_waitcnt lgkmcnt(7)\n"
"v_mfma_f32_16x16x32_f16 v[56:59], v[88:91], v[80:83], v[56:59]\n"
"ds_read_b128 v[64:67], v29 offset:0\n"
"ds_read_b128 v[72:75], v29 offset:512\n"
"v_mfma_f32_16x16x32_f16 v[52:55], v[88:91], v[92:95], v[52:55]\n"
"ds_read_b64 v[80:81], v28 offset:1024\n"
"v_mfma_f32_16x16x32_f16 v[56:59], v[76:79], v[92:95], v[56:59]\n"
"ds_read_b64 v[82:83], v28 offset:1032\n"
"v_mfma_f32_16x16x32_f16 v[48:51], v[88:91], v[68:71], v[48:51]\n"
"ds_read_b128 v[92:95], v29 offset:1024\n"
"s_waitcnt lgkmcnt(9)\n"
"v_mfma_f32_16x16x32_f16 v[52:55], v[76:79], v[68:71], v[52:55]\n"
"v_mfma_f32_16x16x32_f16 v[56:59], v[34:37], v[68:71], v[56:59]\n"
"ds_read_b64 v[68:69], v28 offset:512\n"
"v_mfma_f32_16x16x32_f16 v[44:47], v[88:91], v[84:87], v[44:47]\n"
"ds_read_b64 v[70:71], v28 offset:520\n"
"v_mfma_f32_16x16x32_f16 v[48:51], v[76:79], v[84:87], v[48:51]\n"
"ds_read_b128 v[88:91], v29 offset:1536\n"
"s_waitcnt lgkmcnt(6)\n"
"v_mfma_f32_16x16x32_f16 v[52:55], v[34:37], v[84:87], v[52:55]\n"
"ds_read_b64 v[76:77], v28 offset:0\n"
"v_mfma_f32_16x16x32_f16 v[56:59], v[60:63], v[84:87], v[56:59]\n"
"ds_read_b64 v[78:79], v28 offset:8\n"
"v_mfma_f32_16x16x32_f16 v[44:47], v[2:5], v[64:67], v[44:47]\n"
"v_mfma_f32_16x16x32_f16 v[48:51], v[2:5], v[72:75], v[48:51]\n"
"s_waitcnt lgkmcnt(3)\n"
"v_mfma_f32_16x16x32_f16 v[48:51], v[80:83], v[64:67], v[48:51]\n"
"v_mfma_f32_16x16x32_f16 v[52:55], v[2:5], v[92:95], v[52:55]\n"
"v_mul_f32 v44, v40, v44\n"
"v_mul_f32 v45, v40, v45\n"
"v_mul_f32 v46, v40, v46\n"
"v_mfma_f32_16x16x32_f16 v[52:55], v[80:83], v[72:75], v[52:55]\n"
"v_mul_f32 v47, v40, v47\n"
"v_cvt_pk_bf16_f32 v44, v44, v45\n"
"v_cvt_pk_bf16_f32 v45, v46, v47\n"
"v_mfma_f32_16x16x32_f16 v[52:55], v[68:71], v[64:67], v[52:55]\n"
"v_pk_min_u16 v44, v44, s50\n"
"v_pk_min_u16 v45, v45, s50\n"
"s_waitcnt lgkmcnt(0)\n"
"v_mfma_f32_16x16x32_f16 v[56:59], v[2:5], v[88:91], v[56:59]\n"
"ds_write_b64 v30, v[44:45] offset:0\n"
"v_mfma_f32_16x16x32_f16 v[56:59], v[80:83], v[92:95], v[56:59]\n"
"v_mul_f32 v48, v40, v48\n"
"v_mul_f32 v49, v40, v49\n"
"v_mfma_f32_16x16x32_f16 v[56:59], v[68:71], v[72:75], v[56:59]\n"
"v_mul_f32 v50, v40, v50\n"
"v_mul_f32 v51, v40, v51\n"
"v_mfma_f32_16x16x32_f16 v[56:59], v[76:79], v[64:67], v[56:59]\n"
"v_cvt_pk_bf16_f32 v48, v48, v49\n"
"v_cvt_pk_bf16_f32 v49, v50, v51\n"
"v_pk_min_u16 v48, v48, s50\n"
"v_pk_min_u16 v49, v49, s50\n"
"ds_write_b64 v30, v[48:49] offset:512\n"
"v_mul_f32 v52, v40, v52\n"
"v_mul_f32 v53, v40, v53\n"
"v_mul_f32 v54, v40, v54\n"
"v_mul_f32 v55, v40, v55\n"
"v_cvt_pk_bf16_f32 v52, v52, v53\n"
"v_cvt_pk_bf16_f32 v53, v54, v55\n"
"v_pk_min_u16 v52, v52, s50\n"
"v_pk_min_u16 v53, v53, s50\n"
"ds_write_b64 v30, v[52:53] offset:1024\n"
"v_mul_f32 v56, v40, v56\n"
"v_mul_f32 v57, v40, v57\n"
"v_mul_f32 v58, v40, v58\n"
"v_mul_f32 v59, v40, v59\n"
"v_cvt_pk_bf16_f32 v56, v56, v57\n"
"v_cvt_pk_bf16_f32 v57, v58, v59\n"
"v_pk_min_u16 v56, v56, s50\n"
"v_pk_min_u16 v57, v57, s50\n"
"ds_write_b64 v30, v[56:57] offset:1536\n"
"ds_write_b32 v13, v13 offset:528\n"
"s_mov_b32 s17, 0x100000\n"
".Ltp_291:\n"
"ds_read_b32 v42, v14 offset:528\n"
"s_sub_u32 s17, s17, 1\n"
"s_cbranch_scc1 .Lou_292\n"
"s_waitcnt lgkmcnt(0)\n"
"v_cmp_eq_u32 vcc, 0, v42\n"
"s_cbranch_vccnz .Ltp_291\n"
".Lou_292:\n"
"s_waitcnt lgkmcnt(0)\n"
"ds_read_b64_tr_b16 v[60:61], v31 offset:0\n"
"ds_read_b64_tr_b16 v[62:63], v32 offset:0\n"
"ds_read_b64_tr_b16 v[64:65], v31 offset:1024\n"
"ds_read_b64_tr_b16 v[66:67], v32 offset:1024\n"
"s_add_u32 s47, s46, 0x1400\n"
"s_add_u32 s48, s46, 0xa0000\n"
"s_add_u32 s49, s46, 0xa1400\n"
"s_waitcnt lgkmcnt(3)\n"
"buffer_store_dwordx2 v[60:61], v41, s[12:15], s46 offen\n"
"s_mov_b32 s89, 18\n"
".Ldl_293:\n"
"s_sub_u32 s89, s89, 1\n"
"s_cbranch_scc1 .Lde_294\n"
"s_sleep 1\n"
"s_branch .Ldl_293\n"
".Lde_294:\n"
"s_waitcnt lgkmcnt(2)\n"
"buffer_store_dwordx2 v[62:63], v41, s[12:15], s47 offen\n"
"s_mov_b32 s89, 18\n"
".Ldl_295:\n"
"s_sub_u32 s89, s89, 1\n"
"s_cbranch_scc1 .Lde_296\n"
"s_sleep 1\n"
"s_branch .Ldl_295\n"
".Lde_296:\n"
"s_waitcnt lgkmcnt(1)\n"
"buffer_store_dwordx2 v[64:65], v41, s[12:15], s48 offen\n"
"s_mov_b32 s89, 18\n"
".Ldl_297:\n"
"s_sub_u32 s89, s89, 1\n"
"s_cbranch_scc1 .Lde_298\n"
"s_sleep 1\n"
"s_branch .Ldl_297\n"
".Lde_298:\n"
"s_waitcnt lgkmcnt(0)\n"
"buffer_store_dwordx2 v[66:67], v41, s[12:15], s49 offen\n"
"v_add_u32 v31, 0x16c00, v31\n"
"v_add_u32 v32, 0x16c00, v32\n"
"s_add_u32 s46, s46, 0x140000\n"
"s_add_u32 s47, s47, 0x140000\n"
"s_add_u32 s48, s48, 0x140000\n"
"s_add_u32 s49, s49, 0x140000\n"
"s_lshr_b32 s79, s3, 1\n"
"s_mul_i32 s79, s79, -16\n"
"v_add_u32 v76, s79, v14\n"
"s_mov_b32 s17, 0x100000\n"
".Ltp_299:\n"
"s_sleep 1\n"
"ds_read_b32 v42, v14 offset:576\n"
"s_sub_u32 s17, s17, 1\n"
"s_cbranch_scc1 .Lou_300\n"
"s_waitcnt lgkmcnt(0)\n"
"v_cmp_eq_u32 vcc, 0, v42\n"
"s_cbranch_vccnz .Ltp_299\n"
".Lou_300:\n"
"s_waitcnt lgkmcnt(0)\n"
"ds_read_b64_tr_b16 v[68:69], v31 offset:0\n"
"ds_read_b64_tr_b16 v[70:71], v32 offset:0\n"
"s_waitcnt lgkmcnt(1)\n"
"buffer_store_dwordx2 v[68:69], v41, s[12:15], s46 offen\n"
"s_waitcnt lgkmcnt(0)\n"
"buffer_store_dwordx2 v[70:71], v41, s[12:15], s47 offen\n"
"s_mov_b32 s17, 0x100000\n"
".Ltp_301:\n"
"s_sleep 1\n"
"ds_read_b32 v42, v76 offset:560\n"
"s_sub_u32 s17, s17, 1\n"
"s_cbranch_scc1 .Lou_302\n"
"s_waitcnt lgkmcnt(0)\n"
"v_cmp_eq_u32 vcc, 0, v42\n"
"s_cbranch_vccnz .Ltp_301\n"
".Lou_302:\n"
"s_waitcnt lgkmcnt(0)\n"
"ds_read_b64_tr_b16 v[72:73], v31 offset:1024\n"
"ds_read_b64_tr_b16 v[74:75], v32 offset:1024\n"
"s_waitcnt lgkmcnt(1)\n"
"buffer_store_dwordx2 v[72:73], v41, s[12:15], s48 offen\n"
"s_waitcnt lgkmcnt(0)\n"
"buffer_store_dwordx2 v[74:75], v41, s[12:15], s49 offen\n"
"s_setprio 1\n"
"s_nop 0\n"
".Lnc_276:\n"
"s_add_u32 s31, s2, s3\n"
"s_cmp_lg_u32 s31, 0\n"
"s_cbranch_scc1 .Lfin_303\n"
"v_mov_b32 v32, s16\n"
"s_mov_b64 exec, 1\n"
"buffer_store_dword v32, off, s[4:7], 0 sc1\n"
"s_mov_b64 exec, -1\n"
".Lfin_303:\n"
"s_endpgm\n"
"s_branch .L_xend\n"
".Lsl_7:\n"
"s_nop 7\n"
"s_mov_b32 s17, 0x100000\n"
".Ltp_9:\n"
"ds_read_b32 v90, v14 offset:0\n"
"s_sub_u32 s17, s17, 1\n"
"s_cbranch_scc1 .Lou_10\n"
"s_waitcnt lgkmcnt(0)\n"
"v_cmp_eq_u32 vcc, 0, v90\n"
"s_cbranch_vccnz .Ltp_9\n"
".Lou_10:\n"
"s_waitcnt lgkmcnt(0)\n"
"s_branch .Lbk_8\n"
".Lsl_18:\n"
"s_nop 7\n"
"s_mov_b32 s17, 0x100000\n"
".Ltp_20:\n"
"ds_read_b32 v90, v14 offset:48\n"
"s_sub_u32 s17, s17, 1\n"
"s_cbranch_scc1 .Lou_21\n"
"s_waitcnt lgkmcnt(0)\n"
"v_cmp_eq_u32 vcc, 0, v90\n"
"s_cbranch_vccnz .Ltp_20\n"
".Lou_21:\n"
"s_waitcnt lgkmcnt(0)\n"
"s_branch .Lbk_19\n"
".Lsl_22:\n"
"s_nop 7\n"
"s_mov_b32 s17, 0x100000\n"
".Ltp_24:\n"
"ds_read_b32 v90, v14 offset:64\n"
"s_sub_u32 s17, s17, 1\n"
"s_cbranch_scc1 .Lou_25\n"
"s_waitcnt lgkmcnt(0)\n"
"v_cmp_eq_u32 vcc, 0, v90\n"
"s_cbranch_vccnz .Ltp_24\n"
".Lou_25:\n"
"s_waitcnt lgkmcnt(0)\n"
"s_branch .Lbk_23\n"
".Lsl_26:\n"
"s_nop 7\n"
"s_mov_b32 s17, 0x100000\n"
".Ltp_28:\n"
"ds_read_b32 v90, v14 offset:16\n"
"s_sub_u32 s17, s17, 1\n"
"s_cbranch_scc1 .Lou_29\n"
"s_waitcnt lgkmcnt(0)\n"
"v_cmp_eq_u32 vcc, 0, v90\n"
"s_cbranch_vccnz .Ltp_28\n"
".Lou_29:\n"
"s_waitcnt lgkmcnt(0)\n"
"s_branch .Lbk_27\n"
".Lsl_30:\n"
"s_nop 7\n"
"s_mov_b32 s17, 0x100000\n"
".Ltp_32:\n"
"ds_read_b32 v90, v14 offset:80\n"
"s_sub_u32 s17, s17, 1\n"
"s_cbranch_scc1 .Lou_33\n"
"s_waitcnt lgkmcnt(0)\n"
"v_cmp_eq_u32 vcc, 0, v90\n"
"s_cbranch_vccnz .Ltp_32\n"
".Lou_33:\n"
"s_waitcnt lgkmcnt(0)\n"
"s_branch .Lbk_31\n"
".Lsl_38:\n"
"s_nop 7\n"
"s_mov_b32 s17, 0x100000\n"
".Ltp_40:\n"
"ds_read_b128 v[84:87], v11 offset:0\n"
"ds_read_b32 v91, v12 offset:0\n"
"s_sub_u32 s17, s17, 1\n"
"s_cbranch_scc1 .Lou_41\n"
"s_waitcnt lgkmcnt(0)\n"
"v_max3_u32 v88, v84, v85, v91\n"
"v_max3_u32 v88, v86, v87, v88\n"
"v_cmp_eq_u32 vcc, -1, v88\n"
"s_cbranch_vccnz .Ltp_40\n"
".Lou_41:\n"
"s_waitcnt lgkmcnt(0)\n"
"v_add_f32 v84, v84, v85\n"
"v_add_f32 v86, v86, v87\n"
"v_add_f32 v84, v84, v86\n"
"v_mul_f32 v89, 0x3acccccd, v91\n"
"v_fma_f32 v84, -v91, v89, v84\n"
"v_fmamk_f32 v84, v84, 0x3acccccd, v27\n"
"v_max_f32 v84, v84, v27\n"
"v_rsq_f32 v88, v84\n"
"s_nop 0\n"
"v_mul_f32 v89, -v89, v88\n"
"s_nop 1\n"
"s_branch .Lbk_39\n"
".Lsl_42:\n"
"s_nop 7\n"
"s_mov_b32 s17, 0x100000\n"
".Ltp_44:\n"
"ds_read_b128 v[84:87], v11 offset:320\n"
"ds_read_b32 v91, v12 offset:320\n"
"s_sub_u32 s17, s17, 1\n"
"s_cbranch_scc1 .Lou_45\n"
"s_waitcnt lgkmcnt(0)\n"
"v_max3_u32 v88, v84, v85, v91\n"
"v_max3_u32 v88, v86, v87, v88\n"
"v_cmp_eq_u32 vcc, -1, v88\n"
"s_cbranch_vccnz .Ltp_44\n"
".Lou_45:\n"
"s_waitcnt lgkmcnt(0)\n"
"v_add_f32 v84, v84, v85\n"
"v_add_f32 v86, v86, v87\n"
"v_add_f32 v84, v84, v86\n"
"v_mul_f32 v89, 0x3acccccd, v91\n"
"v_fma_f32 v84, -v91, v89, v84\n"
"v_fmamk_f32 v84, v84, 0x3acccccd, v27\n"
"v_max_f32 v84, v84, v27\n"
"v_rsq_f32 v88, v84\n"
"s_nop 0\n"
"v_mul_f32 v89, -v89, v88\n"
"s_nop 1\n"
"s_branch .Lbk_43\n"
".Lsl_46:\n"
"s_nop 7\n"
"s_mov_b32 s17, 0x100000\n"
".Ltp_48:\n"
"ds_read_b128 v[84:87], v11 offset:640\n"
"ds_read_b32 v91, v12 offset:640\n"
"s_sub_u32 s17, s17, 1\n"
"s_cbranch_scc1 .Lou_49\n"
"s_waitcnt lgkmcnt(0)\n"
"v_max3_u32 v88, v84, v85, v91\n"
"v_max3_u32 v88, v86, v87, v88\n"
"v_cmp_eq_u32 vcc, -1, v88\n"
"s_cbranch_vccnz .Ltp_48\n"
".Lou_49:\n"
"s_waitcnt lgkmcnt(0)\n"
"v_add_f32 v84, v84, v85\n"
"v_add_f32 v86, v86, v87\n"
"v_add_f32 v84, v84, v86\n"
"v_mul_f32 v89, 0x3acccccd, v91\n"
"v_fma_f32 v84, -v91, v89, v84\n"
"v_fmamk_f32 v84, v84, 0x3acccccd, v27\n"
"v_max_f32 v84, v84, v27\n"
"v_rsq_f32 v88, v84\n"
"s_nop 0\n"
"v_mul_f32 v89, -v89, v88\n"
"s_nop 1\n"
"s_branch .Lbk_47\n"
".Lsl_50:\n"
"s_nop 7\n"
"s_mov_b32 s17, 0x100000\n"
".Ltp_52:\n"
"ds_read_b128 v[84:87], v11 offset:960\n"
"ds_read_b32 v91, v12 offset:960\n"
"s_sub_u32 s17, s17, 1\n"
"s_cbranch_scc1 .Lou_53\n"
"s_waitcnt lgkmcnt(0)\n"
"v_max3_u32 v88, v84, v85, v91\n"
"v_max3_u32 v88, v86, v87, v88\n"
"v_cmp_eq_u32 vcc, -1, v88\n"
"s_cbranch_vccnz .Ltp_52\n"
".Lou_53:\n"
"s_waitcnt lgkmcnt(0)\n"
"v_add_f32 v84, v84, v85\n"
"v_add_f32 v86, v86, v87\n"
"v_add_f32 v84, v84, v86\n"
"v_mul_f32 v89, 0x3acccccd, v91\n"
"v_fma_f32 v84, -v91, v89, v84\n"
"v_fmamk_f32 v84, v84, 0x3acccccd, v27\n"
"v_max_f32 v84, v84, v27\n"
"v_rsq_f32 v88, v84\n"
"s_nop 0\n"
"v_mul_f32 v89, -v89, v88\n"
"s_nop 1\n"
"s_branch .Lbk_51\n"
".Lsl_54:\n"
"s_nop 7\n"
"s_mov_b32 s17, 0x100000\n"
".Ltp_56:\n"
"ds_read_b128 v[44:47], v11 offset:1280\n"
"ds_read_b128 v[48:51], v11 offset:1600\n"
"s_sub_u32 s17, s17, 1\n"
"s_cbranch_scc1 .Lou_57\n"
"s_waitcnt lgkmcnt(0)\n"
"v_max3_u32 v52, v44, v45, v51\n"
"v_max3_u32 v52, v46, v47, v52\n"
"v_max3_u32 v52, v48, v49, v52\n"
"v_max3_u32 v52, v50, v51, v52\n"
"v_cmp_eq_u32 vcc, -1, v52\n"
"s_cbranch_vccnz .Ltp_56\n"
".Lou_57:\n"
"s_waitcnt lgkmcnt(0)\n"
"v_add_f32 v44, v44, v45\n"
"v_add_f32 v46, v46, v47\n"
"v_add_f32 v48, v48, v49\n"
"v_add_f32 v50, v50, v51\n"
"v_add_f32 v48, v48, v50\n"
"v_add_f32 v44, v44, v46\n"
"v_mul_f32 v53, 0x3acccccd, v48\n"
"v_fma_f32 v44, -v48, v53, v44\n"
"v_fmamk_f32 v44, v44, 0x3acccccd, v27\n"
"v_max_f32 v44, v44, v27\n"
"v_rsq_f32 v52, v44\n"
"s_nop 0\n"
"v_mul_f32 v53, -v53, v52\n"
"s_nop 1\n"
"s_branch .Lbk_55\n"
".Lsl_50c:\n"
"s_nop 7\n"
"s_mov_b32 s17, 0x100000\n"
".Ltp_52c:\n"
"ds_read_b128 v[84:87], v11 offset:960\n"
"ds_read_b32 v91, v12 offset:960\n"
"s_sub_u32 s17, s17, 1\n"
"s_cbranch_scc1 .Lou_53c\n"
"s_waitcnt lgkmcnt(0)\n"
"v_max3_u32 v88, v84, v85, v91\n"
"v_max3_u32 v88, v86, v87, v88\n"
"v_cmp_eq_u32 vcc, -1, v88\n"
"s_cbranch_vccnz .Ltp_52c\n"
".Lou_53c:\n"
"s_waitcnt lgkmcnt(0)\n"
"v_add_f32 v84, v84, v85\n"
"v_add_f32 v86, v86, v87\n"
"v_add_f32 v84, v84, v86\n"
"v_mul_f32 v89, 0x3acccccd, v91\n"
"v_fma_f32 v84, -v91, v89, v84\n"
"v_fmamk_f32 v84, v84, 0x3acccccd, v27\n"
"v_max_f32 v84, v84, v27\n"
"v_rsq_f32 v88, v84\n"
"s_nop 0\n"
"v_mul_f32 v89, -v89, v88\n"
"s_nop 1\n"
"s_branch .Lbk_51c\n"
".Lsl_54c:\n"
"s_nop 7\n"
"s_mov_b32 s17, 0x100000\n"
".Ltp_56c:\n"
"ds_read_b128 v[44:47], v11 offset:1280\n"
"ds_read_b128 v[48:51], v11 offset:1600\n"
"s_sub_u32 s17, s17, 1\n"
"s_cbranch_scc1 .Lou_57c\n"
"s_waitcnt lgkmcnt(0)\n"
"v_max3_u32 v52, v44, v45, v51\n"
"v_max3_u32 v52, v46, v47, v52\n"
"v_max3_u32 v52, v48, v49, v52\n"
"v_max3_u32 v52, v50, v51, v52\n"
"v_cmp_eq_u32 vcc, -1, v52\n"
"s_cbranch_vccnz .Ltp_56c\n"
".Lou_57c:\n"
"s_waitcnt lgkmcnt(0)\n"
"v_add_f32 v44, v44, v45\n"
"v_add_f32 v46, v46, v47\n"
"v_add_f32 v48, v48, v49\n"
"v_add_f32 v50, v50, v51\n"
"v_add_f32 v48, v48, v50\n"
"v_add_f32 v44, v44, v46\n"
"v_mul_f32 v53, 0x3acccccd, v48\n"
"v_fma_f32 v44, -v48, v53, v44\n"
"v_fmamk_f32 v44, v44, 0x3acccccd, v27\n"
"v_max_f32 v44, v44, v27\n"
"v_rsq_f32 v52, v44\n"
"s_nop 0\n"
"v_mul_f32 v53, -v53, v52\n"
"s_nop 1\n"
"s_branch .Lbk_55c\n"
".Lsl_123:\n"
"s_nop 7\n"
"s_mov_b32 s17, 0x100000\n"
".Ltp_125:\n"
"ds_read_b32 v90, v14 offset:288\n"
"s_sub_u32 s17, s17, 1\n"
"s_cbranch_scc1 .Lou_126\n"
"s_waitcnt lgkmcnt(0)\n"
"v_cmp_eq_u32 vcc, 0, v90\n"
"s_cbranch_vccnz .Ltp_125\n"
".Lou_126:\n"
"s_waitcnt lgkmcnt(0)\n"
"s_branch .Lbk_124\n"
".Lsl_127:\n"
"s_nop 7\n"
"s_mov_b32 s17, 0x100000\n"
".Ltp_129:\n"
"ds_read_b128 v[84:87], v11 offset:1856\n"
"ds_read_b32 v91, v12 offset:1856\n"
"s_sub_u32 s17, s17, 1\n"
"s_cbranch_scc1 .Lou_130\n"
"s_waitcnt lgkmcnt(0)\n"
"v_max3_u32 v88, v84, v85, v91\n"
"v_max3_u32 v88, v86, v87, v88\n"
"v_cmp_eq_u32 vcc, -1, v88\n"
"s_cbranch_vccnz .Ltp_129\n"
".Lou_130:\n"
"s_waitcnt lgkmcnt(0)\n"
"v_add_f32 v84, v84, v85\n"
"v_add_f32 v86, v86, v87\n"
"v_add_f32 v84, v84, v86\n"
"v_mul_f32 v89, 0x3acccccd, v91\n"
"v_fma_f32 v84, -v91, v89, v84\n"
"v_fmamk_f32 v84, v84, 0x3acccccd, v27\n"
"v_add_f32 v92, v92, v84\n"
"v_max_f32 v84, v84, v27\n"
"v_rsq_f32 v88, v84\n"
"s_nop 0\n"
"v_mul_f32 v89, -v89, v88\n"
"s_nop 1\n"
"s_branch .Lbk_128\n"
".Lsl_131:\n"
"s_nop 7\n"
"s_mov_b32 s17, 0x100000\n"
".Ltp_133:\n"
"ds_read_b128 v[84:87], v11 offset:2176\n"
"ds_read_b32 v91, v12 offset:2176\n"
"s_sub_u32 s17, s17, 1\n"
"s_cbranch_scc1 .Lou_134\n"
"s_waitcnt lgkmcnt(0)\n"
"v_max3_u32 v88, v84, v85, v91\n"
"v_max3_u32 v88, v86, v87, v88\n"
"v_cmp_eq_u32 vcc, -1, v88\n"
"s_cbranch_vccnz .Ltp_133\n"
".Lou_134:\n"
"s_waitcnt lgkmcnt(0)\n"
"v_add_f32 v84, v84, v85\n"
"v_add_f32 v86, v86, v87\n"
"v_add_f32 v84, v84, v86\n"
"v_mul_f32 v89, 0x3acccccd, v91\n"
"v_fma_f32 v84, -v91, v89, v84\n"
"v_fmamk_f32 v84, v84, 0x3acccccd, v27\n"
"v_add_f32 v92, v92, v84\n"
"v_max_f32 v84, v84, v27\n"
"v_rsq_f32 v88, v84\n"
"s_nop 0\n"
"v_mul_f32 v89, -v89, v88\n"
"s_nop 1\n"
"s_branch .Lbk_132\n"
".Lsl_135:\n"
"s_nop 7\n"
"s_mov_b32 s17, 0x100000\n"
".Ltp_137:\n"
"ds_read_b128 v[84:87], v11 offset:2496\n"
"ds_read_b32 v91, v12 offset:2496\n"
"s_sub_u32 s17, s17, 1\n"
"s_cbranch_scc1 .Lou_138\n"
"s_waitcnt lgkmcnt(0)\n"
"v_max3_u32 v88, v84, v85, v91\n"
"v_max3_u32 v88, v86, v87, v88\n"
"v_cmp_eq_u32 vcc, -1, v88\n"
"s_cbranch_vccnz .Ltp_137\n"
".Lou_138:\n"
"s_waitcnt lgkmcnt(0)\n"
"v_add_f32 v84, v84, v85\n"
"v_add_f32 v86, v86, v87\n"
"v_add_f32 v84, v84, v86\n"
"v_mul_f32 v89, 0x3acccccd, v91\n"
"v_fma_f32 v84, -v91, v89, v84\n"
"v_fmamk_f32 v84, v84, 0x3acccccd, v27\n"
"v_add_f32 v92, v92, v84\n"
"v_max_f32 v84, v84, v27\n"
"v_rsq_f32 v88, v84\n"
"s_nop 0\n"
"v_mul_f32 v89, -v89, v88\n"
"s_nop 1\n"
"s_branch .Lbk_136\n"
".Lsl_139:\n"
"s_nop 7\n"
"s_mov_b32 s17, 0x100000\n"
".Ltp_141:\n"
"ds_read_b128 v[84:87], v11 offset:2816\n"
"ds_read_b32 v91, v12 offset:2816\n"
"s_sub_u32 s17, s17, 1\n"
"s_cbranch_scc1 .Lou_142\n"
"s_waitcnt lgkmcnt(0)\n"
"v_max3_u32 v88, v84, v85, v91\n"
"v_max3_u32 v88, v86, v87, v88\n"
"v_cmp_eq_u32 vcc, -1, v88\n"
"s_cbranch_vccnz .Ltp_141\n"
".Lou_142:\n"
"s_waitcnt lgkmcnt(0)\n"
"v_add_f32 v84, v84, v85\n"
"v_add_f32 v86, v86, v87\n"
"v_add_f32 v84, v84, v86\n"
"v_mul_f32 v89, 0x3acccccd, v91\n"
"v_fma_f32 v84, -v91, v89, v84\n"
"v_fmamk_f32 v84, v84, 0x3acccccd, v27\n"
"v_add_f32 v92, v92, v84\n"
"v_max_f32 v84, v84, v27\n"
"v_rsq_f32 v88, v84\n"
"s_nop 0\n"
"v_mul_f32 v89, -v89, v88\n"
"s_nop 1\n"
"s_branch .Lbk_140\n"
".Lsl_143:\n"
"s_nop 7\n"
"s_mov_b32 s17, 0x100000\n"
".Ltp_145:\n"
"ds_read_b128 v[44:47], v11 offset:3136\n"
"ds_read_b128 v[48:51], v11 offset:3456\n"
"s_sub_u32 s17, s17, 1\n"
"s_cbranch_scc1 .Lou_146\n"
"s_waitcnt lgkmcnt(0)\n"
"v_max3_u32 v52, v44, v45, v51\n"
"v_max3_u32 v52, v46, v47, v52\n"
"v_max3_u32 v52, v48, v49, v52\n"
"v_max3_u32 v52, v50, v51, v52\n"
"v_cmp_eq_u32 vcc, -1, v52\n"
"s_cbranch_vccnz .Ltp_145\n"
".Lou_146:\n"
"s_waitcnt lgkmcnt(0)\n"
"v_add_f32 v44, v44, v45\n"
"v_add_f32 v46, v46, v47\n"
"v_add_f32 v48, v48, v49\n"
"v_add_f32 v50, v50, v51\n"
"v_add_f32 v48, v48, v50\n"
"v_add_f32 v44, v44, v46\n"
"v_mul_f32 v53, 0x3acccccd, v48\n"
"v_fma_f32 v44, -v48, v53, v44\n"
"v_fmamk_f32 v44, v44, 0x3acccccd, v27\n"
"v_add_f32 v92, v92, v44\n"
"v_max_f32 v44, v44, v27\n"
"v_rsq_f32 v52, v44\n"
"s_nop 0\n"
"v_mul_f32 v53, -v53, v52\n"
"s_nop 1\n"
"s_branch .Lbk_144\n"
".Lsl_139c:\n"
"s_nop 7\n"
"s_mov_b32 s17, 0x100000\n"
".Ltp_141c:\n"
"ds_read_b128 v[84:87], v11 offset:2816\n"
"ds_read_b32 v91, v12 offset:2816\n"
"s_sub_u32 s17, s17, 1\n"
"s_cbranch_scc1 .Lou_142c\n"
"s_waitcnt lgkmcnt(0)\n"
"v_max3_u32 v88, v84, v85, v91\n"
"v_max3_u32 v88, v86, v87, v88\n"
"v_cmp_eq_u32 vcc, -1, v88\n"
"s_cbranch_vccnz .Ltp_141c\n"
".Lou_142c:\n"
"s_waitcnt lgkmcnt(0)\n"
"v_add_f32 v84, v84, v85\n"
"v_add_f32 v86, v86, v87\n"
"v_add_f32 v84, v84, v86\n"
"v_mul_f32 v89, 0x3acccccd, v91\n"
"v_fma_f32 v84, -v91, v89, v84\n"
"v_fmamk_f32 v84, v84, 0x3acccccd, v27\n"
"v_add_f32 v92, v92, v84\n"
"v_max_f32 v84, v84, v27\n"
"v_rsq_f32 v88, v84\n"
"s_nop 0\n"
"v_mul_f32 v89, -v89, v88\n"
"s_nop 1\n"
"s_branch .Lbk_140c\n"
".Lsl_143c:\n"
"s_nop 7\n"
"s_mov_b32 s17, 0x100000\n"
".Ltp_145c:\n"
"ds_read_b128 v[44:47], v11 offset:3136\n"
"ds_read_b128 v[48:51], v11 offset:3456\n"
"s_sub_u32 s17, s17, 1\n"
"s_cbranch_scc1 .Lou_146c\n"
"s_waitcnt lgkmcnt(0)\n"
"v_max3_u32 v52, v44, v45, v51\n"
"v_max3_u32 v52, v46, v47, v52\n"
"v_max3_u32 v52, v48, v49, v52\n"
"v_max3_u32 v52, v50, v51, v52\n"
"v_cmp_eq_u32 vcc, -1, v52\n"
"s_cbranch_vccnz .Ltp_145c\n"
".Lou_146c:\n"
"s_waitcnt lgkmcnt(0)\n"
"v_add_f32 v44, v44, v45\n"
"v_add_f32 v46, v46, v47\n"
"v_add_f32 v48, v48, v49\n"
"v_add_f32 v50, v50, v51\n"
"v_add_f32 v48, v48, v50\n"
"v_add_f32 v44, v44, v46\n"
"v_mul_f32 v53, 0x3acccccd, v48\n"
"v_fma_f32 v44, -v48, v53, v44\n"
"v_fmamk_f32 v44, v44, 0x3acccccd, v27\n"
"v_add_f32 v92, v92, v44\n"
"v_max_f32 v44, v44, v27\n"
"v_rsq_f32 v52, v44\n"
"s_nop 0\n"
"v_mul_f32 v53, -v53, v52\n"
"s_nop 1\n"
"s_branch .Lbk_144c\n"
".Lrd_113:\n"
"s_nop 7\n"
"s_cmp_eq_u32 s54, 0\n"
"s_cbranch_scc1 .Lfi_149\n"
"s_cmp_lg_u32 s3, 3\n"
"s_cbranch_scc1 .Lgo_112c\n"
"s_branch .Lgo_112\n"
".Lfi_149:\n"
"s_mov_b32 s54, 1\n"
"s_sub_u32 s79, s44, 0x5000\n"
"s_add_u32 s78, s29, 0xc28000\n"
"s_max_i32 s46, s79, s78\n"
"s_add_u32 s47, s46, 0x1000\n"
"buffer_load_dwordx4 v[28:31], v1, s[4:7], s46 offen offset:0 sc1\n"
"buffer_load_dwordx4 v[32:35], v1, s[4:7], s46 offen offset:1024 sc1\n"
"buffer_load_dwordx4 v[36:39], v1, s[4:7], s46 offen offset:2048 sc1\n"
"buffer_load_dwordx4 v[40:43], v1, s[4:7], s46 offen offset:3072 sc1\n"
"buffer_load_dwordx4 v[44:47], v1, s[4:7], s47 offen offset:0 sc1\n"
"s_mov_b32 s17, 32768\n"
".Ltp_150:\n"
"s_waitcnt vmcnt(0)\n"
"s_sub_u32 s17, s17, 1\n"
"s_cbranch_scc1 .Lou_151\n"
"v_max_u32 v48, v28, v29\n"
"v_max3_u32 v48, v30, v31, v48\n"
"v_max3_u32 v48, v32, v33, v48\n"
"v_max3_u32 v48, v34, v35, v48\n"
"v_max3_u32 v48, v36, v37, v48\n"
"v_max3_u32 v48, v38, v39, v48\n"
"v_max3_u32 v48, v40, v41, v48\n"
"v_max3_u32 v48, v42, v43, v48\n"
"v_max3_u32 v48, v44, v45, v48\n"
"v_max3_u32 v48, v46, v47, v48\n"
"v_cmp_eq_u32 vcc, -1, v48\n"
"s_cbranch_vccz .Lou_151\n"
"v_max_u32 v48, v30, v31\n"
"v_max3_u32 v48, v28, v29, v48\n"
"v_cmp_eq_u32 vcc, -1, v48\n"
"s_cbranch_vccz .Lsk_152\n"
"s_mov_b64 exec, vcc\n"
"buffer_load_dwordx4 v[28:31], v1, s[4:7], s46 offen offset:0 sc1\n"
"s_mov_b64 exec, -1\n"
".Lsk_152:\n"
"v_max_u32 v48, v34, v35\n"
"v_max3_u32 v48, v32, v33, v48\n"
"v_cmp_eq_u32 vcc, -1, v48\n"
"s_cbranch_vccz .Lsk_153\n"
"s_mov_b64 exec, vcc\n"
"buffer_load_dwordx4 v[32:35], v1, s[4:7], s46 offen offset:1024 sc1\n"
"s_mov_b64 exec, -1\n"
".Lsk_153:\n"
"v_max_u32 v48, v38, v39\n"
"v_max3_u32 v48, v36, v37, v48\n"
"v_cmp_eq_u32 vcc, -1, v48\n"
"s_cbranch_vccz .Lsk_154\n"
"s_mov_b64 exec, vcc\n"
"buffer_load_dwordx4 v[36:39], v1, s[4:7], s46 offen offset:2048 sc1\n"
"s_mov_b64 exec, -1\n"
".Lsk_154:\n"
"v_max_u32 v48, v42, v43\n"
"v_max3_u32 v48, v40, v41, v48\n"
"v_cmp_eq_u32 vcc, -1, v48\n"
"s_cbranch_vccz .Lsk_155\n"
"s_mov_b64 exec, vcc\n"
"buffer_load_dwordx4 v[40:43], v1, s[4:7], s46 offen offset:3072 sc1\n"
"s_mov_b64 exec, -1\n"
".Lsk_155:\n"
"v_max_u32 v48, v46, v47\n"
"v_max3_u32 v48, v44, v45, v48\n"
"v_cmp_eq_u32 vcc, -1, v48\n"
"s_cbranch_vccz .Lsk_156\n"
"s_mov_b64 exec, vcc\n"
"buffer_load_dwordx4 v[44:47], v1, s[4:7], s47 offen offset:0 sc1\n"
"s_mov_b64 exec, -1\n"
".Lsk_156:\n"
"s_branch .Ltp_150\n"
".Lou_151:\n"
"s_waitcnt vmcnt(0)\n"
"s_mov_b32 s46, s44\n"
"s_add_u32 s47, s46, 0x1000\n"
"buffer_load_dwordx4 v[28:31], v1, s[4:7], s46 offen offset:0 sc1\n"
"buffer_load_dwordx4 v[32:35], v1, s[4:7], s46 offen offset:1024 sc1\n"
"buffer_load_dwordx4 v[36:39], v1, s[4:7], s46 offen offset:2048 sc1\n"
"buffer_load_dwordx4 v[40:43], v1, s[4:7], s46 offen offset:3072 sc1\n"
"buffer_load_dwordx4 v[44:47], v1, s[4:7], s47 offen offset:0 sc1\n"
"s_mov_b32 s17, 32768\n"
".Ltp_157:\n"
"s_waitcnt vmcnt(0)\n"
"s_sub_u32 s17, s17, 1\n"
"s_cbranch_scc1 .Lou_158\n"
"v_max_u32 v48, v28, v29\n"
"v_max3_u32 v48, v30, v31, v48\n"
"v_max3_u32 v48, v32, v33, v48\n"
"v_max3_u32 v48, v34, v35, v48\n"
"v_max3_u32 v48, v36, v37, v48\n"
"v_max3_u32 v48, v38, v39, v48\n"
"v_max3_u32 v48, v40, v41, v48\n"
"v_max3_u32 v48, v42, v43, v48\n"
"v_max3_u32 v48, v44, v45, v48\n"
"v_max3_u32 v48, v46, v47, v48\n"
"v_cmp_eq_u32 vcc, -1, v48\n"
"s_cbranch_vccz .Lou_158\n"
"v_max_u32 v48, v30, v31\n"
"v_max3_u32 v48, v28, v29, v48\n"
"v_cmp_eq_u32 vcc, -1, v48\n"
"s_cbranch_vccz .Lsk_159\n"
"s_mov_b64 exec, vcc\n"
"buffer_load_dwordx4 v[28:31], v1, s[4:7], s46 offen offset:0 sc1\n"
"s_mov_b64 exec, -1\n"
".Lsk_159:\n"
"v_max_u32 v48, v34, v35\n"
"v_max3_u32 v48, v32, v33, v48\n"
"v_cmp_eq_u32 vcc, -1, v48\n"
"s_cbranch_vccz .Lsk_160\n"
"s_mov_b64 exec, vcc\n"
"buffer_load_dwordx4 v[32:35], v1, s[4:7], s46 offen offset:1024 sc1\n"
"s_mov_b64 exec, -1\n"
".Lsk_160:\n"
"v_max_u32 v48, v38, v39\n"
"v_max3_u32 v48, v36, v37, v48\n"
"v_cmp_eq_u32 vcc, -1, v48\n"
"s_cbranch_vccz .Lsk_161\n"
"s_mov_b64 exec, vcc\n"
"buffer_load_dwordx4 v[36:39], v1, s[4:7], s46 offen offset:2048 sc1\n"
"s_mov_b64 exec, -1\n"
".Lsk_161:\n"
"v_max_u32 v48, v42, v43\n"
"v_max3_u32 v48, v40, v41, v48\n"
"v_cmp_eq_u32 vcc, -1, v48\n"
"s_cbranch_vccz .Lsk_162\n"
"s_mov_b64 exec, vcc\n"
"buffer_load_dwordx4 v[40:43], v1, s[4:7], s46 offen offset:3072 sc1\n"
"s_mov_b64 exec, -1\n"
".Lsk_162:\n"
"v_max_u32 v48, v46, v47\n"
"v_max3_u32 v48, v44, v45, v48\n"
"v_cmp_eq_u32 vcc, -1, v48\n"
"s_cbranch_vccz .Lsk_163\n"
"s_mov_b64 exec, vcc\n"
"buffer_load_dwordx4 v[44:47], v1, s[4:7], s47 offen offset:0 sc1\n"
"s_mov_b64 exec, -1\n"
".Lsk_163:\n"
"s_branch .Ltp_157\n"
".Lou_158:\n"
"s_waitcnt vmcnt(0)\n"
"s_add_u32 s46, s44, 0x5000\n"
"s_add_u32 s47, s46, 0x1000\n"
"buffer_load_dwordx4 v[28:31], v1, s[4:7], s46 offen offset:0 sc1\n"
"buffer_load_dwordx4 v[32:35], v1, s[4:7], s46 offen offset:1024 sc1\n"
"buffer_load_dwordx4 v[36:39], v1, s[4:7], s46 offen offset:2048 sc1\n"
"buffer_load_dwordx4 v[40:43], v1, s[4:7], s46 offen offset:3072 sc1\n"
"buffer_load_dwordx4 v[44:47], v1, s[4:7], s47 offen offset:0 sc1\n"
"s_mov_b32 s17, 32768\n"
".Ltp_164:\n"
"s_waitcnt vmcnt(0)\n"
"s_sub_u32 s17, s17, 1\n"
"s_cbranch_scc1 .Lou_165\n"
"v_max_u32 v48, v28, v29\n"
"v_max3_u32 v48, v30, v31, v48\n"
"v_max3_u32 v48, v32, v33, v48\n"
"v_max3_u32 v48, v34, v35, v48\n"
"v_max3_u32 v48, v36, v37, v48\n"
"v_max3_u32 v48, v38, v39, v48\n"
"v_max3_u32 v48, v40, v41, v48\n"
"v_max3_u32 v48, v42, v43, v48\n"
"v_max3_u32 v48, v44, v45, v48\n"
"v_max3_u32 v48, v46, v47, v48\n"
"v_cmp_eq_u32 vcc, -1, v48\n"
"s_cbranch_vccz .Lou_165\n"
"v_max_u32 v48, v30, v31\n"
"v_max3_u32 v48, v28, v29, v48\n"
"v_cmp_eq_u32 vcc, -1, v48\n"
"s_cbranch_vccz .Lsk_166\n"
"s_mov_b64 exec, vcc\n"
"buffer_load_dwordx4 v[28:31], v1, s[4:7], s46 offen offset:0 sc1\n"
"s_mov_b64 exec, -1\n"
".Lsk_166:\n"
"v_max_u32 v48, v34, v35\n"
"v_max3_u32 v48, v32, v33, v48\n"
"v_cmp_eq_u32 vcc, -1, v48\n"
"s_cbranch_vccz .Lsk_167\n"
"s_mov_b64 exec, vcc\n"
"buffer_load_dwordx4 v[32:35], v1, s[4:7], s46 offen offset:1024 sc1\n"
"s_mov_b64 exec, -1\n"
".Lsk_167:\n"
"v_max_u32 v48, v38, v39\n"
"v_max3_u32 v48, v36, v37, v48\n"
"v_cmp_eq_u32 vcc, -1, v48\n"
"s_cbranch_vccz .Lsk_168\n"
"s_mov_b64 exec, vcc\n"
"buffer_load_dwordx4 v[36:39], v1, s[4:7], s46 offen offset:2048 sc1\n"
"s_mov_b64 exec, -1\n"
".Lsk_168:\n"
"v_max_u32 v48, v42, v43\n"
"v_max3_u32 v48, v40, v41, v48\n"
"v_cmp_eq_u32 vcc, -1, v48\n"
"s_cbranch_vccz .Lsk_169\n"
"s_mov_b64 exec, vcc\n"
"buffer_load_dwordx4 v[40:43], v1, s[4:7], s46 offen offset:3072 sc1\n"
"s_mov_b64 exec, -1\n"
".Lsk_169:\n"
"v_max_u32 v48, v46, v47\n"
"v_max3_u32 v48, v44, v45, v48\n"
"v_cmp_eq_u32 vcc, -1, v48\n"
"s_cbranch_vccz .Lsk_170\n"
"s_mov_b64 exec, vcc\n"
"buffer_load_dwordx4 v[44:47], v1, s[4:7], s47 offen offset:0 sc1\n"
"s_mov_b64 exec, -1\n"
".Lsk_170:\n"
"s_branch .Ltp_164\n"
".Lou_165:\n"
"s_waitcnt vmcnt(0)\n"
"s_add_u32 s46, s44, 0xa000\n"
"s_add_u32 s47, s46, 0x1000\n"
"buffer_load_dwordx4 v[28:31], v1, s[4:7], s46 offen offset:0 sc1\n"
"buffer_load_dwordx4 v[32:35], v1, s[4:7], s46 offen offset:1024 sc1\n"
"buffer_load_dwordx4 v[36:39], v1, s[4:7], s46 offen offset:2048 sc1\n"
"buffer_load_dwordx4 v[40:43], v1, s[4:7], s46 offen offset:3072 sc1\n"
"buffer_load_dwordx4 v[44:47], v1, s[4:7], s47 offen offset:0 sc1\n"
"s_mov_b32 s17, 32768\n"
".Ltp_171:\n"
"s_waitcnt vmcnt(0)\n"
"s_sub_u32 s17, s17, 1\n"
"s_cbranch_scc1 .Lou_172\n"
"v_max_u32 v48, v28, v29\n"
"v_max3_u32 v48, v30, v31, v48\n"
"v_max3_u32 v48, v32, v33, v48\n"
"v_max3_u32 v48, v34, v35, v48\n"
"v_max3_u32 v48, v36, v37, v48\n"
"v_max3_u32 v48, v38, v39, v48\n"
"v_max3_u32 v48, v40, v41, v48\n"
"v_max3_u32 v48, v42, v43, v48\n"
"v_max3_u32 v48, v44, v45, v48\n"
"v_max3_u32 v48, v46, v47, v48\n"
"v_cmp_eq_u32 vcc, -1, v48\n"
"s_cbranch_vccz .Lou_172\n"
"v_max_u32 v48, v30, v31\n"
"v_max3_u32 v48, v28, v29, v48\n"
"v_cmp_eq_u32 vcc, -1, v48\n"
"s_cbranch_vccz .Lsk_173\n"
"s_mov_b64 exec, vcc\n"
"buffer_load_dwordx4 v[28:31], v1, s[4:7], s46 offen offset:0 sc1\n"
"s_mov_b64 exec, -1\n"
".Lsk_173:\n"
"v_max_u32 v48, v34, v35\n"
"v_max3_u32 v48, v32, v33, v48\n"
"v_cmp_eq_u32 vcc, -1, v48\n"
"s_cbranch_vccz .Lsk_174\n"
"s_mov_b64 exec, vcc\n"
"buffer_load_dwordx4 v[32:35], v1, s[4:7], s46 offen offset:1024 sc1\n"
"s_mov_b64 exec, -1\n"
".Lsk_174:\n"
"v_max_u32 v48, v38, v39\n"
"v_max3_u32 v48, v36, v37, v48\n"
"v_cmp_eq_u32 vcc, -1, v48\n"
"s_cbranch_vccz .Lsk_175\n"
"s_mov_b64 exec, vcc\n"
"buffer_load_dwordx4 v[36:39], v1, s[4:7], s46 offen offset:2048 sc1\n"
"s_mov_b64 exec, -1\n"
".Lsk_175:\n"
"v_max_u32 v48, v42, v43\n"
"v_max3_u32 v48, v40, v41, v48\n"
"v_cmp_eq_u32 vcc, -1, v48\n"
"s_cbranch_vccz .Lsk_176\n"
"s_mov_b64 exec, vcc\n"
"buffer_load_dwordx4 v[40:43], v1, s[4:7], s46 offen offset:3072 sc1\n"
"s_mov_b64 exec, -1\n"
".Lsk_176:\n"
"v_max_u32 v48, v46, v47\n"
"v_max3_u32 v48, v44, v45, v48\n"
"v_cmp_eq_u32 vcc, -1, v48\n"
"s_cbranch_vccz .Lsk_177\n"
"s_mov_b64 exec, vcc\n"
"buffer_load_dwordx4 v[44:47], v1, s[4:7], s47 offen offset:0 sc1\n"
"s_mov_b64 exec, -1\n"
".Lsk_177:\n"
"s_branch .Ltp_171\n"
".Lou_172:\n"
"s_waitcnt vmcnt(0)\n"
"s_add_u32 s46, s44, 0xf000\n"
"s_add_u32 s47, s46, 0x1000\n"
"buffer_load_dwordx4 v[28:31], v1, s[4:7], s46 offen offset:0 sc1\n"
"buffer_load_dwordx4 v[32:35], v1, s[4:7], s46 offen offset:1024 sc1\n"
"buffer_load_dwordx4 v[36:39], v1, s[4:7], s46 offen offset:2048 sc1\n"
"buffer_load_dwordx4 v[40:43], v1, s[4:7], s46 offen offset:3072 sc1\n"
"buffer_load_dwordx4 v[44:47], v1, s[4:7], s47 offen offset:0 sc1\n"
"s_mov_b32 s17, 32768\n"
".Ltp_178:\n"
"s_waitcnt vmcnt(0)\n"
"s_sub_u32 s17, s17, 1\n"
"s_cbranch_scc1 .Lou_179\n"
"v_max_u32 v48, v28, v29\n"
"v_max3_u32 v48, v30, v31, v48\n"
"v_max3_u32 v48, v32, v33, v48\n"
"v_max3_u32 v48, v34, v35, v48\n"
"v_max3_u32 v48, v36, v37, v48\n"
"v_max3_u32 v48, v38, v39, v48\n"
"v_max3_u32 v48, v40, v41, v48\n"
"v_max3_u32 v48, v42, v43, v48\n"
"v_max3_u32 v48, v44, v45, v48\n"
"v_max3_u32 v48, v46, v47, v48\n"
"v_cmp_eq_u32 vcc, -1, v48\n"
"s_cbranch_vccz .Lou_179\n"
"v_max_u32 v48, v30, v31\n"
"v_max3_u32 v48, v28, v29, v48\n"
"v_cmp_eq_u32 vcc, -1, v48\n"
"s_cbranch_vccz .Lsk_180\n"
"s_mov_b64 exec, vcc\n"
"buffer_load_dwordx4 v[28:31], v1, s[4:7], s46 offen offset:0 sc1\n"
"s_mov_b64 exec, -1\n"
".Lsk_180:\n"
"v_max_u32 v48, v34, v35\n"
"v_max3_u32 v48, v32, v33, v48\n"
"v_cmp_eq_u32 vcc, -1, v48\n"
"s_cbranch_vccz .Lsk_181\n"
"s_mov_b64 exec, vcc\n"
"buffer_load_dwordx4 v[32:35], v1, s[4:7], s46 offen offset:1024 sc1\n"
"s_mov_b64 exec, -1\n"
".Lsk_181:\n"
"v_max_u32 v48, v38, v39\n"
"v_max3_u32 v48, v36, v37, v48\n"
"v_cmp_eq_u32 vcc, -1, v48\n"
"s_cbranch_vccz .Lsk_182\n"
"s_mov_b64 exec, vcc\n"
"buffer_load_dwordx4 v[36:39], v1, s[4:7], s46 offen offset:2048 sc1\n"
"s_mov_b64 exec, -1\n"
".Lsk_182:\n"
"v_max_u32 v48, v42, v43\n"
"v_max3_u32 v48, v40, v41, v48\n"
"v_cmp_eq_u32 vcc, -1, v48\n"
"s_cbranch_vccz .Lsk_183\n"
"s_mov_b64 exec, vcc\n"
"buffer_load_dwordx4 v[40:43], v1, s[4:7], s46 offen offset:3072 sc1\n"
"s_mov_b64 exec, -1\n"
".Lsk_183:\n"
"v_max_u32 v48, v46, v47\n"
"v_max3_u32 v48, v44, v45, v48\n"
"v_cmp_eq_u32 vcc, -1, v48\n"
"s_cbranch_vccz .Lsk_184\n"
"s_mov_b64 exec, vcc\n"
"buffer_load_dwordx4 v[44:47], v1, s[4:7], s47 offen offset:0 sc1\n"
"s_mov_b64 exec, -1\n"
".Lsk_184:\n"
"s_branch .Ltp_178\n"
".Lou_179:\n"
"s_waitcnt vmcnt(0)\n"
"s_mov_b32 s17, 0x100000\n"
".Ltp_185:\n"
"ds_read_b32 v90, v14 offset:320\n"
"s_sub_u32 s17, s17, 1\n"
"s_cbranch_scc1 .Lou_186\n"
"s_waitcnt lgkmcnt(0)\n"
"v_cmp_eq_u32 vcc, 0, v90\n"
"s_cbranch_vccnz .Ltp_185\n"
".Lou_186:\n"
"s_waitcnt lgkmcnt(0)\n"
"ds_write_b32 v13, v13 offset:336\n"
"s_mov_b32 s17, 0x100000\n"
".Ltp_187:\n"
"ds_read_b32 v90, v14 offset:336\n"
"s_sub_u32 s17, s17, 1\n"
"s_cbranch_scc1 .Lou_188\n"
"s_waitcnt lgkmcnt(0)\n"
"v_cmp_eq_u32 vcc, 0, v90\n"
"s_cbranch_vccnz .Ltp_187\n"
".Lou_188:\n"
"s_waitcnt lgkmcnt(0)\n"
"v_mov_b32 v90, 0\n"
"ds_write_b32 v13, v90 offset:272\n"
"ds_write_b32 v13, v90 offset:288\n"
"v_mov_b32 v90, -1\n"
"ds_write_b32 v9, v90 offset:1856\n"
"ds_write_b32 v9, v90 offset:2176\n"
"ds_write_b32 v9, v90 offset:2496\n"
"ds_write_b32 v9, v90 offset:2816\n"
"ds_write_b32 v9, v90 offset:3136\n"
"ds_write_b32 v10, v90 offset:3456\n"
"ds_write_b32 v13, v13 offset:352\n"
"s_mov_b32 s17, 0x100000\n"
".Ltp_189:\n"
"ds_read_b32 v90, v14 offset:352\n"
"s_sub_u32 s17, s17, 1\n"
"s_cbranch_scc1 .Lou_190\n"
"s_waitcnt lgkmcnt(0)\n"
"v_cmp_eq_u32 vcc, 0, v90\n"
"s_cbranch_vccnz .Ltp_189\n"
".Lou_190:\n"
"s_waitcnt lgkmcnt(0)\n"
"s_branch .Lag_111\n"
".Lfx_196:\n"
"s_nop 7\n"
"s_add_u32 m0, s29, 0xa000\n"
"s_add_u32 s31, s44, 0x140000\n"
"buffer_load_dwordx4 v1, s[4:7], s31 offen offset:0 sc1 lds\n"
"buffer_load_dwordx4 v1, s[4:7], s31 offen offset:1024 sc1 lds\n"
"buffer_load_dwordx4 v1, s[4:7], s31 offen offset:2048 sc1 lds\n"
"buffer_load_dwordx4 v1, s[4:7], s31 offen offset:3072 sc1 lds\n"
"s_add_u32 m0, m0, 1024\n"
"s_add_u32 s31, s31, 1024\n"
"buffer_load_dwordx4 v1, s[4:7], s31 offen offset:3072 sc1 lds\n"
"s_add_u32 m0, s29, 0xf000\n"
"s_add_u32 s31, s44, 0x145000\n"
"buffer_load_dwordx4 v1, s[4:7], s31 offen offset:0 sc1 lds\n"
"buffer_load_dwordx4 v1, s[4:7], s31 offen offset:1024 sc1 lds\n"
"buffer_load_dwordx4 v1, s[4:7], s31 offen offset:2048 sc1 lds\n"
"buffer_load_dwordx4 v1, s[4:7], s31 offen offset:3072 sc1 lds\n"
"s_add_u32 m0, m0, 1024\n"
"s_add_u32 s31, s31, 1024\n"
"buffer_load_dwordx4 v1, s[4:7], s31 offen offset:3072 sc1 lds\n"
"s_waitcnt vmcnt(0)\n"
"ds_write_b32 v13, v13 offset:384\n"
"s_mov_b32 s17, 0x100000\n"
".Ltp_198:\n"
"ds_read_b32 v90, v14 offset:384\n"
"s_sub_u32 s17, s17, 1\n"
"s_cbranch_scc1 .Lou_199\n"
"s_waitcnt lgkmcnt(0)\n"
"v_cmp_eq_u32 vcc, 0, v90\n"
"s_cbranch_vccnz .Ltp_198\n"
".Lou_199:\n"
"s_waitcnt lgkmcnt(0)\n"
"v_mov_b32 v90, 0\n"
"s_branch .Lbk_197\n"
".Lsl_200:\n"
"s_nop 7\n"
"s_mov_b32 s17, 0x100000\n"
".Ltp_202:\n"
"ds_read_b32 v90, v14 offset:400\n"
"s_sub_u32 s17, s17, 1\n"
"s_cbranch_scc1 .Lou_203\n"
"s_waitcnt lgkmcnt(0)\n"
"v_cmp_eq_u32 vcc, 0, v90\n"
"s_cbranch_vccnz .Ltp_202\n"
".Lou_203:\n"
"s_waitcnt lgkmcnt(0)\n"
"s_branch .Lbk_201\n"
".Lsl_204:\n"
"s_nop 7\n"
"s_mov_b32 s17, 0x100000\n"
".Ltp_206:\n"
"ds_read_b32 v90, v14 offset:416\n"
"s_sub_u32 s17, s17, 1\n"
"s_cbranch_scc1 .Lou_207\n"
"s_waitcnt lgkmcnt(0)\n"
"v_cmp_eq_u32 vcc, 0, v90\n"
"s_cbranch_vccnz .Ltp_206\n"
".Lou_207:\n"
"s_waitcnt lgkmcnt(0)\n"
"s_branch .Lbk_205\n"
".Lsl_208:\n"
"s_nop 7\n"
"s_mov_b32 s17, 0x100000\n"
".Ltp_210:\n"
"ds_read_b128 v[84:87], v11 offset:3712\n"
"ds_read_b32 v91, v12 offset:3712\n"
"s_sub_u32 s17, s17, 1\n"
"s_cbranch_scc1 .Lou_211\n"
"s_waitcnt lgkmcnt(0)\n"
"v_max3_u32 v88, v84, v85, v91\n"
"v_max3_u32 v88, v86, v87, v88\n"
"v_cmp_eq_u32 vcc, -1, v88\n"
"s_cbranch_vccnz .Ltp_210\n"
".Lou_211:\n"
"s_waitcnt lgkmcnt(0)\n"
"v_add_f32 v84, v84, v85\n"
"v_add_f32 v86, v86, v87\n"
"v_add_f32 v84, v84, v86\n"
"v_mul_f32 v89, 0x3acccccd, v91\n"
"v_fma_f32 v84, -v91, v89, v84\n"
"v_fmamk_f32 v84, v84, 0x3acccccd, v27\n"
"v_add_f32 v92, v92, v84\n"
"v_max_f32 v84, v84, v27\n"
"v_rsq_f32 v88, v84\n"
"s_nop 0\n"
"v_mul_f32 v89, -v89, v88\n"
"s_nop 1\n"
"s_branch .Lbk_209\n"
".Lsl_212:\n"
"s_nop 7\n"
"s_mov_b32 s17, 0x100000\n"
".Ltp_214:\n"
"ds_read_b128 v[84:87], v11 offset:4032\n"
"ds_read_b32 v91, v12 offset:4032\n"
"s_sub_u32 s17, s17, 1\n"
"s_cbranch_scc1 .Lou_215\n"
"s_waitcnt lgkmcnt(0)\n"
"v_max3_u32 v88, v84, v85, v91\n"
"v_max3_u32 v88, v86, v87, v88\n"
"v_cmp_eq_u32 vcc, -1, v88\n"
"s_cbranch_vccnz .Ltp_214\n"
".Lou_215:\n"
"s_waitcnt lgkmcnt(0)\n"
"v_add_f32 v84, v84, v85\n"
"v_add_f32 v86, v86, v87\n"
"v_add_f32 v84, v84, v86\n"
"v_mul_f32 v89, 0x3acccccd, v91\n"
"v_fma_f32 v84, -v91, v89, v84\n"
"v_fmamk_f32 v84, v84, 0x3acccccd, v27\n"
"v_add_f32 v92, v92, v84\n"
"v_max_f32 v84, v84, v27\n"
"v_rsq_f32 v88, v84\n"
"s_nop 0\n"
"v_mul_f32 v89, -v89, v88\n"
"s_nop 1\n"
"s_branch .Lbk_213\n"
".Lsl_216:\n"
"s_nop 7\n"
"s_mov_b32 s17, 0x100000\n"
".Ltp_218:\n"
"ds_read_b128 v[84:87], v11 offset:4352\n"
"ds_read_b32 v91, v12 offset:4352\n"
"s_sub_u32 s17, s17, 1\n"
"s_cbranch_scc1 .Lou_219\n"
"s_waitcnt lgkmcnt(0)\n"
"v_max3_u32 v88, v84, v85, v91\n"
"v_max3_u32 v88, v86, v87, v88\n"
"v_cmp_eq_u32 vcc, -1, v88\n"
"s_cbranch_vccnz .Ltp_218\n"
".Lou_219:\n"
"s_waitcnt lgkmcnt(0)\n"
"v_add_f32 v84, v84, v85\n"
"v_add_f32 v86, v86, v87\n"
"v_add_f32 v84, v84, v86\n"
"v_mul_f32 v89, 0x3acccccd, v91\n"
"v_fma_f32 v84, -v91, v89, v84\n"
"v_fmamk_f32 v84, v84, 0x3acccccd, v27\n"
"v_add_f32 v92, v92, v84\n"
"v_max_f32 v84, v84, v27\n"
"v_rsq_f32 v88, v84\n"
"s_nop 0\n"
"v_mul_f32 v89, -v89, v88\n"
"s_nop 1\n"
"s_branch .Lbk_217\n"
".Lsl_220:\n"
"s_nop 7\n"
"s_mov_b32 s17, 0x100000\n"
".Ltp_222:\n"
"ds_read_b128 v[84:87], v11 offset:4672\n"
"ds_read_b32 v91, v12 offset:4672\n"
"s_sub_u32 s17, s17, 1\n"
"s_cbranch_scc1 .Lou_223\n"
"s_waitcnt lgkmcnt(0)\n"
"v_max3_u32 v88, v84, v85, v91\n"
"v_max3_u32 v88, v86, v87, v88\n"
"v_cmp_eq_u32 vcc, -1, v88\n"
"s_cbranch_vccnz .Ltp_222\n"
".Lou_223:\n"
"s_waitcnt lgkmcnt(0)\n"
"v_add_f32 v84, v84, v85\n"
"v_add_f32 v86, v86, v87\n"
"v_add_f32 v84, v84, v86\n"
"v_mul_f32 v89, 0x3acccccd, v91\n"
"v_fma_f32 v84, -v91, v89, v84\n"
"v_fmamk_f32 v84, v84, 0x3acccccd, v27\n"
"v_add_f32 v92, v92, v84\n"
"v_max_f32 v84, v84, v27\n"
"v_rsq_f32 v88, v84\n"
"s_nop 0\n"
"v_mul_f32 v89, -v89, v88\n"
"s_nop 1\n"
"s_branch .Lbk_221\n"
".Lsl_224:\n"
"s_nop 7\n"
"s_mov_b32 s17, 0x100000\n"
".Ltp_226:\n"
"ds_read_b128 v[44:47], v11 offset:4992\n"
"ds_read_b128 v[48:51], v11 offset:5312\n"
"s_sub_u32 s17, s17, 1\n"
"s_cbranch_scc1 .Lou_227\n"
"s_waitcnt lgkmcnt(0)\n"
"v_max3_u32 v52, v44, v45, v51\n"
"v_max3_u32 v52, v46, v47, v52\n"
"v_max3_u32 v52, v48, v49, v52\n"
"v_max3_u32 v52, v50, v51, v52\n"
"v_cmp_eq_u32 vcc, -1, v52\n"
"s_cbranch_vccnz .Ltp_226\n"
".Lou_227:\n"
"s_waitcnt lgkmcnt(0)\n"
"v_add_f32 v44, v44, v45\n"
"v_add_f32 v46, v46, v47\n"
"v_add_f32 v48, v48, v49\n"
"v_add_f32 v50, v50, v51\n"
"v_add_f32 v48, v48, v50\n"
"v_add_f32 v44, v44, v46\n"
"v_mul_f32 v53, 0x3acccccd, v48\n"
"v_fma_f32 v44, -v48, v53, v44\n"
"v_fmamk_f32 v44, v44, 0x3acccccd, v27\n"
"v_add_f32 v92, v92, v44\n"
"v_max_f32 v44, v44, v27\n"
"v_rsq_f32 v52, v44\n"
"s_nop 0\n"
"v_mul_f32 v53, -v53, v52\n"
"s_nop 1\n"
"s_branch .Lbk_225\n"
".Lsl_220c:\n"
"s_nop 7\n"
"s_mov_b32 s17, 0x100000\n"
".Ltp_222c:\n"
"ds_read_b128 v[84:87], v11 offset:4672\n"
"ds_read_b32 v91, v12 offset:4672\n"
"s_sub_u32 s17, s17, 1\n"
"s_cbranch_scc1 .Lou_223c\n"
"s_waitcnt lgkmcnt(0)\n"
"v_max3_u32 v88, v84, v85, v91\n"
"v_max3_u32 v88, v86, v87, v88\n"
"v_cmp_eq_u32 vcc, -1, v88\n"
"s_cbranch_vccnz .Ltp_222c\n"
".Lou_223c:\n"
"s_waitcnt lgkmcnt(0)\n"
"v_add_f32 v84, v84, v85\n"
"v_add_f32 v86, v86, v87\n"
"v_add_f32 v84, v84, v86\n"
"v_mul_f32 v89, 0x3acccccd, v91\n"
"v_fma_f32 v84, -v91, v89, v84\n"
"v_fmamk_f32 v84, v84, 0x3acccccd, v27\n"
"v_add_f32 v92, v92, v84\n"
"v_max_f32 v84, v84, v27\n"
"v_rsq_f32 v88, v84\n"
"s_nop 0\n"
"v_mul_f32 v89, -v89, v88\n"
"s_nop 1\n"
"s_branch .Lbk_221c\n"
".Lsl_224c:\n"
"s_nop 7\n"
"s_mov_b32 s17, 0x100000\n"
".Ltp_226c:\n"
"ds_read_b128 v[44:47], v11 offset:4992\n"
"ds_read_b128 v[48:51], v11 offset:5312\n"
"s_sub_u32 s17, s17, 1\n"
"s_cbranch_scc1 .Lou_227c\n"
"s_waitcnt lgkmcnt(0)\n"
"v_max3_u32 v52, v44, v45, v51\n"
"v_max3_u32 v52, v46, v47, v52\n"
"v_max3_u32 v52, v48, v49, v52\n"
"v_max3_u32 v52, v50, v51, v52\n"
"v_cmp_eq_u32 vcc, -1, v52\n"
"s_cbranch_vccnz .Ltp_226c\n"
".Lou_227c:\n"
"s_waitcnt lgkmcnt(0)\n"
"v_add_f32 v44, v44, v45\n"
"v_add_f32 v46, v46, v47\n"
"v_add_f32 v48, v48, v49\n"
"v_add_f32 v50, v50, v51\n"
"v_add_f32 v48, v48, v50\n"
"v_add_f32 v44, v44, v46\n"
"v_mul_f32 v53, 0x3acccccd, v48\n"
"v_fma_f32 v44, -v48, v53, v44\n"
"v_fmamk_f32 v44, v44, 0x3acccccd, v27\n"
"v_add_f32 v92, v92, v44\n"
"v_max_f32 v44, v44, v27\n"
"v_rsq_f32 v52, v44\n"
"s_nop 0\n"
"v_mul_f32 v53, -v53, v52\n"
"s_nop 1\n"
"s_branch .Lbk_225c\n"
".Lsl_230:\n"
"s_nop 7\n"
"s_mov_b32 s17, 0x100000\n"
".Ltp_232:\n"
"ds_read_b32 v90, v14 offset:320\n"
"s_sub_u32 s17, s17, 1\n"
"s_cbranch_scc1 .Lou_233\n"
"s_waitcnt lgkmcnt(0)\n"
"v_cmp_eq_u32 vcc, 0, v90\n"
"s_cbranch_vccnz .Ltp_232\n"
".Lou_233:\n"
"ds_read_b64 v[56:57], v3 offset:40960\n"
"ds_read_b64 v[58:59], v3 offset:41216\n"
"ds_read_b64 v[60:61], v3 offset:41984\n"
"ds_read_b64 v[62:63], v3 offset:42240\n"
"ds_read_b64 v[64:65], v3 offset:43008\n"
"ds_read_b64 v[66:67], v3 offset:43264\n"
"ds_read_b64 v[68:69], v3 offset:44032\n"
"ds_read_b64 v[70:71], v3 offset:44288\n"
"ds_read_b64 v[72:73], v3 offset:45056\n"
"ds_read_b64 v[74:75], v3 offset:45312\n"
"s_waitcnt lgkmcnt(0)\n"
"s_branch .Lbk_231\n"
".Lrd_195:\n"
"s_nop 7\n"
"s_cmp_eq_u32 s54, 0\n"
"s_cbranch_scc1 .Lfi_234\n"
"s_cmp_lg_u32 s3, 3\n"
"s_cbranch_scc1 .Lgo_194c\n"
"s_branch .Lgo_194\n"
".Lfi_234:\n"
"s_mov_b32 s54, 1\n"
"s_add_u32 s46, s44, 0x13b000\n"
"s_add_u32 s47, s46, 0x1000\n"
"buffer_load_dwordx4 v[28:31], v1, s[4:7], s46 offen offset:0 sc1\n"
"buffer_load_dwordx4 v[32:35], v1, s[4:7], s46 offen offset:1024 sc1\n"
"buffer_load_dwordx4 v[36:39], v1, s[4:7], s46 offen offset:2048 sc1\n"
"buffer_load_dwordx4 v[40:43], v1, s[4:7], s46 offen offset:3072 sc1\n"
"buffer_load_dwordx4 v[44:47], v1, s[4:7], s47 offen offset:0 sc1\n"
"s_mov_b32 s17, 32768\n"
".Ltp_235:\n"
"s_waitcnt vmcnt(0)\n"
"s_sub_u32 s17, s17, 1\n"
"s_cbranch_scc1 .Lou_236\n"
"v_max_u32 v48, v28, v29\n"
"v_max3_u32 v48, v30, v31, v48\n"
"v_max3_u32 v48, v32, v33, v48\n"
"v_max3_u32 v48, v34, v35, v48\n"
"v_max3_u32 v48, v36, v37, v48\n"
"v_max3_u32 v48, v38, v39, v48\n"
"v_max3_u32 v48, v40, v41, v48\n"
"v_max3_u32 v48, v42, v43, v48\n"
"v_max3_u32 v48, v44, v45, v48\n"
"v_max3_u32 v48, v46, v47, v48\n"
"v_cmp_eq_u32 vcc, -1, v48\n"
"s_cbranch_vccz .Lou_236\n"
"v_max_u32 v48, v30, v31\n"
"v_max3_u32 v48, v28, v29, v48\n"
"v_cmp_eq_u32 vcc, -1, v48\n"
"s_cbranch_vccz .Lsk_237\n"
"s_mov_b64 exec, vcc\n"
"buffer_load_dwordx4 v[28:31], v1, s[4:7], s46 offen offset:0 sc1\n"
"s_mov_b64 exec, -1\n"
".Lsk_237:\n"
"v_max_u32 v48, v34, v35\n"
"v_max3_u32 v48, v32, v33, v48\n"
"v_cmp_eq_u32 vcc, -1, v48\n"
"s_cbranch_vccz .Lsk_238\n"
"s_mov_b64 exec, vcc\n"
"buffer_load_dwordx4 v[32:35], v1, s[4:7], s46 offen offset:1024 sc1\n"
"s_mov_b64 exec, -1\n"
".Lsk_238:\n"
"v_max_u32 v48, v38, v39\n"
"v_max3_u32 v48, v36, v37, v48\n"
"v_cmp_eq_u32 vcc, -1, v48\n"
"s_cbranch_vccz .Lsk_239\n"
"s_mov_b64 exec, vcc\n"
"buffer_load_dwordx4 v[36:39], v1, s[4:7], s46 offen offset:2048 sc1\n"
"s_mov_b64 exec, -1\n"
".Lsk_239:\n"
"v_max_u32 v48, v42, v43\n"
"v_max3_u32 v48, v40, v41, v48\n"
"v_cmp_eq_u32 vcc, -1, v48\n"
"s_cbranch_vccz .Lsk_240\n"
"s_mov_b64 exec, vcc\n"
"buffer_load_dwordx4 v[40:43], v1, s[4:7], s46 offen offset:3072 sc1\n"
"s_mov_b64 exec, -1\n"
".Lsk_240:\n"
"v_max_u32 v48, v46, v47\n"
"v_max3_u32 v48, v44, v45, v48\n"
"v_cmp_eq_u32 vcc, -1, v48\n"
"s_cbranch_vccz .Lsk_241\n"
"s_mov_b64 exec, vcc\n"
"buffer_load_dwordx4 v[44:47], v1, s[4:7], s47 offen offset:0 sc1\n"
"s_mov_b64 exec, -1\n"
".Lsk_241:\n"
"s_branch .Ltp_235\n"
".Lou_236:\n"
"s_waitcnt vmcnt(0)\n"
"s_add_u32 s46, s44, 0x140000\n"
"s_add_u32 s47, s46, 0x1000\n"
"buffer_load_dwordx4 v[28:31], v1, s[4:7], s46 offen offset:0 sc1\n"
"buffer_load_dwordx4 v[32:35], v1, s[4:7], s46 offen offset:1024 sc1\n"
"buffer_load_dwordx4 v[36:39], v1, s[4:7], s46 offen offset:2048 sc1\n"
"buffer_load_dwordx4 v[40:43], v1, s[4:7], s46 offen offset:3072 sc1\n"
"buffer_load_dwordx4 v[44:47], v1, s[4:7], s47 offen offset:0 sc1\n"
"s_mov_b32 s17, 32768\n"
".Ltp_242:\n"
"s_waitcnt vmcnt(0)\n"
"s_sub_u32 s17, s17, 1\n"
"s_cbranch_scc1 .Lou_243\n"
"v_max_u32 v48, v28, v29\n"
"v_max3_u32 v48, v30, v31, v48\n"
"v_max3_u32 v48, v32, v33, v48\n"
"v_max3_u32 v48, v34, v35, v48\n"
"v_max3_u32 v48, v36, v37, v48\n"
"v_max3_u32 v48, v38, v39, v48\n"
"v_max3_u32 v48, v40, v41, v48\n"
"v_max3_u32 v48, v42, v43, v48\n"
"v_max3_u32 v48, v44, v45, v48\n"
"v_max3_u32 v48, v46, v47, v48\n"
"v_cmp_eq_u32 vcc, -1, v48\n"
"s_cbranch_vccz .Lou_243\n"
"v_max_u32 v48, v30, v31\n"
"v_max3_u32 v48, v28, v29, v48\n"
"v_cmp_eq_u32 vcc, -1, v48\n"
"s_cbranch_vccz .Lsk_244\n"
"s_mov_b64 exec, vcc\n"
"buffer_load_dwordx4 v[28:31], v1, s[4:7], s46 offen offset:0 sc1\n"
"s_mov_b64 exec, -1\n"
".Lsk_244:\n"
"v_max_u32 v48, v34, v35\n"
"v_max3_u32 v48, v32, v33, v48\n"
"v_cmp_eq_u32 vcc, -1, v48\n"
"s_cbranch_vccz .Lsk_245\n"
"s_mov_b64 exec, vcc\n"
"buffer_load_dwordx4 v[32:35], v1, s[4:7], s46 offen offset:1024 sc1\n"
"s_mov_b64 exec, -1\n"
".Lsk_245:\n"
"v_max_u32 v48, v38, v39\n"
"v_max3_u32 v48, v36, v37, v48\n"
"v_cmp_eq_u32 vcc, -1, v48\n"
"s_cbranch_vccz .Lsk_246\n"
"s_mov_b64 exec, vcc\n"
"buffer_load_dwordx4 v[36:39], v1, s[4:7], s46 offen offset:2048 sc1\n"
"s_mov_b64 exec, -1\n"
".Lsk_246:\n"
"v_max_u32 v48, v42, v43\n"
"v_max3_u32 v48, v40, v41, v48\n"
"v_cmp_eq_u32 vcc, -1, v48\n"
"s_cbranch_vccz .Lsk_247\n"
"s_mov_b64 exec, vcc\n"
"buffer_load_dwordx4 v[40:43], v1, s[4:7], s46 offen offset:3072 sc1\n"
"s_mov_b64 exec, -1\n"
".Lsk_247:\n"
"v_max_u32 v48, v46, v47\n"
"v_max3_u32 v48, v44, v45, v48\n"
"v_cmp_eq_u32 vcc, -1, v48\n"
"s_cbranch_vccz .Lsk_248\n"
"s_mov_b64 exec, vcc\n"
"buffer_load_dwordx4 v[44:47], v1, s[4:7], s47 offen offset:0 sc1\n"
"s_mov_b64 exec, -1\n"
".Lsk_248:\n"
"s_branch .Ltp_242\n"
".Lou_243:\n"
"s_waitcnt vmcnt(0)\n"
"s_add_u32 s46, s44, 0x145000\n"
"s_add_u32 s47, s46, 0x1000\n"
"buffer_load_dwordx4 v[28:31], v1, s[4:7], s46 offen offset:0 sc1\n"
"buffer_load_dwordx4 v[32:35], v1, s[4:7], s46 offen offset:1024 sc1\n"
"buffer_load_dwordx4 v[36:39], v1, s[4:7], s46 offen offset:2048 sc1\n"
"buffer_load_dwordx4 v[40:43], v1, s[4:7], s46 offen offset:3072 sc1\n"
"buffer_load_dwordx4 v[44:47], v1, s[4:7], s47 offen offset:0 sc1\n"
"s_mov_b32 s17, 32768\n"
".Ltp_249:\n"
"s_waitcnt vmcnt(0)\n"
"s_sub_u32 s17, s17, 1\n"
"s_cbranch_scc1 .Lou_250\n"
"v_max_u32 v48, v28, v29\n"
"v_max3_u32 v48, v30, v31, v48\n"
"v_max3_u32 v48, v32, v33, v48\n"
"v_max3_u32 v48, v34, v35, v48\n"
"v_max3_u32 v48, v36, v37, v48\n"
"v_max3_u32 v48, v38, v39, v48\n"
"v_max3_u32 v48, v40, v41, v48\n"
"v_max3_u32 v48, v42, v43, v48\n"
"v_max3_u32 v48, v44, v45, v48\n"
"v_max3_u32 v48, v46, v47, v48\n"
"v_cmp_eq_u32 vcc, -1, v48\n"
"s_cbranch_vccz .Lou_250\n"
"v_max_u32 v48, v30, v31\n"
"v_max3_u32 v48, v28, v29, v48\n"
"v_cmp_eq_u32 vcc, -1, v48\n"
"s_cbranch_vccz .Lsk_251\n"
"s_mov_b64 exec, vcc\n"
"buffer_load_dwordx4 v[28:31], v1, s[4:7], s46 offen offset:0 sc1\n"
"s_mov_b64 exec, -1\n"
".Lsk_251:\n"
"v_max_u32 v48, v34, v35\n"
"v_max3_u32 v48, v32, v33, v48\n"
"v_cmp_eq_u32 vcc, -1, v48\n"
"s_cbranch_vccz .Lsk_252\n"
"s_mov_b64 exec, vcc\n"
"buffer_load_dwordx4 v[32:35], v1, s[4:7], s46 offen offset:1024 sc1\n"
"s_mov_b64 exec, -1\n"
".Lsk_252:\n"
"v_max_u32 v48, v38, v39\n"
"v_max3_u32 v48, v36, v37, v48\n"
"v_cmp_eq_u32 vcc, -1, v48\n"
"s_cbranch_vccz .Lsk_253\n"
"s_mov_b64 exec, vcc\n"
"buffer_load_dwordx4 v[36:39], v1, s[4:7], s46 offen offset:2048 sc1\n"
"s_mov_b64 exec, -1\n"
".Lsk_253:\n"
"v_max_u32 v48, v42, v43\n"
"v_max3_u32 v48, v40, v41, v48\n"
"v_cmp_eq_u32 vcc, -1, v48\n"
"s_cbranch_vccz .Lsk_254\n"
"s_mov_b64 exec, vcc\n"
"buffer_load_dwordx4 v[40:43], v1, s[4:7], s46 offen offset:3072 sc1\n"
"s_mov_b64 exec, -1\n"
".Lsk_254:\n"
"v_max_u32 v48, v46, v47\n"
"v_max3_u32 v48, v44, v45, v48\n"
"v_cmp_eq_u32 vcc, -1, v48\n"
"s_cbranch_vccz .Lsk_255\n"
"s_mov_b64 exec, vcc\n"
"buffer_load_dwordx4 v[44:47], v1, s[4:7], s47 offen offset:0 sc1\n"
"s_mov_b64 exec, -1\n"
".Lsk_255:\n"
"s_branch .Ltp_249\n"
".Lou_250:\n"
"s_waitcnt vmcnt(0)\n"
"s_add_u32 s46, s44, 0x14a000\n"
"s_add_u32 s47, s46, 0x1000\n"
"buffer_load_dwordx4 v[28:31], v1, s[4:7], s46 offen offset:0 sc1\n"
"buffer_load_dwordx4 v[32:35], v1, s[4:7], s46 offen offset:1024 sc1\n"
"buffer_load_dwordx4 v[36:39], v1, s[4:7], s46 offen offset:2048 sc1\n"
"buffer_load_dwordx4 v[40:43], v1, s[4:7], s46 offen offset:3072 sc1\n"
"buffer_load_dwordx4 v[44:47], v1, s[4:7], s47 offen offset:0 sc1\n"
"s_mov_b32 s17, 32768\n"
".Ltp_256:\n"
"s_waitcnt vmcnt(0)\n"
"s_sub_u32 s17, s17, 1\n"
"s_cbranch_scc1 .Lou_257\n"
"v_max_u32 v48, v28, v29\n"
"v_max3_u32 v48, v30, v31, v48\n"
"v_max3_u32 v48, v32, v33, v48\n"
"v_max3_u32 v48, v34, v35, v48\n"
"v_max3_u32 v48, v36, v37, v48\n"
"v_max3_u32 v48, v38, v39, v48\n"
"v_max3_u32 v48, v40, v41, v48\n"
"v_max3_u32 v48, v42, v43, v48\n"
"v_max3_u32 v48, v44, v45, v48\n"
"v_max3_u32 v48, v46, v47, v48\n"
"v_cmp_eq_u32 vcc, -1, v48\n"
"s_cbranch_vccz .Lou_257\n"
"v_max_u32 v48, v30, v31\n"
"v_max3_u32 v48, v28, v29, v48\n"
"v_cmp_eq_u32 vcc, -1, v48\n"
"s_cbranch_vccz .Lsk_258\n"
"s_mov_b64 exec, vcc\n"
"buffer_load_dwordx4 v[28:31], v1, s[4:7], s46 offen offset:0 sc1\n"
"s_mov_b64 exec, -1\n"
".Lsk_258:\n"
"v_max_u32 v48, v34, v35\n"
"v_max3_u32 v48, v32, v33, v48\n"
"v_cmp_eq_u32 vcc, -1, v48\n"
"s_cbranch_vccz .Lsk_259\n"
"s_mov_b64 exec, vcc\n"
"buffer_load_dwordx4 v[32:35], v1, s[4:7], s46 offen offset:1024 sc1\n"
"s_mov_b64 exec, -1\n"
".Lsk_259:\n"
"v_max_u32 v48, v38, v39\n"
"v_max3_u32 v48, v36, v37, v48\n"
"v_cmp_eq_u32 vcc, -1, v48\n"
"s_cbranch_vccz .Lsk_260\n"
"s_mov_b64 exec, vcc\n"
"buffer_load_dwordx4 v[36:39], v1, s[4:7], s46 offen offset:2048 sc1\n"
"s_mov_b64 exec, -1\n"
".Lsk_260:\n"
"v_max_u32 v48, v42, v43\n"
"v_max3_u32 v48, v40, v41, v48\n"
"v_cmp_eq_u32 vcc, -1, v48\n"
"s_cbranch_vccz .Lsk_261\n"
"s_mov_b64 exec, vcc\n"
"buffer_load_dwordx4 v[40:43], v1, s[4:7], s46 offen offset:3072 sc1\n"
"s_mov_b64 exec, -1\n"
".Lsk_261:\n"
"v_max_u32 v48, v46, v47\n"
"v_max3_u32 v48, v44, v45, v48\n"
"v_cmp_eq_u32 vcc, -1, v48\n"
"s_cbranch_vccz .Lsk_262\n"
"s_mov_b64 exec, vcc\n"
"buffer_load_dwordx4 v[44:47], v1, s[4:7], s47 offen offset:0 sc1\n"
"s_mov_b64 exec, -1\n"
".Lsk_262:\n"
"s_branch .Ltp_256\n"
".Lou_257:\n"
"s_waitcnt vmcnt(0)\n"
"s_add_u32 s46, s44, 0x14f000\n"
"s_add_u32 s47, s46, 0x1000\n"
"buffer_load_dwordx4 v[28:31], v1, s[4:7], s46 offen offset:0 sc1\n"
"buffer_load_dwordx4 v[32:35], v1, s[4:7], s46 offen offset:1024 sc1\n"
"buffer_load_dwordx4 v[36:39], v1, s[4:7], s46 offen offset:2048 sc1\n"
"buffer_load_dwordx4 v[40:43], v1, s[4:7], s46 offen offset:3072 sc1\n"
"buffer_load_dwordx4 v[44:47], v1, s[4:7], s47 offen offset:0 sc1\n"
"s_mov_b32 s17, 32768\n"
".Ltp_263:\n"
"s_waitcnt vmcnt(0)\n"
"s_sub_u32 s17, s17, 1\n"
"s_cbranch_scc1 .Lou_264\n"
"v_max_u32 v48, v28, v29\n"
"v_max3_u32 v48, v30, v31, v48\n"
"v_max3_u32 v48, v32, v33, v48\n"
"v_max3_u32 v48, v34, v35, v48\n"
"v_max3_u32 v48, v36, v37, v48\n"
"v_max3_u32 v48, v38, v39, v48\n"
"v_max3_u32 v48, v40, v41, v48\n"
"v_max3_u32 v48, v42, v43, v48\n"
"v_max3_u32 v48, v44, v45, v48\n"
"v_max3_u32 v48, v46, v47, v48\n"
"v_cmp_eq_u32 vcc, -1, v48\n"
"s_cbranch_vccz .Lou_264\n"
"v_max_u32 v48, v30, v31\n"
"v_max3_u32 v48, v28, v29, v48\n"
"v_cmp_eq_u32 vcc, -1, v48\n"
"s_cbranch_vccz .Lsk_265\n"
"s_mov_b64 exec, vcc\n"
"buffer_load_dwordx4 v[28:31], v1, s[4:7], s46 offen offset:0 sc1\n"
"s_mov_b64 exec, -1\n"
".Lsk_265:\n"
"v_max_u32 v48, v34, v35\n"
"v_max3_u32 v48, v32, v33, v48\n"
"v_cmp_eq_u32 vcc, -1, v48\n"
"s_cbranch_vccz .Lsk_266\n"
"s_mov_b64 exec, vcc\n"
"buffer_load_dwordx4 v[32:35], v1, s[4:7], s46 offen offset:1024 sc1\n"
"s_mov_b64 exec, -1\n"
".Lsk_266:\n"
"v_max_u32 v48, v38, v39\n"
"v_max3_u32 v48, v36, v37, v48\n"
"v_cmp_eq_u32 vcc, -1, v48\n"
"s_cbranch_vccz .Lsk_267\n"
"s_mov_b64 exec, vcc\n"
"buffer_load_dwordx4 v[36:39], v1, s[4:7], s46 offen offset:2048 sc1\n"
"s_mov_b64 exec, -1\n"
".Lsk_267:\n"
"v_max_u32 v48, v42, v43\n"
"v_max3_u32 v48, v40, v41, v48\n"
"v_cmp_eq_u32 vcc, -1, v48\n"
"s_cbranch_vccz .Lsk_268\n"
"s_mov_b64 exec, vcc\n"
"buffer_load_dwordx4 v[40:43], v1, s[4:7], s46 offen offset:3072 sc1\n"
"s_mov_b64 exec, -1\n"
".Lsk_268:\n"
"v_max_u32 v48, v46, v47\n"
"v_max3_u32 v48, v44, v45, v48\n"
"v_cmp_eq_u32 vcc, -1, v48\n"
"s_cbranch_vccz .Lsk_269\n"
"s_mov_b64 exec, vcc\n"
"buffer_load_dwordx4 v[44:47], v1, s[4:7], s47 offen offset:0 sc1\n"
"s_mov_b64 exec, -1\n"
".Lsk_269:\n"
"s_branch .Ltp_263\n"
".Lou_264:\n"
"s_waitcnt vmcnt(0)\n"
"ds_write_b32 v13, v13 offset:448\n"
"s_mov_b32 s17, 0x100000\n"
".Ltp_270:\n"
"ds_read_b32 v90, v14 offset:448\n"
"s_sub_u32 s17, s17, 1\n"
"s_cbranch_scc1 .Lou_271\n"
"s_waitcnt lgkmcnt(0)\n"
"v_cmp_eq_u32 vcc, 0, v90\n"
"s_cbranch_vccnz .Ltp_270\n"
".Lou_271:\n"
"s_waitcnt lgkmcnt(0)\n"
"v_mov_b32 v90, 0\n"
"v_mov_b32 v90, -1\n"
"ds_write_b32 v9, v90 offset:3712\n"
"ds_write_b32 v9, v90 offset:4032\n"
"ds_write_b32 v9, v90 offset:4352\n"
"ds_write_b32 v9, v90 offset:4672\n"
"ds_write_b32 v9, v90 offset:4992\n"
"ds_write_b32 v10, v90 offset:5312\n"
"s_add_u32 m0, s29, 0xa000\n"
"s_add_u32 s31, s44, 0x140000\n"
"buffer_load_dwordx4 v1, s[4:7], s31 offen offset:0 sc1 lds\n"
"buffer_load_dwordx4 v1, s[4:7], s31 offen offset:1024 sc1 lds\n"
"buffer_load_dwordx4 v1, s[4:7], s31 offen offset:2048 sc1 lds\n"
"buffer_load_dwordx4 v1, s[4:7], s31 offen offset:3072 sc1 lds\n"
"s_add_u32 m0, m0, 1024\n"
"s_add_u32 s31, s31, 1024\n"
"buffer_load_dwordx4 v1, s[4:7], s31 offen offset:3072 sc1 lds\n"
"s_add_u32 m0, s29, 0xf000\n"
"s_add_u32 s31, s44, 0x145000\n"
"buffer_load_dwordx4 v1, s[4:7], s31 offen offset:0 sc1 lds\n"
"buffer_load_dwordx4 v1, s[4:7], s31 offen offset:1024 sc1 lds\n"
"buffer_load_dwordx4 v1, s[4:7], s31 offen offset:2048 sc1 lds\n"
"buffer_load_dwordx4 v1, s[4:7], s31 offen offset:3072 sc1 lds\n"
"s_add_u32 m0, m0, 1024\n"
"s_add_u32 s31, s31, 1024\n"
"buffer_load_dwordx4 v1, s[4:7], s31 offen offset:3072 sc1 lds\n"
"s_add_u32 m0, s29, 0\n"
"s_add_u32 s31, s44, 0x14a000\n"
"buffer_load_dwordx4 v1, s[4:7], s31 offen offset:0 sc1 lds\n"
"buffer_load_dwordx4 v1, s[4:7], s31 offen offset:1024 sc1 lds\n"
"buffer_load_dwordx4 v1, s[4:7], s31 offen offset:2048 sc1 lds\n"
"buffer_load_dwordx4 v1, s[4:7], s31 offen offset:3072 sc1 lds\n"
"s_add_u32 m0, m0, 1024\n"
"s_add_u32 s31, s31, 1024\n"
"buffer_load_dwordx4 v1, s[4:7], s31 offen offset:3072 sc1 lds\n"
"s_add_u32 m0, s29, 0x5000\n"
"s_add_u32 s31, s44, 0x14f000\n"
"buffer_load_dwordx4 v1, s[4:7], s31 offen offset:0 sc1 lds\n"
"buffer_load_dwordx4 v1, s[4:7], s31 offen offset:1024 sc1 lds\n"
"buffer_load_dwordx4 v1, s[4:7], s31 offen offset:2048 sc1 lds\n"
"buffer_load_dwordx4 v1, s[4:7], s31 offen offset:3072 sc1 lds\n"
"s_add_u32 m0, m0, 1024\n"
"s_add_u32 s31, s31, 1024\n"
"buffer_load_dwordx4 v1, s[4:7], s31 offen offset:3072 sc1 lds\n"
"s_waitcnt vmcnt(0)\n"
"ds_write_b32 v13, v13 offset:464\n"
"s_mov_b32 s17, 0x100000\n"
".Ltp_272:\n"
"ds_read_b32 v90, v14 offset:464\n"
"s_sub_u32 s17, s17, 1\n"
"s_cbranch_scc1 .Lou_273\n"
"s_waitcnt lgkmcnt(0)\n"
"v_cmp_eq_u32 vcc, 0, v90\n"
"s_cbranch_vccnz .Ltp_272\n"
".Lou_273:\n"
"s_waitcnt lgkmcnt(0)\n"
"v_mov_b32 v90, 0\n"
"s_branch .Lag_193\n"
".L_xend:\n"
"v_and_b32 v0, 63, v0\n"
"s_and_b32 s3, s3, 3\n"
"s_and_b32 s79, s2, 7\n"
"s_lshl_b32 s79, s79, 4\n"
"s_lshr_b32 s78, s2, 3\n"
"s_add_u32 s79, s79, s78\n"
"s_and_b32 s78, s2, 3\n"
"s_lshl_b32 s78, s78, 3\n"
"s_and_b32 s77, s2, 4\n"
"s_cmp_eq_i32 s77, 4\n"
"s_cselect_b32 s78, 32, s78\n"
"s_add_u32 s79, s79, s78\n"
"s_and_b32 s78, s2, 4\n"
"s_lshl_b32 s78, s78, 4\n"
"s_add_u32 s78, s2, s78\n"
"s_cmp_lt_i32 s78, 0xc0\n"
"s_cselect_b32 s18, s79, -1\n"
"s_and_b32 s79, s2, 7\n"
"s_lshl_b32 s79, s79, 4\n"
"s_lshr_b32 s78, s2, 3\n"
"s_add_u32 s79, s79, s78\n"
"s_and_b32 s78, s2, 3\n"
"s_lshl_b32 s78, s78, 3\n"
"s_add_u32 s78, s78, 24\n"
"s_and_b32 s77, s2, 4\n"
"s_cmp_eq_i32 s77, 4\n"
"s_cselect_b32 s78, 48, s78\n"
"s_sub_u32 s79, s79, s78\n"
"s_and_b32 s78, s2, 4\n"
"s_lshl_b32 s78, s78, 4\n"
"s_add_u32 s78, s2, s78\n"
"s_cmp_lt_i32 s78, 0xc0\n"
"s_cselect_b32 s19, 0x3e8, s79\n"
"v_lshlrev_b32 v2, 4, v0\n"
"s_mov_b32 s4, s26\n"
"s_mov_b32 s5, s27\n"
"s_mov_b32 s6, 0x1688000\n"
"s_mov_b32 s7, 0x20000\n"
"v_lshlrev_b32 v127, 2, v0\n"
"s_mul_i32 s79, s3, 0x2100\n"
"s_add_u32 s79, s79, 0x14000\n"
"v_add_u32 v152, s79, v127\n"
"v_add_u32 v153, 0x440, v152\n"
"v_add_u32 v154, 0x880, v152\n"
"v_add_u32 v155, 0xcc0, v152\n"
"s_mul_i32 s79, s3, 0x2100\n"
"s_add_u32 s79, s79, 0x14000\n"
"v_and_b32 v99, 15, v0\n"
"v_and_b32 v98, 48, v0\n"
"s_mov_b32 s78, 0x110\n"
"v_mad_i32_i24 v99, v99, s78, v98\n"
"v_add_u32 v141, s79, v99\n"
"s_lshl_b32 s79, s3, 2\n"
"s_add_u32 s79, s79, 0x26400\n"
"v_mov_b32 v99, s79\n"
"v_mov_b32 v98, 0x40000\n"
"v_cmp_eq_i32 vcc, 0, v0\n"
"s_nop 1\n"
"v_cndmask_b32 v4, v98, v99, vcc\n"
"v_and_b32 v99, 3, v0\n"
"v_lshlrev_b32 v99, 2, v99\n"
"v_add_u32 v5, 0x26400, v99\n"
"v_min_i32 v99, 49, v0\n"
"v_lshlrev_b32 v38, 5, v99\n"
"s_and_b32 s79, s2, 1\n"
"s_lshl_b32 s79, s79, 2\n"
"s_lshr_b32 s78, s2, 3\n"
"s_and_b32 s78, s78, 3\n"
"s_add_u32 s28, s79, s78\n"
"s_lshr_b32 s79, s2, 1\n"
"s_and_b32 s79, s79, 3\n"
"s_lshl_b32 s79, s79, 3\n"
"s_lshr_b32 s78, s2, 3\n"
"s_lshr_b32 s78, s78, 2\n"
"s_add_u32 s29, s79, s78\n"
"s_and_b32 s79, s2, 7\n"
"s_lshl_b32 s79, s79, 14\n"
"s_add_u32 s48, s79, 0x4000\n"
"s_mov_b32 s8, s22\n"
"s_mov_b32 s9, s23\n"
"s_mov_b32 s10, 0x280000\n"
"s_mov_b32 s11, 0x20000\n"
"s_mov_b32 s12, s24\n"
"s_mov_b32 s13, s25\n"
"s_mov_b32 s14, 0x280000\n"
"s_mov_b32 s15, 0x20000\n"
"s_mov_b32 s44, 0xcccccccc\n"
"s_mov_b32 s45, 0xcccccccc\n"
"s_mov_b32 s56, 0\n"
"s_mov_b32 s57, -2\n"
"s_mov_b32 s58, 0\n"
"s_mov_b32 s59, -1\n"
"s_lshl_b32 s79, s29, 1\n"
"s_lshr_b32 s78, s3, 1\n"
"s_add_u32 s79, s79, s78\n"
"s_mul_i32 s30, s79, 0x5000\n"
"s_and_b32 s79, s2, 7\n"
"s_lshl_b32 s79, s79, 14\n"
"s_add_u32 s33, s79, 0x3800\n"
"s_lshl_b32 s79, s28, 2\n"
"s_add_u32 s79, s79, s3\n"
"s_mul_i32 s79, s79, 0x6400\n"
"s_add_u32 s32, s79, 0x380000\n"
"s_lshr_b32 s79, s2, 1\n"
"s_and_b32 s79, s79, 3\n"
"s_lshl_b32 s79, s79, 2\n"
"s_lshr_b32 s78, s2, 3\n"
"s_lshr_b32 s78, s78, 3\n"
"s_add_u32 s79, s79, s78\n"
"s_lshl_b32 s79, s79, 2\n"
"s_add_u32 s79, s79, s3\n"
"s_mul_i32 s79, s79, 0x5000\n"
"s_and_b32 s78, s2, 1\n"
"s_lshl_b32 s78, s78, 3\n"
"s_lshr_b32 s77, s2, 3\n"
"s_and_b32 s77, s77, 7\n"
"s_add_u32 s78, s78, s77\n"
"s_mul_i32 s78, s78, 0x500\n"
"s_add_u32 s79, s79, s78\n"
"s_add_u32 s31, s79, 0x448000\n"
"v_mov_b32 v128, 0\n"
"v_mov_b32 v129, 0\n"
"v_mov_b32 v130, 0\n"
"v_mov_b32 v131, 0\n"
"v_mov_b32 v132, 0\n"
"v_mov_b32 v133, 0\n"
"v_mov_b32 v134, 0\n"
"v_mov_b32 v135, 0\n"
"v_mov_b32 v136, 0\n"
"v_mov_b32 v137, 0\n"
"v_mov_b32 v138, 0\n"
"v_mov_b32 v139, 0\n"
"s_waitcnt lgkmcnt(0)\n"
"s_and_b32 s79, s3, 1\n"
"s_lshl_b32 s60, s79, 4\n"
"s_lshr_b32 s39, s39, s60\n"
"s_lshr_b32 s40, s40, s60\n"
"s_lshr_b32 s41, s41, s60\n"
"v_mov_b32 v142, s40\n"
"v_mov_b32 v140, s41\n"
"s_mul_i32 s16, s16, 0x2c9277b5\n"
"s_add_u32 s16, s16, 0xac564b05\n"
"s_cmp_lt_i32 s18, 0\n"
"s_cbranch_scc1 .Lnk_304\n"
"s_setprio 0\n"
"s_mov_b32 s55, 2\n"
"s_mov_b32 s52, 0xc2f00000\n"
"s_mov_b32 s53, 0x3fb8aa3b\n"
"s_mov_b32 s54, 0x3f317218\n"
"v_mov_b32 v31, 0x42f00000\n"
"v_lshlrev_b32 v142, 16, v142\n"
"v_med3_f32 v142, v142, s52, v31\n"
"v_mul_f32 v28, s53, v142\n"
"v_exp_f32 v29, v28\n"
"v_fma_f32 v28, v142, s53, -v28\n"
"v_fmac_f32 v28, 0x32a57060, v142\n"
"v_fma_f32 v28, v28, s54, 1.0\n"
"v_mul_f32 v142, v29, v28\n"
"v_cvt_pk_bf16_f32 v142, v142, v142\n"
"v_lshlrev_b32 v142, 16, v142\n"
"s_waitcnt vmcnt(8)\n"
"s_nop 3\n"
".Lkg_311:\n"
"v_lshlrev_b32 v32, 16, v32\n"
"v_lshlrev_b32 v33, 16, v33\n"
"v_lshlrev_b32 v34, 16, v34\n"
"v_lshlrev_b32 v35, 16, v35\n"
"v_mul_f32 v26, v142, v34\n"
"v_mul_f32 v27, v142, v35\n"
"v_max3_f32 v30, |v26|, |v27|, 0\n"
"s_nop 1\n"
"v_max_u32_dpp v30, v30, v30 row_ror:8 row_mask:0xf bank_mask:0xf\n"
"v_med3_f32 v32, v32, s52, v31\n"
"s_nop 0\n"
"v_max_u32_dpp v30, v30, v30 row_ror:4 row_mask:0xf bank_mask:0xf\n"
"v_mul_f32 v28, s53, v32\n"
"s_nop 0\n"
"v_max_u32_dpp v30, v30, v30 row_ror:2 row_mask:0xf bank_mask:0xf\n"
"v_exp_f32 v29, v28\n"
"s_nop 0\n"
"v_max_u32_dpp v30, v30, v30 row_ror:1 row_mask:0xf bank_mask:0xf\n"
"v_fma_f32 v28, v32, s53, -v28\n"
"v_fmac_f32 v28, 0x32a57060, v32\n"
"v_mov_b32 v34, v30\n"
"v_fma_f32 v28, v28, s54, 1.0\n"
"s_nop 0\n"
"v_permlane16_swap_b32 v30, v34\n"
"v_mul_f32 v32, v29, v28\n"
"v_max_u32 v30, v30, v34\n"
"v_cvt_pk_bf16_f32 v32, v32, v32\n"
"v_lshlrev_b32 v32, 16, v32\n"
"v_mov_b32 v34, v30\n"
"v_mul_f32 v25, v142, v33\n"
"s_nop 0\n"
"v_permlane32_swap_b32 v30, v34\n"
"v_mul_f32 v24, v142, -v32\n"
"v_max_u32 v30, v30, v34\n"
"v_mul_f32 v25, 0.15915494, v25\n"
"v_mul_f32 v24, s53, v24\n"
"v_readfirstlane_b32 s51, v30\n"
"s_nop 2\n"
"s_bfe_u32 s50, s51, 0x80017\n"
"s_max_i32 s50, s50, 37\n"
"s_min_i32 s50, s50, 217\n"
"s_cmp_gt_i32 s51, 0\n"
"s_cselect_b32 s50, s50, 127\n"
"s_sub_u32 s51, 267, s50\n"
"s_lshl_b32 s51, s51, 23\n"
"s_sub_u32 s50, s50, 27\n"
"s_lshl_b32 s50, s50, 23\n"
"v_mul_f32 v26, s51, v26\n"
"v_mul_f32 v27, s51, v27\n"
"v_mul_f32 v111, 0x41800000, v25\n"
"v_mul_f32 v100, 0x41800000, v24\n"
"v_fract_f32 v111, v111\n"
"v_exp_f32 v100, v100\n"
"v_cos_f32 v110, v111\n"
"v_sin_f32 v111, -v111\n"
"v_mul_f32 v110, v110, v100\n"
"v_mul_f32 v111, v111, v100\n"
"v_fract_f32 v113, v25\n"
"v_exp_f32 v100, v24\n"
"v_cos_f32 v112, v113\n"
"v_sin_f32 v113, v113\n"
"v_mul_f32 v112, v112, v100\n"
"v_mul_f32 v113, v113, v100\n"
"v_mul_f32 v115, 0x43800000, v25\n"
"v_mul_f32 v100, 0x43800000, v24\n"
"v_fract_f32 v115, v115\n"
"v_exp_f32 v100, v100\n"
"v_cos_f32 v114, v115\n"
"v_sin_f32 v115, v115\n"
"v_mul_f32 v114, v114, v100\n"
"v_mul_f32 v115, v115, v100\n"
"v_mul_f32 v116, v111, v111\n"
"v_add_f32 v117, v110, v110\n"
"v_fma_f32 v116, v110, v110, -v116\n"
"v_mul_f32 v117, v117, v111\n"
"v_mul_f32 v120, v117, v117\n"
"v_add_f32 v121, v116, v116\n"
"v_fma_f32 v120, v116, v116, -v120\n"
"v_mul_f32 v121, v121, v117\n"
"v_mul_f32 v118, v117, v111\n"
"v_mul_f32 v119, v116, v111\n"
"v_fma_f32 v118, v116, v110, -v118\n"
"v_fmac_f32 v119, v117, v110\n"
"v_mul_f32 v122, v121, v121\n"
"v_add_f32 v123, v120, v120\n"
"v_fma_f32 v122, v120, v120, -v122\n"
"v_mul_f32 v123, v123, v121\n"
"v_mov_b32 v84, 0x7400\n"
"v_mul_f32 v124, 0x46800000, v110\n"
"v_mul_f32 v125, 0x46800000, v111\n"
"v_cvt_pk_f16_f32 v100, v124, v125\n"
"v_mul_f32 v124, 0x46800000, v116\n"
"v_mul_f32 v125, 0x46800000, v117\n"
"v_cvt_pk_f16_f32 v85, v124, v125\n"
"v_mul_f32 v124, 0x46800000, v118\n"
"v_mul_f32 v125, 0x46800000, v119\n"
"v_cvt_pk_f16_f32 v101, v124, v125\n"
"v_cvt_pk_f16_f32 v93, v120, v121\n"
"v_cvt_pk_f16_f32 v94, v122, v123\n"
"v_mul_f32 v124, v123, v121\n"
"v_mul_f32 v125, v122, v121\n"
"v_fma_f32 v124, v122, v120, -v124\n"
"v_fmac_f32 v125, v123, v120\n"
"v_cvt_pk_f16_f32 v95, v124, v125\n"
"ds_write2_b32 v152, v84, v100 offset0:0 offset1:68\n"
"v_pk_mul_f16 v86, v84, v93 op_sel:[0,0] op_sel_hi:[1,0]\n"
"v_pk_mul_f16 v102, v100, v93 op_sel:[0,0] op_sel_hi:[1,0]\n"
"v_pk_fma_f16 v86, v84, v93, v86 op_sel:[1,1,0] op_sel_hi:[0,1,1] neg_lo:[1,0,0]\n"
"v_pk_fma_f16 v102, v100, v93, v102 op_sel:[1,1,0] op_sel_hi:[0,1,1] neg_lo:[1,0,0]\n"
"ds_write2_b32 v152, v85, v101 offset0:136 offset1:204\n"
"v_pk_mul_f16 v87, v85, v93 op_sel:[0,0] op_sel_hi:[1,0]\n"
"v_pk_mul_f16 v103, v101, v93 op_sel:[0,0] op_sel_hi:[1,0]\n"
"v_pk_fma_f16 v87, v85, v93, v87 op_sel:[1,1,0] op_sel_hi:[0,1,1] neg_lo:[1,0,0]\n"
"v_pk_fma_f16 v103, v101, v93, v103 op_sel:[1,1,0] op_sel_hi:[0,1,1] neg_lo:[1,0,0]\n"
"ds_write2_b32 v153, v86, v102 offset0:0 offset1:68\n"
"v_pk_mul_f16 v88, v84, v94 op_sel:[0,0] op_sel_hi:[1,0]\n"
"v_pk_mul_f16 v102, v100, v94 op_sel:[0,0] op_sel_hi:[1,0]\n"
"v_pk_fma_f16 v88, v84, v94, v88 op_sel:[1,1,0] op_sel_hi:[0,1,1] neg_lo:[1,0,0]\n"
"v_pk_fma_f16 v102, v100, v94, v102 op_sel:[1,1,0] op_sel_hi:[0,1,1] neg_lo:[1,0,0]\n"
"ds_write2_b32 v153, v87, v103 offset0:136 offset1:204\n"
"v_pk_mul_f16 v89, v85, v94 op_sel:[0,0] op_sel_hi:[1,0]\n"
"v_pk_mul_f16 v103, v101, v94 op_sel:[0,0] op_sel_hi:[1,0]\n"
"v_pk_fma_f16 v89, v85, v94, v89 op_sel:[1,1,0] op_sel_hi:[0,1,1] neg_lo:[1,0,0]\n"
"v_pk_fma_f16 v103, v101, v94, v103 op_sel:[1,1,0] op_sel_hi:[0,1,1] neg_lo:[1,0,0]\n"
"ds_write2_b32 v154, v88, v102 offset0:0 offset1:68\n"
"v_pk_mul_f16 v90, v84, v95 op_sel:[0,0] op_sel_hi:[1,0]\n"
"v_pk_mul_f16 v102, v100, v95 op_sel:[0,0] op_sel_hi:[1,0]\n"
"v_pk_fma_f16 v90, v84, v95, v90 op_sel:[1,1,0] op_sel_hi:[0,1,1] neg_lo:[1,0,0]\n"
"v_pk_fma_f16 v102, v100, v95, v102 op_sel:[1,1,0] op_sel_hi:[0,1,1] neg_lo:[1,0,0]\n"
"ds_write2_b32 v154, v89, v103 offset0:136 offset1:204\n"
"v_pk_mul_f16 v91, v85, v95 op_sel:[0,0] op_sel_hi:[1,0]\n"
"v_pk_mul_f16 v103, v101, v95 op_sel:[0,0] op_sel_hi:[1,0]\n"
"v_pk_fma_f16 v91, v85, v95, v91 op_sel:[1,1,0] op_sel_hi:[0,1,1] neg_lo:[1,0,0]\n"
"v_pk_fma_f16 v103, v101, v95, v103 op_sel:[1,1,0] op_sel_hi:[0,1,1] neg_lo:[1,0,0]\n"
"ds_write2_b32 v155, v90, v102 offset0:0 offset1:68\n"
"ds_write2_b32 v155, v91, v103 offset0:136 offset1:204\n"
"ds_read_b128 v[52:55], v141 offset:0\n"
"ds_read_b128 v[56:59], v141 offset:64\n"
"ds_read_b128 v[60:63], v141 offset:128\n"
"ds_read_b128 v[64:67], v141 offset:192\n"
"v_mul_f32 v116, v113, v113\n"
"v_add_f32 v117, v112, v112\n"
"v_fma_f32 v116, v112, v112, -v116\n"
"v_mul_f32 v117, v117, v113\n"
"v_mul_f32 v120, v117, v117\n"
"v_add_f32 v121, v116, v116\n"
"v_fma_f32 v120, v116, v116, -v120\n"
"v_mul_f32 v121, v121, v117\n"
"v_mul_f32 v118, v117, v113\n"
"v_mul_f32 v119, v116, v113\n"
"v_fma_f32 v118, v116, v112, -v118\n"
"v_fmac_f32 v119, v117, v112\n"
"v_mul_f32 v122, v121, v121\n"
"v_add_f32 v123, v120, v120\n"
"v_fma_f32 v122, v120, v120, -v122\n"
"v_mul_f32 v123, v123, v121\n"
"v_cvt_pk_f16_f32 v84, v26, v27\n"
"v_mul_f32 v124, v27, v113\n"
"v_mul_f32 v125, v26, v113\n"
"v_fma_f32 v124, v26, v112, -v124\n"
"v_fmac_f32 v125, v27, v112\n"
"v_cvt_pk_f16_f32 v100, v124, v125\n"
"v_mul_f32 v124, v27, v117\n"
"v_mul_f32 v125, v26, v117\n"
"v_fma_f32 v124, v26, v116, -v124\n"
"v_fmac_f32 v125, v27, v116\n"
"v_cvt_pk_f16_f32 v85, v124, v125\n"
"v_mul_f32 v124, v27, v119\n"
"v_mul_f32 v125, v26, v119\n"
"v_fma_f32 v124, v26, v118, -v124\n"
"v_fmac_f32 v125, v27, v118\n"
"v_cvt_pk_f16_f32 v101, v124, v125\n"
"v_cvt_pk_f16_f32 v93, v120, v121\n"
"v_cvt_pk_f16_f32 v94, v122, v123\n"
"v_mul_f32 v124, v123, v121\n"
"v_mul_f32 v125, v122, v121\n"
"v_fma_f32 v124, v122, v120, -v124\n"
"v_fmac_f32 v125, v123, v120\n"
"v_cvt_pk_f16_f32 v95, v124, v125\n"
"ds_write2_b32 v152, v84, v100 offset0:0 offset1:68\n"
"v_pk_mul_f16 v86, v84, v93 op_sel:[0,0] op_sel_hi:[1,0]\n"
"v_pk_mul_f16 v102, v100, v93 op_sel:[0,0] op_sel_hi:[1,0]\n"
"v_pk_fma_f16 v86, v84, v93, v86 op_sel:[1,1,0] op_sel_hi:[0,1,1] neg_lo:[1,0,0]\n"
"v_pk_fma_f16 v102, v100, v93, v102 op_sel:[1,1,0] op_sel_hi:[0,1,1] neg_lo:[1,0,0]\n"
"ds_write2_b32 v152, v85, v101 offset0:136 offset1:204\n"
"v_pk_mul_f16 v87, v85, v93 op_sel:[0,0] op_sel_hi:[1,0]\n"
"v_pk_mul_f16 v103, v101, v93 op_sel:[0,0] op_sel_hi:[1,0]\n"
"v_pk_fma_f16 v87, v85, v93, v87 op_sel:[1,1,0] op_sel_hi:[0,1,1] neg_lo:[1,0,0]\n"
"v_pk_fma_f16 v103, v101, v93, v103 op_sel:[1,1,0] op_sel_hi:[0,1,1] neg_lo:[1,0,0]\n"
"ds_write2_b32 v153, v86, v102 offset0:0 offset1:68\n"
"v_pk_mul_f16 v88, v84, v94 op_sel:[0,0] op_sel_hi:[1,0]\n"
"v_pk_mul_f16 v102, v100, v94 op_sel:[0,0] op_sel_hi:[1,0]\n"
"v_pk_fma_f16 v88, v84, v94, v88 op_sel:[1,1,0] op_sel_hi:[0,1,1] neg_lo:[1,0,0]\n"
"v_pk_fma_f16 v102, v100, v94, v102 op_sel:[1,1,0] op_sel_hi:[0,1,1] neg_lo:[1,0,0]\n"
"ds_write2_b32 v153, v87, v103 offset0:136 offset1:204\n"
"v_pk_mul_f16 v89, v85, v94 op_sel:[0,0] op_sel_hi:[1,0]\n"
"v_pk_mul_f16 v103, v101, v94 op_sel:[0,0] op_sel_hi:[1,0]\n"
"v_pk_fma_f16 v89, v85, v94, v89 op_sel:[1,1,0] op_sel_hi:[0,1,1] neg_lo:[1,0,0]\n"
"v_pk_fma_f16 v103, v101, v94, v103 op_sel:[1,1,0] op_sel_hi:[0,1,1] neg_lo:[1,0,0]\n"
"ds_write2_b32 v154, v88, v102 offset0:0 offset1:68\n"
"v_pk_mul_f16 v90, v84, v95 op_sel:[0,0] op_sel_hi:[1,0]\n"
"v_pk_mul_f16 v102, v100, v95 op_sel:[0,0] op_sel_hi:[1,0]\n"
"v_pk_fma_f16 v90, v84, v95, v90 op_sel:[1,1,0] op_sel_hi:[0,1,1] neg_lo:[1,0,0]\n"
"v_pk_fma_f16 v102, v100, v95, v102 op_sel:[1,1,0] op_sel_hi:[0,1,1] neg_lo:[1,0,0]\n"
"ds_write2_b32 v154, v89, v103 offset0:136 offset1:204\n"
"v_pk_mul_f16 v91, v85, v95 op_sel:[0,0] op_sel_hi:[1,0]\n"
"v_pk_mul_f16 v103, v101, v95 op_sel:[0,0] op_sel_hi:[1,0]\n"
"v_pk_fma_f16 v91, v85, v95, v91 op_sel:[1,1,0] op_sel_hi:[0,1,1] neg_lo:[1,0,0]\n"
"v_pk_fma_f16 v103, v101, v95, v103 op_sel:[1,1,0] op_sel_hi:[0,1,1] neg_lo:[1,0,0]\n"
"ds_write2_b32 v155, v90, v102 offset0:0 offset1:68\n"
"ds_write2_b32 v155, v91, v103 offset0:136 offset1:204\n"
"ds_read_b128 v[68:71], v141 offset:0\n"
"ds_read_b128 v[72:75], v141 offset:64\n"
"ds_read_b128 v[76:79], v141 offset:128\n"
"ds_read_b128 v[80:83], v141 offset:192\n"
"v_mul_f32 v116, v115, v115\n"
"v_add_f32 v117, v114, v114\n"
"v_fma_f32 v116, v114, v114, -v116\n"
"v_mul_f32 v117, v117, v115\n"
"v_mul_f32 v120, v117, v117\n"
"v_add_f32 v121, v116, v116\n"
"v_fma_f32 v120, v116, v116, -v120\n"
"v_mul_f32 v121, v121, v117\n"
"v_mul_f32 v118, v117, v115\n"
"v_mul_f32 v119, v116, v115\n"
"v_fma_f32 v118, v116, v114, -v118\n"
"v_fmac_f32 v119, v117, v114\n"
"v_cvt_pk_f16_f32 v92, v114, v115\n"
"v_cvt_pk_f16_f32 v93, v116, v117\n"
"v_cvt_pk_f16_f32 v95, v120, v121\n"
"v_cvt_pk_f16_f32 v94, v118, v119\n"
"v_mul_f32 v124, v121, v115\n"
"v_mul_f32 v125, v120, v115\n"
"v_fma_f32 v124, v120, v114, -v124\n"
"v_fmac_f32 v125, v121, v114\n"
"v_cvt_pk_f16_f32 v96, v124, v125\n"
"v_mul_f32 v126, v121, v117\n"
"v_mul_f32 v127, v120, v117\n"
"v_fma_f32 v126, v120, v116, -v126\n"
"v_fmac_f32 v127, v121, v116\n"
"v_cvt_pk_f16_f32 v97, v126, v127\n"
"v_mul_f32 v124, v121, v119\n"
"v_mul_f32 v125, v120, v119\n"
"v_fma_f32 v124, v120, v118, -v124\n"
"v_fmac_f32 v125, v121, v118\n"
"v_cvt_pk_f16_f32 v98, v124, v125\n"
"v_mul_f32 v126, v121, v121\n"
"v_mul_f32 v127, v120, v121\n"
"v_fma_f32 v126, v120, v120, -v126\n"
"v_fmac_f32 v127, v121, v120\n"
"v_cvt_pk_f16_f32 v99, v126, v127\n"
"s_setprio 3\n"
"s_waitcnt lgkmcnt(3)\n"
"v_mfma_f32_16x16x32_f16 v[144:147], v[68:71], v[52:55], 0\n"
"v_pk_mul_f16 v100, v84, v92 op_sel:[0,0] op_sel_hi:[1,0]\n"
"v_pk_mul_f16 v101, v85, v92 op_sel:[0,0] op_sel_hi:[1,0]\n"
"s_waitcnt lgkmcnt(2)\n"
"v_mfma_f32_16x16x32_f16 v[144:147], v[72:75], v[56:59], v[144:147]\n"
"v_pk_fma_f16 v100, v84, v92, v100 op_sel:[1,1,0] op_sel_hi:[0,1,1] neg_lo:[1,0,0]\n"
"v_pk_fma_f16 v101, v85, v92, v101 op_sel:[1,1,0] op_sel_hi:[0,1,1] neg_lo:[1,0,0]\n"
"s_waitcnt lgkmcnt(1)\n"
"v_mfma_f32_16x16x32_f16 v[144:147], v[76:79], v[60:63], v[144:147]\n"
"v_pk_mul_f16 v102, v86, v92 op_sel:[0,0] op_sel_hi:[1,0]\n"
"v_pk_mul_f16 v103, v87, v92 op_sel:[0,0] op_sel_hi:[1,0]\n"
"s_waitcnt lgkmcnt(0)\n"
"v_mfma_f32_16x16x32_f16 v[144:147], v[80:83], v[64:67], v[144:147]\n"
"s_setprio 0\n"
"v_pk_fma_f16 v102, v86, v92, v102 op_sel:[1,1,0] op_sel_hi:[0,1,1] neg_lo:[1,0,0]\n"
"v_pk_fma_f16 v103, v87, v92, v103 op_sel:[1,1,0] op_sel_hi:[0,1,1] neg_lo:[1,0,0]\n"
"ds_write2_b32 v152, v100, v101 offset0:0 offset1:68\n"
"v_pk_mul_f16 v100, v88, v92 op_sel:[0,0] op_sel_hi:[1,0]\n"
"v_pk_mul_f16 v101, v89, v92 op_sel:[0,0] op_sel_hi:[1,0]\n"
"v_pk_fma_f16 v100, v88, v92, v100 op_sel:[1,1,0] op_sel_hi:[0,1,1] neg_lo:[1,0,0]\n"
"v_pk_fma_f16 v101, v89, v92, v101 op_sel:[1,1,0] op_sel_hi:[0,1,1] neg_lo:[1,0,0]\n"
"ds_write2_b32 v152, v102, v103 offset0:136 offset1:204\n"
"v_pk_mul_f16 v102, v90, v92 op_sel:[0,0] op_sel_hi:[1,0]\n"
"v_pk_mul_f16 v103, v91, v92 op_sel:[0,0] op_sel_hi:[1,0]\n"
"v_pk_fma_f16 v102, v90, v92, v102 op_sel:[1,1,0] op_sel_hi:[0,1,1] neg_lo:[1,0,0]\n"
"v_pk_fma_f16 v103, v91, v92, v103 op_sel:[1,1,0] op_sel_hi:[0,1,1] neg_lo:[1,0,0]\n"
"ds_write2_b32 v153, v100, v101 offset0:0 offset1:68\n"
"v_pk_mul_f16 v100, v84, v93 op_sel:[0,0] op_sel_hi:[1,0]\n"
"v_pk_mul_f16 v101, v86, v93 op_sel:[0,0] op_sel_hi:[1,0]\n"
"v_pk_fma_f16 v100, v84, v93, v100 op_sel:[1,1,0] op_sel_hi:[0,1,1] neg_lo:[1,0,0]\n"
"v_pk_fma_f16 v101, v86, v93, v101 op_sel:[1,1,0] op_sel_hi:[0,1,1] neg_lo:[1,0,0]\n"
"ds_write2_b32 v153, v102, v103 offset0:136 offset1:204\n"
"v_pk_mul_f16 v102, v88, v93 op_sel:[0,0] op_sel_hi:[1,0]\n"
"v_pk_mul_f16 v103, v90, v93 op_sel:[0,0] op_sel_hi:[1,0]\n"
"v_pk_fma_f16 v102, v88, v93, v102 op_sel:[1,1,0] op_sel_hi:[0,1,1] neg_lo:[1,0,0]\n"
"v_pk_fma_f16 v103, v90, v93, v103 op_sel:[1,1,0] op_sel_hi:[0,1,1] neg_lo:[1,0,0]\n"
"ds_write2_b32 v154, v100, v101 offset0:0 offset1:68\n"
"v_pk_mul_f16 v100, v84, v94 op_sel:[0,0] op_sel_hi:[1,0]\n"
"v_pk_mul_f16 v101, v86, v94 op_sel:[0,0] op_sel_hi:[1,0]\n"
"v_pk_fma_f16 v100, v84, v94, v100 op_sel:[1,1,0] op_sel_hi:[0,1,1] neg_lo:[1,0,0]\n"
"v_pk_fma_f16 v101, v86, v94, v101 op_sel:[1,1,0] op_sel_hi:[0,1,1] neg_lo:[1,0,0]\n"
"ds_write2_b32 v154, v102, v103 offset0:136 offset1:204\n"
"v_pk_mul_f16 v102, v88, v94 op_sel:[0,0] op_sel_hi:[1,0]\n"
"v_pk_mul_f16 v103, v90, v94 op_sel:[0,0] op_sel_hi:[1,0]\n"
"v_pk_fma_f16 v102, v88, v94, v102 op_sel:[1,1,0] op_sel_hi:[0,1,1] neg_lo:[1,0,0]\n"
"v_pk_fma_f16 v103, v90, v94, v103 op_sel:[1,1,0] op_sel_hi:[0,1,1] neg_lo:[1,0,0]\n"
"ds_write2_b32 v155, v100, v101 offset0:0 offset1:68\n"
"ds_write2_b32 v155, v102, v103 offset0:136 offset1:204\n"
"ds_read_b128 v[68:71], v141 offset:0\n"
"ds_read_b128 v[72:75], v141 offset:64\n"
"ds_read_b128 v[76:79], v141 offset:128\n"
"ds_read_b128 v[80:83], v141 offset:192\n"
"v_pk_mul_f16 v100, v84, v95 op_sel:[0,0] op_sel_hi:[1,0]\n"
"v_pk_mul_f16 v101, v88, v95 op_sel:[0,0] op_sel_hi:[1,0]\n"
"v_pk_fma_f16 v100, v84, v95, v100 op_sel:[1,1,0] op_sel_hi:[0,1,1] neg_lo:[1,0,0]\n"
"v_pk_fma_f16 v101, v88, v95, v101 op_sel:[1,1,0] op_sel_hi:[0,1,1] neg_lo:[1,0,0]\n"
"v_pk_mul_f16 v102, v84, v96 op_sel:[0,0] op_sel_hi:[1,0]\n"
"v_pk_mul_f16 v103, v88, v96 op_sel:[0,0] op_sel_hi:[1,0]\n"
"v_pk_fma_f16 v102, v84, v96, v102 op_sel:[1,1,0] op_sel_hi:[0,1,1] neg_lo:[1,0,0]\n"
"v_pk_fma_f16 v103, v88, v96, v103 op_sel:[1,1,0] op_sel_hi:[0,1,1] neg_lo:[1,0,0]\n"
"v_pk_mul_f16 v104, v84, v97 op_sel:[0,0] op_sel_hi:[1,0]\n"
"v_pk_mul_f16 v105, v88, v97 op_sel:[0,0] op_sel_hi:[1,0]\n"
"v_pk_fma_f16 v104, v84, v97, v104 op_sel:[1,1,0] op_sel_hi:[0,1,1] neg_lo:[1,0,0]\n"
"v_pk_fma_f16 v105, v88, v97, v105 op_sel:[1,1,0] op_sel_hi:[0,1,1] neg_lo:[1,0,0]\n"
"v_pk_mul_f16 v106, v84, v98 op_sel:[0,0] op_sel_hi:[1,0]\n"
"v_pk_mul_f16 v107, v88, v98 op_sel:[0,0] op_sel_hi:[1,0]\n"
"v_pk_fma_f16 v106, v84, v98, v106 op_sel:[1,1,0] op_sel_hi:[0,1,1] neg_lo:[1,0,0]\n"
"v_pk_fma_f16 v107, v88, v98, v107 op_sel:[1,1,0] op_sel_hi:[0,1,1] neg_lo:[1,0,0]\n"
"v_pk_mul_f16 v108, v84, v99 op_sel:[0,0] op_sel_hi:[1,0]\n"
"v_pk_mul_f16 v109, v88, v99 op_sel:[0,0] op_sel_hi:[1,0]\n"
"v_pk_fma_f16 v108, v84, v99, v108 op_sel:[1,1,0] op_sel_hi:[0,1,1] neg_lo:[1,0,0]\n"
"v_pk_fma_f16 v109, v88, v99, v109 op_sel:[1,1,0] op_sel_hi:[0,1,1] neg_lo:[1,0,0]\n"
"s_setprio 3\n"
"s_waitcnt lgkmcnt(3)\n"
"v_mfma_f32_16x16x32_f16 v[148:151], v[68:71], v[52:55], 0\n"
"ds_write2_b32 v152, v100, v101 offset0:0 offset1:68\n"
"s_waitcnt lgkmcnt(3)\n"
"v_mfma_f32_16x16x32_f16 v[148:151], v[72:75], v[56:59], v[148:151]\n"
"ds_write2_b32 v152, v102, v103 offset0:136 offset1:204\n"
"s_waitcnt lgkmcnt(3)\n"
"v_mfma_f32_16x16x32_f16 v[148:151], v[76:79], v[60:63], v[148:151]\n"
"ds_write2_b32 v153, v104, v105 offset0:0 offset1:68\n"
"s_waitcnt lgkmcnt(3)\n"
"v_mfma_f32_16x16x32_f16 v[148:151], v[80:83], v[64:67], v[148:151]\n"
"s_setprio 0\n"
"ds_write2_b32 v153, v106, v107 offset0:136 offset1:204\n"
"ds_write2_b32 v154, v108, v109 offset0:0 offset1:68\n"
"ds_read_b128 v[68:71], v141 offset:0\n"
"ds_read_b128 v[72:75], v141 offset:64\n"
"ds_read_b128 v[76:79], v141 offset:128\n"
"ds_read_b128 v[80:83], v141 offset:192\n"
"v_fmac_f32 v128, s50, v144\n"
"v_fmac_f32 v129, s50, v145\n"
"v_fmac_f32 v130, s50, v146\n"
"v_fmac_f32 v131, s50, v147\n"
"v_fmac_f32 v132, s50, v148\n"
"v_fmac_f32 v133, s50, v149\n"
"v_fmac_f32 v134, s50, v150\n"
"v_fmac_f32 v135, s50, v151\n"
"s_setprio 3\n"
"s_waitcnt lgkmcnt(3)\n"
"v_mfma_f32_16x16x32_f16 v[48:51], v[68:71], v[52:55], 0\n"
"s_waitcnt lgkmcnt(2)\n"
"v_mfma_f32_16x16x32_f16 v[48:51], v[72:75], v[56:59], v[48:51]\n"
"s_waitcnt lgkmcnt(1)\n"
"v_mfma_f32_16x16x32_f16 v[48:51], v[76:79], v[60:63], v[48:51]\n"
"s_waitcnt lgkmcnt(0)\n"
"v_mfma_f32_16x16x32_f16 v[48:51], v[80:83], v[64:67], v[48:51]\n"
"s_setprio 0\n"
"s_sub_u32 s55, s55, 1\n"
"s_nop 5\n"
"v_fmac_f32 v136, s50, v48\n"
"v_fmac_f32 v137, s50, v49\n"
"v_fmac_f32 v138, s50, v50\n"
"v_fmac_f32 v139, s50, v51\n"
"s_cbranch_scc0 .Lgo_312\n"
"s_setpc_b64 s[60:61]\n"
".Lgo_312:\n"
"s_waitcnt vmcnt(0)\n"
"v_mov_b32 v32, v40\n"
"v_mov_b32 v40, v44\n"
"v_mov_b32 v33, v41\n"
"v_mov_b32 v41, v45\n"
"v_mov_b32 v34, v42\n"
"v_mov_b32 v42, v46\n"
"v_mov_b32 v35, v43\n"
"v_mov_b32 v43, v47\n"
"s_cmp_lg_u32 s55, 0\n"
"s_cbranch_scc1 .Lkg_311\n"
"s_mov_b32 s40, 0\n"
"s_mov_b32 s40, 1\n"
"v_lshlrev_b32 v32, 16, v32\n"
"v_lshlrev_b32 v33, 16, v33\n"
"v_lshlrev_b32 v34, 16, v34\n"
"v_lshlrev_b32 v35, 16, v35\n"
"v_mul_f32 v26, v142, v34\n"
"v_mul_f32 v27, v142, v35\n"
"v_max3_f32 v30, |v26|, |v27|, 0\n"
"s_nop 1\n"
"v_max_u32_dpp v30, v30, v30 row_ror:8 row_mask:0xf bank_mask:0xf\n"
"v_med3_f32 v32, v32, s52, v31\n"
"s_nop 0\n"
"v_max_u32_dpp v30, v30, v30 row_ror:4 row_mask:0xf bank_mask:0xf\n"
"v_mul_f32 v28, s53, v32\n"
"s_nop 0\n"
"v_max_u32_dpp v30, v30, v30 row_ror:2 row_mask:0xf bank_mask:0xf\n"
"v_exp_f32 v29, v28\n"
"s_nop 0\n"
"v_max_u32_dpp v30, v30, v30 row_ror:1 row_mask:0xf bank_mask:0xf\n"
"v_fma_f32 v28, v32, s53, -v28\n"
"v_fmac_f32 v28, 0x32a57060, v32\n"
"v_mov_b32 v34, v30\n"
"v_fma_f32 v28, v28, s54, 1.0\n"
"s_nop 0\n"
"v_permlane16_swap_b32 v30, v34\n"
"v_mul_f32 v32, v29, v28\n"
"v_max_u32 v30, v30, v34\n"
"v_cvt_pk_bf16_f32 v32, v32, v32\n"
"v_lshlrev_b32 v32, 16, v32\n"
"v_mov_b32 v34, v30\n"
"v_mul_f32 v25, v142, v33\n"
"s_nop 0\n"
"v_permlane32_swap_b32 v30, v34\n"
"v_mul_f32 v24, v142, -v32\n"
"v_max_u32 v30, v30, v34\n"
"v_mul_f32 v25, 0.15915494, v25\n"
"v_mul_f32 v24, s53, v24\n"
"v_readfirstlane_b32 s51, v30\n"
"s_nop 2\n"
"s_bfe_u32 s50, s51, 0x80017\n"
"s_max_i32 s50, s50, 37\n"
"s_min_i32 s50, s50, 217\n"
"s_cmp_gt_i32 s51, 0\n"
"s_cselect_b32 s50, s50, 127\n"
"s_sub_u32 s51, 267, s50\n"
"s_lshl_b32 s51, s51, 23\n"
"s_sub_u32 s50, s50, 27\n"
"s_lshl_b32 s50, s50, 23\n"
"v_mul_f32 v26, s51, v26\n"
"v_mul_f32 v27, s51, v27\n"
"v_mul_f32 v111, 0x41800000, v25\n"
"v_mul_f32 v100, 0x41800000, v24\n"
"v_fract_f32 v111, v111\n"
"v_exp_f32 v100, v100\n"
"v_cos_f32 v110, v111\n"
"v_sin_f32 v111, -v111\n"
"v_mul_f32 v110, v110, v100\n"
"v_mul_f32 v111, v111, v100\n"
"v_fract_f32 v113, v25\n"
"v_exp_f32 v100, v24\n"
"v_cos_f32 v112, v113\n"
"v_sin_f32 v113, v113\n"
"v_mul_f32 v112, v112, v100\n"
"v_mul_f32 v113, v113, v100\n"
"v_mul_f32 v115, 0x43800000, v25\n"
"v_mul_f32 v100, 0x43800000, v24\n"
"v_fract_f32 v115, v115\n"
"v_exp_f32 v100, v100\n"
"v_cos_f32 v114, v115\n"
"v_sin_f32 v115, v115\n"
"v_mul_f32 v114, v114, v100\n"
"v_mul_f32 v115, v115, v100\n"
"v_mul_f32 v116, v111, v111\n"
"v_add_f32 v117, v110, v110\n"
"v_fma_f32 v116, v110, v110, -v116\n"
"v_mul_f32 v117, v117, v111\n"
"v_mul_f32 v120, v117, v117\n"
"v_add_f32 v121, v116, v116\n"
"v_fma_f32 v120, v116, v116, -v120\n"
"v_mul_f32 v121, v121, v117\n"
"v_mul_f32 v118, v117, v111\n"
"v_mul_f32 v119, v116, v111\n"
"v_fma_f32 v118, v116, v110, -v118\n"
"v_fmac_f32 v119, v117, v110\n"
"v_mul_f32 v122, v121, v121\n"
"v_add_f32 v123, v120, v120\n"
"v_fma_f32 v122, v120, v120, -v122\n"
"v_mul_f32 v123, v123, v121\n"
"v_mov_b32 v84, 0x7400\n"
"v_mul_f32 v124, 0x46800000, v110\n"
"v_mul_f32 v125, 0x46800000, v111\n"
"v_cvt_pk_f16_f32 v100, v124, v125\n"
"v_mul_f32 v124, 0x46800000, v116\n"
"v_mul_f32 v125, 0x46800000, v117\n"
"v_cvt_pk_f16_f32 v85, v124, v125\n"
"v_mul_f32 v124, 0x46800000, v118\n"
"v_mul_f32 v125, 0x46800000, v119\n"
"v_cvt_pk_f16_f32 v101, v124, v125\n"
"v_cvt_pk_f16_f32 v93, v120, v121\n"
"v_cvt_pk_f16_f32 v94, v122, v123\n"
"v_mul_f32 v124, v123, v121\n"
"v_mul_f32 v125, v122, v121\n"
"v_fma_f32 v124, v122, v120, -v124\n"
"v_fmac_f32 v125, v123, v120\n"
"v_cvt_pk_f16_f32 v95, v124, v125\n"
"ds_write2_b32 v152, v84, v100 offset0:0 offset1:68\n"
"v_pk_mul_f16 v86, v84, v93 op_sel:[0,0] op_sel_hi:[1,0]\n"
"v_pk_mul_f16 v102, v100, v93 op_sel:[0,0] op_sel_hi:[1,0]\n"
"v_pk_fma_f16 v86, v84, v93, v86 op_sel:[1,1,0] op_sel_hi:[0,1,1] neg_lo:[1,0,0]\n"
"v_pk_fma_f16 v102, v100, v93, v102 op_sel:[1,1,0] op_sel_hi:[0,1,1] neg_lo:[1,0,0]\n"
"ds_write2_b32 v152, v85, v101 offset0:136 offset1:204\n"
"v_pk_mul_f16 v87, v85, v93 op_sel:[0,0] op_sel_hi:[1,0]\n"
"v_pk_mul_f16 v103, v101, v93 op_sel:[0,0] op_sel_hi:[1,0]\n"
"v_pk_fma_f16 v87, v85, v93, v87 op_sel:[1,1,0] op_sel_hi:[0,1,1] neg_lo:[1,0,0]\n"
"v_pk_fma_f16 v103, v101, v93, v103 op_sel:[1,1,0] op_sel_hi:[0,1,1] neg_lo:[1,0,0]\n"
"ds_write2_b32 v153, v86, v102 offset0:0 offset1:68\n"
"v_pk_mul_f16 v88, v84, v94 op_sel:[0,0] op_sel_hi:[1,0]\n"
"v_pk_mul_f16 v102, v100, v94 op_sel:[0,0] op_sel_hi:[1,0]\n"
"v_pk_fma_f16 v88, v84, v94, v88 op_sel:[1,1,0] op_sel_hi:[0,1,1] neg_lo:[1,0,0]\n"
"v_pk_fma_f16 v102, v100, v94, v102 op_sel:[1,1,0] op_sel_hi:[0,1,1] neg_lo:[1,0,0]\n"
"ds_write2_b32 v153, v87, v103 offset0:136 offset1:204\n"
"v_pk_mul_f16 v89, v85, v94 op_sel:[0,0] op_sel_hi:[1,0]\n"
"v_pk_mul_f16 v103, v101, v94 op_sel:[0,0] op_sel_hi:[1,0]\n"
"v_pk_fma_f16 v89, v85, v94, v89 op_sel:[1,1,0] op_sel_hi:[0,1,1] neg_lo:[1,0,0]\n"
"v_pk_fma_f16 v103, v101, v94, v103 op_sel:[1,1,0] op_sel_hi:[0,1,1] neg_lo:[1,0,0]\n"
"ds_write2_b32 v154, v88, v102 offset0:0 offset1:68\n"
"v_pk_mul_f16 v90, v84, v95 op_sel:[0,0] op_sel_hi:[1,0]\n"
"v_pk_mul_f16 v102, v100, v95 op_sel:[0,0] op_sel_hi:[1,0]\n"
"v_pk_fma_f16 v90, v84, v95, v90 op_sel:[1,1,0] op_sel_hi:[0,1,1] neg_lo:[1,0,0]\n"
"v_pk_fma_f16 v102, v100, v95, v102 op_sel:[1,1,0] op_sel_hi:[0,1,1] neg_lo:[1,0,0]\n"
"ds_write2_b32 v154, v89, v103 offset0:136 offset1:204\n"
"v_pk_mul_f16 v91, v85, v95 op_sel:[0,0] op_sel_hi:[1,0]\n"
"v_pk_mul_f16 v103, v101, v95 op_sel:[0,0] op_sel_hi:[1,0]\n"
"v_pk_fma_f16 v91, v85, v95, v91 op_sel:[1,1,0] op_sel_hi:[0,1,1] neg_lo:[1,0,0]\n"
"v_pk_fma_f16 v103, v101, v95, v103 op_sel:[1,1,0] op_sel_hi:[0,1,1] neg_lo:[1,0,0]\n"
"ds_write2_b32 v155, v90, v102 offset0:0 offset1:68\n"
"ds_write2_b32 v155, v91, v103 offset0:136 offset1:204\n"
"ds_read_b128 v[52:55], v141 offset:0\n"
"ds_read_b128 v[56:59], v141 offset:64\n"
"ds_read_b128 v[60:63], v141 offset:128\n"
"ds_read_b128 v[64:67], v141 offset:192\n"
"v_mul_f32 v116, v113, v113\n"
"v_add_f32 v117, v112, v112\n"
"v_fma_f32 v116, v112, v112, -v116\n"
"v_mul_f32 v117, v117, v113\n"
"v_mul_f32 v120, v117, v117\n"
"v_add_f32 v121, v116, v116\n"
"v_fma_f32 v120, v116, v116, -v120\n"
"v_mul_f32 v121, v121, v117\n"
"v_mul_f32 v118, v117, v113\n"
"s_setprio 3\n"
"buffer_load_dwordx4 v[44:47], v38, s[4:7], s33 offen sc1\n"
"s_setprio 0\n"
"v_mul_f32 v119, v116, v113\n"
"v_fma_f32 v118, v116, v112, -v118\n"
"v_fmac_f32 v119, v117, v112\n"
"v_mul_f32 v122, v121, v121\n"
"v_add_f32 v123, v120, v120\n"
"v_fma_f32 v122, v120, v120, -v122\n"
"v_mul_f32 v123, v123, v121\n"
"v_cvt_pk_f16_f32 v84, v26, v27\n"
"v_mul_f32 v124, v27, v113\n"
"v_mul_f32 v125, v26, v113\n"
"v_fma_f32 v124, v26, v112, -v124\n"
"v_fmac_f32 v125, v27, v112\n"
"v_cvt_pk_f16_f32 v100, v124, v125\n"
"v_mul_f32 v124, v27, v117\n"
"v_mul_f32 v125, v26, v117\n"
"v_fma_f32 v124, v26, v116, -v124\n"
"v_fmac_f32 v125, v27, v116\n"
"v_cvt_pk_f16_f32 v85, v124, v125\n"
"v_mul_f32 v124, v27, v119\n"
"v_mul_f32 v125, v26, v119\n"
"v_fma_f32 v124, v26, v118, -v124\n"
"v_fmac_f32 v125, v27, v118\n"
"v_cvt_pk_f16_f32 v101, v124, v125\n"
"v_cvt_pk_f16_f32 v93, v120, v121\n"
"v_cvt_pk_f16_f32 v94, v122, v123\n"
"v_mul_f32 v124, v123, v121\n"
"v_mul_f32 v125, v122, v121\n"
"v_fma_f32 v124, v122, v120, -v124\n"
"v_fmac_f32 v125, v123, v120\n"
"v_cvt_pk_f16_f32 v95, v124, v125\n"
"ds_write2_b32 v152, v84, v100 offset0:0 offset1:68\n"
"v_pk_mul_f16 v86, v84, v93 op_sel:[0,0] op_sel_hi:[1,0]\n"
"v_pk_mul_f16 v102, v100, v93 op_sel:[0,0] op_sel_hi:[1,0]\n"
"v_pk_fma_f16 v86, v84, v93, v86 op_sel:[1,1,0] op_sel_hi:[0,1,1] neg_lo:[1,0,0]\n"
"v_pk_fma_f16 v102, v100, v93, v102 op_sel:[1,1,0] op_sel_hi:[0,1,1] neg_lo:[1,0,0]\n"
"ds_write2_b32 v152, v85, v101 offset0:136 offset1:204\n"
"v_pk_mul_f16 v87, v85, v93 op_sel:[0,0] op_sel_hi:[1,0]\n"
"v_pk_mul_f16 v103, v101, v93 op_sel:[0,0] op_sel_hi:[1,0]\n"
"v_pk_fma_f16 v87, v85, v93, v87 op_sel:[1,1,0] op_sel_hi:[0,1,1] neg_lo:[1,0,0]\n"
"v_pk_fma_f16 v103, v101, v93, v103 op_sel:[1,1,0] op_sel_hi:[0,1,1] neg_lo:[1,0,0]\n"
"ds_write2_b32 v153, v86, v102 offset0:0 offset1:68\n"
"v_pk_mul_f16 v88, v84, v94 op_sel:[0,0] op_sel_hi:[1,0]\n"
"v_pk_mul_f16 v102, v100, v94 op_sel:[0,0] op_sel_hi:[1,0]\n"
"v_pk_fma_f16 v88, v84, v94, v88 op_sel:[1,1,0] op_sel_hi:[0,1,1] neg_lo:[1,0,0]\n"
"v_pk_fma_f16 v102, v100, v94, v102 op_sel:[1,1,0] op_sel_hi:[0,1,1] neg_lo:[1,0,0]\n"
"ds_write2_b32 v153, v87, v103 offset0:136 offset1:204\n"
"v_pk_mul_f16 v89, v85, v94 op_sel:[0,0] op_sel_hi:[1,0]\n"
"v_pk_mul_f16 v103, v101, v94 op_sel:[0,0] op_sel_hi:[1,0]\n"
"v_pk_fma_f16 v89, v85, v94, v89 op_sel:[1,1,0] op_sel_hi:[0,1,1] neg_lo:[1,0,0]\n"
"v_pk_fma_f16 v103, v101, v94, v103 op_sel:[1,1,0] op_sel_hi:[0,1,1] neg_lo:[1,0,0]\n"
"ds_write2_b32 v154, v88, v102 offset0:0 offset1:68\n"
"v_pk_mul_f16 v90, v84, v95 op_sel:[0,0] op_sel_hi:[1,0]\n"
"v_pk_mul_f16 v102, v100, v95 op_sel:[0,0] op_sel_hi:[1,0]\n"
"v_pk_fma_f16 v90, v84, v95, v90 op_sel:[1,1,0] op_sel_hi:[0,1,1] neg_lo:[1,0,0]\n"
"s_waitcnt vmcnt(0)\n"
"v_xor_b32 v44, s16, v44\n"
"v_xor_b32 v45, s16, v45\n"
"v_xor_b32 v46, s16, v46\n"
"v_xor_b32 v47, s16, v47\n"
"v_or_b32 v44, v44, v45\n"
"v_or3_b32 v44, v44, v46, v47\n"
"v_cmp_ne_u32 vcc, 0, v44\n"
"s_cbranch_vccnz .Lsl_307\n"
".Lbk_308:\n"
"v_pk_fma_f16 v102, v100, v95, v102 op_sel:[1,1,0] op_sel_hi:[0,1,1] neg_lo:[1,0,0]\n"
"ds_write2_b32 v154, v89, v103 offset0:136 offset1:204\n"
"v_pk_mul_f16 v91, v85, v95 op_sel:[0,0] op_sel_hi:[1,0]\n"
"v_pk_mul_f16 v103, v101, v95 op_sel:[0,0] op_sel_hi:[1,0]\n"
"v_pk_fma_f16 v91, v85, v95, v91 op_sel:[1,1,0] op_sel_hi:[0,1,1] neg_lo:[1,0,0]\n"
"v_pk_fma_f16 v103, v101, v95, v103 op_sel:[1,1,0] op_sel_hi:[0,1,1] neg_lo:[1,0,0]\n"
"buffer_load_dwordx4 v[156:159], v2, s[4:7], s32 offen offset:0 sc1\n"
"ds_write2_b32 v155, v90, v102 offset0:0 offset1:68\n"
"ds_write2_b32 v155, v91, v103 offset0:136 offset1:204\n"
"ds_read_b128 v[68:71], v141 offset:0\n"
"ds_read_b128 v[72:75], v141 offset:64\n"
"ds_read_b128 v[76:79], v141 offset:128\n"
"ds_read_b128 v[80:83], v141 offset:192\n"
"buffer_load_dwordx4 v[160:163], v2, s[4:7], s32 offen offset:1024 sc1\n"
"v_mul_f32 v116, v115, v115\n"
"v_add_f32 v117, v114, v114\n"
"v_fma_f32 v116, v114, v114, -v116\n"
"v_mul_f32 v117, v117, v115\n"
"v_mul_f32 v120, v117, v117\n"
"v_add_f32 v121, v116, v116\n"
"buffer_load_dwordx4 v[164:167], v2, s[4:7], s32 offen offset:2048 sc1\n"
"v_fma_f32 v120, v116, v116, -v120\n"
"v_mul_f32 v121, v121, v117\n"
"v_mul_f32 v118, v117, v115\n"
"v_mul_f32 v119, v116, v115\n"
"v_fma_f32 v118, v116, v114, -v118\n"
"v_fmac_f32 v119, v117, v114\n"
"buffer_load_dwordx4 v[168:171], v2, s[4:7], s32 offen offset:3072 sc1\n"
"v_cvt_pk_f16_f32 v92, v114, v115\n"
"v_cvt_pk_f16_f32 v93, v116, v117\n"
"v_cvt_pk_f16_f32 v95, v120, v121\n"
"v_cvt_pk_f16_f32 v94, v118, v119\n"
"v_mul_f32 v124, v121, v115\n"
"v_mul_f32 v125, v120, v115\n"
"s_add_u32 s32, s32, 4096\n"
"buffer_load_dwordx4 v[172:175], v2, s[4:7], s32 offen offset:0 sc1\n"
"v_fma_f32 v124, v120, v114, -v124\n"
"v_fmac_f32 v125, v121, v114\n"
"v_cvt_pk_f16_f32 v96, v124, v125\n"
"v_mul_f32 v126, v121, v117\n"
"v_mul_f32 v127, v120, v117\n"
"v_fma_f32 v126, v120, v116, -v126\n"
"buffer_load_dwordx4 v[176:179], v2, s[4:7], s32 offen offset:1024 sc1\n"
"v_fmac_f32 v127, v121, v116\n"
"v_cvt_pk_f16_f32 v97, v126, v127\n"
"v_mul_f32 v124, v121, v119\n"
"v_mul_f32 v125, v120, v119\n"
"v_fma_f32 v124, v120, v118, -v124\n"
"v_fmac_f32 v125, v121, v118\n"
"s_waitcnt vmcnt(5)\n"
"buffer_load_dwordx4 v[180:183], v2, s[4:7], s32 offen offset:2048 sc1\n"
"v_cvt_pk_f16_f32 v98, v124, v125\n"
"v_mul_f32 v126, v121, v121\n"
"v_mul_f32 v127, v120, v121\n"
"v_fma_f32 v126, v120, v120, -v126\n"
"v_fmac_f32 v127, v121, v120\n"
"v_cvt_pk_f16_f32 v99, v126, v127\n"
"s_waitcnt vmcnt(5)\n"
"buffer_load_dwordx4 v[184:187], v2, s[4:7], s32 offen offset:3072 sc1\n"
"s_setprio 3\n"
"s_waitcnt lgkmcnt(3)\n"
"v_mfma_f32_16x16x32_f16 v[144:147], v[68:71], v[52:55], 0\n"
"v_pk_mul_f16 v100, v84, v92 op_sel:[0,0] op_sel_hi:[1,0]\n"
"v_pk_mul_f16 v101, v85, v92 op_sel:[0,0] op_sel_hi:[1,0]\n"
"s_waitcnt lgkmcnt(2)\n"
"v_mfma_f32_16x16x32_f16 v[144:147], v[72:75], v[56:59], v[144:147]\n"
"v_pk_fma_f16 v100, v84, v92, v100 op_sel:[1,1,0] op_sel_hi:[0,1,1] neg_lo:[1,0,0]\n"
"s_add_u32 s32, s32, 4096\n"
"s_waitcnt vmcnt(5)\n"
"buffer_load_dwordx4 v[188:191], v2, s[4:7], s32 offen offset:0 sc1\n"
"v_pk_fma_f16 v101, v85, v92, v101 op_sel:[1,1,0] op_sel_hi:[0,1,1] neg_lo:[1,0,0]\n"
"s_waitcnt lgkmcnt(1)\n"
"v_mfma_f32_16x16x32_f16 v[144:147], v[76:79], v[60:63], v[144:147]\n"
"v_pk_mul_f16 v102, v86, v92 op_sel:[0,0] op_sel_hi:[1,0]\n"
"v_pk_mul_f16 v103, v87, v92 op_sel:[0,0] op_sel_hi:[1,0]\n"
"s_waitcnt lgkmcnt(0)\n"
"v_mfma_f32_16x16x32_f16 v[144:147], v[80:83], v[64:67], v[144:147]\n"
"s_setprio 0\n"
"s_waitcnt vmcnt(5)\n"
"buffer_load_dwordx4 v[192:195], v2, s[4:7], s32 offen offset:1024 sc1\n"
"v_pk_fma_f16 v102, v86, v92, v102 op_sel:[1,1,0] op_sel_hi:[0,1,1] neg_lo:[1,0,0]\n"
"v_pk_fma_f16 v103, v87, v92, v103 op_sel:[1,1,0] op_sel_hi:[0,1,1] neg_lo:[1,0,0]\n"
"ds_write2_b32 v152, v100, v101 offset0:0 offset1:68\n"
"v_pk_mul_f16 v100, v88, v92 op_sel:[0,0] op_sel_hi:[1,0]\n"
"v_pk_mul_f16 v101, v89, v92 op_sel:[0,0] op_sel_hi:[1,0]\n"
"v_pk_fma_f16 v100, v88, v92, v100 op_sel:[1,1,0] op_sel_hi:[0,1,1] neg_lo:[1,0,0]\n"
"s_waitcnt vmcnt(5)\n"
"buffer_load_dwordx4 v[196:199], v2, s[4:7], s32 offen offset:2048 sc1\n"
"v_pk_fma_f16 v101, v89, v92, v101 op_sel:[1,1,0] op_sel_hi:[0,1,1] neg_lo:[1,0,0]\n"
"ds_write2_b32 v152, v102, v103 offset0:136 offset1:204\n"
"v_pk_mul_f16 v102, v90, v92 op_sel:[0,0] op_sel_hi:[1,0]\n"
"v_pk_mul_f16 v103, v91, v92 op_sel:[0,0] op_sel_hi:[1,0]\n"
"v_pk_fma_f16 v102, v90, v92, v102 op_sel:[1,1,0] op_sel_hi:[0,1,1] neg_lo:[1,0,0]\n"
"v_pk_fma_f16 v103, v91, v92, v103 op_sel:[1,1,0] op_sel_hi:[0,1,1] neg_lo:[1,0,0]\n"
"s_waitcnt vmcnt(5)\n"
"buffer_load_dwordx4 v[200:203], v2, s[4:7], s32 offen offset:3072 sc1\n"
"ds_write2_b32 v153, v100, v101 offset0:0 offset1:68\n"
"v_pk_mul_f16 v100, v84, v93 op_sel:[0,0] op_sel_hi:[1,0]\n"
"v_pk_mul_f16 v101, v86, v93 op_sel:[0,0] op_sel_hi:[1,0]\n"
"v_pk_fma_f16 v100, v84, v93, v100 op_sel:[1,1,0] op_sel_hi:[0,1,1] neg_lo:[1,0,0]\n"
"v_pk_fma_f16 v101, v86, v93, v101 op_sel:[1,1,0] op_sel_hi:[0,1,1] neg_lo:[1,0,0]\n"
"ds_write2_b32 v153, v102, v103 offset0:136 offset1:204\n"
"s_add_u32 s32, s32, 4096\n"
"s_waitcnt vmcnt(5)\n"
"buffer_load_dwordx4 v[204:207], v2, s[4:7], s32 offen offset:0 sc1\n"
"v_pk_mul_f16 v102, v88, v93 op_sel:[0,0] op_sel_hi:[1,0]\n"
"v_pk_mul_f16 v103, v90, v93 op_sel:[0,0] op_sel_hi:[1,0]\n"
"v_pk_fma_f16 v102, v88, v93, v102 op_sel:[1,1,0] op_sel_hi:[0,1,1] neg_lo:[1,0,0]\n"
"v_pk_fma_f16 v103, v90, v93, v103 op_sel:[1,1,0] op_sel_hi:[0,1,1] neg_lo:[1,0,0]\n"
"ds_write2_b32 v154, v100, v101 offset0:0 offset1:68\n"
"v_pk_mul_f16 v100, v84, v94 op_sel:[0,0] op_sel_hi:[1,0]\n"
"s_waitcnt vmcnt(5)\n"
"buffer_load_dwordx4 v[208:211], v2, s[4:7], s32 offen offset:1024 sc1\n"
"v_pk_mul_f16 v101, v86, v94 op_sel:[0,0] op_sel_hi:[1,0]\n"
"v_pk_fma_f16 v100, v84, v94, v100 op_sel:[1,1,0] op_sel_hi:[0,1,1] neg_lo:[1,0,0]\n"
"v_pk_fma_f16 v101, v86, v94, v101 op_sel:[1,1,0] op_sel_hi:[0,1,1] neg_lo:[1,0,0]\n"
"ds_write2_b32 v154, v102, v103 offset0:136 offset1:204\n"
"v_pk_mul_f16 v102, v88, v94 op_sel:[0,0] op_sel_hi:[1,0]\n"
"v_pk_mul_f16 v103, v90, v94 op_sel:[0,0] op_sel_hi:[1,0]\n"
"s_waitcnt vmcnt(5)\n"
"buffer_load_dwordx4 v[212:215], v2, s[4:7], s32 offen offset:2048 sc1\n"
"v_pk_fma_f16 v102, v88, v94, v102 op_sel:[1,1,0] op_sel_hi:[0,1,1] neg_lo:[1,0,0]\n"
"v_pk_fma_f16 v103, v90, v94, v103 op_sel:[1,1,0] op_sel_hi:[0,1,1] neg_lo:[1,0,0]\n"
"ds_write2_b32 v155, v100, v101 offset0:0 offset1:68\n"
"ds_write2_b32 v155, v102, v103 offset0:136 offset1:204\n"
"ds_read_b128 v[68:71], v141 offset:0\n"
"ds_read_b128 v[72:75], v141 offset:64\n"
"s_waitcnt vmcnt(5)\n"
"buffer_load_dwordx4 v[216:219], v2, s[4:7], s32 offen offset:3072 sc1\n"
"ds_read_b128 v[76:79], v141 offset:128\n"
"ds_read_b128 v[80:83], v141 offset:192\n"
"v_pk_mul_f16 v100, v84, v95 op_sel:[0,0] op_sel_hi:[1,0]\n"
"v_pk_mul_f16 v101, v88, v95 op_sel:[0,0] op_sel_hi:[1,0]\n"
"v_pk_fma_f16 v100, v84, v95, v100 op_sel:[1,1,0] op_sel_hi:[0,1,1] neg_lo:[1,0,0]\n"
"v_pk_fma_f16 v101, v88, v95, v101 op_sel:[1,1,0] op_sel_hi:[0,1,1] neg_lo:[1,0,0]\n"
"s_add_u32 s32, s32, 4096\n"
"s_waitcnt vmcnt(5)\n"
"buffer_load_dwordx4 v[220:223], v2, s[4:7], s32 offen offset:0 sc1\n"
"v_pk_mul_f16 v102, v84, v96 op_sel:[0,0] op_sel_hi:[1,0]\n"
"v_pk_mul_f16 v103, v88, v96 op_sel:[0,0] op_sel_hi:[1,0]\n"
"v_pk_fma_f16 v102, v84, v96, v102 op_sel:[1,1,0] op_sel_hi:[0,1,1] neg_lo:[1,0,0]\n"
"v_pk_fma_f16 v103, v88, v96, v103 op_sel:[1,1,0] op_sel_hi:[0,1,1] neg_lo:[1,0,0]\n"
"v_pk_mul_f16 v104, v84, v97 op_sel:[0,0] op_sel_hi:[1,0]\n"
"v_pk_mul_f16 v105, v88, v97 op_sel:[0,0] op_sel_hi:[1,0]\n"
"s_waitcnt vmcnt(5)\n"
"buffer_load_dwordx4 v[224:227], v2, s[4:7], s32 offen offset:1024 sc1\n"
"v_pk_fma_f16 v104, v84, v97, v104 op_sel:[1,1,0] op_sel_hi:[0,1,1] neg_lo:[1,0,0]\n"
"v_pk_fma_f16 v105, v88, v97, v105 op_sel:[1,1,0] op_sel_hi:[0,1,1] neg_lo:[1,0,0]\n"
"v_pk_mul_f16 v106, v84, v98 op_sel:[0,0] op_sel_hi:[1,0]\n"
"v_pk_mul_f16 v107, v88, v98 op_sel:[0,0] op_sel_hi:[1,0]\n"
"v_pk_fma_f16 v106, v84, v98, v106 op_sel:[1,1,0] op_sel_hi:[0,1,1] neg_lo:[1,0,0]\n"
"v_pk_fma_f16 v107, v88, v98, v107 op_sel:[1,1,0] op_sel_hi:[0,1,1] neg_lo:[1,0,0]\n"
"s_waitcnt vmcnt(5)\n"
"buffer_load_dwordx4 v[228:231], v2, s[4:7], s32 offen offset:2048 sc1\n"
"v_pk_mul_f16 v108, v84, v99 op_sel:[0,0] op_sel_hi:[1,0]\n"
"v_pk_mul_f16 v109, v88, v99 op_sel:[0,0] op_sel_hi:[1,0]\n"
"v_pk_fma_f16 v108, v84, v99, v108 op_sel:[1,1,0] op_sel_hi:[0,1,1] neg_lo:[1,0,0]\n"
"v_pk_fma_f16 v109, v88, v99, v109 op_sel:[1,1,0] op_sel_hi:[0,1,1] neg_lo:[1,0,0]\n"
"s_setprio 3\n"
"s_waitcnt lgkmcnt(3)\n"
"v_mfma_f32_16x16x32_f16 v[148:151], v[68:71], v[52:55], 0\n"
"s_waitcnt vmcnt(5)\n"
"buffer_load_dwordx4 v[232:235], v2, s[4:7], s32 offen offset:3072 sc1\n"
"ds_write2_b32 v152, v100, v101 offset0:0 offset1:68\n"
"s_waitcnt lgkmcnt(3)\n"
"v_mfma_f32_16x16x32_f16 v[148:151], v[72:75], v[56:59], v[148:151]\n"
"ds_write2_b32 v152, v102, v103 offset0:136 offset1:204\n"
"s_waitcnt lgkmcnt(3)\n"
"v_mfma_f32_16x16x32_f16 v[148:151], v[76:79], v[60:63], v[148:151]\n"
"ds_write2_b32 v153, v104, v105 offset0:0 offset1:68\n"
"s_waitcnt lgkmcnt(3)\n"
"v_mfma_f32_16x16x32_f16 v[148:151], v[80:83], v[64:67], v[148:151]\n"
"s_add_u32 s32, s32, 4096\n"
"s_waitcnt vmcnt(5)\n"
"buffer_load_dwordx4 v[236:239], v2, s[4:7], s32 offen offset:0 sc1\n"
"s_setprio 0\n"
"ds_write2_b32 v153, v106, v107 offset0:136 offset1:204\n"
"ds_write2_b32 v154, v108, v109 offset0:0 offset1:68\n"
"ds_read_b128 v[68:71], v141 offset:0\n"
"ds_read_b128 v[72:75], v141 offset:64\n"
"ds_read_b128 v[76:79], v141 offset:128\n"
"s_waitcnt vmcnt(5)\n"
"buffer_load_dwordx4 v[240:243], v2, s[4:7], s32 offen offset:1024 sc1\n"
"ds_read_b128 v[80:83], v141 offset:192\n"
"v_fmac_f32 v128, s50, v144\n"
"v_fmac_f32 v129, s50, v145\n"
"v_fmac_f32 v130, s50, v146\n"
"v_fmac_f32 v131, s50, v147\n"
"v_fmac_f32 v132, s50, v148\n"
"s_waitcnt vmcnt(5)\n"
"buffer_load_dwordx4 v[244:247], v2, s[4:7], s32 offen offset:2048 sc1\n"
"v_fmac_f32 v133, s50, v149\n"
"v_fmac_f32 v134, s50, v150\n"
"v_fmac_f32 v135, s50, v151\n"
"s_setprio 3\n"
"s_waitcnt lgkmcnt(3)\n"
"v_mfma_f32_16x16x32_f16 v[48:51], v[68:71], v[52:55], 0\n"
"s_waitcnt lgkmcnt(2)\n"
"v_mfma_f32_16x16x32_f16 v[48:51], v[72:75], v[56:59], v[48:51]\n"
"s_waitcnt vmcnt(5)\n"
"buffer_load_dwordx4 v[248:251], v2, s[4:7], s32 offen offset:3072 sc1\n"
"s_waitcnt lgkmcnt(1)\n"
"v_mfma_f32_16x16x32_f16 v[48:51], v[76:79], v[60:63], v[48:51]\n"
"s_waitcnt lgkmcnt(0)\n"
"v_mfma_f32_16x16x32_f16 v[48:51], v[80:83], v[64:67], v[48:51]\n"
"s_setprio 0\n"
"s_nop 6\n"
"v_fmac_f32 v136, s50, v48\n"
"v_fmac_f32 v137, s50, v49\n"
"v_fmac_f32 v138, s50, v50\n"
"s_add_u32 s32, s32, 4096\n"
"s_waitcnt vmcnt(5)\n"
"buffer_load_dwordx4 v[252:255], v2, s[4:7], s32 offen offset:0 sc1\n"
"v_fmac_f32 v139, s50, v51\n"
"v_and_b32 v79, 15, v0\n"
"v_and_b32 v78, 1, v79\n"
"v_lshrrev_b32 v77, 4, v0\n"
"v_lshlrev_b32 v76, 3, v77\n"
"v_lshlrev_b32 v75, 8, v77\n"
"v_lshlrev_b32 v74, 7, v78\n"
"v_add_u32 v73, v75, v74\n"
"v_lshrrev_b32 v72, 1, v79\n"
"v_lshlrev_b32 v71, 4, v72\n"
"v_add_u32 v17, v73, v71\n"
"v_mul_i32_i24 v70, 0x500, v79\n"
"v_add_u32 v14, v70, v76\n"
"s_mov_b32 s79, 0x25400\n"
"s_cmp_eq_i32 s3, 0\n"
"s_cselect_b32 s79, s79, 0x21000\n"
"s_mov_b32 s78, 0x25c00\n"
"s_cmp_eq_i32 s3, 2\n"
"s_cselect_b32 s78, s78, 0x23400\n"
"s_cmp_lt_i32 s3, 2\n"
"s_cselect_b32 s79, s79, s78\n"
"v_add_u32 v6, s79, v2\n"
"s_mov_b32 s79, 0x40000\n"
"s_cmp_eq_i32 s3, 0\n"
"s_cselect_b32 s79, s79, 0x20c00\n"
"s_mov_b32 s78, 0x40000\n"
"s_cmp_eq_i32 s3, 2\n"
"s_cselect_b32 s78, s78, 0x23000\n"
"s_cmp_lt_i32 s3, 2\n"
"s_cselect_b32 s79, s79, s78\n"
"v_add_u32 v7, s79, v2\n"
"s_mov_b32 s79, 0x23c00\n"
"s_cmp_eq_i32 s3, 0\n"
"s_cselect_b32 s79, s79, 0x1f200\n"
"s_mov_b32 s78, 0x21800\n"
"s_cmp_eq_i32 s3, 2\n"
"s_cselect_b32 s78, s78, 0x1ce00\n"
"s_cmp_lt_i32 s3, 2\n"
"s_cselect_b32 s79, s79, s78\n"
"v_add_u32 v8, s79, v2\n"
"s_mov_b32 s79, 0x20400\n"
"s_cmp_eq_i32 s3, 0\n"
"s_cselect_b32 s79, s79, 0x24c00\n"
"s_mov_b32 s78, 0x1e000\n"
"s_cmp_eq_i32 s3, 2\n"
"s_cselect_b32 s78, s78, 0x22800\n"
"s_cmp_lt_i32 s3, 2\n"
"s_cselect_b32 s79, s79, s78\n"
"v_add_u32 v9, s79, v2\n"
"s_mov_b32 s79, 0x24400\n"
"s_cmp_eq_i32 s3, 0\n"
"s_cselect_b32 s79, s79, 0x24800\n"
"s_mov_b32 s78, 0x22000\n"
"s_cmp_eq_i32 s3, 2\n"
"s_cselect_b32 s78, s78, 0x22400\n"
"s_cmp_lt_i32 s3, 2\n"
"s_cselect_b32 s79, s79, s78\n"
"v_add_u32 v10, s79, v2\n"
"s_and_b32 s79, s3, 1\n"
"s_lshl_b32 s79, s79, 30\n"
"v_add_u32 v15, s79, v14\n"
"v_lshlrev_b32 v99, 3, v79\n"
"v_mov_b32 v98, 0x40000000\n"
"v_lshrrev_b32 v97, 2, v79\n"
"v_lshl_add_u32 v97, v78, 3, v97\n"
"v_cmp_eq_i32 vcc, v97, v77\n"
"s_nop 1\n"
"v_cndmask_b32 v16, v98, v99, vcc\n"
"s_and_b32 s79, s3, 1\n"
"s_lshl_b32 s79, s79, 30\n"
"v_add_u32 v18, s79, v17\n"
"s_and_b32 s79, s3, 1\n"
"s_sub_u32 s79, s79, 1\n"
"v_mov_b32 v19, s79\n"
"v_lshrrev_b32 v99, 1, v77\n"
"v_and_b32 v98, 1, v77\n"
"v_and_b32 v97, 4, v79\n"
"v_and_b32 v96, 3, v79\n"
"v_lshrrev_b32 v95, 3, v79\n"
"v_lshlrev_b32 v95, 6, v95\n"
"v_lshl_add_u32 v96, v96, 4, v95\n"
"v_lshl_add_u32 v97, v97, 1, v96\n"
"v_lshl_add_u32 v98, v98, 8, v97\n"
"v_lshl_add_u32 v99, v99, 7, v98\n"
"v_sub_u32 v39, v99, v14\n"
"s_mov_b32 s79, 0x20c00\n"
"s_cmp_eq_i32 s3, 0\n"
"s_cselect_b32 s79, s79, 0x25400\n"
"s_mov_b32 s78, 0x23000\n"
"s_cmp_eq_i32 s3, 2\n"
"s_cselect_b32 s78, s78, 0x25c00\n"
"s_cmp_lt_i32 s3, 2\n"
"s_cselect_b32 s79, s79, s78\n"
"v_add_u32 v11, s79, v2\n"
"s_mov_b32 s79, 0x21800\n"
"s_cmp_eq_i32 s3, 0\n"
"s_cselect_b32 s79, s79, 0x1ce00\n"
"s_mov_b32 s78, 0x23c00\n"
"s_cmp_eq_i32 s3, 2\n"
"s_cselect_b32 s78, s78, 0x1f200\n"
"s_cmp_lt_i32 s3, 2\n"
"s_cselect_b32 s79, s79, s78\n"
"v_add_u32 v12, s79, v2\n"
"s_mov_b32 s79, 0x22400\n"
"s_cmp_eq_i32 s3, 0\n"
"s_cselect_b32 s79, s79, 0x1e000\n"
"s_mov_b32 s78, 0x24800\n"
"s_cmp_eq_i32 s3, 2\n"
"s_cselect_b32 s78, s78, 0x20400\n"
"s_cmp_lt_i32 s3, 2\n"
"s_cselect_b32 s79, s79, s78\n"
"v_add_u32 v13, s79, v2\n"
"v_mov_b32 v20, 0x3f803f80\n"
"v_mov_b32 v21, 0x3f803f80\n"
"v_mov_b32 v22, 0x3f803f80\n"
"v_mov_b32 v23, 0x3f803f80\n"
"s_branch .Ljk_305\n"
".Lnk_304:\n"
"v_and_b32 v79, 15, v0\n"
"v_and_b32 v78, 1, v79\n"
"v_lshrrev_b32 v77, 4, v0\n"
"v_lshlrev_b32 v76, 3, v77\n"
"v_lshlrev_b32 v75, 8, v77\n"
"v_lshlrev_b32 v74, 7, v78\n"
"v_add_u32 v73, v75, v74\n"
"v_lshrrev_b32 v72, 1, v79\n"
"v_lshlrev_b32 v71, 4, v72\n"
"v_add_u32 v17, v73, v71\n"
"v_mul_i32_i24 v70, 0x500, v79\n"
"v_add_u32 v14, v70, v76\n"
"s_mov_b32 s79, 0x25400\n"
"s_cmp_eq_i32 s3, 0\n"
"s_cselect_b32 s79, s79, 0x21000\n"
"s_mov_b32 s78, 0x25c00\n"
"s_cmp_eq_i32 s3, 2\n"
"s_cselect_b32 s78, s78, 0x23400\n"
"s_cmp_lt_i32 s3, 2\n"
"s_cselect_b32 s79, s79, s78\n"
"v_add_u32 v6, s79, v2\n"
"s_mov_b32 s79, 0x40000\n"
"s_cmp_eq_i32 s3, 0\n"
"s_cselect_b32 s79, s79, 0x20c00\n"
"s_mov_b32 s78, 0x40000\n"
"s_cmp_eq_i32 s3, 2\n"
"s_cselect_b32 s78, s78, 0x23000\n"
"s_cmp_lt_i32 s3, 2\n"
"s_cselect_b32 s79, s79, s78\n"
"v_add_u32 v7, s79, v2\n"
"s_mov_b32 s79, 0x23c00\n"
"s_cmp_eq_i32 s3, 0\n"
"s_cselect_b32 s79, s79, 0x1f200\n"
"s_mov_b32 s78, 0x21800\n"
"s_cmp_eq_i32 s3, 2\n"
"s_cselect_b32 s78, s78, 0x1ce00\n"
"s_cmp_lt_i32 s3, 2\n"
"s_cselect_b32 s79, s79, s78\n"
"v_add_u32 v8, s79, v2\n"
"s_mov_b32 s79, 0x20400\n"
"s_cmp_eq_i32 s3, 0\n"
"s_cselect_b32 s79, s79, 0x24c00\n"
"s_mov_b32 s78, 0x1e000\n"
"s_cmp_eq_i32 s3, 2\n"
"s_cselect_b32 s78, s78, 0x22800\n"
"s_cmp_lt_i32 s3, 2\n"
"s_cselect_b32 s79, s79, s78\n"
"v_add_u32 v9, s79, v2\n"
"s_mov_b32 s79, 0x24400\n"
"s_cmp_eq_i32 s3, 0\n"
"s_cselect_b32 s79, s79, 0x24800\n"
"s_mov_b32 s78, 0x22000\n"
"s_cmp_eq_i32 s3, 2\n"
"s_cselect_b32 s78, s78, 0x22400\n"
"s_cmp_lt_i32 s3, 2\n"
"s_cselect_b32 s79, s79, s78\n"
"v_add_u32 v10, s79, v2\n"
"s_and_b32 s79, s3, 1\n"
"s_lshl_b32 s79, s79, 30\n"
"v_add_u32 v15, s79, v14\n"
"v_lshlrev_b32 v99, 3, v79\n"
"v_mov_b32 v98, 0x40000000\n"
"v_lshrrev_b32 v97, 2, v79\n"
"v_lshl_add_u32 v97, v78, 3, v97\n"
"v_cmp_eq_i32 vcc, v97, v77\n"
"s_nop 1\n"
"v_cndmask_b32 v16, v98, v99, vcc\n"
"s_and_b32 s79, s3, 1\n"
"s_lshl_b32 s79, s79, 30\n"
"v_add_u32 v18, s79, v17\n"
"s_and_b32 s79, s3, 1\n"
"s_sub_u32 s79, s79, 1\n"
"v_mov_b32 v19, s79\n"
"v_lshrrev_b32 v99, 1, v77\n"
"v_and_b32 v98, 1, v77\n"
"v_and_b32 v97, 4, v79\n"
"v_and_b32 v96, 3, v79\n"
"v_lshrrev_b32 v95, 3, v79\n"
"v_lshlrev_b32 v95, 6, v95\n"
"v_lshl_add_u32 v96, v96, 4, v95\n"
"v_lshl_add_u32 v97, v97, 1, v96\n"
"v_lshl_add_u32 v98, v98, 8, v97\n"
"v_lshl_add_u32 v99, v99, 7, v98\n"
"v_sub_u32 v39, v99, v14\n"
"s_mov_b32 s79, 0x20c00\n"
"s_cmp_eq_i32 s3, 0\n"
"s_cselect_b32 s79, s79, 0x25400\n"
"s_mov_b32 s78, 0x23000\n"
"s_cmp_eq_i32 s3, 2\n"
"s_cselect_b32 s78, s78, 0x25c00\n"
"s_cmp_lt_i32 s3, 2\n"
"s_cselect_b32 s79, s79, s78\n"
"v_add_u32 v11, s79, v2\n"
"s_mov_b32 s79, 0x21800\n"
"s_cmp_eq_i32 s3, 0\n"
"s_cselect_b32 s79, s79, 0x1ce00\n"
"s_mov_b32 s78, 0x23c00\n"
"s_cmp_eq_i32 s3, 2\n"
"s_cselect_b32 s78, s78, 0x1f200\n"
"s_cmp_lt_i32 s3, 2\n"
"s_cselect_b32 s79, s79, s78\n"
"v_add_u32 v12, s79, v2\n"
"s_mov_b32 s79, 0x22400\n"
"s_cmp_eq_i32 s3, 0\n"
"s_cselect_b32 s79, s79, 0x1e000\n"
"s_mov_b32 s78, 0x24800\n"
"s_cmp_eq_i32 s3, 2\n"
"s_cselect_b32 s78, s78, 0x20400\n"
"s_cmp_lt_i32 s3, 2\n"
"s_cselect_b32 s79, s79, s78\n"
"v_add_u32 v13, s79, v2\n"
"v_mov_b32 v20, 0x3f803f80\n"
"v_mov_b32 v21, 0x3f803f80\n"
"v_mov_b32 v22, 0x3f803f80\n"
"v_mov_b32 v23, 0x3f803f80\n"
"s_mov_b32 s17, 0x100000\n"
".Ltp_313:\n"
"s_sleep 2\n"
"ds_read_b32 v3, v5 offset:16\n"
"s_sub_u32 s17, s17, 1\n"
"s_cbranch_scc1 .Lou_314\n"
"s_waitcnt lgkmcnt(0)\n"
"v_cmp_eq_u32 vcc, 0, v3\n"
"s_cbranch_vccnz .Ltp_313\n"
".Lou_314:\n"
"s_waitcnt lgkmcnt(0)\n"
"s_nop 2\n"
"s_mov_b32 s17, 32768\n"
".Ltp_315:\n"
"buffer_load_dwordx4 v[40:43], v38, s[4:7], s33 offen sc1\n"
"s_sub_u32 s17, s17, 1\n"
"s_cbranch_scc1 .Lou_316\n"
"s_waitcnt vmcnt(0)\n"
"v_xor_b32 v40, s16, v40\n"
"v_xor_b32 v41, s16, v41\n"
"v_xor_b32 v42, s16, v42\n"
"v_xor_b32 v43, s16, v43\n"
"v_or_b32 v40, v40, v41\n"
"v_or3_b32 v40, v40, v42, v43\n"
"v_cmp_ne_u32 vcc, 0, v40\n"
"s_cbranch_vccz .Lou_316\n"
"s_sleep 1\n"
"s_branch .Ltp_315\n"
".Lou_316:\n"
"s_waitcnt vmcnt(0)\n"
".Lpl_306:\n"
"s_mov_b32 s17, 0x100000\n"
".Ltp_317:\n"
"s_sleep 2\n"
"ds_read_b32 v3, v5 offset:32\n"
"s_sub_u32 s17, s17, 1\n"
"s_cbranch_scc1 .Lou_318\n"
"s_waitcnt lgkmcnt(0)\n"
"v_cmp_eq_u32 vcc, 0, v3\n"
"s_cbranch_vccnz .Ltp_317\n"
".Lou_318:\n"
"s_waitcnt lgkmcnt(0)\n"
"buffer_load_dwordx4 v[156:159], v2, s[4:7], s32 offen offset:0 sc1\n"
"buffer_load_dwordx4 v[160:163], v2, s[4:7], s32 offen offset:1024 sc1\n"
"buffer_load_dwordx4 v[164:167], v2, s[4:7], s32 offen offset:2048 sc1\n"
"buffer_load_dwordx4 v[168:171], v2, s[4:7], s32 offen offset:3072 sc1\n"
"s_add_u32 s32, s32, 4096\n"
"buffer_load_dwordx4 v[172:175], v2, s[4:7], s32 offen offset:0 sc1\n"
"buffer_load_dwordx4 v[176:179], v2, s[4:7], s32 offen offset:1024 sc1\n"
"s_waitcnt vmcnt(5)\n"
"buffer_load_dwordx4 v[180:183], v2, s[4:7], s32 offen offset:2048 sc1\n"
"s_waitcnt vmcnt(5)\n"
"buffer_load_dwordx4 v[184:187], v2, s[4:7], s32 offen offset:3072 sc1\n"
"s_add_u32 s32, s32, 4096\n"
"s_waitcnt vmcnt(5)\n"
"buffer_load_dwordx4 v[188:191], v2, s[4:7], s32 offen offset:0 sc1\n"
"s_waitcnt vmcnt(5)\n"
"buffer_load_dwordx4 v[192:195], v2, s[4:7], s32 offen offset:1024 sc1\n"
"s_waitcnt vmcnt(5)\n"
"buffer_load_dwordx4 v[196:199], v2, s[4:7], s32 offen offset:2048 sc1\n"
"s_waitcnt vmcnt(5)\n"
"buffer_load_dwordx4 v[200:203], v2, s[4:7], s32 offen offset:3072 sc1\n"
"s_add_u32 s32, s32, 4096\n"
"s_waitcnt vmcnt(5)\n"
"buffer_load_dwordx4 v[204:207], v2, s[4:7], s32 offen offset:0 sc1\n"
"s_waitcnt vmcnt(5)\n"
"buffer_load_dwordx4 v[208:211], v2, s[4:7], s32 offen offset:1024 sc1\n"
"s_waitcnt vmcnt(5)\n"
"buffer_load_dwordx4 v[212:215], v2, s[4:7], s32 offen offset:2048 sc1\n"
"s_waitcnt vmcnt(5)\n"
"buffer_load_dwordx4 v[216:219], v2, s[4:7], s32 offen offset:3072 sc1\n"
"s_add_u32 s32, s32, 4096\n"
"s_waitcnt vmcnt(5)\n"
"buffer_load_dwordx4 v[220:223], v2, s[4:7], s32 offen offset:0 sc1\n"
"s_waitcnt vmcnt(5)\n"
"buffer_load_dwordx4 v[224:227], v2, s[4:7], s32 offen offset:1024 sc1\n"
"s_waitcnt vmcnt(5)\n"
"buffer_load_dwordx4 v[228:231], v2, s[4:7], s32 offen offset:2048 sc1\n"
"s_waitcnt vmcnt(5)\n"
"buffer_load_dwordx4 v[232:235], v2, s[4:7], s32 offen offset:3072 sc1\n"
"s_add_u32 s32, s32, 4096\n"
"s_waitcnt vmcnt(5)\n"
"buffer_load_dwordx4 v[236:239], v2, s[4:7], s32 offen offset:0 sc1\n"
"s_waitcnt vmcnt(5)\n"
"buffer_load_dwordx4 v[240:243], v2, s[4:7], s32 offen offset:1024 sc1\n"
"s_waitcnt vmcnt(5)\n"
"buffer_load_dwordx4 v[244:247], v2, s[4:7], s32 offen offset:2048 sc1\n"
"s_waitcnt vmcnt(5)\n"
"buffer_load_dwordx4 v[248:251], v2, s[4:7], s32 offen offset:3072 sc1\n"
"s_add_u32 s32, s32, 4096\n"
"s_waitcnt vmcnt(5)\n"
"buffer_load_dwordx4 v[252:255], v2, s[4:7], s32 offen offset:0 sc1\n"
".Ljk_305:\n"
"s_cmp_lt_i32 s18, 0\n"
"s_cselect_b32 s43, 3, 0\n"
"s_setprio 2\n"
"v_mov_b32 v40, -1\n"
"v_mov_b32 v41, -1\n"
"v_mov_b32 v42, -1\n"
"v_mov_b32 v43, -1\n"
"s_lshl_b32 s79, s2, 2\n"
"s_add_u32 s79, s79, s3\n"
"s_lshl_b32 s79, s79, 10\n"
"s_add_u32 s49, s79, 0xc28000\n"
"buffer_store_dwordx4 v[40:43], v2, s[4:7], s49 offen sc1\n"
"s_add_u32 s49, s49, 0x100000\n"
"buffer_store_dwordx4 v[40:43], v2, s[4:7], s49 offen sc1\n"
"s_add_u32 s49, s49, 0x100000\n"
"s_sub_u32 s79, 0xea7fff, s49\n"
"s_and_b32 s79, s79, 0x80000000\n"
"s_or_b32 s49, s49, s79\n"
"buffer_store_dwordx4 v[40:43], v2, s[4:7], s49 offen sc1\n"
"s_nop 1\n"
"s_setprio 0\n"
"s_cmp_lg_u32 s43, 0\n"
"s_cbranch_scc1 .Lsk_337\n"
"ds_read_b32 v3, v5 offset:0\n"
"s_waitcnt lgkmcnt(0)\n"
"v_cmp_eq_u32 vcc, 0, v3\n"
"s_cbranch_vccz .Len_336\n"
"s_call_b64 s[46:47], .Lst_319\n"
".Lsk_337:\n"
"s_cmp_lg_u32 s43, 1\n"
"s_cbranch_scc1 .Lsk_338\n"
"ds_read_b32 v3, v5 offset:128\n"
"s_waitcnt lgkmcnt(0)\n"
"v_cmp_eq_u32 vcc, 0, v3\n"
"s_cbranch_vccz .Len_336\n"
"s_call_b64 s[46:47], .Lst_320\n"
".Lsk_338:\n"
"s_cmp_lg_u32 s43, 2\n"
"s_cbranch_scc1 .Lsk_339\n"
"ds_read_b32 v3, v5 offset:128\n"
"s_waitcnt lgkmcnt(0)\n"
"v_cmp_eq_u32 vcc, 0, v3\n"
"s_cbranch_vccz .Len_336\n"
"s_call_b64 s[46:47], .Lst_321\n"
".Lsk_339:\n"
".Len_336:\n"
"s_setprio 2\n"
"s_mul_i32 s79, s28, 0xa0\n"
"s_add_u32 s79, s30, s79\n"
"s_and_b32 s78, s3, 1\n"
"s_mul_i32 s78, s78, 0x60\n"
"s_add_u32 s36, s79, s78\n"
"s_mul_i32 s79, s3, 0x1400\n"
"s_add_u32 s79, s30, s79\n"
"s_add_u32 s32, s79, 0x448000\n"
"s_add_u32 s33, s32, 0x1000\n"
"s_lshl_b32 s79, s29, 1\n"
"s_lshr_b32 s78, s3, 1\n"
"s_xor_b32 s78, s78, 1\n"
"s_add_u32 s79, s79, s78\n"
"s_mul_i32 s79, s79, 0x5000\n"
"s_mul_i32 s78, s3, 0x1400\n"
"s_add_u32 s79, s79, s78\n"
"s_add_u32 s34, s79, 0x448000\n"
"s_add_u32 s35, s34, 0x1000\n"
"s_lshl_b32 s79, s30, 1\n"
"s_mul_i32 s78, s28, 0x1400\n"
"s_add_u32 s79, s79, s78\n"
"s_and_b32 s78, s3, 1\n"
"s_mul_i32 s78, s78, 0xc00\n"
"s_add_u32 s79, s79, s78\n"
"s_add_u32 s37, s79, 0x6c8000\n"
"s_lshl_b32 s79, s29, 1\n"
"s_lshr_b32 s78, s3, 1\n"
"s_add_u32 s79, s79, s78\n"
"s_lshl_b32 s79, s79, 11\n"
"s_lshl_b32 s78, s28, 8\n"
"s_add_u32 s79, s79, s78\n"
"s_and_b32 s78, s3, 1\n"
"s_lshl_b32 s78, s78, 7\n"
"s_add_u32 s79, s79, s78\n"
"s_add_u32 s38, s79, 0xbc8000\n"
"buffer_load_dwordx2 v[40:41], v14, s[8:11], s36 offen offset:0\n"
"buffer_load_dwordx2 v[42:43], v14, s[8:11], s36 offen offset:32\n"
"buffer_load_dwordx2 v[44:45], v15, s[8:11], s36 offen offset:64\n"
"s_mov_b32 s17, 0x100000\n"
".Ltp_340:\n"
"s_sleep 2\n"
"ds_read_b32 v3, v5 offset:96\n"
"s_sub_u32 s17, s17, 1\n"
"s_cbranch_scc1 .Lou_341\n"
"s_waitcnt lgkmcnt(0)\n"
"v_cmp_eq_u32 vcc, 0, v3\n"
"s_cbranch_vccnz .Ltp_340\n"
".Lou_341:\n"
"s_waitcnt lgkmcnt(0)\n"
"s_cmp_eq_u32 s17, 0xfffff\n"
"s_cbranch_scc1 .Lsk_342\n"
".Lsk_342:\n"
"v_lshlrev_b32 v38, 5, v0\n"
"s_waitcnt vmcnt(0)\n"
"s_nop 3\n"
"s_mov_b32 s17, 32768\n"
".Ltp_344:\n"
"buffer_load_dwordx4 v[88:91], v38, s[4:7], s48 offen sc1\n"
"s_sub_u32 s17, s17, 1\n"
"s_cbranch_scc1 .Lou_345\n"
"s_waitcnt vmcnt(0)\n"
"v_xor_b32 v88, s16, v88\n"
"v_xor_b32 v89, s16, v89\n"
"v_xor_b32 v90, s16, v90\n"
"v_xor_b32 v91, s16, v91\n"
"v_or_b32 v88, v88, v89\n"
"v_or3_b32 v88, v88, v90, v91\n"
"v_cmp_ne_u32 vcc, 0, v88\n"
"s_cbranch_vccz .Lou_345\n"
"s_mov_b32 s89, 6\n"
".Ldl_346:\n"
"s_sub_u32 s89, s89, 1\n"
"s_cbranch_scc1 .Lde_347\n"
"s_sleep 1\n"
"s_branch .Ldl_346\n"
".Lde_347:\n"
"s_branch .Ltp_344\n"
".Lou_345:\n"
"s_waitcnt vmcnt(0)\n"
"v_lshlrev_b32 v48, 16, v40\n"
"v_and_b32 v49, 0xffff0000, v40\n"
"v_lshlrev_b32 v50, 16, v41\n"
"v_and_b32 v51, 0xffff0000, v41\n"
"v_lshlrev_b32 v52, 16, v42\n"
"v_and_b32 v53, 0xffff0000, v42\n"
"v_lshlrev_b32 v54, 16, v43\n"
"v_and_b32 v55, 0xffff0000, v43\n"
"v_lshlrev_b32 v56, 16, v44\n"
"v_and_b32 v57, 0xffff0000, v44\n"
"v_lshlrev_b32 v58, 16, v45\n"
"v_and_b32 v59, 0xffff0000, v45\n"
"v_mov_b32 v46, 0\n"
"v_mov_b32 v47, 0\n"
"s_mov_b32 s17, 0x100000\n"
".Ltp_348:\n"
"s_sleep 1\n"
"ds_read_b32 v3, v5 offset:128\n"
"s_sub_u32 s17, s17, 1\n"
"s_cbranch_scc1 .Lou_349\n"
"s_waitcnt lgkmcnt(0)\n"
"v_cmp_eq_u32 vcc, 0, v3\n"
"s_cbranch_vccnz .Ltp_348\n"
".Lou_349:\n"
"s_waitcnt lgkmcnt(0)\n"
"s_cmp_eq_u32 s17, 0xfffff\n"
"s_cbranch_scc1 .Lsk_350\n"
"s_mov_b32 s89, 18\n"
".Ldl_352:\n"
"s_sub_u32 s89, s89, 1\n"
"s_cbranch_scc1 .Lde_353\n"
"s_sleep 1\n"
"s_branch .Ldl_352\n"
".Lde_353:\n"
"s_branch .Ljn_351\n"
".Lsk_350:\n"
".Ljn_351:\n"
"buffer_load_dwordx4 v[108:111], v2, s[4:7], s34 offen offset:0 sc1\n"
"s_nop 9\n"
"buffer_load_dwordx4 v[112:115], v2, s[4:7], s34 offen offset:1024 sc1\n"
"s_nop 9\n"
"buffer_load_dwordx4 v[116:119], v2, s[4:7], s34 offen offset:2048 sc1\n"
"s_nop 9\n"
"buffer_load_dwordx4 v[120:123], v2, s[4:7], s34 offen offset:3072 sc1\n"
"s_nop 9\n"
"buffer_load_dwordx4 v[124:127], v2, s[4:7], s35 offen offset:0 sc1\n"
"s_nop 9\n"
"buffer_load_dwordx4 v[88:91], v2, s[4:7], s32 offen offset:0 sc1\n"
"s_nop 9\n"
"buffer_load_dwordx4 v[92:95], v2, s[4:7], s32 offen offset:1024 sc1\n"
"s_nop 9\n"
"buffer_load_dwordx4 v[96:99], v2, s[4:7], s32 offen offset:2048 sc1\n"
"s_nop 9\n"
"buffer_load_dwordx4 v[100:103], v2, s[4:7], s32 offen offset:3072 sc1\n"
"s_nop 9\n"
"buffer_load_dwordx4 v[104:107], v2, s[4:7], s33 offen offset:0 sc1\n"
"s_waitcnt vmcnt(9)\n"
"v_max_u32 v38, v108, v109\n"
"v_max3_u32 v38, v110, v111, v38\n"
"v_cmp_eq_u32 vcc, -1, v38\n"
"s_cbranch_vccnz .Lsl_354\n"
".Lbk_355:\n"
"v_mfma_f32_16x16x32_bf16 v[68:71], v[156:159], v[108:111], 0\n"
"v_mfma_f32_16x16x32_bf16 v[72:75], v[160:163], v[108:111], 0\n"
"v_mfma_f32_16x16x32_bf16 v[76:79], v[164:167], v[108:111], 0\n"
"s_waitcnt vmcnt(8)\n"
"v_max_u32 v38, v112, v113\n"
"v_max3_u32 v38, v114, v115, v38\n"
"v_mfma_f32_16x16x32_bf16 v[80:83], v[168:171], v[108:111], 0\n"
"v_cmp_eq_u32 vcc, -1, v38\n"
"v_mfma_f32_16x16x32_bf16 v[84:87], v[172:175], v[108:111], 0\n"
"s_cbranch_vccnz .Lsl_356\n"
".Lbk_357:\n"
"v_mfma_f32_16x16x32_bf16 v[68:71], v[176:179], v[112:115], v[68:71]\n"
"v_mfma_f32_16x16x32_bf16 v[72:75], v[180:183], v[112:115], v[72:75]\n"
"v_mfma_f32_16x16x32_bf16 v[76:79], v[184:187], v[112:115], v[76:79]\n"
"s_waitcnt vmcnt(7)\n"
"v_max_u32 v38, v116, v117\n"
"v_max3_u32 v38, v118, v119, v38\n"
"v_mfma_f32_16x16x32_bf16 v[80:83], v[188:191], v[112:115], v[80:83]\n"
"v_cmp_eq_u32 vcc, -1, v38\n"
"v_mfma_f32_16x16x32_bf16 v[84:87], v[192:195], v[112:115], v[84:87]\n"
"s_cbranch_vccnz .Lsl_358\n"
".Lbk_359:\n"
"v_mfma_f32_16x16x32_bf16 v[68:71], v[196:199], v[116:119], v[68:71]\n"
"v_mfma_f32_16x16x32_bf16 v[72:75], v[200:203], v[116:119], v[72:75]\n"
"v_mfma_f32_16x16x32_bf16 v[76:79], v[204:207], v[116:119], v[76:79]\n"
"s_waitcnt vmcnt(6)\n"
"v_max_u32 v38, v120, v121\n"
"v_max3_u32 v38, v122, v123, v38\n"
"v_mfma_f32_16x16x32_bf16 v[80:83], v[208:211], v[116:119], v[80:83]\n"
"v_cmp_eq_u32 vcc, -1, v38\n"
"v_mfma_f32_16x16x32_bf16 v[84:87], v[212:215], v[116:119], v[84:87]\n"
"s_cbranch_vccnz .Lsl_360\n"
".Lbk_361:\n"
"v_mfma_f32_16x16x32_bf16 v[68:71], v[216:219], v[120:123], v[68:71]\n"
"v_mfma_f32_16x16x32_bf16 v[72:75], v[220:223], v[120:123], v[72:75]\n"
"v_mfma_f32_16x16x32_bf16 v[76:79], v[224:227], v[120:123], v[76:79]\n"
"s_waitcnt vmcnt(5)\n"
"v_max_u32 v38, v124, v125\n"
"v_max3_u32 v38, v126, v127, v38\n"
"v_mfma_f32_16x16x32_bf16 v[80:83], v[228:231], v[120:123], v[80:83]\n"
"v_cmp_eq_u32 vcc, -1, v38\n"
"v_mfma_f32_16x16x32_bf16 v[84:87], v[232:235], v[120:123], v[84:87]\n"
"s_cbranch_vccnz .Lsl_362\n"
".Lbk_363:\n"
"v_mfma_f32_16x16x32_bf16 v[68:71], v[236:239], v[124:127], v[68:71]\n"
"v_mfma_f32_16x16x32_bf16 v[72:75], v[240:243], v[124:127], v[72:75]\n"
"v_mfma_f32_16x16x32_bf16 v[76:79], v[244:247], v[124:127], v[76:79]\n"
"s_waitcnt vmcnt(4)\n"
"v_max_u32 v38, v88, v89\n"
"v_max3_u32 v38, v90, v91, v38\n"
"v_mfma_f32_16x16x32_bf16 v[80:83], v[248:251], v[124:127], v[80:83]\n"
"v_cmp_eq_u32 vcc, -1, v38\n"
"v_mfma_f32_16x16x32_bf16 v[84:87], v[252:255], v[124:127], v[84:87]\n"
"ds_write_b128 v8, v[68:71] offset:0\n"
"s_cbranch_vccnz .Lsl_364\n"
".Lbk_365:\n"
"v_mfma_f32_16x16x32_bf16 v[56:59], v[164:167], v[88:91], v[56:59]\n"
"s_waitcnt vmcnt(3)\n"
"v_max_u32 v38, v92, v93\n"
"v_max3_u32 v38, v94, v95, v38\n"
"ds_write_b128 v8, v[72:75] offset:1024\n"
"v_mfma_f32_16x16x32_bf16 v[60:63], v[168:171], v[88:91], 0\n"
"v_cmp_eq_u32 vcc, -1, v38\n"
"ds_write_b128 v10, v[76:79] offset:0\n"
"v_mfma_f32_16x16x32_bf16 v[64:67], v[172:175], v[88:91], 0\n"
"v_mov_b32 v117, s16\n"
"s_cbranch_vccnz .Lsl_366\n"
".Lbk_367:\n"
"v_mfma_f32_16x16x32_bf16 v[56:59], v[184:187], v[92:95], v[56:59]\n"
"s_waitcnt vmcnt(2)\n"
"v_max_u32 v38, v96, v97\n"
"v_max3_u32 v38, v98, v99, v38\n"
"v_mov_b32 v119, s16\n"
"v_mfma_f32_16x16x32_bf16 v[60:63], v[188:191], v[92:95], v[60:63]\n"
"v_cmp_eq_u32 vcc, -1, v38\n"
"ds_write_b128 v9, v[80:83] offset:0\n"
"v_mfma_f32_16x16x32_bf16 v[64:67], v[192:195], v[92:95], v[64:67]\n"
"ds_write_b128 v9, v[84:87] offset:1024\n"
"s_cbranch_vccnz .Lsl_368\n"
".Lbk_369:\n"
"v_mfma_f32_16x16x32_bf16 v[56:59], v[204:207], v[96:99], v[56:59]\n"
"s_waitcnt vmcnt(1)\n"
"v_max_u32 v38, v100, v101\n"
"v_max3_u32 v38, v102, v103, v38\n"
"ds_write_b32 v4, v4 offset:592\n"
"v_mfma_f32_16x16x32_bf16 v[60:63], v[208:211], v[96:99], v[60:63]\n"
"v_cmp_eq_u32 vcc, -1, v38\n"
"v_mov_b32 v121, s16\n"
"v_mov_b32 v123, s16\n"
"v_mfma_f32_16x16x32_bf16 v[64:67], v[212:215], v[96:99], v[64:67]\n"
"v_mov_b32 v125, s16\n"
"s_cbranch_vccnz .Lsl_370\n"
".Lbk_371:\n"
"v_mfma_f32_16x16x32_bf16 v[56:59], v[224:227], v[100:103], v[56:59]\n"
"s_waitcnt vmcnt(0)\n"
"v_max_u32 v38, v104, v105\n"
"v_max3_u32 v38, v106, v107, v38\n"
"v_mov_b32 v127, s16\n"
"v_mfma_f32_16x16x32_bf16 v[60:63], v[228:231], v[100:103], v[60:63]\n"
"v_cmp_eq_u32 vcc, -1, v38\n"
"ds_read_b32 v3, v5 offset:592\n"
"v_mfma_f32_16x16x32_bf16 v[64:67], v[232:235], v[100:103], v[64:67]\n"
"s_cbranch_vccnz .Lsl_372\n"
".Lbk_373:\n"
"v_mfma_f32_16x16x32_bf16 v[56:59], v[244:247], v[104:107], v[56:59]\n"
"ds_read_b128 v[76:79], v12 offset:2048\n"
"ds_read_b128 v[108:111], v13 offset:2048\n"
"v_mfma_f32_16x16x32_bf16 v[60:63], v[248:251], v[104:107], v[60:63]\n"
"ds_read_b128 v[68:71], v12 offset:0\n"
"ds_read_b128 v[80:83], v13 offset:0\n"
"v_mfma_f32_16x16x32_bf16 v[64:67], v[252:255], v[104:107], v[64:67]\n"
"ds_read_b128 v[72:75], v12 offset:1024\n"
"ds_read_b128 v[84:87], v13 offset:1024\n"
"v_mfma_f32_16x16x32_bf16 v[48:51], v[156:159], v[88:91], v[48:51]\n"
"ds_write_b128 v7, v[56:59] offset:0\n"
"v_mfma_f32_16x16x32_bf16 v[52:55], v[160:163], v[88:91], v[52:55]\n"
"ds_write_b128 v6, v[60:63] offset:0\n"
"v_mfma_f32_16x16x32_bf16 v[48:51], v[176:179], v[92:95], v[48:51]\n"
"ds_write_b128 v6, v[64:67] offset:1024\n"
"v_mfma_f32_16x16x32_bf16 v[52:55], v[180:183], v[92:95], v[52:55]\n"
"ds_write_b32 v4, v4 offset:608\n"
"v_mfma_f32_16x16x32_bf16 v[48:51], v[196:199], v[96:99], v[48:51]\n"
"s_mov_b32 s17, 0x100000\n"
"s_waitcnt lgkmcnt(10)\n"
"v_cmp_eq_u32 vcc, 0, v3\n"
"s_cbranch_vccz .Lok_375\n"
".Ltp_374:\n"
"ds_read_b32 v3, v5 offset:592\n"
"ds_read_b128 v[76:79], v12 offset:2048\n"
"ds_read_b128 v[108:111], v13 offset:2048\n"
"ds_read_b128 v[68:71], v12 offset:0\n"
"ds_read_b128 v[80:83], v13 offset:0\n"
"ds_read_b128 v[72:75], v12 offset:1024\n"
"ds_read_b128 v[84:87], v13 offset:1024\n"
"s_sub_u32 s17, s17, 1\n"
"s_cbranch_scc1 .Lok_375\n"
"s_waitcnt lgkmcnt(6)\n"
"v_cmp_eq_u32 vcc, 0, v3\n"
"s_cbranch_vccnz .Ltp_374\n"
"s_waitcnt lgkmcnt(0)\n"
".Lok_375:\n"
"v_mfma_f32_16x16x32_bf16 v[52:55], v[200:203], v[96:99], v[52:55]\n"
"s_waitcnt lgkmcnt(8)\n"
"v_add_f32 v76, v76, v108\n"
"v_add_f32 v77, v77, v109\n"
"v_add_f32 v78, v78, v110\n"
"v_mfma_f32_16x16x32_bf16 v[48:51], v[216:219], v[100:103], v[48:51]\n"
"v_add_f32 v79, v79, v111\n"
"s_waitcnt lgkmcnt(6)\n"
"v_add_f32 v68, v68, v80\n"
"v_add_f32 v69, v69, v81\n"
"v_mfma_f32_16x16x32_bf16 v[52:55], v[220:223], v[100:103], v[52:55]\n"
"v_add_f32 v70, v70, v82\n"
"v_add_f32 v71, v71, v83\n"
"s_waitcnt lgkmcnt(4)\n"
"v_add_f32 v72, v72, v84\n"
"v_mfma_f32_16x16x32_bf16 v[48:51], v[236:239], v[104:107], v[48:51]\n"
"v_add_f32 v73, v73, v85\n"
"v_add_f32 v74, v74, v86\n"
"v_add_f32 v75, v75, v87\n"
"v_mfma_f32_16x16x32_bf16 v[52:55], v[240:243], v[104:107], v[52:55]\n"
"ds_read_b32 v3, v5 offset:608\n"
"ds_read_b128 v[112:115], v11 offset:2048\n"
"ds_read_b128 v[60:63], v11 offset:0\n"
"ds_read_b128 v[64:67], v11 offset:1024\n"
"v_add_f32 v56, v56, v76\n"
"v_add_f32 v57, v57, v77\n"
"v_add_f32 v58, v58, v78\n"
"v_add_f32 v59, v59, v79\n"
"v_add_f32 v48, v48, v68\n"
"v_add_f32 v49, v49, v69\n"
"v_add_f32 v50, v50, v70\n"
"v_add_f32 v51, v51, v71\n"
"v_add_f32 v52, v52, v72\n"
"v_add_f32 v53, v53, v73\n"
"v_add_f32 v54, v54, v74\n"
"v_add_f32 v55, v55, v75\n"
"s_mov_b32 s17, 0x100000\n"
"s_waitcnt lgkmcnt(3)\n"
"v_cmp_eq_u32 vcc, 0, v3\n"
"s_cbranch_vccz .Lok_377\n"
".Ltp_376:\n"
"ds_read_b32 v3, v5 offset:608\n"
"ds_read_b128 v[112:115], v11 offset:2048\n"
"ds_read_b128 v[60:63], v11 offset:0\n"
"ds_read_b128 v[64:67], v11 offset:1024\n"
"s_sub_u32 s17, s17, 1\n"
"s_cbranch_scc1 .Lok_377\n"
"s_waitcnt lgkmcnt(3)\n"
"v_cmp_eq_u32 vcc, 0, v3\n"
"s_cbranch_vccnz .Ltp_376\n"
"s_waitcnt lgkmcnt(0)\n"
".Lok_377:\n"
"s_waitcnt lgkmcnt(2)\n"
"v_add_f32 v56, v56, v112\n"
"v_add_f32 v57, v57, v113\n"
"v_add_f32 v58, v58, v114\n"
"v_add_f32 v59, v59, v115\n"
"v_cvt_pk_bf16_f32 v44, v56, v57\n"
"v_cvt_pk_bf16_f32 v45, v58, v59\n"
"v_and_b32 v44, v19, v44\n"
"v_and_b32 v45, v19, v45\n"
"s_waitcnt lgkmcnt(1)\n"
"v_add_f32 v48, v48, v60\n"
"v_add_f32 v49, v49, v61\n"
"v_add_f32 v50, v50, v62\n"
"v_add_f32 v51, v51, v63\n"
"v_cvt_pk_bf16_f32 v40, v48, v49\n"
"v_cvt_pk_bf16_f32 v41, v50, v51\n"
"v_mfma_f32_16x16x32_bf16 v[88:91], v[44:47], v[44:47], 0\n"
"v_mfma_f32_16x16x32_bf16 v[92:95], v[20:23], v[44:47], 0\n"
"s_waitcnt lgkmcnt(0)\n"
"v_add_f32 v52, v52, v64\n"
"v_add_f32 v53, v53, v65\n"
"v_add_f32 v54, v54, v66\n"
"v_add_f32 v55, v55, v67\n"
"v_cvt_pk_bf16_f32 v42, v52, v53\n"
"v_cvt_pk_bf16_f32 v43, v54, v55\n"
"v_cvt_pk_bf16_f32 v116, v48, v49\n"
"v_cvt_pk_bf16_f32 v118, v50, v51\n"
"v_mfma_f32_16x16x32_bf16 v[88:91], v[40:43], v[40:43], v[88:91]\n"
"v_mfma_f32_16x16x32_bf16 v[92:95], v[20:23], v[40:43], v[92:95]\n"
"v_cvt_pk_bf16_f32 v120, v52, v53\n"
"v_cvt_pk_bf16_f32 v122, v54, v55\n"
"v_cvt_pk_bf16_f32 v124, v56, v57\n"
"v_cvt_pk_bf16_f32 v126, v58, v59\n"
"s_nop 0\n"
"v_cndmask_b32 v101, v88, v90, s[44:45]\n"
"v_cndmask_b32 v88, v89, v91, s[44:45]\n"
"s_nop 0\n"
"v_min_u32 v100, -2, v92\n"
"v_min_u32 v88, -2, v88\n"
"v_min_u32 v101, -2, v101\n"
"v_mov_b32_dpp v102, v100 quad_perm:[1,0,3,2] row_mask:0xf bank_mask:0xf\n"
"v_mov_b32_dpp v103, v88 quad_perm:[1,0,3,2] row_mask:0xf bank_mask:0xf\n"
"buffer_store_dwordx4 v[100:103], v16, s[4:7], s38 offen sc1\n"
"buffer_store_dwordx4 v[116:119], v17, s[4:7], s37 offen offset:0 sc1\n"
"buffer_store_dwordx4 v[120:123], v17, s[4:7], s37 offen offset:1024 sc1\n"
"buffer_store_dwordx4 v[124:127], v18, s[4:7], s37 offen offset:2048 sc1\n"
"ds_write_b32 v4, v4 offset:176\n"
"s_xor_b32 s79, s3, 1\n"
"s_lshl_b32 s79, s79, 8\n"
"s_add_u32 s32, s19, s79\n"
"s_cmp_ge_u32 s32, 64\n"
"s_cbranch_scc1 .Lnr_378\n"
"s_lshl_b32 s79, s19, 11\n"
"s_add_u32 s32, s79, 0xbc8000\n"
"s_lshl_b32 s79, s19, 7\n"
"s_add_u32 s33, s79, 0xc08000\n"
"v_lshrrev_b32 v99, 3, v0\n"
"v_and_b32 v98, 7, v0\n"
"v_lshlrev_b32 v98, 4, v98\n"
"v_lshl_add_u32 v40, v99, 14, v98\n"
"v_mov_b32 v41, 0x3727c5ac\n"
"s_mov_b32 s89, 15\n"
".Ldl_379:\n"
"s_sub_u32 s89, s89, 1\n"
"s_cbranch_scc1 .Lde_380\n"
"s_sleep 1\n"
"s_branch .Ldl_379\n"
".Lde_380:\n"
"buffer_load_dwordx4 v[48:51], v2, s[4:7], s32 offen offset:0 sc1\n"
"buffer_load_dwordx4 v[52:55], v2, s[4:7], s32 offen offset:1024 sc1\n"
"s_mov_b32 s17, 32768\n"
".Ltp_381:\n"
"s_waitcnt vmcnt(0)\n"
"s_sub_u32 s17, s17, 1\n"
"s_cbranch_scc1 .Lou_382\n"
"v_max_u32 v38, v48, v49\n"
"v_max3_u32 v38, v50, v51, v38\n"
"v_max3_u32 v38, v52, v53, v38\n"
"v_max3_u32 v38, v54, v55, v38\n"
"v_cmp_eq_u32 vcc, -1, v38\n"
"s_cbranch_vccz .Lou_382\n"
"v_max_u32 v38, v50, v51\n"
"v_max3_u32 v38, v48, v49, v38\n"
"v_cmp_eq_u32 vcc, -1, v38\n"
"s_cbranch_vccz .Lsk_383\n"
"s_mov_b64 exec, vcc\n"
"buffer_load_dwordx4 v[48:51], v2, s[4:7], s32 offen offset:0 sc1\n"
"s_mov_b64 exec, -1\n"
".Lsk_383:\n"
"v_max_u32 v38, v54, v55\n"
"v_max3_u32 v38, v52, v53, v38\n"
"v_cmp_eq_u32 vcc, -1, v38\n"
"s_cbranch_vccz .Lsk_384\n"
"s_mov_b64 exec, vcc\n"
"buffer_load_dwordx4 v[52:55], v2, s[4:7], s32 offen offset:1024 sc1\n"
"s_mov_b64 exec, -1\n"
".Lsk_384:\n"
"s_branch .Ltp_381\n"
".Lou_382:\n"
"s_waitcnt vmcnt(0)\n"
"v_add_f32 v48, v48, v52\n"
"v_add_f32 v49, v49, v53\n"
"v_add_f32 v50, v50, v54\n"
"v_add_f32 v51, v51, v55\n"
"v_add_f32_dpp v52, v48, v48 row_ror:8 row_mask:0xf bank_mask:0xf\n"
"v_add_f32_dpp v53, v49, v49 row_ror:8 row_mask:0xf bank_mask:0xf\n"
"v_add_f32_dpp v54, v50, v50 row_ror:8 row_mask:0xf bank_mask:0xf\n"
"v_add_f32_dpp v55, v51, v51 row_ror:8 row_mask:0xf bank_mask:0xf\n"
"v_mov_b32 v48, v52\n"
"v_mov_b32 v49, v53\n"
"v_mov_b32 v50, v54\n"
"v_mov_b32 v51, v55\n"
"v_permlane16_swap_b32 v48, v52\n"
"v_permlane16_swap_b32 v49, v53\n"
"v_permlane16_swap_b32 v50, v54\n"
"v_permlane16_swap_b32 v51, v55\n"
"v_add_f32 v52, v48, v52\n"
"v_add_f32 v53, v49, v53\n"
"v_add_f32 v54, v50, v54\n"
"v_add_f32 v55, v51, v55\n"
"v_mov_b32 v48, v52\n"
"v_mov_b32 v49, v53\n"
"v_mov_b32 v50, v54\n"
"v_mov_b32 v51, v55\n"
"v_permlane32_swap_b32 v48, v52\n"
"v_permlane32_swap_b32 v49, v53\n"
"v_permlane32_swap_b32 v50, v54\n"
"v_permlane32_swap_b32 v51, v55\n"
"v_add_f32 v52, v48, v52\n"
"v_add_f32 v53, v49, v53\n"
"v_add_f32 v54, v50, v54\n"
"v_add_f32 v55, v51, v55\n"
"v_mul_f32 v48, 0x3acccccd, v52\n"
"v_mul_f32 v50, 0x3acccccd, v54\n"
"v_fma_f32 v53, -v52, v48, v53\n"
"v_fma_f32 v55, -v54, v50, v55\n"
"v_fmamk_f32 v53, v53, 0x3acccccd, v41\n"
"v_fmamk_f32 v55, v55, 0x3acccccd, v41\n"
"v_max_f32 v53, v53, v41\n"
"v_max_f32 v55, v55, v41\n"
"v_rsq_f32 v49, v53\n"
"v_rsq_f32 v51, v55\n"
"v_mul_f32 v48, -v48, v49\n"
"v_mul_f32 v50, -v50, v51\n"
"v_min_u32 v49, -2, v49\n"
"v_min_u32 v51, -2, v51\n"
"v_min_u32 v48, -2, v48\n"
"v_min_u32 v50, -2, v50\n"
"buffer_store_dwordx4 v[48:51], v40, s[4:7], s33 offen sc1\n"
"s_nop 3\n"
".Lnr_378:\n"
"v_lshlrev_b32 v99, 4, v0\n"
"v_mov_b32 v98, 0x40000000\n"
"v_cmp_gt_i32 vcc, 16, v0\n"
"s_nop 1\n"
"v_cndmask_b32 v43, v98, v99, vcc\n"
"v_mov_b32 v44, -1\n"
"v_mov_b32 v45, -1\n"
"v_mov_b32 v46, -1\n"
"v_mov_b32 v47, -1\n"
"s_add_u32 s49, s31, 0xa60000\n"
"buffer_store_dwordx4 v[44:47], v2, s[4:7], s49 offen offset:0 sc1\n"
"buffer_store_dwordx4 v[44:47], v43, s[4:7], s49 offen offset:1024 sc1\n"
"s_add_u32 s49, s31, 0xba0000\n"
"buffer_store_dwordx4 v[44:47], v2, s[4:7], s49 offen offset:0 sc1\n"
"buffer_store_dwordx4 v[44:47], v43, s[4:7], s49 offen offset:1024 sc1\n"
"s_lshl_b32 s79, s2, 2\n"
"s_add_u32 s79, s79, s3\n"
"s_lshl_b32 s79, s79, 10\n"
"s_add_u32 s49, s79, 0x1628000\n"
"s_sub_u32 s79, 0x1687fff, s49\n"
"s_and_b32 s79, s79, 0x80000000\n"
"s_or_b32 s49, s49, s79\n"
"buffer_store_dwordx4 v[44:47], v2, s[4:7], s49 offen sc1\n"
"s_nop 1\n"
"s_setprio 0\n"
"s_cmp_lg_u32 s43, 0\n"
"s_cbranch_scc1 .Lsk_386\n"
"s_call_b64 s[46:47], .Lst_319\n"
".Lsk_386:\n"
"s_cmp_lg_u32 s43, 1\n"
"s_cbranch_scc1 .Lsk_387\n"
"s_call_b64 s[46:47], .Lst_320\n"
".Lsk_387:\n"
"s_cmp_lg_u32 s43, 2\n"
"s_cbranch_scc1 .Lsk_388\n"
"ds_read_b32 v3, v5 offset:144\n"
"s_waitcnt lgkmcnt(0)\n"
"v_cmp_eq_u32 vcc, 0, v3\n"
"s_cbranch_vccz .Len_385\n"
"s_call_b64 s[46:47], .Lst_321\n"
".Lsk_388:\n"
".Len_385:\n"
"s_setprio 2\n"
"s_mul_i32 s79, s28, 0xa0\n"
"s_add_u32 s79, s30, s79\n"
"s_and_b32 s78, s3, 1\n"
"s_mul_i32 s78, s78, 0x60\n"
"s_add_u32 s79, s79, s78\n"
"s_add_u32 s36, s79, 0x140000\n"
"s_mul_i32 s79, s3, 0x1400\n"
"s_add_u32 s79, s30, s79\n"
"s_add_u32 s32, s79, 0x588000\n"
"s_add_u32 s33, s32, 0x1000\n"
"s_lshl_b32 s79, s29, 1\n"
"s_lshr_b32 s78, s3, 1\n"
"s_xor_b32 s78, s78, 1\n"
"s_add_u32 s79, s79, s78\n"
"s_mul_i32 s79, s79, 0x5000\n"
"s_mul_i32 s78, s3, 0x1400\n"
"s_add_u32 s79, s79, s78\n"
"s_add_u32 s34, s79, 0x588000\n"
"s_add_u32 s35, s34, 0x1000\n"
"s_lshl_b32 s79, s30, 1\n"
"s_mul_i32 s78, s28, 0x1400\n"
"s_add_u32 s79, s79, s78\n"
"s_and_b32 s78, s3, 1\n"
"s_mul_i32 s78, s78, 0xc00\n"
"s_add_u32 s79, s79, s78\n"
"s_add_u32 s37, s79, 0x948000\n"
"s_lshl_b32 s79, s29, 1\n"
"s_lshr_b32 s78, s3, 1\n"
"s_add_u32 s79, s79, s78\n"
"s_lshl_b32 s79, s79, 11\n"
"s_lshl_b32 s78, s28, 8\n"
"s_add_u32 s79, s79, s78\n"
"s_and_b32 s78, s3, 1\n"
"s_lshl_b32 s78, s78, 7\n"
"s_add_u32 s79, s79, s78\n"
"s_add_u32 s38, s79, 0xbe8000\n"
"buffer_load_dwordx2 v[40:41], v14, s[8:11], s36 offen offset:0\n"
"buffer_load_dwordx2 v[42:43], v14, s[8:11], s36 offen offset:32\n"
"buffer_load_dwordx2 v[44:45], v15, s[8:11], s36 offen offset:64\n"
"s_mov_b32 s17, 0x100000\n"
".Ltp_389:\n"
"ds_read_b32 v3, v5 offset:176\n"
"s_sub_u32 s17, s17, 1\n"
"s_cbranch_scc1 .Lou_390\n"
"s_waitcnt lgkmcnt(0)\n"
"v_cmp_eq_u32 vcc, 0, v3\n"
"s_cbranch_vccnz .Ltp_389\n"
".Lou_390:\n"
"s_waitcnt lgkmcnt(0)\n"
"s_waitcnt vmcnt(2)\n"
"v_lshlrev_b32 v48, 16, v40\n"
"v_and_b32 v49, 0xffff0000, v40\n"
"v_lshlrev_b32 v50, 16, v41\n"
"v_and_b32 v51, 0xffff0000, v41\n"
"s_waitcnt vmcnt(1)\n"
"v_lshlrev_b32 v52, 16, v42\n"
"v_and_b32 v53, 0xffff0000, v42\n"
"v_lshlrev_b32 v54, 16, v43\n"
"v_and_b32 v55, 0xffff0000, v43\n"
"s_waitcnt vmcnt(0)\n"
"v_lshlrev_b32 v56, 16, v44\n"
"v_and_b32 v57, 0xffff0000, v44\n"
"v_lshlrev_b32 v58, 16, v45\n"
"v_and_b32 v59, 0xffff0000, v45\n"
"v_mov_b32 v46, 0\n"
"v_mov_b32 v47, 0\n"
"s_mov_b32 s17, 0x100000\n"
".Ltp_391:\n"
"s_sleep 1\n"
"ds_read_b32 v3, v5 offset:160\n"
"s_sub_u32 s17, s17, 1\n"
"s_cbranch_scc1 .Lou_392\n"
"s_waitcnt lgkmcnt(0)\n"
"v_cmp_eq_u32 vcc, 0, v3\n"
"s_cbranch_vccnz .Ltp_391\n"
".Lou_392:\n"
"s_waitcnt lgkmcnt(0)\n"
"s_cmp_eq_u32 s17, 0xfffff\n"
"s_cbranch_scc1 .Lsk_393\n"
"s_mov_b32 s89, 24\n"
".Ldl_395:\n"
"s_sub_u32 s89, s89, 1\n"
"s_cbranch_scc1 .Lde_396\n"
"s_sleep 1\n"
"s_branch .Ldl_395\n"
".Lde_396:\n"
"s_branch .Ljn_394\n"
".Lsk_393:\n"
".Ljn_394:\n"
"buffer_load_dwordx4 v[108:111], v2, s[4:7], s34 offen offset:0 sc1\n"
"s_nop 9\n"
"buffer_load_dwordx4 v[112:115], v2, s[4:7], s34 offen offset:1024 sc1\n"
"s_nop 9\n"
"buffer_load_dwordx4 v[116:119], v2, s[4:7], s34 offen offset:2048 sc1\n"
"s_nop 9\n"
"buffer_load_dwordx4 v[120:123], v2, s[4:7], s34 offen offset:3072 sc1\n"
"s_nop 9\n"
"buffer_load_dwordx4 v[124:127], v2, s[4:7], s35 offen offset:0 sc1\n"
"s_nop 9\n"
"buffer_load_dwordx4 v[88:91], v2, s[4:7], s32 offen offset:0 sc1\n"
"s_nop 9\n"
"buffer_load_dwordx4 v[92:95], v2, s[4:7], s32 offen offset:1024 sc1\n"
"s_nop 9\n"
"buffer_load_dwordx4 v[96:99], v2, s[4:7], s32 offen offset:2048 sc1\n"
"s_nop 9\n"
"buffer_load_dwordx4 v[100:103], v2, s[4:7], s32 offen offset:3072 sc1\n"
"s_nop 9\n"
"buffer_load_dwordx4 v[104:107], v2, s[4:7], s33 offen offset:0 sc1\n"
"s_waitcnt vmcnt(9)\n"
"v_max_u32 v38, v108, v109\n"
"v_max3_u32 v38, v110, v111, v38\n"
"v_cmp_eq_u32 vcc, -1, v38\n"
"s_cbranch_vccnz .Lsl_397\n"
".Lbk_398:\n"
"v_mfma_f32_16x16x32_bf16 v[68:71], v[156:159], v[108:111], 0\n"
"v_mfma_f32_16x16x32_bf16 v[72:75], v[160:163], v[108:111], 0\n"
"v_mfma_f32_16x16x32_bf16 v[76:79], v[164:167], v[108:111], 0\n"
"s_waitcnt vmcnt(8)\n"
"v_max_u32 v38, v112, v113\n"
"v_max3_u32 v38, v114, v115, v38\n"
"v_mfma_f32_16x16x32_bf16 v[80:83], v[168:171], v[108:111], 0\n"
"v_cmp_eq_u32 vcc, -1, v38\n"
"v_mfma_f32_16x16x32_bf16 v[84:87], v[172:175], v[108:111], 0\n"
"s_cbranch_vccnz .Lsl_399\n"
".Lbk_400:\n"
"v_mfma_f32_16x16x32_bf16 v[68:71], v[176:179], v[112:115], v[68:71]\n"
"v_mfma_f32_16x16x32_bf16 v[72:75], v[180:183], v[112:115], v[72:75]\n"
"v_mfma_f32_16x16x32_bf16 v[76:79], v[184:187], v[112:115], v[76:79]\n"
"s_waitcnt vmcnt(7)\n"
"v_max_u32 v38, v116, v117\n"
"v_max3_u32 v38, v118, v119, v38\n"
"v_mfma_f32_16x16x32_bf16 v[80:83], v[188:191], v[112:115], v[80:83]\n"
"v_cmp_eq_u32 vcc, -1, v38\n"
"v_mfma_f32_16x16x32_bf16 v[84:87], v[192:195], v[112:115], v[84:87]\n"
"s_cbranch_vccnz .Lsl_401\n"
".Lbk_402:\n"
"v_mfma_f32_16x16x32_bf16 v[68:71], v[196:199], v[116:119], v[68:71]\n"
"v_mfma_f32_16x16x32_bf16 v[72:75], v[200:203], v[116:119], v[72:75]\n"
"v_mfma_f32_16x16x32_bf16 v[76:79], v[204:207], v[116:119], v[76:79]\n"
"s_waitcnt vmcnt(6)\n"
"v_max_u32 v38, v120, v121\n"
"v_max3_u32 v38, v122, v123, v38\n"
"v_mfma_f32_16x16x32_bf16 v[80:83], v[208:211], v[116:119], v[80:83]\n"
"v_cmp_eq_u32 vcc, -1, v38\n"
"v_mfma_f32_16x16x32_bf16 v[84:87], v[212:215], v[116:119], v[84:87]\n"
"s_cbranch_vccnz .Lsl_403\n"
".Lbk_404:\n"
"v_mfma_f32_16x16x32_bf16 v[68:71], v[216:219], v[120:123], v[68:71]\n"
"v_mfma_f32_16x16x32_bf16 v[72:75], v[220:223], v[120:123], v[72:75]\n"
"v_mfma_f32_16x16x32_bf16 v[76:79], v[224:227], v[120:123], v[76:79]\n"
"s_waitcnt vmcnt(5)\n"
"v_max_u32 v38, v124, v125\n"
"v_max3_u32 v38, v126, v127, v38\n"
"v_mfma_f32_16x16x32_bf16 v[80:83], v[228:231], v[120:123], v[80:83]\n"
"v_cmp_eq_u32 vcc, -1, v38\n"
"v_mfma_f32_16x16x32_bf16 v[84:87], v[232:235], v[120:123], v[84:87]\n"
"s_cbranch_vccnz .Lsl_405\n"
".Lbk_406:\n"
"v_mfma_f32_16x16x32_bf16 v[68:71], v[236:239], v[124:127], v[68:71]\n"
"v_mfma_f32_16x16x32_bf16 v[72:75], v[240:243], v[124:127], v[72:75]\n"
"v_mfma_f32_16x16x32_bf16 v[76:79], v[244:247], v[124:127], v[76:79]\n"
"s_waitcnt vmcnt(4)\n"
"v_max_u32 v38, v88, v89\n"
"v_max3_u32 v38, v90, v91, v38\n"
"v_mfma_f32_16x16x32_bf16 v[80:83], v[248:251], v[124:127], v[80:83]\n"
"v_cmp_eq_u32 vcc, -1, v38\n"
"v_mfma_f32_16x16x32_bf16 v[84:87], v[252:255], v[124:127], v[84:87]\n"
"ds_write_b128 v8, v[68:71] offset:0\n"
"s_cbranch_vccnz .Lsl_407\n"
".Lbk_408:\n"
"v_mfma_f32_16x16x32_bf16 v[56:59], v[164:167], v[88:91], v[56:59]\n"
"s_waitcnt vmcnt(3)\n"
"v_max_u32 v38, v92, v93\n"
"v_max3_u32 v38, v94, v95, v38\n"
"ds_write_b128 v8, v[72:75] offset:1024\n"
"v_mfma_f32_16x16x32_bf16 v[60:63], v[168:171], v[88:91], 0\n"
"v_cmp_eq_u32 vcc, -1, v38\n"
"ds_write_b128 v10, v[76:79] offset:0\n"
"v_mfma_f32_16x16x32_bf16 v[64:67], v[172:175], v[88:91], 0\n"
"v_mov_b32 v117, s16\n"
"s_cbranch_vccnz .Lsl_409\n"
".Lbk_410:\n"
"v_mfma_f32_16x16x32_bf16 v[56:59], v[184:187], v[92:95], v[56:59]\n"
"s_waitcnt vmcnt(2)\n"
"v_max_u32 v38, v96, v97\n"
"v_max3_u32 v38, v98, v99, v38\n"
"v_mov_b32 v119, s16\n"
"v_mfma_f32_16x16x32_bf16 v[60:63], v[188:191], v[92:95], v[60:63]\n"
"v_cmp_eq_u32 vcc, -1, v38\n"
"ds_write_b128 v9, v[80:83] offset:0\n"
"v_mfma_f32_16x16x32_bf16 v[64:67], v[192:195], v[92:95], v[64:67]\n"
"ds_write_b128 v9, v[84:87] offset:1024\n"
"s_cbranch_vccnz .Lsl_411\n"
".Lbk_412:\n"
"v_mfma_f32_16x16x32_bf16 v[56:59], v[204:207], v[96:99], v[56:59]\n"
"s_waitcnt vmcnt(1)\n"
"v_max_u32 v38, v100, v101\n"
"v_max3_u32 v38, v102, v103, v38\n"
"ds_write_b32 v4, v4 offset:624\n"
"v_mfma_f32_16x16x32_bf16 v[60:63], v[208:211], v[96:99], v[60:63]\n"
"v_cmp_eq_u32 vcc, -1, v38\n"
"v_mov_b32 v121, s16\n"
"v_mov_b32 v123, s16\n"
"v_mfma_f32_16x16x32_bf16 v[64:67], v[212:215], v[96:99], v[64:67]\n"
"v_mov_b32 v125, s16\n"
"s_cbranch_vccnz .Lsl_413\n"
".Lbk_414:\n"
"v_mfma_f32_16x16x32_bf16 v[56:59], v[224:227], v[100:103], v[56:59]\n"
"s_waitcnt vmcnt(0)\n"
"v_max_u32 v38, v104, v105\n"
"v_max3_u32 v38, v106, v107, v38\n"
"v_mov_b32 v127, s16\n"
"v_mfma_f32_16x16x32_bf16 v[60:63], v[228:231], v[100:103], v[60:63]\n"
"v_cmp_eq_u32 vcc, -1, v38\n"
"ds_read_b32 v3, v5 offset:624\n"
"v_mfma_f32_16x16x32_bf16 v[64:67], v[232:235], v[100:103], v[64:67]\n"
"s_cbranch_vccnz .Lsl_415\n"
".Lbk_416:\n"
"v_mfma_f32_16x16x32_bf16 v[56:59], v[244:247], v[104:107], v[56:59]\n"
"ds_read_b128 v[76:79], v12 offset:2048\n"
"ds_read_b128 v[108:111], v13 offset:2048\n"
"v_mfma_f32_16x16x32_bf16 v[60:63], v[248:251], v[104:107], v[60:63]\n"
"ds_read_b128 v[68:71], v12 offset:0\n"
"ds_read_b128 v[80:83], v13 offset:0\n"
"v_mfma_f32_16x16x32_bf16 v[64:67], v[252:255], v[104:107], v[64:67]\n"
"ds_read_b128 v[72:75], v12 offset:1024\n"
"ds_read_b128 v[84:87], v13 offset:1024\n"
"v_mfma_f32_16x16x32_bf16 v[48:51], v[156:159], v[88:91], v[48:51]\n"
"ds_write_b128 v7, v[56:59] offset:0\n"
"v_mfma_f32_16x16x32_bf16 v[52:55], v[160:163], v[88:91], v[52:55]\n"
"ds_write_b128 v6, v[60:63] offset:0\n"
"v_mfma_f32_16x16x32_bf16 v[48:51], v[176:179], v[92:95], v[48:51]\n"
"ds_write_b128 v6, v[64:67] offset:1024\n"
"v_mfma_f32_16x16x32_bf16 v[52:55], v[180:183], v[92:95], v[52:55]\n"
"ds_write_b32 v4, v4 offset:640\n"
"v_mfma_f32_16x16x32_bf16 v[48:51], v[196:199], v[96:99], v[48:51]\n"
"s_mov_b32 s17, 0x100000\n"
"s_waitcnt lgkmcnt(10)\n"
"v_cmp_eq_u32 vcc, 0, v3\n"
"s_cbranch_vccz .Lok_418\n"
".Ltp_417:\n"
"ds_read_b32 v3, v5 offset:624\n"
"ds_read_b128 v[76:79], v12 offset:2048\n"
"ds_read_b128 v[108:111], v13 offset:2048\n"
"ds_read_b128 v[68:71], v12 offset:0\n"
"ds_read_b128 v[80:83], v13 offset:0\n"
"ds_read_b128 v[72:75], v12 offset:1024\n"
"ds_read_b128 v[84:87], v13 offset:1024\n"
"s_sub_u32 s17, s17, 1\n"
"s_cbranch_scc1 .Lok_418\n"
"s_waitcnt lgkmcnt(6)\n"
"v_cmp_eq_u32 vcc, 0, v3\n"
"s_cbranch_vccnz .Ltp_417\n"
"s_waitcnt lgkmcnt(0)\n"
".Lok_418:\n"
"v_mfma_f32_16x16x32_bf16 v[52:55], v[200:203], v[96:99], v[52:55]\n"
"s_waitcnt lgkmcnt(8)\n"
"v_add_f32 v76, v76, v108\n"
"v_add_f32 v77, v77, v109\n"
"v_add_f32 v78, v78, v110\n"
"v_mfma_f32_16x16x32_bf16 v[48:51], v[216:219], v[100:103], v[48:51]\n"
"v_add_f32 v79, v79, v111\n"
"s_waitcnt lgkmcnt(6)\n"
"v_add_f32 v68, v68, v80\n"
"v_add_f32 v69, v69, v81\n"
"v_mfma_f32_16x16x32_bf16 v[52:55], v[220:223], v[100:103], v[52:55]\n"
"v_add_f32 v70, v70, v82\n"
"v_add_f32 v71, v71, v83\n"
"s_waitcnt lgkmcnt(4)\n"
"v_add_f32 v72, v72, v84\n"
"v_mfma_f32_16x16x32_bf16 v[48:51], v[236:239], v[104:107], v[48:51]\n"
"v_add_f32 v73, v73, v85\n"
"v_add_f32 v74, v74, v86\n"
"v_add_f32 v75, v75, v87\n"
"v_mfma_f32_16x16x32_bf16 v[52:55], v[240:243], v[104:107], v[52:55]\n"
"ds_read_b32 v3, v5 offset:640\n"
"ds_read_b128 v[112:115], v11 offset:2048\n"
"ds_read_b128 v[60:63], v11 offset:0\n"
"ds_read_b128 v[64:67], v11 offset:1024\n"
"v_add_f32 v56, v56, v76\n"
"v_add_f32 v57, v57, v77\n"
"v_add_f32 v58, v58, v78\n"
"v_add_f32 v59, v59, v79\n"
"v_add_f32 v48, v48, v68\n"
"v_add_f32 v49, v49, v69\n"
"v_add_f32 v50, v50, v70\n"
"v_add_f32 v51, v51, v71\n"
"v_add_f32 v52, v52, v72\n"
"v_add_f32 v53, v53, v73\n"
"v_add_f32 v54, v54, v74\n"
"v_add_f32 v55, v55, v75\n"
"s_mov_b32 s17, 0x100000\n"
"s_waitcnt lgkmcnt(3)\n"
"v_cmp_eq_u32 vcc, 0, v3\n"
"s_cbranch_vccz .Lok_420\n"
".Ltp_419:\n"
"ds_read_b32 v3, v5 offset:640\n"
"ds_read_b128 v[112:115], v11 offset:2048\n"
"ds_read_b128 v[60:63], v11 offset:0\n"
"ds_read_b128 v[64:67], v11 offset:1024\n"
"s_sub_u32 s17, s17, 1\n"
"s_cbranch_scc1 .Lok_420\n"
"s_waitcnt lgkmcnt(3)\n"
"v_cmp_eq_u32 vcc, 0, v3\n"
"s_cbranch_vccnz .Ltp_419\n"
"s_waitcnt lgkmcnt(0)\n"
".Lok_420:\n"
"s_waitcnt lgkmcnt(2)\n"
"v_add_f32 v56, v56, v112\n"
"v_add_f32 v57, v57, v113\n"
"v_add_f32 v58, v58, v114\n"
"v_add_f32 v59, v59, v115\n"
"v_cvt_pk_bf16_f32 v44, v56, v57\n"
"v_cvt_pk_bf16_f32 v45, v58, v59\n"
"v_and_b32 v44, v19, v44\n"
"v_and_b32 v45, v19, v45\n"
"s_waitcnt lgkmcnt(1)\n"
"v_add_f32 v48, v48, v60\n"
"v_add_f32 v49, v49, v61\n"
"v_add_f32 v50, v50, v62\n"
"v_add_f32 v51, v51, v63\n"
"v_cvt_pk_bf16_f32 v40, v48, v49\n"
"v_cvt_pk_bf16_f32 v41, v50, v51\n"
"v_mfma_f32_16x16x32_bf16 v[88:91], v[44:47], v[44:47], 0\n"
"v_mfma_f32_16x16x32_bf16 v[92:95], v[20:23], v[44:47], 0\n"
"s_waitcnt lgkmcnt(0)\n"
"v_add_f32 v52, v52, v64\n"
"v_add_f32 v53, v53, v65\n"
"v_add_f32 v54, v54, v66\n"
"v_add_f32 v55, v55, v67\n"
"v_cvt_pk_bf16_f32 v42, v52, v53\n"
"v_cvt_pk_bf16_f32 v43, v54, v55\n"
"v_cvt_pk_bf16_f32 v116, v48, v49\n"
"v_cvt_pk_bf16_f32 v118, v50, v51\n"
"v_mfma_f32_16x16x32_bf16 v[88:91], v[40:43], v[40:43], v[88:91]\n"
"v_mfma_f32_16x16x32_bf16 v[92:95], v[20:23], v[40:43], v[92:95]\n"
"v_cvt_pk_bf16_f32 v120, v52, v53\n"
"v_cvt_pk_bf16_f32 v122, v54, v55\n"
"v_cvt_pk_bf16_f32 v124, v56, v57\n"
"v_cvt_pk_bf16_f32 v126, v58, v59\n"
"s_nop 0\n"
"v_cndmask_b32 v101, v88, v90, s[44:45]\n"
"v_cndmask_b32 v88, v89, v91, s[44:45]\n"
"s_nop 0\n"
"v_min_u32 v100, -2, v92\n"
"v_min_u32 v88, -2, v88\n"
"v_min_u32 v101, -2, v101\n"
"v_mov_b32_dpp v102, v100 quad_perm:[1,0,3,2] row_mask:0xf bank_mask:0xf\n"
"v_mov_b32_dpp v103, v88 quad_perm:[1,0,3,2] row_mask:0xf bank_mask:0xf\n"
"buffer_store_dwordx4 v[100:103], v16, s[4:7], s38 offen sc1\n"
"buffer_store_dwordx4 v[116:119], v17, s[4:7], s37 offen offset:0 sc1\n"
"buffer_store_dwordx4 v[120:123], v17, s[4:7], s37 offen offset:1024 sc1\n"
"buffer_store_dwordx4 v[124:127], v18, s[4:7], s37 offen offset:2048 sc1\n"
"ds_write_b32 v4, v4 offset:256\n"
"s_xor_b32 s79, s3, 1\n"
"s_lshl_b32 s79, s79, 8\n"
"s_add_u32 s32, s19, s79\n"
"s_cmp_ge_u32 s32, 64\n"
"s_cbranch_scc1 .Lnr_421\n"
"s_lshl_b32 s79, s19, 11\n"
"s_add_u32 s32, s79, 0xbe8000\n"
"s_lshl_b32 s79, s19, 7\n"
"s_add_u32 s33, s79, 0xc0a000\n"
"v_lshrrev_b32 v99, 3, v0\n"
"v_and_b32 v98, 7, v0\n"
"v_lshlrev_b32 v98, 4, v98\n"
"v_lshl_add_u32 v40, v99, 14, v98\n"
"v_mov_b32 v41, 0x3727c5ac\n"
"s_mov_b32 s89, 12\n"
".Ldl_422:\n"
"s_sub_u32 s89, s89, 1\n"
"s_cbranch_scc1 .Lde_423\n"
"s_sleep 1\n"
"s_branch .Ldl_422\n"
".Lde_423:\n"
"buffer_load_dwordx4 v[48:51], v2, s[4:7], s32 offen offset:0 sc1\n"
"buffer_load_dwordx4 v[52:55], v2, s[4:7], s32 offen offset:1024 sc1\n"
"s_mov_b32 s17, 32768\n"
".Ltp_424:\n"
"s_waitcnt vmcnt(0)\n"
"s_sub_u32 s17, s17, 1\n"
"s_cbranch_scc1 .Lou_425\n"
"v_max_u32 v38, v48, v49\n"
"v_max3_u32 v38, v50, v51, v38\n"
"v_max3_u32 v38, v52, v53, v38\n"
"v_max3_u32 v38, v54, v55, v38\n"
"v_cmp_eq_u32 vcc, -1, v38\n"
"s_cbranch_vccz .Lou_425\n"
"v_max_u32 v38, v50, v51\n"
"v_max3_u32 v38, v48, v49, v38\n"
"v_cmp_eq_u32 vcc, -1, v38\n"
"s_cbranch_vccz .Lsk_426\n"
"s_mov_b64 exec, vcc\n"
"buffer_load_dwordx4 v[48:51], v2, s[4:7], s32 offen offset:0 sc1\n"
"s_mov_b64 exec, -1\n"
".Lsk_426:\n"
"v_max_u32 v38, v54, v55\n"
"v_max3_u32 v38, v52, v53, v38\n"
"v_cmp_eq_u32 vcc, -1, v38\n"
"s_cbranch_vccz .Lsk_427\n"
"s_mov_b64 exec, vcc\n"
"buffer_load_dwordx4 v[52:55], v2, s[4:7], s32 offen offset:1024 sc1\n"
"s_mov_b64 exec, -1\n"
".Lsk_427:\n"
"s_branch .Ltp_424\n"
".Lou_425:\n"
"s_waitcnt vmcnt(0)\n"
"v_add_f32 v48, v48, v52\n"
"v_add_f32 v49, v49, v53\n"
"v_add_f32 v50, v50, v54\n"
"v_add_f32 v51, v51, v55\n"
"v_add_f32_dpp v52, v48, v48 row_ror:8 row_mask:0xf bank_mask:0xf\n"
"v_add_f32_dpp v53, v49, v49 row_ror:8 row_mask:0xf bank_mask:0xf\n"
"v_add_f32_dpp v54, v50, v50 row_ror:8 row_mask:0xf bank_mask:0xf\n"
"v_add_f32_dpp v55, v51, v51 row_ror:8 row_mask:0xf bank_mask:0xf\n"
"v_mov_b32 v48, v52\n"
"v_mov_b32 v49, v53\n"
"v_mov_b32 v50, v54\n"
"v_mov_b32 v51, v55\n"
"v_permlane16_swap_b32 v48, v52\n"
"v_permlane16_swap_b32 v49, v53\n"
"v_permlane16_swap_b32 v50, v54\n"
"v_permlane16_swap_b32 v51, v55\n"
"v_add_f32 v52, v48, v52\n"
"v_add_f32 v53, v49, v53\n"
"v_add_f32 v54, v50, v54\n"
"v_add_f32 v55, v51, v55\n"
"v_mov_b32 v48, v52\n"
"v_mov_b32 v49, v53\n"
"v_mov_b32 v50, v54\n"
"v_mov_b32 v51, v55\n"
"v_permlane32_swap_b32 v48, v52\n"
"v_permlane32_swap_b32 v49, v53\n"
"v_permlane32_swap_b32 v50, v54\n"
"v_permlane32_swap_b32 v51, v55\n"
"v_add_f32 v52, v48, v52\n"
"v_add_f32 v53, v49, v53\n"
"v_add_f32 v54, v50, v54\n"
"v_add_f32 v55, v51, v55\n"
"v_mul_f32 v48, 0x3acccccd, v52\n"
"v_mul_f32 v50, 0x3acccccd, v54\n"
"v_fma_f32 v53, -v52, v48, v53\n"
"v_fma_f32 v55, -v54, v50, v55\n"
"v_fmamk_f32 v53, v53, 0x3acccccd, v41\n"
"v_fmamk_f32 v55, v55, 0x3acccccd, v41\n"
"v_max_f32 v53, v53, v41\n"
"v_max_f32 v55, v55, v41\n"
"v_rsq_f32 v49, v53\n"
"v_rsq_f32 v51, v55\n"
"v_mul_f32 v48, -v48, v49\n"
"v_mul_f32 v50, -v50, v51\n"
"v_min_u32 v49, -2, v49\n"
"v_min_u32 v51, -2, v51\n"
"v_min_u32 v48, -2, v48\n"
"v_min_u32 v50, -2, v50\n"
"buffer_store_dwordx4 v[48:51], v40, s[4:7], s33 offen sc1\n"
"s_nop 3\n"
".Lnr_421:\n"
"s_cmp_lt_i32 s18, 0\n"
"s_cbranch_scc1 .Lnc_428\n"
"s_nop 0\n"
"s_setprio 0\n"
"s_cmp_lg_u32 s43, 0\n"
"s_cbranch_scc1 .Lsk_430\n"
"s_call_b64 s[46:47], .Lst_319\n"
".Lsk_430:\n"
"s_cmp_lg_u32 s43, 1\n"
"s_cbranch_scc1 .Lsk_431\n"
"s_call_b64 s[46:47], .Lst_320\n"
".Lsk_431:\n"
"s_cmp_lg_u32 s43, 2\n"
"s_cbranch_scc1 .Lsk_432\n"
"s_call_b64 s[46:47], .Lst_321\n"
".Lsk_432:\n"
".Len_429:\n"
"s_setprio 2\n"
"s_mov_b32 s17, 0x100000\n"
".Ltp_433:\n"
"s_sleep 1\n"
"ds_read_b32 v3, v5 offset:208\n"
"s_sub_u32 s17, s17, 1\n"
"s_cbranch_scc1 .Lou_434\n"
"s_waitcnt lgkmcnt(0)\n"
"v_cmp_eq_u32 vcc, 0, v3\n"
"s_cbranch_vccnz .Ltp_433\n"
".Lou_434:\n"
"s_waitcnt lgkmcnt(0)\n"
"s_setprio 0\n"
"s_lshl_b32 s79, s18, 8\n"
"s_add_u32 s32, s79, 0x948000\n"
"s_add_u32 s33, s32, 0x140000\n"
"s_and_b32 s79, s2, 7\n"
"s_lshl_b32 s79, s79, 14\n"
"s_add_u32 s34, s79, 0xc0a000\n"
"s_add_u32 s35, s34, 0x1000\n"
"v_and_b32 v126, 0xffffff80, v29\n"
"v_add_u32 v126, v126, v29\n"
"ds_read_b128 v[88:91], v25 offset:448\n"
"ds_read_b128 v[92:95], v25 offset:432\n"
"ds_read_b64 v[96:97], v24 offset:448\n"
"ds_read_b64 v[98:99], v24 offset:456\n"
"ds_read_b128 v[100:103], v25 offset:960\n"
"ds_read_b128 v[104:107], v25 offset:1472\n"
"s_waitcnt lgkmcnt(1)\n"
"v_mfma_f32_16x16x32_f16 v[64:67], v[88:91], v[144:147], 0\n"
"ds_read_b128 v[40:43], v25 offset:1984\n"
"ds_read_b64 v[44:45], v24 offset:960\n"
"v_mfma_f32_16x16x32_f16 v[68:71], v[92:95], v[148:151], 0\n"
"ds_read_b64 v[46:47], v24 offset:968\n"
"ds_read_b64 v[72:73], v24 offset:1472\n"
"v_mfma_f32_16x16x32_f16 v[48:51], v[152:155], v[88:91], 0\n"
"ds_read_b64 v[74:75], v24 offset:1480\n"
"ds_read_b64 v[76:77], v24 offset:384\n"
"v_mfma_f32_16x16x32_f16 v[48:51], v[96:99], v[100:103], v[48:51]\n"
"ds_read_b64 v[78:79], v24 offset:392\n"
"s_waitcnt lgkmcnt(4)\n"
"v_mfma_f32_16x16x32_f16 v[52:55], v[96:99], v[104:107], 0\n"
"ds_read_b128 v[84:87], v25 offset:896\n"
"ds_read_b128 v[92:95], v25 offset:1408\n"
"v_mfma_f32_16x16x32_f16 v[56:59], v[96:99], v[40:43], 0\n"
"ds_read_b128 v[88:91], v25 offset:1920\n"
"ds_read_b64 v[100:101], v24 offset:896\n"
"v_mfma_f32_16x16x32_f16 v[52:55], v[44:47], v[40:43], v[52:55]\n"
"ds_read_b64 v[102:103], v24 offset:904\n"
"ds_read_b64 v[96:97], v24 offset:1408\n"
"v_cvt_pk_f16_f32 v80, v64, v65\n"
"v_mfma_f32_16x16x32_f16 v[48:51], v[44:47], v[104:107], v[48:51]\n"
"ds_read_b64 v[98:99], v24 offset:1416\n"
"s_waitcnt lgkmcnt(4)\n"
"v_mfma_f32_16x16x32_f16 v[48:51], v[72:75], v[40:43], v[48:51]\n"
"ds_read_b128 v[104:107], v25 offset:960\n"
"v_cvt_pk_f16_f32 v81, v66, v67\n"
"v_mfma_f32_16x16x32_f16 v[48:51], v[76:79], v[84:87], v[48:51]\n"
"ds_read_b128 v[44:47], v25 offset:944\n"
"v_cvt_pk_f16_f32 v82, v68, v69\n"
"v_mfma_f32_16x16x32_f16 v[52:55], v[76:79], v[92:95], v[52:55]\n"
"ds_read_b64 v[40:41], v24 offset:320\n"
"ds_read_b64 v[42:43], v24 offset:328\n"
"v_mfma_f32_16x16x32_f16 v[56:59], v[76:79], v[88:91], v[56:59]\n"
"ds_read_b128 v[72:75], v25 offset:832\n"
"s_waitcnt lgkmcnt(5)\n"
"v_mfma_f32_16x16x32_f16 v[52:55], v[100:103], v[88:91], v[52:55]\n"
"ds_read_b128 v[84:87], v25 offset:1344\n"
"v_cvt_pk_f16_f32 v83, v70, v71\n"
"v_mfma_f32_16x16x32_f16 v[48:51], v[100:103], v[92:95], v[48:51]\n"
"ds_read_b128 v[76:79], v25 offset:1856\n"
"ds_read_b64 v[92:93], v24 offset:832\n"
"v_mfma_f32_16x16x32_f16 v[48:51], v[96:99], v[88:91], v[48:51]\n"
"ds_read_b64 v[94:95], v24 offset:840\n"
"v_mfma_f32_16x16x32_f16 v[48:51], v[80:83], v[128:131], v[48:51]\n"
"ds_read_b64 v[100:101], v24 offset:1344\n"
"s_waitcnt lgkmcnt(9)\n"
"v_mfma_f32_16x16x32_f16 v[52:55], v[80:83], v[132:135], v[52:55]\n"
"ds_read_b64 v[102:103], v24 offset:1352\n"
"ds_read_b64 v[88:89], v24 offset:256\n"
"v_mfma_f32_16x16x32_f16 v[56:59], v[80:83], v[136:139], v[56:59]\n"
"ds_read_b64 v[90:91], v24 offset:264\n"
"v_mfma_f32_16x16x32_f16 v[60:63], v[80:83], v[140:143], 0\n"
"ds_read_b128 v[96:99], v25 offset:768\n"
"v_mfma_f32_16x16x32_f16 v[64:67], v[104:107], v[144:147], 0\n"
"s_waitcnt lgkmcnt(8)\n"
"v_mfma_f32_16x16x32_f16 v[68:71], v[44:47], v[148:151], 0\n"
"ds_read_b128 v[44:47], v25 offset:1280\n"
"v_mfma_f32_16x16x32_f16 v[52:55], v[152:155], v[104:107], v[52:55]\n"
"ds_read_b128 v[104:107], v25 offset:1792\n"
"v_mfma_f32_16x16x32_f16 v[48:51], v[40:43], v[72:75], v[48:51]\n"
"ds_read_b64 v[72:73], v24 offset:768\n"
"v_mfma_f32_16x16x32_f16 v[52:55], v[40:43], v[84:87], v[52:55]\n"
"ds_read_b64 v[74:75], v24 offset:776\n"
"s_waitcnt lgkmcnt(7)\n"
"v_mfma_f32_16x16x32_f16 v[56:59], v[40:43], v[76:79], v[56:59]\n"
"ds_read_b64 v[40:41], v24 offset:1280\n"
"v_mfma_f32_16x16x32_f16 v[52:55], v[92:95], v[76:79], v[52:55]\n"
"ds_read_b64 v[42:43], v24 offset:1288\n"
"v_cvt_pk_f16_f32 v80, v64, v65\n"
"v_mfma_f32_16x16x32_f16 v[48:51], v[92:95], v[84:87], v[48:51]\n"
"ds_read_b128 v[84:87], v25 offset:1472\n"
"v_cvt_pk_f16_f32 v81, v66, v67\n"
"v_mfma_f32_16x16x32_f16 v[48:51], v[100:103], v[76:79], v[48:51]\n"
"ds_read_b128 v[92:95], v25 offset:1456\n"
"s_waitcnt lgkmcnt(4)\n"
"v_mfma_f32_16x16x32_f16 v[48:51], v[88:91], v[96:99], v[48:51]\n"
"ds_read_b64 v[76:77], v24 offset:192\n"
"ds_read_b64 v[78:79], v24 offset:200\n"
"v_mfma_f32_16x16x32_f16 v[52:55], v[88:91], v[44:47], v[52:55]\n"
"ds_read_b128 v[100:103], v25 offset:704\n"
"v_cvt_pk_f16_f32 v82, v68, v69\n"
"v_mfma_f32_16x16x32_f16 v[56:59], v[88:91], v[104:107], v[56:59]\n"
"ds_read_b128 v[96:99], v25 offset:1216\n"
"v_cvt_pk_f16_f32 v83, v70, v71\n"
"v_mfma_f32_16x16x32_f16 v[52:55], v[72:75], v[104:107], v[52:55]\n"
"ds_read_b128 v[88:91], v25 offset:1728\n"
"s_waitcnt lgkmcnt(7)\n"
"v_mfma_f32_16x16x32_f16 v[48:51], v[72:75], v[44:47], v[48:51]\n"
"ds_read_b64 v[44:45], v24 offset:704\n"
"v_mfma_f32_16x16x32_f16 v[48:51], v[40:43], v[104:107], v[48:51]\n"
"ds_read_b64 v[46:47], v24 offset:712\n"
"ds_read_b64 v[72:73], v24 offset:1216\n"
"v_mfma_f32_16x16x32_f16 v[52:55], v[80:83], v[128:131], v[52:55]\n"
"ds_read_b64 v[74:75], v24 offset:1224\n"
"ds_read_b64 v[104:105], v24 offset:128\n"
"v_mfma_f32_16x16x32_f16 v[56:59], v[80:83], v[132:135], v[56:59]\n"
"ds_read_b64 v[106:107], v24 offset:136\n"
"s_waitcnt lgkmcnt(11)\n"
"v_mfma_f32_16x16x32_f16 v[60:63], v[80:83], v[136:139], v[60:63]\n"
"ds_read_b128 v[40:43], v25 offset:640\n"
"v_mfma_f32_16x16x32_f16 v[64:67], v[84:87], v[144:147], 0\n"
"v_mfma_f32_16x16x32_f16 v[68:71], v[92:95], v[148:151], 0\n"
"ds_read_b128 v[92:95], v25 offset:1152\n"
"v_mfma_f32_16x16x32_f16 v[56:59], v[152:155], v[84:87], v[56:59]\n"
"ds_read_b128 v[84:87], v25 offset:1664\n"
"s_waitcnt lgkmcnt(7)\n"
"v_mfma_f32_16x16x32_f16 v[48:51], v[76:79], v[100:103], v[48:51]\n"
"ds_read_b64 v[100:101], v24 offset:640\n"
"v_mfma_f32_16x16x32_f16 v[52:55], v[76:79], v[96:99], v[52:55]\n"
"ds_read_b64 v[102:103], v24 offset:648\n"
"v_mfma_f32_16x16x32_f16 v[56:59], v[76:79], v[88:91], v[56:59]\n"
"ds_read_b64 v[76:77], v24 offset:1152\n"
"v_mfma_f32_16x16x32_f16 v[52:55], v[44:47], v[88:91], v[52:55]\n"
"ds_read_b64 v[78:79], v24 offset:1160\n"
"s_waitcnt lgkmcnt(5)\n"
"v_mfma_f32_16x16x32_f16 v[48:51], v[44:47], v[96:99], v[48:51]\n"
"ds_read_b128 v[96:99], v25 offset:1984\n"
"v_cvt_pk_f16_f32 v80, v64, v65\n"
"v_mfma_f32_16x16x32_f16 v[48:51], v[72:75], v[88:91], v[48:51]\n"
"ds_read_b128 v[44:47], v25 offset:1968\n"
"ds_read_b64 v[88:89], v24 offset:64\n"
"v_mfma_f32_16x16x32_f16 v[48:51], v[104:107], v[40:43], v[48:51]\n"
"ds_read_b64 v[90:91], v24 offset:72\n"
"ds_read_b128 v[72:75], v25 offset:576\n"
"v_mfma_f32_16x16x32_f16 v[52:55], v[104:107], v[92:95], v[52:55]\n"
"ds_read_b128 v[40:43], v25 offset:1088\n"
"s_waitcnt lgkmcnt(6)\n"
"v_mfma_f32_16x16x32_f16 v[56:59], v[104:107], v[84:87], v[56:59]\n"
"ds_read_b128 v[104:107], v25 offset:1600\n"
"v_cvt_pk_f16_f32 v81, v66, v67\n"
"v_mfma_f32_16x16x32_f16 v[52:55], v[100:103], v[84:87], v[52:55]\n"
"v_cvt_pk_f16_f32 v82, v68, v69\n"
"v_cvt_pk_f16_f32 v83, v70, v71\n"
"v_mfma_f32_16x16x32_f16 v[48:51], v[100:103], v[92:95], v[48:51]\n"
"ds_read_b64 v[92:93], v24 offset:576\n"
"ds_read_b64 v[94:95], v24 offset:584\n"
"v_mfma_f32_16x16x32_f16 v[48:51], v[76:79], v[84:87], v[48:51]\n"
"ds_read_b64 v[100:101], v24 offset:1088\n"
"s_waitcnt lgkmcnt(8)\n"
"v_mfma_f32_16x16x32_f16 v[56:59], v[80:83], v[128:131], v[56:59]\n"
"ds_read_b64 v[102:103], v24 offset:1096\n"
"ds_read_b64 v[84:85], v24 offset:0\n"
"v_mfma_f32_16x16x32_f16 v[60:63], v[80:83], v[132:135], v[60:63]\n"
"ds_read_b64 v[86:87], v24 offset:8\n"
"v_mfma_f32_16x16x32_f16 v[64:67], v[96:99], v[144:147], 0\n"
"ds_read_b128 v[76:79], v25 offset:512\n"
"v_mfma_f32_16x16x32_f16 v[68:71], v[44:47], v[148:151], 0\n"
"ds_read_b128 v[44:47], v25 offset:1024\n"
"s_waitcnt lgkmcnt(8)\n"
"v_mfma_f32_16x16x32_f16 v[60:63], v[152:155], v[96:99], v[60:63]\n"
"ds_read_b128 v[96:99], v25 offset:1536\n"
"v_mfma_f32_16x16x32_f16 v[48:51], v[88:91], v[72:75], v[48:51]\n"
"ds_read_b64 v[72:73], v24 offset:512\n"
"v_mfma_f32_16x16x32_f16 v[52:55], v[88:91], v[40:43], v[52:55]\n"
"ds_read_b64 v[74:75], v24 offset:520\n"
"v_mfma_f32_16x16x32_f16 v[56:59], v[88:91], v[104:107], v[56:59]\n"
"ds_read_b64 v[88:89], v24 offset:1024\n"
"s_waitcnt lgkmcnt(5)\n"
"v_mfma_f32_16x16x32_f16 v[52:55], v[92:95], v[104:107], v[52:55]\n"
"ds_read_b64 v[90:91], v24 offset:1032\n"
"v_cvt_pk_f16_f32 v80, v64, v65\n"
"v_mfma_f32_16x16x32_f16 v[48:51], v[92:95], v[40:43], v[48:51]\n"
"v_cvt_pk_f16_f32 v81, v66, v67\n"
"v_cvt_pk_f16_f32 v82, v68, v69\n"
"v_mfma_f32_16x16x32_f16 v[48:51], v[100:103], v[104:107], v[48:51]\n"
"v_cvt_pk_f16_f32 v83, v70, v71\n"
"v_mfma_f32_16x16x32_f16 v[48:51], v[84:87], v[76:79], v[48:51]\n"
"s_waitcnt lgkmcnt(2)\n"
"v_mfma_f32_16x16x32_f16 v[52:55], v[84:87], v[44:47], v[52:55]\n"
"v_mfma_f32_16x16x32_f16 v[56:59], v[84:87], v[96:99], v[56:59]\n"
"v_mfma_f32_16x16x32_f16 v[52:55], v[72:75], v[96:99], v[52:55]\n"
"v_mfma_f32_16x16x32_f16 v[48:51], v[72:75], v[44:47], v[48:51]\n"
"s_waitcnt lgkmcnt(0)\n"
"v_mfma_f32_16x16x32_f16 v[48:51], v[88:91], v[96:99], v[48:51]\n"
"v_mfma_f32_16x16x32_f16 v[60:63], v[80:83], v[128:131], v[60:63]\n"
"s_setprio 2\n"
"s_mov_b32 s17, 0x100000\n"
".Ltp_435:\n"
"ds_read_b32 v3, v5 offset:256\n"
"s_sub_u32 s17, s17, 1\n"
"s_cbranch_scc1 .Lou_436\n"
"s_waitcnt lgkmcnt(0)\n"
"v_cmp_eq_u32 vcc, 0, v3\n"
"s_cbranch_vccnz .Ltp_435\n"
".Lou_436:\n"
"s_waitcnt lgkmcnt(0)\n"
"buffer_load_dwordx4 v[110:113], v126, s[4:7], s32 offen offset:0 sc1\n"
"buffer_load_dwordx4 v[114:117], v126, s[4:7], s32 offen offset:128 sc1\n"
"buffer_load_dwordx4 v[118:121], v126, s[4:7], s33 offen offset:0 sc1\n"
"buffer_load_dwordx4 v[122:125], v126, s[4:7], s33 offen offset:128 sc1\n"
"s_mov_b32 s38, 0x05040100\n"
"s_mov_b32 s39, 0x07060302\n"
"s_mov_b32 s17, 32768\n"
".Ltp_437:\n"
"s_waitcnt vmcnt(0)\n"
"s_sub_u32 s17, s17, 1\n"
"s_cbranch_scc1 .Lou_438\n"
"v_bitop3_b32 v64, v111, v113, s16 bitop3:0x7e\n"
"v_bitop3_b32 v65, v115, v117, s16 bitop3:0x7e\n"
"v_bitop3_b32 v66, v119, v121, s16 bitop3:0x7e\n"
"v_bitop3_b32 v67, v123, v125, s16 bitop3:0x7e\n"
"v_or3_b32 v68, v64, v65, v66\n"
"v_or_b32 v68, v68, v67\n"
"v_cmp_ne_u32 vcc, 0, v68\n"
"s_cbranch_vccz .Lou_438\n"
"s_sleep 2\n"
"v_cmp_ne_u32 vcc, 0, v64\n"
"s_cbranch_vccz .Lsk_439\n"
"s_mov_b64 exec, vcc\n"
"buffer_load_dwordx4 v[110:113], v126, s[4:7], s32 offen offset:0 sc1\n"
"s_mov_b64 exec, -1\n"
".Lsk_439:\n"
"v_cmp_ne_u32 vcc, 0, v65\n"
"s_cbranch_vccz .Lsk_440\n"
"s_mov_b64 exec, vcc\n"
"buffer_load_dwordx4 v[114:117], v126, s[4:7], s32 offen offset:128 sc1\n"
"s_mov_b64 exec, -1\n"
".Lsk_440:\n"
"v_cmp_ne_u32 vcc, 0, v66\n"
"s_cbranch_vccz .Lsk_441\n"
"s_mov_b64 exec, vcc\n"
"buffer_load_dwordx4 v[118:121], v126, s[4:7], s33 offen offset:0 sc1\n"
"s_mov_b64 exec, -1\n"
".Lsk_441:\n"
"v_cmp_ne_u32 vcc, 0, v67\n"
"s_cbranch_vccz .Lsk_442\n"
"s_mov_b64 exec, vcc\n"
"buffer_load_dwordx4 v[122:125], v126, s[4:7], s33 offen offset:128 sc1\n"
"s_mov_b64 exec, -1\n"
".Lsk_442:\n"
"s_branch .Ltp_437\n"
".Lou_438:\n"
"s_waitcnt vmcnt(0)\n"
"s_mov_b32 s89, 2\n"
".Ldl_443:\n"
"s_sub_u32 s89, s89, 1\n"
"s_cbranch_scc1 .Lde_444\n"
"s_sleep 1\n"
"s_branch .Ldl_443\n"
".Lde_444:\n"
"buffer_load_dwordx4 v[88:91], v30, s[4:7], s34 offen sc1\n"
"buffer_load_dwordx4 v[92:95], v30, s[4:7], s35 offen sc1\n"
"v_perm_b32 v64, v114, v110, s38\n"
"v_perm_b32 v65, v114, v110, s39\n"
"v_perm_b32 v66, v116, v112, s38\n"
"v_perm_b32 v67, v116, v112, s39\n"
"ds_write_b32 v32, v64 offset:0\n"
"ds_write_b32 v33, v65 offset:2048\n"
"ds_write_b32 v32, v66 offset:4224\n"
"ds_write_b32 v33, v67 offset:6272\n"
"v_perm_b32 v64, v122, v118, s38\n"
"v_perm_b32 v65, v122, v118, s39\n"
"v_perm_b32 v66, v124, v120, s38\n"
"v_perm_b32 v67, v124, v120, s39\n"
"ds_write_b32 v32, v64 offset:1024\n"
"ds_write_b32 v33, v65 offset:3072\n"
"ds_write_b32 v32, v66 offset:5248\n"
"ds_write_b32 v33, v67 offset:7296\n"
"v_lshlrev_b32 v64, 16, v110\n"
"v_lshlrev_b32 v65, 16, v114\n"
"v_and_b32 v66, 0xffff0000, v110\n"
"v_and_b32 v67, 0xffff0000, v114\n"
"v_lshlrev_b32 v68, 16, v112\n"
"v_lshlrev_b32 v69, 16, v116\n"
"v_and_b32 v70, 0xffff0000, v112\n"
"v_and_b32 v71, 0xffff0000, v116\n"
"v_lshlrev_b32 v72, 16, v118\n"
"v_lshlrev_b32 v73, 16, v122\n"
"v_and_b32 v74, 0xffff0000, v118\n"
"v_and_b32 v75, 0xffff0000, v122\n"
"v_lshlrev_b32 v76, 16, v120\n"
"v_lshlrev_b32 v77, 16, v124\n"
"v_and_b32 v78, 0xffff0000, v120\n"
"v_and_b32 v79, 0xffff0000, v124\n"
"s_mov_b32 s17, 32768\n"
".Ltp_445:\n"
"s_waitcnt vmcnt(0)\n"
"s_sub_u32 s17, s17, 1\n"
"s_cbranch_scc1 .Lou_446\n"
"v_max_u32 v110, v88, v89\n"
"v_max3_u32 v110, v90, v91, v110\n"
"v_max3_u32 v110, v92, v93, v110\n"
"v_max3_u32 v110, v94, v95, v110\n"
"v_cmp_eq_u32 vcc, -1, v110\n"
"s_cbranch_vccz .Lou_446\n"
"v_max_u32 v110, v90, v91\n"
"v_max3_u32 v110, v88, v89, v110\n"
"v_cmp_eq_u32 vcc, -1, v110\n"
"s_cbranch_vccz .Lsk_447\n"
"s_mov_b64 exec, vcc\n"
"buffer_load_dwordx4 v[88:91], v30, s[4:7], s34 offen sc1\n"
"s_mov_b64 exec, -1\n"
".Lsk_447:\n"
"v_max_u32 v110, v94, v95\n"
"v_max3_u32 v110, v92, v93, v110\n"
"v_cmp_eq_u32 vcc, -1, v110\n"
"s_cbranch_vccz .Lsk_448\n"
"s_mov_b64 exec, vcc\n"
"buffer_load_dwordx4 v[92:95], v30, s[4:7], s35 offen sc1\n"
"s_mov_b64 exec, -1\n"
".Lsk_448:\n"
"s_branch .Ltp_445\n"
".Lou_446:\n"
"s_waitcnt vmcnt(0)\n"
"v_fma_f32 v64, v64, v89, v88\n"
"v_fma_f32 v65, v65, v91, v90\n"
"v_cvt_pk_f16_f32 v64, v64, v65\n"
"v_fma_f32 v66, v66, v89, v88\n"
"v_fma_f32 v67, v67, v91, v90\n"
"v_cvt_pk_f16_f32 v66, v66, v67\n"
"v_fma_f32 v68, v68, v89, v88\n"
"v_fma_f32 v69, v69, v91, v90\n"
"v_cvt_pk_f16_f32 v68, v68, v69\n"
"v_fma_f32 v70, v70, v89, v88\n"
"v_fma_f32 v71, v71, v91, v90\n"
"v_cvt_pk_f16_f32 v70, v70, v71\n"
"ds_write2st64_b32 v31, v64, v66 offset0:0 offset1:18\n"
"ds_write2st64_b32 v31, v68, v70 offset0:36 offset1:54\n"
"v_fma_f32 v72, v72, v93, v92\n"
"v_fma_f32 v73, v73, v95, v94\n"
"v_cvt_pk_f16_f32 v72, v72, v73\n"
"v_fma_f32 v74, v74, v93, v92\n"
"v_fma_f32 v75, v75, v95, v94\n"
"v_cvt_pk_f16_f32 v74, v74, v75\n"
"v_fma_f32 v76, v76, v93, v92\n"
"v_fma_f32 v77, v77, v95, v94\n"
"v_cvt_pk_f16_f32 v76, v76, v77\n"
"v_fma_f32 v78, v78, v93, v92\n"
"v_fma_f32 v79, v79, v95, v94\n"
"v_cvt_pk_f16_f32 v78, v78, v79\n"
"ds_write2st64_b32 v31, v72, v74 offset0:4 offset1:22\n"
"ds_write2st64_b32 v31, v76, v78 offset0:40 offset1:58\n"
"ds_write_b32 v4, v4 offset:656\n"
"s_mov_b32 s17, 0x100000\n"
".Ltp_449:\n"
"ds_read_b32 v3, v5 offset:656\n"
"s_sub_u32 s17, s17, 1\n"
"s_cbranch_scc1 .Lou_450\n"
"s_waitcnt lgkmcnt(0)\n"
"v_cmp_eq_u32 vcc, 0, v3\n"
"s_cbranch_vccnz .Ltp_449\n"
".Lou_450:\n"
"s_waitcnt lgkmcnt(0)\n"
"s_mov_b32 s36, 0xfffefffe\n"
"ds_read_b64 v[88:89], v24 offset:1984\n"
"ds_read_b64 v[90:91], v24 offset:1992\n"
"ds_read_b128 v[92:95], v25 offset:4032\n"
"ds_read_b128 v[96:99], v25 offset:3520\n"
"ds_read_b64 v[100:101], v24 offset:1472\n"
"ds_read_b64 v[102:103], v24 offset:1480\n"
"ds_read_b128 v[104:107], v25 offset:3008\n"
"s_waitcnt lgkmcnt(0)\n"
"v_mfma_f32_16x16x32_f16 v[60:63], v[88:91], v[92:95], v[60:63]\n"
"ds_read_b64 v[108:109], v24 offset:960\n"
"ds_read_b64 v[110:111], v24 offset:968\n"
"v_mfma_f32_16x16x32_f16 v[56:59], v[88:91], v[96:99], v[56:59]\n"
"ds_read_b128 v[112:115], v25 offset:2496\n"
"ds_read_b64 v[116:117], v24 offset:448\n"
"v_mfma_f32_16x16x32_f16 v[60:63], v[100:103], v[96:99], v[60:63]\n"
"ds_read_b64 v[118:119], v24 offset:456\n"
"ds_read_b64 v[120:121], v24 offset:1920\n"
"v_mfma_f32_16x16x32_f16 v[52:55], v[88:91], v[104:107], v[52:55]\n"
"ds_read_b64 v[122:123], v24 offset:1928\n"
"s_waitcnt lgkmcnt(4)\n"
"v_mfma_f32_16x16x32_f16 v[56:59], v[100:103], v[104:107], v[56:59]\n"
"ds_read_b128 v[124:127], v25 offset:3968\n"
"ds_read_b128 v[40:43], v25 offset:3456\n"
"v_mfma_f32_16x16x32_f16 v[60:63], v[108:111], v[104:107], v[60:63]\n"
"ds_read_b64 v[44:45], v24 offset:1408\n"
"ds_read_b64 v[46:47], v24 offset:1416\n"
"v_mfma_f32_16x16x32_f16 v[48:51], v[88:91], v[112:115], v[48:51]\n"
"ds_read_b128 v[92:95], v25 offset:2944\n"
"ds_read_b64 v[96:97], v24 offset:896\n"
"v_mfma_f32_16x16x32_f16 v[52:55], v[100:103], v[112:115], v[52:55]\n"
"ds_read_b64 v[98:99], v24 offset:904\n"
"s_waitcnt lgkmcnt(5)\n"
"v_mfma_f32_16x16x32_f16 v[56:59], v[108:111], v[112:115], v[56:59]\n"
"ds_read_b128 v[104:107], v25 offset:2432\n"
"ds_read_b64 v[88:89], v24 offset:384\n"
"v_mfma_f32_16x16x32_f16 v[60:63], v[116:119], v[112:115], v[60:63]\n"
"ds_read_b64 v[90:91], v24 offset:392\n"
"ds_read_b64 v[100:101], v24 offset:1856\n"
"v_mfma_f32_16x16x32_f16 v[60:63], v[120:123], v[124:127], v[60:63]\n"
"ds_read_b64 v[102:103], v24 offset:1864\n"
"ds_read_b128 v[108:111], v25 offset:3904\n"
"v_mfma_f32_16x16x32_f16 v[56:59], v[120:123], v[40:43], v[56:59]\n"
"ds_read_b128 v[112:115], v25 offset:3392\n"
"s_waitcnt lgkmcnt(7)\n"
"v_mfma_f32_16x16x32_f16 v[60:63], v[44:47], v[40:43], v[60:63]\n"
"ds_read_b64 v[116:117], v26 offset:0\n"
"ds_read_b64 v[124:125], v24 offset:1344\n"
"v_mfma_f32_16x16x32_f16 v[52:55], v[120:123], v[92:95], v[52:55]\n"
"ds_read_b64 v[126:127], v24 offset:1352\n"
"v_mfma_f32_16x16x32_f16 v[56:59], v[44:47], v[92:95], v[56:59]\n"
"ds_read_b128 v[40:43], v25 offset:2880\n"
"v_mfma_f32_16x16x32_f16 v[60:63], v[96:99], v[92:95], v[60:63]\n"
"ds_read_b64 v[92:93], v24 offset:832\n"
"s_waitcnt lgkmcnt(9)\n"
"v_mfma_f32_16x16x32_f16 v[48:51], v[120:123], v[104:107], v[48:51]\n"
"ds_read_b64 v[94:95], v24 offset:840\n"
"v_mfma_f32_16x16x32_f16 v[52:55], v[44:47], v[104:107], v[52:55]\n"
"ds_read_b128 v[120:123], v25 offset:2368\n"
"v_mfma_f32_16x16x32_f16 v[56:59], v[96:99], v[104:107], v[56:59]\n"
"ds_read_b64 v[44:45], v24 offset:320\n"
"v_mfma_f32_16x16x32_f16 v[60:63], v[88:91], v[104:107], v[60:63]\n"
"ds_read_b64 v[46:47], v24 offset:328\n"
"s_waitcnt lgkmcnt(6)\n"
"v_mfma_f32_16x16x32_f16 v[60:63], v[100:103], v[108:111], v[60:63]\n"
"ds_read_b64 v[96:97], v26 offset:512\n"
"ds_read_b64 v[104:105], v24 offset:1792\n"
"v_mfma_f32_16x16x32_f16 v[56:59], v[100:103], v[112:115], v[56:59]\n"
"ds_read_b64 v[106:107], v24 offset:1800\n"
"ds_read_b128 v[88:91], v25 offset:3840\n"
"v_mfma_f32_16x16x16_bf16 v[48:51], v[34:35], v[116:117], v[48:51]\n"
"ds_read_b128 v[108:111], v25 offset:3328\n"
"ds_read_b64 v[116:117], v24 offset:1280\n"
"v_mfma_f32_16x16x32_f16 v[60:63], v[124:127], v[112:115], v[60:63]\n"
"ds_read_b64 v[118:119], v24 offset:1288\n"
"s_waitcnt lgkmcnt(9)\n"
"v_mfma_f32_16x16x32_f16 v[52:55], v[100:103], v[40:43], v[52:55]\n"
"ds_read_b128 v[112:115], v25 offset:2816\n"
"s_and_b32 s79, s18, 0xfc\n"
"v_mfma_f32_16x16x32_f16 v[56:59], v[124:127], v[40:43], v[56:59]\n"
"s_and_b32 s78, s18, 1\n"
"s_lshl_b32 s78, s78, 1\n"
"v_mfma_f32_16x16x32_f16 v[60:63], v[92:95], v[40:43], v[60:63]\n"
"ds_read_b64 v[40:41], v24 offset:768\n"
"s_add_u32 s79, s79, s78\n"
"v_mfma_f32_16x16x32_f16 v[48:51], v[100:103], v[120:123], v[48:51]\n"
"ds_read_b64 v[42:43], v24 offset:776\n"
"s_waitcnt lgkmcnt(9)\n"
"v_mfma_f32_16x16x32_f16 v[52:55], v[124:127], v[120:123], v[52:55]\n"
"ds_read_b128 v[100:103], v25 offset:2304\n"
"s_lshr_b32 s78, s18, 1\n"
"v_mfma_f32_16x16x32_f16 v[56:59], v[92:95], v[120:123], v[56:59]\n"
"ds_read_b64 v[124:125], v24 offset:256\n"
"s_and_b32 s78, s78, 1\n"
"v_mfma_f32_16x16x32_f16 v[60:63], v[44:47], v[120:123], v[60:63]\n"
"ds_read_b64 v[126:127], v24 offset:264\n"
"ds_read_b64 v[92:93], v24 offset:1728\n"
"v_mfma_f32_16x16x16_bf16 v[52:55], v[34:35], v[96:97], v[52:55]\n"
"ds_read_b64 v[94:95], v24 offset:1736\n"
"s_waitcnt lgkmcnt(7)\n"
"v_mfma_f32_16x16x32_f16 v[60:63], v[104:107], v[88:91], v[60:63]\n"
"ds_read_b128 v[120:123], v25 offset:3776\n"
"ds_read_b128 v[44:47], v25 offset:3264\n"
"v_mfma_f32_16x16x32_f16 v[56:59], v[104:107], v[108:111], v[56:59]\n"
"ds_read_b64 v[96:97], v24 offset:1216\n"
"ds_read_b64 v[98:99], v24 offset:1224\n"
"v_mfma_f32_16x16x32_f16 v[60:63], v[116:119], v[108:111], v[60:63]\n"
"ds_read_b128 v[88:91], v25 offset:2752\n"
"s_add_u32 s79, s79, s78\n"
"v_mfma_f32_16x16x32_f16 v[52:55], v[104:107], v[112:115], v[52:55]\n"
"ds_read_b64 v[108:109], v26 offset:1024\n"
"s_waitcnt lgkmcnt(10)\n"
"v_mfma_f32_16x16x32_f16 v[56:59], v[116:119], v[112:115], v[56:59]\n"
"s_lshl_b32 s79, s79, 7\n"
"s_add_u32 s32, s79, 0xd68000\n"
"v_mfma_f32_16x16x32_f16 v[60:63], v[40:43], v[112:115], v[60:63]\n"
"ds_read_b64 v[112:113], v24 offset:704\n"
"v_mfma_f32_16x16x32_f16 v[48:51], v[104:107], v[100:103], v[48:51]\n"
"ds_read_b64 v[114:115], v24 offset:712\n"
"v_mfma_f32_16x16x32_f16 v[52:55], v[116:119], v[100:103], v[52:55]\n"
"ds_read_b128 v[104:107], v25 offset:2240\n"
"s_waitcnt lgkmcnt(7)\n"
"v_mfma_f32_16x16x32_f16 v[56:59], v[40:43], v[100:103], v[56:59]\n"
"ds_read_b64 v[116:117], v24 offset:192\n"
"v_mfma_f32_16x16x32_f16 v[60:63], v[124:127], v[100:103], v[60:63]\n"
"ds_read_b64 v[118:119], v24 offset:200\n"
"ds_read_b64 v[40:41], v24 offset:1664\n"
"v_mfma_f32_16x16x32_f16 v[60:63], v[92:95], v[120:123], v[60:63]\n"
"ds_read_b64 v[42:43], v24 offset:1672\n"
"ds_read_b128 v[100:103], v25 offset:3712\n"
"v_mfma_f32_16x16x32_f16 v[56:59], v[92:95], v[44:47], v[56:59]\n"
"ds_read_b128 v[124:127], v25 offset:3200\n"
"s_waitcnt lgkmcnt(9)\n"
"v_mfma_f32_16x16x32_f16 v[60:63], v[96:99], v[44:47], v[60:63]\n"
"ds_read_b64 v[120:121], v24 offset:1152\n"
"ds_read_b64 v[122:123], v24 offset:1160\n"
"v_mfma_f32_16x16x32_f16 v[52:55], v[92:95], v[88:91], v[52:55]\n"
"ds_read_b128 v[44:47], v25 offset:2688\n"
"v_mfma_f32_16x16x16_bf16 v[56:59], v[34:35], v[108:109], v[56:59]\n"
"ds_read_b64 v[108:109], v24 offset:640\n"
"s_nop 4\n"
"v_mfma_f32_16x16x32_f16 v[56:59], v[96:99], v[88:91], v[56:59]\n"
"ds_read_b64 v[110:111], v24 offset:648\n"
"s_waitcnt lgkmcnt(11)\n"
"v_mfma_f32_16x16x32_f16 v[60:63], v[112:115], v[88:91], v[60:63]\n"
"ds_read_b128 v[88:91], v25 offset:2176\n"
"v_mfma_f32_16x16x32_f16 v[48:51], v[92:95], v[104:107], v[48:51]\n"
"ds_read_b64 v[92:93], v26 offset:1536\n"
"v_mfma_f32_16x16x32_f16 v[52:55], v[96:99], v[104:107], v[52:55]\n"
"v_mfma_f32_16x16x32_f16 v[56:59], v[112:115], v[104:107], v[56:59]\n"
"ds_read_b64 v[96:97], v24 offset:128\n"
"s_waitcnt lgkmcnt(6)\n"
"v_mfma_f32_16x16x32_f16 v[60:63], v[116:119], v[104:107], v[60:63]\n"
"ds_read_b64 v[98:99], v24 offset:136\n"
"ds_read_b64 v[112:113], v24 offset:1600\n"
"v_mfma_f32_16x16x32_f16 v[60:63], v[40:43], v[100:103], v[60:63]\n"
"ds_read_b64 v[114:115], v24 offset:1608\n"
"ds_read_b128 v[104:107], v25 offset:3648\n"
"v_mfma_f32_16x16x32_f16 v[56:59], v[40:43], v[124:127], v[56:59]\n"
"ds_read_b128 v[116:119], v25 offset:3136\n"
"ds_read_b64 v[100:101], v24 offset:1088\n"
"v_mfma_f32_16x16x32_f16 v[60:63], v[120:123], v[124:127], v[60:63]\n"
"ds_read_b64 v[102:103], v24 offset:1096\n"
"s_waitcnt lgkmcnt(9)\n"
"v_mfma_f32_16x16x32_f16 v[52:55], v[40:43], v[44:47], v[52:55]\n"
"ds_read_b128 v[124:127], v25 offset:2624\n"
"v_mfma_f32_16x16x32_f16 v[56:59], v[120:123], v[44:47], v[56:59]\n"
"v_mfma_f32_16x16x32_f16 v[60:63], v[108:111], v[44:47], v[60:63]\n"
"ds_read_b64 v[44:45], v24 offset:576\n"
"v_mfma_f32_16x16x32_f16 v[48:51], v[40:43], v[88:91], v[48:51]\n"
"ds_read_b64 v[46:47], v24 offset:584\n"
"s_waitcnt lgkmcnt(9)\n"
"s_nop 0\n"
"v_mfma_f32_16x16x16_bf16 v[60:63], v[34:35], v[92:93], v[60:63]\n"
"ds_read_b128 v[40:43], v25 offset:2112\n"
"v_mfma_f32_16x16x32_f16 v[52:55], v[120:123], v[88:91], v[52:55]\n"
"ds_read_b64 v[92:93], v24 offset:64\n"
"v_mfma_f32_16x16x32_f16 v[56:59], v[108:111], v[88:91], v[56:59]\n"
"ds_read_b64 v[94:95], v24 offset:72\n"
"ds_read_b64 v[120:121], v24 offset:1536\n"
"v_mfma_f32_16x16x32_f16 v[60:63], v[96:99], v[88:91], v[60:63]\n"
"ds_read_b64 v[122:123], v24 offset:1544\n"
"s_waitcnt lgkmcnt(7)\n"
"v_mfma_f32_16x16x32_f16 v[60:63], v[112:115], v[104:107], v[60:63]\n"
"ds_read_b128 v[108:111], v25 offset:2048\n"
"ds_read_b128 v[88:91], v25 offset:2560\n"
"v_mfma_f32_16x16x32_f16 v[56:59], v[112:115], v[116:119], v[56:59]\n"
"ds_read_b64 v[96:97], v24 offset:1024\n"
"ds_read_b64 v[98:99], v24 offset:1032\n"
"v_mfma_f32_16x16x32_f16 v[60:63], v[100:103], v[116:119], v[60:63]\n"
"ds_read_b128 v[104:107], v25 offset:3072\n"
"v_mfma_f32_16x16x32_f16 v[52:55], v[112:115], v[124:127], v[52:55]\n"
"ds_read_b64 v[116:117], v24 offset:512\n"
"s_waitcnt lgkmcnt(10)\n"
"v_mfma_f32_16x16x32_f16 v[56:59], v[100:103], v[124:127], v[56:59]\n"
"ds_read_b64 v[118:119], v24 offset:520\n"
"v_mfma_f32_16x16x32_f16 v[60:63], v[44:47], v[124:127], v[60:63]\n"
"ds_read_b128 v[124:127], v25 offset:3584\n"
"v_mfma_f32_16x16x32_f16 v[48:51], v[112:115], v[40:43], v[48:51]\n"
"v_mfma_f32_16x16x32_f16 v[52:55], v[100:103], v[40:43], v[52:55]\n"
"ds_read_b64 v[112:113], v24 offset:0\n"
"s_waitcnt lgkmcnt(7)\n"
"v_mfma_f32_16x16x32_f16 v[56:59], v[44:47], v[40:43], v[56:59]\n"
"ds_read_b64 v[114:115], v24 offset:8\n"
"v_mfma_f32_16x16x32_f16 v[60:63], v[92:95], v[40:43], v[60:63]\n"
"v_mfma_f32_16x16x32_f16 v[48:51], v[120:123], v[108:111], v[48:51]\n"
"v_mfma_f32_16x16x32_f16 v[52:55], v[120:123], v[88:91], v[52:55]\n"
"s_waitcnt lgkmcnt(3)\n"
"v_mfma_f32_16x16x32_f16 v[52:55], v[96:99], v[108:111], v[52:55]\n"
"v_mfma_f32_16x16x32_f16 v[56:59], v[120:123], v[104:107], v[56:59]\n"
"v_mul_f32 v48, v36, v48\n"
"v_mul_f32 v49, v36, v49\n"
"v_mul_f32 v50, v36, v50\n"
"v_mfma_f32_16x16x32_f16 v[56:59], v[96:99], v[88:91], v[56:59]\n"
"v_mul_f32 v51, v36, v51\n"
"v_cvt_pk_bf16_f32 v48, v48, v49\n"
"v_cvt_pk_bf16_f32 v49, v50, v51\n"
"v_mfma_f32_16x16x32_f16 v[56:59], v[116:119], v[108:111], v[56:59]\n"
"v_pk_min_u16 v48, v48, s36\n"
"v_pk_min_u16 v49, v49, s36\n"
"s_waitcnt lgkmcnt(0)\n"
"v_mfma_f32_16x16x32_f16 v[60:63], v[120:123], v[124:127], v[60:63]\n"
"ds_write_b64 v26, v[48:49] offset:0\n"
"v_mfma_f32_16x16x32_f16 v[60:63], v[96:99], v[104:107], v[60:63]\n"
"v_mul_f32 v52, v36, v52\n"
"v_mul_f32 v53, v36, v53\n"
"v_mfma_f32_16x16x32_f16 v[60:63], v[116:119], v[88:91], v[60:63]\n"
"v_mul_f32 v54, v36, v54\n"
"v_mul_f32 v55, v36, v55\n"
"v_mfma_f32_16x16x32_f16 v[60:63], v[112:115], v[108:111], v[60:63]\n"
"v_cvt_pk_bf16_f32 v52, v52, v53\n"
"v_cvt_pk_bf16_f32 v53, v54, v55\n"
"v_pk_min_u16 v52, v52, s36\n"
"v_pk_min_u16 v53, v53, s36\n"
"ds_write_b64 v26, v[52:53] offset:512\n"
"v_mul_f32 v56, v36, v56\n"
"v_mul_f32 v57, v36, v57\n"
"v_mul_f32 v58, v36, v58\n"
"v_mul_f32 v59, v36, v59\n"
"v_cvt_pk_bf16_f32 v56, v56, v57\n"
"v_cvt_pk_bf16_f32 v57, v58, v59\n"
"v_pk_min_u16 v56, v56, s36\n"
"v_pk_min_u16 v57, v57, s36\n"
"ds_write_b64 v26, v[56:57] offset:1024\n"
"v_mul_f32 v60, v36, v60\n"
"v_mul_f32 v61, v36, v61\n"
"v_mul_f32 v62, v36, v62\n"
"v_mul_f32 v63, v36, v63\n"
"v_cvt_pk_bf16_f32 v60, v60, v61\n"
"v_cvt_pk_bf16_f32 v61, v62, v63\n"
"v_pk_min_u16 v60, v60, s36\n"
"v_pk_min_u16 v61, v61, s36\n"
"ds_write_b64 v26, v[60:61] offset:1536\n"
"ds_write_b32 v4, v4 offset:672\n"
"s_mov_b32 s17, 0x100000\n"
".Ltp_451:\n"
"ds_read_b32 v3, v5 offset:672\n"
"s_sub_u32 s17, s17, 1\n"
"s_cbranch_scc1 .Lou_452\n"
"s_waitcnt lgkmcnt(0)\n"
"v_cmp_eq_u32 vcc, 0, v3\n"
"s_cbranch_vccnz .Ltp_451\n"
".Lou_452:\n"
"s_waitcnt lgkmcnt(0)\n"
"ds_read_b64_tr_b16 v[64:65], v27 offset:0\n"
"ds_read_b64_tr_b16 v[66:67], v28 offset:0\n"
"ds_read_b64_tr_b16 v[68:69], v27 offset:1024\n"
"ds_read_b64_tr_b16 v[70:71], v28 offset:1024\n"
"s_add_u32 s33, s32, 0xa0000\n"
"s_waitcnt lgkmcnt(2)\n"
"buffer_store_dwordx4 v[64:67], v29, s[4:7], s32 offen sc1\n"
"s_waitcnt lgkmcnt(0)\n"
"buffer_store_dwordx4 v[68:71], v29, s[4:7], s33 offen sc1\n"
"ds_write_b32 v4, v4 offset:688\n"
"s_nop 0\n"
"s_branch .Ljc_453\n"
".Lnc_428:\n"
"s_mov_b32 s17, 0x100000\n"
".Ltp_454:\n"
"s_sleep 2\n"
"ds_read_b32 v3, v5 offset:272\n"
"s_sub_u32 s17, s17, 1\n"
"s_cbranch_scc1 .Lou_455\n"
"s_waitcnt lgkmcnt(0)\n"
"v_cmp_eq_u32 vcc, 0, v3\n"
"s_cbranch_vccnz .Ltp_454\n"
".Lou_455:\n"
"s_waitcnt lgkmcnt(0)\n"
"s_and_b32 s79, s2, 7\n"
"s_lshl_b32 s79, s79, 14\n"
"s_add_u32 s32, s79, 0xc0a000\n"
"s_add_u32 s33, s32, 0x1000\n"
"buffer_load_dwordx4 v[48:51], v2, s[4:7], s32 offen offset:0 sc1\n"
"buffer_load_dwordx4 v[52:55], v2, s[4:7], s32 offen offset:1024 sc1\n"
"buffer_load_dwordx4 v[56:59], v2, s[4:7], s32 offen offset:2048 sc1\n"
"buffer_load_dwordx4 v[60:63], v2, s[4:7], s32 offen offset:3072 sc1\n"
"buffer_load_dwordx4 v[64:67], v2, s[4:7], s33 offen offset:0 sc1\n"
"buffer_load_dwordx4 v[68:71], v2, s[4:7], s33 offen offset:1024 sc1\n"
"buffer_load_dwordx4 v[72:75], v2, s[4:7], s33 offen offset:2048 sc1\n"
"buffer_load_dwordx4 v[76:79], v2, s[4:7], s33 offen offset:3072 sc1\n"
"s_mov_b32 s17, 32768\n"
".Ltp_456:\n"
"s_waitcnt vmcnt(0)\n"
"s_sub_u32 s17, s17, 1\n"
"s_cbranch_scc1 .Lou_457\n"
"v_max_u32 v80, v48, v49\n"
"v_max3_u32 v80, v50, v51, v80\n"
"v_max3_u32 v80, v52, v53, v80\n"
"v_max3_u32 v80, v54, v55, v80\n"
"v_max3_u32 v80, v56, v57, v80\n"
"v_max3_u32 v80, v58, v59, v80\n"
"v_max3_u32 v80, v60, v61, v80\n"
"v_max3_u32 v80, v62, v63, v80\n"
"v_max3_u32 v80, v64, v65, v80\n"
"v_max3_u32 v80, v66, v67, v80\n"
"v_max3_u32 v80, v68, v69, v80\n"
"v_max3_u32 v80, v70, v71, v80\n"
"v_max3_u32 v80, v72, v73, v80\n"
"v_max3_u32 v80, v74, v75, v80\n"
"v_max3_u32 v80, v76, v77, v80\n"
"v_max3_u32 v80, v78, v79, v80\n"
"v_cmp_eq_u32 vcc, -1, v80\n"
"s_cbranch_vccz .Lou_457\n"
"s_sleep 4\n"
"v_max_u32 v80, v50, v51\n"
"v_max3_u32 v80, v48, v49, v80\n"
"v_cmp_eq_u32 vcc, -1, v80\n"
"s_cbranch_vccz .Lsk_458\n"
"s_mov_b64 exec, vcc\n"
"buffer_load_dwordx4 v[48:51], v2, s[4:7], s32 offen offset:0 sc1\n"
"s_mov_b64 exec, -1\n"
".Lsk_458:\n"
"v_max_u32 v80, v54, v55\n"
"v_max3_u32 v80, v52, v53, v80\n"
"v_cmp_eq_u32 vcc, -1, v80\n"
"s_cbranch_vccz .Lsk_459\n"
"s_mov_b64 exec, vcc\n"
"buffer_load_dwordx4 v[52:55], v2, s[4:7], s32 offen offset:1024 sc1\n"
"s_mov_b64 exec, -1\n"
".Lsk_459:\n"
"v_max_u32 v80, v58, v59\n"
"v_max3_u32 v80, v56, v57, v80\n"
"v_cmp_eq_u32 vcc, -1, v80\n"
"s_cbranch_vccz .Lsk_460\n"
"s_mov_b64 exec, vcc\n"
"buffer_load_dwordx4 v[56:59], v2, s[4:7], s32 offen offset:2048 sc1\n"
"s_mov_b64 exec, -1\n"
".Lsk_460:\n"
"v_max_u32 v80, v62, v63\n"
"v_max3_u32 v80, v60, v61, v80\n"
"v_cmp_eq_u32 vcc, -1, v80\n"
"s_cbranch_vccz .Lsk_461\n"
"s_mov_b64 exec, vcc\n"
"buffer_load_dwordx4 v[60:63], v2, s[4:7], s32 offen offset:3072 sc1\n"
"s_mov_b64 exec, -1\n"
".Lsk_461:\n"
"v_max_u32 v80, v66, v67\n"
"v_max3_u32 v80, v64, v65, v80\n"
"v_cmp_eq_u32 vcc, -1, v80\n"
"s_cbranch_vccz .Lsk_462\n"
"s_mov_b64 exec, vcc\n"
"buffer_load_dwordx4 v[64:67], v2, s[4:7], s33 offen offset:0 sc1\n"
"s_mov_b64 exec, -1\n"
".Lsk_462:\n"
"v_max_u32 v80, v70, v71\n"
"v_max3_u32 v80, v68, v69, v80\n"
"v_cmp_eq_u32 vcc, -1, v80\n"
"s_cbranch_vccz .Lsk_463\n"
"s_mov_b64 exec, vcc\n"
"buffer_load_dwordx4 v[68:71], v2, s[4:7], s33 offen offset:1024 sc1\n"
"s_mov_b64 exec, -1\n"
".Lsk_463:\n"
"v_max_u32 v80, v74, v75\n"
"v_max3_u32 v80, v72, v73, v80\n"
"v_cmp_eq_u32 vcc, -1, v80\n"
"s_cbranch_vccz .Lsk_464\n"
"s_mov_b64 exec, vcc\n"
"buffer_load_dwordx4 v[72:75], v2, s[4:7], s33 offen offset:2048 sc1\n"
"s_mov_b64 exec, -1\n"
".Lsk_464:\n"
"v_max_u32 v80, v78, v79\n"
"v_max3_u32 v80, v76, v77, v80\n"
"v_cmp_eq_u32 vcc, -1, v80\n"
"s_cbranch_vccz .Lsk_465\n"
"s_mov_b64 exec, vcc\n"
"buffer_load_dwordx4 v[76:79], v2, s[4:7], s33 offen offset:3072 sc1\n"
"s_mov_b64 exec, -1\n"
".Lsk_465:\n"
"s_branch .Ltp_456\n"
".Lou_457:\n"
"s_waitcnt vmcnt(0)\n"
".Ljc_453:\n"
"s_setprio 0\n"
"s_lshr_b32 s79, s2, 1\n"
"s_and_b32 s79, s79, 3\n"
"s_lshl_b32 s79, s79, 2\n"
"s_lshr_b32 s78, s2, 3\n"
"s_lshr_b32 s78, s78, 3\n"
"s_add_u32 s79, s79, s78\n"
"s_lshl_b32 s79, s79, 2\n"
"s_add_u32 s79, s79, 64\n"
"s_mul_i32 s79, s79, 0x5000\n"
"s_mul_i32 s78, s3, 0x1400\n"
"s_add_u32 s79, s79, s78\n"
"s_add_u32 s50, s79, 0xc28000\n"
"s_add_u32 s51, s50, 0x1000\n"
"v_min_i32 v99, 3, v0\n"
"v_mul_i32_i24 v48, 0x500, v99\n"
"s_mul_i32 s79, s3, 0x1400\n"
"s_add_u32 s79, s79, 0xa000\n"
"v_add_u32 v50, s79, v2\n"
"s_and_b32 s79, s2, 7\n"
"s_lshl_b32 s79, s79, 3\n"
"s_add_u32 s79, s79, 0x47\n"
"s_mul_i32 s79, s79, 0x5000\n"
"s_and_b32 s78, s2, 7\n"
"s_lshr_b32 s78, s78, 1\n"
"s_lshl_b32 s78, s78, 7\n"
"s_add_u32 s79, s79, s78\n"
"s_mul_i32 s78, s3, 0x1400\n"
"s_add_u32 s79, s79, s78\n"
"s_add_u32 s49, s79, 0xc28000\n"
"s_mov_b32 s17, 0x100000\n"
".Ltp_466:\n"
"s_sleep 2\n"
"ds_read_b32 v3, v5 offset:3264\n"
"s_sub_u32 s17, s17, 1\n"
"s_cbranch_scc1 .Lou_467\n"
"s_waitcnt lgkmcnt(0)\n"
"v_cmp_eq_u32 vcc, -1, v3\n"
"s_cbranch_vccnz .Ltp_466\n"
".Lou_467:\n"
"s_waitcnt lgkmcnt(0)\n"
"s_mov_b32 s89, 8\n"
".Ldl_468:\n"
"s_sub_u32 s89, s89, 1\n"
"s_cbranch_scc1 .Lde_469\n"
"s_sleep 1\n"
"s_branch .Ldl_468\n"
".Lde_469:\n"
"s_mov_b32 s17, 32768\n"
".Ltp_470:\n"
"buffer_load_dword v49, v48, s[4:7], s49 offen sc1\n"
"s_sub_u32 s17, s17, 1\n"
"s_cbranch_scc1 .Lou_471\n"
"s_waitcnt vmcnt(0)\n"
"v_cmp_eq_u32 vcc, -1, v49\n"
"s_cbranch_vccz .Lou_471\n"
"s_mov_b32 s89, 2\n"
".Ldl_472:\n"
"s_sub_u32 s89, s89, 1\n"
"s_cbranch_scc1 .Lde_473\n"
"s_sleep 1\n"
"s_branch .Ldl_472\n"
".Lde_473:\n"
"s_branch .Ltp_470\n"
".Lou_471:\n"
"s_waitcnt vmcnt(0)\n"
"buffer_load_dwordx4 v[52:55], v2, s[4:7], s50 offen offset:0 sc1\n"
"buffer_load_dwordx4 v[56:59], v2, s[4:7], s50 offen offset:1024 sc1\n"
"buffer_load_dwordx4 v[60:63], v2, s[4:7], s50 offen offset:2048 sc1\n"
"buffer_load_dwordx4 v[64:67], v2, s[4:7], s50 offen offset:3072 sc1\n"
"buffer_load_dwordx4 v[68:71], v2, s[4:7], s51 offen offset:0 sc1\n"
"s_mov_b32 s17, 32768\n"
".Ltp_474:\n"
"s_waitcnt vmcnt(0)\n"
"s_sub_u32 s17, s17, 1\n"
"s_cbranch_scc1 .Lou_475\n"
"v_max_u32 v38, v52, v53\n"
"v_max3_u32 v38, v54, v55, v38\n"
"v_max3_u32 v38, v56, v57, v38\n"
"v_max3_u32 v38, v58, v59, v38\n"
"v_max3_u32 v38, v60, v61, v38\n"
"v_max3_u32 v38, v62, v63, v38\n"
"v_max3_u32 v38, v64, v65, v38\n"
"v_max3_u32 v38, v66, v67, v38\n"
"v_max3_u32 v38, v68, v69, v38\n"
"v_max3_u32 v38, v70, v71, v38\n"
"v_cmp_eq_u32 vcc, -1, v38\n"
"s_cbranch_vccz .Lou_475\n"
"s_sleep 1\n"
"v_max_u32 v38, v54, v55\n"
"v_max3_u32 v38, v52, v53, v38\n"
"v_cmp_eq_u32 vcc, -1, v38\n"
"s_cbranch_vccz .Lsk_476\n"
"s_mov_b64 exec, vcc\n"
"buffer_load_dwordx4 v[52:55], v2, s[4:7], s50 offen offset:0 sc1\n"
"s_mov_b64 exec, -1\n"
".Lsk_476:\n"
"v_max_u32 v38, v58, v59\n"
"v_max3_u32 v38, v56, v57, v38\n"
"v_cmp_eq_u32 vcc, -1, v38\n"
"s_cbranch_vccz .Lsk_477\n"
"s_mov_b64 exec, vcc\n"
"buffer_load_dwordx4 v[56:59], v2, s[4:7], s50 offen offset:1024 sc1\n"
"s_mov_b64 exec, -1\n"
".Lsk_477:\n"
"v_max_u32 v38, v62, v63\n"
"v_max3_u32 v38, v60, v61, v38\n"
"v_cmp_eq_u32 vcc, -1, v38\n"
"s_cbranch_vccz .Lsk_478\n"
"s_mov_b64 exec, vcc\n"
"buffer_load_dwordx4 v[60:63], v2, s[4:7], s50 offen offset:2048 sc1\n"
"s_mov_b64 exec, -1\n"
".Lsk_478:\n"
"v_max_u32 v38, v66, v67\n"
"v_max3_u32 v38, v64, v65, v38\n"
"v_cmp_eq_u32 vcc, -1, v38\n"
"s_cbranch_vccz .Lsk_479\n"
"s_mov_b64 exec, vcc\n"
"buffer_load_dwordx4 v[64:67], v2, s[4:7], s50 offen offset:3072 sc1\n"
"s_mov_b64 exec, -1\n"
".Lsk_479:\n"
"v_max_u32 v38, v70, v71\n"
"v_max3_u32 v38, v68, v69, v38\n"
"v_cmp_eq_u32 vcc, -1, v38\n"
"s_cbranch_vccz .Lsk_480\n"
"s_mov_b64 exec, vcc\n"
"buffer_load_dwordx4 v[68:71], v2, s[4:7], s51 offen offset:0 sc1\n"
"s_mov_b64 exec, -1\n"
".Lsk_480:\n"
"s_branch .Ltp_474\n"
".Lou_475:\n"
"s_waitcnt vmcnt(0)\n"
"s_mov_b32 s17, 0x100000\n"
".Ltp_481:\n"
"s_sleep 2\n"
"ds_read_b32 v3, v5 offset:3584\n"
"s_sub_u32 s17, s17, 1\n"
"s_cbranch_scc1 .Lou_482\n"
"s_waitcnt lgkmcnt(0)\n"
"v_cmp_eq_u32 vcc, -1, v3\n"
"s_cbranch_vccnz .Ltp_481\n"
".Lou_482:\n"
"s_waitcnt lgkmcnt(0)\n"
"ds_write_b128 v50, v[52:55] offset:0\n"
"ds_write_b128 v50, v[56:59] offset:1024\n"
"ds_write_b128 v50, v[60:63] offset:2048\n"
"ds_write_b128 v50, v[64:67] offset:3072\n"
"ds_write_b128 v50, v[68:71] offset:4096\n"
"ds_write_b32 v4, v4 offset:704\n"
"s_mov_b32 s17, 0x100000\n"
".Ltp_483:\n"
"s_sleep 2\n"
"ds_read_b32 v3, v5 offset:3904\n"
"s_sub_u32 s17, s17, 1\n"
"s_cbranch_scc1 .Lou_484\n"
"s_waitcnt lgkmcnt(0)\n"
"v_cmp_eq_u32 vcc, -1, v3\n"
"s_cbranch_vccnz .Ltp_483\n"
".Lou_484:\n"
"s_waitcnt lgkmcnt(0)\n"
"s_mul_i32 s79, s3, 0x1400\n"
"s_add_u32 m0, s79, 0xf000\n"
"s_add_u32 s49, s50, 0x5000\n"
"buffer_load_dwordx4 v2, s[4:7], s49 offen offset:0 sc1 lds\n"
"buffer_load_dwordx4 v2, s[4:7], s49 offen offset:1024 sc1 lds\n"
"buffer_load_dwordx4 v2, s[4:7], s49 offen offset:2048 sc1 lds\n"
"buffer_load_dwordx4 v2, s[4:7], s49 offen offset:3072 sc1 lds\n"
"s_add_u32 m0, m0, 1024\n"
"s_add_u32 s49, s49, 1024\n"
"buffer_load_dwordx4 v2, s[4:7], s49 offen offset:3072 sc1 lds\n"
"s_setprio 2\n"
"v_add_u32 v14, v39, v14\n"
"v_add_u32 v15, v39, v15\n"
"s_mul_i32 s79, s28, 0xa00\n"
"s_add_u32 s79, s30, s79\n"
"s_and_b32 s78, s3, 1\n"
"s_mul_i32 s78, s78, 0x600\n"
"s_add_u32 s79, s79, s78\n"
"s_add_u32 s36, s79, 0xc28000\n"
"s_mul_i32 s79, s3, 0x1400\n"
"s_add_u32 s79, s30, s79\n"
"s_add_u32 s32, s79, 0xea8000\n"
"s_add_u32 s33, s32, 0x1000\n"
"s_lshl_b32 s79, s29, 1\n"
"s_lshr_b32 s78, s3, 1\n"
"s_xor_b32 s78, s78, 1\n"
"s_add_u32 s79, s79, s78\n"
"s_mul_i32 s79, s79, 0x5000\n"
"s_mul_i32 s78, s3, 0x1400\n"
"s_add_u32 s79, s79, s78\n"
"s_add_u32 s34, s79, 0xea8000\n"
"s_add_u32 s35, s34, 0x1000\n"
"s_lshl_b32 s79, s30, 1\n"
"s_mul_i32 s78, s28, 0x1400\n"
"s_add_u32 s79, s79, s78\n"
"s_and_b32 s78, s3, 1\n"
"s_mul_i32 s78, s78, 0xc00\n"
"s_add_u32 s79, s79, s78\n"
"s_add_u32 s37, s79, 0x1128000\n"
"s_lshl_b32 s79, s29, 1\n"
"s_lshr_b32 s78, s3, 1\n"
"s_add_u32 s79, s79, s78\n"
"s_lshl_b32 s79, s79, 11\n"
"s_lshl_b32 s78, s28, 8\n"
"s_add_u32 s79, s79, s78\n"
"s_and_b32 s78, s3, 1\n"
"s_lshl_b32 s78, s78, 7\n"
"s_add_u32 s79, s79, s78\n"
"s_add_u32 s38, s79, 0x1628000\n"
"s_mov_b32 s17, 0x100000\n"
".Ltp_485:\n"
"ds_read_b32 v3, v5 offset:256\n"
"s_sub_u32 s17, s17, 1\n"
"s_cbranch_scc1 .Lou_486\n"
"s_waitcnt lgkmcnt(0)\n"
"v_cmp_eq_u32 vcc, 0, v3\n"
"s_cbranch_vccnz .Ltp_485\n"
".Lou_486:\n"
"s_waitcnt lgkmcnt(0)\n"
"s_cmp_lt_i32 s18, 0\n"
"s_cbranch_scc1 .Lcw_487\n"
"s_mov_b32 s17, 0x100000\n"
".Ltp_488:\n"
"ds_read_b32 v3, v5 offset:688\n"
"s_sub_u32 s17, s17, 1\n"
"s_cbranch_scc1 .Lou_489\n"
"s_waitcnt lgkmcnt(0)\n"
"v_cmp_eq_u32 vcc, 0, v3\n"
"s_cbranch_vccnz .Ltp_488\n"
".Lou_489:\n"
"s_waitcnt lgkmcnt(0)\n"
".Lcw_487:\n"
"buffer_load_dwordx2 v[40:41], v14, s[4:7], s36 offen offset:0 sc1\n"
"buffer_load_dwordx2 v[42:43], v14, s[4:7], s36 offen offset:512 sc1\n"
"buffer_load_dwordx2 v[44:45], v15, s[4:7], s36 offen offset:1024 sc1\n"
"s_waitcnt vmcnt(3)\n"
"ds_write_b32 v4, v4 offset:320\n"
"s_mov_b32 s17, 0x100000\n"
".Ltp_493:\n"
"s_sleep 1\n"
"ds_read_b32 v3, v5 offset:304\n"
"s_sub_u32 s17, s17, 1\n"
"s_cbranch_scc1 .Lou_494\n"
"s_waitcnt lgkmcnt(0)\n"
"v_cmp_eq_u32 vcc, 0, v3\n"
"s_cbranch_vccnz .Ltp_493\n"
".Lou_494:\n"
"s_waitcnt lgkmcnt(0)\n"
"s_mul_i32 m0, s3, 0x1400\n"
"s_add_u32 s49, s50, 0xa000\n"
"buffer_load_dwordx4 v2, s[4:7], s49 offen offset:0 sc1 lds\n"
"buffer_load_dwordx4 v2, s[4:7], s49 offen offset:1024 sc1 lds\n"
"buffer_load_dwordx4 v2, s[4:7], s49 offen offset:2048 sc1 lds\n"
"buffer_load_dwordx4 v2, s[4:7], s49 offen offset:3072 sc1 lds\n"
"s_add_u32 m0, m0, 1024\n"
"s_add_u32 s49, s49, 1024\n"
"buffer_load_dwordx4 v2, s[4:7], s49 offen offset:3072 sc1 lds\n"
"s_waitcnt vmcnt(5)\n"
"s_mov_b32 s17, 32768\n"
"s_branch .Lmd_492\n"
".Ltp_490:\n"
"buffer_load_dwordx2 v[40:41], v14, s[4:7], s36 offen offset:0 sc1\n"
"buffer_load_dwordx2 v[42:43], v14, s[4:7], s36 offen offset:512 sc1\n"
"buffer_load_dwordx2 v[44:45], v15, s[4:7], s36 offen offset:1024 sc1\n"
"s_waitcnt vmcnt(0)\n"
".Lmd_492:\n"
"s_sub_u32 s17, s17, 1\n"
"s_cbranch_scc1 .Lou_491\n"
"v_max3_u32 v38, v40, v41, v42\n"
"v_max3_u32 v38, v43, v44, v38\n"
"v_max_u32 v38, v45, v38\n"
"v_cmp_eq_u32 vcc, -1, v38\n"
"s_cbranch_vccnz .Ltp_490\n"
".Lou_491:\n"
"v_lshlrev_b32 v48, 16, v40\n"
"v_and_b32 v49, 0xffff0000, v40\n"
"v_lshlrev_b32 v50, 16, v41\n"
"v_and_b32 v51, 0xffff0000, v41\n"
"v_lshlrev_b32 v52, 16, v42\n"
"v_and_b32 v53, 0xffff0000, v42\n"
"v_lshlrev_b32 v54, 16, v43\n"
"v_and_b32 v55, 0xffff0000, v43\n"
"v_lshlrev_b32 v56, 16, v44\n"
"v_and_b32 v57, 0xffff0000, v44\n"
"v_lshlrev_b32 v58, 16, v45\n"
"v_and_b32 v59, 0xffff0000, v45\n"
"v_mov_b32 v46, 0\n"
"v_mov_b32 v47, 0\n"
"s_mov_b32 s17, 0x100000\n"
".Ltp_495:\n"
"s_sleep 1\n"
"ds_read_b32 v3, v5 offset:368\n"
"s_sub_u32 s17, s17, 1\n"
"s_cbranch_scc1 .Lou_496\n"
"s_waitcnt lgkmcnt(0)\n"
"v_cmp_eq_u32 vcc, 0, v3\n"
"s_cbranch_vccnz .Ltp_495\n"
".Lou_496:\n"
"s_waitcnt lgkmcnt(0)\n"
"s_mul_i32 s79, s3, 0x1400\n"
"s_add_u32 m0, s79, 0x5000\n"
"s_add_u32 s49, s50, 0xf000\n"
"buffer_load_dwordx4 v2, s[4:7], s49 offen offset:0 sc1 lds\n"
"buffer_load_dwordx4 v2, s[4:7], s49 offen offset:1024 sc1 lds\n"
"buffer_load_dwordx4 v2, s[4:7], s49 offen offset:2048 sc1 lds\n"
"buffer_load_dwordx4 v2, s[4:7], s49 offen offset:3072 sc1 lds\n"
"s_add_u32 m0, m0, 1024\n"
"s_add_u32 s49, s49, 1024\n"
"buffer_load_dwordx4 v2, s[4:7], s49 offen offset:3072 sc1 lds\n"
"s_waitcnt vmcnt(5)\n"
"ds_write_b32 v4, v4 offset:400\n"
"s_waitcnt vmcnt(0)\n"
"ds_write_b32 v4, v4 offset:416\n"
"buffer_load_dwordx4 v[108:111], v2, s[4:7], s34 offen offset:0 sc1\n"
"s_nop 9\n"
"buffer_load_dwordx4 v[112:115], v2, s[4:7], s34 offen offset:1024 sc1\n"
"s_nop 9\n"
"buffer_load_dwordx4 v[116:119], v2, s[4:7], s34 offen offset:2048 sc1\n"
"s_nop 9\n"
"buffer_load_dwordx4 v[120:123], v2, s[4:7], s34 offen offset:3072 sc1\n"
"s_nop 9\n"
"buffer_load_dwordx4 v[124:127], v2, s[4:7], s35 offen offset:0 sc1\n"
"s_nop 9\n"
"buffer_load_dwordx4 v[88:91], v2, s[4:7], s32 offen offset:0 sc1\n"
"s_nop 9\n"
"buffer_load_dwordx4 v[92:95], v2, s[4:7], s32 offen offset:1024 sc1\n"
"s_nop 9\n"
"buffer_load_dwordx4 v[96:99], v2, s[4:7], s32 offen offset:2048 sc1\n"
"s_nop 9\n"
"buffer_load_dwordx4 v[100:103], v2, s[4:7], s32 offen offset:3072 sc1\n"
"s_nop 9\n"
"buffer_load_dwordx4 v[104:107], v2, s[4:7], s33 offen offset:0 sc1\n"
"s_waitcnt vmcnt(9)\n"
"v_max_u32 v38, v108, v109\n"
"v_max3_u32 v38, v110, v111, v38\n"
"v_cmp_eq_u32 vcc, -1, v38\n"
"s_cbranch_vccnz .Lsl_497\n"
".Lbk_498:\n"
"v_mfma_f32_16x16x32_bf16 v[68:71], v[156:159], v[108:111], 0\n"
"v_mfma_f32_16x16x32_bf16 v[72:75], v[160:163], v[108:111], 0\n"
"v_mfma_f32_16x16x32_bf16 v[76:79], v[164:167], v[108:111], 0\n"
"s_waitcnt vmcnt(8)\n"
"v_max_u32 v38, v112, v113\n"
"v_max3_u32 v38, v114, v115, v38\n"
"v_mfma_f32_16x16x32_bf16 v[80:83], v[168:171], v[108:111], 0\n"
"v_cmp_eq_u32 vcc, -1, v38\n"
"v_mfma_f32_16x16x32_bf16 v[84:87], v[172:175], v[108:111], 0\n"
"s_cbranch_vccnz .Lsl_499\n"
".Lbk_500:\n"
"v_mfma_f32_16x16x32_bf16 v[68:71], v[176:179], v[112:115], v[68:71]\n"
"v_mfma_f32_16x16x32_bf16 v[72:75], v[180:183], v[112:115], v[72:75]\n"
"v_mfma_f32_16x16x32_bf16 v[76:79], v[184:187], v[112:115], v[76:79]\n"
"s_waitcnt vmcnt(7)\n"
"v_max_u32 v38, v116, v117\n"
"v_max3_u32 v38, v118, v119, v38\n"
"v_mfma_f32_16x16x32_bf16 v[80:83], v[188:191], v[112:115], v[80:83]\n"
"v_cmp_eq_u32 vcc, -1, v38\n"
"v_mfma_f32_16x16x32_bf16 v[84:87], v[192:195], v[112:115], v[84:87]\n"
"s_cbranch_vccnz .Lsl_501\n"
".Lbk_502:\n"
"v_mfma_f32_16x16x32_bf16 v[68:71], v[196:199], v[116:119], v[68:71]\n"
"v_mfma_f32_16x16x32_bf16 v[72:75], v[200:203], v[116:119], v[72:75]\n"
"v_mfma_f32_16x16x32_bf16 v[76:79], v[204:207], v[116:119], v[76:79]\n"
"s_waitcnt vmcnt(6)\n"
"v_max_u32 v38, v120, v121\n"
"v_max3_u32 v38, v122, v123, v38\n"
"v_mfma_f32_16x16x32_bf16 v[80:83], v[208:211], v[116:119], v[80:83]\n"
"v_cmp_eq_u32 vcc, -1, v38\n"
"v_mfma_f32_16x16x32_bf16 v[84:87], v[212:215], v[116:119], v[84:87]\n"
"s_cbranch_vccnz .Lsl_503\n"
".Lbk_504:\n"
"v_mfma_f32_16x16x32_bf16 v[68:71], v[216:219], v[120:123], v[68:71]\n"
"v_mfma_f32_16x16x32_bf16 v[72:75], v[220:223], v[120:123], v[72:75]\n"
"v_mfma_f32_16x16x32_bf16 v[76:79], v[224:227], v[120:123], v[76:79]\n"
"s_waitcnt vmcnt(5)\n"
"v_max_u32 v38, v124, v125\n"
"v_max3_u32 v38, v126, v127, v38\n"
"v_mfma_f32_16x16x32_bf16 v[80:83], v[228:231], v[120:123], v[80:83]\n"
"v_cmp_eq_u32 vcc, -1, v38\n"
"v_mfma_f32_16x16x32_bf16 v[84:87], v[232:235], v[120:123], v[84:87]\n"
"s_cbranch_vccnz .Lsl_505\n"
".Lbk_506:\n"
"v_mfma_f32_16x16x32_bf16 v[68:71], v[236:239], v[124:127], v[68:71]\n"
"v_mfma_f32_16x16x32_bf16 v[72:75], v[240:243], v[124:127], v[72:75]\n"
"v_mfma_f32_16x16x32_bf16 v[76:79], v[244:247], v[124:127], v[76:79]\n"
"s_waitcnt vmcnt(4)\n"
"v_max_u32 v38, v88, v89\n"
"v_max3_u32 v38, v90, v91, v38\n"
"v_mfma_f32_16x16x32_bf16 v[80:83], v[248:251], v[124:127], v[80:83]\n"
"v_cmp_eq_u32 vcc, -1, v38\n"
"v_mfma_f32_16x16x32_bf16 v[84:87], v[252:255], v[124:127], v[84:87]\n"
"ds_write_b128 v8, v[68:71] offset:0\n"
"s_cbranch_vccnz .Lsl_507\n"
".Lbk_508:\n"
"v_mfma_f32_16x16x32_bf16 v[56:59], v[164:167], v[88:91], v[56:59]\n"
"s_waitcnt vmcnt(3)\n"
"v_max_u32 v38, v92, v93\n"
"v_max3_u32 v38, v94, v95, v38\n"
"ds_write_b128 v8, v[72:75] offset:1024\n"
"v_mfma_f32_16x16x32_bf16 v[60:63], v[168:171], v[88:91], 0\n"
"v_cmp_eq_u32 vcc, -1, v38\n"
"ds_write_b128 v10, v[76:79] offset:0\n"
"v_mfma_f32_16x16x32_bf16 v[64:67], v[172:175], v[88:91], 0\n"
"v_mov_b32 v117, s16\n"
"s_cbranch_vccnz .Lsl_509\n"
".Lbk_510:\n"
"v_mfma_f32_16x16x32_bf16 v[56:59], v[184:187], v[92:95], v[56:59]\n"
"s_waitcnt vmcnt(2)\n"
"v_max_u32 v38, v96, v97\n"
"v_max3_u32 v38, v98, v99, v38\n"
"v_mov_b32 v119, s16\n"
"v_mfma_f32_16x16x32_bf16 v[60:63], v[188:191], v[92:95], v[60:63]\n"
"v_cmp_eq_u32 vcc, -1, v38\n"
"ds_write_b128 v9, v[80:83] offset:0\n"
"v_mfma_f32_16x16x32_bf16 v[64:67], v[192:195], v[92:95], v[64:67]\n"
"ds_write_b128 v9, v[84:87] offset:1024\n"
"s_cbranch_vccnz .Lsl_511\n"
".Lbk_512:\n"
"v_mfma_f32_16x16x32_bf16 v[56:59], v[204:207], v[96:99], v[56:59]\n"
"s_waitcnt vmcnt(1)\n"
"v_max_u32 v38, v100, v101\n"
"v_max3_u32 v38, v102, v103, v38\n"
"ds_write_b32 v4, v4 offset:720\n"
"v_mfma_f32_16x16x32_bf16 v[60:63], v[208:211], v[96:99], v[60:63]\n"
"v_cmp_eq_u32 vcc, -1, v38\n"
"v_mov_b32 v121, s16\n"
"v_mov_b32 v123, s16\n"
"v_mfma_f32_16x16x32_bf16 v[64:67], v[212:215], v[96:99], v[64:67]\n"
"v_mov_b32 v125, s16\n"
"s_cbranch_vccnz .Lsl_513\n"
".Lbk_514:\n"
"v_mfma_f32_16x16x32_bf16 v[56:59], v[224:227], v[100:103], v[56:59]\n"
"s_waitcnt vmcnt(0)\n"
"v_max_u32 v38, v104, v105\n"
"v_max3_u32 v38, v106, v107, v38\n"
"v_mov_b32 v127, s16\n"
"v_mfma_f32_16x16x32_bf16 v[60:63], v[228:231], v[100:103], v[60:63]\n"
"v_cmp_eq_u32 vcc, -1, v38\n"
"ds_read_b32 v3, v5 offset:720\n"
"v_mfma_f32_16x16x32_bf16 v[64:67], v[232:235], v[100:103], v[64:67]\n"
"s_cbranch_vccnz .Lsl_515\n"
".Lbk_516:\n"
"v_mfma_f32_16x16x32_bf16 v[56:59], v[244:247], v[104:107], v[56:59]\n"
"ds_read_b128 v[76:79], v12 offset:2048\n"
"ds_read_b128 v[108:111], v13 offset:2048\n"
"v_mfma_f32_16x16x32_bf16 v[60:63], v[248:251], v[104:107], v[60:63]\n"
"ds_read_b128 v[68:71], v12 offset:0\n"
"ds_read_b128 v[80:83], v13 offset:0\n"
"v_mfma_f32_16x16x32_bf16 v[64:67], v[252:255], v[104:107], v[64:67]\n"
"ds_read_b128 v[72:75], v12 offset:1024\n"
"ds_read_b128 v[84:87], v13 offset:1024\n"
"v_mfma_f32_16x16x32_bf16 v[48:51], v[156:159], v[88:91], v[48:51]\n"
"ds_write_b128 v7, v[56:59] offset:0\n"
"v_mfma_f32_16x16x32_bf16 v[52:55], v[160:163], v[88:91], v[52:55]\n"
"ds_write_b128 v6, v[60:63] offset:0\n"
"v_mfma_f32_16x16x32_bf16 v[48:51], v[176:179], v[92:95], v[48:51]\n"
"ds_write_b128 v6, v[64:67] offset:1024\n"
"v_mfma_f32_16x16x32_bf16 v[52:55], v[180:183], v[92:95], v[52:55]\n"
"ds_write_b32 v4, v4 offset:736\n"
"v_mfma_f32_16x16x32_bf16 v[48:51], v[196:199], v[96:99], v[48:51]\n"
"s_mov_b32 s17, 0x100000\n"
"s_waitcnt lgkmcnt(10)\n"
"v_cmp_eq_u32 vcc, 0, v3\n"
"s_cbranch_vccz .Lok_518\n"
".Ltp_517:\n"
"ds_read_b32 v3, v5 offset:720\n"
"ds_read_b128 v[76:79], v12 offset:2048\n"
"ds_read_b128 v[108:111], v13 offset:2048\n"
"ds_read_b128 v[68:71], v12 offset:0\n"
"ds_read_b128 v[80:83], v13 offset:0\n"
"ds_read_b128 v[72:75], v12 offset:1024\n"
"ds_read_b128 v[84:87], v13 offset:1024\n"
"s_sub_u32 s17, s17, 1\n"
"s_cbranch_scc1 .Lok_518\n"
"s_waitcnt lgkmcnt(6)\n"
"v_cmp_eq_u32 vcc, 0, v3\n"
"s_cbranch_vccnz .Ltp_517\n"
"s_waitcnt lgkmcnt(0)\n"
".Lok_518:\n"
"v_mfma_f32_16x16x32_bf16 v[52:55], v[200:203], v[96:99], v[52:55]\n"
"s_waitcnt lgkmcnt(8)\n"
"v_add_f32 v76, v76, v108\n"
"v_add_f32 v77, v77, v109\n"
"v_add_f32 v78, v78, v110\n"
"v_mfma_f32_16x16x32_bf16 v[48:51], v[216:219], v[100:103], v[48:51]\n"
"v_add_f32 v79, v79, v111\n"
"s_waitcnt lgkmcnt(6)\n"
"v_add_f32 v68, v68, v80\n"
"v_add_f32 v69, v69, v81\n"
"v_mfma_f32_16x16x32_bf16 v[52:55], v[220:223], v[100:103], v[52:55]\n"
"v_add_f32 v70, v70, v82\n"
"v_add_f32 v71, v71, v83\n"
"s_waitcnt lgkmcnt(4)\n"
"v_add_f32 v72, v72, v84\n"
"v_mfma_f32_16x16x32_bf16 v[48:51], v[236:239], v[104:107], v[48:51]\n"
"v_add_f32 v73, v73, v85\n"
"v_add_f32 v74, v74, v86\n"
"v_add_f32 v75, v75, v87\n"
"v_mfma_f32_16x16x32_bf16 v[52:55], v[240:243], v[104:107], v[52:55]\n"
"ds_read_b32 v3, v5 offset:736\n"
"ds_read_b128 v[112:115], v11 offset:2048\n"
"ds_read_b128 v[60:63], v11 offset:0\n"
"ds_read_b128 v[64:67], v11 offset:1024\n"
"v_add_f32 v56, v56, v76\n"
"v_add_f32 v57, v57, v77\n"
"v_add_f32 v58, v58, v78\n"
"v_add_f32 v59, v59, v79\n"
"v_add_f32 v48, v48, v68\n"
"v_add_f32 v49, v49, v69\n"
"v_add_f32 v50, v50, v70\n"
"v_add_f32 v51, v51, v71\n"
"v_add_f32 v52, v52, v72\n"
"v_add_f32 v53, v53, v73\n"
"v_add_f32 v54, v54, v74\n"
"v_add_f32 v55, v55, v75\n"
"s_mov_b32 s17, 0x100000\n"
"s_waitcnt lgkmcnt(3)\n"
"v_cmp_eq_u32 vcc, 0, v3\n"
"s_cbranch_vccz .Lok_520\n"
".Ltp_519:\n"
"ds_read_b32 v3, v5 offset:736\n"
"ds_read_b128 v[112:115], v11 offset:2048\n"
"ds_read_b128 v[60:63], v11 offset:0\n"
"ds_read_b128 v[64:67], v11 offset:1024\n"
"s_sub_u32 s17, s17, 1\n"
"s_cbranch_scc1 .Lok_520\n"
"s_waitcnt lgkmcnt(3)\n"
"v_cmp_eq_u32 vcc, 0, v3\n"
"s_cbranch_vccnz .Ltp_519\n"
"s_waitcnt lgkmcnt(0)\n"
".Lok_520:\n"
"s_waitcnt lgkmcnt(2)\n"
"v_add_f32 v56, v56, v112\n"
"v_add_f32 v57, v57, v113\n"
"v_add_f32 v58, v58, v114\n"
"v_add_f32 v59, v59, v115\n"
"v_cvt_pk_bf16_f32 v44, v56, v57\n"
"v_cvt_pk_bf16_f32 v45, v58, v59\n"
"v_and_b32 v44, v19, v44\n"
"v_and_b32 v45, v19, v45\n"
"s_waitcnt lgkmcnt(1)\n"
"v_add_f32 v48, v48, v60\n"
"v_add_f32 v49, v49, v61\n"
"v_add_f32 v50, v50, v62\n"
"v_add_f32 v51, v51, v63\n"
"v_cvt_pk_bf16_f32 v40, v48, v49\n"
"v_cvt_pk_bf16_f32 v41, v50, v51\n"
"v_mfma_f32_16x16x32_bf16 v[88:91], v[44:47], v[44:47], 0\n"
"v_mfma_f32_16x16x32_bf16 v[92:95], v[20:23], v[44:47], 0\n"
"s_waitcnt lgkmcnt(0)\n"
"v_add_f32 v52, v52, v64\n"
"v_add_f32 v53, v53, v65\n"
"v_add_f32 v54, v54, v66\n"
"v_add_f32 v55, v55, v67\n"
"v_cvt_pk_bf16_f32 v42, v52, v53\n"
"v_cvt_pk_bf16_f32 v43, v54, v55\n"
"v_cvt_pk_bf16_f32 v116, v48, v49\n"
"v_cvt_pk_bf16_f32 v118, v50, v51\n"
"v_mfma_f32_16x16x32_bf16 v[88:91], v[40:43], v[40:43], v[88:91]\n"
"v_mfma_f32_16x16x32_bf16 v[92:95], v[20:23], v[40:43], v[92:95]\n"
"v_cvt_pk_bf16_f32 v120, v52, v53\n"
"v_cvt_pk_bf16_f32 v122, v54, v55\n"
"v_cvt_pk_bf16_f32 v124, v56, v57\n"
"v_cvt_pk_bf16_f32 v126, v58, v59\n"
"s_nop 0\n"
"v_cndmask_b32 v101, v88, v90, s[44:45]\n"
"v_cndmask_b32 v88, v89, v91, s[44:45]\n"
"s_nop 0\n"
"v_min_u32 v100, -2, v92\n"
"v_min_u32 v88, -2, v88\n"
"v_min_u32 v101, -2, v101\n"
"v_mov_b32_dpp v102, v100 quad_perm:[1,0,3,2] row_mask:0xf bank_mask:0xf\n"
"v_mov_b32_dpp v103, v88 quad_perm:[1,0,3,2] row_mask:0xf bank_mask:0xf\n"
"buffer_store_dwordx4 v[100:103], v16, s[4:7], s38 offen sc1\n"
"buffer_store_dwordx4 v[116:119], v17, s[4:7], s37 offen offset:0 sc1\n"
"buffer_store_dwordx4 v[120:123], v17, s[4:7], s37 offen offset:1024 sc1\n"
"buffer_store_dwordx4 v[124:127], v18, s[4:7], s37 offen offset:2048 sc1\n"
"ds_write_b32 v4, v4 offset:496\n"
"s_xor_b32 s79, s3, 1\n"
"s_lshl_b32 s79, s79, 8\n"
"s_add_u32 s32, s19, s79\n"
"s_cmp_ge_u32 s32, 64\n"
"s_cbranch_scc1 .Lnr_521\n"
"s_lshl_b32 s79, s19, 11\n"
"s_add_u32 s32, s79, 0x1628000\n"
"s_lshl_b32 s79, s19, 7\n"
"s_add_u32 s33, s79, 0x1668000\n"
"v_lshrrev_b32 v99, 3, v0\n"
"v_and_b32 v98, 7, v0\n"
"v_lshlrev_b32 v98, 4, v98\n"
"v_lshl_add_u32 v40, v99, 14, v98\n"
"v_mov_b32 v41, 0x3727c5ac\n"
"s_mov_b32 s89, 8\n"
".Ldl_522:\n"
"s_sub_u32 s89, s89, 1\n"
"s_cbranch_scc1 .Lde_523\n"
"s_sleep 1\n"
"s_branch .Ldl_522\n"
".Lde_523:\n"
"buffer_load_dwordx4 v[48:51], v2, s[4:7], s32 offen offset:0 sc1\n"
"buffer_load_dwordx4 v[52:55], v2, s[4:7], s32 offen offset:1024 sc1\n"
"s_mov_b32 s17, 32768\n"
".Ltp_524:\n"
"s_waitcnt vmcnt(0)\n"
"s_sub_u32 s17, s17, 1\n"
"s_cbranch_scc1 .Lou_525\n"
"v_max_u32 v38, v48, v49\n"
"v_max3_u32 v38, v50, v51, v38\n"
"v_max3_u32 v38, v52, v53, v38\n"
"v_max3_u32 v38, v54, v55, v38\n"
"v_cmp_eq_u32 vcc, -1, v38\n"
"s_cbranch_vccz .Lou_525\n"
"v_max_u32 v38, v50, v51\n"
"v_max3_u32 v38, v48, v49, v38\n"
"v_cmp_eq_u32 vcc, -1, v38\n"
"s_cbranch_vccz .Lsk_526\n"
"s_mov_b64 exec, vcc\n"
"buffer_load_dwordx4 v[48:51], v2, s[4:7], s32 offen offset:0 sc1\n"
"s_mov_b64 exec, -1\n"
".Lsk_526:\n"
"v_max_u32 v38, v54, v55\n"
"v_max3_u32 v38, v52, v53, v38\n"
"v_cmp_eq_u32 vcc, -1, v38\n"
"s_cbranch_vccz .Lsk_527\n"
"s_mov_b64 exec, vcc\n"
"buffer_load_dwordx4 v[52:55], v2, s[4:7], s32 offen offset:1024 sc1\n"
"s_mov_b64 exec, -1\n"
".Lsk_527:\n"
"s_branch .Ltp_524\n"
".Lou_525:\n"
"s_waitcnt vmcnt(0)\n"
"v_add_f32 v48, v48, v52\n"
"v_add_f32 v49, v49, v53\n"
"v_add_f32 v50, v50, v54\n"
"v_add_f32 v51, v51, v55\n"
"v_add_f32_dpp v52, v48, v48 row_ror:8 row_mask:0xf bank_mask:0xf\n"
"v_add_f32_dpp v53, v49, v49 row_ror:8 row_mask:0xf bank_mask:0xf\n"
"v_add_f32_dpp v54, v50, v50 row_ror:8 row_mask:0xf bank_mask:0xf\n"
"v_add_f32_dpp v55, v51, v51 row_ror:8 row_mask:0xf bank_mask:0xf\n"
"v_mov_b32 v48, v52\n"
"v_mov_b32 v49, v53\n"
"v_mov_b32 v50, v54\n"
"v_mov_b32 v51, v55\n"
"v_permlane16_swap_b32 v48, v52\n"
"v_permlane16_swap_b32 v49, v53\n"
"v_permlane16_swap_b32 v50, v54\n"
"v_permlane16_swap_b32 v51, v55\n"
"v_add_f32 v52, v48, v52\n"
"v_add_f32 v53, v49, v53\n"
"v_add_f32 v54, v50, v54\n"
"v_add_f32 v55, v51, v55\n"
"v_mov_b32 v48, v52\n"
"v_mov_b32 v49, v53\n"
"v_mov_b32 v50, v54\n"
"v_mov_b32 v51, v55\n"
"v_permlane32_swap_b32 v48, v52\n"
"v_permlane32_swap_b32 v49, v53\n"
"v_permlane32_swap_b32 v50, v54\n"
"v_permlane32_swap_b32 v51, v55\n"
"v_add_f32 v52, v48, v52\n"
"v_add_f32 v53, v49, v53\n"
"v_add_f32 v54, v50, v54\n"
"v_add_f32 v55, v51, v55\n"
"v_mul_f32 v48, 0x3acccccd, v52\n"
"v_mul_f32 v50, 0x3acccccd, v54\n"
"v_fma_f32 v53, -v52, v48, v53\n"
"v_fma_f32 v55, -v54, v50, v55\n"
"v_fmamk_f32 v53, v53, 0x3acccccd, v41\n"
"v_fmamk_f32 v55, v55, 0x3acccccd, v41\n"
"v_max_f32 v53, v53, v41\n"
"v_max_f32 v55, v55, v41\n"
"v_rsq_f32 v49, v53\n"
"v_rsq_f32 v51, v55\n"
"v_mul_f32 v48, -v48, v49\n"
"v_mul_f32 v50, -v50, v51\n"
"v_min_u32 v49, -2, v49\n"
"v_min_u32 v51, -2, v51\n"
"v_min_u32 v48, -2, v48\n"
"v_min_u32 v50, -2, v50\n"
"buffer_store_dwordx4 v[48:51], v40, s[4:7], s33 offen sc1\n"
"s_nop 3\n"
".Lnr_521:\n"
"s_mul_i32 s79, s28, 0xa00\n"
"s_add_u32 s79, s30, s79\n"
"s_and_b32 s78, s3, 1\n"
"s_mul_i32 s78, s78, 0x600\n"
"s_add_u32 s79, s79, s78\n"
"s_add_u32 s36, s79, 0xd68000\n"
"s_mul_i32 s79, s3, 0x1400\n"
"s_add_u32 s79, s30, s79\n"
"s_add_u32 s32, s79, 0xfe8000\n"
"s_add_u32 s33, s32, 0x1000\n"
"s_lshl_b32 s79, s29, 1\n"
"s_lshr_b32 s78, s3, 1\n"
"s_xor_b32 s78, s78, 1\n"
"s_add_u32 s79, s79, s78\n"
"s_mul_i32 s79, s79, 0x5000\n"
"s_mul_i32 s78, s3, 0x1400\n"
"s_add_u32 s79, s79, s78\n"
"s_add_u32 s34, s79, 0xfe8000\n"
"s_add_u32 s35, s34, 0x1000\n"
"s_lshl_b32 s79, s30, 1\n"
"s_mul_i32 s78, s28, 0x1400\n"
"s_add_u32 s79, s79, s78\n"
"s_and_b32 s78, s3, 1\n"
"s_mul_i32 s78, s78, 0xc00\n"
"s_add_u32 s79, s79, s78\n"
"s_add_u32 s37, s79, 0x13a8000\n"
"s_lshl_b32 s79, s29, 1\n"
"s_lshr_b32 s78, s3, 1\n"
"s_add_u32 s79, s79, s78\n"
"s_lshl_b32 s79, s79, 11\n"
"s_lshl_b32 s78, s28, 8\n"
"s_add_u32 s79, s79, s78\n"
"s_and_b32 s78, s3, 1\n"
"s_lshl_b32 s78, s78, 7\n"
"s_add_u32 s79, s79, s78\n"
"s_add_u32 s38, s79, 0x1648000\n"
"s_mov_b32 s17, 0x100000\n"
".Ltp_528:\n"
"ds_read_b32 v3, v5 offset:496\n"
"s_sub_u32 s17, s17, 1\n"
"s_cbranch_scc1 .Lou_529\n"
"s_waitcnt lgkmcnt(0)\n"
"v_cmp_eq_u32 vcc, 0, v3\n"
"s_cbranch_vccnz .Ltp_528\n"
".Lou_529:\n"
"s_waitcnt lgkmcnt(0)\n"
"s_mov_b32 s17, 0x100000\n"
".Ltp_533:\n"
"s_sleep 2\n"
"ds_read_b32 v3, v5 offset:4800\n"
"s_sub_u32 s17, s17, 1\n"
"s_cbranch_scc1 .Lou_534\n"
"s_waitcnt lgkmcnt(0)\n"
"v_cmp_eq_u32 vcc, -1, v3\n"
"s_cbranch_vccnz .Ltp_533\n"
".Lou_534:\n"
"s_waitcnt lgkmcnt(0)\n"
"s_mov_b32 s17, 32768\n"
".Ltp_530:\n"
"buffer_load_dwordx2 v[40:41], v14, s[4:7], s36 offen offset:0 sc1\n"
"buffer_load_dwordx2 v[42:43], v14, s[4:7], s36 offen offset:512 sc1\n"
"buffer_load_dwordx2 v[44:45], v15, s[4:7], s36 offen offset:1024 sc1\n"
"s_waitcnt vmcnt(0)\n"
".Lmd_532:\n"
"s_sub_u32 s17, s17, 1\n"
"s_cbranch_scc1 .Lou_531\n"
"v_max3_u32 v38, v40, v41, v42\n"
"v_max3_u32 v38, v43, v44, v38\n"
"v_max_u32 v38, v45, v38\n"
"v_cmp_eq_u32 vcc, -1, v38\n"
"s_cbranch_vccnz .Ltp_530\n"
".Lou_531:\n"
"v_lshlrev_b32 v48, 16, v40\n"
"v_and_b32 v49, 0xffff0000, v40\n"
"v_lshlrev_b32 v50, 16, v41\n"
"v_and_b32 v51, 0xffff0000, v41\n"
"v_lshlrev_b32 v52, 16, v42\n"
"v_and_b32 v53, 0xffff0000, v42\n"
"v_lshlrev_b32 v54, 16, v43\n"
"v_and_b32 v55, 0xffff0000, v43\n"
"v_lshlrev_b32 v56, 16, v44\n"
"v_and_b32 v57, 0xffff0000, v44\n"
"v_lshlrev_b32 v58, 16, v45\n"
"v_and_b32 v59, 0xffff0000, v45\n"
"v_mov_b32 v46, 0\n"
"v_mov_b32 v47, 0\n"
"s_mov_b32 s17, 0x100000\n"
".Ltp_535:\n"
"s_sleep 1\n"
"ds_read_b32 v3, v5 offset:480\n"
"s_sub_u32 s17, s17, 1\n"
"s_cbranch_scc1 .Lou_536\n"
"s_waitcnt lgkmcnt(0)\n"
"v_cmp_eq_u32 vcc, 0, v3\n"
"s_cbranch_vccnz .Ltp_535\n"
".Lou_536:\n"
"s_waitcnt lgkmcnt(0)\n"
"s_cmp_eq_u32 s17, 0xfffff\n"
"s_cbranch_scc1 .Lsk_537\n"
"s_mov_b32 s89, 19\n"
".Ldl_539:\n"
"s_sub_u32 s89, s89, 1\n"
"s_cbranch_scc1 .Lde_540\n"
"s_sleep 1\n"
"s_branch .Ldl_539\n"
".Lde_540:\n"
"s_branch .Ljn_538\n"
".Lsk_537:\n"
"s_mov_b32 s89, 12\n"
".Ldl_541:\n"
"s_sub_u32 s89, s89, 1\n"
"s_cbranch_scc1 .Lde_542\n"
"s_sleep 1\n"
"s_branch .Ldl_541\n"
".Lde_542:\n"
".Ljn_538:\n"
"buffer_load_dwordx4 v[108:111], v2, s[4:7], s34 offen offset:0 sc1\n"
"s_nop 9\n"
"buffer_load_dwordx4 v[112:115], v2, s[4:7], s34 offen offset:1024 sc1\n"
"s_nop 9\n"
"buffer_load_dwordx4 v[116:119], v2, s[4:7], s34 offen offset:2048 sc1\n"
"s_nop 9\n"
"buffer_load_dwordx4 v[120:123], v2, s[4:7], s34 offen offset:3072 sc1\n"
"s_nop 9\n"
"buffer_load_dwordx4 v[124:127], v2, s[4:7], s35 offen offset:0 sc1\n"
"s_nop 9\n"
"buffer_load_dwordx4 v[88:91], v2, s[4:7], s32 offen offset:0 sc1\n"
"s_nop 9\n"
"buffer_load_dwordx4 v[92:95], v2, s[4:7], s32 offen offset:1024 sc1\n"
"s_nop 9\n"
"buffer_load_dwordx4 v[96:99], v2, s[4:7], s32 offen offset:2048 sc1\n"
"s_nop 9\n"
"buffer_load_dwordx4 v[100:103], v2, s[4:7], s32 offen offset:3072 sc1\n"
"s_nop 9\n"
"buffer_load_dwordx4 v[104:107], v2, s[4:7], s33 offen offset:0 sc1\n"
"s_waitcnt vmcnt(9)\n"
"v_max_u32 v38, v108, v109\n"
"v_max3_u32 v38, v110, v111, v38\n"
"v_cmp_eq_u32 vcc, -1, v38\n"
"s_cbranch_vccnz .Lsl_543\n"
".Lbk_544:\n"
"v_mfma_f32_16x16x32_bf16 v[68:71], v[156:159], v[108:111], 0\n"
"v_mfma_f32_16x16x32_bf16 v[72:75], v[160:163], v[108:111], 0\n"
"v_mfma_f32_16x16x32_bf16 v[76:79], v[164:167], v[108:111], 0\n"
"s_waitcnt vmcnt(8)\n"
"v_max_u32 v38, v112, v113\n"
"v_max3_u32 v38, v114, v115, v38\n"
"v_mfma_f32_16x16x32_bf16 v[80:83], v[168:171], v[108:111], 0\n"
"v_cmp_eq_u32 vcc, -1, v38\n"
"v_mfma_f32_16x16x32_bf16 v[84:87], v[172:175], v[108:111], 0\n"
"s_cbranch_vccnz .Lsl_545\n"
".Lbk_546:\n"
"v_mfma_f32_16x16x32_bf16 v[68:71], v[176:179], v[112:115], v[68:71]\n"
"v_mfma_f32_16x16x32_bf16 v[72:75], v[180:183], v[112:115], v[72:75]\n"
"v_mfma_f32_16x16x32_bf16 v[76:79], v[184:187], v[112:115], v[76:79]\n"
"s_waitcnt vmcnt(7)\n"
"v_max_u32 v38, v116, v117\n"
"v_max3_u32 v38, v118, v119, v38\n"
"v_mfma_f32_16x16x32_bf16 v[80:83], v[188:191], v[112:115], v[80:83]\n"
"v_cmp_eq_u32 vcc, -1, v38\n"
"v_mfma_f32_16x16x32_bf16 v[84:87], v[192:195], v[112:115], v[84:87]\n"
"s_cbranch_vccnz .Lsl_547\n"
".Lbk_548:\n"
"v_mfma_f32_16x16x32_bf16 v[68:71], v[196:199], v[116:119], v[68:71]\n"
"v_mfma_f32_16x16x32_bf16 v[72:75], v[200:203], v[116:119], v[72:75]\n"
"v_mfma_f32_16x16x32_bf16 v[76:79], v[204:207], v[116:119], v[76:79]\n"
"s_waitcnt vmcnt(6)\n"
"v_max_u32 v38, v120, v121\n"
"v_max3_u32 v38, v122, v123, v38\n"
"v_mfma_f32_16x16x32_bf16 v[80:83], v[208:211], v[116:119], v[80:83]\n"
"v_cmp_eq_u32 vcc, -1, v38\n"
"v_mfma_f32_16x16x32_bf16 v[84:87], v[212:215], v[116:119], v[84:87]\n"
"s_cbranch_vccnz .Lsl_549\n"
".Lbk_550:\n"
"v_mfma_f32_16x16x32_bf16 v[68:71], v[216:219], v[120:123], v[68:71]\n"
"v_mfma_f32_16x16x32_bf16 v[72:75], v[220:223], v[120:123], v[72:75]\n"
"v_mfma_f32_16x16x32_bf16 v[76:79], v[224:227], v[120:123], v[76:79]\n"
"s_waitcnt vmcnt(5)\n"
"v_max_u32 v38, v124, v125\n"
"v_max3_u32 v38, v126, v127, v38\n"
"v_mfma_f32_16x16x32_bf16 v[80:83], v[228:231], v[120:123], v[80:83]\n"
"v_cmp_eq_u32 vcc, -1, v38\n"
"v_mfma_f32_16x16x32_bf16 v[84:87], v[232:235], v[120:123], v[84:87]\n"
"s_cbranch_vccnz .Lsl_551\n"
".Lbk_552:\n"
"v_mfma_f32_16x16x32_bf16 v[68:71], v[236:239], v[124:127], v[68:71]\n"
"v_mfma_f32_16x16x32_bf16 v[72:75], v[240:243], v[124:127], v[72:75]\n"
"v_mfma_f32_16x16x32_bf16 v[76:79], v[244:247], v[124:127], v[76:79]\n"
"s_waitcnt vmcnt(4)\n"
"v_max_u32 v38, v88, v89\n"
"v_max3_u32 v38, v90, v91, v38\n"
"v_mfma_f32_16x16x32_bf16 v[80:83], v[248:251], v[124:127], v[80:83]\n"
"v_cmp_eq_u32 vcc, -1, v38\n"
"v_mfma_f32_16x16x32_bf16 v[84:87], v[252:255], v[124:127], v[84:87]\n"
"ds_write_b128 v8, v[68:71] offset:0\n"
"s_cbranch_vccnz .Lsl_553\n"
".Lbk_554:\n"
"v_mfma_f32_16x16x32_bf16 v[56:59], v[164:167], v[88:91], v[56:59]\n"
"s_waitcnt vmcnt(3)\n"
"v_max_u32 v38, v92, v93\n"
"v_max3_u32 v38, v94, v95, v38\n"
"ds_write_b128 v8, v[72:75] offset:1024\n"
"v_mfma_f32_16x16x32_bf16 v[60:63], v[168:171], v[88:91], 0\n"
"v_cmp_eq_u32 vcc, -1, v38\n"
"ds_write_b128 v10, v[76:79] offset:0\n"
"v_mfma_f32_16x16x32_bf16 v[64:67], v[172:175], v[88:91], 0\n"
"v_mov_b32 v117, s16\n"
"s_cbranch_vccnz .Lsl_555\n"
".Lbk_556:\n"
"v_mfma_f32_16x16x32_bf16 v[56:59], v[184:187], v[92:95], v[56:59]\n"
"s_waitcnt vmcnt(2)\n"
"v_max_u32 v38, v96, v97\n"
"v_max3_u32 v38, v98, v99, v38\n"
"v_mov_b32 v119, s16\n"
"v_mfma_f32_16x16x32_bf16 v[60:63], v[188:191], v[92:95], v[60:63]\n"
"v_cmp_eq_u32 vcc, -1, v38\n"
"ds_write_b128 v9, v[80:83] offset:0\n"
"v_mfma_f32_16x16x32_bf16 v[64:67], v[192:195], v[92:95], v[64:67]\n"
"ds_write_b128 v9, v[84:87] offset:1024\n"
"s_cbranch_vccnz .Lsl_557\n"
".Lbk_558:\n"
"v_mfma_f32_16x16x32_bf16 v[56:59], v[204:207], v[96:99], v[56:59]\n"
"s_waitcnt vmcnt(1)\n"
"v_max_u32 v38, v100, v101\n"
"v_max3_u32 v38, v102, v103, v38\n"
"ds_write_b32 v4, v4 offset:752\n"
"v_mfma_f32_16x16x32_bf16 v[60:63], v[208:211], v[96:99], v[60:63]\n"
"v_cmp_eq_u32 vcc, -1, v38\n"
"v_mov_b32 v121, s16\n"
"v_mov_b32 v123, s16\n"
"v_mfma_f32_16x16x32_bf16 v[64:67], v[212:215], v[96:99], v[64:67]\n"
"v_mov_b32 v125, s16\n"
"s_cbranch_vccnz .Lsl_559\n"
".Lbk_560:\n"
"v_mfma_f32_16x16x32_bf16 v[56:59], v[224:227], v[100:103], v[56:59]\n"
"s_waitcnt vmcnt(0)\n"
"v_max_u32 v38, v104, v105\n"
"v_max3_u32 v38, v106, v107, v38\n"
"v_mov_b32 v127, s16\n"
"v_mfma_f32_16x16x32_bf16 v[60:63], v[228:231], v[100:103], v[60:63]\n"
"v_cmp_eq_u32 vcc, -1, v38\n"
"ds_read_b32 v3, v5 offset:752\n"
"v_mfma_f32_16x16x32_bf16 v[64:67], v[232:235], v[100:103], v[64:67]\n"
"s_cbranch_vccnz .Lsl_561\n"
".Lbk_562:\n"
"v_mfma_f32_16x16x32_bf16 v[56:59], v[244:247], v[104:107], v[56:59]\n"
"ds_read_b128 v[76:79], v12 offset:2048\n"
"ds_read_b128 v[108:111], v13 offset:2048\n"
"v_mfma_f32_16x16x32_bf16 v[60:63], v[248:251], v[104:107], v[60:63]\n"
"ds_read_b128 v[68:71], v12 offset:0\n"
"ds_read_b128 v[80:83], v13 offset:0\n"
"v_mfma_f32_16x16x32_bf16 v[64:67], v[252:255], v[104:107], v[64:67]\n"
"ds_read_b128 v[72:75], v12 offset:1024\n"
"ds_read_b128 v[84:87], v13 offset:1024\n"
"v_mfma_f32_16x16x32_bf16 v[48:51], v[156:159], v[88:91], v[48:51]\n"
"ds_write_b128 v7, v[56:59] offset:0\n"
"v_mfma_f32_16x16x32_bf16 v[52:55], v[160:163], v[88:91], v[52:55]\n"
"ds_write_b128 v6, v[60:63] offset:0\n"
"v_mfma_f32_16x16x32_bf16 v[48:51], v[176:179], v[92:95], v[48:51]\n"
"ds_write_b128 v6, v[64:67] offset:1024\n"
"v_mfma_f32_16x16x32_bf16 v[52:55], v[180:183], v[92:95], v[52:55]\n"
"ds_write_b32 v4, v4 offset:768\n"
"v_mfma_f32_16x16x32_bf16 v[48:51], v[196:199], v[96:99], v[48:51]\n"
"s_mov_b32 s17, 0x100000\n"
"s_waitcnt lgkmcnt(10)\n"
"v_cmp_eq_u32 vcc, 0, v3\n"
"s_cbranch_vccz .Lok_564\n"
".Ltp_563:\n"
"ds_read_b32 v3, v5 offset:752\n"
"ds_read_b128 v[76:79], v12 offset:2048\n"
"ds_read_b128 v[108:111], v13 offset:2048\n"
"ds_read_b128 v[68:71], v12 offset:0\n"
"ds_read_b128 v[80:83], v13 offset:0\n"
"ds_read_b128 v[72:75], v12 offset:1024\n"
"ds_read_b128 v[84:87], v13 offset:1024\n"
"s_sub_u32 s17, s17, 1\n"
"s_cbranch_scc1 .Lok_564\n"
"s_waitcnt lgkmcnt(6)\n"
"v_cmp_eq_u32 vcc, 0, v3\n"
"s_cbranch_vccnz .Ltp_563\n"
"s_waitcnt lgkmcnt(0)\n"
".Lok_564:\n"
"v_mfma_f32_16x16x32_bf16 v[52:55], v[200:203], v[96:99], v[52:55]\n"
"s_waitcnt lgkmcnt(8)\n"
"v_add_f32 v76, v76, v108\n"
"v_add_f32 v77, v77, v109\n"
"v_add_f32 v78, v78, v110\n"
"v_mfma_f32_16x16x32_bf16 v[48:51], v[216:219], v[100:103], v[48:51]\n"
"v_add_f32 v79, v79, v111\n"
"s_waitcnt lgkmcnt(6)\n"
"v_add_f32 v68, v68, v80\n"
"v_add_f32 v69, v69, v81\n"
"v_mfma_f32_16x16x32_bf16 v[52:55], v[220:223], v[100:103], v[52:55]\n"
"v_add_f32 v70, v70, v82\n"
"v_add_f32 v71, v71, v83\n"
"s_waitcnt lgkmcnt(4)\n"
"v_add_f32 v72, v72, v84\n"
"v_mfma_f32_16x16x32_bf16 v[48:51], v[236:239], v[104:107], v[48:51]\n"
"v_add_f32 v73, v73, v85\n"
"v_add_f32 v74, v74, v86\n"
"v_add_f32 v75, v75, v87\n"
"v_mfma_f32_16x16x32_bf16 v[52:55], v[240:243], v[104:107], v[52:55]\n"
"ds_read_b32 v3, v5 offset:768\n"
"ds_read_b128 v[112:115], v11 offset:2048\n"
"ds_read_b128 v[60:63], v11 offset:0\n"
"ds_read_b128 v[64:67], v11 offset:1024\n"
"v_add_f32 v56, v56, v76\n"
"v_add_f32 v57, v57, v77\n"
"v_add_f32 v58, v58, v78\n"
"v_add_f32 v59, v59, v79\n"
"v_add_f32 v48, v48, v68\n"
"v_add_f32 v49, v49, v69\n"
"v_add_f32 v50, v50, v70\n"
"v_add_f32 v51, v51, v71\n"
"v_add_f32 v52, v52, v72\n"
"v_add_f32 v53, v53, v73\n"
"v_add_f32 v54, v54, v74\n"
"v_add_f32 v55, v55, v75\n"
"s_mov_b32 s17, 0x100000\n"
"s_waitcnt lgkmcnt(3)\n"
"v_cmp_eq_u32 vcc, 0, v3\n"
"s_cbranch_vccz .Lok_566\n"
".Ltp_565:\n"
"ds_read_b32 v3, v5 offset:768\n"
"ds_read_b128 v[112:115], v11 offset:2048\n"
"ds_read_b128 v[60:63], v11 offset:0\n"
"ds_read_b128 v[64:67], v11 offset:1024\n"
"s_sub_u32 s17, s17, 1\n"
"s_cbranch_scc1 .Lok_566\n"
"s_waitcnt lgkmcnt(3)\n"
"v_cmp_eq_u32 vcc, 0, v3\n"
"s_cbranch_vccnz .Ltp_565\n"
"s_waitcnt lgkmcnt(0)\n"
".Lok_566:\n"
"s_waitcnt lgkmcnt(2)\n"
"v_add_f32 v56, v56, v112\n"
"v_add_f32 v57, v57, v113\n"
"v_add_f32 v58, v58, v114\n"
"v_add_f32 v59, v59, v115\n"
"v_cvt_pk_bf16_f32 v44, v56, v57\n"
"v_cvt_pk_bf16_f32 v45, v58, v59\n"
"v_and_b32 v44, v19, v44\n"
"v_and_b32 v45, v19, v45\n"
"s_waitcnt lgkmcnt(1)\n"
"v_add_f32 v48, v48, v60\n"
"v_add_f32 v49, v49, v61\n"
"v_add_f32 v50, v50, v62\n"
"v_add_f32 v51, v51, v63\n"
"v_cvt_pk_bf16_f32 v40, v48, v49\n"
"v_cvt_pk_bf16_f32 v41, v50, v51\n"
"v_mfma_f32_16x16x32_bf16 v[88:91], v[44:47], v[44:47], 0\n"
"v_mfma_f32_16x16x32_bf16 v[92:95], v[20:23], v[44:47], 0\n"
"s_waitcnt lgkmcnt(0)\n"
"v_add_f32 v52, v52, v64\n"
"v_add_f32 v53, v53, v65\n"
"v_add_f32 v54, v54, v66\n"
"v_add_f32 v55, v55, v67\n"
"v_cvt_pk_bf16_f32 v42, v52, v53\n"
"v_cvt_pk_bf16_f32 v43, v54, v55\n"
"v_cvt_pk_bf16_f32 v116, v48, v49\n"
"v_cvt_pk_bf16_f32 v118, v50, v51\n"
"v_mfma_f32_16x16x32_bf16 v[88:91], v[40:43], v[40:43], v[88:91]\n"
"v_mfma_f32_16x16x32_bf16 v[92:95], v[20:23], v[40:43], v[92:95]\n"
"v_cvt_pk_bf16_f32 v120, v52, v53\n"
"v_cvt_pk_bf16_f32 v122, v54, v55\n"
"v_cvt_pk_bf16_f32 v124, v56, v57\n"
"v_cvt_pk_bf16_f32 v126, v58, v59\n"
"s_nop 0\n"
"v_cndmask_b32 v101, v88, v90, s[44:45]\n"
"v_cndmask_b32 v88, v89, v91, s[44:45]\n"
"s_nop 0\n"
"v_min_u32 v100, -2, v92\n"
"v_min_u32 v88, -2, v88\n"
"v_min_u32 v101, -2, v101\n"
"v_mov_b32_dpp v102, v100 quad_perm:[1,0,3,2] row_mask:0xf bank_mask:0xf\n"
"v_mov_b32_dpp v103, v88 quad_perm:[1,0,3,2] row_mask:0xf bank_mask:0xf\n"
"buffer_store_dwordx4 v[100:103], v16, s[4:7], s38 offen sc1\n"
"buffer_store_dwordx4 v[116:119], v17, s[4:7], s37 offen offset:0 sc1\n"
"buffer_store_dwordx4 v[120:123], v17, s[4:7], s37 offen offset:1024 sc1\n"
"buffer_store_dwordx4 v[124:127], v18, s[4:7], s37 offen offset:2048 sc1\n"
"ds_write_b32 v4, v4 offset:784\n"
"s_xor_b32 s79, s3, 1\n"
"s_lshl_b32 s79, s79, 8\n"
"s_add_u32 s32, s19, s79\n"
"s_cmp_ge_u32 s32, 64\n"
"s_cbranch_scc1 .Lnr_567\n"
"s_lshl_b32 s79, s19, 11\n"
"s_add_u32 s32, s79, 0x1648000\n"
"s_lshl_b32 s79, s19, 7\n"
"s_add_u32 s33, s79, 0x166a000\n"
"v_lshrrev_b32 v99, 3, v0\n"
"v_and_b32 v98, 7, v0\n"
"v_lshlrev_b32 v98, 4, v98\n"
"v_lshl_add_u32 v40, v99, 14, v98\n"
"v_mov_b32 v41, 0x3727c5ac\n"
"s_mov_b32 s89, 16\n"
".Ldl_568:\n"
"s_sub_u32 s89, s89, 1\n"
"s_cbranch_scc1 .Lde_569\n"
"s_sleep 1\n"
"s_branch .Ldl_568\n"
".Lde_569:\n"
"buffer_load_dwordx4 v[48:51], v2, s[4:7], s32 offen offset:0 sc1\n"
"buffer_load_dwordx4 v[52:55], v2, s[4:7], s32 offen offset:1024 sc1\n"
"s_mov_b32 s17, 32768\n"
".Ltp_570:\n"
"s_waitcnt vmcnt(0)\n"
"s_sub_u32 s17, s17, 1\n"
"s_cbranch_scc1 .Lou_571\n"
"v_max_u32 v38, v48, v49\n"
"v_max3_u32 v38, v50, v51, v38\n"
"v_max3_u32 v38, v52, v53, v38\n"
"v_max3_u32 v38, v54, v55, v38\n"
"v_cmp_eq_u32 vcc, -1, v38\n"
"s_cbranch_vccz .Lou_571\n"
"v_max_u32 v38, v50, v51\n"
"v_max3_u32 v38, v48, v49, v38\n"
"v_cmp_eq_u32 vcc, -1, v38\n"
"s_cbranch_vccz .Lsk_572\n"
"s_mov_b64 exec, vcc\n"
"buffer_load_dwordx4 v[48:51], v2, s[4:7], s32 offen offset:0 sc1\n"
"s_mov_b64 exec, -1\n"
".Lsk_572:\n"
"v_max_u32 v38, v54, v55\n"
"v_max3_u32 v38, v52, v53, v38\n"
"v_cmp_eq_u32 vcc, -1, v38\n"
"s_cbranch_vccz .Lsk_573\n"
"s_mov_b64 exec, vcc\n"
"buffer_load_dwordx4 v[52:55], v2, s[4:7], s32 offen offset:1024 sc1\n"
"s_mov_b64 exec, -1\n"
".Lsk_573:\n"
"s_branch .Ltp_570\n"
".Lou_571:\n"
"s_waitcnt vmcnt(0)\n"
"v_add_f32 v48, v48, v52\n"
"v_add_f32 v49, v49, v53\n"
"v_add_f32 v50, v50, v54\n"
"v_add_f32 v51, v51, v55\n"
"v_add_f32_dpp v52, v48, v48 row_ror:8 row_mask:0xf bank_mask:0xf\n"
"v_add_f32_dpp v53, v49, v49 row_ror:8 row_mask:0xf bank_mask:0xf\n"
"v_add_f32_dpp v54, v50, v50 row_ror:8 row_mask:0xf bank_mask:0xf\n"
"v_add_f32_dpp v55, v51, v51 row_ror:8 row_mask:0xf bank_mask:0xf\n"
"v_mov_b32 v48, v52\n"
"v_mov_b32 v49, v53\n"
"v_mov_b32 v50, v54\n"
"v_mov_b32 v51, v55\n"
"v_permlane16_swap_b32 v48, v52\n"
"v_permlane16_swap_b32 v49, v53\n"
"v_permlane16_swap_b32 v50, v54\n"
"v_permlane16_swap_b32 v51, v55\n"
"v_add_f32 v52, v48, v52\n"
"v_add_f32 v53, v49, v53\n"
"v_add_f32 v54, v50, v54\n"
"v_add_f32 v55, v51, v55\n"
"v_mov_b32 v48, v52\n"
"v_mov_b32 v49, v53\n"
"v_mov_b32 v50, v54\n"
"v_mov_b32 v51, v55\n"
"v_permlane32_swap_b32 v48, v52\n"
"v_permlane32_swap_b32 v49, v53\n"
"v_permlane32_swap_b32 v50, v54\n"
"v_permlane32_swap_b32 v51, v55\n"
"v_add_f32 v52, v48, v52\n"
"v_add_f32 v53, v49, v53\n"
"v_add_f32 v54, v50, v54\n"
"v_add_f32 v55, v51, v55\n"
"v_mul_f32 v48, 0x3acccccd, v52\n"
"v_mul_f32 v50, 0x3acccccd, v54\n"
"v_fma_f32 v53, -v52, v48, v53\n"
"v_fma_f32 v55, -v54, v50, v55\n"
"v_fmamk_f32 v53, v53, 0x3acccccd, v41\n"
"v_fmamk_f32 v55, v55, 0x3acccccd, v41\n"
"v_max_f32 v53, v53, v41\n"
"v_max_f32 v55, v55, v41\n"
"v_rsq_f32 v49, v53\n"
"v_rsq_f32 v51, v55\n"
"v_mul_f32 v48, -v48, v49\n"
"v_mul_f32 v50, -v50, v51\n"
"v_min_u32 v49, -2, v49\n"
"v_min_u32 v51, -2, v51\n"
"v_min_u32 v48, -2, v48\n"
"v_min_u32 v50, -2, v50\n"
"buffer_store_dwordx4 v[48:51], v40, s[4:7], s33 offen sc1\n"
"s_nop 3\n"
".Lnr_567:\n"
"s_cmp_lt_i32 s18, 0\n"
"s_cbranch_scc1 .Lnc_574\n"
"s_mov_b32 s17, 0x100000\n"
".Ltp_575:\n"
"s_sleep 1\n"
"ds_read_b32 v3, v5 offset:512\n"
"s_sub_u32 s17, s17, 1\n"
"s_cbranch_scc1 .Lou_576\n"
"s_waitcnt lgkmcnt(0)\n"
"v_cmp_eq_u32 vcc, 0, v3\n"
"s_cbranch_vccnz .Ltp_575\n"
".Lou_576:\n"
"s_waitcnt lgkmcnt(0)\n"
"s_setprio 0\n"
"s_lshl_b32 s79, s18, 8\n"
"s_add_u32 s32, s79, 0x13a8000\n"
"s_add_u32 s33, s32, 0x140000\n"
"s_and_b32 s79, s2, 7\n"
"s_lshl_b32 s79, s79, 14\n"
"s_add_u32 s34, s79, 0x166a000\n"
"s_add_u32 s35, s34, 0x1000\n"
"v_and_b32 v126, 0xffffff80, v29\n"
"v_add_u32 v126, v126, v29\n"
"ds_read_b128 v[88:91], v25 offset:448\n"
"ds_read_b128 v[92:95], v25 offset:432\n"
"ds_read_b64 v[96:97], v24 offset:448\n"
"ds_read_b64 v[98:99], v24 offset:456\n"
"ds_read_b128 v[100:103], v25 offset:960\n"
"ds_read_b128 v[104:107], v25 offset:1472\n"
"s_waitcnt lgkmcnt(1)\n"
"v_mfma_f32_16x16x32_f16 v[64:67], v[88:91], v[144:147], 0\n"
"ds_read_b128 v[40:43], v25 offset:1984\n"
"ds_read_b64 v[44:45], v24 offset:960\n"
"v_mfma_f32_16x16x32_f16 v[68:71], v[92:95], v[148:151], 0\n"
"ds_read_b64 v[46:47], v24 offset:968\n"
"ds_read_b64 v[72:73], v24 offset:1472\n"
"v_mfma_f32_16x16x32_f16 v[48:51], v[152:155], v[88:91], 0\n"
"ds_read_b64 v[74:75], v24 offset:1480\n"
"ds_read_b64 v[76:77], v24 offset:384\n"
"v_mfma_f32_16x16x32_f16 v[48:51], v[96:99], v[100:103], v[48:51]\n"
"ds_read_b64 v[78:79], v24 offset:392\n"
"s_waitcnt lgkmcnt(4)\n"
"v_mfma_f32_16x16x32_f16 v[52:55], v[96:99], v[104:107], 0\n"
"ds_read_b128 v[84:87], v25 offset:896\n"
"ds_read_b128 v[92:95], v25 offset:1408\n"
"v_mfma_f32_16x16x32_f16 v[56:59], v[96:99], v[40:43], 0\n"
"ds_read_b128 v[88:91], v25 offset:1920\n"
"ds_read_b64 v[100:101], v24 offset:896\n"
"v_mfma_f32_16x16x32_f16 v[52:55], v[44:47], v[40:43], v[52:55]\n"
"ds_read_b64 v[102:103], v24 offset:904\n"
"ds_read_b64 v[96:97], v24 offset:1408\n"
"v_cvt_pk_f16_f32 v80, v64, v65\n"
"v_mfma_f32_16x16x32_f16 v[48:51], v[44:47], v[104:107], v[48:51]\n"
"ds_read_b64 v[98:99], v24 offset:1416\n"
"s_waitcnt lgkmcnt(4)\n"
"v_mfma_f32_16x16x32_f16 v[48:51], v[72:75], v[40:43], v[48:51]\n"
"ds_read_b128 v[104:107], v25 offset:960\n"
"v_cvt_pk_f16_f32 v81, v66, v67\n"
"v_mfma_f32_16x16x32_f16 v[48:51], v[76:79], v[84:87], v[48:51]\n"
"ds_read_b128 v[44:47], v25 offset:944\n"
"v_cvt_pk_f16_f32 v82, v68, v69\n"
"v_mfma_f32_16x16x32_f16 v[52:55], v[76:79], v[92:95], v[52:55]\n"
"ds_read_b64 v[40:41], v24 offset:320\n"
"ds_read_b64 v[42:43], v24 offset:328\n"
"v_mfma_f32_16x16x32_f16 v[56:59], v[76:79], v[88:91], v[56:59]\n"
"ds_read_b128 v[72:75], v25 offset:832\n"
"s_waitcnt lgkmcnt(5)\n"
"v_mfma_f32_16x16x32_f16 v[52:55], v[100:103], v[88:91], v[52:55]\n"
"ds_read_b128 v[84:87], v25 offset:1344\n"
"v_cvt_pk_f16_f32 v83, v70, v71\n"
"v_mfma_f32_16x16x32_f16 v[48:51], v[100:103], v[92:95], v[48:51]\n"
"ds_read_b128 v[76:79], v25 offset:1856\n"
"ds_read_b64 v[92:93], v24 offset:832\n"
"v_mfma_f32_16x16x32_f16 v[48:51], v[96:99], v[88:91], v[48:51]\n"
"ds_read_b64 v[94:95], v24 offset:840\n"
"v_mfma_f32_16x16x32_f16 v[48:51], v[80:83], v[128:131], v[48:51]\n"
"ds_read_b64 v[100:101], v24 offset:1344\n"
"s_waitcnt lgkmcnt(9)\n"
"v_mfma_f32_16x16x32_f16 v[52:55], v[80:83], v[132:135], v[52:55]\n"
"ds_read_b64 v[102:103], v24 offset:1352\n"
"ds_read_b64 v[88:89], v24 offset:256\n"
"v_mfma_f32_16x16x32_f16 v[56:59], v[80:83], v[136:139], v[56:59]\n"
"ds_read_b64 v[90:91], v24 offset:264\n"
"v_mfma_f32_16x16x32_f16 v[60:63], v[80:83], v[140:143], 0\n"
"ds_read_b128 v[96:99], v25 offset:768\n"
"v_mfma_f32_16x16x32_f16 v[64:67], v[104:107], v[144:147], 0\n"
"s_waitcnt lgkmcnt(8)\n"
"v_mfma_f32_16x16x32_f16 v[68:71], v[44:47], v[148:151], 0\n"
"ds_read_b128 v[44:47], v25 offset:1280\n"
"v_mfma_f32_16x16x32_f16 v[52:55], v[152:155], v[104:107], v[52:55]\n"
"ds_read_b128 v[104:107], v25 offset:1792\n"
"v_mfma_f32_16x16x32_f16 v[48:51], v[40:43], v[72:75], v[48:51]\n"
"ds_read_b64 v[72:73], v24 offset:768\n"
"v_mfma_f32_16x16x32_f16 v[52:55], v[40:43], v[84:87], v[52:55]\n"
"ds_read_b64 v[74:75], v24 offset:776\n"
"s_waitcnt lgkmcnt(7)\n"
"v_mfma_f32_16x16x32_f16 v[56:59], v[40:43], v[76:79], v[56:59]\n"
"ds_read_b64 v[40:41], v24 offset:1280\n"
"v_mfma_f32_16x16x32_f16 v[52:55], v[92:95], v[76:79], v[52:55]\n"
"ds_read_b64 v[42:43], v24 offset:1288\n"
"v_cvt_pk_f16_f32 v80, v64, v65\n"
"v_mfma_f32_16x16x32_f16 v[48:51], v[92:95], v[84:87], v[48:51]\n"
"ds_read_b128 v[84:87], v25 offset:1472\n"
"v_cvt_pk_f16_f32 v81, v66, v67\n"
"v_mfma_f32_16x16x32_f16 v[48:51], v[100:103], v[76:79], v[48:51]\n"
"ds_read_b128 v[92:95], v25 offset:1456\n"
"s_waitcnt lgkmcnt(4)\n"
"v_mfma_f32_16x16x32_f16 v[48:51], v[88:91], v[96:99], v[48:51]\n"
"ds_read_b64 v[76:77], v24 offset:192\n"
"ds_read_b64 v[78:79], v24 offset:200\n"
"v_mfma_f32_16x16x32_f16 v[52:55], v[88:91], v[44:47], v[52:55]\n"
"ds_read_b128 v[100:103], v25 offset:704\n"
"v_cvt_pk_f16_f32 v82, v68, v69\n"
"v_mfma_f32_16x16x32_f16 v[56:59], v[88:91], v[104:107], v[56:59]\n"
"ds_read_b128 v[96:99], v25 offset:1216\n"
"v_cvt_pk_f16_f32 v83, v70, v71\n"
"v_mfma_f32_16x16x32_f16 v[52:55], v[72:75], v[104:107], v[52:55]\n"
"ds_read_b128 v[88:91], v25 offset:1728\n"
"s_waitcnt lgkmcnt(7)\n"
"v_mfma_f32_16x16x32_f16 v[48:51], v[72:75], v[44:47], v[48:51]\n"
"ds_read_b64 v[44:45], v24 offset:704\n"
"v_mfma_f32_16x16x32_f16 v[48:51], v[40:43], v[104:107], v[48:51]\n"
"ds_read_b64 v[46:47], v24 offset:712\n"
"ds_read_b64 v[72:73], v24 offset:1216\n"
"v_mfma_f32_16x16x32_f16 v[52:55], v[80:83], v[128:131], v[52:55]\n"
"ds_read_b64 v[74:75], v24 offset:1224\n"
"ds_read_b64 v[104:105], v24 offset:128\n"
"v_mfma_f32_16x16x32_f16 v[56:59], v[80:83], v[132:135], v[56:59]\n"
"ds_read_b64 v[106:107], v24 offset:136\n"
"s_waitcnt lgkmcnt(11)\n"
"v_mfma_f32_16x16x32_f16 v[60:63], v[80:83], v[136:139], v[60:63]\n"
"ds_read_b128 v[40:43], v25 offset:640\n"
"v_mfma_f32_16x16x32_f16 v[64:67], v[84:87], v[144:147], 0\n"
"v_mfma_f32_16x16x32_f16 v[68:71], v[92:95], v[148:151], 0\n"
"ds_read_b128 v[92:95], v25 offset:1152\n"
"v_mfma_f32_16x16x32_f16 v[56:59], v[152:155], v[84:87], v[56:59]\n"
"ds_read_b128 v[84:87], v25 offset:1664\n"
"s_waitcnt lgkmcnt(7)\n"
"v_mfma_f32_16x16x32_f16 v[48:51], v[76:79], v[100:103], v[48:51]\n"
"ds_read_b64 v[100:101], v24 offset:640\n"
"v_mfma_f32_16x16x32_f16 v[52:55], v[76:79], v[96:99], v[52:55]\n"
"ds_read_b64 v[102:103], v24 offset:648\n"
"v_mfma_f32_16x16x32_f16 v[56:59], v[76:79], v[88:91], v[56:59]\n"
"ds_read_b64 v[76:77], v24 offset:1152\n"
"v_mfma_f32_16x16x32_f16 v[52:55], v[44:47], v[88:91], v[52:55]\n"
"ds_read_b64 v[78:79], v24 offset:1160\n"
"s_waitcnt lgkmcnt(5)\n"
"v_mfma_f32_16x16x32_f16 v[48:51], v[44:47], v[96:99], v[48:51]\n"
"ds_read_b128 v[96:99], v25 offset:1984\n"
"v_cvt_pk_f16_f32 v80, v64, v65\n"
"v_mfma_f32_16x16x32_f16 v[48:51], v[72:75], v[88:91], v[48:51]\n"
"ds_read_b128 v[44:47], v25 offset:1968\n"
"ds_read_b64 v[88:89], v24 offset:64\n"
"v_mfma_f32_16x16x32_f16 v[48:51], v[104:107], v[40:43], v[48:51]\n"
"ds_read_b64 v[90:91], v24 offset:72\n"
"ds_read_b128 v[72:75], v25 offset:576\n"
"v_mfma_f32_16x16x32_f16 v[52:55], v[104:107], v[92:95], v[52:55]\n"
"ds_read_b128 v[40:43], v25 offset:1088\n"
"s_waitcnt lgkmcnt(6)\n"
"v_mfma_f32_16x16x32_f16 v[56:59], v[104:107], v[84:87], v[56:59]\n"
"ds_read_b128 v[104:107], v25 offset:1600\n"
"v_cvt_pk_f16_f32 v81, v66, v67\n"
"v_mfma_f32_16x16x32_f16 v[52:55], v[100:103], v[84:87], v[52:55]\n"
"v_cvt_pk_f16_f32 v82, v68, v69\n"
"v_cvt_pk_f16_f32 v83, v70, v71\n"
"v_mfma_f32_16x16x32_f16 v[48:51], v[100:103], v[92:95], v[48:51]\n"
"ds_read_b64 v[92:93], v24 offset:576\n"
"ds_read_b64 v[94:95], v24 offset:584\n"
"v_mfma_f32_16x16x32_f16 v[48:51], v[76:79], v[84:87], v[48:51]\n"
"ds_read_b64 v[100:101], v24 offset:1088\n"
"s_waitcnt lgkmcnt(8)\n"
"v_mfma_f32_16x16x32_f16 v[56:59], v[80:83], v[128:131], v[56:59]\n"
"ds_read_b64 v[102:103], v24 offset:1096\n"
"ds_read_b64 v[84:85], v24 offset:0\n"
"v_mfma_f32_16x16x32_f16 v[60:63], v[80:83], v[132:135], v[60:63]\n"
"ds_read_b64 v[86:87], v24 offset:8\n"
"v_mfma_f32_16x16x32_f16 v[64:67], v[96:99], v[144:147], 0\n"
"ds_read_b128 v[76:79], v25 offset:512\n"
"v_mfma_f32_16x16x32_f16 v[68:71], v[44:47], v[148:151], 0\n"
"ds_read_b128 v[44:47], v25 offset:1024\n"
"s_waitcnt lgkmcnt(8)\n"
"v_mfma_f32_16x16x32_f16 v[60:63], v[152:155], v[96:99], v[60:63]\n"
"ds_read_b128 v[96:99], v25 offset:1536\n"
"v_mfma_f32_16x16x32_f16 v[48:51], v[88:91], v[72:75], v[48:51]\n"
"ds_read_b64 v[72:73], v24 offset:512\n"
"v_mfma_f32_16x16x32_f16 v[52:55], v[88:91], v[40:43], v[52:55]\n"
"ds_read_b64 v[74:75], v24 offset:520\n"
"v_mfma_f32_16x16x32_f16 v[56:59], v[88:91], v[104:107], v[56:59]\n"
"ds_read_b64 v[88:89], v24 offset:1024\n"
"s_waitcnt lgkmcnt(5)\n"
"v_mfma_f32_16x16x32_f16 v[52:55], v[92:95], v[104:107], v[52:55]\n"
"ds_read_b64 v[90:91], v24 offset:1032\n"
"v_cvt_pk_f16_f32 v80, v64, v65\n"
"v_mfma_f32_16x16x32_f16 v[48:51], v[92:95], v[40:43], v[48:51]\n"
"v_cvt_pk_f16_f32 v81, v66, v67\n"
"v_cvt_pk_f16_f32 v82, v68, v69\n"
"v_mfma_f32_16x16x32_f16 v[48:51], v[100:103], v[104:107], v[48:51]\n"
"v_cvt_pk_f16_f32 v83, v70, v71\n"
"v_mfma_f32_16x16x32_f16 v[48:51], v[84:87], v[76:79], v[48:51]\n"
"s_waitcnt lgkmcnt(2)\n"
"v_mfma_f32_16x16x32_f16 v[52:55], v[84:87], v[44:47], v[52:55]\n"
"v_mfma_f32_16x16x32_f16 v[56:59], v[84:87], v[96:99], v[56:59]\n"
"v_mfma_f32_16x16x32_f16 v[52:55], v[72:75], v[96:99], v[52:55]\n"
"v_mfma_f32_16x16x32_f16 v[48:51], v[72:75], v[44:47], v[48:51]\n"
"s_waitcnt lgkmcnt(0)\n"
"v_mfma_f32_16x16x32_f16 v[48:51], v[88:91], v[96:99], v[48:51]\n"
"v_mfma_f32_16x16x32_f16 v[60:63], v[80:83], v[128:131], v[60:63]\n"
"s_setprio 2\n"
"s_mov_b32 s17, 0x100000\n"
".Ltp_577:\n"
"ds_read_b32 v3, v5 offset:784\n"
"s_sub_u32 s17, s17, 1\n"
"s_cbranch_scc1 .Lou_578\n"
"s_waitcnt lgkmcnt(0)\n"
"v_cmp_eq_u32 vcc, 0, v3\n"
"s_cbranch_vccnz .Ltp_577\n"
".Lou_578:\n"
"s_waitcnt lgkmcnt(0)\n"
"buffer_load_dwordx4 v[110:113], v126, s[4:7], s32 offen offset:0 sc1\n"
"buffer_load_dwordx4 v[114:117], v126, s[4:7], s32 offen offset:128 sc1\n"
"buffer_load_dwordx4 v[118:121], v126, s[4:7], s33 offen offset:0 sc1\n"
"buffer_load_dwordx4 v[122:125], v126, s[4:7], s33 offen offset:128 sc1\n"
"ds_read_b64 v[128:129], v24 offset:1984\n"
"ds_read_b64 v[130:131], v24 offset:1992\n"
"ds_read_b64 v[132:133], v24 offset:1920\n"
"ds_read_b64 v[134:135], v24 offset:1928\n"
"ds_read_b64 v[136:137], v24 offset:1856\n"
"ds_read_b64 v[138:139], v24 offset:1864\n"
"ds_read_b64 v[140:141], v24 offset:1792\n"
"ds_read_b64 v[142:143], v24 offset:1800\n"
"ds_read_b64 v[144:145], v24 offset:1728\n"
"ds_read_b64 v[146:147], v24 offset:1736\n"
"ds_read_b64 v[148:149], v24 offset:1664\n"
"ds_read_b64 v[150:151], v24 offset:1672\n"
"ds_read_b64 v[152:153], v24 offset:1600\n"
"ds_read_b64 v[154:155], v24 offset:1608\n"
"ds_read_b64 v[156:157], v24 offset:1536\n"
"ds_read_b64 v[158:159], v24 offset:1544\n"
"ds_read_b64 v[160:161], v24 offset:1472\n"
"ds_read_b64 v[162:163], v24 offset:1480\n"
"ds_read_b64 v[164:165], v24 offset:1408\n"
"ds_read_b64 v[166:167], v24 offset:1416\n"
"ds_read_b64 v[168:169], v24 offset:1344\n"
"ds_read_b64 v[170:171], v24 offset:1352\n"
"ds_read_b64 v[172:173], v24 offset:1280\n"
"ds_read_b64 v[174:175], v24 offset:1288\n"
"ds_read_b64 v[176:177], v24 offset:1216\n"
"ds_read_b64 v[178:179], v24 offset:1224\n"
"ds_read_b64 v[180:181], v24 offset:1152\n"
"ds_read_b64 v[182:183], v24 offset:1160\n"
"ds_read_b64 v[184:185], v24 offset:1088\n"
"ds_read_b64 v[186:187], v24 offset:1096\n"
"ds_read_b64 v[188:189], v24 offset:1024\n"
"ds_read_b64 v[190:191], v24 offset:1032\n"
"ds_read_b64 v[192:193], v24 offset:960\n"
"ds_read_b64 v[194:195], v24 offset:968\n"
"ds_read_b64 v[196:197], v24 offset:896\n"
"ds_read_b64 v[198:199], v24 offset:904\n"
"ds_read_b64 v[200:201], v24 offset:832\n"
"ds_read_b64 v[202:203], v24 offset:840\n"
"ds_read_b64 v[204:205], v24 offset:768\n"
"ds_read_b64 v[206:207], v24 offset:776\n"
"ds_read_b64 v[208:209], v24 offset:704\n"
"ds_read_b64 v[210:211], v24 offset:712\n"
"ds_read_b64 v[212:213], v24 offset:640\n"
"ds_read_b64 v[214:215], v24 offset:648\n"
"ds_read_b64 v[216:217], v24 offset:576\n"
"ds_read_b64 v[218:219], v24 offset:584\n"
"ds_read_b64 v[220:221], v24 offset:512\n"
"ds_read_b64 v[222:223], v24 offset:520\n"
"ds_read_b64 v[224:225], v24 offset:448\n"
"ds_read_b64 v[226:227], v24 offset:456\n"
"ds_read_b64 v[228:229], v24 offset:384\n"
"ds_read_b64 v[230:231], v24 offset:392\n"
"ds_read_b64 v[232:233], v24 offset:320\n"
"ds_read_b64 v[234:235], v24 offset:328\n"
"ds_read_b64 v[236:237], v24 offset:256\n"
"ds_read_b64 v[238:239], v24 offset:264\n"
"ds_read_b64 v[240:241], v24 offset:192\n"
"ds_read_b64 v[242:243], v24 offset:200\n"
"ds_read_b64 v[244:245], v24 offset:128\n"
"ds_read_b64 v[246:247], v24 offset:136\n"
"ds_read_b64 v[248:249], v24 offset:64\n"
"ds_read_b64 v[250:251], v24 offset:72\n"
"ds_read_b64 v[252:253], v24 offset:0\n"
"ds_read_b64 v[254:255], v24 offset:8\n"
"s_mov_b32 s38, 0x05040100\n"
"s_mov_b32 s39, 0x07060302\n"
"s_mov_b32 s17, 32768\n"
".Ltp_579:\n"
"s_waitcnt vmcnt(0)\n"
"s_sub_u32 s17, s17, 1\n"
"s_cbranch_scc1 .Lou_580\n"
"v_bitop3_b32 v64, v111, v113, s16 bitop3:0x7e\n"
"v_bitop3_b32 v65, v115, v117, s16 bitop3:0x7e\n"
"v_bitop3_b32 v66, v119, v121, s16 bitop3:0x7e\n"
"v_bitop3_b32 v67, v123, v125, s16 bitop3:0x7e\n"
"v_or3_b32 v68, v64, v65, v66\n"
"v_or_b32 v68, v68, v67\n"
"v_cmp_ne_u32 vcc, 0, v68\n"
"s_cbranch_vccz .Lou_580\n"
"s_sleep 2\n"
"v_cmp_ne_u32 vcc, 0, v64\n"
"s_cbranch_vccz .Lsk_581\n"
"s_mov_b64 exec, vcc\n"
"buffer_load_dwordx4 v[110:113], v126, s[4:7], s32 offen offset:0 sc1\n"
"s_mov_b64 exec, -1\n"
".Lsk_581:\n"
"v_cmp_ne_u32 vcc, 0, v65\n"
"s_cbranch_vccz .Lsk_582\n"
"s_mov_b64 exec, vcc\n"
"buffer_load_dwordx4 v[114:117], v126, s[4:7], s32 offen offset:128 sc1\n"
"s_mov_b64 exec, -1\n"
".Lsk_582:\n"
"v_cmp_ne_u32 vcc, 0, v66\n"
"s_cbranch_vccz .Lsk_583\n"
"s_mov_b64 exec, vcc\n"
"buffer_load_dwordx4 v[118:121], v126, s[4:7], s33 offen offset:0 sc1\n"
"s_mov_b64 exec, -1\n"
".Lsk_583:\n"
"v_cmp_ne_u32 vcc, 0, v67\n"
"s_cbranch_vccz .Lsk_584\n"
"s_mov_b64 exec, vcc\n"
"buffer_load_dwordx4 v[122:125], v126, s[4:7], s33 offen offset:128 sc1\n"
"s_mov_b64 exec, -1\n"
".Lsk_584:\n"
"s_branch .Ltp_579\n"
".Lou_580:\n"
"s_waitcnt vmcnt(0)\n"
"buffer_load_dwordx4 v[88:91], v30, s[4:7], s34 offen sc1\n"
"buffer_load_dwordx4 v[92:95], v30, s[4:7], s35 offen sc1\n"
"v_perm_b32 v64, v114, v110, s38\n"
"v_perm_b32 v65, v114, v110, s39\n"
"v_perm_b32 v66, v116, v112, s38\n"
"v_perm_b32 v67, v116, v112, s39\n"
"ds_write_b32 v32, v64 offset:0\n"
"ds_write_b32 v33, v65 offset:2048\n"
"ds_write_b32 v32, v66 offset:4224\n"
"ds_write_b32 v33, v67 offset:6272\n"
"v_perm_b32 v64, v122, v118, s38\n"
"v_perm_b32 v65, v122, v118, s39\n"
"v_perm_b32 v66, v124, v120, s38\n"
"v_perm_b32 v67, v124, v120, s39\n"
"ds_write_b32 v32, v64 offset:1024\n"
"ds_write_b32 v33, v65 offset:3072\n"
"ds_write_b32 v32, v66 offset:5248\n"
"ds_write_b32 v33, v67 offset:7296\n"
"v_lshlrev_b32 v64, 16, v110\n"
"v_lshlrev_b32 v65, 16, v114\n"
"v_and_b32 v66, 0xffff0000, v110\n"
"v_and_b32 v67, 0xffff0000, v114\n"
"v_lshlrev_b32 v68, 16, v112\n"
"v_lshlrev_b32 v69, 16, v116\n"
"v_and_b32 v70, 0xffff0000, v112\n"
"v_and_b32 v71, 0xffff0000, v116\n"
"v_lshlrev_b32 v72, 16, v118\n"
"v_lshlrev_b32 v73, 16, v122\n"
"v_and_b32 v74, 0xffff0000, v118\n"
"v_and_b32 v75, 0xffff0000, v122\n"
"v_lshlrev_b32 v76, 16, v120\n"
"v_lshlrev_b32 v77, 16, v124\n"
"v_and_b32 v78, 0xffff0000, v120\n"
"v_and_b32 v79, 0xffff0000, v124\n"
"s_mov_b32 s17, 32768\n"
".Ltp_585:\n"
"s_waitcnt vmcnt(0)\n"
"s_sub_u32 s17, s17, 1\n"
"s_cbranch_scc1 .Lou_586\n"
"v_max_u32 v110, v88, v89\n"
"v_max3_u32 v110, v90, v91, v110\n"
"v_max3_u32 v110, v92, v93, v110\n"
"v_max3_u32 v110, v94, v95, v110\n"
"v_cmp_eq_u32 vcc, -1, v110\n"
"s_cbranch_vccz .Lou_586\n"
"v_max_u32 v110, v90, v91\n"
"v_max3_u32 v110, v88, v89, v110\n"
"v_cmp_eq_u32 vcc, -1, v110\n"
"s_cbranch_vccz .Lsk_587\n"
"s_mov_b64 exec, vcc\n"
"buffer_load_dwordx4 v[88:91], v30, s[4:7], s34 offen sc1\n"
"s_mov_b64 exec, -1\n"
".Lsk_587:\n"
"v_max_u32 v110, v94, v95\n"
"v_max3_u32 v110, v92, v93, v110\n"
"v_cmp_eq_u32 vcc, -1, v110\n"
"s_cbranch_vccz .Lsk_588\n"
"s_mov_b64 exec, vcc\n"
"buffer_load_dwordx4 v[92:95], v30, s[4:7], s35 offen sc1\n"
"s_mov_b64 exec, -1\n"
".Lsk_588:\n"
"s_branch .Ltp_585\n"
".Lou_586:\n"
"s_waitcnt vmcnt(0)\n"
"v_fma_f32 v64, v64, v89, v88\n"
"v_fma_f32 v65, v65, v91, v90\n"
"v_cvt_pk_f16_f32 v64, v64, v65\n"
"v_fma_f32 v66, v66, v89, v88\n"
"v_fma_f32 v67, v67, v91, v90\n"
"v_cvt_pk_f16_f32 v66, v66, v67\n"
"v_fma_f32 v68, v68, v89, v88\n"
"v_fma_f32 v69, v69, v91, v90\n"
"v_cvt_pk_f16_f32 v68, v68, v69\n"
"v_fma_f32 v70, v70, v89, v88\n"
"v_fma_f32 v71, v71, v91, v90\n"
"v_cvt_pk_f16_f32 v70, v70, v71\n"
"ds_write2st64_b32 v31, v64, v66 offset0:0 offset1:18\n"
"ds_write2st64_b32 v31, v68, v70 offset0:36 offset1:54\n"
"v_fma_f32 v72, v72, v93, v92\n"
"v_fma_f32 v73, v73, v95, v94\n"
"v_cvt_pk_f16_f32 v72, v72, v73\n"
"v_fma_f32 v74, v74, v93, v92\n"
"v_fma_f32 v75, v75, v95, v94\n"
"v_cvt_pk_f16_f32 v74, v74, v75\n"
"v_fma_f32 v76, v76, v93, v92\n"
"v_fma_f32 v77, v77, v95, v94\n"
"v_cvt_pk_f16_f32 v76, v76, v77\n"
"v_fma_f32 v78, v78, v93, v92\n"
"v_fma_f32 v79, v79, v95, v94\n"
"v_cvt_pk_f16_f32 v78, v78, v79\n"
"ds_write2st64_b32 v31, v72, v74 offset0:4 offset1:22\n"
"ds_write2st64_b32 v31, v76, v78 offset0:40 offset1:58\n"
"ds_write_b32 v4, v4 offset:800\n"
"s_mov_b32 s17, 0x100000\n"
".Ltp_589:\n"
"ds_read_b32 v3, v5 offset:800\n"
"s_sub_u32 s17, s17, 1\n"
"s_cbranch_scc1 .Lou_590\n"
"s_waitcnt lgkmcnt(0)\n"
"v_cmp_eq_u32 vcc, 0, v3\n"
"s_cbranch_vccnz .Ltp_589\n"
".Lou_590:\n"
"s_waitcnt lgkmcnt(0)\n"
"s_mov_b32 s36, 0xfffefffe\n"
"ds_read_b64 v[88:89], v26 offset:0\n"
"ds_read_b64 v[92:93], v26 offset:512\n"
"ds_read_b64 v[96:97], v26 offset:1024\n"
"ds_read_b64 v[100:101], v26 offset:1536\n"
"ds_read_b128 v[104:107], v25 offset:2496\n"
"s_waitcnt lgkmcnt(1)\n"
"v_mfma_f32_16x16x16_bf16 v[48:51], v[34:35], v[88:89], v[48:51]\n"
"ds_read_b128 v[108:111], v25 offset:3008\n"
"ds_read_b128 v[112:115], v25 offset:2432\n"
"v_mfma_f32_16x16x16_bf16 v[52:55], v[34:35], v[92:93], v[52:55]\n"
"ds_read_b128 v[116:119], v25 offset:2944\n"
"ds_read_b128 v[120:123], v25 offset:2368\n"
"v_mfma_f32_16x16x16_bf16 v[56:59], v[34:35], v[96:97], v[56:59]\n"
"ds_read_b128 v[124:127], v25 offset:2880\n"
"v_mfma_f32_16x16x16_bf16 v[60:63], v[34:35], v[100:101], v[60:63]\n"
"ds_read_b128 v[40:43], v25 offset:2304\n"
"s_waitcnt lgkmcnt(4)\n"
"v_mfma_f32_16x16x32_f16 v[48:51], v[128:131], v[104:107], v[48:51]\n"
"ds_read_b128 v[44:47], v25 offset:2816\n"
"v_mfma_f32_16x16x32_f16 v[52:55], v[160:163], v[104:107], v[52:55]\n"
"ds_read_b128 v[88:91], v25 offset:2240\n"
"v_mfma_f32_16x16x32_f16 v[52:55], v[128:131], v[108:111], v[52:55]\n"
"ds_read_b128 v[92:95], v25 offset:2752\n"
"v_mfma_f32_16x16x32_f16 v[48:51], v[132:135], v[112:115], v[48:51]\n"
"ds_read_b128 v[96:99], v25 offset:2176\n"
"s_waitcnt lgkmcnt(6)\n"
"v_mfma_f32_16x16x32_f16 v[52:55], v[164:167], v[112:115], v[52:55]\n"
"v_mfma_f32_16x16x32_f16 v[52:55], v[132:135], v[116:119], v[52:55]\n"
"ds_read_b128 v[100:103], v25 offset:2688\n"
"v_mfma_f32_16x16x32_f16 v[48:51], v[136:139], v[120:123], v[48:51]\n"
"ds_read_b128 v[104:107], v25 offset:2112\n"
"v_mfma_f32_16x16x32_f16 v[52:55], v[168:171], v[120:123], v[52:55]\n"
"s_waitcnt lgkmcnt(5)\n"
"v_mfma_f32_16x16x32_f16 v[52:55], v[136:139], v[124:127], v[52:55]\n"
"ds_read_b128 v[108:111], v25 offset:2624\n"
"v_mfma_f32_16x16x32_f16 v[48:51], v[140:143], v[40:43], v[48:51]\n"
"ds_read_b128 v[112:115], v25 offset:2048\n"
"v_mfma_f32_16x16x32_f16 v[52:55], v[172:175], v[40:43], v[52:55]\n"
"v_mfma_f32_16x16x32_f16 v[52:55], v[140:143], v[44:47], v[52:55]\n"
"ds_read_b128 v[116:119], v25 offset:2560\n"
"s_waitcnt lgkmcnt(5)\n"
"v_mfma_f32_16x16x32_f16 v[48:51], v[144:147], v[88:91], v[48:51]\n"
"ds_read_b128 v[120:123], v25 offset:2496\n"
"v_mfma_f32_16x16x32_f16 v[52:55], v[176:179], v[88:91], v[52:55]\n"
"ds_read_b128 v[124:127], v25 offset:3008\n"
"v_mfma_f32_16x16x32_f16 v[52:55], v[144:147], v[92:95], v[52:55]\n"
"ds_read_b128 v[40:43], v25 offset:3520\n"
"v_mfma_f32_16x16x32_f16 v[48:51], v[148:151], v[96:99], v[48:51]\n"
"ds_read_b128 v[44:47], v25 offset:2432\n"
"s_waitcnt lgkmcnt(7)\n"
"v_mfma_f32_16x16x32_f16 v[52:55], v[180:183], v[96:99], v[52:55]\n"
"ds_read_b128 v[88:91], v25 offset:2944\n"
"v_mfma_f32_16x16x32_f16 v[52:55], v[148:151], v[100:103], v[52:55]\n"
"ds_read_b128 v[92:95], v25 offset:3456\n"
"v_mfma_f32_16x16x32_f16 v[48:51], v[152:155], v[104:107], v[48:51]\n"
"ds_read_b128 v[96:99], v25 offset:2368\n"
"v_mfma_f32_16x16x32_f16 v[52:55], v[184:187], v[104:107], v[52:55]\n"
"ds_read_b128 v[100:103], v25 offset:2880\n"
"s_waitcnt lgkmcnt(8)\n"
"v_mfma_f32_16x16x32_f16 v[52:55], v[152:155], v[108:111], v[52:55]\n"
"ds_read_b128 v[104:107], v25 offset:3392\n"
"v_mfma_f32_16x16x32_f16 v[48:51], v[156:159], v[112:115], v[48:51]\n"
"ds_read_b128 v[108:111], v25 offset:2304\n"
"v_mfma_f32_16x16x32_f16 v[52:55], v[188:191], v[112:115], v[52:55]\n"
"ds_read_b128 v[112:115], v25 offset:2816\n"
"v_mfma_f32_16x16x32_f16 v[52:55], v[156:159], v[116:119], v[52:55]\n"
"ds_read_b128 v[116:119], v25 offset:3328\n"
"s_waitcnt lgkmcnt(8)\n"
"v_mfma_f32_16x16x32_f16 v[56:59], v[192:195], v[120:123], v[56:59]\n"
"ds_read_b128 v[120:123], v25 offset:2240\n"
"v_mfma_f32_16x16x32_f16 v[56:59], v[160:163], v[124:127], v[56:59]\n"
"ds_read_b128 v[124:127], v25 offset:2752\n"
"v_mfma_f32_16x16x32_f16 v[56:59], v[128:131], v[40:43], v[56:59]\n"
"ds_read_b128 v[40:43], v25 offset:3264\n"
"v_mul_f32 v48, v36, v48\n"
"v_mul_f32 v49, v36, v49\n"
"v_mul_f32 v50, v36, v50\n"
"v_mfma_f32_16x16x32_f16 v[56:59], v[196:199], v[44:47], v[56:59]\n"
"ds_read_b128 v[44:47], v25 offset:2176\n"
"v_mul_f32 v51, v36, v51\n"
"v_cvt_pk_bf16_f32 v48, v48, v49\n"
"s_waitcnt lgkmcnt(8)\n"
"v_mfma_f32_16x16x32_f16 v[56:59], v[164:167], v[88:91], v[56:59]\n"
"ds_read_b128 v[88:91], v25 offset:2688\n"
"v_cvt_pk_bf16_f32 v49, v50, v51\n"
"v_pk_min_u16 v48, v48, s36\n"
"v_pk_min_u16 v49, v49, s36\n"
"v_mfma_f32_16x16x32_f16 v[56:59], v[132:135], v[92:95], v[56:59]\n"
"ds_read_b128 v[92:95], v25 offset:3200\n"
"ds_write_b64 v26, v[48:49] offset:0\n"
"v_mfma_f32_16x16x32_f16 v[56:59], v[200:203], v[96:99], v[56:59]\n"
"ds_read_b128 v[96:99], v25 offset:2112\n"
"v_mul_f32 v52, v36, v52\n"
"v_mul_f32 v53, v36, v53\n"
"v_mul_f32 v54, v36, v54\n"
"v_mfma_f32_16x16x32_f16 v[56:59], v[168:171], v[100:103], v[56:59]\n"
"ds_read_b128 v[100:103], v25 offset:2624\n"
"v_mul_f32 v55, v36, v55\n"
"v_cvt_pk_bf16_f32 v52, v52, v53\n"
"s_waitcnt lgkmcnt(9)\n"
"v_mfma_f32_16x16x32_f16 v[56:59], v[136:139], v[104:107], v[56:59]\n"
"ds_read_b128 v[104:107], v25 offset:3136\n"
"v_cvt_pk_bf16_f32 v53, v54, v55\n"
"v_pk_min_u16 v52, v52, s36\n"
"v_pk_min_u16 v53, v53, s36\n"
"v_mfma_f32_16x16x32_f16 v[56:59], v[204:207], v[108:111], v[56:59]\n"
"ds_read_b128 v[108:111], v25 offset:2048\n"
"ds_write_b64 v26, v[52:53] offset:512\n"
"v_mfma_f32_16x16x32_f16 v[56:59], v[172:175], v[112:115], v[56:59]\n"
"ds_read_b128 v[112:115], v25 offset:2560\n"
"ds_write_b32 v4, v4 offset:576\n"
"v_mfma_f32_16x16x32_f16 v[56:59], v[140:143], v[116:119], v[56:59]\n"
"ds_read_b128 v[116:119], v25 offset:3072\n"
"s_waitcnt lgkmcnt(11)\n"
"v_mfma_f32_16x16x32_f16 v[56:59], v[208:211], v[120:123], v[56:59]\n"
"ds_read_b128 v[120:123], v25 offset:2496\n"
"v_mfma_f32_16x16x32_f16 v[56:59], v[176:179], v[124:127], v[56:59]\n"
"ds_read_b128 v[124:127], v25 offset:3008\n"
"v_mfma_f32_16x16x32_f16 v[56:59], v[144:147], v[40:43], v[56:59]\n"
"ds_read_b128 v[40:43], v25 offset:3520\n"
"v_mfma_f32_16x16x32_f16 v[56:59], v[212:215], v[44:47], v[56:59]\n"
"ds_read_b128 v[44:47], v25 offset:4032\n"
"s_waitcnt lgkmcnt(10)\n"
"v_mfma_f32_16x16x32_f16 v[56:59], v[180:183], v[88:91], v[56:59]\n"
"ds_read_b128 v[88:91], v25 offset:2432\n"
"v_mfma_f32_16x16x32_f16 v[56:59], v[148:151], v[92:95], v[56:59]\n"
"ds_read_b128 v[92:95], v25 offset:2944\n"
"v_mfma_f32_16x16x32_f16 v[56:59], v[216:219], v[96:99], v[56:59]\n"
"ds_read_b128 v[96:99], v25 offset:3456\n"
"v_mfma_f32_16x16x32_f16 v[56:59], v[184:187], v[100:103], v[56:59]\n"
"ds_read_b128 v[100:103], v25 offset:3968\n"
"s_waitcnt lgkmcnt(8)\n"
"v_mfma_f32_16x16x32_f16 v[56:59], v[152:155], v[104:107], v[56:59]\n"
"ds_read_b128 v[104:107], v25 offset:2368\n"
"v_mfma_f32_16x16x32_f16 v[56:59], v[220:223], v[108:111], v[56:59]\n"
"ds_read_b128 v[108:111], v25 offset:2880\n"
"v_mfma_f32_16x16x32_f16 v[56:59], v[188:191], v[112:115], v[56:59]\n"
"ds_read_b128 v[112:115], v25 offset:3392\n"
"v_mfma_f32_16x16x32_f16 v[56:59], v[156:159], v[116:119], v[56:59]\n"
"ds_read_b128 v[116:119], v25 offset:3904\n"
"s_waitcnt lgkmcnt(8)\n"
"v_mfma_f32_16x16x32_f16 v[60:63], v[224:227], v[120:123], v[60:63]\n"
"ds_read_b128 v[120:123], v25 offset:2304\n"
"v_mfma_f32_16x16x32_f16 v[60:63], v[192:195], v[124:127], v[60:63]\n"
"ds_read_b128 v[124:127], v25 offset:2816\n"
"v_mfma_f32_16x16x32_f16 v[60:63], v[160:163], v[40:43], v[60:63]\n"
"ds_read_b128 v[40:43], v25 offset:3328\n"
"v_mul_f32 v56, v36, v56\n"
"v_mul_f32 v57, v36, v57\n"
"v_mul_f32 v58, v36, v58\n"
"v_mul_f32 v59, v36, v59\n"
"v_mfma_f32_16x16x32_f16 v[60:63], v[128:131], v[44:47], v[60:63]\n"
"ds_read_b128 v[44:47], v25 offset:3840\n"
"v_cvt_pk_bf16_f32 v56, v56, v57\n"
"v_cvt_pk_bf16_f32 v57, v58, v59\n"
"v_pk_min_u16 v56, v56, s36\n"
"s_waitcnt lgkmcnt(8)\n"
"v_mfma_f32_16x16x32_f16 v[60:63], v[228:231], v[88:91], v[60:63]\n"
"ds_read_b128 v[88:91], v25 offset:2240\n"
"v_pk_min_u16 v57, v57, s36\n"
"ds_write_b64 v26, v[56:57] offset:1024\n"
"v_mfma_f32_16x16x32_f16 v[60:63], v[196:199], v[92:95], v[60:63]\n"
"ds_read_b128 v[92:95], v25 offset:2752\n"
"ds_write_b32 v4, v4 offset:560\n"
"v_mfma_f32_16x16x32_f16 v[60:63], v[164:167], v[96:99], v[60:63]\n"
"ds_read_b128 v[96:99], v25 offset:3264\n"
"v_mfma_f32_16x16x32_f16 v[60:63], v[132:135], v[100:103], v[60:63]\n"
"ds_read_b128 v[100:103], v25 offset:3776\n"
"s_waitcnt lgkmcnt(10)\n"
"v_mfma_f32_16x16x32_f16 v[60:63], v[232:235], v[104:107], v[60:63]\n"
"ds_read_b128 v[104:107], v25 offset:2176\n"
"v_mfma_f32_16x16x32_f16 v[60:63], v[200:203], v[108:111], v[60:63]\n"
"ds_read_b128 v[108:111], v25 offset:2688\n"
"v_mfma_f32_16x16x32_f16 v[60:63], v[168:171], v[112:115], v[60:63]\n"
"ds_read_b128 v[112:115], v25 offset:3200\n"
"v_mfma_f32_16x16x32_f16 v[60:63], v[136:139], v[116:119], v[60:63]\n"
"ds_read_b128 v[116:119], v25 offset:3712\n"
"s_waitcnt lgkmcnt(10)\n"
"v_mfma_f32_16x16x32_f16 v[60:63], v[236:239], v[120:123], v[60:63]\n"
"ds_read_b128 v[120:123], v25 offset:2112\n"
"v_mfma_f32_16x16x32_f16 v[60:63], v[204:207], v[124:127], v[60:63]\n"
"ds_read_b128 v[124:127], v25 offset:2624\n"
"v_mfma_f32_16x16x32_f16 v[60:63], v[172:175], v[40:43], v[60:63]\n"
"ds_read_b128 v[40:43], v25 offset:3136\n"
"v_mfma_f32_16x16x32_f16 v[60:63], v[140:143], v[44:47], v[60:63]\n"
"ds_read_b128 v[44:47], v25 offset:3648\n"
"s_waitcnt lgkmcnt(8)\n"
"v_mfma_f32_16x16x32_f16 v[60:63], v[240:243], v[88:91], v[60:63]\n"
"ds_read_b128 v[88:91], v25 offset:2048\n"
"v_mfma_f32_16x16x32_f16 v[60:63], v[208:211], v[92:95], v[60:63]\n"
"ds_read_b128 v[92:95], v25 offset:2560\n"
"v_mfma_f32_16x16x32_f16 v[60:63], v[176:179], v[96:99], v[60:63]\n"
"ds_read_b128 v[96:99], v25 offset:3072\n"
"v_mfma_f32_16x16x32_f16 v[60:63], v[144:147], v[100:103], v[60:63]\n"
"ds_read_b128 v[100:103], v25 offset:3584\n"
"s_waitcnt lgkmcnt(8)\n"
"v_mfma_f32_16x16x32_f16 v[60:63], v[244:247], v[104:107], v[60:63]\n"
"v_mfma_f32_16x16x32_f16 v[60:63], v[212:215], v[108:111], v[60:63]\n"
"v_mfma_f32_16x16x32_f16 v[60:63], v[180:183], v[112:115], v[60:63]\n"
"v_mfma_f32_16x16x32_f16 v[60:63], v[148:151], v[116:119], v[60:63]\n"
"s_waitcnt lgkmcnt(4)\n"
"v_mfma_f32_16x16x32_f16 v[60:63], v[248:251], v[120:123], v[60:63]\n"
"v_mfma_f32_16x16x32_f16 v[60:63], v[216:219], v[124:127], v[60:63]\n"
"v_mfma_f32_16x16x32_f16 v[60:63], v[184:187], v[40:43], v[60:63]\n"
"v_mfma_f32_16x16x32_f16 v[60:63], v[152:155], v[44:47], v[60:63]\n"
"s_waitcnt lgkmcnt(0)\n"
"v_mfma_f32_16x16x32_f16 v[60:63], v[252:255], v[88:91], v[60:63]\n"
"v_mfma_f32_16x16x32_f16 v[60:63], v[220:223], v[92:95], v[60:63]\n"
"v_mfma_f32_16x16x32_f16 v[60:63], v[188:191], v[96:99], v[60:63]\n"
"v_mfma_f32_16x16x32_f16 v[60:63], v[156:159], v[100:103], v[60:63]\n"
"s_nop 7\n"
"v_mul_f32 v60, v36, v60\n"
"v_mul_f32 v61, v36, v61\n"
"v_mul_f32 v62, v36, v62\n"
"v_mul_f32 v63, v36, v63\n"
"v_cvt_pk_bf16_f32 v60, v60, v61\n"
"v_cvt_pk_bf16_f32 v61, v62, v63\n"
"v_pk_min_u16 v60, v60, s36\n"
"v_pk_min_u16 v61, v61, s36\n"
"ds_write_b64 v26, v[60:61] offset:1536\n"
"ds_write_b32 v4, v4 offset:544\n"
"s_nop 2\n"
".Lnc_574:\n"
"s_branch .L_yend\n"
".Lsl_307:\n"
"s_nop 7\n"
"s_mov_b32 s17, 32768\n"
".Ltp_309:\n"
"buffer_load_dwordx4 v[44:47], v38, s[4:7], s33 offen sc1\n"
"s_sub_u32 s17, s17, 1\n"
"s_cbranch_scc1 .Lou_310\n"
"s_waitcnt vmcnt(0)\n"
"v_xor_b32 v44, s16, v44\n"
"v_xor_b32 v45, s16, v45\n"
"v_xor_b32 v46, s16, v46\n"
"v_xor_b32 v47, s16, v47\n"
"v_or_b32 v44, v44, v45\n"
"v_or3_b32 v44, v44, v46, v47\n"
"v_cmp_ne_u32 vcc, 0, v44\n"
"s_cbranch_vccz .Lou_310\n"
"s_sleep 16\n"
"s_branch .Ltp_309\n"
".Lou_310:\n"
"s_waitcnt vmcnt(0)\n"
"s_branch .Lbk_308\n"
".Lst_319:\n"
"s_nop 7\n"
"s_lshl_b32 s79, s3, 2\n"
"s_add_u32 s79, s79, 0x26800\n"
"v_mov_b32 v36, s79\n"
"s_cmp_lg_u32 s40, 0\n"
"s_cbranch_scc1 .Lhv_322\n"
"s_call_b64 s[60:61], .Lkg_311\n"
"s_setprio 0\n"
".Lhv_322:\n"
"v_and_b32 v79, 15, v0\n"
"v_lshrrev_b32 v78, 4, v0\n"
"v_lshlrev_b32 v77, 3, v78\n"
"v_sub_u32 v76, v77, v79\n"
"v_lshlrev_b32 v75, 4, v78\n"
"v_lshlrev_b32 v74, 8, v78\n"
"v_lshlrev_b32 v73, 3, v79\n"
"v_add_u32 v72, v74, v73\n"
"v_sub_u32 v71, 0x40a, v76\n"
"v_sub_u32 v70, 0x40b, v76\n"
"v_sub_u32 v69, 0x40c, v76\n"
"v_sub_u32 v68, 0x40d, v76\n"
"v_sub_u32 v67, 0x40e, v76\n"
"v_sub_u32 v66, 0x40f, v76\n"
"v_sub_u32 v65, 0x410, v76\n"
"s_mul_i32 s79, s3, 0x2100\n"
"s_add_u32 s79, s79, 0x15100\n"
"s_add_u32 s79, s79, 0x800\n"
"v_add_u32 v64, s79, v72\n"
"s_mul_i32 s79, s3, 0x2100\n"
"s_add_u32 s79, s79, 0x15100\n"
"v_lshl_add_u32 v99, v79, 6, v75\n"
"v_add_u32 v80, s79, v99\n"
"s_mul_i32 s79, s3, 0x2100\n"
"s_add_u32 s79, s79, 0x15100\n"
"s_add_u32 s79, s79, 0x400\n"
"v_lshl_add_u32 v99, v79, 5, v75\n"
"v_lshl_add_u32 v98, v79, 4, v74\n"
"v_cmp_gt_i32 vcc, 2, v78\n"
"s_nop 1\n"
"v_cndmask_b32 v99, v98, v99, vcc\n"
"v_add_u32 v81, s79, v99\n"
"v_mov_b32 v99, 0x40000\n"
"v_cmp_ge_i32 vcc, 32, v0\n"
"s_nop 1\n"
"v_cndmask_b32 v82, v99, v64, vcc\n"
"v_mov_b32 v99, 0x40000\n"
"v_cmp_gt_i32 vcc, 32, v0\n"
"s_nop 1\n"
"v_cndmask_b32 v83, v99, v64, vcc\n"
"s_mul_i32 s79, s3, 0x2100\n"
"s_add_u32 s79, s79, 0x15100\n"
"s_add_u32 s79, s79, 0x7ec\n"
"v_lshlrev_b32 v99, 5, v78\n"
"v_sub_u32 v99, v73, v99\n"
"v_add_u32 v84, s79, v99\n"
"s_mul_i32 s79, s3, 0x2100\n"
"s_add_u32 s79, s79, 0x15100\n"
"s_add_u32 s79, s79, 0x400\n"
"v_sub_u32 v99, 0x411, v76\n"
"v_bfe_u32 v98, v65, 2, 1\n"
"v_add_u32 v99, v99, v98\n"
"v_and_b32 v99, 0xffc, v99\n"
"v_min_i32 v99, 0x400, v99\n"
"v_add_u32 v100, s79, v99\n"
"s_mul_i32 s79, s3, 0x2100\n"
"s_add_u32 s79, s79, 0x15100\n"
"s_add_u32 s79, s79, 0x400\n"
"v_bfe_u32 v99, v66, 2, 1\n"
"v_add_u32 v99, v65, v99\n"
"v_and_b32 v99, 0xffc, v99\n"
"v_min_i32 v99, 0x400, v99\n"
"v_add_u32 v101, s79, v99\n"
"s_mul_i32 s79, s3, 0x2100\n"
"s_add_u32 s79, s79, 0x15100\n"
"s_add_u32 s79, s79, 0x400\n"
"v_bfe_u32 v99, v67, 2, 1\n"
"v_add_u32 v99, v66, v99\n"
"v_and_b32 v99, 0xffc, v99\n"
"v_min_i32 v99, 0x400, v99\n"
"v_add_u32 v102, s79, v99\n"
"s_mul_i32 s79, s3, 0x2100\n"
"s_add_u32 s79, s79, 0x15100\n"
"s_add_u32 s79, s79, 0x400\n"
"v_bfe_u32 v99, v68, 2, 1\n"
"v_add_u32 v99, v67, v99\n"
"v_and_b32 v99, 0xffc, v99\n"
"v_min_i32 v99, 0x400, v99\n"
"v_add_u32 v103, s79, v99\n"
"s_mul_i32 s79, s3, 0x2100\n"
"s_add_u32 s79, s79, 0x15100\n"
"s_add_u32 s79, s79, 0x400\n"
"v_bfe_u32 v99, v69, 2, 1\n"
"v_add_u32 v99, v68, v99\n"
"v_and_b32 v99, 0xffc, v99\n"
"v_min_i32 v99, 0x400, v99\n"
"v_add_u32 v104, s79, v99\n"
"s_mul_i32 s79, s3, 0x2100\n"
"s_add_u32 s79, s79, 0x15100\n"
"s_add_u32 s79, s79, 0x400\n"
"v_bfe_u32 v99, v70, 2, 1\n"
"v_add_u32 v99, v69, v99\n"
"v_and_b32 v99, 0xffc, v99\n"
"v_min_i32 v99, 0x400, v99\n"
"v_add_u32 v105, s79, v99\n"
"s_mul_i32 s79, s3, 0x2100\n"
"s_add_u32 s79, s79, 0x15100\n"
"s_add_u32 s79, s79, 0x400\n"
"v_bfe_u32 v99, v71, 2, 1\n"
"v_add_u32 v99, v70, v99\n"
"v_and_b32 v99, 0xffc, v99\n"
"v_min_i32 v99, 0x400, v99\n"
"v_add_u32 v106, s79, v99\n"
"s_mul_i32 s79, s3, 0x2100\n"
"s_add_u32 s79, s79, 0x15100\n"
"s_add_u32 s79, s79, 0x400\n"
"v_sub_u32 v99, 0x409, v76\n"
"v_bfe_u32 v99, v99, 2, 1\n"
"v_add_u32 v99, v71, v99\n"
"v_and_b32 v99, 0xffc, v99\n"
"v_min_i32 v99, 0x400, v99\n"
"v_add_u32 v107, s79, v99\n"
"v_cmp_eq_u32 vcc, 0, v0\n"
"v_lshlrev_b32 v140, 16, v140\n"
"v_mul_f32 v140, 0.5, v140\n"
"v_cndmask_b32 v140, 0, v140, vcc\n"
"v_add_f32 v128, v128, v140\n"
"v_cndmask_b32 v136, v136, 0, s[56:57]\n"
"v_cndmask_b32 v137, v137, 0, s[58:59]\n"
"v_cndmask_b32 v138, v138, 0, s[58:59]\n"
"v_cndmask_b32 v139, v139, 0, s[58:59]\n"
"v_max3_f32 v85, |v128|, |v129|, 0\n"
"v_max3_f32 v85, |v130|, |v131|, v85\n"
"v_max3_f32 v85, |v132|, |v133|, v85\n"
"v_max3_f32 v85, |v134|, |v135|, v85\n"
"v_max3_f32 v85, |v136|, |v137|, v85\n"
"v_max3_f32 v85, |v138|, |v139|, v85\n"
"s_nop 1\n"
"v_max_u32_dpp v85, v85, v85 row_ror:8 row_mask:0xf bank_mask:0xf\n"
"s_nop 1\n"
"v_max_u32_dpp v85, v85, v85 row_ror:4 row_mask:0xf bank_mask:0xf\n"
"s_nop 1\n"
"v_max_u32_dpp v85, v85, v85 row_ror:2 row_mask:0xf bank_mask:0xf\n"
"s_nop 1\n"
"v_max_u32_dpp v85, v85, v85 row_ror:1 row_mask:0xf bank_mask:0xf\n"
"v_mov_b32 v86, v85\n"
"s_nop 1\n"
"v_permlane16_swap_b32 v85, v86\n"
"v_max_u32 v85, v85, v86\n"
"v_mov_b32 v86, v85\n"
"s_nop 1\n"
"v_permlane32_swap_b32 v85, v86\n"
"v_max_u32 v85, v85, v86\n"
"s_nop 0\n"
"v_readfirstlane_b32 s51, v85\n"
"s_nop 2\n"
"s_bfe_u32 s50, s51, 0x80017\n"
"s_max_i32 s50, s50, 77\n"
"s_min_i32 s50, s50, 227\n"
"s_cmp_gt_i32 s51, 0\n"
"s_cselect_b32 s50, s50, 127\n"
"s_sub_u32 s51, 268, s50\n"
"s_lshl_b32 s51, s51, 23\n"
"s_sub_u32 s50, s50, 13\n"
"s_lshl_b32 s50, s50, 23\n"
"v_mul_f32 v128, s51, v128\n"
"v_mul_f32 v129, s51, v129\n"
"v_mul_f32 v130, s51, v130\n"
"v_mul_f32 v131, s51, v131\n"
"v_mul_f32 v132, s51, v132\n"
"v_mul_f32 v133, s51, v133\n"
"v_mul_f32 v134, s51, v134\n"
"v_mul_f32 v135, s51, v135\n"
"v_mul_f32 v136, s51, v136\n"
"v_mul_f32 v137, s51, v137\n"
"v_mul_f32 v138, s51, v138\n"
"v_mul_f32 v139, s51, v139\n"
"ds_write_b128 v80, v[128:131]\n"
"ds_write_b128 v81, v[132:135]\n"
"ds_write_b64 v82, v[136:137]\n"
"ds_write_b64 v83, v[138:139] offset:128\n"
"v_mov_b32 v87, s50\n"
"ds_write_b32 v36, v87\n"
"ds_read_b32 v100, v100\n"
"ds_read_b32 v101, v101\n"
"ds_read_b32 v102, v102\n"
"ds_read_b32 v103, v103\n"
"ds_read_b32 v104, v104\n"
"ds_read_b32 v105, v105\n"
"ds_read_b32 v106, v106\n"
"ds_read_b32 v107, v107\n"
"ds_read2_b32 v[48:49], v84 offset0:6 offset1:4\n"
"ds_read2_b32 v[50:51], v84 offset0:2 offset1:0\n"
"ds_read2_b32 v[52:53], v84 offset0:7 offset1:5\n"
"ds_read2_b32 v[54:55], v84 offset0:3 offset1:1\n"
"ds_read2_b32 v[56:57], v84 offset0:38 offset1:36\n"
"ds_read2_b32 v[58:59], v84 offset0:34 offset1:32\n"
"ds_read2_b32 v[60:61], v84 offset0:39 offset1:37\n"
"ds_read2_b32 v[62:63], v84 offset0:35 offset1:33\n"
"ds_read2_b32 v[64:65], v84 offset0:70 offset1:68\n"
"ds_read2_b32 v[66:67], v84 offset0:66 offset1:64\n"
"ds_read2_b32 v[68:69], v84 offset0:71 offset1:69\n"
"ds_read2_b32 v[70:71], v84 offset0:67 offset1:65\n"
"ds_read2_b32 v[72:73], v84 offset0:102 offset1:100\n"
"ds_read2_b32 v[74:75], v84 offset0:98 offset1:96\n"
"ds_read2_b32 v[76:77], v84 offset0:103 offset1:101\n"
"ds_read2_b32 v[78:79], v84 offset0:99 offset1:97\n"
"s_waitcnt lgkmcnt(15)\n"
"v_cvt_pk_f16_f32 v152, v100, v101\n"
"v_cvt_pk_f16_f32 v153, v102, v103\n"
"v_cvt_pk_f16_f32 v154, v104, v105\n"
"v_cvt_pk_f16_f32 v155, v106, v107\n"
"v_cvt_pk_f16_f32 v128, v48, v49\n"
"s_waitcnt lgkmcnt(14)\n"
"v_cvt_pk_f16_f32 v129, v50, v51\n"
"s_waitcnt lgkmcnt(13)\n"
"v_cvt_pk_f16_f32 v130, v52, v53\n"
"s_waitcnt lgkmcnt(12)\n"
"v_cvt_pk_f16_f32 v131, v54, v55\n"
"s_waitcnt lgkmcnt(11)\n"
"v_cvt_pk_f16_f32 v132, v56, v57\n"
"s_waitcnt lgkmcnt(10)\n"
"v_cvt_pk_f16_f32 v133, v58, v59\n"
"s_waitcnt lgkmcnt(9)\n"
"v_cvt_pk_f16_f32 v134, v60, v61\n"
"s_waitcnt lgkmcnt(8)\n"
"v_cvt_pk_f16_f32 v135, v62, v63\n"
"s_waitcnt lgkmcnt(7)\n"
"v_cvt_pk_f16_f32 v136, v64, v65\n"
"s_waitcnt lgkmcnt(6)\n"
"v_cvt_pk_f16_f32 v137, v66, v67\n"
"s_waitcnt lgkmcnt(5)\n"
"v_cvt_pk_f16_f32 v138, v68, v69\n"
"s_waitcnt lgkmcnt(4)\n"
"v_cvt_pk_f16_f32 v139, v70, v71\n"
"s_waitcnt lgkmcnt(3)\n"
"v_cvt_pk_f16_f32 v140, v72, v73\n"
"s_waitcnt lgkmcnt(2)\n"
"v_cvt_pk_f16_f32 v141, v74, v75\n"
"s_waitcnt lgkmcnt(1)\n"
"v_cvt_pk_f16_f32 v142, v76, v77\n"
"s_waitcnt lgkmcnt(0)\n"
"v_cvt_pk_f16_f32 v143, v78, v79\n"
"ds_write_b32 v4, v4 offset:192\n"
"s_mov_b32 s43, 1\n"
"s_nop 2\n"
"s_setpc_b64 s[46:47]\n"
".Lst_320:\n"
"s_nop 7\n"
"s_mov_b32 s43, 2\n"
"s_nop 0\n"
"s_setpc_b64 s[46:47]\n"
".Lst_321:\n"
"s_nop 7\n"
"v_and_b32 v79, 15, v0\n"
"v_and_b32 v78, 1, v79\n"
"v_and_b32 v77, 2, v79\n"
"v_lshlrev_b32 v76, 4, v0\n"
"v_lshrrev_b32 v75, 4, v0\n"
"v_xor_b32 v74, s3, v79\n"
"v_and_b32 v73, 1, v74\n"
"v_lshlrev_b32 v72, 1, v73\n"
"v_lshlrev_b32 v71, 1, v75\n"
"v_lshlrev_b32 v70, 3, v75\n"
"v_sub_u32 v69, v70, v79\n"
"v_lshlrev_b32 v68, 6, v75\n"
"v_lshlrev_b32 v67, 4, v77\n"
"v_lshlrev_b32 v66, 5, v79\n"
"v_lshrrev_b32 v65, 1, v75\n"
"v_lshrrev_b32 v64, 1, v79\n"
"v_lshrrev_b32 v63, 2, v79\n"
"v_lshrrev_b32 v62, 3, v79\n"
"v_lshlrev_b32 v61, 7, v62\n"
"v_xor_b32 v60, v63, v65\n"
"s_lshl_b32 s79, s3, 10\n"
"v_add_u32 v30, s79, v76\n"
"v_lshrrev_b32 v99, 2, v30\n"
"v_lshrrev_b32 v98, 6, v30\n"
"v_and_b32 v98, 24, v98\n"
"v_xor_b32 v99, v99, v98\n"
"v_add_u32 v32, 0x20c00, v99\n"
"s_lshl_b32 s79, s3, 8\n"
"s_add_u32 s79, s79, 0x20c00\n"
"v_and_b32 v99, 1, v60\n"
"v_add_u32 v99, v72, v99\n"
"v_lshl_add_u32 v98, v63, 11, v61\n"
"v_add_u32 v98, v98, v68\n"
"v_add_u32 v98, v98, v67\n"
"v_lshl_add_u32 v99, v99, 3, v98\n"
"v_add_u32 v27, s79, v99\n"
"s_mul_i32 s79, s3, 0x2100\n"
"s_add_u32 s79, s79, 0x13840\n"
"v_add_u32 v99, 0x3ef, v69\n"
"v_lshrrev_b32 v99, 2, v99\n"
"v_add_u32 v98, 0x3ef, v69\n"
"v_and_b32 v98, 3, v98\n"
"v_mul_i32_i24 v98, 0x840, v98\n"
"v_lshl_add_u32 v99, v99, 3, v98\n"
"v_add_u32 v24, s79, v99\n"
"s_mul_i32 s79, s3, 0x1200\n"
"s_add_u32 s79, s79, 0x1c420\n"
"v_lshl_add_u32 v99, v75, 4, v66\n"
"v_add_u32 v25, s79, v99\n"
"s_lshl_b32 s79, s3, 11\n"
"s_lshr_b32 s78, s3, 1\n"
"s_lshl_b32 s78, s78, 7\n"
"s_add_u32 s79, s79, s78\n"
"s_add_u32 s79, s79, 0x20c00\n"
"s_and_b32 s78, s3, 1\n"
"v_xor_b32 v99, v75, v63\n"
"v_xor_b32 v99, s78, v99\n"
"v_and_b32 v99, 3, v99\n"
"v_lshl_add_u32 v99, v99, 3, v66\n"
"v_add_u32 v26, s79, v99\n"
"v_xor_b32 v28, 8, v27\n"
"v_lshrrev_b32 v99, 7, v30\n"
"v_and_b32 v98, 0x70, v30\n"
"s_mov_b32 s79, 0x5000\n"
"v_mad_i32_i24 v29, v99, s79, v98\n"
"v_lshrrev_b32 v99, 2, v30\n"
"v_add_u32 v31, 0x1ce00, v99\n"
"v_xor_b32 v33, 8, v32\n"
"v_mov_b32 v99, 0xffff0000\n"
"v_mov_b32 v98, 0xffff\n"
"v_cmp_eq_i32 vcc, 1, v78\n"
"s_nop 1\n"
"v_cndmask_b32 v99, v98, v99, vcc\n"
"v_cmp_eq_i32 vcc, v64, v71\n"
"s_nop 1\n"
"v_cndmask_b32 v34, 0, v99, vcc\n"
"v_mov_b32 v99, 0xffff0000\n"
"v_mov_b32 v98, 0xffff\n"
"v_cmp_eq_i32 vcc, 1, v78\n"
"s_nop 1\n"
"v_cndmask_b32 v99, v98, v99, vcc\n"
"v_add_u32 v98, 1, v71\n"
"v_cmp_eq_i32 vcc, v64, v98\n"
"s_nop 1\n"
"v_cndmask_b32 v35, 0, v99, vcc\n"
"s_lshl_b32 s79, s3, 2\n"
"s_add_u32 s79, s79, 0x26800\n"
"v_mov_b32 v36, s79\n"
"v_lshrrev_b32 v99, 6, v30\n"
"v_and_b32 v98, 48, v30\n"
"v_mul_i32_i24 v98, 0x50, v98\n"
"s_mov_b32 s79, 0x2800\n"
"v_mad_i32_i24 v37, v99, s79, v98\n"
"v_lshrrev_b32 v99, 4, v0\n"
"v_lshlrev_b32 v99, 3, v99\n"
"v_and_b32 v98, 15, v0\n"
"v_sub_u32 v60, v99, v98\n"
"v_add_u32 v99, 15, v60\n"
"v_min_i32 v99, 31, v99\n"
"v_mov_b32 v98, 0x7f00000\n"
"v_lshrrev_b32 v61, v99, v98\n"
"v_add_u32 v99, 16, v60\n"
"v_min_i32 v99, 31, v99\n"
"v_mov_b32 v98, 0x1ff00000\n"
"v_lshrrev_b32 v60, v99, v98\n"
"v_bfe_u32 v99, v61, 1, 1\n"
"v_and_b32 v98, 1, v61\n"
"v_lshl_add_u32 v144, v99, 16, v98\n"
"v_mul_u32_u24 v144, 0x3c00, v144\n"
"v_bfe_u32 v99, v61, 3, 1\n"
"v_bfe_u32 v98, v61, 2, 1\n"
"v_lshl_add_u32 v145, v99, 16, v98\n"
"v_mul_u32_u24 v145, 0x3c00, v145\n"
"v_bfe_u32 v99, v61, 5, 1\n"
"v_bfe_u32 v98, v61, 4, 1\n"
"v_lshl_add_u32 v146, v99, 16, v98\n"
"v_mul_u32_u24 v146, 0x3c00, v146\n"
"v_bfe_u32 v99, v61, 7, 1\n"
"v_bfe_u32 v98, v61, 6, 1\n"
"v_lshl_add_u32 v147, v99, 16, v98\n"
"v_mul_u32_u24 v147, 0x3c00, v147\n"
"v_bfe_u32 v99, v60, 1, 1\n"
"v_and_b32 v98, 1, v60\n"
"v_lshl_add_u32 v148, v99, 16, v98\n"
"v_mul_u32_u24 v148, 0x3c00, v148\n"
"v_bfe_u32 v99, v60, 3, 1\n"
"v_bfe_u32 v98, v60, 2, 1\n"
"v_lshl_add_u32 v149, v99, 16, v98\n"
"v_mul_u32_u24 v149, 0x3c00, v149\n"
"v_bfe_u32 v99, v60, 5, 1\n"
"v_bfe_u32 v98, v60, 4, 1\n"
"v_lshl_add_u32 v150, v99, 16, v98\n"
"v_mul_u32_u24 v150, 0x3c00, v150\n"
"v_bfe_u32 v99, v60, 7, 1\n"
"v_bfe_u32 v98, v60, 6, 1\n"
"v_lshl_add_u32 v151, v99, 16, v98\n"
"v_mul_u32_u24 v151, 0x3c00, v151\n"
"v_lshrrev_b32 v99, 4, v0\n"
"v_lshlrev_b32 v99, 3, v99\n"
"v_and_b32 v98, 15, v0\n"
"v_sub_u32 v99, v99, v98\n"
"v_add_u32 v99, 15, v99\n"
"v_min_i32 v119, 31, v99\n"
"v_mov_b32 v121, 0xf8000000\n"
"v_ashrrev_i32 v121, v119, v121\n"
"v_lshrrev_b32 v99, 4, v0\n"
"v_and_b32 v98, 15, v0\n"
"v_sub_u32 v98, 15, v98\n"
"v_lshl_add_u32 v99, v99, 2, v98\n"
"v_mov_b32 v98, 0xffff\n"
"v_lshrrev_b32 v118, v99, v98\n"
"v_bfe_u32 v99, v121, 1, 1\n"
"v_and_b32 v98, 1, v121\n"
"v_lshl_add_u32 v122, v99, 16, v98\n"
"v_mul_u32_u24 v122, 0xffff, v122\n"
"v_bfe_u32 v99, v121, 3, 1\n"
"v_bfe_u32 v98, v121, 2, 1\n"
"v_lshl_add_u32 v123, v99, 16, v98\n"
"v_mul_u32_u24 v123, 0xffff, v123\n"
"v_bfe_u32 v99, v121, 5, 1\n"
"v_bfe_u32 v98, v121, 4, 1\n"
"v_lshl_add_u32 v124, v99, 16, v98\n"
"v_mul_u32_u24 v124, 0xffff, v124\n"
"v_bfe_u32 v99, v121, 7, 1\n"
"v_bfe_u32 v98, v121, 6, 1\n"
"v_lshl_add_u32 v125, v99, 16, v98\n"
"v_mul_u32_u24 v125, 0xffff, v125\n"
"v_bfe_u32 v99, v118, 1, 1\n"
"v_and_b32 v98, 1, v118\n"
"v_lshl_add_u32 v126, v99, 16, v98\n"
"v_mul_u32_u24 v126, 0xffff, v126\n"
"v_bfe_u32 v99, v118, 3, 1\n"
"v_bfe_u32 v98, v118, 2, 1\n"
"v_lshl_add_u32 v127, v99, 16, v98\n"
"v_mul_u32_u24 v127, 0xffff, v127\n"
"v_and_b32 v152, v122, v152\n"
"v_and_b32 v128, v126, v128\n"
"v_and_b32 v153, v123, v153\n"
"v_and_b32 v129, v127, v129\n"
"v_and_b32 v154, v124, v154\n"
"v_and_b32 v130, v126, v130\n"
"v_and_b32 v155, v125, v155\n"
"v_and_b32 v131, v127, v131\n"
"ds_read_b32 v36, v36\n"
"s_waitcnt lgkmcnt(0)\n"
"v_sub_u32 v76, 0x7f000000, v36\n"
"v_lshrrev_b32 v77, 16, v76\n"
"v_or_b32 v76, v76, v77\n"
"v_and_b32 v34, v76, v34\n"
"v_and_b32 v35, v76, v35\n"
"s_mov_b32 s43, 3\n"
"s_nop 3\n"
"s_setpc_b64 s[46:47]\n"
".Loa_323:\n"
"s_nop 7\n"
"s_mov_b32 s17, 32768\n"
".Ltp_324:\n"
"s_waitcnt vmcnt(0)\n"
"s_sub_u32 s17, s17, 1\n"
"s_cbranch_scc1 .Lou_325\n"
"v_max_u32 v38, v88, v89\n"
"v_max3_u32 v38, v90, v91, v38\n"
"v_max3_u32 v38, v92, v93, v38\n"
"v_max3_u32 v38, v94, v95, v38\n"
"v_max3_u32 v38, v96, v97, v38\n"
"v_max3_u32 v38, v98, v99, v38\n"
"v_max3_u32 v38, v100, v101, v38\n"
"v_max3_u32 v38, v102, v103, v38\n"
"v_max3_u32 v38, v104, v105, v38\n"
"v_max3_u32 v38, v106, v107, v38\n"
"v_max3_u32 v38, v108, v109, v38\n"
"v_max3_u32 v38, v110, v111, v38\n"
"v_max3_u32 v38, v112, v113, v38\n"
"v_max3_u32 v38, v114, v115, v38\n"
"v_max3_u32 v38, v116, v117, v38\n"
"v_max3_u32 v38, v118, v119, v38\n"
"v_max3_u32 v38, v120, v121, v38\n"
"v_max3_u32 v38, v122, v123, v38\n"
"v_max3_u32 v38, v124, v125, v38\n"
"v_max3_u32 v38, v126, v127, v38\n"
"v_cmp_eq_u32 vcc, -1, v38\n"
"s_cbranch_vccz .Lou_325\n"
"s_sleep 1\n"
"v_max_u32 v38, v90, v91\n"
"v_max3_u32 v38, v88, v89, v38\n"
"v_cmp_eq_u32 vcc, -1, v38\n"
"s_cbranch_vccz .Lsk_326\n"
"s_mov_b64 exec, vcc\n"
"buffer_load_dwordx4 v[88:91], v2, s[4:7], s32 offen offset:0 sc1\n"
"s_mov_b64 exec, -1\n"
".Lsk_326:\n"
"v_max_u32 v38, v94, v95\n"
"v_max3_u32 v38, v92, v93, v38\n"
"v_cmp_eq_u32 vcc, -1, v38\n"
"s_cbranch_vccz .Lsk_327\n"
"s_mov_b64 exec, vcc\n"
"buffer_load_dwordx4 v[92:95], v2, s[4:7], s32 offen offset:1024 sc1\n"
"s_mov_b64 exec, -1\n"
".Lsk_327:\n"
"v_max_u32 v38, v98, v99\n"
"v_max3_u32 v38, v96, v97, v38\n"
"v_cmp_eq_u32 vcc, -1, v38\n"
"s_cbranch_vccz .Lsk_328\n"
"s_mov_b64 exec, vcc\n"
"buffer_load_dwordx4 v[96:99], v2, s[4:7], s32 offen offset:2048 sc1\n"
"s_mov_b64 exec, -1\n"
".Lsk_328:\n"
"v_max_u32 v38, v102, v103\n"
"v_max3_u32 v38, v100, v101, v38\n"
"v_cmp_eq_u32 vcc, -1, v38\n"
"s_cbranch_vccz .Lsk_329\n"
"s_mov_b64 exec, vcc\n"
"buffer_load_dwordx4 v[100:103], v2, s[4:7], s32 offen offset:3072 sc1\n"
"s_mov_b64 exec, -1\n"
".Lsk_329:\n"
"v_max_u32 v38, v106, v107\n"
"v_max3_u32 v38, v104, v105, v38\n"
"v_cmp_eq_u32 vcc, -1, v38\n"
"s_cbranch_vccz .Lsk_330\n"
"s_mov_b64 exec, vcc\n"
"buffer_load_dwordx4 v[104:107], v2, s[4:7], s33 offen offset:0 sc1\n"
"s_mov_b64 exec, -1\n"
".Lsk_330:\n"
"v_max_u32 v38, v110, v111\n"
"v_max3_u32 v38, v108, v109, v38\n"
"v_cmp_eq_u32 vcc, -1, v38\n"
"s_cbranch_vccz .Lsk_331\n"
"s_mov_b64 exec, vcc\n"
"buffer_load_dwordx4 v[108:111], v2, s[4:7], s34 offen offset:0 sc1\n"
"s_mov_b64 exec, -1\n"
".Lsk_331:\n"
"v_max_u32 v38, v114, v115\n"
"v_max3_u32 v38, v112, v113, v38\n"
"v_cmp_eq_u32 vcc, -1, v38\n"
"s_cbranch_vccz .Lsk_332\n"
"s_mov_b64 exec, vcc\n"
"buffer_load_dwordx4 v[112:115], v2, s[4:7], s34 offen offset:1024 sc1\n"
"s_mov_b64 exec, -1\n"
".Lsk_332:\n"
"v_max_u32 v38, v118, v119\n"
"v_max3_u32 v38, v116, v117, v38\n"
"v_cmp_eq_u32 vcc, -1, v38\n"
"s_cbranch_vccz .Lsk_333\n"
"s_mov_b64 exec, vcc\n"
"buffer_load_dwordx4 v[116:119], v2, s[4:7], s34 offen offset:2048 sc1\n"
"s_mov_b64 exec, -1\n"
".Lsk_333:\n"
"v_max_u32 v38, v122, v123\n"
"v_max3_u32 v38, v120, v121, v38\n"
"v_cmp_eq_u32 vcc, -1, v38\n"
"s_cbranch_vccz .Lsk_334\n"
"s_mov_b64 exec, vcc\n"
"buffer_load_dwordx4 v[120:123], v2, s[4:7], s34 offen offset:3072 sc1\n"
"s_mov_b64 exec, -1\n"
".Lsk_334:\n"
"v_max_u32 v38, v126, v127\n"
"v_max3_u32 v38, v124, v125, v38\n"
"v_cmp_eq_u32 vcc, -1, v38\n"
"s_cbranch_vccz .Lsk_335\n"
"s_mov_b64 exec, vcc\n"
"buffer_load_dwordx4 v[124:127], v2, s[4:7], s35 offen offset:0 sc1\n"
"s_mov_b64 exec, -1\n"
".Lsk_335:\n"
"s_branch .Ltp_324\n"
".Lou_325:\n"
"s_waitcnt vmcnt(0)\n"
"s_setpc_b64 s[46:47]\n"
".Lsl_354:\n"
"s_nop 7\n"
"s_call_b64 s[46:47], .Loa_323\n"
"s_branch .Lbk_355\n"
".Lsl_356:\n"
"s_nop 7\n"
"s_call_b64 s[46:47], .Loa_323\n"
"s_branch .Lbk_357\n"
".Lsl_358:\n"
"s_nop 7\n"
"s_call_b64 s[46:47], .Loa_323\n"
"s_branch .Lbk_359\n"
".Lsl_360:\n"
"s_nop 7\n"
"s_call_b64 s[46:47], .Loa_323\n"
"s_branch .Lbk_361\n"
".Lsl_362:\n"
"s_nop 7\n"
"s_call_b64 s[46:47], .Loa_323\n"
"s_branch .Lbk_363\n"
".Lsl_364:\n"
"s_nop 7\n"
"s_call_b64 s[46:47], .Loa_323\n"
"s_branch .Lbk_365\n"
".Lsl_366:\n"
"s_nop 7\n"
"s_call_b64 s[46:47], .Loa_323\n"
"s_branch .Lbk_367\n"
".Lsl_368:\n"
"s_nop 7\n"
"s_call_b64 s[46:47], .Loa_323\n"
"s_branch .Lbk_369\n"
".Lsl_370:\n"
"s_nop 7\n"
"s_call_b64 s[46:47], .Loa_323\n"
"s_branch .Lbk_371\n"
".Lsl_372:\n"
"s_nop 7\n"
"s_call_b64 s[46:47], .Loa_323\n"
"s_branch .Lbk_373\n"
".Lsl_397:\n"
"s_nop 7\n"
"s_call_b64 s[46:47], .Loa_323\n"
"s_branch .Lbk_398\n"
".Lsl_399:\n"
"s_nop 7\n"
"s_call_b64 s[46:47], .Loa_323\n"
"s_branch .Lbk_400\n"
".Lsl_401:\n"
"s_nop 7\n"
"s_call_b64 s[46:47], .Loa_323\n"
"s_branch .Lbk_402\n"
".Lsl_403:\n"
"s_nop 7\n"
"s_call_b64 s[46:47], .Loa_323\n"
"s_branch .Lbk_404\n"
".Lsl_405:\n"
"s_nop 7\n"
"s_call_b64 s[46:47], .Loa_323\n"
"s_branch .Lbk_406\n"
".Lsl_407:\n"
"s_nop 7\n"
"s_call_b64 s[46:47], .Loa_323\n"
"s_branch .Lbk_408\n"
".Lsl_409:\n"
"s_nop 7\n"
"s_call_b64 s[46:47], .Loa_323\n"
"s_branch .Lbk_410\n"
".Lsl_411:\n"
"s_nop 7\n"
"s_call_b64 s[46:47], .Loa_323\n"
"s_branch .Lbk_412\n"
".Lsl_413:\n"
"s_nop 7\n"
"s_call_b64 s[46:47], .Loa_323\n"
"s_branch .Lbk_414\n"
".Lsl_415:\n"
"s_nop 7\n"
"s_call_b64 s[46:47], .Loa_323\n"
"s_branch .Lbk_416\n"
".Lsl_497:\n"
"s_nop 7\n"
"s_call_b64 s[46:47], .Loa_323\n"
"s_branch .Lbk_498\n"
".Lsl_499:\n"
"s_nop 7\n"
"s_call_b64 s[46:47], .Loa_323\n"
"s_branch .Lbk_500\n"
".Lsl_501:\n"
"s_nop 7\n"
"s_call_b64 s[46:47], .Loa_323\n"
"s_branch .Lbk_502\n"
".Lsl_503:\n"
"s_nop 7\n"
"s_call_b64 s[46:47], .Loa_323\n"
"s_branch .Lbk_504\n"
".Lsl_505:\n"
"s_nop 7\n"
"s_call_b64 s[46:47], .Loa_323\n"
"s_branch .Lbk_506\n"
".Lsl_507:\n"
"s_nop 7\n"
"s_call_b64 s[46:47], .Loa_323\n"
"s_branch .Lbk_508\n"
".Lsl_509:\n"
"s_nop 7\n"
"s_call_b64 s[46:47], .Loa_323\n"
"s_branch .Lbk_510\n"
".Lsl_511:\n"
"s_nop 7\n"
"s_call_b64 s[46:47], .Loa_323\n"
"s_branch .Lbk_512\n"
".Lsl_513:\n"
"s_nop 7\n"
"s_call_b64 s[46:47], .Loa_323\n"
"s_branch .Lbk_514\n"
".Lsl_515:\n"
"s_nop 7\n"
"s_call_b64 s[46:47], .Loa_323\n"
"s_branch .Lbk_516\n"
".Lsl_543:\n"
"s_nop 7\n"
"s_call_b64 s[46:47], .Loa_323\n"
"s_branch .Lbk_544\n"
".Lsl_545:\n"
"s_nop 7\n"
"s_call_b64 s[46:47], .Loa_323\n"
"s_branch .Lbk_546\n"
".Lsl_547:\n"
"s_nop 7\n"
"s_call_b64 s[46:47], .Loa_323\n"
"s_branch .Lbk_548\n"
".Lsl_549:\n"
"s_nop 7\n"
"s_call_b64 s[46:47], .Loa_323\n"
"s_branch .Lbk_550\n"
".Lsl_551:\n"
"s_nop 7\n"
"s_call_b64 s[46:47], .Loa_323\n"
"s_branch .Lbk_552\n"
".Lsl_553:\n"
"s_nop 7\n"
"s_call_b64 s[46:47], .Loa_323\n"
"s_branch .Lbk_554\n"
".Lsl_555:\n"
"s_nop 7\n"
"s_call_b64 s[46:47], .Loa_323\n"
"s_branch .Lbk_556\n"
".Lsl_557:\n"
"s_nop 7\n"
"s_call_b64 s[46:47], .Loa_323\n"
"s_branch .Lbk_558\n"
".Lsl_559:\n"
"s_nop 7\n"
"s_call_b64 s[46:47], .Loa_323\n"
"s_branch .Lbk_560\n"
".Lsl_561:\n"
"s_nop 7\n"
"s_call_b64 s[46:47], .Loa_323\n"
"s_branch .Lbk_562\n"
".L_yend:\n"
      :: "{s[0:1]}"(__builtin_amdgcn_kernarg_segment_ptr()), "{s2}"((int)__builtin_amdgcn_workgroup_id_x()),
         "{s3}"(__builtin_amdgcn_readfirstlane(tid >> 6)), "{v0}"(tid),
         "{v1}"((unsigned)(size_t)(__attribute__((address_space(3))) unsigned char*)smem)
      : "memory", "vcc", "scc", "m0", "v255", "s95");
}

extern "C" void kernel_launch(void* const* d_in, const int* in_sizes, int n_in,
                              void* d_out, int out_size, void* d_ws, size_t ws_size,
                              hipStream_t stream) {
  mega_kernel<<<256, 512, 0, stream>>>((const u16*)d_in[0], (const u16*)d_in[1], (const u16*)d_in[2],
                                       (const u16*)d_in[4], (const u16*)d_in[5], (const u16*)d_in[6],
                                       (const u16*)d_in[7], (const u16*)d_in[8], (const u16*)d_in[9],
                                       (u16*)d_out, (unsigned char*)d_ws);
}
```

```cpp
#include <hip/hip_runtime.h>
typedef unsigned short u16;

__global__ __launch_bounds__(512, 2) void mega_kernel(
    const u16* __restrict__ x_, const u16* __restrict__ wqkv_, const u16* __restrict__ wo_,
    const u16* __restrict__ log_A_real_, const u16* __restrict__ A_imag_,
    const u16* __restrict__ C_real_, const u16* __restrict__ C_imag_, const u16* __restrict__ log_dt_,
    const u16* __restrict__ D_skip, u16* __restrict__ out, unsigned char* __restrict__ ws_) {
  __shared__ __attribute__((aligned(16))) unsigned char smem[163840];
  const int tid = (int)__builtin_amdgcn_workitem_id_x();
  asm volatile(
"s_cmp_lt_u32 s3, 4\n"
"s_cbranch_scc1 .Lxs_1\n"
"s_load_dwordx8 s[8:15], s[0:1], 0x18\n"
"s_load_dwordx4 s[16:19], s[0:1], 0x38\n"
"s_load_dwordx2 s[22:23], s[0:1], 0x0\n"
"s_load_dwordx4 s[24:27], s[0:1], 0x48\n"
"s_getpc_b64 s[4:5]\n"
".Lmk_a:\n"
"v_lshrrev_b32 v79, 6, v0\n"
"v_and_b32 v78, 7, v0\n"
"v_lshl_add_u32 v79, v79, 3, v78\n"
"s_lshr_b32 s79, s2, 3\n"
"s_lshl_b32 s79, s79, 7\n"
"s_sub_u32 s79, s79, 0x20000\n"
"v_lshl_add_u32 v2, v79, 12, s79\n"
"v_min_u32 v2, .L_yend-.Lmk_a-4, v2\n"
"s_mov_b32 m0, 163328\n"
"v_mov_b32 v4, 0\n"
"global_load_lds_dword v2, s[4:5]\n"
"v_lshlrev_b32 v79, 2, v0\n"
"v_add_u32 v2, 0x26000, v79\n"
"ds_write_b32 v2, v4\n"
"v_lshlrev_b32 v79, 4, v0\n"
"v_add_u32 v2, 0x25840, v79\n"
"v_mov_b32 v8, -1\n"
"v_mov_b32 v9, -1\n"
"v_mov_b32 v10, -1\n"
"v_mov_b32 v11, -1\n"
"ds_write_b128 v2, v[8:11]\n"
"ds_write_b128 v2, v[8:11] offset:4096\n"
"s_and_b32 s79, s2, 7\n"
"s_lshl_b32 s79, s79, 4\n"
"s_lshr_b32 s78, s2, 3\n"
"s_add_u32 s79, s79, s78\n"
"s_and_b32 s78, s2, 3\n"
"s_lshl_b32 s78, s78, 3\n"
"s_and_b32 s77, s2, 4\n"
"s_cmp_eq_i32 s77, 4\n"
"s_cselect_b32 s78, 32, s78\n"
"s_add_u32 s79, s79, s78\n"
"s_lshl_b32 s79, s79, 2\n"
"s_and_b32 s78, s3, 3\n"
"s_add_u32 s6, s79, s78\n"
"v_and_b32 v79, 63, v0\n"
"s_mul_i32 s79, s6, 0x180\n"
"v_lshl_add_u32 v2, v79, 1, s79\n"
"s_lshl_b32 s79, s6, 1\n"
"s_and_b32 s20, s79, 0xffc\n"
"s_and_b32 s79, s2, 4\n"
"s_lshl_b32 s79, s79, 4\n"
"s_add_u32 s7, s2, s79\n"
"s_waitcnt lgkmcnt(0)\n"
"s_barrier\n"
"s_cmp_ge_u32 s7, 192\n"
"s_cbranch_scc1 .Lnl_2\n"
"s_load_dword s40, s[16:17], s20\n"
"global_load_ushort v32, v2, s[8:9] offset:0\n"
"global_load_ushort v33, v2, s[10:11] offset:0\n"
"global_load_ushort v34, v2, s[12:13] offset:0\n"
"global_load_ushort v35, v2, s[14:15] offset:0\n"
"global_load_ushort v40, v2, s[8:9] offset:128\n"
"global_load_ushort v41, v2, s[10:11] offset:128\n"
"global_load_ushort v42, v2, s[12:13] offset:128\n"
"global_load_ushort v43, v2, s[14:15] offset:128\n"
"global_load_ushort v44, v2, s[8:9] offset:256\n"
"global_load_ushort v45, v2, s[10:11] offset:256\n"
"global_load_ushort v46, v2, s[12:13] offset:256\n"
"global_load_ushort v47, v2, s[14:15] offset:256\n"
"s_load_dword s41, s[18:19], s20\n"
".Lnl_2:\n"
"s_load_dword s16, s[26:27], 0x0\n"
"s_branch .L_xend\n"
".Lxs_1:\n"
"s_load_dwordx4 s[20:23], s[0:1], 0x0\n"
"s_load_dwordx2 s[56:57], s[0:1], 0x10\n"
"s_load_dwordx4 s[24:27], s[0:1], 0x48\n"
"v_and_b32 v0, 63, v0\n"
"s_barrier\n"
"s_lshl_b32 s79, s2, 2\n"
"s_add_u32 s31, s79, s3\n"
"s_mul_i32 s79, s31, 0xcd\n"
"s_lshr_b32 s46, s79, 11\n"
"s_mul_i32 s79, s46, 10\n"
"s_sub_u32 s47, s31, s79\n"
"s_lshl_b32 s79, s47, 6\n"
"v_add_u32 v68, s79, v0\n"
"v_lshlrev_b32 v80, 1, v68\n"
"s_mul_i32 s48, s46, 0x7800\n"
"s_mov_b32 s14, 0x258000\n"
"s_mov_b32 s15, 0x20000\n"
"s_waitcnt lgkmcnt(0)\n"
"s_mov_b64 s[12:13], s[22:23]\n"
"s_cmp_ge_u32 s2, 200\n"
"s_cbranch_scc1 .Lnorep_3\n"
"buffer_load_ushort v28, v80, s[12:15], s48 offen offset:0\n"
"buffer_load_ushort v36, v80, s[12:15], s48 offen offset:1280\n"
"buffer_load_ushort v44, v80, s[12:15], s48 offen offset:2560\n"
"s_add_u32 s48, s48, 3840\n"
"buffer_load_ushort v29, v80, s[12:15], s48 offen offset:0\n"
"buffer_load_ushort v37, v80, s[12:15], s48 offen offset:1280\n"
"buffer_load_ushort v45, v80, s[12:15], s48 offen offset:2560\n"
"s_add_u32 s48, s48, 3840\n"
"buffer_load_ushort v30, v80, s[12:15], s48 offen offset:0\n"
"buffer_load_ushort v38, v80, s[12:15], s48 offen offset:1280\n"
"buffer_load_ushort v46, v80, s[12:15], s48 offen offset:2560\n"
"s_add_u32 s48, s48, 3840\n"
"buffer_load_ushort v31, v80, s[12:15], s48 offen offset:0\n"
"buffer_load_ushort v39, v80, s[12:15], s48 offen offset:1280\n"
"buffer_load_ushort v47, v80, s[12:15], s48 offen offset:2560\n"
"s_add_u32 s48, s48, 3840\n"
"buffer_load_ushort v32, v80, s[12:15], s48 offen offset:0\n"
"buffer_load_ushort v40, v80, s[12:15], s48 offen offset:1280\n"
"buffer_load_ushort v48, v80, s[12:15], s48 offen offset:2560\n"
"s_add_u32 s48, s48, 3840\n"
"buffer_load_ushort v33, v80, s[12:15], s48 offen offset:0\n"
"buffer_load_ushort v41, v80, s[12:15], s48 offen offset:1280\n"
"buffer_load_ushort v49, v80, s[12:15], s48 offen offset:2560\n"
"s_add_u32 s48, s48, 3840\n"
"buffer_load_ushort v34, v80, s[12:15], s48 offen offset:0\n"
"buffer_load_ushort v42, v80, s[12:15], s48 offen offset:1280\n"
"buffer_load_ushort v50, v80, s[12:15], s48 offen offset:2560\n"
"s_add_u32 s48, s48, 3840\n"
"buffer_load_ushort v35, v80, s[12:15], s48 offen offset:0\n"
"buffer_load_ushort v43, v80, s[12:15], s48 offen offset:1280\n"
"buffer_load_ushort v51, v80, s[12:15], s48 offen offset:2560\n"
"s_branch .Lrep_4\n"
".Lnorep_3:\n"
"s_sleep 20\n"
"s_mov_b64 s[12:13], s[56:57]\n"
"s_cmp_lt_i32 s31, 0x3e8\n"
"s_cselect_b32 s14, 0xc8000, 0\n"
"v_lshlrev_b32 v80, 1, v0\n"
"s_lshl_b32 s79, s31, 2\n"
"s_add_u32 s79, s79, 0xfffff380\n"
"s_mul_i32 s79, s79, 0xcd\n"
"s_lshr_b32 s80, s79, 11\n"
"s_lshl_b32 s79, s31, 2\n"
"s_add_u32 s79, s79, 0xfffff380\n"
"s_mul_i32 s78, s80, 10\n"
"s_sub_u32 s84, s79, s78\n"
"s_mul_i32 s79, s80, 0x67\n"
"s_lshr_b32 s79, s79, 9\n"
"s_mul_i32 s79, s79, 20\n"
"s_lshl_b32 s78, s80, 2\n"
"s_add_u32 s79, s79, s78\n"
"s_mul_i32 s79, s79, 0x500\n"
"s_lshl_b32 s78, s84, 7\n"
"s_add_u32 s48, s79, s78\n"
"s_add_u32 s49, s48, 0x6400\n"
"buffer_load_ushort v100, v80, s[12:15], s48 offen offset:0\n"
"buffer_load_ushort v101, v80, s[12:15], s48 offen offset:1280\n"
"buffer_load_ushort v102, v80, s[12:15], s48 offen offset:2560\n"
"buffer_load_ushort v103, v80, s[12:15], s48 offen offset:3840\n"
"buffer_load_ushort v104, v80, s[12:15], s49 offen offset:0\n"
"buffer_load_ushort v105, v80, s[12:15], s49 offen offset:1280\n"
"buffer_load_ushort v106, v80, s[12:15], s49 offen offset:2560\n"
"buffer_load_ushort v107, v80, s[12:15], s49 offen offset:3840\n"
"s_lshl_b32 s79, s31, 2\n"
"s_add_u32 s79, s79, 0xfffff381\n"
"s_mul_i32 s79, s79, 0xcd\n"
"s_lshr_b32 s81, s79, 11\n"
"s_lshl_b32 s79, s31, 2\n"
"s_add_u32 s79, s79, 0xfffff381\n"
"s_mul_i32 s78, s81, 10\n"
"s_sub_u32 s85, s79, s78\n"
"s_mul_i32 s79, s81, 0x67\n"
"s_lshr_b32 s79, s79, 9\n"
"s_mul_i32 s79, s79, 20\n"
"s_lshl_b32 s78, s81, 2\n"
"s_add_u32 s79, s79, s78\n"
"s_mul_i32 s79, s79, 0x500\n"
"s_lshl_b32 s78, s85, 7\n"
"s_add_u32 s48, s79, s78\n"
"s_add_u32 s49, s48, 0x6400\n"
"buffer_load_ushort v108, v80, s[12:15], s48 offen offset:0\n"
"buffer_load_ushort v109, v80, s[12:15], s48 offen offset:1280\n"
"buffer_load_ushort v110, v80, s[12:15], s48 offen offset:2560\n"
"buffer_load_ushort v111, v80, s[12:15], s48 offen offset:3840\n"
"buffer_load_ushort v112, v80, s[12:15], s49 offen offset:0\n"
"buffer_load_ushort v113, v80, s[12:15], s49 offen offset:1280\n"
"buffer_load_ushort v114, v80, s[12:15], s49 offen offset:2560\n"
"buffer_load_ushort v115, v80, s[12:15], s49 offen offset:3840\n"
"s_lshl_b32 s79, s31, 2\n"
"s_add_u32 s79, s79, 0xfffff382\n"
"s_mul_i32 s79, s79, 0xcd\n"
"s_lshr_b32 s82, s79, 11\n"
"s_lshl_b32 s79, s31, 2\n"
"s_add_u32 s79, s79, 0xfffff382\n"
"s_mul_i32 s78, s82, 10\n"
"s_sub_u32 s86, s79, s78\n"
"s_mul_i32 s79, s82, 0x67\n"
"s_lshr_b32 s79, s79, 9\n"
"s_mul_i32 s79, s79, 20\n"
"s_lshl_b32 s78, s82, 2\n"
"s_add_u32 s79, s79, s78\n"
"s_mul_i32 s79, s79, 0x500\n"
"s_lshl_b32 s78, s86, 7\n"
"s_add_u32 s48, s79, s78\n"
"s_add_u32 s49, s48, 0x6400\n"
"buffer_load_ushort v116, v80, s[12:15], s48 offen offset:0\n"
"buffer_load_ushort v117, v80, s[12:15], s48 offen offset:1280\n"
"buffer_load_ushort v118, v80, s[12:15], s48 offen offset:2560\n"
"buffer_load_ushort v119, v80, s[12:15], s48 offen offset:3840\n"
"buffer_load_ushort v120, v80, s[12:15], s49 offen offset:0\n"
"buffer_load_ushort v121, v80, s[12:15], s49 offen offset:1280\n"
"buffer_load_ushort v122, v80, s[12:15], s49 offen offset:2560\n"
"buffer_load_ushort v123, v80, s[12:15], s49 offen offset:3840\n"
"s_lshl_b32 s79, s31, 2\n"
"s_add_u32 s79, s79, 0xfffff383\n"
"s_mul_i32 s79, s79, 0xcd\n"
"s_lshr_b32 s83, s79, 11\n"
"s_lshl_b32 s79, s31, 2\n"
"s_add_u32 s79, s79, 0xfffff383\n"
"s_mul_i32 s78, s83, 10\n"
"s_sub_u32 s87, s79, s78\n"
"s_mul_i32 s79, s83, 0x67\n"
"s_lshr_b32 s79, s79, 9\n"
"s_mul_i32 s79, s79, 20\n"
"s_lshl_b32 s78, s83, 2\n"
"s_add_u32 s79, s79, s78\n"
"s_mul_i32 s79, s79, 0x500\n"
"s_lshl_b32 s78, s87, 7\n"
"s_add_u32 s48, s79, s78\n"
"s_add_u32 s49, s48, 0x6400\n"
"buffer_load_ushort v124, v80, s[12:15], s48 offen offset:0\n"
"buffer_load_ushort v125, v80, s[12:15], s48 offen offset:1280\n"
"buffer_load_ushort v126, v80, s[12:15], s48 offen offset:2560\n"
"buffer_load_ushort v127, v80, s[12:15], s48 offen offset:3840\n"
"buffer_load_ushort v128, v80, s[12:15], s49 offen offset:0\n"
"buffer_load_ushort v129, v80, s[12:15], s49 offen offset:1280\n"
"buffer_load_ushort v130, v80, s[12:15], s49 offen offset:2560\n"
"buffer_load_ushort v131, v80, s[12:15], s49 offen offset:3840\n"
".Lrep_4:\n"
"s_mov_b32 s4, s26\n"
"s_mov_b32 s5, s27\n"
"s_mov_b32 s6, 0x1688000\n"
"s_mov_b32 s7, 0x20000\n"
"s_load_dword s16, s[26:27], 0x0\n"
"v_lshlrev_b32 v1, 4, v0\n"
"s_and_b32 s79, s2, 7\n"
"s_lshl_b32 s79, s79, 4\n"
"s_lshr_b32 s78, s2, 3\n"
"s_add_u32 s79, s79, s78\n"
"s_and_b32 s78, s2, 3\n"
"s_lshl_b32 s78, s78, 3\n"
"s_and_b32 s77, s2, 4\n"
"s_cmp_eq_i32 s77, 4\n"
"s_cselect_b32 s78, 32, s78\n"
"s_add_u32 s79, s79, s78\n"
"s_and_b32 s78, s2, 4\n"
"s_lshl_b32 s78, s78, 4\n"
"s_add_u32 s78, s2, s78\n"
"s_cmp_lt_i32 s78, 0xc0\n"
"s_cselect_b32 s45, s79, -1\n"
"s_mov_b32 s8, s20\n"
"s_mov_b32 s9, s21\n"
"s_mov_b32 s10, 0x280000\n"
"s_mov_b32 s11, 0x20000\n"
"s_mov_b32 s12, s24\n"
"s_mov_b32 s13, s25\n"
"s_mov_b32 s14, 0x280000\n"
"s_mov_b32 s15, 0x20000\n"
"s_and_b32 s79, s2, 1\n"
"s_lshl_b32 s79, s79, 3\n"
"s_lshr_b32 s78, s2, 3\n"
"s_and_b32 s78, s78, 7\n"
"s_add_u32 s18, s79, s78\n"
"s_lshr_b32 s79, s2, 1\n"
"s_and_b32 s79, s79, 3\n"
"s_lshl_b32 s79, s79, 2\n"
"s_lshr_b32 s78, s2, 3\n"
"s_lshr_b32 s78, s78, 3\n"
"s_add_u32 s19, s79, s78\n"
"s_mul_i32 s79, s19, 0\n"
"s_add_u32 s79, s3, s79\n"
"s_and_b32 s43, s79, 3\n"
"v_mul_i32_i24 v79, 0xcd, v68\n"
"v_lshrrev_b32 v84, 13, v79\n"
"v_mul_i32_i24 v79, 40, v84\n"
"v_sub_u32 v85, v68, v79\n"
"v_mul_i32_i24 v79, 0xa0, v84\n"
"v_lshlrev_b32 v84, 10, v79\n"
"v_subrev_u32 v79, 24, v85\n"
"v_and_b32 v78, 15, v85\n"
"v_cmp_le_i32 vcc, 32, v85\n"
"s_nop 1\n"
"v_cndmask_b32 v79, v78, v79, vcc\n"
"v_lshrrev_b32 v78, 4, v85\n"
"s_mov_b32 s79, 0xa000\n"
"v_mad_i32_i24 v78, v78, s79, v84\n"
"v_lshl_add_u32 v64, v79, 4, v78\n"
"v_mov_b32 v86, v85\n"
"v_and_b32 v79, 15, v86\n"
"v_lshrrev_b32 v78, 4, v86\n"
"v_min_i32 v78, 3, v78\n"
"s_mov_b32 s79, 0xa000\n"
"v_mad_i32_i24 v78, v78, s79, v84\n"
"v_mov_b32 v77, 0\n"
"v_mov_b32 v76, 0x400\n"
"v_lshrrev_b32 v75, 4, v86\n"
"v_cmp_eq_i32 vcc, 3, v75\n"
"s_nop 1\n"
"v_cndmask_b32 v77, v76, v77, vcc\n"
"v_add_u32 v78, v78, v77\n"
"v_lshl_add_u32 v65, v79, 4, v78\n"
"v_add_u32 v86, 40, v85\n"
"v_and_b32 v79, 15, v86\n"
"v_lshrrev_b32 v78, 4, v86\n"
"v_min_i32 v78, 3, v78\n"
"s_mov_b32 s79, 0xa000\n"
"v_mad_i32_i24 v78, v78, s79, v84\n"
"v_mov_b32 v77, 0\n"
"v_mov_b32 v76, 0x400\n"
"v_lshrrev_b32 v75, 4, v86\n"
"v_cmp_eq_i32 vcc, 3, v75\n"
"s_nop 1\n"
"v_cndmask_b32 v77, v76, v77, vcc\n"
"v_add_u32 v78, v78, v77\n"
"v_lshl_add_u32 v66, v79, 4, v78\n"
"v_subrev_u32 v79, 0x80, v64\n"
"v_mov_b32 v78, 0x40000000\n"
"v_cmp_le_i32 vcc, 32, v85\n"
"s_nop 1\n"
"v_cndmask_b32 v67, v78, v79, vcc\n"
"s_lshr_b32 s79, s46, 2\n"
"s_lshl_b32 s79, s79, 11\n"
"s_and_b32 s78, s46, 3\n"
"s_lshl_b32 s78, s78, 8\n"
"s_add_u32 s79, s79, s78\n"
"s_add_u32 s48, s79, 0x100000\n"
"s_lshl_b32 s79, s47, 10\n"
"s_lshr_b32 s78, s46, 2\n"
"s_lshl_b32 s78, s78, 5\n"
"s_add_u32 s79, s79, s78\n"
"s_and_b32 s78, s46, 3\n"
"s_lshl_b32 s78, s78, 2\n"
"s_add_u32 s79, s79, s78\n"
"s_lshl_b32 s78, s2, 5\n"
"s_lshl_b32 s77, s3, 2\n"
"s_add_u32 s78, s78, s77\n"
"s_add_u32 s78, s78, 0xf00\n"
"s_cmp_lt_i32 s31, 0x320\n"
"s_cselect_b32 s79, s79, s78\n"
"s_add_u32 s32, s79, 0x1000\n"
"v_mov_b32 v20, 0x3f803f80\n"
"v_mov_b32 v21, 0x3f803f80\n"
"v_mov_b32 v22, 0x3f803f80\n"
"v_mov_b32 v23, 0x3f803f80\n"
"s_mov_b32 s79, 0x55\n"
"s_cmp_lt_i32 s47, 5\n"
"s_cselect_b32 s79, s79, 0xaa\n"
"s_cmp_lt_i32 s31, 0x320\n"
"s_cselect_b32 s58, s79, 0xff\n"
"s_mov_b32 s59, 0\n"
"s_mul_i32 s79, s19, 0x14000\n"
"s_mul_i32 s78, s43, 0x140\n"
"s_add_u32 s28, s79, s78\n"
"s_mul_i32 s29, s43, 0x1400\n"
"s_lshr_b32 s79, s2, 3\n"
"s_and_b32 s31, s79, 7\n"
"s_mul_i32 s33, s43, 0x140\n"
"s_sub_u32 s79, s31, 5\n"
"s_cmp_ge_i32 s31, 5\n"
"s_cselect_b32 s31, s79, s31\n"
"s_mul_i32 s79, s31, 0x5000\n"
"s_add_u32 s79, s79, s28\n"
"s_sub_u32 s79, s79, 0x5000\n"
"s_max_i32 s60, s79, s33\n"
"s_mul_i32 s79, s31, 0x5000\n"
"s_mov_b32 s78, 0x12400\n"
"s_cmp_ge_i32 s31, 2\n"
"s_cselect_b32 s78, s78, 0xa000\n"
"s_add_u32 s79, s79, s78\n"
"s_cmp_eq_i32 s31, 4\n"
"s_cselect_b32 s79, 0, s79\n"
"s_add_u32 s61, s29, s79\n"
"s_sub_u32 s79, s31, 4\n"
"s_add_u32 s78, s31, 1\n"
"s_cmp_ge_i32 s31, 4\n"
"s_cselect_b32 s31, s79, s78\n"
"s_mul_i32 s79, s31, 0x5000\n"
"s_add_u32 s79, s79, s28\n"
"s_sub_u32 s79, s79, 0x5000\n"
"s_max_i32 s62, s79, s33\n"
"s_mul_i32 s79, s31, 0x5000\n"
"s_mov_b32 s78, 0x12400\n"
"s_cmp_ge_i32 s31, 2\n"
"s_cselect_b32 s78, s78, 0xa000\n"
"s_add_u32 s79, s79, s78\n"
"s_cmp_eq_i32 s31, 4\n"
"s_cselect_b32 s79, 0, s79\n"
"s_add_u32 s63, s29, s79\n"
"s_sub_u32 s79, s31, 4\n"
"s_add_u32 s78, s31, 1\n"
"s_cmp_ge_i32 s31, 4\n"
"s_cselect_b32 s31, s79, s78\n"
"s_mul_i32 s79, s31, 0x5000\n"
"s_add_u32 s79, s79, s28\n"
"s_sub_u32 s79, s79, 0x5000\n"
"s_max_i32 s64, s79, s33\n"
"s_mul_i32 s79, s31, 0x5000\n"
"s_mov_b32 s78, 0x12400\n"
"s_cmp_ge_i32 s31, 2\n"
"s_cselect_b32 s78, s78, 0xa000\n"
"s_add_u32 s79, s79, s78\n"
"s_cmp_eq_i32 s31, 4\n"
"s_cselect_b32 s79, 0, s79\n"
"s_add_u32 s65, s29, s79\n"
"s_sub_u32 s79, s31, 4\n"
"s_add_u32 s78, s31, 1\n"
"s_cmp_ge_i32 s31, 4\n"
"s_cselect_b32 s31, s79, s78\n"
"s_mul_i32 s79, s31, 0x5000\n"
"s_add_u32 s79, s79, s28\n"
"s_sub_u32 s79, s79, 0x5000\n"
"s_max_i32 s66, s79, s33\n"
"s_mul_i32 s79, s31, 0x5000\n"
"s_mov_b32 s78, 0x12400\n"
"s_cmp_ge_i32 s31, 2\n"
"s_cselect_b32 s78, s78, 0xa000\n"
"s_add_u32 s79, s79, s78\n"
"s_cmp_eq_i32 s31, 4\n"
"s_cselect_b32 s79, 0, s79\n"
"s_add_u32 s67, s29, s79\n"
"s_sub_u32 s79, s31, 4\n"
"s_add_u32 s78, s31, 1\n"
"s_cmp_ge_i32 s31, 4\n"
"s_cselect_b32 s31, s79, s78\n"
"s_mul_i32 s79, s31, 0x5000\n"
"s_add_u32 s79, s79, s28\n"
"s_sub_u32 s79, s79, 0x5000\n"
"s_max_i32 s68, s79, s33\n"
"s_mul_i32 s79, s31, 0x5000\n"
"s_mov_b32 s78, 0x12400\n"
"s_cmp_ge_i32 s31, 2\n"
"s_cselect_b32 s78, s78, 0xa000\n"
"s_add_u32 s79, s79, s78\n"
"s_cmp_eq_i32 s31, 4\n"
"s_cselect_b32 s79, 0, s79\n"
"s_add_u32 s69, s29, s79\n"
"v_and_b32 v79, 3, v0\n"
"v_lshrrev_b32 v78, 5, v0\n"
"v_lshlrev_b32 v78, 1, v78\n"
"v_xor_b32 v79, v79, v78\n"
"v_lshrrev_b32 v78, 2, v0\n"
"v_mul_i32_i24 v78, 0x500, v78\n"
"v_lshl_add_u32 v2, v79, 4, v78\n"
"v_min_i32 v79, 24, v0\n"
"s_lshr_b32 s79, s2, 3\n"
"s_and_b32 s79, s79, 7\n"
"s_lshl_b32 s79, s79, 2\n"
"s_add_u32 s79, s79, s3\n"
"s_mul_i32 s79, s79, 0xc80\n"
"s_lshl_b32 s78, s19, 6\n"
"s_add_u32 s78, s78, 0x3f0\n"
"s_mul_i32 s78, s78, 0x500\n"
"s_add_u32 s79, s79, s78\n"
"v_lshl_add_u32 v89, v79, 7, s79\n"
"s_mov_b32 s31, 0x05040100\n"
"s_cmp_ge_u32 s2, 200\n"
"s_cbranch_scc1 .Lnorep_5\n"
"buffer_store_dwordx4 v[20:23], v67, s[4:7], s48 offen sc1\n"
"s_waitcnt vmcnt(1)\n"
"v_perm_b32 v52, v29, v28, s31\n"
"v_perm_b32 v53, v31, v30, s31\n"
"v_perm_b32 v54, v33, v32, s31\n"
"v_perm_b32 v55, v35, v34, s31\n"
"buffer_store_dwordx4 v[52:55], v64, s[4:7], s48 offen sc1\n"
"v_perm_b32 v56, v37, v36, s31\n"
"v_perm_b32 v57, v39, v38, s31\n"
"v_perm_b32 v58, v41, v40, s31\n"
"v_perm_b32 v59, v43, v42, s31\n"
"buffer_store_dwordx4 v[56:59], v65, s[4:7], s48 offen sc1\n"
"v_perm_b32 v60, v45, v44, s31\n"
"v_perm_b32 v61, v47, v46, s31\n"
"v_perm_b32 v62, v49, v48, s31\n"
"v_perm_b32 v63, v51, v50, s31\n"
"buffer_store_dwordx4 v[60:63], v66, s[4:7], s48 offen sc1\n"
"s_branch .Lrep_6\n"
".Lnorep_5:\n"
"s_lshl_b32 s79, s84, 6\n"
"v_add_u32 v68, s79, v0\n"
"v_mul_i32_i24 v79, 0xcd, v68\n"
"v_lshrrev_b32 v81, 14, v79\n"
"s_lshr_b32 s79, s80, 2\n"
"s_mul_i32 s79, s79, 13\n"
"s_lshr_b32 s79, s79, 6\n"
"s_and_b32 s79, s79, 1\n"
"s_lshl_b32 s79, s79, 1\n"
"v_mul_i32_i24 v79, 0x50, v81\n"
"v_sub_u32 v79, v68, v79\n"
"v_lshrrev_b32 v79, 4, v79\n"
"v_add_u32 v82, s79, v79\n"
"v_subrev_u32 v79, 5, v82\n"
"v_cmp_le_i32 vcc, 5, v82\n"
"s_nop 1\n"
"v_cndmask_b32 v79, v82, v79, vcc\n"
"s_mov_b32 s79, 0x64\n"
"v_mad_i32_i24 v79, v81, s79, v79\n"
"v_and_b32 v78, 15, v68\n"
"v_lshlrev_b32 v78, 4, v78\n"
"v_lshl_add_u32 v64, v79, 10, v78\n"
"s_lshr_b32 s79, s80, 2\n"
"s_mul_i32 s79, s79, 0x1400\n"
"s_and_b32 s78, s80, 3\n"
"s_lshl_b32 s78, s78, 8\n"
"s_add_u32 s79, s79, s78\n"
"s_add_u32 s79, s79, 0x380000\n"
"s_cmp_lt_i32 s80, 0x50\n"
"s_cselect_b32 s80, s79, 0x40000000\n"
"s_lshl_b32 s79, s85, 6\n"
"v_add_u32 v68, s79, v0\n"
"v_mul_i32_i24 v79, 0xcd, v68\n"
"v_lshrrev_b32 v81, 14, v79\n"
"s_lshr_b32 s79, s81, 2\n"
"s_mul_i32 s79, s79, 13\n"
"s_lshr_b32 s79, s79, 6\n"
"s_and_b32 s79, s79, 1\n"
"s_lshl_b32 s79, s79, 1\n"
"v_mul_i32_i24 v79, 0x50, v81\n"
"v_sub_u32 v79, v68, v79\n"
"v_lshrrev_b32 v79, 4, v79\n"
"v_add_u32 v82, s79, v79\n"
"v_subrev_u32 v79, 5, v82\n"
"v_cmp_le_i32 vcc, 5, v82\n"
"s_nop 1\n"
"v_cndmask_b32 v79, v82, v79, vcc\n"
"s_mov_b32 s79, 0x64\n"
"v_mad_i32_i24 v79, v81, s79, v79\n"
"v_and_b32 v78, 15, v68\n"
"v_lshlrev_b32 v78, 4, v78\n"
"v_lshl_add_u32 v65, v79, 10, v78\n"
"s_lshr_b32 s79, s81, 2\n"
"s_mul_i32 s79, s79, 0x1400\n"
"s_and_b32 s78, s81, 3\n"
"s_lshl_b32 s78, s78, 8\n"
"s_add_u32 s79, s79, s78\n"
"s_add_u32 s79, s79, 0x380000\n"
"s_cmp_lt_i32 s81, 0x50\n"
"s_cselect_b32 s81, s79, 0x40000000\n"
"s_lshl_b32 s79, s86, 6\n"
"v_add_u32 v68, s79, v0\n"
"v_mul_i32_i24 v79, 0xcd, v68\n"
"v_lshrrev_b32 v81, 14, v79\n"
"s_lshr_b32 s79, s82, 2\n"
"s_mul_i32 s79, s79, 13\n"
"s_lshr_b32 s79, s79, 6\n"
"s_and_b32 s79, s79, 1\n"
"s_lshl_b32 s79, s79, 1\n"
"v_mul_i32_i24 v79, 0x50, v81\n"
"v_sub_u32 v79, v68, v79\n"
"v_lshrrev_b32 v79, 4, v79\n"
"v_add_u32 v82, s79, v79\n"
"v_subrev_u32 v79, 5, v82\n"
"v_cmp_le_i32 vcc, 5, v82\n"
"s_nop 1\n"
"v_cndmask_b32 v79, v82, v79, vcc\n"
"s_mov_b32 s79, 0x64\n"
"v_mad_i32_i24 v79, v81, s79, v79\n"
"v_and_b32 v78, 15, v68\n"
"v_lshlrev_b32 v78, 4, v78\n"
"v_lshl_add_u32 v66, v79, 10, v78\n"
"s_lshr_b32 s79, s82, 2\n"
"s_mul_i32 s79, s79, 0x1400\n"
"s_and_b32 s78, s82, 3\n"
"s_lshl_b32 s78, s78, 8\n"
"s_add_u32 s79, s79, s78\n"
"s_add_u32 s79, s79, 0x380000\n"
"s_cmp_lt_i32 s82, 0x50\n"
"s_cselect_b32 s82, s79, 0x40000000\n"
"s_lshl_b32 s79, s87, 6\n"
"v_add_u32 v68, s79, v0\n"
"v_mul_i32_i24 v79, 0xcd, v68\n"
"v_lshrrev_b32 v81, 14, v79\n"
"s_lshr_b32 s79, s83, 2\n"
"s_mul_i32 s79, s79, 13\n"
"s_lshr_b32 s79, s79, 6\n"
"s_and_b32 s79, s79, 1\n"
"s_lshl_b32 s79, s79, 1\n"
"v_mul_i32_i24 v79, 0x50, v81\n"
"v_sub_u32 v79, v68, v79\n"
"v_lshrrev_b32 v79, 4, v79\n"
"v_add_u32 v82, s79, v79\n"
"v_subrev_u32 v79, 5, v82\n"
"v_cmp_le_i32 vcc, 5, v82\n"
"s_nop 1\n"
"v_cndmask_b32 v79, v82, v79, vcc\n"
"s_mov_b32 s79, 0x64\n"
"v_mad_i32_i24 v79, v81, s79, v79\n"
"v_and_b32 v78, 15, v68\n"
"v_lshlrev_b32 v78, 4, v78\n"
"v_lshl_add_u32 v67, v79, 10, v78\n"
"s_lshr_b32 s79, s83, 2\n"
"s_mul_i32 s79, s79, 0x1400\n"
"s_and_b32 s78, s83, 3\n"
"s_lshl_b32 s78, s78, 8\n"
"s_add_u32 s79, s79, s78\n"
"s_add_u32 s79, s79, 0x380000\n"
"s_cmp_lt_i32 s83, 0x50\n"
"s_cselect_b32 s83, s79, 0x40000000\n"
"s_waitcnt vmcnt(0)\n"
"v_perm_b32 v28, v101, v100, s31\n"
"v_perm_b32 v29, v103, v102, s31\n"
"v_perm_b32 v30, v105, v104, s31\n"
"v_perm_b32 v31, v107, v106, s31\n"
"buffer_store_dwordx4 v[28:31], v64, s[4:7], s80 offen sc1\n"
"v_perm_b32 v32, v109, v108, s31\n"
"v_perm_b32 v33, v111, v110, s31\n"
"v_perm_b32 v34, v113, v112, s31\n"
"v_perm_b32 v35, v115, v114, s31\n"
"buffer_store_dwordx4 v[32:35], v65, s[4:7], s81 offen sc1\n"
"v_perm_b32 v36, v117, v116, s31\n"
"v_perm_b32 v37, v119, v118, s31\n"
"v_perm_b32 v38, v121, v120, s31\n"
"v_perm_b32 v39, v123, v122, s31\n"
"buffer_store_dwordx4 v[36:39], v66, s[4:7], s82 offen sc1\n"
"v_perm_b32 v40, v125, v124, s31\n"
"v_perm_b32 v41, v127, v126, s31\n"
"v_perm_b32 v42, v129, v128, s31\n"
"v_perm_b32 v43, v131, v130, s31\n"
"buffer_store_dwordx4 v[40:43], v67, s[4:7], s83 offen sc1\n"
".Lrep_6:\n"
"v_lshlrev_b32 v80, 14, v0\n"
"s_mov_b32 s34, 0xaaaaaaaa\n"
"s_mov_b32 s35, 0xaaaaaaaa\n"
"s_mov_b32 s40, 0xffff\n"
"s_mov_b32 s41, 0\n"
"s_mov_b32 s36, 0xfff0ffff\n"
"s_mov_b32 s37, 0xf000ff00\n"
"s_mov_b32 s38, 0xffc0fffc\n"
"s_mov_b32 s39, 0xc000fc00\n"
"s_lshl_b32 s79, s19, 2\n"
"s_add_u32 s78, s3, 1\n"
"s_and_b32 s78, s78, 3\n"
"s_add_u32 s79, s79, s78\n"
"s_mul_i32 s79, s79, 0x5000\n"
"s_mul_i32 s78, s18, 0x500\n"
"s_add_u32 s79, s79, s78\n"
"s_add_u32 s30, s79, 0x448000\n"
"s_mul_i32 s79, s18, 40\n"
"s_lshr_b32 s79, s79, 6\n"
"s_lshl_b32 s79, s79, 10\n"
"v_lshl_add_u32 v79, v0, 5, s79\n"
"v_min_i32 v78, 39, v0\n"
"v_subrev_u32 v78, 20, v78\n"
"s_mul_i32 s79, s18, 40\n"
"s_add_u32 s79, s79, 39\n"
"s_lshr_b32 s79, s79, 6\n"
"s_lshl_b32 s79, s79, 10\n"
"v_lshl_add_u32 v78, v78, 5, s79\n"
"v_cmp_gt_i32 vcc, 20, v0\n"
"s_nop 1\n"
"v_cndmask_b32 v19, v78, v79, vcc\n"
"s_and_b32 s79, s2, 7\n"
"s_lshl_b32 s79, s79, 14\n"
"s_add_u32 s50, s79, 0x1000\n"
"s_mul_i32 s79, s19, 0x14000\n"
"s_mul_i32 s78, s43, 0x1400\n"
"s_add_u32 s79, s79, s78\n"
"s_add_u32 s44, s79, 0xc28000\n"
"s_add_u32 s79, s3, 1\n"
"s_and_b32 s79, s79, 3\n"
"s_add_u32 s79, s19, s79\n"
"s_cmp_eq_i32 s79, 0\n"
"s_cselect_b32 s42, 0xfe967699, 0\n"
"s_lshl_b32 s79, s18, 2\n"
"s_add_u32 s79, s79, s3\n"
"s_mul_i32 s79, s79, 0xa000\n"
"s_add_u32 s31, s79, 0x100000\n"
"s_lshl_b32 s79, s43, 11\n"
"s_add_u32 s52, s31, s79\n"
"s_mov_b32 s79, 0xffffe800\n"
"s_cmp_ge_i32 s43, 3\n"
"s_cselect_b32 s79, s79, 0x8800\n"
"s_add_u32 s53, s52, s79\n"
"s_mov_b32 s79, 0xfffff000\n"
"s_cmp_ge_i32 s43, 2\n"
"s_cselect_b32 s79, s79, 0x9000\n"
"s_add_u32 s54, s52, s79\n"
"s_mov_b32 s79, 0xfffff800\n"
"s_cmp_ge_i32 s43, 1\n"
"s_cselect_b32 s79, s79, 0x9800\n"
"s_add_u32 s55, s52, s79\n"
"s_mov_b32 s20, 0xcccccccc\n"
"s_mov_b32 s21, 0xcccccccc\n"
"s_mov_b32 s22, 0xf0000f\n"
"s_mov_b32 s23, 0xf0000f00\n"
"v_and_b32 v161, 15, v0\n"
"v_and_b32 v162, 3, v0\n"
"s_mul_i32 s70, s43, 0x1400\n"
"s_lshl_b32 s71, s3, 2\n"
"s_lshl_b32 s72, s3, 5\n"
"s_lshl_b32 s73, s43, 10\n"
"v_lshrrev_b32 v163, 4, v0\n"
"v_and_b32 v164, 3, v161\n"
"v_mul_i32_i24 v165, 0xf8, v161\n"
"v_lshlrev_b32 v166, 4, v161\n"
"v_lshlrev_b32 v167, 3, v163\n"
"v_lshlrev_b32 v79, 2, v161\n"
"v_add_u32 v12, 0x26940, v79\n"
"s_add_u32 s79, s3, 1\n"
"s_and_b32 s79, s79, 3\n"
"s_lshl_b32 s74, s79, 4\n"
"s_add_u32 s79, s71, 0x26840\n"
"v_add_u32 v168, s79, v166\n"
"s_lshl_b32 s79, s3, 11\n"
"s_add_u32 s79, s79, 0x5000\n"
"v_add_u32 v79, s79, v166\n"
"s_mov_b32 vcc_lo, 0xf0000f\n"
"s_mov_b32 vcc_hi, 0xf0000f00\n"
"v_cndmask_b32 v92, 2.0, v79, vcc\n"
"v_add_u32 v79, s74, v161\n"
"v_mul_i32_i24 v79, 0xf8, v79\n"
"v_add_u32 v24, 0x5000, v79\n"
"v_lshrrev_b32 v79, 3, v161\n"
"v_lshlrev_b32 v79, 1, v79\n"
"v_xor_b32 v79, v163, v79\n"
"v_lshlrev_b32 v79, 4, v79\n"
"v_lshl_add_u32 v169, v161, 6, v79\n"
"v_lshl_add_u32 v79, v164, 2, v166\n"
"v_lshl_add_u32 v79, v163, 8, v79\n"
"v_add_u32 v17, 0x5000, v79\n"
"v_add_u32 v79, 48, v167\n"
"v_mov_b32 v78, 0xf0\n"
"s_mov_b32 vcc_lo, 0\n"
"s_mov_b32 vcc_hi, -1\n"
"v_cndmask_b32 v79, v78, v79, vcc\n"
"s_cmp_eq_i32 s3, 3\n"
"s_cselect_b32 s79, 0xb0, s72\n"
"v_add_u32 v78, s79, v167\n"
"v_mov_b32 v77, s3\n"
"v_cmp_eq_i32 vcc, 2, v77\n"
"s_nop 1\n"
"v_cndmask_b32 v79, v78, v79, vcc\n"
"s_mov_b32 s79, 0x5000\n"
"v_add3_u32 v7, v165, v79, s79\n"
"s_add_u32 s79, s72, 0x50\n"
"s_cmp_eq_i32 s3, 3\n"
"s_cselect_b32 s79, 0xd0, s79\n"
"s_add_u32 s79, s79, 0x5000\n"
"v_add3_u32 v8, v165, v167, s79\n"
"v_mov_b32 v79, 0x40000000\n"
"s_xor_b32 s79, s3, 2\n"
"s_lshl_b32 s79, s79, 2\n"
"v_add_u32 v78, s79, v163\n"
"v_mov_b32 v77, 1\n"
"s_mov_b32 vcc_lo, 0xf000f\n"
"s_mov_b32 vcc_hi, 0xf000f\n"
"v_cndmask_b32 v77, 0, v77, vcc\n"
"v_cmp_eq_i32 vcc, v78, v77\n"
"s_nop 1\n"
"v_cndmask_b32 v79, v79, v12, vcc\n"
"s_mov_b32 vcc_lo, 0xf0000f\n"
"s_mov_b32 vcc_hi, 0xf0000f00\n"
"v_cndmask_b32 v9, v79, v168, vcc\n"
"v_add_u32 v11, 0x26840, v166\n"
"s_mov_b32 vcc_lo, 0xffff\n"
"s_mov_b32 vcc_hi, 0\n"
"v_cndmask_b32 v10, 2.0, v168, vcc\n"
"s_add_u32 s79, s71, 0x26400\n"
"v_mov_b32 v79, s79\n"
"s_mov_b32 vcc_lo, 1\n"
"s_mov_b32 vcc_hi, 0\n"
"v_cndmask_b32 v13, 2.0, v79, vcc\n"
"v_lshlrev_b32 v79, 2, v162\n"
"v_add_u32 v14, 0x26400, v79\n"
"v_add_u32 v25, v24, v167\n"
"v_lshl_add_u32 v79, v163, 2, s74\n"
"v_lshrrev_b32 v78, 2, v161\n"
"v_add_u32 v79, v79, v78\n"
"v_mul_i32_i24 v79, 0xf8, v79\n"
"v_lshl_add_u32 v79, v164, 3, v79\n"
"v_add_u32 v26, 0x50a0, v79\n"
"v_mov_b32 v27, 0x3727c5ac\n"
"v_xor_b32 v18, 4, v17\n"
"v_xor_b32 v79, 1, v164\n"
"v_lshl_add_u32 v91, v79, 2, v92\n"
"v_lshrrev_b32 v79, 2, v0\n"
"v_lshlrev_b32 v78, 4, v162\n"
"v_lshl_add_u32 v79, v79, 10, v78\n"
"v_add_u32 v79, s70, v79\n"
"s_mov_b32 vcc_lo, 0xfffff\n"
"s_mov_b32 vcc_hi, 0\n"
"v_cndmask_b32 v93, 2.0, v79, vcc\n"
"v_lshlrev_b32 v79, 3, v161\n"
"v_add_u32 v94, 0x9d80, v79\n"
"v_lshlrev_b32 v79, 3, v0\n"
"v_add_u32 v16, 0x9d80, v79\n"
"v_add_u32 v88, s70, v169\n"
"v_add_u32 v3, s73, v169\n"
"s_add_u32 s79, s73, 0xfffff400\n"
"s_add_u32 s78, s73, 0x4400\n"
"s_cmp_ge_i32 s43, 3\n"
"s_cselect_b32 s79, s79, s78\n"
"v_add_u32 v4, s79, v169\n"
"s_add_u32 s79, s73, 0xfffff800\n"
"s_add_u32 s78, s73, 0x4800\n"
"s_cmp_ge_i32 s43, 2\n"
"s_cselect_b32 s79, s79, s78\n"
"v_add_u32 v5, s79, v169\n"
"s_add_u32 s79, s73, 0xfffffc00\n"
"s_add_u32 s78, s73, 0x4c00\n"
"s_cmp_ge_i32 s43, 1\n"
"s_cselect_b32 s79, s79, s78\n"
"v_add_u32 v6, s79, v169\n"
"s_waitcnt lgkmcnt(0)\n"
"s_mul_i32 s16, s16, 0x2c9277b5\n"
"s_add_u32 s16, s16, 0xac564b05\n"
"v_mov_b32 v82, s16\n"
"s_waitcnt vmcnt(0)\n"
"s_mov_b64 exec, s[58:59]\n"
"buffer_store_dword v82, v80, s[4:7], s32 offen sc1\n"
"s_mov_b64 exec, -1\n"
"s_setprio 1\n"
"s_add_u32 m0, s61, 0\n"
"s_nop 0\n"
"buffer_load_dwordx4 v2, s[8:11], s60 offen offset:0 lds\n"
"s_add_u32 m0, s61, 0x3c0\n"
"s_nop 0\n"
"buffer_load_dwordx4 v2, s[8:11], s60 offen offset:64 lds\n"
"s_add_u32 m0, s61, 0x780\n"
"s_nop 0\n"
"buffer_load_dwordx4 v2, s[8:11], s60 offen offset:128 lds\n"
"s_add_u32 m0, s61, 0xb40\n"
"s_nop 0\n"
"buffer_load_dwordx4 v2, s[8:11], s60 offen offset:192 lds\n"
"s_add_u32 m0, s61, 0xf00\n"
"s_nop 0\n"
"buffer_load_dwordx4 v2, s[8:11], s60 offen offset:256 lds\n"
"s_add_u32 m0, s63, 0\n"
"s_nop 0\n"
"buffer_load_dwordx4 v2, s[8:11], s62 offen offset:0 lds\n"
"s_add_u32 m0, s63, 0x3c0\n"
"s_nop 0\n"
"buffer_load_dwordx4 v2, s[8:11], s62 offen offset:64 lds\n"
"s_add_u32 m0, s63, 0x780\n"
"s_nop 0\n"
"buffer_load_dwordx4 v2, s[8:11], s62 offen offset:128 lds\n"
"s_add_u32 m0, s63, 0xb40\n"
"s_nop 0\n"
"buffer_load_dwordx4 v2, s[8:11], s62 offen offset:192 lds\n"
"s_add_u32 m0, s63, 0xf00\n"
"s_nop 0\n"
"buffer_load_dwordx4 v2, s[8:11], s62 offen offset:256 lds\n"
"s_add_u32 m0, s65, 0\n"
"s_nop 0\n"
"buffer_load_dwordx4 v2, s[8:11], s64 offen offset:0 lds\n"
"s_add_u32 m0, s65, 0x3c0\n"
"s_nop 0\n"
"buffer_load_dwordx4 v2, s[8:11], s64 offen offset:64 lds\n"
"s_add_u32 m0, s65, 0x780\n"
"s_nop 0\n"
"buffer_load_dwordx4 v2, s[8:11], s64 offen offset:128 lds\n"
"s_add_u32 m0, s65, 0xb40\n"
"s_nop 0\n"
"buffer_load_dwordx4 v2, s[8:11], s64 offen offset:192 lds\n"
"s_add_u32 m0, s65, 0xf00\n"
"s_nop 0\n"
"buffer_load_dwordx4 v2, s[8:11], s64 offen offset:256 lds\n"
"s_add_u32 m0, s67, 0\n"
"s_nop 0\n"
"buffer_load_dwordx4 v2, s[8:11], s66 offen offset:0 lds\n"
"s_add_u32 m0, s67, 0x3c0\n"
"s_nop 0\n"
"buffer_load_dwordx4 v2, s[8:11], s66 offen offset:64 lds\n"
"s_add_u32 m0, s67, 0x780\n"
"s_nop 0\n"
"buffer_load_dwordx4 v2, s[8:11], s66 offen offset:128 lds\n"
"s_add_u32 m0, s67, 0xb40\n"
"s_nop 0\n"
"buffer_load_dwordx4 v2, s[8:11], s66 offen offset:192 lds\n"
"s_add_u32 m0, s67, 0xf00\n"
"s_nop 0\n"
"buffer_load_dwordx4 v2, s[8:11], s66 offen offset:256 lds\n"
"s_add_u32 m0, s69, 0\n"
"s_nop 0\n"
"buffer_load_dwordx4 v2, s[8:11], s68 offen offset:0 lds\n"
"s_add_u32 m0, s69, 0x3c0\n"
"s_nop 0\n"
"buffer_load_dwordx4 v2, s[8:11], s68 offen offset:64 lds\n"
"s_add_u32 m0, s69, 0x780\n"
"s_nop 0\n"
"buffer_load_dwordx4 v2, s[8:11], s68 offen offset:128 lds\n"
"s_add_u32 m0, s69, 0xb40\n"
"s_nop 0\n"
"buffer_load_dwordx4 v2, s[8:11], s68 offen offset:192 lds\n"
"s_add_u32 m0, s69, 0xf00\n"
"s_nop 0\n"
"buffer_load_dwordx4 v2, s[8:11], s68 offen offset:256 lds\n"
"buffer_load_dwordx4 v[96:99], v19, s[4:7], s50 offen sc1\n"
"s_waitcnt vmcnt(1)\n"
"ds_write_b128 v93, v[20:23] offset:40960\n"
"ds_write_b32 v13, v13 offset:0\n"
"v_add_u32 v88, 0xa000, v88\n"
"ds_read_b128 v[68:71], v88 offset:0\n"
"ds_read_b128 v[72:75], v88 offset:1024\n"
"ds_read_b128 v[76:79], v88 offset:2048\n"
"ds_read_b128 v[80:83], v88 offset:3072\n"
"s_waitcnt vmcnt(0)\n"
"v_xor_b32 v96, s16, v96\n"
"v_xor_b32 v97, s16, v97\n"
"v_xor_b32 v98, s16, v98\n"
"v_xor_b32 v99, s16, v99\n"
"v_or_b32 v96, v96, v97\n"
"v_or3_b32 v96, v96, v98, v99\n"
"v_cmp_ne_u32 vcc, 0, v96\n"
"s_cbranch_vccnz .Lwt_11\n"
"buffer_load_dwordx4 v[96:99], v1, s[4:7], s52 offen offset:0 sc1\n"
"s_waitcnt lgkmcnt(3)\n"
"v_mfma_f32_16x16x32_bf16 v[28:31], v[68:71], v[68:71], 0\n"
"ds_read_b128 v[84:87], v88 offset:4096\n"
"v_mfma_f32_16x16x32_bf16 v[32:35], v[20:23], v[68:71], 0\n"
"s_waitcnt lgkmcnt(3)\n"
"v_mfma_f32_16x16x32_bf16 v[28:31], v[72:75], v[72:75], v[28:31]\n"
"buffer_load_dwordx4 v[100:103], v1, s[4:7], s52 offen offset:1024 sc1\n"
"ds_read_b128 v[68:71], v88 offset:20480\n"
"v_mfma_f32_16x16x32_bf16 v[32:35], v[20:23], v[72:75], v[32:35]\n"
"s_waitcnt lgkmcnt(3)\n"
"v_mfma_f32_16x16x32_bf16 v[28:31], v[76:79], v[76:79], v[28:31]\n"
"ds_read_b128 v[72:75], v88 offset:21504\n"
"v_mfma_f32_16x16x32_bf16 v[32:35], v[20:23], v[76:79], v[32:35]\n"
"buffer_load_dwordx4 v[104:107], v1, s[4:7], s52 offen offset:2048 sc1\n"
"s_waitcnt lgkmcnt(3)\n"
"v_mfma_f32_16x16x32_bf16 v[28:31], v[80:83], v[80:83], v[28:31]\n"
"ds_read_b128 v[76:79], v88 offset:22528\n"
"v_mfma_f32_16x16x32_bf16 v[32:35], v[20:23], v[80:83], v[32:35]\n"
"s_waitcnt lgkmcnt(3)\n"
"v_mfma_f32_16x16x32_bf16 v[28:31], v[84:87], v[84:87], v[28:31]\n"
"ds_read_b128 v[80:83], v88 offset:23552\n"
"buffer_load_dwordx4 v[108:111], v1, s[4:7], s52 offen offset:3072 sc1\n"
"v_mfma_f32_16x16x32_bf16 v[32:35], v[20:23], v[84:87], v[32:35]\n"
"s_waitcnt lgkmcnt(3)\n"
"v_mfma_f32_16x16x32_bf16 v[36:39], v[68:71], v[68:71], 0\n"
"ds_read_b128 v[84:87], v88 offset:24576\n"
"v_mfma_f32_16x16x32_bf16 v[40:43], v[20:23], v[68:71], 0\n"
"s_waitcnt lgkmcnt(3)\n"
"v_mfma_f32_16x16x32_bf16 v[36:39], v[72:75], v[72:75], v[36:39]\n"
"s_add_u32 s52, s52, 4096\n"
"buffer_load_dwordx4 v[112:115], v1, s[4:7], s52 offen offset:0 sc1\n"
"v_add_u32 v88, 0x12400, v88\n"
"ds_read_b128 v[68:71], v88 offset:0\n"
"v_mfma_f32_16x16x32_bf16 v[40:43], v[20:23], v[72:75], v[40:43]\n"
"ds_write_b128 v92, v[28:31] offset:0\n"
"buffer_load_dwordx4 v[116:119], v1, s[4:7], s52 offen offset:1024 sc1\n"
"s_waitcnt lgkmcnt(4)\n"
"v_mfma_f32_16x16x32_bf16 v[36:39], v[76:79], v[76:79], v[36:39]\n"
"ds_read_b128 v[72:75], v88 offset:1024\n"
"v_mfma_f32_16x16x32_bf16 v[40:43], v[20:23], v[76:79], v[40:43]\n"
"ds_write_b32 v91, v32 offset:0\n"
"s_waitcnt lgkmcnt(5)\n"
"v_mfma_f32_16x16x32_bf16 v[36:39], v[80:83], v[80:83], v[36:39]\n"
"buffer_load_dwordx4 v[120:123], v1, s[4:7], s52 offen offset:2048 sc1\n"
"ds_read_b128 v[76:79], v88 offset:2048\n"
"v_mfma_f32_16x16x32_bf16 v[40:43], v[20:23], v[80:83], v[40:43]\n"
"s_waitcnt lgkmcnt(5)\n"
"v_mfma_f32_16x16x32_bf16 v[36:39], v[84:87], v[84:87], v[36:39]\n"
"ds_read_b128 v[80:83], v88 offset:3072\n"
"v_mfma_f32_16x16x32_bf16 v[40:43], v[20:23], v[84:87], v[40:43]\n"
"buffer_load_dwordx4 v[124:127], v1, s[4:7], s52 offen offset:3072 sc1\n"
"s_waitcnt lgkmcnt(5)\n"
"v_mfma_f32_16x16x32_bf16 v[44:47], v[68:71], v[68:71], 0\n"
"ds_read_b128 v[84:87], v88 offset:4096\n"
"v_mfma_f32_16x16x32_bf16 v[48:51], v[20:23], v[68:71], 0\n"
"s_waitcnt lgkmcnt(4)\n"
"v_mfma_f32_16x16x32_bf16 v[44:47], v[72:75], v[72:75], v[44:47]\n"
"ds_read_b128 v[68:71], v88 offset:20480\n"
"s_add_u32 s52, s52, 4096\n"
"buffer_load_dwordx4 v[128:131], v1, s[4:7], s52 offen offset:0 sc1\n"
"v_mfma_f32_16x16x32_bf16 v[48:51], v[20:23], v[72:75], v[48:51]\n"
"ds_write_b128 v92, v[36:39] offset:256\n"
"s_waitcnt lgkmcnt(4)\n"
"v_mfma_f32_16x16x32_bf16 v[44:47], v[76:79], v[76:79], v[44:47]\n"
"ds_read_b128 v[72:75], v88 offset:21504\n"
"buffer_load_dwordx4 v[132:135], v1, s[4:7], s52 offen offset:1024 sc1\n"
"v_mfma_f32_16x16x32_bf16 v[48:51], v[20:23], v[76:79], v[48:51]\n"
"ds_write_b32 v91, v40 offset:256\n"
"s_waitcnt lgkmcnt(5)\n"
"v_mfma_f32_16x16x32_bf16 v[44:47], v[80:83], v[80:83], v[44:47]\n"
"ds_read_b128 v[76:79], v88 offset:22528\n"
"v_mfma_f32_16x16x32_bf16 v[48:51], v[20:23], v[80:83], v[48:51]\n"
"buffer_load_dwordx4 v[136:139], v1, s[4:7], s52 offen offset:2048 sc1\n"
"s_waitcnt lgkmcnt(5)\n"
"v_mfma_f32_16x16x32_bf16 v[44:47], v[84:87], v[84:87], v[44:47]\n"
"ds_read_b128 v[80:83], v88 offset:23552\n"
"v_mfma_f32_16x16x32_bf16 v[48:51], v[20:23], v[84:87], v[48:51]\n"
"s_waitcnt lgkmcnt(5)\n"
"v_mfma_f32_16x16x32_bf16 v[52:55], v[68:71], v[68:71], 0\n"
"ds_read_b128 v[84:87], v88 offset:24576\n"
"buffer_load_dwordx4 v[140:143], v1, s[4:7], s52 offen offset:3072 sc1\n"
"v_mfma_f32_16x16x32_bf16 v[56:59], v[20:23], v[68:71], 0\n"
"s_waitcnt lgkmcnt(4)\n"
"v_mfma_f32_16x16x32_bf16 v[52:55], v[72:75], v[72:75], v[52:55]\n"
"v_add_u32 v88, 0xfffe3c00, v88\n"
"ds_read_b128 v[68:71], v88 offset:0\n"
"v_mfma_f32_16x16x32_bf16 v[56:59], v[20:23], v[72:75], v[56:59]\n"
"s_add_u32 s52, s52, 4096\n"
"buffer_load_dwordx4 v[144:147], v1, s[4:7], s52 offen offset:0 sc1\n"
"ds_write_b128 v92, v[44:47] offset:512\n"
"s_waitcnt lgkmcnt(4)\n"
"v_mfma_f32_16x16x32_bf16 v[52:55], v[76:79], v[76:79], v[52:55]\n"
"ds_read_b128 v[72:75], v88 offset:1024\n"
"v_mfma_f32_16x16x32_bf16 v[56:59], v[20:23], v[76:79], v[56:59]\n"
"buffer_load_dwordx4 v[148:151], v1, s[4:7], s52 offen offset:1024 sc1\n"
"ds_write_b32 v91, v48 offset:512\n"
"ds_read_b32 v90, v14 offset:0\n"
"s_waitcnt lgkmcnt(6)\n"
"v_mfma_f32_16x16x32_bf16 v[52:55], v[80:83], v[80:83], v[52:55]\n"
"ds_read_b128 v[76:79], v88 offset:2048\n"
"v_mfma_f32_16x16x32_bf16 v[56:59], v[20:23], v[80:83], v[56:59]\n"
"buffer_load_dwordx4 v[152:155], v1, s[4:7], s52 offen offset:2048 sc1\n"
"s_waitcnt lgkmcnt(6)\n"
"v_mfma_f32_16x16x32_bf16 v[52:55], v[84:87], v[84:87], v[52:55]\n"
"ds_read_b128 v[80:83], v88 offset:3072\n"
"v_mfma_f32_16x16x32_bf16 v[56:59], v[20:23], v[84:87], v[56:59]\n"
"s_waitcnt lgkmcnt(6)\n"
"v_mfma_f32_16x16x32_bf16 v[60:63], v[68:71], v[68:71], 0\n"
"ds_read_b128 v[84:87], v88 offset:4096\n"
"buffer_load_dwordx4 v[156:159], v1, s[4:7], s52 offen offset:3072 sc1\n"
"v_mfma_f32_16x16x32_bf16 v[64:67], v[20:23], v[68:71], 0\n"
"s_waitcnt lgkmcnt(3)\n"
"v_cmp_eq_u32 vcc, 0, v90\n"
"v_mfma_f32_16x16x32_bf16 v[60:63], v[72:75], v[72:75], v[60:63]\n"
"v_mfma_f32_16x16x32_bf16 v[64:67], v[20:23], v[72:75], v[64:67]\n"
"ds_write_b128 v92, v[52:55] offset:768\n"
"s_cbranch_vccnz .Lsl_7\n"
".Lbk_8:\n"
"s_waitcnt lgkmcnt(3)\n"
"v_mfma_f32_16x16x32_bf16 v[60:63], v[76:79], v[76:79], v[60:63]\n"
"v_add_u32 v3, 0x5000, v3\n"
"v_mfma_f32_16x16x32_bf16 v[64:67], v[20:23], v[76:79], v[64:67]\n"
"ds_read_b128 v[68:71], v3 offset:20480\n"
"ds_write_b32 v91, v56 offset:768\n"
"ds_read_b128 v[72:75], v3 offset:40960\n"
"s_waitcnt lgkmcnt(5)\n"
"v_mfma_f32_16x16x32_bf16 v[60:63], v[80:83], v[80:83], v[60:63]\n"
"v_add_u32 v95, 0x17400, v3\n"
"v_mfma_f32_16x16x32_bf16 v[64:67], v[20:23], v[80:83], v[64:67]\n"
"ds_read_b128 v[76:79], v95 offset:0\n"
"s_waitcnt lgkmcnt(5)\n"
"v_mfma_f32_16x16x32_bf16 v[60:63], v[84:87], v[84:87], v[60:63]\n"
"ds_read_b128 v[80:83], v95 offset:20480\n"
"v_mfma_f32_16x16x32_bf16 v[64:67], v[20:23], v[84:87], v[64:67]\n"
"s_nop 4\n"
"ds_write_b128 v92, v[60:63] offset:1024\n"
"s_nop 1\n"
"ds_write_b32 v91, v64 offset:1024\n"
"s_branch .Ljn_12\n"
".Lwt_11:\n"
"s_waitcnt lgkmcnt(0)\n"
"v_mfma_f32_16x16x32_bf16 v[28:31], v[68:71], v[68:71], 0\n"
"ds_read_b128 v[84:87], v88 offset:4096\n"
"v_mfma_f32_16x16x32_bf16 v[32:35], v[20:23], v[68:71], 0\n"
"v_mfma_f32_16x16x32_bf16 v[28:31], v[72:75], v[72:75], v[28:31]\n"
"ds_read_b128 v[68:71], v88 offset:20480\n"
"v_mfma_f32_16x16x32_bf16 v[32:35], v[20:23], v[72:75], v[32:35]\n"
"v_mfma_f32_16x16x32_bf16 v[28:31], v[76:79], v[76:79], v[28:31]\n"
"ds_read_b128 v[72:75], v88 offset:21504\n"
"v_mfma_f32_16x16x32_bf16 v[32:35], v[20:23], v[76:79], v[32:35]\n"
"v_mfma_f32_16x16x32_bf16 v[28:31], v[80:83], v[80:83], v[28:31]\n"
"ds_read_b128 v[76:79], v88 offset:22528\n"
"v_mfma_f32_16x16x32_bf16 v[32:35], v[20:23], v[80:83], v[32:35]\n"
"s_waitcnt lgkmcnt(3)\n"
"v_mfma_f32_16x16x32_bf16 v[28:31], v[84:87], v[84:87], v[28:31]\n"
"ds_read_b128 v[80:83], v88 offset:23552\n"
"v_mfma_f32_16x16x32_bf16 v[32:35], v[20:23], v[84:87], v[32:35]\n"
"s_waitcnt lgkmcnt(3)\n"
"v_mfma_f32_16x16x32_bf16 v[36:39], v[68:71], v[68:71], 0\n"
"ds_read_b128 v[84:87], v88 offset:24576\n"
"v_mfma_f32_16x16x32_bf16 v[40:43], v[20:23], v[68:71], 0\n"
"s_waitcnt lgkmcnt(3)\n"
"v_mfma_f32_16x16x32_bf16 v[36:39], v[72:75], v[72:75], v[36:39]\n"
"v_add_u32 v88, 0x12400, v88\n"
"ds_read_b128 v[68:71], v88 offset:0\n"
"v_mfma_f32_16x16x32_bf16 v[40:43], v[20:23], v[72:75], v[40:43]\n"
"ds_write_b128 v92, v[28:31] offset:0\n"
"s_waitcnt lgkmcnt(4)\n"
"v_mfma_f32_16x16x32_bf16 v[36:39], v[76:79], v[76:79], v[36:39]\n"
"ds_read_b128 v[72:75], v88 offset:1024\n"
"v_mfma_f32_16x16x32_bf16 v[40:43], v[20:23], v[76:79], v[40:43]\n"
"ds_write_b32 v91, v32 offset:0\n"
"s_waitcnt lgkmcnt(5)\n"
"v_mfma_f32_16x16x32_bf16 v[36:39], v[80:83], v[80:83], v[36:39]\n"
"ds_read_b128 v[76:79], v88 offset:2048\n"
"v_mfma_f32_16x16x32_bf16 v[40:43], v[20:23], v[80:83], v[40:43]\n"
"s_waitcnt lgkmcnt(5)\n"
"v_mfma_f32_16x16x32_bf16 v[36:39], v[84:87], v[84:87], v[36:39]\n"
"ds_read_b128 v[80:83], v88 offset:3072\n"
"v_mfma_f32_16x16x32_bf16 v[40:43], v[20:23], v[84:87], v[40:43]\n"
"s_waitcnt lgkmcnt(5)\n"
"v_mfma_f32_16x16x32_bf16 v[44:47], v[68:71], v[68:71], 0\n"
"ds_read_b128 v[84:87], v88 offset:4096\n"
"v_mfma_f32_16x16x32_bf16 v[48:51], v[20:23], v[68:71], 0\n"
"s_waitcnt lgkmcnt(4)\n"
"v_mfma_f32_16x16x32_bf16 v[44:47], v[72:75], v[72:75], v[44:47]\n"
"ds_read_b128 v[68:71], v88 offset:20480\n"
"v_mfma_f32_16x16x32_bf16 v[48:51], v[20:23], v[72:75], v[48:51]\n"
"ds_write_b128 v92, v[36:39] offset:256\n"
"s_waitcnt lgkmcnt(4)\n"
"v_mfma_f32_16x16x32_bf16 v[44:47], v[76:79], v[76:79], v[44:47]\n"
"ds_read_b128 v[72:75], v88 offset:21504\n"
"v_mfma_f32_16x16x32_bf16 v[48:51], v[20:23], v[76:79], v[48:51]\n"
"ds_write_b32 v91, v40 offset:256\n"
"s_waitcnt lgkmcnt(5)\n"
"v_mfma_f32_16x16x32_bf16 v[44:47], v[80:83], v[80:83], v[44:47]\n"
"ds_read_b128 v[76:79], v88 offset:22528\n"
"v_mfma_f32_16x16x32_bf16 v[48:51], v[20:23], v[80:83], v[48:51]\n"
"s_waitcnt lgkmcnt(5)\n"
"v_mfma_f32_16x16x32_bf16 v[44:47], v[84:87], v[84:87], v[44:47]\n"
"ds_read_b128 v[80:83], v88 offset:23552\n"
"v_mfma_f32_16x16x32_bf16 v[48:51], v[20:23], v[84:87], v[48:51]\n"
"s_waitcnt lgkmcnt(5)\n"
"v_mfma_f32_16x16x32_bf16 v[52:55], v[68:71], v[68:71], 0\n"
"ds_read_b128 v[84:87], v88 offset:24576\n"
"v_mfma_f32_16x16x32_bf16 v[56:59], v[20:23], v[68:71], 0\n"
"s_waitcnt lgkmcnt(4)\n"
"v_mfma_f32_16x16x32_bf16 v[52:55], v[72:75], v[72:75], v[52:55]\n"
"v_add_u32 v88, 0xfffe3c00, v88\n"
"ds_read_b128 v[68:71], v88 offset:0\n"
"v_mfma_f32_16x16x32_bf16 v[56:59], v[20:23], v[72:75], v[56:59]\n"
"ds_write_b128 v92, v[44:47] offset:512\n"
"s_waitcnt lgkmcnt(4)\n"
"v_mfma_f32_16x16x32_bf16 v[52:55], v[76:79], v[76:79], v[52:55]\n"
"ds_read_b128 v[72:75], v88 offset:1024\n"
"v_mfma_f32_16x16x32_bf16 v[56:59], v[20:23], v[76:79], v[56:59]\n"
"ds_write_b32 v91, v48 offset:512\n"
"ds_read_b32 v90, v14 offset:0\n"
"s_waitcnt lgkmcnt(6)\n"
"v_mfma_f32_16x16x32_bf16 v[52:55], v[80:83], v[80:83], v[52:55]\n"
"ds_read_b128 v[76:79], v88 offset:2048\n"
"v_mfma_f32_16x16x32_bf16 v[56:59], v[20:23], v[80:83], v[56:59]\n"
"s_waitcnt lgkmcnt(6)\n"
"v_mfma_f32_16x16x32_bf16 v[52:55], v[84:87], v[84:87], v[52:55]\n"
"ds_read_b128 v[80:83], v88 offset:3072\n"
"v_mfma_f32_16x16x32_bf16 v[56:59], v[20:23], v[84:87], v[56:59]\n"
"s_waitcnt lgkmcnt(6)\n"
"v_mfma_f32_16x16x32_bf16 v[60:63], v[68:71], v[68:71], 0\n"
"ds_read_b128 v[84:87], v88 offset:4096\n"
"v_mfma_f32_16x16x32_bf16 v[64:67], v[20:23], v[68:71], 0\n"
"s_waitcnt lgkmcnt(3)\n"
"v_cmp_eq_u32 vcc, 0, v90\n"
"v_mfma_f32_16x16x32_bf16 v[60:63], v[72:75], v[72:75], v[60:63]\n"
"v_mfma_f32_16x16x32_bf16 v[64:67], v[20:23], v[72:75], v[64:67]\n"
"ds_write_b128 v92, v[52:55] offset:768\n"
"s_waitcnt lgkmcnt(3)\n"
"v_mfma_f32_16x16x32_bf16 v[60:63], v[76:79], v[76:79], v[60:63]\n"
"v_mfma_f32_16x16x32_bf16 v[64:67], v[20:23], v[76:79], v[64:67]\n"
"ds_write_b32 v91, v56 offset:768\n"
"s_waitcnt lgkmcnt(3)\n"
"v_mfma_f32_16x16x32_bf16 v[60:63], v[80:83], v[80:83], v[60:63]\n"
"v_mfma_f32_16x16x32_bf16 v[64:67], v[20:23], v[80:83], v[64:67]\n"
"s_waitcnt lgkmcnt(2)\n"
"v_mfma_f32_16x16x32_bf16 v[60:63], v[84:87], v[84:87], v[60:63]\n"
"v_mfma_f32_16x16x32_bf16 v[64:67], v[20:23], v[84:87], v[64:67]\n"
"buffer_load_dwordx4 v[96:99], v19, s[4:7], s50 offen sc1\n"
"s_mov_b32 s17, 0x100000\n"
".Ltp_13:\n"
"ds_read_b32 v90, v14 offset:0\n"
"s_sub_u32 s17, s17, 1\n"
"s_cbranch_scc1 .Lou_14\n"
"s_waitcnt lgkmcnt(0)\n"
"v_cmp_eq_u32 vcc, 0, v90\n"
"s_cbranch_vccnz .Ltp_13\n"
".Lou_14:\n"
"s_waitcnt lgkmcnt(0)\n"
"v_add_u32 v3, 0x5000, v3\n"
"ds_read_b128 v[68:71], v3 offset:20480\n"
"ds_read_b128 v[72:75], v3 offset:40960\n"
"v_add_u32 v95, 0x17400, v3\n"
"ds_read_b128 v[76:79], v95 offset:0\n"
"ds_read_b128 v[80:83], v95 offset:20480\n"
"ds_write_b128 v92, v[60:63] offset:1024\n"
"ds_write_b32 v91, v64 offset:1024\n"
"s_waitcnt vmcnt(0) lgkmcnt(2)\n"
"s_mov_b32 s17, 32768\n"
"s_branch .Lmd_17\n"
".Ltp_15:\n"
"buffer_load_dwordx4 v[96:99], v19, s[4:7], s50 offen sc1\n"
".Lmd_17:\n"
"s_sub_u32 s17, s17, 1\n"
"s_cbranch_scc1 .Lou_16\n"
"s_waitcnt vmcnt(0)\n"
"v_xor_b32 v96, s16, v96\n"
"v_xor_b32 v97, s16, v97\n"
"v_xor_b32 v98, s16, v98\n"
"v_xor_b32 v99, s16, v99\n"
"v_or_b32 v96, v96, v97\n"
"v_or3_b32 v96, v96, v98, v99\n"
"v_cmp_ne_u32 vcc, 0, v96\n"
"s_cbranch_vccz .Lou_16\n"
"s_sleep 8\n"
"s_branch .Ltp_15\n"
".Lou_16:\n"
"s_waitcnt vmcnt(0)\n"
"buffer_load_dwordx4 v[96:99], v1, s[4:7], s52 offen offset:0 sc1\n"
"buffer_load_dwordx4 v[100:103], v1, s[4:7], s52 offen offset:1024 sc1\n"
"buffer_load_dwordx4 v[104:107], v1, s[4:7], s52 offen offset:2048 sc1\n"
"buffer_load_dwordx4 v[108:111], v1, s[4:7], s52 offen offset:3072 sc1\n"
"s_add_u32 s52, s52, 4096\n"
"buffer_load_dwordx4 v[112:115], v1, s[4:7], s52 offen offset:0 sc1\n"
"buffer_load_dwordx4 v[116:119], v1, s[4:7], s52 offen offset:1024 sc1\n"
"buffer_load_dwordx4 v[120:123], v1, s[4:7], s52 offen offset:2048 sc1\n"
"buffer_load_dwordx4 v[124:127], v1, s[4:7], s52 offen offset:3072 sc1\n"
"s_add_u32 s52, s52, 4096\n"
"buffer_load_dwordx4 v[128:131], v1, s[4:7], s52 offen offset:0 sc1\n"
"buffer_load_dwordx4 v[132:135], v1, s[4:7], s52 offen offset:1024 sc1\n"
"buffer_load_dwordx4 v[136:139], v1, s[4:7], s52 offen offset:2048 sc1\n"
"buffer_load_dwordx4 v[140:143], v1, s[4:7], s52 offen offset:3072 sc1\n"
"s_add_u32 s52, s52, 4096\n"
"buffer_load_dwordx4 v[144:147], v1, s[4:7], s52 offen offset:0 sc1\n"
"buffer_load_dwordx4 v[148:151], v1, s[4:7], s52 offen offset:1024 sc1\n"
"buffer_load_dwordx4 v[152:155], v1, s[4:7], s52 offen offset:2048 sc1\n"
"buffer_load_dwordx4 v[156:159], v1, s[4:7], s52 offen offset:3072 sc1\n"
".Ljn_12:\n"
"s_waitcnt lgkmcnt(2)\n"
"ds_write_b32 v13, v13 offset:16\n"
"s_waitcnt vmcnt(14)\n"
"v_mfma_f32_16x16x32_bf16 v[28:31], v[96:99], v[68:71], 0\n"
"ds_read_b128 v[84:87], v3 offset:21504\n"
"s_add_u32 s52, s52, 4096\n"
"v_mfma_f32_16x16x32_bf16 v[32:35], v[100:103], v[68:71], 0\n"
"v_mfma_f32_16x16x32_bf16 v[36:39], v[96:99], v[72:75], 0\n"
"ds_read_b128 v[68:71], v3 offset:41984\n"
"buffer_load_dwordx4 v[160:163], v1, s[4:7], s52 offen offset:0 sc1\n"
"v_mfma_f32_16x16x32_bf16 v[40:43], v[100:103], v[72:75], 0\n"
"v_mfma_f32_16x16x32_bf16 v[44:47], v[96:99], v[76:79], 0\n"
"ds_read_b128 v[72:75], v95 offset:1024\n"
"v_mfma_f32_16x16x32_bf16 v[48:51], v[100:103], v[76:79], 0\n"
"buffer_load_dwordx4 v[164:167], v1, s[4:7], s52 offen offset:1024 sc1\n"
"v_mfma_f32_16x16x32_bf16 v[52:55], v[96:99], v[80:83], 0\n"
"ds_read_b128 v[76:79], v95 offset:21504\n"
"v_mfma_f32_16x16x32_bf16 v[56:59], v[100:103], v[80:83], 0\n"
"s_waitcnt vmcnt(14) lgkmcnt(2)\n"
"v_mfma_f32_16x16x32_bf16 v[28:31], v[104:107], v[84:87], v[28:31]\n"
"ds_read_b128 v[80:83], v3 offset:22528\n"
"buffer_load_dwordx4 v[168:171], v1, s[4:7], s52 offen offset:2048 sc1\n"
"v_mfma_f32_16x16x32_bf16 v[32:35], v[108:111], v[84:87], v[32:35]\n"
"v_mfma_f32_16x16x32_bf16 v[36:39], v[104:107], v[68:71], v[36:39]\n"
"ds_read_b128 v[84:87], v3 offset:43008\n"
"v_mfma_f32_16x16x32_bf16 v[40:43], v[108:111], v[68:71], v[40:43]\n"
"buffer_load_dwordx4 v[172:175], v1, s[4:7], s52 offen offset:3072 sc1\n"
"v_mov_b32 v20, -1\n"
"s_waitcnt lgkmcnt(2)\n"
"v_mfma_f32_16x16x32_bf16 v[44:47], v[104:107], v[72:75], v[44:47]\n"
"ds_read_b128 v[68:71], v95 offset:2048\n"
"s_add_u32 s52, s52, 4096\n"
"v_mov_b32 v21, -1\n"
"v_mfma_f32_16x16x32_bf16 v[48:51], v[108:111], v[72:75], v[48:51]\n"
"v_mov_b32 v22, -1\n"
"v_mov_b32 v23, -1\n"
"v_cndmask_b32 v91, 2.0, v1, s[40:41]\n"
"v_mfma_f32_16x16x32_bf16 v[52:55], v[104:107], v[76:79], v[52:55]\n"
"ds_read_b128 v[72:75], v95 offset:22528\n"
"buffer_load_dwordx4 v[176:179], v1, s[4:7], s52 offen offset:0 sc1\n"
"s_mov_b32 s33, s30\n"
"v_mfma_f32_16x16x32_bf16 v[56:59], v[108:111], v[76:79], v[56:59]\n"
"buffer_store_dwordx4 v[20:23], v1, s[4:7], s33 offen offset:0 sc1\n"
"s_waitcnt vmcnt(16) lgkmcnt(2)\n"
"v_mfma_f32_16x16x32_bf16 v[28:31], v[112:115], v[80:83], v[28:31]\n"
"ds_read_b128 v[76:79], v3 offset:23552\n"
"v_mfma_f32_16x16x32_bf16 v[32:35], v[116:119], v[80:83], v[32:35]\n"
"buffer_load_dwordx4 v[180:183], v1, s[4:7], s52 offen offset:1024 sc1\n"
"v_mfma_f32_16x16x32_bf16 v[36:39], v[112:115], v[84:87], v[36:39]\n"
"ds_read_b128 v[80:83], v3 offset:44032\n"
"v_mfma_f32_16x16x32_bf16 v[40:43], v[116:119], v[84:87], v[40:43]\n"
"s_waitcnt lgkmcnt(2)\n"
"v_mfma_f32_16x16x32_bf16 v[44:47], v[112:115], v[68:71], v[44:47]\n"
"ds_read_b128 v[84:87], v95 offset:3072\n"
"buffer_load_dwordx4 v[184:187], v1, s[4:7], s52 offen offset:2048 sc1\n"
"buffer_store_dwordx4 v[20:23], v91, s[4:7], s33 offen offset:1024 sc1\n"
"v_mfma_f32_16x16x32_bf16 v[48:51], v[116:119], v[68:71], v[48:51]\n"
"s_add_u32 s33, s30, 0x140000\n"
"ds_read_b32 v90, v14 offset:16\n"
"v_mfma_f32_16x16x32_bf16 v[52:55], v[112:115], v[72:75], v[52:55]\n"
"ds_read_b128 v[68:71], v95 offset:23552\n"
"v_mfma_f32_16x16x32_bf16 v[56:59], v[116:119], v[72:75], v[56:59]\n"
"buffer_load_dwordx4 v[188:191], v1, s[4:7], s52 offen offset:3072 sc1\n"
"s_waitcnt vmcnt(18) lgkmcnt(3)\n"
"v_mfma_f32_16x16x32_bf16 v[28:31], v[120:123], v[76:79], v[28:31]\n"
"ds_read_b128 v[72:75], v3 offset:24576\n"
"s_add_u32 s52, s52, 4096\n"
"v_mfma_f32_16x16x32_bf16 v[32:35], v[124:127], v[76:79], v[32:35]\n"
"buffer_store_dwordx4 v[20:23], v1, s[4:7], s33 offen offset:0 sc1\n"
"v_mfma_f32_16x16x32_bf16 v[36:39], v[120:123], v[80:83], v[36:39]\n"
"ds_read_b128 v[76:79], v3 offset:45056\n"
"buffer_load_dwordx4 v[192:195], v1, s[4:7], s52 offen offset:0 sc1\n"
"v_mfma_f32_16x16x32_bf16 v[40:43], v[124:127], v[80:83], v[40:43]\n"
"s_waitcnt lgkmcnt(2)\n"
"v_mfma_f32_16x16x32_bf16 v[44:47], v[120:123], v[84:87], v[44:47]\n"
"ds_read_b128 v[80:83], v95 offset:4096\n"
"v_mfma_f32_16x16x32_bf16 v[48:51], v[124:127], v[84:87], v[48:51]\n"
"buffer_load_dwordx4 v[196:199], v1, s[4:7], s52 offen offset:1024 sc1\n"
"v_mfma_f32_16x16x32_bf16 v[52:55], v[120:123], v[68:71], v[52:55]\n"
"ds_read_b128 v[84:87], v95 offset:24576\n"
"buffer_store_dwordx4 v[20:23], v91, s[4:7], s33 offen offset:1024 sc1\n"
"v_mfma_f32_16x16x32_bf16 v[56:59], v[124:127], v[68:71], v[56:59]\n"
"s_lshl_b32 s79, s2, 2\n"
"s_add_u32 s79, s79, s3\n"
"v_cmp_eq_u32 vcc, 0, v90\n"
"s_waitcnt vmcnt(20) lgkmcnt(2)\n"
"v_mfma_f32_16x16x32_bf16 v[28:31], v[128:131], v[72:75], v[28:31]\n"
"ds_read_b128 v[68:71], v3 offset:25600\n"
"buffer_load_dwordx4 v[200:203], v1, s[4:7], s52 offen offset:2048 sc1\n"
"s_lshl_b32 s79, s79, 10\n"
"v_mfma_f32_16x16x32_bf16 v[32:35], v[132:135], v[72:75], v[32:35]\n"
"s_add_u32 s33, s79, 0xbc8000\n"
"s_sub_u32 s79, 0xc27fff, s33\n"
"s_and_b32 s79, s79, 0x80000000\n"
"s_cbranch_vccnz .Lsl_26\n"
".Lbk_27:\n"
"v_mfma_f32_16x16x32_bf16 v[36:39], v[128:131], v[76:79], v[36:39]\n"
"ds_read_b128 v[72:75], v3 offset:46080\n"
"s_or_b32 s33, s33, s79\n"
"v_mfma_f32_16x16x32_bf16 v[40:43], v[132:135], v[76:79], v[40:43]\n"
"buffer_load_dwordx4 v[204:207], v1, s[4:7], s52 offen offset:3072 sc1\n"
"buffer_store_dwordx4 v[20:23], v1, s[4:7], s33 offen sc1\n"
"ds_read2st64_b32 v[60:61], v17 offset0:0 offset1:8\n"
"s_waitcnt lgkmcnt(3)\n"
"v_mfma_f32_16x16x32_bf16 v[44:47], v[128:131], v[80:83], v[44:47]\n"
"ds_read_b128 v[76:79], v95 offset:5120\n"
"s_add_u32 s52, s52, 4096\n"
"v_mfma_f32_16x16x32_bf16 v[48:51], v[132:135], v[80:83], v[48:51]\n"
"ds_read2st64_b32 v[62:63], v17 offset0:16 offset1:24\n"
"v_mfma_f32_16x16x32_bf16 v[52:55], v[128:131], v[84:87], v[52:55]\n"
"ds_read_b128 v[80:83], v95 offset:25600\n"
"buffer_load_dwordx4 v[208:211], v1, s[4:7], s52 offen offset:0 sc1\n"
"v_mfma_f32_16x16x32_bf16 v[56:59], v[132:135], v[84:87], v[56:59]\n"
"ds_read2st64_b32 v[64:65], v18 offset0:0 offset1:8\n"
"s_waitcnt vmcnt(22) lgkmcnt(5)\n"
"v_mfma_f32_16x16x32_bf16 v[28:31], v[136:139], v[68:71], v[28:31]\n"
"ds_read_b128 v[84:87], v3 offset:26624\n"
"v_mfma_f32_16x16x32_bf16 v[32:35], v[140:143], v[68:71], v[32:35]\n"
"buffer_load_dwordx4 v[212:215], v1, s[4:7], s52 offen offset:1024 sc1\n"
"ds_read2st64_b32 v[66:67], v18 offset0:16 offset1:24\n"
"v_mfma_f32_16x16x32_bf16 v[36:39], v[136:139], v[72:75], v[36:39]\n"
"ds_read_b128 v[68:71], v3 offset:47104\n"
"v_mfma_f32_16x16x32_bf16 v[40:43], v[140:143], v[72:75], v[40:43]\n"
"s_waitcnt lgkmcnt(4)\n"
"v_mfma_f32_16x16x32_bf16 v[44:47], v[136:139], v[76:79], v[44:47]\n"
"ds_read_b128 v[72:75], v95 offset:6144\n"
"buffer_load_dwordx4 v[216:219], v1, s[4:7], s52 offen offset:2048 sc1\n"
"v_mfma_f32_16x16x32_bf16 v[48:51], v[140:143], v[76:79], v[48:51]\n"
"v_mfma_f32_16x16x32_bf16 v[52:55], v[136:139], v[80:83], v[52:55]\n"
"ds_read_b128 v[76:79], v95 offset:26624\n"
"v_mfma_f32_16x16x32_bf16 v[56:59], v[140:143], v[80:83], v[56:59]\n"
"buffer_load_dwordx4 v[220:223], v1, s[4:7], s52 offen offset:3072 sc1\n"
"s_waitcnt vmcnt(23) lgkmcnt(2)\n"
"v_mfma_f32_16x16x32_bf16 v[28:31], v[144:147], v[84:87], v[28:31]\n"
"ds_read_b128 v[80:83], v3 offset:27648\n"
"s_add_u32 s52, s52, 4096\n"
"v_mfma_f32_16x16x32_bf16 v[32:35], v[148:151], v[84:87], v[32:35]\n"
"v_mfma_f32_16x16x32_bf16 v[36:39], v[144:147], v[68:71], v[36:39]\n"
"ds_read_b128 v[84:87], v3 offset:48128\n"
"buffer_load_dwordx4 v[224:227], v1, s[4:7], s52 offen offset:0 sc1\n"
"v_mfma_f32_16x16x32_bf16 v[40:43], v[148:151], v[68:71], v[40:43]\n"
"v_add_f32 v60, v60, v61\n"
"s_waitcnt lgkmcnt(2)\n"
"v_mfma_f32_16x16x32_bf16 v[44:47], v[144:147], v[72:75], v[44:47]\n"
"ds_read_b128 v[68:71], v95 offset:7168\n"
"v_add_f32 v62, v62, v63\n"
"v_mfma_f32_16x16x32_bf16 v[48:51], v[148:151], v[72:75], v[48:51]\n"
"buffer_load_dwordx4 v[228:231], v1, s[4:7], s52 offen offset:1024 sc1\n"
"v_add_f32 v64, v64, v65\n"
"v_mfma_f32_16x16x32_bf16 v[52:55], v[144:147], v[76:79], v[52:55]\n"
"ds_read_b128 v[72:75], v95 offset:27648\n"
"v_add_f32 v66, v66, v67\n"
"v_mfma_f32_16x16x32_bf16 v[56:59], v[148:151], v[76:79], v[56:59]\n"
"v_add_f32 v64, v64, v66\n"
"s_waitcnt vmcnt(23) lgkmcnt(2)\n"
"v_mfma_f32_16x16x32_bf16 v[28:31], v[152:155], v[80:83], v[28:31]\n"
"ds_read_b128 v[76:79], v3 offset:28672\n"
"buffer_load_dwordx4 v[232:235], v1, s[4:7], s53 offen offset:0 sc1\n"
"v_mfma_f32_16x16x32_bf16 v[32:35], v[156:159], v[80:83], v[32:35]\n"
"v_add_f32 v60, v60, v62\n"
"v_mul_f32 v63, 0x3acccccd, v64\n"
"v_mfma_f32_16x16x32_bf16 v[36:39], v[152:155], v[84:87], v[36:39]\n"
"ds_read_b128 v[80:83], v3 offset:49152\n"
"v_fma_f32 v60, -v64, v63, v60\n"
"v_mfma_f32_16x16x32_bf16 v[40:43], v[156:159], v[84:87], v[40:43]\n"
"buffer_load_dwordx4 v[236:239], v1, s[4:7], s53 offen offset:1024 sc1\n"
"s_waitcnt lgkmcnt(2)\n"
"v_mfma_f32_16x16x32_bf16 v[44:47], v[152:155], v[68:71], v[44:47]\n"
"ds_read_b128 v[84:87], v95 offset:8192\n"
"v_fmamk_f32 v60, v60, 0x3acccccd, v27\n"
"v_mfma_f32_16x16x32_bf16 v[48:51], v[156:159], v[68:71], v[48:51]\n"
"v_max_f32 v60, v60, v27\n"
"v_rsq_f32 v62, v60\n"
"v_mfma_f32_16x16x32_bf16 v[52:55], v[152:155], v[72:75], v[52:55]\n"
"ds_read_b128 v[68:71], v95 offset:28672\n"
"buffer_load_dwordx4 v[240:243], v1, s[4:7], s54 offen offset:0 sc1\n"
"v_mfma_f32_16x16x32_bf16 v[56:59], v[156:159], v[72:75], v[56:59]\n"
"v_mul_f32 v63, -v63, v62\n"
"s_waitcnt vmcnt(24) lgkmcnt(2)\n"
"v_mfma_f32_16x16x32_bf16 v[28:31], v[160:163], v[76:79], v[28:31]\n"
"ds_read_b128 v[72:75], v3 offset:29696\n"
"v_mfma_f32_16x16x32_bf16 v[32:35], v[164:167], v[76:79], v[32:35]\n"
"buffer_load_dwordx4 v[244:247], v1, s[4:7], s54 offen offset:1024 sc1\n"
"v_mfma_f32_16x16x32_bf16 v[36:39], v[160:163], v[80:83], v[36:39]\n"
"ds_read_b128 v[76:79], v3 offset:50176\n"
"v_mfma_f32_16x16x32_bf16 v[40:43], v[164:167], v[80:83], v[40:43]\n"
"ds_write_b64 v16, v[62:63] offset:0\n"
"s_waitcnt lgkmcnt(3)\n"
"v_mfma_f32_16x16x32_bf16 v[44:47], v[160:163], v[84:87], v[44:47]\n"
"ds_read_b128 v[80:83], v95 offset:9216\n"
"buffer_load_dwordx4 v[248:251], v1, s[4:7], s55 offen offset:0 sc1\n"
"v_mfma_f32_16x16x32_bf16 v[48:51], v[164:167], v[84:87], v[48:51]\n"
"v_mfma_f32_16x16x32_bf16 v[52:55], v[160:163], v[68:71], v[52:55]\n"
"ds_read_b128 v[84:87], v95 offset:29696\n"
"v_mfma_f32_16x16x32_bf16 v[56:59], v[164:167], v[68:71], v[56:59]\n"
"buffer_load_dwordx4 v[252:255], v1, s[4:7], s55 offen offset:1024 sc1\n"
"ds_read2st64_b32 v[60:61], v17 offset0:4 offset1:12\n"
"s_waitcnt vmcnt(25) lgkmcnt(4)\n"
"v_mfma_f32_16x16x32_bf16 v[28:31], v[168:171], v[72:75], v[28:31]\n"
"ds_read_b128 v[68:71], v3 offset:30720\n"
"s_mov_b32 m0, 163328\n"
"v_mfma_f32_16x16x32_bf16 v[32:35], v[172:175], v[72:75], v[32:35]\n"
"ds_read2st64_b32 v[62:63], v17 offset0:20 offset1:28\n"
"v_mfma_f32_16x16x32_bf16 v[36:39], v[168:171], v[76:79], v[36:39]\n"
"ds_read_b128 v[72:75], v3 offset:51200\n"
"buffer_load_dword v89, s[8:11], 0 offen lds\n"
"v_mfma_f32_16x16x32_bf16 v[40:43], v[172:175], v[76:79], v[40:43]\n"
"ds_read2st64_b32 v[64:65], v18 offset0:4 offset1:12\n"
"s_waitcnt lgkmcnt(5)\n"
"v_mfma_f32_16x16x32_bf16 v[44:47], v[168:171], v[80:83], v[44:47]\n"
"ds_read_b128 v[76:79], v95 offset:10240\n"
"ds_write_b32 v13, v13 offset:32\n"
"v_mfma_f32_16x16x32_bf16 v[48:51], v[172:175], v[80:83], v[48:51]\n"
"ds_read2st64_b32 v[66:67], v18 offset0:20 offset1:28\n"
"v_mfma_f32_16x16x32_bf16 v[52:55], v[168:171], v[84:87], v[52:55]\n"
"ds_read_b128 v[80:83], v95 offset:30720\n"
"v_mfma_f32_16x16x32_bf16 v[56:59], v[172:175], v[84:87], v[56:59]\n"
"s_waitcnt vmcnt(23) lgkmcnt(5)\n"
"v_mfma_f32_16x16x32_bf16 v[28:31], v[176:179], v[68:71], v[28:31]\n"
"ds_read_b128 v[84:87], v3 offset:31744\n"
"v_mfma_f32_16x16x32_bf16 v[32:35], v[180:183], v[68:71], v[32:35]\n"
"v_mfma_f32_16x16x32_bf16 v[36:39], v[176:179], v[72:75], v[36:39]\n"
"ds_read_b128 v[68:71], v3 offset:52224\n"
"v_mfma_f32_16x16x32_bf16 v[40:43], v[180:183], v[72:75], v[40:43]\n"
"s_waitcnt lgkmcnt(2)\n"
"v_mfma_f32_16x16x32_bf16 v[44:47], v[176:179], v[76:79], v[44:47]\n"
"ds_read_b128 v[72:75], v95 offset:11264\n"
"v_mfma_f32_16x16x32_bf16 v[48:51], v[180:183], v[76:79], v[48:51]\n"
"s_waitcnt vmcnt(13)\n"
"ds_write_b32 v13, v13 offset:48\n"
"v_mfma_f32_16x16x32_bf16 v[52:55], v[176:179], v[80:83], v[52:55]\n"
"ds_read_b128 v[76:79], v95 offset:31744\n"
"v_mfma_f32_16x16x32_bf16 v[56:59], v[180:183], v[80:83], v[56:59]\n"
"v_add_f32 v60, v60, v61\n"
"s_waitcnt lgkmcnt(3)\n"
"v_mfma_f32_16x16x32_bf16 v[28:31], v[184:187], v[84:87], v[28:31]\n"
"ds_read_b128 v[80:83], v3 offset:32768\n"
"v_add_f32 v62, v62, v63\n"
"v_mfma_f32_16x16x32_bf16 v[32:35], v[188:191], v[84:87], v[32:35]\n"
"v_add_f32 v64, v64, v65\n"
"v_add_f32 v66, v66, v67\n"
"v_mfma_f32_16x16x32_bf16 v[36:39], v[184:187], v[68:71], v[36:39]\n"
"ds_read_b128 v[84:87], v3 offset:53248\n"
"v_add_f32 v64, v64, v66\n"
"v_mfma_f32_16x16x32_bf16 v[40:43], v[188:191], v[68:71], v[40:43]\n"
"v_add_f32 v60, v60, v62\n"
"s_waitcnt lgkmcnt(2)\n"
"v_mfma_f32_16x16x32_bf16 v[44:47], v[184:187], v[72:75], v[44:47]\n"
"ds_read_b128 v[68:71], v95 offset:12288\n"
"v_mul_f32 v63, 0x3acccccd, v64\n"
"v_mfma_f32_16x16x32_bf16 v[48:51], v[188:191], v[72:75], v[48:51]\n"
"v_fma_f32 v60, -v64, v63, v60\n"
"v_fmamk_f32 v60, v60, 0x3acccccd, v27\n"
"v_mfma_f32_16x16x32_bf16 v[52:55], v[184:187], v[76:79], v[52:55]\n"
"ds_read_b128 v[72:75], v95 offset:32768\n"
"v_max_f32 v60, v60, v27\n"
"v_mfma_f32_16x16x32_bf16 v[56:59], v[188:191], v[76:79], v[56:59]\n"
"ds_read_b32 v90, v14 offset:48\n"
"s_waitcnt lgkmcnt(3)\n"
"v_mfma_f32_16x16x32_bf16 v[28:31], v[192:195], v[80:83], v[28:31]\n"
"ds_read_b128 v[76:79], v3 offset:33792\n"
"v_rsq_f32 v62, v60\n"
"v_mfma_f32_16x16x32_bf16 v[32:35], v[196:199], v[80:83], v[32:35]\n"
"v_mul_f32 v63, -v63, v62\n"
"v_cndmask_b32 v16, 2.0, v16, s[40:41]\n"
"v_mfma_f32_16x16x32_bf16 v[36:39], v[192:195], v[84:87], v[36:39]\n"
"ds_read_b128 v[80:83], v3 offset:54272\n"
"v_mfma_f32_16x16x32_bf16 v[40:43], v[196:199], v[84:87], v[40:43]\n"
"s_waitcnt lgkmcnt(3)\n"
"v_mfma_f32_16x16x32_bf16 v[44:47], v[192:195], v[68:71], v[44:47]\n"
"ds_read_b128 v[84:87], v95 offset:13312\n"
"v_mfma_f32_16x16x32_bf16 v[48:51], v[196:199], v[68:71], v[48:51]\n"
"v_mfma_f32_16x16x32_bf16 v[52:55], v[192:195], v[72:75], v[52:55]\n"
"ds_read_b128 v[68:71], v95 offset:33792\n"
"v_mfma_f32_16x16x32_bf16 v[56:59], v[196:199], v[72:75], v[56:59]\n"
"s_waitcnt lgkmcnt(4)\n"
"v_cmp_eq_u32 vcc, 0, v90\n"
"ds_write_b64 v16, v[62:63] offset:512\n"
"s_waitcnt lgkmcnt(3)\n"
"v_mfma_f32_16x16x32_bf16 v[28:31], v[200:203], v[76:79], v[28:31]\n"
"ds_read_b128 v[72:75], v3 offset:34816\n"
"v_mfma_f32_16x16x32_bf16 v[32:35], v[204:207], v[76:79], v[32:35]\n"
"s_cbranch_vccnz .Lsl_18\n"
".Lbk_19:\n"
"ds_write_b32 v13, v13 offset:80\n"
"v_mfma_f32_16x16x32_bf16 v[36:39], v[200:203], v[80:83], v[36:39]\n"
"ds_read_b128 v[76:79], v3 offset:55296\n"
"v_lshrrev_b32 v19, 3, v0\n"
"v_mfma_f32_16x16x32_bf16 v[40:43], v[204:207], v[80:83], v[40:43]\n"
"v_add_u32 v19, s3, v19\n"
"s_waitcnt lgkmcnt(4)\n"
"v_mfma_f32_16x16x32_bf16 v[44:47], v[200:203], v[84:87], v[44:47]\n"
"ds_read_b128 v[80:83], v95 offset:14336\n"
"v_lshlrev_b32 v91, 27, v19\n"
"v_mfma_f32_16x16x32_bf16 v[48:51], v[204:207], v[84:87], v[48:51]\n"
"v_lshl_add_u32 v91, v0, 14, v91\n"
"v_mov_b32 v19, s16\n"
"v_mfma_f32_16x16x32_bf16 v[52:55], v[200:203], v[68:71], v[52:55]\n"
"ds_read_b128 v[84:87], v95 offset:34816\n"
"s_lshr_b32 s79, s2, 2\n"
"v_mfma_f32_16x16x32_bf16 v[56:59], v[204:207], v[68:71], v[56:59]\n"
"s_lshl_b32 s79, s79, 5\n"
"s_waitcnt vmcnt(11) lgkmcnt(2)\n"
"v_mfma_f32_16x16x32_bf16 v[28:31], v[208:211], v[72:75], v[28:31]\n"
"ds_read_b128 v[68:71], v3 offset:35840\n"
"s_and_b32 s78, s2, 3\n"
"v_mfma_f32_16x16x32_bf16 v[32:35], v[212:215], v[72:75], v[32:35]\n"
"s_lshl_b32 s78, s78, 2\n"
"s_add_u32 s79, s79, s78\n"
"v_mfma_f32_16x16x32_bf16 v[36:39], v[208:211], v[76:79], v[36:39]\n"
"ds_read_b128 v[72:75], v3 offset:56320\n"
"s_add_u32 s32, s79, 0x4000\n"
"v_mfma_f32_16x16x32_bf16 v[40:43], v[212:215], v[76:79], v[40:43]\n"
"buffer_store_dword v19, v91, s[4:7], s32 offen sc1\n"
"s_waitcnt lgkmcnt(2)\n"
"v_mfma_f32_16x16x32_bf16 v[44:47], v[208:211], v[80:83], v[44:47]\n"
"ds_read_b128 v[76:79], v95 offset:15360\n"
"v_mfma_f32_16x16x32_bf16 v[48:51], v[212:215], v[80:83], v[48:51]\n"
"v_mfma_f32_16x16x32_bf16 v[52:55], v[208:211], v[84:87], v[52:55]\n"
"ds_read_b128 v[80:83], v95 offset:35840\n"
"v_mfma_f32_16x16x32_bf16 v[56:59], v[212:215], v[84:87], v[56:59]\n"
"s_waitcnt vmcnt(10) lgkmcnt(2)\n"
"v_mfma_f32_16x16x32_bf16 v[28:31], v[216:219], v[68:71], v[28:31]\n"
"ds_read_b128 v[84:87], v3 offset:36864\n"
"v_mfma_f32_16x16x32_bf16 v[32:35], v[220:223], v[68:71], v[32:35]\n"
"ds_read_b32 v90, v14 offset:80\n"
"v_mfma_f32_16x16x32_bf16 v[36:39], v[216:219], v[72:75], v[36:39]\n"
"ds_read_b128 v[68:71], v4 offset:40960\n"
"v_mfma_f32_16x16x32_bf16 v[40:43], v[220:223], v[72:75], v[40:43]\n"
"s_waitcnt lgkmcnt(3)\n"
"v_mfma_f32_16x16x32_bf16 v[44:47], v[216:219], v[76:79], v[44:47]\n"
"ds_read_b128 v[72:75], v5 offset:40960\n"
"v_mfma_f32_16x16x32_bf16 v[48:51], v[220:223], v[76:79], v[48:51]\n"
"ds_read_b64 v[88:89], v94 offset:0\n"
"v_mfma_f32_16x16x32_bf16 v[52:55], v[216:219], v[80:83], v[52:55]\n"
"ds_read_b128 v[76:79], v6 offset:40960\n"
"v_mfma_f32_16x16x32_bf16 v[56:59], v[220:223], v[80:83], v[56:59]\n"
"s_waitcnt vmcnt(6) lgkmcnt(3)\n"
"v_mfma_f32_16x16x32_bf16 v[28:31], v[224:227], v[84:87], v[28:31]\n"
"ds_read_b128 v[80:83], v3 offset:57344\n"
"v_mfma_f32_16x16x32_bf16 v[32:35], v[228:231], v[84:87], v[32:35]\n"
"ds_read_b64 v[92:93], v94 offset:128\n"
"v_mfma_f32_16x16x32_bf16 v[28:31], v[232:235], v[68:71], v[28:31]\n"
"ds_read_b128 v[84:87], v4 offset:61440\n"
"v_mfma_f32_16x16x32_bf16 v[32:35], v[236:239], v[68:71], v[32:35]\n"
"v_cmp_eq_u32 vcc, 0, v90\n"
"s_waitcnt vmcnt(2) lgkmcnt(3)\n"
"v_mfma_f32_16x16x32_bf16 v[28:31], v[240:243], v[72:75], v[28:31]\n"
"ds_read_b128 v[68:71], v5 offset:61440\n"
"v_mfma_f32_16x16x32_bf16 v[32:35], v[244:247], v[72:75], v[32:35]\n"
"s_cbranch_vccnz .Lsl_30\n"
".Lbk_31:\n"
"v_mfma_f32_16x16x32_bf16 v[28:31], v[248:251], v[76:79], v[28:31]\n"
"ds_read_b128 v[72:75], v6 offset:61440\n"
"v_mfma_f32_16x16x32_bf16 v[32:35], v[252:255], v[76:79], v[32:35]\n"
"s_waitcnt lgkmcnt(2)\n"
"v_mfma_f32_16x16x32_bf16 v[36:39], v[224:227], v[80:83], v[36:39]\n"
"ds_read_b128 v[76:79], v95 offset:16384\n"
"v_mfma_f32_16x16x32_bf16 v[40:43], v[228:231], v[80:83], v[40:43]\n"
"v_mov_b32_dpp v16, v28 row_newbcast:0 row_mask:0xf bank_mask:0xf\n"
"v_mov_b32_dpp v17, v29 row_newbcast:0 row_mask:0xf bank_mask:0xf\n"
"v_mov_b32_dpp v18, v30 row_newbcast:0 row_mask:0xf bank_mask:0xf\n"
"v_mfma_f32_16x16x32_bf16 v[36:39], v[232:235], v[84:87], v[36:39]\n"
"v_add_u32 v4, 0x1c400, v4\n"
"ds_read_b128 v[80:83], v4 offset:0\n"
"v_mov_b32_dpp v19, v31 row_newbcast:0 row_mask:0xf bank_mask:0xf\n"
"v_mfma_f32_16x16x32_bf16 v[40:43], v[236:239], v[84:87], v[40:43]\n"
"v_mov_b32_dpp v20, v32 row_newbcast:0 row_mask:0xf bank_mask:0xf\n"
"v_mov_b32_dpp v21, v33 row_newbcast:0 row_mask:0xf bank_mask:0xf\n"
"s_waitcnt lgkmcnt(2)\n"
"v_mfma_f32_16x16x32_bf16 v[36:39], v[240:243], v[68:71], v[36:39]\n"
"v_add_u32 v5, 0x1c400, v5\n"
"ds_read_b128 v[84:87], v5 offset:0\n"
"v_mfma_f32_16x16x32_bf16 v[40:43], v[244:247], v[68:71], v[40:43]\n"
"v_mov_b32_dpp v22, v34 row_newbcast:0 row_mask:0xf bank_mask:0xf\n"
"v_mov_b32_dpp v23, v35 row_newbcast:0 row_mask:0xf bank_mask:0xf\n"
"v_mul_f32 v28, v88, v28\n"
"v_mfma_f32_16x16x32_bf16 v[36:39], v[248:251], v[72:75], v[36:39]\n"
"v_add_u32 v6, 0x1c400, v6\n"
"ds_read_b128 v[68:71], v6 offset:0\n"
"v_mul_f32 v29, v88, v29\n"
"v_mfma_f32_16x16x32_bf16 v[40:43], v[252:255], v[72:75], v[40:43]\n"
"v_mul_f32 v30, v88, v30\n"
"v_mul_f32 v31, v88, v31\n"
"s_waitcnt lgkmcnt(2)\n"
"v_mfma_f32_16x16x32_bf16 v[44:47], v[224:227], v[76:79], v[44:47]\n"
"ds_read_b128 v[72:75], v95 offset:36864\n"
"v_fmac_f32 v28, v89, v16\n"
"v_fmac_f32 v29, v89, v17\n"
"v_mfma_f32_16x16x32_bf16 v[48:51], v[228:231], v[76:79], v[48:51]\n"
"ds_write_b32 v13, v13 offset:64\n"
"v_fmac_f32 v30, v89, v18\n"
"v_mfma_f32_16x16x32_bf16 v[44:47], v[232:235], v[80:83], v[44:47]\n"
"ds_read_b128 v[76:79], v4 offset:20480\n"
"v_fmac_f32 v31, v89, v19\n"
"v_cvt_pk_bf16_f32 v28, v28, v29\n"
"v_mfma_f32_16x16x32_bf16 v[48:51], v[236:239], v[80:83], v[48:51]\n"
"v_cvt_pk_bf16_f32 v29, v30, v31\n"
"ds_write_b64 v7, v[28:29] offset:0\n"
"s_waitcnt lgkmcnt(4)\n"
"v_mfma_f32_16x16x32_bf16 v[44:47], v[240:243], v[84:87], v[44:47]\n"
"ds_read_b128 v[80:83], v5 offset:20480\n"
"v_mul_f32 v32, v88, v32\n"
"v_mul_f32 v33, v88, v33\n"
"v_mfma_f32_16x16x32_bf16 v[48:51], v[244:247], v[84:87], v[48:51]\n"
"v_mul_f32 v34, v88, v34\n"
"v_mul_f32 v35, v88, v35\n"
"v_fmac_f32 v32, v89, v20\n"
"v_mfma_f32_16x16x32_bf16 v[44:47], v[248:251], v[68:71], v[44:47]\n"
"ds_read_b128 v[84:87], v6 offset:20480\n"
"v_fmac_f32 v33, v89, v21\n"
"v_fmac_f32 v34, v89, v22\n"
"v_mfma_f32_16x16x32_bf16 v[48:51], v[252:255], v[68:71], v[48:51]\n"
"v_fmac_f32 v35, v89, v23\n"
"v_cvt_pk_bf16_f32 v32, v32, v33\n"
"s_waitcnt lgkmcnt(3)\n"
"v_mfma_f32_16x16x32_bf16 v[52:55], v[224:227], v[72:75], v[52:55]\n"
"v_add_u32 v3, 0xffffb000, v3\n"
"ds_read_b128 v[68:71], v3 offset:0\n"
"v_cvt_pk_bf16_f32 v33, v34, v35\n"
"v_mfma_f32_16x16x32_bf16 v[56:59], v[228:231], v[72:75], v[56:59]\n"
"ds_read_b32 v90, v14 offset:64\n"
"ds_write_b64 v8, v[32:33] offset:0\n"
"v_mfma_f32_16x16x32_bf16 v[52:55], v[232:235], v[76:79], v[52:55]\n"
"ds_read_b128 v[72:75], v3 offset:1024\n"
"ds_read_b64 v[88:89], v94 offset:256\n"
"v_mul_f32 v36, v92, v36\n"
"v_mfma_f32_16x16x32_bf16 v[56:59], v[236:239], v[76:79], v[56:59]\n"
"v_mul_f32 v37, v92, v37\n"
"v_mul_f32 v38, v92, v38\n"
"s_waitcnt lgkmcnt(5)\n"
"v_mfma_f32_16x16x32_bf16 v[52:55], v[240:243], v[80:83], v[52:55]\n"
"ds_read_b128 v[76:79], v3 offset:2048\n"
"v_mul_f32 v39, v92, v39\n"
"v_fmac_f32 v36, v93, v16\n"
"v_mfma_f32_16x16x32_bf16 v[56:59], v[244:247], v[80:83], v[56:59]\n"
"v_fmac_f32 v37, v93, v17\n"
"v_fmac_f32 v38, v93, v18\n"
"v_fmac_f32 v39, v93, v19\n"
"v_mfma_f32_16x16x32_bf16 v[52:55], v[248:251], v[84:87], v[52:55]\n"
"ds_read_b128 v[80:83], v3 offset:3072\n"
"v_cvt_pk_bf16_f32 v36, v36, v37\n"
"v_cvt_pk_bf16_f32 v37, v38, v39\n"
"v_mfma_f32_16x16x32_bf16 v[56:59], v[252:255], v[84:87], v[56:59]\n"
"ds_write_b64 v7, v[36:37] offset:3968\n"
"s_waitcnt lgkmcnt(4)\n"
"v_mfma_f32_16x16x32_bf16 v[60:63], v[96:99], v[68:71], 0\n"
"ds_read_b128 v[84:87], v3 offset:4096\n"
"v_mul_f32 v40, v92, v40\n"
"v_mul_f32 v41, v92, v41\n"
"v_mfma_f32_16x16x32_bf16 v[64:67], v[100:103], v[68:71], 0\n"
"v_cmp_eq_u32 vcc, 0, v90\n"
"v_mul_f32 v42, v92, v42\n"
"v_mul_f32 v43, v92, v43\n"
"v_mfma_f32_16x16x32_bf16 v[60:63], v[104:107], v[72:75], v[60:63]\n"
"ds_read_b128 v[68:71], v3 offset:5120\n"
"v_fmac_f32 v40, v93, v20\n"
"v_fmac_f32 v41, v93, v21\n"
"v_mfma_f32_16x16x32_bf16 v[64:67], v[108:111], v[72:75], v[64:67]\n"
"s_cbranch_vccnz .Lsl_22\n"
".Lbk_23:\n"
"s_waitcnt lgkmcnt(3)\n"
"v_mfma_f32_16x16x32_bf16 v[60:63], v[112:115], v[76:79], v[60:63]\n"
"ds_read_b128 v[72:75], v3 offset:6144\n"
"v_fmac_f32 v42, v93, v22\n"
"v_fmac_f32 v43, v93, v23\n"
"v_mfma_f32_16x16x32_bf16 v[64:67], v[116:119], v[76:79], v[64:67]\n"
"v_cvt_pk_bf16_f32 v40, v40, v41\n"
"v_cvt_pk_bf16_f32 v41, v42, v43\n"
"ds_write_b64 v8, v[40:41] offset:3968\n"
"v_mfma_f32_16x16x32_bf16 v[60:63], v[120:123], v[80:83], v[60:63]\n"
"ds_read_b128 v[76:79], v3 offset:7168\n"
"ds_read_b64 v[92:93], v94 offset:384\n"
"v_mul_f32 v44, v88, v44\n"
"v_mfma_f32_16x16x32_bf16 v[64:67], v[124:127], v[80:83], v[64:67]\n"
"s_add_u32 s31, s28, 0x140000\n"
"v_mul_f32 v45, v88, v45\n"
"s_waitcnt lgkmcnt(4)\n"
"v_mfma_f32_16x16x32_bf16 v[60:63], v[128:131], v[84:87], v[60:63]\n"
"ds_read_b128 v[80:83], v3 offset:8192\n"
"s_add_u32 m0, s29, 0xa000\n"
"v_mul_f32 v46, v88, v46\n"
"v_mfma_f32_16x16x32_bf16 v[64:67], v[132:135], v[84:87], v[64:67]\n"
"buffer_load_dwordx4 v2, s[8:11], s31 offen offset:0 lds\n"
"v_mul_f32 v47, v88, v47\n"
"v_fmac_f32 v44, v89, v16\n"
"v_mfma_f32_16x16x32_bf16 v[60:63], v[136:139], v[68:71], v[60:63]\n"
"ds_read_b128 v[84:87], v3 offset:9216\n"
"s_add_u32 m0, s29, 0xa3c0\n"
"v_fmac_f32 v45, v89, v17\n"
"v_mfma_f32_16x16x32_bf16 v[64:67], v[140:143], v[68:71], v[64:67]\n"
"v_fmac_f32 v46, v89, v18\n"
"v_fmac_f32 v47, v89, v19\n"
"s_waitcnt lgkmcnt(3)\n"
"v_mfma_f32_16x16x32_bf16 v[60:63], v[144:147], v[72:75], v[60:63]\n"
"ds_read_b128 v[68:71], v3 offset:10240\n"
"buffer_load_dwordx4 v2, s[8:11], s31 offen offset:64 lds\n"
"v_cvt_pk_bf16_f32 v44, v44, v45\n"
"v_mfma_f32_16x16x32_bf16 v[64:67], v[148:151], v[72:75], v[64:67]\n"
"s_add_u32 m0, s29, 0xa780\n"
"v_cvt_pk_bf16_f32 v45, v46, v47\n"
"ds_write_b64 v7, v[44:45] offset:7936\n"
"v_mfma_f32_16x16x32_bf16 v[60:63], v[152:155], v[76:79], v[60:63]\n"
"ds_read_b128 v[72:75], v3 offset:11264\n"
"v_mul_f32 v48, v88, v48\n"
"v_mul_f32 v49, v88, v49\n"
"v_mfma_f32_16x16x32_bf16 v[64:67], v[156:159], v[76:79], v[64:67]\n"
"buffer_load_dwordx4 v2, s[8:11], s31 offen offset:128 lds\n"
"v_mul_f32 v50, v88, v50\n"
"s_waitcnt lgkmcnt(3)\n"
"v_mfma_f32_16x16x32_bf16 v[60:63], v[160:163], v[80:83], v[60:63]\n"
"ds_read_b128 v[76:79], v3 offset:12288\n"
"s_add_u32 m0, s29, 0xab40\n"
"v_mul_f32 v51, v88, v51\n"
"v_mfma_f32_16x16x32_bf16 v[64:67], v[164:167], v[80:83], v[64:67]\n"
"v_fmac_f32 v48, v89, v20\n"
"v_fmac_f32 v49, v89, v21\n"
"v_fmac_f32 v50, v89, v22\n"
"v_mfma_f32_16x16x32_bf16 v[60:63], v[168:171], v[84:87], v[60:63]\n"
"ds_read_b128 v[80:83], v3 offset:13312\n"
"buffer_load_dwordx4 v2, s[8:11], s31 offen offset:192 lds\n"
"v_fmac_f32 v51, v89, v23\n"
"v_mfma_f32_16x16x32_bf16 v[64:67], v[172:175], v[84:87], v[64:67]\n"
"s_add_u32 m0, s29, 0xaf00\n"
"v_cvt_pk_bf16_f32 v48, v48, v49\n"
"s_waitcnt lgkmcnt(2)\n"
"v_mfma_f32_16x16x32_bf16 v[60:63], v[176:179], v[68:71], v[60:63]\n"
"ds_read_b128 v[84:87], v3 offset:14336\n"
"v_cvt_pk_bf16_f32 v49, v50, v51\n"
"ds_write_b64 v8, v[48:49] offset:7936\n"
"v_mfma_f32_16x16x32_bf16 v[64:67], v[180:183], v[68:71], v[64:67]\n"
"buffer_load_dwordx4 v2, s[8:11], s31 offen offset:256 lds\n"
"ds_read_b64 v[88:89], v94 offset:512\n"
"v_mul_f32 v52, v92, v52\n"
"v_mfma_f32_16x16x32_bf16 v[60:63], v[184:187], v[72:75], v[60:63]\n"
"ds_read_b128 v[68:71], v3 offset:15360\n"
"s_add_u32 s31, s28, 0x145000\n"
"v_mul_f32 v53, v92, v53\n"
"v_mfma_f32_16x16x32_bf16 v[64:67], v[188:191], v[72:75], v[64:67]\n"
"s_add_u32 m0, s29, 0xf000\n"
"v_mul_f32 v54, v92, v54\n"
"s_waitcnt lgkmcnt(4)\n"
"v_mfma_f32_16x16x32_bf16 v[60:63], v[192:195], v[76:79], v[60:63]\n"
"ds_read_b128 v[72:75], v3 offset:16384\n"
"buffer_load_dwordx4 v2, s[8:11], s31 offen offset:0 lds\n"
"v_mul_f32 v55, v92, v55\n"
"v_mfma_f32_16x16x32_bf16 v[64:67], v[196:199], v[76:79], v[64:67]\n"
"s_add_u32 m0, s29, 0xf3c0\n"
"v_fmac_f32 v52, v93, v16\n"
"v_fmac_f32 v53, v93, v17\n"
"v_mfma_f32_16x16x32_bf16 v[60:63], v[200:203], v[80:83], v[60:63]\n"
"v_add_u32 v4, 0xfffe3c00, v4\n"
"ds_read_b128 v[76:79], v4 offset:0\n"
"v_fmac_f32 v54, v93, v18\n"
"v_mfma_f32_16x16x32_bf16 v[64:67], v[204:207], v[80:83], v[64:67]\n"
"buffer_load_dwordx4 v2, s[8:11], s31 offen offset:64 lds\n"
"v_fmac_f32 v55, v93, v19\n"
"s_waitcnt lgkmcnt(2)\n"
"v_mfma_f32_16x16x32_bf16 v[60:63], v[208:211], v[84:87], v[60:63]\n"
"v_add_u32 v5, 0xfffe3c00, v5\n"
"ds_read_b128 v[80:83], v5 offset:0\n"
"v_cvt_pk_bf16_f32 v52, v52, v53\n"
"v_mfma_f32_16x16x32_bf16 v[64:67], v[212:215], v[84:87], v[64:67]\n"
"s_add_u32 m0, s29, 0xf780\n"
"v_cvt_pk_bf16_f32 v53, v54, v55\n"
"ds_write_b64 v7, v[52:53] offset:11904\n"
"v_mfma_f32_16x16x32_bf16 v[60:63], v[216:219], v[68:71], v[60:63]\n"
"v_add_u32 v6, 0xfffe3c00, v6\n"
"ds_read_b128 v[84:87], v6 offset:0\n"
"buffer_load_dwordx4 v2, s[8:11], s31 offen offset:128 lds\n"
"v_mfma_f32_16x16x32_bf16 v[64:67], v[220:223], v[68:71], v[64:67]\n"
"s_add_u32 m0, s29, 0xfb40\n"
"v_mul_f32 v56, v92, v56\n"
"s_waitcnt lgkmcnt(3)\n"
"v_mfma_f32_16x16x32_bf16 v[60:63], v[224:227], v[72:75], v[60:63]\n"
"v_mul_f32 v57, v92, v57\n"
"v_mul_f32 v58, v92, v58\n"
"v_mul_f32 v59, v92, v59\n"
"v_mfma_f32_16x16x32_bf16 v[64:67], v[228:231], v[72:75], v[64:67]\n"
"buffer_load_dwordx4 v2, s[8:11], s31 offen offset:192 lds\n"
"v_fmac_f32 v56, v93, v20\n"
"v_fmac_f32 v57, v93, v21\n"
"v_mfma_f32_16x16x32_bf16 v[60:63], v[232:235], v[76:79], v[60:63]\n"
"s_add_u32 m0, s29, 0xff00\n"
"v_fmac_f32 v58, v93, v22\n"
"v_mfma_f32_16x16x32_bf16 v[64:67], v[236:239], v[76:79], v[64:67]\n"
"v_fmac_f32 v59, v93, v23\n"
"s_waitcnt lgkmcnt(0)\n"
"v_mfma_f32_16x16x32_bf16 v[60:63], v[240:243], v[80:83], v[60:63]\n"
"buffer_load_dwordx4 v2, s[8:11], s31 offen offset:256 lds\n"
"v_cvt_pk_bf16_f32 v56, v56, v57\n"
"v_mfma_f32_16x16x32_bf16 v[64:67], v[244:247], v[80:83], v[64:67]\n"
"v_cvt_pk_bf16_f32 v57, v58, v59\n"
"ds_write_b64 v8, v[56:57] offset:11904\n"
"v_mfma_f32_16x16x32_bf16 v[60:63], v[248:251], v[84:87], v[60:63]\n"
"v_mfma_f32_16x16x32_bf16 v[64:67], v[252:255], v[84:87], v[64:67]\n"
"s_nop 4\n"
"v_mul_f32 v60, v88, v60\n"
"v_mul_f32 v61, v88, v61\n"
"v_mul_f32 v62, v88, v62\n"
"v_mul_f32 v63, v88, v63\n"
"v_fmac_f32 v60, v89, v16\n"
"v_fmac_f32 v61, v89, v17\n"
"v_fmac_f32 v62, v89, v18\n"
"v_fmac_f32 v63, v89, v19\n"
"v_cvt_pk_bf16_f32 v60, v60, v61\n"
"v_cvt_pk_bf16_f32 v61, v62, v63\n"
"ds_write_b64 v7, v[60:61] offset:15872\n"
"v_mul_f32 v64, v88, v64\n"
"v_mul_f32 v65, v88, v65\n"
"v_mul_f32 v66, v88, v66\n"
"v_mul_f32 v67, v88, v67\n"
"v_fmac_f32 v64, v89, v20\n"
"v_fmac_f32 v65, v89, v21\n"
"v_fmac_f32 v66, v89, v22\n"
"v_fmac_f32 v67, v89, v23\n"
"v_cvt_pk_bf16_f32 v64, v64, v65\n"
"v_cvt_pk_bf16_f32 v65, v66, v67\n"
"ds_write_b64 v8, v[64:65] offset:15872\n"
"ds_write_b32 v13, v13 offset:96\n"
"s_cmp_lt_i32 s3, 2\n"
"s_cselect_b32 s33, 1, 0\n"
"v_mov_b32 v46, 0\n"
"v_mov_b32 v47, 0\n"
"v_mov_b32 v50, 0\n"
"v_mov_b32 v51, 0\n"
"v_mov_b32 v72, s42\n"
"v_mov_b32 v73, s42\n"
"v_mov_b32 v74, s42\n"
"v_mov_b32 v75, s42\n"
"s_mov_b32 s17, 0x100000\n"
".Ltp_34:\n"
"ds_read_b32 v90, v14 offset:96\n"
"s_sub_u32 s17, s17, 1\n"
"s_cbranch_scc1 .Lou_35\n"
"s_waitcnt lgkmcnt(0)\n"
"v_cmp_eq_u32 vcc, 0, v90\n"
"s_cbranch_vccnz .Ltp_34\n"
".Lou_35:\n"
"s_waitcnt lgkmcnt(0)\n"
"s_mov_b64 exec, 0xffff\n"
"ds_read_b64 v[46:47], v24 offset:4000\n"
"ds_read_b64 v[50:51], v24 offset:4040\n"
"s_mov_b64 exec, -1\n"
"ds_read_b64 v[44:45], v25 offset:3968\n"
"ds_read_b64 v[56:57], v25 offset:80\n"
"ds_read_b64 v[58:59], v24 offset:112\n"
"ds_read_b64 v[64:65], v25 offset:4048\n"
"ds_read_b64 v[66:67], v24 offset:4080\n"
"ds_read_b64 v[48:49], v25 offset:4008\n"
"ds_read_b64 v[60:61], v25 offset:120\n"
"ds_read_b64 v[62:63], v24 offset:152\n"
"ds_read_b64 v[68:69], v25 offset:4088\n"
"ds_read_b64 v[70:71], v24 offset:4120\n"
"s_add_u32 s31, s28, 0x14a000\n"
"s_add_u32 m0, s29, 0\n"
"s_nop 0\n"
"buffer_load_dwordx4 v2, s[8:11], s31 offen offset:0 lds\n"
"ds_read_b64_tr_b16 v[28:29], v26 offset:0\n"
"ds_read_b64_tr_b16 v[30:31], v26 offset:3968\n"
"ds_read_b64_tr_b16 v[32:33], v26 offset:40\n"
"v_mov_b32 v52, 0x0c0c0c0c\n"
"v_mov_b32 v53, 0xc0c0100\n"
"v_mov_b32 v54, 0x1000c0c\n"
"v_cndmask_b32 v53, v53, v54, s[34:35]\n"
"v_cndmask_b32 v80, v53, v52, s[36:37]\n"
"v_cndmask_b32 v82, v52, v53, s[36:37]\n"
"v_mov_b32 v53, 0xc0c0302\n"
"v_mov_b32 v54, 0x3020c0c\n"
"v_cndmask_b32 v53, v53, v54, s[34:35]\n"
"v_cndmask_b32 v81, v53, v52, s[38:39]\n"
"v_cndmask_b32 v83, v52, v53, s[38:39]\n"
"s_waitcnt lgkmcnt(10)\n"
"v_mfma_f32_16x16x32_bf16 v[56:59], v[56:59], v[44:47], v[72:75]\n"
"s_waitcnt lgkmcnt(8)\n"
"v_mfma_f32_16x16x32_bf16 v[64:67], v[64:67], v[44:47], 0\n"
"ds_read_b64_tr_b16 v[34:35], v26 offset:4008\n"
"s_add_u32 m0, s29, 0x3c0\n"
"s_nop 0\n"
"buffer_load_dwordx4 v2, s[8:11], s31 offen offset:64 lds\n"
"ds_read_b64_tr_b16 v[36:37], v26 offset:32\n"
"ds_read_b64_tr_b16 v[38:39], v26 offset:4000\n"
"s_waitcnt lgkmcnt(8)\n"
"v_mfma_f32_16x16x32_bf16 v[60:63], v[60:63], v[48:51], v[72:75]\n"
"s_waitcnt lgkmcnt(6)\n"
"v_mfma_f32_16x16x32_bf16 v[68:71], v[68:71], v[48:51], 0\n"
"ds_read_b64_tr_b16 v[40:41], v26 offset:72\n"
"ds_read_b64_tr_b16 v[42:43], v26 offset:4040\n"
"v_mov_b32 v84, 0x3f803f80\n"
"v_mov_b32 v85, 0x3f803f80\n"
"v_mov_b32 v86, 0x3f803f80\n"
"v_mov_b32 v87, 0x3f803f80\n"
"v_cndmask_b32 v76, v56, v57, s[34:35]\n"
"v_cndmask_b32 v72, v64, v65, s[34:35]\n"
"v_cndmask_b32 v76, v76, v72, s[36:37]\n"
"v_cndmask_b32 v77, v58, v59, s[34:35]\n"
"v_cndmask_b32 v72, v66, v67, s[34:35]\n"
"v_cndmask_b32 v77, v77, v72, s[38:39]\n"
"v_max_f32 v52, v76, v77\n"
"s_add_u32 m0, s29, 0x780\n"
"s_nop 0\n"
"buffer_load_dwordx4 v2, s[8:11], s31 offen offset:128 lds\n"
"v_max_f32 v54, v76, v77\n"
"v_cndmask_b32 v78, v60, v61, s[34:35]\n"
"v_cndmask_b32 v72, v68, v69, s[34:35]\n"
"v_cndmask_b32 v78, v78, v72, s[36:37]\n"
"v_cndmask_b32 v79, v62, v63, s[34:35]\n"
"v_cndmask_b32 v72, v70, v71, s[34:35]\n"
"v_cndmask_b32 v79, v79, v72, s[38:39]\n"
"v_max_f32 v53, v78, v79\n"
"v_max_f32 v55, v78, v79\n"
"v_permlane16_swap_b32 v52, v54\n"
"s_nop 0\n"
"v_permlane16_swap_b32 v53, v55\n"
"v_max_f32 v72, v52, v54\n"
"v_max_f32 v52, v52, v54\n"
"v_max_f32 v73, v53, v55\n"
"v_max_f32 v53, v53, v55\n"
"v_permlane32_swap_b32 v52, v72\n"
"s_nop 0\n"
"v_permlane32_swap_b32 v53, v73\n"
"s_add_u32 m0, s29, 0xb40\n"
"s_nop 0\n"
"buffer_load_dwordx4 v2, s[8:11], s31 offen offset:192 lds\n"
"v_max_f32 v52, v52, v72\n"
"v_max_f32 v53, v53, v73\n"
"v_mul_f32 v52, 0xbea52b5b, v52\n"
"v_mul_f32 v53, 0xbea52b5b, v53\n"
"v_fmamk_f32 v76, v76, 0x3ea52b5b, v52\n"
"v_fmamk_f32 v77, v77, 0x3ea52b5b, v52\n"
"v_fmamk_f32 v78, v78, 0x3ea52b5b, v53\n"
"v_fmamk_f32 v79, v79, 0x3ea52b5b, v53\n"
"v_exp_f32 v76, v76\n"
"v_exp_f32 v77, v77\n"
"v_exp_f32 v78, v78\n"
"v_exp_f32 v79, v79\n"
"v_cvt_pk_bf16_f32 v52, v76, v77\n"
"v_cvt_pk_bf16_f32 v53, v78, v79\n"
"v_perm_b32 v44, v52, v52, v80\n"
"v_perm_b32 v45, v52, v52, v81\n"
"v_perm_b32 v46, v52, v52, v82\n"
"s_add_u32 m0, s29, 0xf00\n"
"s_nop 0\n"
"buffer_load_dwordx4 v2, s[8:11], s31 offen offset:256 lds\n"
"v_perm_b32 v47, v52, v52, v83\n"
"v_perm_b32 v48, v53, v53, v80\n"
"v_perm_b32 v49, v53, v53, v81\n"
"v_perm_b32 v50, v53, v53, v82\n"
"v_perm_b32 v51, v53, v53, v83\n"
"s_waitcnt lgkmcnt(0)\n"
"s_waitcnt vmcnt(5)\n"
"ds_write_b32 v13, v13 offset:112\n"
"v_mfma_f32_16x16x32_bf16 v[56:59], v[84:87], v[44:47], 0\n"
"v_mfma_f32_16x16x32_bf16 v[60:63], v[84:87], v[48:51], 0\n"
"v_mfma_f32_16x16x32_bf16 v[28:31], v[28:31], v[44:47], 0\n"
"v_mfma_f32_16x16x32_bf16 v[32:35], v[32:35], v[48:51], 0\n"
"v_mfma_f32_16x16x32_bf16 v[36:39], v[36:39], v[44:47], 0\n"
"v_mfma_f32_16x16x32_bf16 v[40:43], v[40:43], v[48:51], 0\n"
"v_cndmask_b32 v88, 2.0, v1, s[40:41]\n"
"v_rcp_f32 v52, v56\n"
"v_rcp_f32 v53, v60\n"
"s_mov_b32 s17, 0x100000\n"
".Ltp_36:\n"
"ds_read_b32 v90, v14 offset:112\n"
"s_sub_u32 s17, s17, 1\n"
"s_cbranch_scc1 .Lou_37\n"
"s_waitcnt lgkmcnt(0)\n"
"v_cmp_eq_u32 vcc, 0, v90\n"
"s_cbranch_vccnz .Ltp_36\n"
".Lou_37:\n"
"s_waitcnt lgkmcnt(0)\n"
"ds_read_b128 v[56:59], v3 offset:40960\n"
"s_mov_b32 s31, s30\n"
"v_mul_f32 v28, v52, v28\n"
"v_mul_f32 v29, v52, v29\n"
"v_mul_f32 v30, v52, v30\n"
"v_mul_f32 v31, v52, v31\n"
"v_mul_f32 v32, v53, v32\n"
"ds_read_b128 v[60:63], v3 offset:41984\n"
"v_mul_f32 v33, v53, v33\n"
"v_mul_f32 v34, v53, v34\n"
"v_mul_f32 v35, v53, v35\n"
"v_cvt_pk_bf16_f32 v28, v28, v29\n"
"v_cvt_pk_bf16_f32 v29, v30, v31\n"
"v_cvt_pk_bf16_f32 v30, v32, v33\n"
"ds_read_b128 v[64:67], v3 offset:43008\n"
"v_cvt_pk_bf16_f32 v31, v34, v35\n"
"v_min_u32 v28, -2, v28\n"
"v_min_u32 v29, -2, v29\n"
"v_min_u32 v30, -2, v30\n"
"v_min_u32 v31, -2, v31\n"
"buffer_store_dwordx4 v[28:31], v1, s[4:7], s31 offen offset:0 sc1\n"
"ds_read_b128 v[68:71], v3 offset:44032\n"
"v_mul_f32 v36, v52, v36\n"
"v_mul_f32 v37, v52, v37\n"
"v_mul_f32 v38, v52, v38\n"
"v_mul_f32 v39, v52, v39\n"
"v_mul_f32 v40, v53, v40\n"
"v_mul_f32 v41, v53, v41\n"
"ds_read_b128 v[72:75], v3 offset:45056\n"
"v_mul_f32 v42, v53, v42\n"
"v_mul_f32 v43, v53, v43\n"
"v_cvt_pk_bf16_f32 v36, v36, v37\n"
"v_cvt_pk_bf16_f32 v37, v38, v39\n"
"v_cvt_pk_bf16_f32 v38, v40, v41\n"
"v_cvt_pk_bf16_f32 v39, v42, v43\n"
"v_min_u32 v36, -2, v36\n"
"v_min_u32 v37, -2, v37\n"
"v_min_u32 v38, -2, v38\n"
"v_min_u32 v39, -2, v39\n"
"buffer_store_dwordx4 v[36:39], v88, s[4:7], s31 offen offset:1024 sc1\n"
"ds_write_b32 v13, v13 offset:128\n"
"s_mul_i32 s79, s43, 0x1400\n"
"v_lshrrev_b32 v92, 2, v0\n"
"v_and_b32 v91, 3, v0\n"
"v_lshlrev_b32 v91, 4, v91\n"
"v_lshl_add_u32 v92, v92, 10, v91\n"
"v_add_u32 v92, s79, v92\n"
"v_mov_b32 v91, 0x40000000\n"
"v_cmp_gt_i32 vcc, 20, v0\n"
"s_nop 1\n"
"v_cndmask_b32 v93, v91, v92, vcc\n"
"s_waitcnt lgkmcnt(4)\n"
"v_mfma_f32_16x16x32_bf16 v[28:31], v[96:99], v[56:59], 0\n"
"ds_read_b128 v[76:79], v3 offset:46080\n"
"s_add_u32 s31, s28, 0x14f000\n"
"v_mfma_f32_16x16x32_bf16 v[32:35], v[100:103], v[56:59], 0\n"
"s_add_u32 m0, s29, 0x5000\n"
"v_mfma_f32_16x16x32_bf16 v[52:55], v[56:59], v[56:59], 0\n"
"v_mfma_f32_16x16x32_bf16 v[28:31], v[104:107], v[60:63], v[28:31]\n"
"ds_read_b128 v[56:59], v3 offset:47104\n"
"buffer_load_dwordx4 v2, s[8:11], s31 offen offset:0 lds\n"
"v_mfma_f32_16x16x32_bf16 v[32:35], v[108:111], v[60:63], v[32:35]\n"
"s_add_u32 m0, s29, 0x53c0\n"
"s_waitcnt lgkmcnt(4)\n"
"v_mfma_f32_16x16x32_bf16 v[28:31], v[112:115], v[64:67], v[28:31]\n"
"ds_read_b128 v[60:63], v3 offset:48128\n"
"v_mfma_f32_16x16x32_bf16 v[32:35], v[116:119], v[64:67], v[32:35]\n"
"v_mfma_f32_16x16x32_bf16 v[28:31], v[120:123], v[68:71], v[28:31]\n"
"ds_read_b128 v[64:67], v3 offset:49152\n"
"buffer_load_dwordx4 v2, s[8:11], s31 offen offset:64 lds\n"
"v_mfma_f32_16x16x32_bf16 v[32:35], v[124:127], v[68:71], v[32:35]\n"
"s_add_u32 m0, s29, 0x5780\n"
"s_waitcnt lgkmcnt(3)\n"
"v_mfma_f32_16x16x32_bf16 v[28:31], v[128:131], v[72:75], v[28:31]\n"
"ds_read_b128 v[68:71], v3 offset:50176\n"
"v_mfma_f32_16x16x32_bf16 v[32:35], v[132:135], v[72:75], v[32:35]\n"
"v_mfma_f32_16x16x32_bf16 v[52:55], v[72:75], v[72:75], v[52:55]\n"
"buffer_load_dwordx4 v2, s[8:11], s31 offen offset:128 lds\n"
"v_mfma_f32_16x16x32_bf16 v[28:31], v[136:139], v[76:79], v[28:31]\n"
"ds_read_b128 v[72:75], v3 offset:51200\n"
"s_add_u32 m0, s29, 0x5b40\n"
"v_mfma_f32_16x16x32_bf16 v[32:35], v[140:143], v[76:79], v[32:35]\n"
"s_waitcnt lgkmcnt(3)\n"
"v_mfma_f32_16x16x32_bf16 v[28:31], v[144:147], v[56:59], v[28:31]\n"
"ds_read_b128 v[76:79], v3 offset:52224\n"
"v_mfma_f32_16x16x32_bf16 v[32:35], v[148:151], v[56:59], v[32:35]\n"
"buffer_load_dwordx4 v2, s[8:11], s31 offen offset:192 lds\n"
"v_mfma_f32_16x16x32_bf16 v[28:31], v[152:155], v[60:63], v[28:31]\n"
"ds_read_b128 v[56:59], v3 offset:53248\n"
"s_add_u32 m0, s29, 0x5f00\n"
"v_mfma_f32_16x16x32_bf16 v[32:35], v[156:159], v[60:63], v[32:35]\n"
"s_waitcnt lgkmcnt(3)\n"
"v_mfma_f32_16x16x32_bf16 v[28:31], v[160:163], v[64:67], v[28:31]\n"
"ds_read_b128 v[60:63], v3 offset:54272\n"
"v_mfma_f32_16x16x32_bf16 v[32:35], v[164:167], v[64:67], v[32:35]\n"
"buffer_load_dwordx4 v2, s[8:11], s31 offen offset:256 lds\n"
"v_mfma_f32_16x16x32_bf16 v[52:55], v[64:67], v[64:67], v[52:55]\n"
"v_mfma_f32_16x16x32_bf16 v[28:31], v[168:171], v[68:71], v[28:31]\n"
"ds_read_b128 v[64:67], v3 offset:55296\n"
"v_mfma_f32_16x16x32_bf16 v[32:35], v[172:175], v[68:71], v[32:35]\n"
"s_waitcnt lgkmcnt(3)\n"
"v_mfma_f32_16x16x32_bf16 v[28:31], v[176:179], v[72:75], v[28:31]\n"
"ds_read_b128 v[68:71], v3 offset:56320\n"
"v_mfma_f32_16x16x32_bf16 v[32:35], v[180:183], v[72:75], v[32:35]\n"
"v_mfma_f32_16x16x32_bf16 v[28:31], v[184:187], v[76:79], v[28:31]\n"
"ds_read_b128 v[72:75], v3 offset:57344\n"
"v_mfma_f32_16x16x32_bf16 v[32:35], v[188:191], v[76:79], v[32:35]\n"
"s_waitcnt lgkmcnt(3)\n"
"v_mfma_f32_16x16x32_bf16 v[28:31], v[192:195], v[56:59], v[28:31]\n"
"ds_read_b128 v[76:79], v4 offset:40960\n"
"v_mfma_f32_16x16x32_bf16 v[32:35], v[196:199], v[56:59], v[32:35]\n"
"v_mfma_f32_16x16x32_bf16 v[52:55], v[56:59], v[56:59], v[52:55]\n"
"v_mfma_f32_16x16x32_bf16 v[28:31], v[200:203], v[60:63], v[28:31]\n"
"ds_read_b128 v[56:59], v5 offset:40960\n"
"v_mfma_f32_16x16x32_bf16 v[32:35], v[204:207], v[60:63], v[32:35]\n"
"s_waitcnt lgkmcnt(3)\n"
"v_mfma_f32_16x16x32_bf16 v[28:31], v[208:211], v[64:67], v[28:31]\n"
"ds_read_b128 v[60:63], v6 offset:40960\n"
"v_mfma_f32_16x16x32_bf16 v[32:35], v[212:215], v[64:67], v[32:35]\n"
"v_mfma_f32_16x16x32_bf16 v[28:31], v[216:219], v[68:71], v[28:31]\n"
"v_add_u32 v3, 0x5000, v3\n"
"ds_read_b128 v[64:67], v3 offset:40960\n"
"v_mfma_f32_16x16x32_bf16 v[32:35], v[220:223], v[68:71], v[32:35]\n"
"s_waitcnt lgkmcnt(3)\n"
"v_mfma_f32_16x16x32_bf16 v[28:31], v[224:227], v[72:75], v[28:31]\n"
"ds_read_b128 v[68:71], v3 offset:41984\n"
"v_mfma_f32_16x16x32_bf16 v[32:35], v[228:231], v[72:75], v[32:35]\n"
"v_mfma_f32_16x16x32_bf16 v[52:55], v[72:75], v[72:75], v[52:55]\n"
"v_mfma_f32_16x16x32_bf16 v[28:31], v[232:235], v[76:79], v[28:31]\n"
"ds_read_b128 v[72:75], v3 offset:43008\n"
"v_mfma_f32_16x16x32_bf16 v[32:35], v[236:239], v[76:79], v[32:35]\n"
"s_waitcnt lgkmcnt(3)\n"
"v_mfma_f32_16x16x32_bf16 v[28:31], v[240:243], v[56:59], v[28:31]\n"
"ds_read_b128 v[76:79], v3 offset:44032\n"
"v_cndmask_b32 v94, v52, v53, s[34:35]\n"
"v_mfma_f32_16x16x32_bf16 v[32:35], v[244:247], v[56:59], v[32:35]\n"
"v_cndmask_b32 v54, v54, v55, s[34:35]\n"
"v_cndmask_b32 v94, v94, v54, s[20:21]\n"
"v_mfma_f32_16x16x32_bf16 v[28:31], v[248:251], v[60:63], v[28:31]\n"
"ds_read_b128 v[56:59], v3 offset:45056\n"
"v_mfma_f32_16x16x32_bf16 v[32:35], v[252:255], v[60:63], v[32:35]\n"
"s_waitcnt lgkmcnt(3)\n"
"v_mfma_f32_16x16x32_bf16 v[36:39], v[96:99], v[64:67], 0\n"
"ds_read_b128 v[60:63], v3 offset:46080\n"
"v_mfma_f32_16x16x32_bf16 v[40:43], v[100:103], v[64:67], 0\n"
"v_cndmask_b32 v94, v28, v94, s[22:23]\n"
"s_waitcnt vmcnt(7)\n"
"v_mfma_f32_16x16x32_bf16 v[52:55], v[64:67], v[64:67], 0\n"
"ds_write_b32 v9, v94 offset:0\n"
"v_mfma_f32_16x16x32_bf16 v[36:39], v[104:107], v[68:71], v[36:39]\n"
"ds_read_b128 v[64:67], v3 offset:47104\n"
"v_mfma_f32_16x16x32_bf16 v[40:43], v[108:111], v[68:71], v[40:43]\n"
"s_waitcnt lgkmcnt(4)\n"
"v_mfma_f32_16x16x32_bf16 v[36:39], v[112:115], v[72:75], v[36:39]\n"
"ds_read_b128 v[68:71], v3 offset:48128\n"
"v_mfma_f32_16x16x32_bf16 v[40:43], v[116:119], v[72:75], v[40:43]\n"
"v_mfma_f32_16x16x32_bf16 v[36:39], v[120:123], v[76:79], v[36:39]\n"
"ds_read_b128 v[72:75], v3 offset:49152\n"
"v_mfma_f32_16x16x32_bf16 v[40:43], v[124:127], v[76:79], v[40:43]\n"
"s_waitcnt lgkmcnt(4)\n"
"v_mfma_f32_16x16x32_bf16 v[36:39], v[128:131], v[56:59], v[36:39]\n"
"ds_read_b128 v[76:79], v3 offset:50176\n"
"v_mfma_f32_16x16x32_bf16 v[40:43], v[132:135], v[56:59], v[40:43]\n"
"v_mfma_f32_16x16x32_bf16 v[52:55], v[56:59], v[56:59], v[52:55]\n"
"v_mfma_f32_16x16x32_bf16 v[36:39], v[136:139], v[60:63], v[36:39]\n"
"ds_read_b128 v[56:59], v3 offset:51200\n"
"v_mfma_f32_16x16x32_bf16 v[40:43], v[140:143], v[60:63], v[40:43]\n"
"s_waitcnt lgkmcnt(3)\n"
"v_mfma_f32_16x16x32_bf16 v[36:39], v[144:147], v[64:67], v[36:39]\n"
"ds_read_b128 v[60:63], v3 offset:52224\n"
"ds_read_b128 v[84:87], v11 offset:0\n"
"ds_read_b32 v91, v12 offset:0\n"
"v_mfma_f32_16x16x32_bf16 v[40:43], v[148:151], v[64:67], v[40:43]\n"
"v_mfma_f32_16x16x32_bf16 v[36:39], v[152:155], v[68:71], v[36:39]\n"
"ds_read_b128 v[64:67], v3 offset:53248\n"
"v_mfma_f32_16x16x32_bf16 v[40:43], v[156:159], v[68:71], v[40:43]\n"
"s_waitcnt lgkmcnt(5)\n"
"v_mfma_f32_16x16x32_bf16 v[36:39], v[160:163], v[72:75], v[36:39]\n"
"ds_read_b128 v[68:71], v3 offset:54272\n"
"v_mfma_f32_16x16x32_bf16 v[40:43], v[164:167], v[72:75], v[40:43]\n"
"v_mfma_f32_16x16x32_bf16 v[52:55], v[72:75], v[72:75], v[52:55]\n"
"v_mfma_f32_16x16x32_bf16 v[36:39], v[168:171], v[76:79], v[36:39]\n"
"ds_read_b128 v[72:75], v3 offset:55296\n"
"s_waitcnt lgkmcnt(4)\n"
"v_add_f32 v84, v84, v85\n"
"v_mfma_f32_16x16x32_bf16 v[40:43], v[172:175], v[76:79], v[40:43]\n"
"v_add_f32 v86, v86, v87\n"
"v_mfma_f32_16x16x32_bf16 v[36:39], v[176:179], v[56:59], v[36:39]\n"
"ds_read_b128 v[76:79], v3 offset:56320\n"
"v_add_f32 v84, v84, v86\n"
"v_mfma_f32_16x16x32_bf16 v[40:43], v[180:183], v[56:59], v[40:43]\n"
"s_waitcnt lgkmcnt(4)\n"
"v_mul_f32 v89, 0x3acccccd, v91\n"
"v_fma_f32 v84, -v91, v89, v84\n"
"v_mfma_f32_16x16x32_bf16 v[36:39], v[184:187], v[60:63], v[36:39]\n"
"ds_read_b128 v[56:59], v3 offset:57344\n"
"v_fmamk_f32 v84, v84, 0x3acccccd, v27\n"
"v_mfma_f32_16x16x32_bf16 v[40:43], v[188:191], v[60:63], v[40:43]\n"
"v_cmp_u_f32 vcc, v84, v84\n"
"s_waitcnt lgkmcnt(3)\n"
"v_mfma_f32_16x16x32_bf16 v[36:39], v[192:195], v[64:67], v[36:39]\n"
"ds_read_b128 v[60:63], v4 offset:61440\n"
"v_max_f32 v84, v84, v27\n"
"v_mfma_f32_16x16x32_bf16 v[40:43], v[196:199], v[64:67], v[40:43]\n"
"v_rsq_f32 v88, v84\n"
"v_mfma_f32_16x16x32_bf16 v[52:55], v[64:67], v[64:67], v[52:55]\n"
"v_mul_f32 v89, -v89, v88\n"
"v_mfma_f32_16x16x32_bf16 v[36:39], v[200:203], v[68:71], v[36:39]\n"
"ds_read_b128 v[64:67], v5 offset:61440\n"
"s_cbranch_vccnz .Lsl_38\n"
".Lbk_39:\n"
"v_mfma_f32_16x16x32_bf16 v[40:43], v[204:207], v[68:71], v[40:43]\n"
"s_add_u32 s31, s28, 0x13b000\n"
"s_waitcnt lgkmcnt(3)\n"
"v_mfma_f32_16x16x32_bf16 v[36:39], v[208:211], v[72:75], v[36:39]\n"
"ds_read_b128 v[68:71], v6 offset:61440\n"
"s_add_u32 m0, s29, 0xa000\n"
"v_mfma_f32_16x16x32_bf16 v[40:43], v[212:215], v[72:75], v[40:43]\n"
"v_mul_f32 v28, v88, v28\n"
"v_mul_f32 v29, v88, v29\n"
"v_mfma_f32_16x16x32_bf16 v[36:39], v[216:219], v[76:79], v[36:39]\n"
"v_add_u32 v3, 0xffffb000, v3\n"
"ds_read_b128 v[72:75], v3 offset:0\n"
"buffer_load_dwordx4 v2, s[8:11], s31 offen offset:0 lds\n"
"v_mfma_f32_16x16x32_bf16 v[40:43], v[220:223], v[76:79], v[40:43]\n"
"s_add_u32 m0, s29, 0xa3c0\n"
"s_waitcnt lgkmcnt(3)\n"
"v_mfma_f32_16x16x32_bf16 v[36:39], v[224:227], v[56:59], v[36:39]\n"
"ds_read_b128 v[76:79], v3 offset:1024\n"
"v_mul_f32 v30, v88, v30\n"
"v_mfma_f32_16x16x32_bf16 v[40:43], v[228:231], v[56:59], v[40:43]\n"
"v_mul_f32 v31, v88, v31\n"
"v_fmac_f32 v28, v89, v16\n"
"v_mfma_f32_16x16x32_bf16 v[52:55], v[56:59], v[56:59], v[52:55]\n"
"buffer_load_dwordx4 v2, s[8:11], s31 offen offset:64 lds\n"
"v_fmac_f32 v29, v89, v17\n"
"v_mfma_f32_16x16x32_bf16 v[36:39], v[232:235], v[60:63], v[36:39]\n"
"ds_read_b128 v[56:59], v3 offset:2048\n"
"s_add_u32 m0, s29, 0xa780\n"
"v_mfma_f32_16x16x32_bf16 v[40:43], v[236:239], v[60:63], v[40:43]\n"
"v_fmac_f32 v30, v89, v18\n"
"s_waitcnt lgkmcnt(3)\n"
"v_mfma_f32_16x16x32_bf16 v[36:39], v[240:243], v[64:67], v[36:39]\n"
"ds_read_b128 v[60:63], v3 offset:3072\n"
"v_cndmask_b32 v94, v52, v53, s[34:35]\n"
"v_mfma_f32_16x16x32_bf16 v[40:43], v[244:247], v[64:67], v[40:43]\n"
"v_cndmask_b32 v54, v54, v55, s[34:35]\n"
"v_cndmask_b32 v94, v94, v54, s[20:21]\n"
"buffer_load_dwordx4 v2, s[8:11], s31 offen offset:128 lds\n"
"v_mfma_f32_16x16x32_bf16 v[36:39], v[248:251], v[68:71], v[36:39]\n"
"ds_read_b128 v[64:67], v3 offset:4096\n"
"s_add_u32 m0, s29, 0xab40\n"
"v_mfma_f32_16x16x32_bf16 v[40:43], v[252:255], v[68:71], v[40:43]\n"
"v_fmac_f32 v31, v89, v19\n"
"s_waitcnt lgkmcnt(3)\n"
"v_mfma_f32_16x16x32_bf16 v[44:47], v[96:99], v[72:75], 0\n"
"ds_read_b128 v[68:71], v3 offset:5120\n"
"v_cvt_pk_bf16_f32 v28, v28, v29\n"
"v_mfma_f32_16x16x32_bf16 v[48:51], v[100:103], v[72:75], 0\n"
"v_cndmask_b32 v94, v36, v94, s[22:23]\n"
"s_waitcnt vmcnt(3)\n"
"buffer_load_dwordx4 v2, s[8:11], s31 offen offset:192 lds\n"
"v_mfma_f32_16x16x32_bf16 v[52:55], v[72:75], v[72:75], 0\n"
"ds_write_b32 v9, v94 offset:320\n"
"v_mfma_f32_16x16x32_bf16 v[44:47], v[104:107], v[76:79], v[44:47]\n"
"ds_read_b128 v[72:75], v3 offset:6144\n"
"s_add_u32 m0, s29, 0xaf00\n"
"v_mfma_f32_16x16x32_bf16 v[48:51], v[108:111], v[76:79], v[48:51]\n"
"v_cvt_pk_bf16_f32 v29, v30, v31\n"
"s_waitcnt lgkmcnt(4)\n"
"v_mfma_f32_16x16x32_bf16 v[44:47], v[112:115], v[56:59], v[44:47]\n"
"ds_read_b128 v[76:79], v3 offset:7168\n"
"buffer_load_dwordx4 v2, s[8:11], s31 offen offset:256 lds\n"
"v_mfma_f32_16x16x32_bf16 v[48:51], v[116:119], v[56:59], v[48:51]\n"
"v_mul_f32 v32, v88, v32\n"
"v_mul_f32 v33, v88, v33\n"
"v_mfma_f32_16x16x32_bf16 v[44:47], v[120:123], v[60:63], v[44:47]\n"
"ds_read_b128 v[56:59], v3 offset:8192\n"
"v_mul_f32 v34, v88, v34\n"
"v_mfma_f32_16x16x32_bf16 v[48:51], v[124:127], v[60:63], v[48:51]\n"
"v_mul_f32 v35, v88, v35\n"
"s_waitcnt lgkmcnt(4)\n"
"v_mfma_f32_16x16x32_bf16 v[44:47], v[128:131], v[64:67], v[44:47]\n"
"ds_read_b128 v[60:63], v3 offset:9216\n"
"v_fmac_f32 v32, v89, v20\n"
"v_mfma_f32_16x16x32_bf16 v[48:51], v[132:135], v[64:67], v[48:51]\n"
"v_fmac_f32 v33, v89, v21\n"
"v_fmac_f32 v34, v89, v22\n"
"v_mfma_f32_16x16x32_bf16 v[52:55], v[64:67], v[64:67], v[52:55]\n"
"v_fmac_f32 v35, v89, v23\n"
"v_cvt_pk_bf16_f32 v32, v32, v33\n"
"v_mfma_f32_16x16x32_bf16 v[44:47], v[136:139], v[68:71], v[44:47]\n"
"ds_read_b128 v[64:67], v3 offset:10240\n"
"v_cvt_pk_bf16_f32 v33, v34, v35\n"
"v_mfma_f32_16x16x32_bf16 v[48:51], v[140:143], v[68:71], v[48:51]\n"
"s_waitcnt lgkmcnt(3)\n"
"v_mfma_f32_16x16x32_bf16 v[44:47], v[144:147], v[72:75], v[44:47]\n"
"ds_read_b128 v[68:71], v3 offset:11264\n"
"ds_read_b128 v[84:87], v11 offset:320\n"
"ds_read_b32 v91, v12 offset:320\n"
"v_mfma_f32_16x16x32_bf16 v[48:51], v[148:151], v[72:75], v[48:51]\n"
"v_mfma_f32_16x16x32_bf16 v[44:47], v[152:155], v[76:79], v[44:47]\n"
"ds_read_b128 v[72:75], v3 offset:12288\n"
"v_mfma_f32_16x16x32_bf16 v[48:51], v[156:159], v[76:79], v[48:51]\n"
"s_waitcnt lgkmcnt(5)\n"
"v_mfma_f32_16x16x32_bf16 v[44:47], v[160:163], v[56:59], v[44:47]\n"
"ds_read_b128 v[76:79], v3 offset:13312\n"
"v_mfma_f32_16x16x32_bf16 v[48:51], v[164:167], v[56:59], v[48:51]\n"
"v_mfma_f32_16x16x32_bf16 v[52:55], v[56:59], v[56:59], v[52:55]\n"
"v_mfma_f32_16x16x32_bf16 v[44:47], v[168:171], v[60:63], v[44:47]\n"
"ds_read_b128 v[56:59], v3 offset:14336\n"
"s_waitcnt lgkmcnt(4)\n"
"v_add_f32 v84, v84, v85\n"
"v_mfma_f32_16x16x32_bf16 v[48:51], v[172:175], v[60:63], v[48:51]\n"
"v_add_f32 v86, v86, v87\n"
"v_mfma_f32_16x16x32_bf16 v[44:47], v[176:179], v[64:67], v[44:47]\n"
"ds_read_b128 v[60:63], v3 offset:15360\n"
"v_add_f32 v84, v84, v86\n"
"v_mfma_f32_16x16x32_bf16 v[48:51], v[180:183], v[64:67], v[48:51]\n"
"s_waitcnt lgkmcnt(4)\n"
"v_mul_f32 v89, 0x3acccccd, v91\n"
"v_fma_f32 v84, -v91, v89, v84\n"
"v_mfma_f32_16x16x32_bf16 v[44:47], v[184:187], v[68:71], v[44:47]\n"
"ds_read_b128 v[64:67], v3 offset:16384\n"
"v_fmamk_f32 v84, v84, 0x3acccccd, v27\n"
"v_mfma_f32_16x16x32_bf16 v[48:51], v[188:191], v[68:71], v[48:51]\n"
"v_cmp_u_f32 vcc, v84, v84\n"
"s_waitcnt lgkmcnt(3)\n"
"v_mfma_f32_16x16x32_bf16 v[44:47], v[192:195], v[72:75], v[44:47]\n"
"ds_read_b128 v[68:71], v4 offset:0\n"
"v_max_f32 v84, v84, v27\n"
"v_mfma_f32_16x16x32_bf16 v[48:51], v[196:199], v[72:75], v[48:51]\n"
"v_rsq_f32 v88, v84\n"
"v_mfma_f32_16x16x32_bf16 v[52:55], v[72:75], v[72:75], v[52:55]\n"
"v_mul_f32 v89, -v89, v88\n"
"v_mfma_f32_16x16x32_bf16 v[44:47], v[200:203], v[76:79], v[44:47]\n"
"ds_read_b128 v[72:75], v5 offset:0\n"
"s_cbranch_vccnz .Lsl_42\n"
".Lbk_43:\n"
"v_mfma_f32_16x16x32_bf16 v[48:51], v[204:207], v[76:79], v[48:51]\n"
"ds_write_b64 v7, v[28:29] offset:44928\n"
"s_waitcnt lgkmcnt(4)\n"
"v_mfma_f32_16x16x32_bf16 v[44:47], v[208:211], v[56:59], v[44:47]\n"
"ds_read_b128 v[76:79], v6 offset:0\n"
"v_mul_f32 v36, v88, v36\n"
"v_mfma_f32_16x16x32_bf16 v[48:51], v[212:215], v[56:59], v[48:51]\n"
"v_mul_f32 v37, v88, v37\n"
"v_mul_f32 v38, v88, v38\n"
"v_mfma_f32_16x16x32_bf16 v[44:47], v[216:219], v[60:63], v[44:47]\n"
"ds_read_b128 v[56:59], v3 offset:20480\n"
"v_mul_f32 v39, v88, v39\n"
"v_mfma_f32_16x16x32_bf16 v[48:51], v[220:223], v[60:63], v[48:51]\n"
"v_fmac_f32 v36, v89, v16\n"
"s_waitcnt lgkmcnt(4)\n"
"v_mfma_f32_16x16x32_bf16 v[44:47], v[224:227], v[64:67], v[44:47]\n"
"ds_read_b128 v[60:63], v3 offset:21504\n"
"v_fmac_f32 v37, v89, v17\n"
"v_mfma_f32_16x16x32_bf16 v[48:51], v[228:231], v[64:67], v[48:51]\n"
"v_mov_b32 v84, 0x3f803f80\n"
"v_mov_b32 v85, 0x3f803f80\n"
"v_mfma_f32_16x16x32_bf16 v[52:55], v[64:67], v[64:67], v[52:55]\n"
"v_mov_b32 v86, 0x3f803f80\n"
"v_mov_b32 v87, 0x3f803f80\n"
"ds_write_b64 v8, v[32:33] offset:44928\n"
"v_mfma_f32_16x16x32_bf16 v[44:47], v[232:235], v[68:71], v[44:47]\n"
"ds_read_b128 v[64:67], v3 offset:22528\n"
"v_fmac_f32 v38, v89, v18\n"
"v_mfma_f32_16x16x32_bf16 v[48:51], v[236:239], v[68:71], v[48:51]\n"
"v_fmac_f32 v39, v89, v19\n"
"s_waitcnt lgkmcnt(4)\n"
"v_mfma_f32_16x16x32_bf16 v[44:47], v[240:243], v[72:75], v[44:47]\n"
"ds_read_b128 v[68:71], v3 offset:23552\n"
"v_cndmask_b32 v94, v52, v53, s[34:35]\n"
"v_mfma_f32_16x16x32_bf16 v[48:51], v[244:247], v[72:75], v[48:51]\n"
"v_cndmask_b32 v54, v54, v55, s[34:35]\n"
"v_cndmask_b32 v94, v94, v54, s[20:21]\n"
"v_mfma_f32_16x16x32_bf16 v[44:47], v[248:251], v[76:79], v[44:47]\n"
"ds_read_b128 v[72:75], v3 offset:24576\n"
"v_cvt_pk_bf16_f32 v36, v36, v37\n"
"v_mfma_f32_16x16x32_bf16 v[48:51], v[252:255], v[76:79], v[48:51]\n"
"v_cvt_pk_bf16_f32 v37, v38, v39\n"
"s_waitcnt lgkmcnt(4)\n"
"v_mfma_f32_16x16x32_bf16 v[28:31], v[96:99], v[56:59], 0\n"
"ds_read_b128 v[76:79], v3 offset:25600\n"
"v_mul_f32 v40, v88, v40\n"
"v_mfma_f32_16x16x32_bf16 v[32:35], v[100:103], v[56:59], 0\n"
"v_cndmask_b32 v94, v44, v94, s[22:23]\n"
"s_waitcnt vmcnt(0)\n"
"ds_write_b128 v93, v[84:87] offset:40960\n"
"v_mfma_f32_16x16x32_bf16 v[52:55], v[56:59], v[56:59], 0\n"
"ds_write_b32 v9, v94 offset:640\n"
"v_mfma_f32_16x16x32_bf16 v[28:31], v[104:107], v[60:63], v[28:31]\n"
"ds_read_b128 v[56:59], v3 offset:26624\n"
"v_mul_f32 v41, v88, v41\n"
"v_mfma_f32_16x16x32_bf16 v[32:35], v[108:111], v[60:63], v[32:35]\n"
"v_mul_f32 v42, v88, v42\n"
"s_waitcnt lgkmcnt(5)\n"
"v_mfma_f32_16x16x32_bf16 v[28:31], v[112:115], v[64:67], v[28:31]\n"
"ds_read_b128 v[60:63], v3 offset:27648\n"
"v_mul_f32 v43, v88, v43\n"
"v_mfma_f32_16x16x32_bf16 v[32:35], v[116:119], v[64:67], v[32:35]\n"
"v_fmac_f32 v40, v89, v20\n"
"v_fmac_f32 v41, v89, v21\n"
"v_mfma_f32_16x16x32_bf16 v[28:31], v[120:123], v[68:71], v[28:31]\n"
"ds_read_b128 v[64:67], v3 offset:28672\n"
"v_fmac_f32 v42, v89, v22\n"
"v_mfma_f32_16x16x32_bf16 v[32:35], v[124:127], v[68:71], v[32:35]\n"
"v_fmac_f32 v43, v89, v23\n"
"s_waitcnt lgkmcnt(5)\n"
"v_mfma_f32_16x16x32_bf16 v[28:31], v[128:131], v[72:75], v[28:31]\n"
"ds_read_b128 v[68:71], v3 offset:29696\n"
"v_cvt_pk_bf16_f32 v40, v40, v41\n"
"v_mfma_f32_16x16x32_bf16 v[32:35], v[132:135], v[72:75], v[32:35]\n"
"v_cvt_pk_bf16_f32 v41, v42, v43\n"
"v_mfma_f32_16x16x32_bf16 v[52:55], v[72:75], v[72:75], v[52:55]\n"
"v_mfma_f32_16x16x32_bf16 v[28:31], v[136:139], v[76:79], v[28:31]\n"
"ds_read_b128 v[72:75], v3 offset:30720\n"
"v_mfma_f32_16x16x32_bf16 v[32:35], v[140:143], v[76:79], v[32:35]\n"
"s_waitcnt lgkmcnt(3)\n"
"v_mfma_f32_16x16x32_bf16 v[28:31], v[144:147], v[56:59], v[28:31]\n"
"ds_read_b128 v[76:79], v3 offset:31744\n"
"ds_read_b128 v[84:87], v11 offset:640\n"
"ds_read_b32 v91, v12 offset:640\n"
"v_mfma_f32_16x16x32_bf16 v[32:35], v[148:151], v[56:59], v[32:35]\n"
"v_mfma_f32_16x16x32_bf16 v[28:31], v[152:155], v[60:63], v[28:31]\n"
"ds_read_b128 v[56:59], v3 offset:32768\n"
"v_mfma_f32_16x16x32_bf16 v[32:35], v[156:159], v[60:63], v[32:35]\n"
"s_waitcnt lgkmcnt(5)\n"
"v_mfma_f32_16x16x32_bf16 v[28:31], v[160:163], v[64:67], v[28:31]\n"
"ds_read_b128 v[60:63], v3 offset:33792\n"
"v_mfma_f32_16x16x32_bf16 v[32:35], v[164:167], v[64:67], v[32:35]\n"
"v_mfma_f32_16x16x32_bf16 v[52:55], v[64:67], v[64:67], v[52:55]\n"
"v_mfma_f32_16x16x32_bf16 v[28:31], v[168:171], v[68:71], v[28:31]\n"
"ds_read_b128 v[64:67], v3 offset:34816\n"
"s_waitcnt lgkmcnt(4)\n"
"v_add_f32 v84, v84, v85\n"
"v_mfma_f32_16x16x32_bf16 v[32:35], v[172:175], v[68:71], v[32:35]\n"
"v_add_f32 v86, v86, v87\n"
"v_mfma_f32_16x16x32_bf16 v[28:31], v[176:179], v[72:75], v[28:31]\n"
"ds_read_b128 v[68:71], v3 offset:35840\n"
"v_add_f32 v84, v84, v86\n"
"v_mfma_f32_16x16x32_bf16 v[32:35], v[180:183], v[72:75], v[32:35]\n"
"s_waitcnt lgkmcnt(4)\n"
"v_mul_f32 v89, 0x3acccccd, v91\n"
"v_fma_f32 v84, -v91, v89, v84\n"
"v_mfma_f32_16x16x32_bf16 v[28:31], v[184:187], v[76:79], v[28:31]\n"
"ds_read_b128 v[72:75], v3 offset:36864\n"
"v_fmamk_f32 v84, v84, 0x3acccccd, v27\n"
"v_mfma_f32_16x16x32_bf16 v[32:35], v[188:191], v[76:79], v[32:35]\n"
"v_cmp_u_f32 vcc, v84, v84\n"
"s_waitcnt lgkmcnt(3)\n"
"v_mfma_f32_16x16x32_bf16 v[28:31], v[192:195], v[56:59], v[28:31]\n"
"ds_read_b128 v[76:79], v4 offset:20480\n"
"v_max_f32 v84, v84, v27\n"
"v_mfma_f32_16x16x32_bf16 v[32:35], v[196:199], v[56:59], v[32:35]\n"
"v_rsq_f32 v88, v84\n"
"v_mfma_f32_16x16x32_bf16 v[52:55], v[56:59], v[56:59], v[52:55]\n"
"v_mul_f32 v89, -v89, v88\n"
"v_mfma_f32_16x16x32_bf16 v[28:31], v[200:203], v[60:63], v[28:31]\n"
"ds_read_b128 v[56:59], v5 offset:20480\n"
"s_cbranch_vccnz .Lsl_46\n"
".Lbk_47:\n"
"v_mfma_f32_16x16x32_bf16 v[32:35], v[204:207], v[60:63], v[32:35]\n"
"ds_write_b64 v7, v[36:37] offset:48896\n"
"s_waitcnt lgkmcnt(4)\n"
"v_mfma_f32_16x16x32_bf16 v[28:31], v[208:211], v[64:67], v[28:31]\n"
"ds_read_b128 v[60:63], v6 offset:20480\n"
"v_mul_f32 v44, v88, v44\n"
"v_mul_f32 v45, v88, v45\n"
"v_mfma_f32_16x16x32_bf16 v[32:35], v[212:215], v[64:67], v[32:35]\n"
"v_mul_f32 v46, v88, v46\n"
"v_mul_f32 v47, v88, v47\n"
"v_fmac_f32 v44, v89, v16\n"
"v_mfma_f32_16x16x32_bf16 v[28:31], v[216:219], v[68:71], v[28:31]\n"
"ds_read_b128 v[64:67], v3 offset:40960\n"
"v_fmac_f32 v45, v89, v17\n"
"v_fmac_f32 v46, v89, v18\n"
"v_mfma_f32_16x16x32_bf16 v[32:35], v[220:223], v[68:71], v[32:35]\n"
"v_fmac_f32 v47, v89, v19\n"
"s_waitcnt lgkmcnt(4)\n"
"v_mfma_f32_16x16x32_bf16 v[28:31], v[224:227], v[72:75], v[28:31]\n"
"ds_read_b128 v[68:71], v3 offset:45056\n"
"v_cvt_pk_bf16_f32 v44, v44, v45\n"
"v_mfma_f32_16x16x32_bf16 v[32:35], v[228:231], v[72:75], v[32:35]\n"
"v_cvt_pk_bf16_f32 v45, v46, v47\n"
"v_mul_f32 v48, v88, v48\n"
"v_mfma_f32_16x16x32_bf16 v[52:55], v[72:75], v[72:75], v[52:55]\n"
"ds_write_b64 v8, v[40:41] offset:48896\n"
"v_mfma_f32_16x16x32_bf16 v[28:31], v[232:235], v[76:79], v[28:31]\n"
"ds_read_b128 v[72:75], v3 offset:49152\n"
"v_mul_f32 v49, v88, v49\n"
"v_mfma_f32_16x16x32_bf16 v[32:35], v[236:239], v[76:79], v[32:35]\n"
"v_mul_f32 v50, v88, v50\n"
"s_waitcnt lgkmcnt(4)\n"
"v_mfma_f32_16x16x32_bf16 v[28:31], v[240:243], v[56:59], v[28:31]\n"
"ds_read_b128 v[76:79], v3 offset:53248\n"
"v_cndmask_b32 v94, v52, v53, s[34:35]\n"
"v_mfma_f32_16x16x32_bf16 v[32:35], v[244:247], v[56:59], v[32:35]\n"
"v_cndmask_b32 v54, v54, v55, s[34:35]\n"
"v_cndmask_b32 v94, v94, v54, s[20:21]\n"
"v_mfma_f32_16x16x32_bf16 v[28:31], v[248:251], v[60:63], v[28:31]\n"
"ds_read_b128 v[56:59], v3 offset:57344\n"
"v_mul_f32 v51, v88, v51\n"
"v_mfma_f32_16x16x32_bf16 v[32:35], v[252:255], v[60:63], v[32:35]\n"
"v_fmac_f32 v48, v89, v20\n"
"s_waitcnt lgkmcnt(4)\n"
"s_cmp_eq_u32 s3, 3\n"
"s_cbranch_scc1 .Lfu_58\n"
"ds_read_b128 v[60:63], v3 offset:41984\n"
"v_fmac_f32 v49, v89, v21\n"
"v_mfma_f32_16x16x32_bf16 v[40:43], v[100:103], v[64:67], 0\n"
"v_cndmask_b32 v94, v28, v94, s[22:23]\n"
"v_fmac_f32 v50, v89, v22\n"
"v_mfma_f32_16x16x32_bf16 v[52:55], v[64:67], v[64:67], 0\n"
"ds_write_b32 v9, v94 offset:960\n"
"ds_read_b128 v[64:67], v3 offset:43008\n"
"v_fmac_f32 v51, v89, v23\n"
"v_mfma_f32_16x16x32_bf16 v[40:43], v[132:135], v[68:71], v[40:43]\n"
"v_cvt_pk_bf16_f32 v48, v48, v49\n"
"v_cvt_pk_bf16_f32 v49, v50, v51\n"
"v_mfma_f32_16x16x32_bf16 v[52:55], v[68:71], v[68:71], v[52:55]\n"
"ds_write_b64 v7, v[44:45] offset:52864\n"
"s_waitcnt lgkmcnt(5)\n"
"ds_read_b128 v[68:71], v3 offset:44032\n"
"v_mfma_f32_16x16x32_bf16 v[40:43], v[164:167], v[72:75], v[40:43]\n"
"v_mfma_f32_16x16x32_bf16 v[52:55], v[72:75], v[72:75], v[52:55]\n"
"ds_write_b64 v8, v[48:49] offset:52864\n"
"ds_read_b128 v[72:75], v3 offset:46080\n"
"v_mfma_f32_16x16x32_bf16 v[40:43], v[196:199], v[76:79], v[40:43]\n"
"v_mfma_f32_16x16x32_bf16 v[52:55], v[76:79], v[76:79], v[52:55]\n"
"ds_read_b128 v[84:87], v11 offset:960\n"
"ds_read_b32 v91, v12 offset:960\n"
"s_waitcnt lgkmcnt(8)\n"
"ds_read_b128 v[76:79], v3 offset:47104\n"
"v_mfma_f32_16x16x32_bf16 v[40:43], v[228:231], v[56:59], v[40:43]\n"
"v_mfma_f32_16x16x32_bf16 v[52:55], v[56:59], v[56:59], v[52:55]\n"
"ds_read_b128 v[56:59], v3 offset:48128\n"
"v_mfma_f32_16x16x32_bf16 v[40:43], v[108:111], v[60:63], v[40:43]\n"
"s_waitcnt lgkmcnt(6)\n"
"ds_read_b128 v[60:63], v3 offset:50176\n"
"s_nop 2\n"
"ds_write_b32 v10, v52 offset:1600\n"
"v_cndmask_b32 v94, v52, v53, s[34:35]\n"
"v_cndmask_b32 v54, v54, v55, s[34:35]\n"
"v_mfma_f32_16x16x32_bf16 v[40:43], v[116:119], v[64:67], v[40:43]\n"
"s_waitcnt lgkmcnt(5)\n"
"v_add_f32 v84, v84, v85\n"
"v_add_f32 v86, v86, v87\n"
"v_add_f32 v84, v84, v86\n"
"v_cndmask_b32 v94, v94, v54, s[20:21]\n"
"ds_write_b32 v9, v94 offset:1280\n"
"ds_read_b128 v[64:67], v3 offset:51200\n"
"s_waitcnt lgkmcnt(6)\n"
"v_mul_f32 v89, 0x3acccccd, v91\n"
"v_fma_f32 v84, -v91, v89, v84\n"
"v_mfma_f32_16x16x32_bf16 v[40:43], v[124:127], v[68:71], v[40:43]\n"
"v_fmamk_f32 v84, v84, 0x3acccccd, v27\n"
"s_waitcnt lgkmcnt(5)\n"
"ds_read_b128 v[68:71], v3 offset:52224\n"
"v_cmp_u_f32 vcc, v84, v84\n"
"v_mfma_f32_16x16x32_bf16 v[40:43], v[140:143], v[72:75], v[40:43]\n"
"v_max_f32 v84, v84, v27\n"
"v_rsq_f32 v88, v84\n"
"ds_read_b128 v[72:75], v3 offset:54272\n"
"v_mul_f32 v89, -v89, v88\n"
"v_mfma_f32_16x16x32_bf16 v[40:43], v[148:151], v[76:79], v[40:43]\n"
"s_cbranch_vccnz .Lsl_50c\n"
".Lbk_51c:\n"
"s_waitcnt lgkmcnt(5)\n"
"ds_read_b128 v[76:79], v3 offset:55296\n"
"v_mul_f32 v28, v88, v28\n"
"v_mul_f32 v29, v88, v29\n"
"v_mfma_f32_16x16x32_bf16 v[40:43], v[156:159], v[56:59], v[40:43]\n"
"ds_read_b128 v[44:47], v11 offset:1280\n"
"ds_read_b128 v[48:51], v11 offset:1600\n"
"v_mul_f32 v30, v88, v30\n"
"ds_read_b128 v[56:59], v3 offset:56320\n"
"v_mul_f32 v31, v88, v31\n"
"v_fmac_f32 v28, v89, v16\n"
"v_mfma_f32_16x16x32_bf16 v[40:43], v[172:175], v[60:63], v[40:43]\n"
"v_fmac_f32 v29, v89, v17\n"
"v_fmac_f32 v30, v89, v18\n"
"s_waitcnt lgkmcnt(5)\n"
"ds_read_b128 v[60:63], v4 offset:40960\n"
"v_fmac_f32 v31, v89, v19\n"
"v_cvt_pk_bf16_f32 v28, v28, v29\n"
"v_mfma_f32_16x16x32_bf16 v[40:43], v[180:183], v[64:67], v[40:43]\n"
"v_cvt_pk_bf16_f32 v29, v30, v31\n"
"ds_write_b64 v7, v[28:29] offset:56832\n"
"ds_read_b128 v[64:67], v5 offset:40960\n"
"v_mul_f32 v32, v88, v32\n"
"v_mul_f32 v33, v88, v33\n"
"v_mfma_f32_16x16x32_bf16 v[40:43], v[188:191], v[68:71], v[40:43]\n"
"v_mul_f32 v34, v88, v34\n"
"v_mul_f32 v35, v88, v35\n"
"s_waitcnt lgkmcnt(6)\n"
"ds_read_b128 v[68:71], v6 offset:40960\n"
"s_waitcnt lgkmcnt(6)\n"
"v_add_f32 v44, v44, v45\n"
"v_fmac_f32 v32, v89, v20\n"
"v_mfma_f32_16x16x32_bf16 v[40:43], v[204:207], v[72:75], v[40:43]\n"
"v_add_f32 v46, v46, v47\n"
"s_waitcnt lgkmcnt(5)\n"
"v_add_f32 v48, v48, v49\n"
"v_fmac_f32 v33, v89, v21\n"
"v_add_f32 v50, v50, v51\n"
"v_add_f32 v48, v48, v50\n"
"v_fmac_f32 v34, v89, v22\n"
"v_mfma_f32_16x16x32_bf16 v[40:43], v[212:215], v[76:79], v[40:43]\n"
"v_add_f32 v44, v44, v46\n"
"v_fmac_f32 v35, v89, v23\n"
"s_waitcnt lgkmcnt(3)\n"
"v_mul_f32 v53, 0x3acccccd, v48\n"
"v_fma_f32 v44, -v48, v53, v44\n"
"v_cvt_pk_bf16_f32 v32, v32, v33\n"
"v_mfma_f32_16x16x32_bf16 v[40:43], v[220:223], v[56:59], v[40:43]\n"
"v_fmamk_f32 v44, v44, 0x3acccccd, v27\n"
"v_cmp_u_f32 vcc, v44, v44\n"
"v_max_f32 v44, v44, v27\n"
"v_rsq_f32 v52, v44\n"
"v_mfma_f32_16x16x32_bf16 v[40:43], v[236:239], v[60:63], v[40:43]\n"
"v_mul_f32 v53, -v53, v52\n"
"s_waitcnt lgkmcnt(0)\n"
"s_cbranch_vccnz .Lsl_54c\n"
".Lbk_55c:\n"
"v_mfma_f32_16x16x32_bf16 v[40:43], v[244:247], v[64:67], v[40:43]\n"
"v_cvt_pk_bf16_f32 v33, v34, v35\n"
"ds_write_b64 v8, v[32:33] offset:56832\n"
"v_mfma_f32_16x16x32_bf16 v[40:43], v[252:255], v[68:71], v[40:43]\n"
"s_nop 7\n"
"v_mul_f32 v40, v52, v40\n"
"v_mul_f32 v41, v52, v41\n"
"v_mul_f32 v42, v52, v42\n"
"v_mul_f32 v43, v52, v43\n"
"v_fmac_f32 v40, v53, v20\n"
"v_fmac_f32 v41, v53, v21\n"
"v_fmac_f32 v42, v53, v22\n"
"v_fmac_f32 v43, v53, v23\n"
"v_cvt_pk_bf16_f32 v40, v40, v41\n"
"v_cvt_pk_bf16_f32 v41, v42, v43\n"
"ds_write_b64 v8, v[40:41] offset:40960\n"
"ds_write_b32 v13, v13 offset:144\n"
"s_branch .Ljn_59\n"
".Lfu_58:\n"
"s_nop 0\n"
"v_mfma_f32_16x16x32_bf16 v[36:39], v[96:99], v[64:67], 0\n"
"ds_read_b128 v[60:63], v3 offset:41984\n"
"v_fmac_f32 v49, v89, v21\n"
"v_mfma_f32_16x16x32_bf16 v[40:43], v[100:103], v[64:67], 0\n"
"v_cndmask_b32 v94, v28, v94, s[22:23]\n"
"v_fmac_f32 v50, v89, v22\n"
"v_mfma_f32_16x16x32_bf16 v[52:55], v[64:67], v[64:67], 0\n"
"ds_write_b32 v9, v94 offset:960\n"
"v_mfma_f32_16x16x32_bf16 v[36:39], v[128:131], v[68:71], v[36:39]\n"
"ds_read_b128 v[64:67], v3 offset:43008\n"
"v_fmac_f32 v51, v89, v23\n"
"v_mfma_f32_16x16x32_bf16 v[40:43], v[132:135], v[68:71], v[40:43]\n"
"v_cvt_pk_bf16_f32 v48, v48, v49\n"
"v_cvt_pk_bf16_f32 v49, v50, v51\n"
"v_mfma_f32_16x16x32_bf16 v[52:55], v[68:71], v[68:71], v[52:55]\n"
"ds_write_b64 v7, v[44:45] offset:52864\n"
"s_waitcnt lgkmcnt(5)\n"
"v_mfma_f32_16x16x32_bf16 v[36:39], v[160:163], v[72:75], v[36:39]\n"
"ds_read_b128 v[68:71], v3 offset:44032\n"
"v_mfma_f32_16x16x32_bf16 v[40:43], v[164:167], v[72:75], v[40:43]\n"
"v_mfma_f32_16x16x32_bf16 v[52:55], v[72:75], v[72:75], v[52:55]\n"
"ds_write_b64 v8, v[48:49] offset:52864\n"
"v_mfma_f32_16x16x32_bf16 v[36:39], v[192:195], v[76:79], v[36:39]\n"
"ds_read_b128 v[72:75], v3 offset:46080\n"
"v_mfma_f32_16x16x32_bf16 v[40:43], v[196:199], v[76:79], v[40:43]\n"
"v_mfma_f32_16x16x32_bf16 v[52:55], v[76:79], v[76:79], v[52:55]\n"
"ds_read_b128 v[84:87], v11 offset:960\n"
"ds_read_b32 v91, v12 offset:960\n"
"s_waitcnt lgkmcnt(8)\n"
"v_mfma_f32_16x16x32_bf16 v[36:39], v[224:227], v[56:59], v[36:39]\n"
"ds_read_b128 v[76:79], v3 offset:47104\n"
"v_mfma_f32_16x16x32_bf16 v[40:43], v[228:231], v[56:59], v[40:43]\n"
"v_mfma_f32_16x16x32_bf16 v[52:55], v[56:59], v[56:59], v[52:55]\n"
"v_mfma_f32_16x16x32_bf16 v[36:39], v[104:107], v[60:63], v[36:39]\n"
"ds_read_b128 v[56:59], v3 offset:48128\n"
"v_mfma_f32_16x16x32_bf16 v[40:43], v[108:111], v[60:63], v[40:43]\n"
"s_waitcnt lgkmcnt(6)\n"
"v_mfma_f32_16x16x32_bf16 v[36:39], v[112:115], v[64:67], v[36:39]\n"
"ds_read_b128 v[60:63], v3 offset:50176\n"
"ds_write_b32 v10, v52 offset:1600\n"
"v_cndmask_b32 v94, v52, v53, s[34:35]\n"
"v_cndmask_b32 v54, v54, v55, s[34:35]\n"
"v_mfma_f32_16x16x32_bf16 v[40:43], v[116:119], v[64:67], v[40:43]\n"
"s_waitcnt lgkmcnt(5)\n"
"v_add_f32 v84, v84, v85\n"
"v_add_f32 v86, v86, v87\n"
"v_add_f32 v84, v84, v86\n"
"v_cndmask_b32 v94, v94, v54, s[20:21]\n"
"ds_write_b32 v9, v94 offset:1280\n"
"v_mfma_f32_16x16x32_bf16 v[36:39], v[120:123], v[68:71], v[36:39]\n"
"ds_read_b128 v[64:67], v3 offset:51200\n"
"s_waitcnt lgkmcnt(6)\n"
"v_mul_f32 v89, 0x3acccccd, v91\n"
"v_fma_f32 v84, -v91, v89, v84\n"
"v_mfma_f32_16x16x32_bf16 v[40:43], v[124:127], v[68:71], v[40:43]\n"
"v_fmamk_f32 v84, v84, 0x3acccccd, v27\n"
"s_waitcnt lgkmcnt(5)\n"
"v_mfma_f32_16x16x32_bf16 v[36:39], v[136:139], v[72:75], v[36:39]\n"
"ds_read_b128 v[68:71], v3 offset:52224\n"
"v_cmp_u_f32 vcc, v84, v84\n"
"v_mfma_f32_16x16x32_bf16 v[40:43], v[140:143], v[72:75], v[40:43]\n"
"v_max_f32 v84, v84, v27\n"
"v_rsq_f32 v88, v84\n"
"v_mfma_f32_16x16x32_bf16 v[36:39], v[144:147], v[76:79], v[36:39]\n"
"ds_read_b128 v[72:75], v3 offset:54272\n"
"v_mul_f32 v89, -v89, v88\n"
"v_mfma_f32_16x16x32_bf16 v[40:43], v[148:151], v[76:79], v[40:43]\n"
"s_cbranch_vccnz .Lsl_50\n"
".Lbk_51:\n"
"s_waitcnt lgkmcnt(5)\n"
"v_mfma_f32_16x16x32_bf16 v[36:39], v[152:155], v[56:59], v[36:39]\n"
"ds_read_b128 v[76:79], v3 offset:55296\n"
"v_mul_f32 v28, v88, v28\n"
"v_mul_f32 v29, v88, v29\n"
"v_mfma_f32_16x16x32_bf16 v[40:43], v[156:159], v[56:59], v[40:43]\n"
"ds_read_b128 v[44:47], v11 offset:1280\n"
"ds_read_b128 v[48:51], v11 offset:1600\n"
"v_mul_f32 v30, v88, v30\n"
"v_mfma_f32_16x16x32_bf16 v[36:39], v[168:171], v[60:63], v[36:39]\n"
"ds_read_b128 v[56:59], v3 offset:56320\n"
"v_mul_f32 v31, v88, v31\n"
"v_fmac_f32 v28, v89, v16\n"
"v_mfma_f32_16x16x32_bf16 v[40:43], v[172:175], v[60:63], v[40:43]\n"
"v_fmac_f32 v29, v89, v17\n"
"v_fmac_f32 v30, v89, v18\n"
"s_waitcnt lgkmcnt(5)\n"
"v_mfma_f32_16x16x32_bf16 v[36:39], v[176:179], v[64:67], v[36:39]\n"
"ds_read_b128 v[60:63], v4 offset:40960\n"
"v_fmac_f32 v31, v89, v19\n"
"v_cvt_pk_bf16_f32 v28, v28, v29\n"
"v_mfma_f32_16x16x32_bf16 v[40:43], v[180:183], v[64:67], v[40:43]\n"
"v_cvt_pk_bf16_f32 v29, v30, v31\n"
"ds_write_b64 v7, v[28:29] offset:56832\n"
"v_mfma_f32_16x16x32_bf16 v[36:39], v[184:187], v[68:71], v[36:39]\n"
"ds_read_b128 v[64:67], v5 offset:40960\n"
"v_mul_f32 v32, v88, v32\n"
"v_mul_f32 v33, v88, v33\n"
"v_mfma_f32_16x16x32_bf16 v[40:43], v[188:191], v[68:71], v[40:43]\n"
"v_mul_f32 v34, v88, v34\n"
"v_mul_f32 v35, v88, v35\n"
"s_waitcnt lgkmcnt(6)\n"
"v_mfma_f32_16x16x32_bf16 v[36:39], v[200:203], v[72:75], v[36:39]\n"
"ds_read_b128 v[68:71], v6 offset:40960\n"
"s_waitcnt lgkmcnt(6)\n"
"v_add_f32 v44, v44, v45\n"
"v_fmac_f32 v32, v89, v20\n"
"v_mfma_f32_16x16x32_bf16 v[40:43], v[204:207], v[72:75], v[40:43]\n"
"v_add_f32 v46, v46, v47\n"
"s_waitcnt lgkmcnt(5)\n"
"v_add_f32 v48, v48, v49\n"
"v_fmac_f32 v33, v89, v21\n"
"v_mfma_f32_16x16x32_bf16 v[36:39], v[208:211], v[76:79], v[36:39]\n"
"v_add_f32 v50, v50, v51\n"
"v_add_f32 v48, v48, v50\n"
"v_fmac_f32 v34, v89, v22\n"
"v_mfma_f32_16x16x32_bf16 v[40:43], v[212:215], v[76:79], v[40:43]\n"
"v_add_f32 v44, v44, v46\n"
"v_fmac_f32 v35, v89, v23\n"
"s_waitcnt lgkmcnt(3)\n"
"v_mfma_f32_16x16x32_bf16 v[36:39], v[216:219], v[56:59], v[36:39]\n"
"v_mul_f32 v53, 0x3acccccd, v48\n"
"v_fma_f32 v44, -v48, v53, v44\n"
"v_cvt_pk_bf16_f32 v32, v32, v33\n"
"v_mfma_f32_16x16x32_bf16 v[40:43], v[220:223], v[56:59], v[40:43]\n"
"v_fmamk_f32 v44, v44, 0x3acccccd, v27\n"
"v_cmp_u_f32 vcc, v44, v44\n"
"v_mfma_f32_16x16x32_bf16 v[36:39], v[232:235], v[60:63], v[36:39]\n"
"v_max_f32 v44, v44, v27\n"
"v_rsq_f32 v52, v44\n"
"v_mfma_f32_16x16x32_bf16 v[40:43], v[236:239], v[60:63], v[40:43]\n"
"v_mul_f32 v53, -v53, v52\n"
"s_waitcnt lgkmcnt(0)\n"
"v_mfma_f32_16x16x32_bf16 v[36:39], v[240:243], v[64:67], v[36:39]\n"
"s_cbranch_vccnz .Lsl_54\n"
".Lbk_55:\n"
"v_mfma_f32_16x16x32_bf16 v[40:43], v[244:247], v[64:67], v[40:43]\n"
"v_cvt_pk_bf16_f32 v33, v34, v35\n"
"ds_write_b64 v8, v[32:33] offset:56832\n"
"v_mfma_f32_16x16x32_bf16 v[36:39], v[248:251], v[68:71], v[36:39]\n"
"v_mfma_f32_16x16x32_bf16 v[40:43], v[252:255], v[68:71], v[40:43]\n"
"s_nop 4\n"
"v_mul_f32 v36, v52, v36\n"
"v_mul_f32 v37, v52, v37\n"
"ds_write_b32 v13, v13 offset:144\n"
"v_mul_f32 v38, v52, v38\n"
"v_mul_f32 v39, v52, v39\n"
"v_fmac_f32 v36, v53, v16\n"
"v_fmac_f32 v37, v53, v17\n"
"v_fmac_f32 v38, v53, v18\n"
"v_fmac_f32 v39, v53, v19\n"
"v_cvt_pk_bf16_f32 v36, v36, v37\n"
"v_cvt_pk_bf16_f32 v37, v38, v39\n"
"ds_write_b64 v7, v[36:37] offset:40960\n"
"v_mul_f32 v40, v52, v40\n"
"v_mul_f32 v41, v52, v41\n"
"v_mul_f32 v42, v52, v42\n"
"v_mul_f32 v43, v52, v43\n"
"v_fmac_f32 v40, v53, v20\n"
"v_fmac_f32 v41, v53, v21\n"
"v_fmac_f32 v42, v53, v22\n"
"v_fmac_f32 v43, v53, v23\n"
"v_cvt_pk_bf16_f32 v40, v40, v41\n"
"v_cvt_pk_bf16_f32 v41, v42, v43\n"
"ds_write_b64 v8, v[40:41] offset:40960\n"
".Ljn_59:\n"
"v_mov_b32 v46, 0\n"
"v_mov_b32 v47, 0\n"
"v_mov_b32 v50, 0\n"
"v_mov_b32 v51, 0\n"
"s_cmp_lg_u32 s3, 2\n"
"s_cbranch_scc1 .Lnw_60\n"
"s_mov_b32 s17, 0x100000\n"
".Ltp_61:\n"
"ds_read_b32 v90, v14 offset:144\n"
"s_sub_u32 s17, s17, 1\n"
"s_cbranch_scc1 .Lou_62\n"
"s_waitcnt lgkmcnt(0)\n"
"v_cmp_eq_u32 vcc, 0, v90\n"
"s_cbranch_vccnz .Ltp_61\n"
".Lou_62:\n"
"s_waitcnt lgkmcnt(0)\n"
".Lnw_60:\n"
"ds_read_b32 v90, v14 offset:144\n"
"s_mov_b64 exec, 0xffff\n"
"ds_read_b64 v[46:47], v24 offset:44960\n"
"ds_read_b64 v[50:51], v24 offset:45000\n"
"s_mov_b64 exec, -1\n"
"ds_read_b64 v[44:45], v25 offset:44928\n"
"ds_read_b64 v[56:57], v25 offset:41040\n"
"ds_read_b64 v[58:59], v24 offset:41072\n"
"ds_read_b64 v[64:65], v25 offset:45008\n"
"ds_read_b64 v[66:67], v24 offset:45040\n"
"ds_read_b64 v[48:49], v25 offset:44968\n"
"ds_read_b64 v[60:61], v25 offset:41080\n"
"ds_read_b64 v[62:63], v24 offset:41112\n"
"ds_read_b64 v[68:69], v25 offset:45048\n"
"ds_read_b64 v[70:71], v24 offset:45080\n"
"s_waitcnt lgkmcnt(12)\n"
"v_or_b32 v90, s33, v90\n"
"s_mov_b32 s17, 0x100000\n"
"v_cmp_eq_u32 vcc, 0, v90\n"
"s_cbranch_vccz .Lok_64\n"
".Ltp_63:\n"
"ds_read_b32 v90, v14 offset:144\n"
"s_mov_b64 exec, 0xffff\n"
"ds_read_b64 v[46:47], v24 offset:44960\n"
"ds_read_b64 v[50:51], v24 offset:45000\n"
"s_mov_b64 exec, -1\n"
"ds_read_b64 v[44:45], v25 offset:44928\n"
"ds_read_b64 v[56:57], v25 offset:41040\n"
"ds_read_b64 v[58:59], v24 offset:41072\n"
"ds_read_b64 v[64:65], v25 offset:45008\n"
"ds_read_b64 v[66:67], v24 offset:45040\n"
"ds_read_b64 v[48:49], v25 offset:44968\n"
"ds_read_b64 v[60:61], v25 offset:41080\n"
"ds_read_b64 v[62:63], v24 offset:41112\n"
"ds_read_b64 v[68:69], v25 offset:45048\n"
"ds_read_b64 v[70:71], v24 offset:45080\n"
"s_sub_u32 s17, s17, 1\n"
"s_cbranch_scc1 .Lok_64\n"
"s_waitcnt lgkmcnt(12)\n"
"v_cmp_eq_u32 vcc, 0, v90\n"
"s_cbranch_vccnz .Ltp_63\n"
"s_waitcnt lgkmcnt(0)\n"
".Lok_64:\n"
"ds_read_b64_tr_b16 v[28:29], v26 offset:40960\n"
"ds_read_b64_tr_b16 v[30:31], v26 offset:44928\n"
"ds_read_b64_tr_b16 v[32:33], v26 offset:41000\n"
"s_waitcnt lgkmcnt(10)\n"
"v_mfma_f32_16x16x32_bf16 v[56:59], v[56:59], v[44:47], 0\n"
"s_waitcnt lgkmcnt(8)\n"
"v_mfma_f32_16x16x32_bf16 v[64:67], v[64:67], v[44:47], 0\n"
"ds_read_b64_tr_b16 v[34:35], v26 offset:44968\n"
"ds_read_b64_tr_b16 v[36:37], v26 offset:40992\n"
"ds_read_b64_tr_b16 v[38:39], v26 offset:44960\n"
"s_waitcnt lgkmcnt(8)\n"
"v_mfma_f32_16x16x32_bf16 v[60:63], v[60:63], v[48:51], 0\n"
"s_waitcnt lgkmcnt(6)\n"
"v_mfma_f32_16x16x32_bf16 v[68:71], v[68:71], v[48:51], 0\n"
"ds_read_b64_tr_b16 v[40:41], v26 offset:41032\n"
"ds_read_b64_tr_b16 v[42:43], v26 offset:45000\n"
"v_mov_b32 v84, 0x3f803f80\n"
"v_mov_b32 v85, 0x3f803f80\n"
"v_mov_b32 v86, 0x3f803f80\n"
"v_mov_b32 v87, 0x3f803f80\n"
"v_cndmask_b32 v76, v56, v57, s[34:35]\n"
"v_cndmask_b32 v72, v64, v65, s[34:35]\n"
"v_cndmask_b32 v76, v76, v72, s[36:37]\n"
"v_cndmask_b32 v77, v58, v59, s[34:35]\n"
"v_cndmask_b32 v72, v66, v67, s[34:35]\n"
"v_cndmask_b32 v77, v77, v72, s[38:39]\n"
"v_max_f32 v52, v76, v77\n"
"v_max_f32 v54, v76, v77\n"
"v_cndmask_b32 v78, v60, v61, s[34:35]\n"
"v_cndmask_b32 v72, v68, v69, s[34:35]\n"
"v_cndmask_b32 v78, v78, v72, s[36:37]\n"
"v_cndmask_b32 v79, v62, v63, s[34:35]\n"
"v_cndmask_b32 v72, v70, v71, s[34:35]\n"
"v_cndmask_b32 v79, v79, v72, s[38:39]\n"
"v_max_f32 v53, v78, v79\n"
"v_max_f32 v55, v78, v79\n"
"v_permlane16_swap_b32 v52, v54\n"
"s_nop 0\n"
"v_permlane16_swap_b32 v53, v55\n"
"v_max_f32 v72, v52, v54\n"
"v_max_f32 v52, v52, v54\n"
"v_max_f32 v73, v53, v55\n"
"v_max_f32 v53, v53, v55\n"
"v_permlane32_swap_b32 v52, v72\n"
"s_nop 0\n"
"v_permlane32_swap_b32 v53, v73\n"
"v_max_f32 v52, v52, v72\n"
"v_max_f32 v53, v53, v73\n"
"v_mul_f32 v52, 0xbea52b5b, v52\n"
"v_mul_f32 v53, 0xbea52b5b, v53\n"
"v_fmamk_f32 v76, v76, 0x3ea52b5b, v52\n"
"v_fmamk_f32 v77, v77, 0x3ea52b5b, v52\n"
"v_fmamk_f32 v78, v78, 0x3ea52b5b, v53\n"
"v_fmamk_f32 v79, v79, 0x3ea52b5b, v53\n"
"v_exp_f32 v76, v76\n"
"v_exp_f32 v77, v77\n"
"v_exp_f32 v78, v78\n"
"v_exp_f32 v79, v79\n"
"v_cvt_pk_bf16_f32 v52, v76, v77\n"
"v_cvt_pk_bf16_f32 v53, v78, v79\n"
"v_perm_b32 v44, v52, v52, v80\n"
"v_perm_b32 v45, v52, v52, v81\n"
"v_perm_b32 v46, v52, v52, v82\n"
"v_perm_b32 v47, v52, v52, v83\n"
"v_perm_b32 v48, v53, v53, v80\n"
"v_perm_b32 v49, v53, v53, v81\n"
"v_perm_b32 v50, v53, v53, v82\n"
"v_perm_b32 v51, v53, v53, v83\n"
"v_mfma_f32_16x16x32_bf16 v[56:59], v[84:87], v[44:47], 0\n"
"s_nop 0\n"
"v_mfma_f32_16x16x32_bf16 v[60:63], v[84:87], v[48:51], 0\n"
"s_waitcnt lgkmcnt(6)\n"
"v_mfma_f32_16x16x32_bf16 v[28:31], v[28:31], v[44:47], 0\n"
"s_waitcnt lgkmcnt(4)\n"
"v_mfma_f32_16x16x32_bf16 v[32:35], v[32:35], v[48:51], 0\n"
"s_waitcnt lgkmcnt(2)\n"
"v_mfma_f32_16x16x32_bf16 v[36:39], v[36:39], v[44:47], 0\n"
"s_waitcnt lgkmcnt(0)\n"
"v_mfma_f32_16x16x32_bf16 v[40:43], v[40:43], v[48:51], 0\n"
"v_cndmask_b32 v88, 2.0, v1, s[40:41]\n"
"v_rcp_f32 v52, v56\n"
"v_rcp_f32 v53, v60\n"
"s_add_u32 s31, s30, 0x140000\n"
"v_mul_f32 v28, v52, v28\n"
"v_mul_f32 v29, v52, v29\n"
"v_mul_f32 v30, v52, v30\n"
"v_mul_f32 v31, v52, v31\n"
"v_mul_f32 v32, v53, v32\n"
"v_mul_f32 v33, v53, v33\n"
"v_mul_f32 v34, v53, v34\n"
"v_mul_f32 v35, v53, v35\n"
"v_cvt_pk_bf16_f32 v28, v28, v29\n"
"v_cvt_pk_bf16_f32 v29, v30, v31\n"
"v_cvt_pk_bf16_f32 v30, v32, v33\n"
"v_cvt_pk_bf16_f32 v31, v34, v35\n"
"v_min_u32 v28, -2, v28\n"
"v_min_u32 v29, -2, v29\n"
"v_min_u32 v30, -2, v30\n"
"v_min_u32 v31, -2, v31\n"
"buffer_store_dwordx4 v[28:31], v1, s[4:7], s31 offen offset:0 sc1\n"
"v_mul_f32 v36, v52, v36\n"
"v_mul_f32 v37, v52, v37\n"
"v_mul_f32 v38, v52, v38\n"
"v_mul_f32 v39, v52, v39\n"
"v_mul_f32 v40, v53, v40\n"
"v_mul_f32 v41, v53, v41\n"
"v_mul_f32 v42, v53, v42\n"
"v_mul_f32 v43, v53, v43\n"
"v_cvt_pk_bf16_f32 v36, v36, v37\n"
"v_cvt_pk_bf16_f32 v37, v38, v39\n"
"v_cvt_pk_bf16_f32 v38, v40, v41\n"
"v_cvt_pk_bf16_f32 v39, v42, v43\n"
"v_min_u32 v36, -2, v36\n"
"v_min_u32 v37, -2, v37\n"
"v_min_u32 v38, -2, v38\n"
"v_min_u32 v39, -2, v39\n"
"buffer_store_dwordx4 v[36:39], v88, s[4:7], s31 offen offset:1024 sc1\n"
"ds_write_b32 v13, v13 offset:160\n"
"s_nop 2\n"
"s_cmp_lt_i32 s45, 0\n"
"s_cbranch_scc1 .Lnc_65\n"
"s_setprio 1\n"
"s_lshl_b32 s79, s3, 10\n"
"v_lshl_add_u32 v34, v0, 4, s79\n"
"v_lshrrev_b32 v59, 7, v34\n"
"v_and_b32 v58, 0x70, v34\n"
"s_mov_b32 s79, 0x5000\n"
"v_mad_i32_i24 v33, v59, s79, v58\n"
"s_mov_b32 s17, 0x100000\n"
".Ltp_66:\n"
"s_sleep 1\n"
"ds_read_b32 v42, v14 offset:144\n"
"s_sub_u32 s17, s17, 1\n"
"s_cbranch_scc1 .Lou_67\n"
"s_waitcnt lgkmcnt(0)\n"
"v_cmp_eq_u32 vcc, 0, v42\n"
"s_cbranch_vccnz .Ltp_66\n"
".Lou_67:\n"
"s_waitcnt lgkmcnt(0)\n"
"s_mov_b32 s17, 0x100000\n"
".Ltp_68:\n"
"s_sleep 1\n"
"ds_read_b32 v42, v14 offset:176\n"
"s_sub_u32 s17, s17, 1\n"
"s_cbranch_scc1 .Lou_69\n"
"s_waitcnt lgkmcnt(0)\n"
"v_cmp_eq_u32 vcc, 0, v42\n"
"s_cbranch_vccnz .Ltp_68\n"
".Lou_69:\n"
"s_waitcnt lgkmcnt(0)\n"
"s_lshl_b32 s79, s45, 8\n"
"s_add_u32 s46, s79, 0x6c8000\n"
"s_add_u32 s47, s46, 0x140000\n"
"s_and_b32 s79, s2, 7\n"
"s_lshl_b32 s79, s79, 14\n"
"s_add_u32 s48, s79, 0xc08000\n"
"s_add_u32 s49, s48, 0x1000\n"
"v_and_b32 v94, 0xffffff80, v33\n"
"v_add_u32 v94, v94, v33\n"
"buffer_load_dwordx4 v[78:81], v94, s[4:7], s46 offen offset:0 sc1\n"
"buffer_load_dwordx4 v[82:85], v94, s[4:7], s46 offen offset:128 sc1\n"
"buffer_load_dwordx4 v[86:89], v94, s[4:7], s47 offen offset:0 sc1\n"
"buffer_load_dwordx4 v[90:93], v94, s[4:7], s47 offen offset:128 sc1\n"
"s_mul_i32 s79, s3, 0x1200\n"
"s_add_u32 s79, s79, 0x1c400\n"
"v_lshl_add_u32 v44, v0, 3, s79\n"
"v_mov_b32 v46, 0\n"
"v_mov_b32 v47, 0\n"
"ds_write_b64 v44, v[46:47]\n"
"v_lshrrev_b32 v44, 4, v0\n"
"v_mov_b32 v59, 0xffff0000\n"
"v_mov_b32 v58, 0xffff\n"
"s_mov_b32 vcc_lo, 0xaaaaaaaa\n"
"s_mov_b32 vcc_hi, 0xaaaaaaaa\n"
"v_cndmask_b32 v45, v58, v59, vcc\n"
"v_and_b32 v59, 15, v0\n"
"v_lshlrev_b32 v46, 5, v59\n"
"v_and_b32 v59, 15, v0\n"
"v_lshrrev_b32 v47, 2, v59\n"
"s_lshl_b32 s79, s3, 10\n"
"v_lshl_add_u32 v48, v0, 4, s79\n"
"v_lshrrev_b32 v49, 2, v48\n"
"v_lshrrev_b32 v50, 6, v48\n"
"v_lshlrev_b32 v59, 3, v44\n"
"v_and_b32 v58, 15, v0\n"
"v_sub_u32 v59, v59, v58\n"
"v_add_u32 v51, 0x3ef, v59\n"
"v_and_b32 v59, 24, v50\n"
"v_xor_b32 v59, v49, v59\n"
"v_add_u32 v36, 0xa000, v59\n"
"s_lshl_b32 s79, s3, 8\n"
"s_add_u32 s79, s79, 0xa000\n"
"v_and_b32 v59, 15, v0\n"
"v_xor_b32 v59, s3, v59\n"
"v_and_b32 v59, 1, v59\n"
"v_lshrrev_b32 v58, 1, v44\n"
"v_xor_b32 v58, v47, v58\n"
"v_and_b32 v58, 1, v58\n"
"v_lshl_add_u32 v59, v59, 1, v58\n"
"v_and_b32 v58, 15, v0\n"
"v_and_b32 v58, 2, v58\n"
"v_and_b32 v57, 15, v0\n"
"v_lshrrev_b32 v57, 3, v57\n"
"v_lshlrev_b32 v57, 7, v57\n"
"v_lshl_add_u32 v57, v47, 11, v57\n"
"v_lshl_add_u32 v57, v44, 6, v57\n"
"v_lshl_add_u32 v58, v58, 4, v57\n"
"v_lshl_add_u32 v59, v59, 3, v58\n"
"v_add_u32 v31, s79, v59\n"
"s_mul_i32 s79, s3, 0x2100\n"
"s_add_u32 s79, s79, 0x13840\n"
"v_lshrrev_b32 v59, 2, v51\n"
"v_and_b32 v58, 3, v51\n"
"v_mul_i32_i24 v58, 0x840, v58\n"
"v_lshl_add_u32 v59, v59, 3, v58\n"
"v_add_u32 v28, s79, v59\n"
"s_mul_i32 s79, s3, 0x1200\n"
"s_add_u32 s79, s79, 0x1c420\n"
"v_lshl_add_u32 v59, v44, 4, v46\n"
"v_add_u32 v29, s79, v59\n"
"s_lshl_b32 s79, s3, 11\n"
"s_lshr_b32 s78, s3, 1\n"
"s_lshl_b32 s78, s78, 7\n"
"s_add_u32 s79, s79, s78\n"
"s_add_u32 s79, s79, 0xa000\n"
"s_and_b32 s78, s3, 1\n"
"v_xor_b32 v59, v44, v47\n"
"v_xor_b32 v59, s78, v59\n"
"v_and_b32 v59, 3, v59\n"
"v_lshl_add_u32 v59, v59, 3, v46\n"
"v_add_u32 v30, s79, v59\n"
"v_xor_b32 v32, 8, v31\n"
"v_add_u32 v35, 0x1c600, v49\n"
"v_xor_b32 v37, 8, v36\n"
"s_mov_b32 vcc_lo, 0x300003\n"
"s_mov_b32 vcc_hi, 0x30000300\n"
"v_cndmask_b32 v38, 0, v45, vcc\n"
"s_mov_b32 vcc_lo, 0xc0000c\n"
"s_mov_b32 vcc_hi, 0xc0000c00\n"
"v_cndmask_b32 v39, 0, v45, vcc\n"
"s_lshl_b32 s79, s3, 2\n"
"s_add_u32 s79, s79, 0x26800\n"
"v_mov_b32 v40, s79\n"
"v_and_b32 v59, 48, v48\n"
"v_mul_i32_i24 v59, 0x50, v59\n"
"s_mov_b32 s79, 0x2800\n"
"v_mad_i32_i24 v41, v50, s79, v59\n"
"s_mul_i32 s72, s3, 0x2100\n"
"v_lshlrev_b32 v47, 4, v0\n"
"v_lshrrev_b32 v48, 5, v0\n"
"s_add_u32 s73, s72, 0x14000\n"
"s_add_u32 s74, s72, 0x15100\n"
"s_add_u32 s79, s74, 0x7f4\n"
"v_lshlrev_b32 v57, 3, v0\n"
"v_sub_u32 v60, s79, v57\n"
"s_sub_u32 s79, s74, 8\n"
"v_lshlrev_b32 v57, 5, v0\n"
"v_sub_u32 v57, 0x7e0, v57\n"
"v_sub_u32 v56, 0x5f0, v47\n"
"s_mov_b32 vcc_lo, 0\n"
"s_mov_b32 vcc_hi, -1\n"
"v_cndmask_b32 v57, v56, v57, vcc\n"
"v_add_u32 v49, s79, v57\n"
"v_mov_b32 v57, 4\n"
"s_mov_b32 vcc_lo, 0\n"
"s_mov_b32 vcc_hi, 0x80000000\n"
"v_cndmask_b32 v57, 0, v57, vcc\n"
"v_sub_u32 v61, v60, v57\n"
"v_mov_b32 v57, 4\n"
"s_mov_b32 vcc_lo, 0x80000000\n"
"s_mov_b32 vcc_hi, -1\n"
"v_cndmask_b32 v57, 0, v57, vcc\n"
"v_sub_u32 v62, v49, v57\n"
"v_add_u32 v67, s73, v47\n"
"s_add_u32 s79, s73, 0x800\n"
"v_and_b32 v57, 15, v0\n"
"v_lshrrev_b32 v56, 4, v0\n"
"v_mul_i32_i24 v56, 0x840, v56\n"
"v_lshl_add_u32 v57, v57, 2, v56\n"
"v_add_u32 v68, s79, v57\n"
"v_mov_b32 v69, 0\n"
"v_mad_i32_i24 v63, v48, 0, v49\n"
"v_lshl_add_u32 v64, v48, 2, v49\n"
"v_lshl_add_u32 v65, v48, 3, v49\n"
"v_mad_i32_i24 v66, v48, 12, v49\n"
"s_mov_b32 s17, 0x100000\n"
".Ltp_70:\n"
"s_sleep 1\n"
"ds_read_b32 v42, v14 offset:192\n"
"s_sub_u32 s17, s17, 1\n"
"s_cbranch_scc1 .Lou_71\n"
"s_waitcnt lgkmcnt(0)\n"
"v_cmp_eq_u32 vcc, 0, v42\n"
"s_cbranch_vccnz .Ltp_70\n"
".Lou_71:\n"
"s_waitcnt lgkmcnt(0)\n"
"ds_read_b32 v44, v60 offset:12\n"
"ds_read_b32 v45, v60 offset:8\n"
"ds_read_b32 v46, v60 offset:4\n"
"ds_read_b32 v47, v61 offset:4\n"
"ds_read_b32 v48, v61 offset:0\n"
"ds_read_b32 v49, v66 offset:24\n"
"ds_read_b32 v50, v66 offset:20\n"
"ds_read_b32 v51, v66 offset:16\n"
"ds_read_b32 v52, v65 offset:16\n"
"ds_read_b32 v53, v64 offset:16\n"
"ds_read_b32 v54, v64 offset:12\n"
"ds_read_b32 v55, v64 offset:8\n"
"ds_read_b32 v56, v63 offset:8\n"
"ds_read_b32 v57, v62 offset:8\n"
"ds_read_b32 v58, v62 offset:4\n"
"ds_read_b32 v59, v62 offset:0\n"
"v_cmp_ne_u32 vcc, 63, v0\n"
"s_waitcnt lgkmcnt(2)\n"
"s_nop 0\n"
"v_cndmask_b32 v57, 0, v57, vcc\n"
"s_waitcnt lgkmcnt(1)\n"
"v_cndmask_b32 v58, 0, v58, vcc\n"
"s_waitcnt lgkmcnt(0)\n"
"v_cndmask_b32 v59, 0, v59, vcc\n"
"v_cvt_pk_f16_f32 v70, v44, v44\n"
"v_cvt_pk_f16_f32 v71, v45, v45\n"
"v_cvt_pk_f16_f32 v72, v45, v46\n"
"v_cvt_pk_f16_f32 v73, v46, v46\n"
"ds_write_b128 v67, v[70:73] offset:0\n"
"v_cvt_pk_f16_f32 v74, v44, v45\n"
"v_cvt_pk_f16_f32 v75, v45, v45\n"
"v_cvt_pk_f16_f32 v76, v46, v46\n"
"v_cvt_pk_f16_f32 v77, v46, v46\n"
"ds_write_b128 v67, v[74:77] offset:2112\n"
"v_cvt_pk_f16_f32 v70, v45, v45\n"
"v_cvt_pk_f16_f32 v71, v45, v46\n"
"v_cvt_pk_f16_f32 v72, v46, v46\n"
"v_cvt_pk_f16_f32 v73, v46, v47\n"
"ds_write_b128 v67, v[70:73] offset:4224\n"
"v_cvt_pk_f16_f32 v74, v45, v45\n"
"v_cvt_pk_f16_f32 v75, v46, v46\n"
"v_cvt_pk_f16_f32 v76, v46, v46\n"
"v_cvt_pk_f16_f32 v77, v47, v48\n"
"ds_write_b128 v67, v[74:77] offset:6336\n"
"v_cvt_pk_f16_f32 v70, v49, v50\n"
"v_cvt_pk_f16_f32 v71, v51, v52\n"
"v_cvt_pk_f16_f32 v72, v53, v54\n"
"v_cvt_pk_f16_f32 v73, v55, v56\n"
"ds_write_b128 v67, v[70:73] offset:1024\n"
"v_cvt_pk_f16_f32 v74, v50, v51\n"
"v_cvt_pk_f16_f32 v75, v52, v53\n"
"v_cvt_pk_f16_f32 v76, v54, v55\n"
"v_cvt_pk_f16_f32 v77, v56, v57\n"
"ds_write_b128 v67, v[74:77] offset:3136\n"
"v_cvt_pk_f16_f32 v70, v51, v52\n"
"v_cvt_pk_f16_f32 v71, v53, v54\n"
"v_cvt_pk_f16_f32 v72, v55, v56\n"
"v_cvt_pk_f16_f32 v73, v57, v58\n"
"ds_write_b128 v67, v[70:73] offset:5248\n"
"v_cvt_pk_f16_f32 v74, v52, v53\n"
"v_cvt_pk_f16_f32 v75, v54, v55\n"
"v_cvt_pk_f16_f32 v76, v56, v57\n"
"v_cvt_pk_f16_f32 v77, v58, v59\n"
"ds_write_b128 v67, v[74:77] offset:7360\n"
"ds_write_b32 v68, v69\n"
"ds_read_b32 v40, v40\n"
"s_waitcnt lgkmcnt(0)\n"
"v_sub_u32 v44, 0x7f000000, v40\n"
"v_lshrrev_b32 v45, 16, v44\n"
"v_or_b32 v44, v44, v45\n"
"v_and_b32 v38, v44, v38\n"
"v_and_b32 v39, v44, v39\n"
"s_mov_b32 s52, 0x05040100\n"
"s_mov_b32 s53, 0x07060302\n"
"s_mov_b32 s17, 32768\n"
".Ltp_72:\n"
"s_waitcnt vmcnt(0)\n"
"s_sub_u32 s17, s17, 1\n"
"s_cbranch_scc1 .Lou_73\n"
"v_bitop3_b32 v54, v79, v81, s16 bitop3:0x7e\n"
"v_bitop3_b32 v55, v83, v85, s16 bitop3:0x7e\n"
"v_bitop3_b32 v56, v87, v89, s16 bitop3:0x7e\n"
"v_bitop3_b32 v57, v91, v93, s16 bitop3:0x7e\n"
"v_or3_b32 v58, v54, v55, v56\n"
"v_or_b32 v58, v58, v57\n"
"v_cmp_ne_u32 vcc, 0, v58\n"
"s_cbranch_vccz .Lou_73\n"
"s_sleep 2\n"
"v_cmp_ne_u32 vcc, 0, v54\n"
"s_cbranch_vccz .Lsk_74\n"
"s_mov_b64 exec, vcc\n"
"buffer_load_dwordx4 v[78:81], v94, s[4:7], s46 offen offset:0 sc1\n"
"s_mov_b64 exec, -1\n"
".Lsk_74:\n"
"v_cmp_ne_u32 vcc, 0, v55\n"
"s_cbranch_vccz .Lsk_75\n"
"s_mov_b64 exec, vcc\n"
"buffer_load_dwordx4 v[82:85], v94, s[4:7], s46 offen offset:128 sc1\n"
"s_mov_b64 exec, -1\n"
".Lsk_75:\n"
"v_cmp_ne_u32 vcc, 0, v56\n"
"s_cbranch_vccz .Lsk_76\n"
"s_mov_b64 exec, vcc\n"
"buffer_load_dwordx4 v[86:89], v94, s[4:7], s47 offen offset:0 sc1\n"
"s_mov_b64 exec, -1\n"
".Lsk_76:\n"
"v_cmp_ne_u32 vcc, 0, v57\n"
"s_cbranch_vccz .Lsk_77\n"
"s_mov_b64 exec, vcc\n"
"buffer_load_dwordx4 v[90:93], v94, s[4:7], s47 offen offset:128 sc1\n"
"s_mov_b64 exec, -1\n"
".Lsk_77:\n"
"s_branch .Ltp_72\n"
".Lou_73:\n"
"s_waitcnt vmcnt(0)\n"
"buffer_load_dwordx4 v[70:73], v34, s[4:7], s48 offen sc1\n"
"buffer_load_dwordx4 v[74:77], v34, s[4:7], s49 offen sc1\n"
"v_perm_b32 v54, v82, v78, s52\n"
"v_perm_b32 v55, v82, v78, s53\n"
"v_perm_b32 v56, v84, v80, s52\n"
"v_perm_b32 v57, v84, v80, s53\n"
"ds_write_b32 v36, v54 offset:0\n"
"ds_write_b32 v37, v55 offset:2048\n"
"ds_write_b32 v36, v56 offset:4224\n"
"ds_write_b32 v37, v57 offset:6272\n"
"v_perm_b32 v54, v90, v86, s52\n"
"v_perm_b32 v55, v90, v86, s53\n"
"v_perm_b32 v56, v92, v88, s52\n"
"v_perm_b32 v57, v92, v88, s53\n"
"ds_write_b32 v36, v54 offset:1024\n"
"ds_write_b32 v37, v55 offset:3072\n"
"ds_write_b32 v36, v56 offset:5248\n"
"ds_write_b32 v37, v57 offset:7296\n"
"v_lshlrev_b32 v54, 16, v78\n"
"v_lshlrev_b32 v55, 16, v82\n"
"v_and_b32 v56, 0xffff0000, v78\n"
"v_and_b32 v57, 0xffff0000, v82\n"
"v_lshlrev_b32 v58, 16, v80\n"
"v_lshlrev_b32 v59, 16, v84\n"
"v_and_b32 v60, 0xffff0000, v80\n"
"v_and_b32 v61, 0xffff0000, v84\n"
"v_lshlrev_b32 v62, 16, v86\n"
"v_lshlrev_b32 v63, 16, v90\n"
"v_and_b32 v64, 0xffff0000, v86\n"
"v_and_b32 v65, 0xffff0000, v90\n"
"v_lshlrev_b32 v66, 16, v88\n"
"v_lshlrev_b32 v67, 16, v92\n"
"v_and_b32 v68, 0xffff0000, v88\n"
"v_and_b32 v69, 0xffff0000, v92\n"
"s_mov_b32 s17, 32768\n"
".Ltp_78:\n"
"s_waitcnt vmcnt(0)\n"
"s_sub_u32 s17, s17, 1\n"
"s_cbranch_scc1 .Lou_79\n"
"v_max_u32 v78, v70, v71\n"
"v_max3_u32 v78, v72, v73, v78\n"
"v_max3_u32 v78, v74, v75, v78\n"
"v_max3_u32 v78, v76, v77, v78\n"
"v_cmp_eq_u32 vcc, -1, v78\n"
"s_cbranch_vccz .Lou_79\n"
"v_max_u32 v78, v72, v73\n"
"v_max3_u32 v78, v70, v71, v78\n"
"v_cmp_eq_u32 vcc, -1, v78\n"
"s_cbranch_vccz .Lsk_80\n"
"s_mov_b64 exec, vcc\n"
"buffer_load_dwordx4 v[70:73], v34, s[4:7], s48 offen sc1\n"
"s_mov_b64 exec, -1\n"
".Lsk_80:\n"
"v_max_u32 v78, v76, v77\n"
"v_max3_u32 v78, v74, v75, v78\n"
"v_cmp_eq_u32 vcc, -1, v78\n"
"s_cbranch_vccz .Lsk_81\n"
"s_mov_b64 exec, vcc\n"
"buffer_load_dwordx4 v[74:77], v34, s[4:7], s49 offen sc1\n"
"s_mov_b64 exec, -1\n"
".Lsk_81:\n"
"s_branch .Ltp_78\n"
".Lou_79:\n"
"s_waitcnt vmcnt(0)\n"
"v_fma_f32 v54, v54, v71, v70\n"
"v_fma_f32 v55, v55, v73, v72\n"
"v_cvt_pk_f16_f32 v54, v54, v55\n"
"v_fma_f32 v56, v56, v71, v70\n"
"v_fma_f32 v57, v57, v73, v72\n"
"v_cvt_pk_f16_f32 v56, v56, v57\n"
"v_fma_f32 v58, v58, v71, v70\n"
"v_fma_f32 v59, v59, v73, v72\n"
"v_cvt_pk_f16_f32 v58, v58, v59\n"
"v_fma_f32 v60, v60, v71, v70\n"
"v_fma_f32 v61, v61, v73, v72\n"
"v_cvt_pk_f16_f32 v60, v60, v61\n"
"ds_write2st64_b32 v35, v54, v56 offset0:0 offset1:18\n"
"ds_write2st64_b32 v35, v58, v60 offset0:36 offset1:54\n"
"v_fma_f32 v62, v62, v75, v74\n"
"v_fma_f32 v63, v63, v77, v76\n"
"v_cvt_pk_f16_f32 v62, v62, v63\n"
"v_fma_f32 v64, v64, v75, v74\n"
"v_fma_f32 v65, v65, v77, v76\n"
"v_cvt_pk_f16_f32 v64, v64, v65\n"
"v_fma_f32 v66, v66, v75, v74\n"
"v_fma_f32 v67, v67, v77, v76\n"
"v_cvt_pk_f16_f32 v66, v66, v67\n"
"v_fma_f32 v68, v68, v75, v74\n"
"v_fma_f32 v69, v69, v77, v76\n"
"v_cvt_pk_f16_f32 v68, v68, v69\n"
"ds_write2st64_b32 v35, v62, v64 offset0:4 offset1:22\n"
"ds_write2st64_b32 v35, v66, v68 offset0:40 offset1:58\n"
"ds_write_b32 v13, v13 offset:208\n"
"s_mov_b32 s17, 0x100000\n"
".Ltp_82:\n"
"ds_read_b32 v42, v14 offset:208\n"
"s_sub_u32 s17, s17, 1\n"
"s_cbranch_scc1 .Lou_83\n"
"s_waitcnt lgkmcnt(0)\n"
"v_cmp_eq_u32 vcc, 0, v42\n"
"s_cbranch_vccnz .Ltp_82\n"
".Lou_83:\n"
"s_waitcnt lgkmcnt(0)\n"
"s_setprio 1\n"
"s_mov_b32 s50, 0xfffefffe\n"
"ds_read_b64 v[60:61], v28 offset:1984\n"
"ds_read_b64 v[62:63], v28 offset:1992\n"
"ds_read_b128 v[64:67], v29 offset:1984\n"
"ds_read_b128 v[68:71], v29 offset:1472\n"
"ds_read_b64 v[72:73], v28 offset:1472\n"
"ds_read_b64 v[74:75], v28 offset:1480\n"
"ds_read_b128 v[76:79], v29 offset:960\n"
"s_waitcnt lgkmcnt(0)\n"
"v_mfma_f32_16x16x32_f16 v[56:59], v[60:63], v[64:67], 0\n"
"ds_read_b64 v[80:81], v28 offset:960\n"
"ds_read_b64 v[82:83], v28 offset:968\n"
"v_mfma_f32_16x16x32_f16 v[52:55], v[60:63], v[68:71], 0\n"
"ds_read_b128 v[84:87], v29 offset:448\n"
"ds_read_b64 v[88:89], v28 offset:448\n"
"v_mfma_f32_16x16x32_f16 v[56:59], v[72:75], v[68:71], v[56:59]\n"
"ds_read_b64 v[90:91], v28 offset:456\n"
"ds_read_b64 v[92:93], v28 offset:1920\n"
"v_mfma_f32_16x16x32_f16 v[48:51], v[60:63], v[76:79], 0\n"
"ds_read_b64 v[94:95], v28 offset:1928\n"
"s_waitcnt lgkmcnt(4)\n"
"v_mfma_f32_16x16x32_f16 v[52:55], v[72:75], v[76:79], v[52:55]\n"
"ds_read_b128 v[2:5], v29 offset:1920\n"
"ds_read_b128 v[34:37], v29 offset:1408\n"
"v_mfma_f32_16x16x32_f16 v[56:59], v[80:83], v[76:79], v[56:59]\n"
"ds_read_b64 v[64:65], v28 offset:1408\n"
"ds_read_b64 v[66:67], v28 offset:1416\n"
"v_mfma_f32_16x16x32_f16 v[44:47], v[60:63], v[84:87], 0\n"
"ds_read_b128 v[68:71], v29 offset:896\n"
"ds_read_b64 v[76:77], v28 offset:896\n"
"v_mfma_f32_16x16x32_f16 v[48:51], v[72:75], v[84:87], v[48:51]\n"
"ds_read_b64 v[78:79], v28 offset:904\n"
"s_waitcnt lgkmcnt(5)\n"
"v_mfma_f32_16x16x32_f16 v[52:55], v[80:83], v[84:87], v[52:55]\n"
"ds_read_b128 v[60:63], v29 offset:384\n"
"ds_read_b64 v[72:73], v28 offset:384\n"
"v_mfma_f32_16x16x32_f16 v[56:59], v[88:91], v[84:87], v[56:59]\n"
"ds_read_b64 v[74:75], v28 offset:392\n"
"ds_read_b64 v[80:81], v28 offset:1856\n"
"v_mfma_f32_16x16x32_f16 v[56:59], v[92:95], v[2:5], v[56:59]\n"
"ds_read_b64 v[82:83], v28 offset:1864\n"
"ds_read_b128 v[84:87], v29 offset:1856\n"
"v_mfma_f32_16x16x32_f16 v[52:55], v[92:95], v[34:37], v[52:55]\n"
"ds_read_b128 v[88:91], v29 offset:1344\n"
"s_waitcnt lgkmcnt(7)\n"
"v_mfma_f32_16x16x32_f16 v[56:59], v[64:67], v[34:37], v[56:59]\n"
"ds_read_b64 v[2:3], v30 offset:0\n"
"ds_read_b64 v[34:35], v28 offset:1344\n"
"v_mfma_f32_16x16x32_f16 v[48:51], v[92:95], v[68:71], v[48:51]\n"
"ds_read_b64 v[36:37], v28 offset:1352\n"
"v_mfma_f32_16x16x32_f16 v[52:55], v[64:67], v[68:71], v[52:55]\n"
"v_mfma_f32_16x16x32_f16 v[56:59], v[76:79], v[68:71], v[56:59]\n"
"ds_read_b128 v[68:71], v29 offset:832\n"
"s_waitcnt lgkmcnt(8)\n"
"v_mfma_f32_16x16x32_f16 v[44:47], v[92:95], v[60:63], v[44:47]\n"
"ds_read_b64 v[92:93], v28 offset:832\n"
"v_mfma_f32_16x16x32_f16 v[48:51], v[64:67], v[60:63], v[48:51]\n"
"ds_read_b64 v[94:95], v28 offset:840\n"
"v_mfma_f32_16x16x32_f16 v[52:55], v[76:79], v[60:63], v[52:55]\n"
"ds_read_b128 v[64:67], v29 offset:320\n"
"ds_read_b64 v[76:77], v28 offset:320\n"
"v_mfma_f32_16x16x32_f16 v[56:59], v[72:75], v[60:63], v[56:59]\n"
"ds_read_b64 v[78:79], v28 offset:328\n"
"s_waitcnt lgkmcnt(6)\n"
"v_mfma_f32_16x16x32_f16 v[56:59], v[80:83], v[84:87], v[56:59]\n"
"ds_read_b64 v[60:61], v30 offset:512\n"
"ds_read_b64 v[72:73], v28 offset:1792\n"
"v_mfma_f32_16x16x32_f16 v[52:55], v[80:83], v[88:91], v[52:55]\n"
"ds_read_b64 v[74:75], v28 offset:1800\n"
"s_and_b32 s79, s45, 0xfc\n"
"v_mfma_f32_16x16x16_bf16 v[44:47], v[38:39], v[2:3], v[44:47]\n"
"ds_read_b128 v[84:87], v29 offset:1792\n"
"s_and_b32 s78, s45, 1\n"
"v_mfma_f32_16x16x32_f16 v[56:59], v[34:37], v[88:91], v[56:59]\n"
"ds_read_b128 v[2:5], v29 offset:1280\n"
"s_waitcnt lgkmcnt(7)\n"
"v_mfma_f32_16x16x32_f16 v[48:51], v[80:83], v[68:71], v[48:51]\n"
"ds_read_b64 v[88:89], v28 offset:1280\n"
"s_lshl_b32 s78, s78, 1\n"
"v_mfma_f32_16x16x32_f16 v[52:55], v[34:37], v[68:71], v[52:55]\n"
"ds_read_b64 v[90:91], v28 offset:1288\n"
"s_add_u32 s79, s79, s78\n"
"v_mfma_f32_16x16x32_f16 v[56:59], v[92:95], v[68:71], v[56:59]\n"
"ds_read_b128 v[68:71], v29 offset:768\n"
"s_lshr_b32 s78, s45, 1\n"
"v_mfma_f32_16x16x32_f16 v[44:47], v[80:83], v[64:67], v[44:47]\n"
"ds_read_b64 v[80:81], v28 offset:768\n"
"s_waitcnt lgkmcnt(8)\n"
"v_mfma_f32_16x16x32_f16 v[48:51], v[34:37], v[64:67], v[48:51]\n"
"ds_read_b64 v[82:83], v28 offset:776\n"
"s_and_b32 s78, s78, 1\n"
"v_mfma_f32_16x16x32_f16 v[52:55], v[92:95], v[64:67], v[52:55]\n"
"ds_read_b128 v[34:37], v29 offset:256\n"
"ds_read_b64 v[92:93], v28 offset:256\n"
"v_mfma_f32_16x16x32_f16 v[56:59], v[76:79], v[64:67], v[56:59]\n"
"ds_read_b64 v[94:95], v28 offset:264\n"
"ds_read_b64 v[64:65], v28 offset:1728\n"
"v_mfma_f32_16x16x16_bf16 v[48:51], v[38:39], v[60:61], v[48:51]\n"
"ds_read_b64 v[66:67], v28 offset:1736\n"
"s_waitcnt lgkmcnt(7)\n"
"v_mfma_f32_16x16x32_f16 v[56:59], v[72:75], v[84:87], v[56:59]\n"
"ds_read_b128 v[76:79], v29 offset:1728\n"
"ds_read_b128 v[60:63], v29 offset:1216\n"
"v_mfma_f32_16x16x32_f16 v[52:55], v[72:75], v[2:5], v[52:55]\n"
"ds_read_b64 v[84:85], v28 offset:1216\n"
"s_add_u32 s79, s79, s78\n"
"v_mfma_f32_16x16x32_f16 v[56:59], v[88:91], v[2:5], v[56:59]\n"
"ds_read_b64 v[86:87], v28 offset:1224\n"
"s_lshl_b32 s79, s79, 7\n"
"v_mfma_f32_16x16x32_f16 v[48:51], v[72:75], v[68:71], v[48:51]\n"
"ds_read_b128 v[2:5], v29 offset:704\n"
"s_waitcnt lgkmcnt(9)\n"
"v_mfma_f32_16x16x32_f16 v[52:55], v[88:91], v[68:71], v[52:55]\n"
"s_add_u32 s46, s79, 0xc28000\n"
"v_mfma_f32_16x16x32_f16 v[56:59], v[80:83], v[68:71], v[56:59]\n"
"ds_read_b64 v[68:69], v30 offset:1024\n"
"v_mfma_f32_16x16x32_f16 v[44:47], v[72:75], v[34:37], v[44:47]\n"
"ds_read_b64 v[72:73], v28 offset:704\n"
"v_mfma_f32_16x16x32_f16 v[48:51], v[88:91], v[34:37], v[48:51]\n"
"ds_read_b64 v[74:75], v28 offset:712\n"
"s_waitcnt lgkmcnt(6)\n"
"v_mfma_f32_16x16x32_f16 v[52:55], v[80:83], v[34:37], v[52:55]\n"
"ds_read_b128 v[88:91], v29 offset:192\n"
"ds_read_b64 v[80:81], v28 offset:192\n"
"v_mfma_f32_16x16x32_f16 v[56:59], v[92:95], v[34:37], v[56:59]\n"
"ds_read_b64 v[82:83], v28 offset:200\n"
"ds_read_b64 v[34:35], v28 offset:1664\n"
"v_mfma_f32_16x16x32_f16 v[56:59], v[64:67], v[76:79], v[56:59]\n"
"ds_read_b64 v[36:37], v28 offset:1672\n"
"ds_read_b128 v[92:95], v29 offset:1664\n"
"v_mfma_f32_16x16x32_f16 v[52:55], v[64:67], v[60:63], v[52:55]\n"
"ds_read_b128 v[76:79], v29 offset:1152\n"
"s_waitcnt lgkmcnt(9)\n"
"v_mfma_f32_16x16x32_f16 v[56:59], v[84:87], v[60:63], v[56:59]\n"
"ds_read_b64 v[60:61], v28 offset:1152\n"
"v_mfma_f32_16x16x32_f16 v[48:51], v[64:67], v[2:5], v[48:51]\n"
"ds_read_b64 v[62:63], v28 offset:1160\n"
"v_mfma_f32_16x16x16_bf16 v[52:55], v[38:39], v[68:69], v[52:55]\n"
"ds_read_b128 v[68:71], v29 offset:640\n"
"s_nop 4\n"
"v_mfma_f32_16x16x32_f16 v[52:55], v[84:87], v[2:5], v[52:55]\n"
"s_waitcnt lgkmcnt(9)\n"
"v_mfma_f32_16x16x32_f16 v[56:59], v[72:75], v[2:5], v[56:59]\n"
"ds_read_b64 v[2:3], v28 offset:640\n"
"v_mfma_f32_16x16x32_f16 v[44:47], v[64:67], v[88:91], v[44:47]\n"
"ds_read_b64 v[4:5], v28 offset:648\n"
"v_mfma_f32_16x16x32_f16 v[48:51], v[84:87], v[88:91], v[48:51]\n"
"ds_read_b128 v[64:67], v29 offset:128\n"
"v_mfma_f32_16x16x32_f16 v[52:55], v[72:75], v[88:91], v[52:55]\n"
"ds_read_b64 v[84:85], v30 offset:1536\n"
"s_waitcnt lgkmcnt(5)\n"
"v_mfma_f32_16x16x32_f16 v[56:59], v[80:83], v[88:91], v[56:59]\n"
"ds_read_b64 v[72:73], v28 offset:128\n"
"ds_read_b64 v[74:75], v28 offset:136\n"
"v_mfma_f32_16x16x32_f16 v[56:59], v[34:37], v[92:95], v[56:59]\n"
"ds_read_b64 v[88:89], v28 offset:1600\n"
"ds_read_b64 v[90:91], v28 offset:1608\n"
"v_mfma_f32_16x16x32_f16 v[52:55], v[34:37], v[76:79], v[52:55]\n"
"ds_read_b128 v[80:83], v29 offset:1600\n"
"v_mfma_f32_16x16x32_f16 v[56:59], v[60:63], v[76:79], v[56:59]\n"
"ds_read_b128 v[92:95], v29 offset:1088\n"
"s_waitcnt lgkmcnt(7)\n"
"v_mfma_f32_16x16x32_f16 v[48:51], v[34:37], v[68:71], v[48:51]\n"
"ds_read_b64 v[76:77], v28 offset:1088\n"
"v_mfma_f32_16x16x32_f16 v[52:55], v[60:63], v[68:71], v[52:55]\n"
"ds_read_b64 v[78:79], v28 offset:1096\n"
"v_mfma_f32_16x16x32_f16 v[56:59], v[2:5], v[68:71], v[56:59]\n"
"ds_read_b128 v[68:71], v29 offset:576\n"
"v_mfma_f32_16x16x32_f16 v[44:47], v[34:37], v[64:67], v[44:47]\n"
"ds_read_b64 v[34:35], v28 offset:576\n"
"s_waitcnt lgkmcnt(8)\n"
"s_nop 0\n"
"v_mfma_f32_16x16x16_bf16 v[56:59], v[38:39], v[84:85], v[56:59]\n"
"ds_read_b64 v[36:37], v28 offset:584\n"
"v_mfma_f32_16x16x32_f16 v[48:51], v[60:63], v[64:67], v[48:51]\n"
"ds_read_b128 v[84:87], v29 offset:64\n"
"ds_read_b64 v[60:61], v28 offset:64\n"
"v_mfma_f32_16x16x32_f16 v[52:55], v[2:5], v[64:67], v[52:55]\n"
"ds_read_b64 v[62:63], v28 offset:72\n"
"ds_read_b64 v[2:3], v28 offset:1536\n"
"v_mfma_f32_16x16x32_f16 v[56:59], v[72:75], v[64:67], v[56:59]\n"
"ds_read_b64 v[4:5], v28 offset:1544\n"
"s_waitcnt lgkmcnt(7)\n"
"v_mfma_f32_16x16x32_f16 v[56:59], v[88:91], v[80:83], v[56:59]\n"
"ds_read_b128 v[64:67], v29 offset:0\n"
"ds_read_b128 v[72:75], v29 offset:512\n"
"v_mfma_f32_16x16x32_f16 v[52:55], v[88:91], v[92:95], v[52:55]\n"
"ds_read_b64 v[80:81], v28 offset:1024\n"
"v_mfma_f32_16x16x32_f16 v[56:59], v[76:79], v[92:95], v[56:59]\n"
"ds_read_b64 v[82:83], v28 offset:1032\n"
"v_mfma_f32_16x16x32_f16 v[48:51], v[88:91], v[68:71], v[48:51]\n"
"ds_read_b128 v[92:95], v29 offset:1024\n"
"s_waitcnt lgkmcnt(9)\n"
"v_mfma_f32_16x16x32_f16 v[52:55], v[76:79], v[68:71], v[52:55]\n"
"v_mfma_f32_16x16x32_f16 v[56:59], v[34:37], v[68:71], v[56:59]\n"
"ds_read_b64 v[68:69], v28 offset:512\n"
"v_mfma_f32_16x16x32_f16 v[44:47], v[88:91], v[84:87], v[44:47]\n"
"ds_read_b64 v[70:71], v28 offset:520\n"
"v_mfma_f32_16x16x32_f16 v[48:51], v[76:79], v[84:87], v[48:51]\n"
"ds_read_b128 v[88:91], v29 offset:1536\n"
"s_waitcnt lgkmcnt(6)\n"
"v_mfma_f32_16x16x32_f16 v[52:55], v[34:37], v[84:87], v[52:55]\n"
"ds_read_b64 v[76:77], v28 offset:0\n"
"v_mfma_f32_16x16x32_f16 v[56:59], v[60:63], v[84:87], v[56:59]\n"
"ds_read_b64 v[78:79], v28 offset:8\n"
"v_mfma_f32_16x16x32_f16 v[44:47], v[2:5], v[64:67], v[44:47]\n"
"v_mfma_f32_16x16x32_f16 v[48:51], v[2:5], v[72:75], v[48:51]\n"
"s_waitcnt lgkmcnt(3)\n"
"v_mfma_f32_16x16x32_f16 v[48:51], v[80:83], v[64:67], v[48:51]\n"
"v_mfma_f32_16x16x32_f16 v[52:55], v[2:5], v[92:95], v[52:55]\n"
"v_mul_f32 v44, v40, v44\n"
"v_mul_f32 v45, v40, v45\n"
"v_mul_f32 v46, v40, v46\n"
"v_mfma_f32_16x16x32_f16 v[52:55], v[80:83], v[72:75], v[52:55]\n"
"v_mul_f32 v47, v40, v47\n"
"v_cvt_pk_bf16_f32 v44, v44, v45\n"
"v_cvt_pk_bf16_f32 v45, v46, v47\n"
"v_mfma_f32_16x16x32_f16 v[52:55], v[68:71], v[64:67], v[52:55]\n"
"v_pk_min_u16 v44, v44, s50\n"
"v_pk_min_u16 v45, v45, s50\n"
"s_waitcnt lgkmcnt(0)\n"
"v_mfma_f32_16x16x32_f16 v[56:59], v[2:5], v[88:91], v[56:59]\n"
"ds_write_b64 v30, v[44:45] offset:0\n"
"v_mfma_f32_16x16x32_f16 v[56:59], v[80:83], v[92:95], v[56:59]\n"
"v_mul_f32 v48, v40, v48\n"
"v_mul_f32 v49, v40, v49\n"
"v_mfma_f32_16x16x32_f16 v[56:59], v[68:71], v[72:75], v[56:59]\n"
"v_mul_f32 v50, v40, v50\n"
"v_mul_f32 v51, v40, v51\n"
"v_mfma_f32_16x16x32_f16 v[56:59], v[76:79], v[64:67], v[56:59]\n"
"v_cvt_pk_bf16_f32 v48, v48, v49\n"
"v_cvt_pk_bf16_f32 v49, v50, v51\n"
"v_pk_min_u16 v48, v48, s50\n"
"v_pk_min_u16 v49, v49, s50\n"
"ds_write_b64 v30, v[48:49] offset:512\n"
"v_mul_f32 v52, v40, v52\n"
"v_mul_f32 v53, v40, v53\n"
"v_mul_f32 v54, v40, v54\n"
"v_mul_f32 v55, v40, v55\n"
"v_cvt_pk_bf16_f32 v52, v52, v53\n"
"v_cvt_pk_bf16_f32 v53, v54, v55\n"
"v_pk_min_u16 v52, v52, s50\n"
"v_pk_min_u16 v53, v53, s50\n"
"ds_write_b64 v30, v[52:53] offset:1024\n"
"v_mul_f32 v56, v40, v56\n"
"v_mul_f32 v57, v40, v57\n"
"v_mul_f32 v58, v40, v58\n"
"v_mul_f32 v59, v40, v59\n"
"v_cvt_pk_bf16_f32 v56, v56, v57\n"
"v_cvt_pk_bf16_f32 v57, v58, v59\n"
"v_pk_min_u16 v56, v56, s50\n"
"v_pk_min_u16 v57, v57, s50\n"
"ds_write_b64 v30, v[56:57] offset:1536\n"
"ds_write_b32 v13, v13 offset:224\n"
"s_mov_b32 s17, 0x100000\n"
".Ltp_84:\n"
"ds_read_b32 v42, v14 offset:224\n"
"s_sub_u32 s17, s17, 1\n"
"s_cbranch_scc1 .Lou_85\n"
"s_waitcnt lgkmcnt(0)\n"
"v_cmp_eq_u32 vcc, 0, v42\n"
"s_cbranch_vccnz .Ltp_84\n"
".Lou_85:\n"
"s_waitcnt lgkmcnt(0)\n"
"ds_read_b64_tr_b16 v[60:61], v31 offset:0\n"
"ds_read_b64_tr_b16 v[62:63], v32 offset:0\n"
"ds_read_b64_tr_b16 v[64:65], v31 offset:1024\n"
"ds_read_b64_tr_b16 v[66:67], v32 offset:1024\n"
"s_add_u32 s47, s46, 0xa0000\n"
"s_waitcnt lgkmcnt(2)\n"
"buffer_store_dwordx4 v[60:63], v33, s[4:7], s46 offen sc1\n"
"s_waitcnt lgkmcnt(0)\n"
"buffer_store_dwordx4 v[64:67], v33, s[4:7], s47 offen sc1\n"
"ds_write_b32 v13, v13 offset:240\n"
"s_setprio 1\n"
"s_mov_b32 s17, 0x100000\n"
".Ltp_86:\n"
"ds_read_b32 v42, v14 offset:240\n"
"s_sub_u32 s17, s17, 1\n"
"s_cbranch_scc1 .Lou_87\n"
"s_waitcnt lgkmcnt(0)\n"
"v_cmp_eq_u32 vcc, 0, v42\n"
"s_cbranch_vccnz .Ltp_86\n"
".Lou_87:\n"
"s_waitcnt lgkmcnt(0)\n"
"s_nop 2\n"
".Lnc_65:\n"
"v_and_b32 v33, 15, v0\n"
"s_lshl_b32 s74, s43, 10\n"
"v_lshrrev_b32 v34, 4, v0\n"
"v_and_b32 v79, 4, v33\n"
"v_lshrrev_b32 v78, 3, v33\n"
"v_and_b32 v77, 3, v33\n"
"v_lshrrev_b32 v76, 1, v34\n"
"v_and_b32 v75, 1, v34\n"
"v_lshlrev_b32 v75, 7, v75\n"
"v_lshl_add_u32 v76, v76, 9, v75\n"
"v_lshl_add_u32 v77, v77, 4, v76\n"
"v_lshl_add_u32 v78, v78, 6, v77\n"
"v_lshl_add_u32 v35, v79, 1, v78\n"
"v_add_u32 v3, s74, v35\n"
"s_add_u32 s79, s74, 0xfffff400\n"
"s_add_u32 s78, s74, 0x4400\n"
"s_cmp_ge_i32 s43, 3\n"
"s_cselect_b32 s79, s79, s78\n"
"v_add_u32 v4, s79, v35\n"
"s_add_u32 s79, s74, 0xfffff800\n"
"s_add_u32 s78, s74, 0x4800\n"
"s_cmp_ge_i32 s43, 2\n"
"s_cselect_b32 s79, s79, s78\n"
"v_add_u32 v5, s79, v35\n"
"s_add_u32 s79, s74, 0xfffffc00\n"
"s_add_u32 s78, s74, 0x4c00\n"
"s_cmp_ge_i32 s43, 1\n"
"s_cselect_b32 s79, s79, s78\n"
"v_add_u32 v6, s79, v35\n"
"s_mul_i32 s79, s43, 0x1400\n"
"v_lshl_add_u32 v79, v0, 7, s79\n"
"v_mov_b32 v78, 0x40000000\n"
"v_cmp_gt_i32 vcc, 40, v0\n"
"s_nop 1\n"
"v_cndmask_b32 v93, v78, v79, vcc\n"
"v_mov_b32 v28, 0x0c0c0c0c\n"
"v_mov_b32 v29, 0xc0c0100\n"
"v_mov_b32 v30, 0x1000c0c\n"
"v_cndmask_b32 v29, v29, v30, s[34:35]\n"
"v_cndmask_b32 v80, v29, v28, s[36:37]\n"
"v_cndmask_b32 v82, v28, v29, s[36:37]\n"
"v_mov_b32 v29, 0xc0c0302\n"
"v_mov_b32 v30, 0x3020c0c\n"
"v_cndmask_b32 v29, v29, v30, s[34:35]\n"
"v_cndmask_b32 v81, v29, v28, s[38:39]\n"
"v_cndmask_b32 v83, v28, v29, s[38:39]\n"
"s_mov_b32 s17, 0x100000\n"
".Ltp_88:\n"
"ds_read_b32 v90, v14 offset:160\n"
"s_sub_u32 s17, s17, 1\n"
"s_cbranch_scc1 .Lou_89\n"
"s_waitcnt lgkmcnt(0)\n"
"v_cmp_eq_u32 vcc, 0, v90\n"
"s_cbranch_vccnz .Ltp_88\n"
".Lou_89:\n"
"s_waitcnt lgkmcnt(0)\n"
"s_cmp_ge_i32 s45, 0\n"
"s_cbranch_scc1 .Lnw_90\n"
"s_mov_b32 s17, 0x100000\n"
".Ltp_91:\n"
"s_sleep 1\n"
"ds_read_b32 v90, v14 offset:176\n"
"s_sub_u32 s17, s17, 1\n"
"s_cbranch_scc1 .Lou_92\n"
"s_waitcnt lgkmcnt(0)\n"
"v_cmp_eq_u32 vcc, 0, v90\n"
"s_cbranch_vccnz .Ltp_91\n"
".Lou_92:\n"
"s_waitcnt lgkmcnt(0)\n"
"s_mov_b32 s89, 30\n"
".Ldl_93:\n"
"s_sub_u32 s89, s89, 1\n"
"s_cbranch_scc1 .Lde_94\n"
"s_sleep 1\n"
"s_branch .Ldl_93\n"
".Lde_94:\n"
"s_and_b32 s79, s2, 7\n"
"s_lshl_b32 s79, s79, 14\n"
"s_add_u32 s46, s79, 0xc08000\n"
"s_add_u32 s47, s46, 0x1000\n"
"buffer_load_dwordx4 v[28:31], v1, s[4:7], s46 offen offset:0 sc1\n"
"buffer_load_dwordx4 v[32:35], v1, s[4:7], s46 offen offset:1024 sc1\n"
"buffer_load_dwordx4 v[36:39], v1, s[4:7], s46 offen offset:2048 sc1\n"
"buffer_load_dwordx4 v[40:43], v1, s[4:7], s46 offen offset:3072 sc1\n"
"buffer_load_dwordx4 v[44:47], v1, s[4:7], s47 offen offset:0 sc1\n"
"buffer_load_dwordx4 v[48:51], v1, s[4:7], s47 offen offset:1024 sc1\n"
"buffer_load_dwordx4 v[52:55], v1, s[4:7], s47 offen offset:2048 sc1\n"
"buffer_load_dwordx4 v[56:59], v1, s[4:7], s47 offen offset:3072 sc1\n"
"s_mov_b32 s17, 32768\n"
".Ltp_95:\n"
"s_waitcnt vmcnt(0)\n"
"s_sub_u32 s17, s17, 1\n"
"s_cbranch_scc1 .Lou_96\n"
"v_max_u32 v60, v28, v29\n"
"v_max3_u32 v60, v30, v31, v60\n"
"v_max3_u32 v60, v32, v33, v60\n"
"v_max3_u32 v60, v34, v35, v60\n"
"v_max3_u32 v60, v36, v37, v60\n"
"v_max3_u32 v60, v38, v39, v60\n"
"v_max3_u32 v60, v40, v41, v60\n"
"v_max3_u32 v60, v42, v43, v60\n"
"v_max3_u32 v60, v44, v45, v60\n"
"v_max3_u32 v60, v46, v47, v60\n"
"v_max3_u32 v60, v48, v49, v60\n"
"v_max3_u32 v60, v50, v51, v60\n"
"v_max3_u32 v60, v52, v53, v60\n"
"v_max3_u32 v60, v54, v55, v60\n"
"v_max3_u32 v60, v56, v57, v60\n"
"v_max3_u32 v60, v58, v59, v60\n"
"v_cmp_eq_u32 vcc, -1, v60\n"
"s_cbranch_vccz .Lou_96\n"
"s_sleep 4\n"
"v_max_u32 v60, v30, v31\n"
"v_max3_u32 v60, v28, v29, v60\n"
"v_cmp_eq_u32 vcc, -1, v60\n"
"s_cbranch_vccz .Lsk_97\n"
"s_mov_b64 exec, vcc\n"
"buffer_load_dwordx4 v[28:31], v1, s[4:7], s46 offen offset:0 sc1\n"
"s_mov_b64 exec, -1\n"
".Lsk_97:\n"
"v_max_u32 v60, v34, v35\n"
"v_max3_u32 v60, v32, v33, v60\n"
"v_cmp_eq_u32 vcc, -1, v60\n"
"s_cbranch_vccz .Lsk_98\n"
"s_mov_b64 exec, vcc\n"
"buffer_load_dwordx4 v[32:35], v1, s[4:7], s46 offen offset:1024 sc1\n"
"s_mov_b64 exec, -1\n"
".Lsk_98:\n"
"v_max_u32 v60, v38, v39\n"
"v_max3_u32 v60, v36, v37, v60\n"
"v_cmp_eq_u32 vcc, -1, v60\n"
"s_cbranch_vccz .Lsk_99\n"
"s_mov_b64 exec, vcc\n"
"buffer_load_dwordx4 v[36:39], v1, s[4:7], s46 offen offset:2048 sc1\n"
"s_mov_b64 exec, -1\n"
".Lsk_99:\n"
"v_max_u32 v60, v42, v43\n"
"v_max3_u32 v60, v40, v41, v60\n"
"v_cmp_eq_u32 vcc, -1, v60\n"
"s_cbranch_vccz .Lsk_100\n"
"s_mov_b64 exec, vcc\n"
"buffer_load_dwordx4 v[40:43], v1, s[4:7], s46 offen offset:3072 sc1\n"
"s_mov_b64 exec, -1\n"
".Lsk_100:\n"
"v_max_u32 v60, v46, v47\n"
"v_max3_u32 v60, v44, v45, v60\n"
"v_cmp_eq_u32 vcc, -1, v60\n"
"s_cbranch_vccz .Lsk_101\n"
"s_mov_b64 exec, vcc\n"
"buffer_load_dwordx4 v[44:47], v1, s[4:7], s47 offen offset:0 sc1\n"
"s_mov_b64 exec, -1\n"
".Lsk_101:\n"
"v_max_u32 v60, v50, v51\n"
"v_max3_u32 v60, v48, v49, v60\n"
"v_cmp_eq_u32 vcc, -1, v60\n"
"s_cbranch_vccz .Lsk_102\n"
"s_mov_b64 exec, vcc\n"
"buffer_load_dwordx4 v[48:51], v1, s[4:7], s47 offen offset:1024 sc1\n"
"s_mov_b64 exec, -1\n"
".Lsk_102:\n"
"v_max_u32 v60, v54, v55\n"
"v_max3_u32 v60, v52, v53, v60\n"
"v_cmp_eq_u32 vcc, -1, v60\n"
"s_cbranch_vccz .Lsk_103\n"
"s_mov_b64 exec, vcc\n"
"buffer_load_dwordx4 v[52:55], v1, s[4:7], s47 offen offset:2048 sc1\n"
"s_mov_b64 exec, -1\n"
".Lsk_103:\n"
"v_max_u32 v60, v58, v59\n"
"v_max3_u32 v60, v56, v57, v60\n"
"v_cmp_eq_u32 vcc, -1, v60\n"
"s_cbranch_vccz .Lsk_104\n"
"s_mov_b64 exec, vcc\n"
"buffer_load_dwordx4 v[56:59], v1, s[4:7], s47 offen offset:3072 sc1\n"
"s_mov_b64 exec, -1\n"
".Lsk_104:\n"
"s_branch .Ltp_95\n"
".Lou_96:\n"
"s_waitcnt vmcnt(0)\n"
"s_mov_b32 s89, 30\n"
".Ldl_105:\n"
"s_sub_u32 s89, s89, 1\n"
"s_cbranch_scc1 .Lde_106\n"
"s_sleep 1\n"
"s_branch .Ldl_105\n"
".Lde_106:\n"
".Lnw_90:\n"
"s_mov_b32 s89, 8\n"
".Ldl_107:\n"
"s_sub_u32 s89, s89, 1\n"
"s_cbranch_scc1 .Lde_108\n"
"s_sleep 1\n"
"s_branch .Ldl_107\n"
".Lde_108:\n"
"s_cmp_ge_i32 s45, 0\n"
"s_cbranch_scc1 .Lnw_109\n"
"v_min_i32 v79, 3, v0\n"
"v_mul_i32_i24 v28, 0x500, v79\n"
"s_and_b32 s79, s2, 7\n"
"s_lshl_b32 s79, s79, 3\n"
"s_add_u32 s79, s79, 7\n"
"s_mul_i32 s79, s79, 0x5000\n"
"s_and_b32 s78, s2, 7\n"
"s_lshr_b32 s78, s78, 1\n"
"s_lshl_b32 s78, s78, 7\n"
"s_add_u32 s79, s79, s78\n"
"s_add_u32 s79, s79, s29\n"
"s_add_u32 s46, s79, 0xc28000\n"
"s_mov_b32 s17, 32768\n"
".Ltp_110:\n"
"buffer_load_dword v29, v28, s[4:7], s46 offen sc1\n"
"s_sub_u32 s17, s17, 1\n"
"s_cbranch_scc1 .Lou_111\n"
"s_waitcnt vmcnt(0)\n"
"v_cmp_eq_u32 vcc, -1, v29\n"
"s_cbranch_vccz .Lou_111\n"
"s_mov_b32 s89, 2\n"
".Ldl_112:\n"
"s_sub_u32 s89, s89, 1\n"
"s_cbranch_scc1 .Lde_113\n"
"s_sleep 1\n"
"s_branch .Ldl_112\n"
".Lde_113:\n"
"s_branch .Ltp_110\n"
".Lou_111:\n"
"s_waitcnt vmcnt(0)\n"
".Lnw_109:\n"
"s_mov_b32 s17, 0x100000\n"
".Ltp_114:\n"
"ds_read_b32 v90, v14 offset:256\n"
"s_sub_u32 s17, s17, 1\n"
"s_cbranch_scc1 .Lou_115\n"
"s_waitcnt lgkmcnt(0)\n"
"v_cmp_eq_u32 vcc, 0, v90\n"
"s_cbranch_vccnz .Ltp_114\n"
".Lou_115:\n"
"s_waitcnt lgkmcnt(0)\n"
"s_mov_b32 s54, 0\n"
"s_nop 1\n"
".Lag_116:\n"
"v_mov_b32 v92, 0\n"
"s_mov_b32 s46, s44\n"
"s_add_u32 s47, s46, 0x1000\n"
"v_add_u32 v2, s29, v1\n"
"buffer_load_dwordx4 v[28:31], v1, s[4:7], s46 offen offset:0 sc1\n"
"buffer_load_dwordx4 v[32:35], v1, s[4:7], s46 offen offset:1024 sc1\n"
"buffer_load_dwordx4 v[36:39], v1, s[4:7], s46 offen offset:2048 sc1\n"
"buffer_load_dwordx4 v[40:43], v1, s[4:7], s46 offen offset:3072 sc1\n"
"buffer_load_dwordx4 v[44:47], v1, s[4:7], s47 offen offset:0 sc1\n"
"s_mov_b32 s17, 32768\n"
".Ltp_119:\n"
"s_waitcnt vmcnt(0)\n"
"s_sub_u32 s17, s17, 1\n"
"s_cbranch_scc1 .Lou_120\n"
"v_max_u32 v48, v28, v29\n"
"v_max3_u32 v48, v30, v31, v48\n"
"v_max3_u32 v48, v32, v33, v48\n"
"v_max3_u32 v48, v34, v35, v48\n"
"v_max3_u32 v48, v36, v37, v48\n"
"v_max3_u32 v48, v38, v39, v48\n"
"v_max3_u32 v48, v40, v41, v48\n"
"v_max3_u32 v48, v42, v43, v48\n"
"v_max3_u32 v48, v44, v45, v48\n"
"v_max3_u32 v48, v46, v47, v48\n"
"v_cmp_eq_u32 vcc, -1, v48\n"
"s_cbranch_vccz .Lou_120\n"
"s_sleep 1\n"
"v_max_u32 v48, v30, v31\n"
"v_max3_u32 v48, v28, v29, v48\n"
"v_cmp_eq_u32 vcc, -1, v48\n"
"s_cbranch_vccz .Lsk_121\n"
"s_mov_b64 exec, vcc\n"
"buffer_load_dwordx4 v[28:31], v1, s[4:7], s46 offen offset:0 sc1\n"
"s_mov_b64 exec, -1\n"
".Lsk_121:\n"
"v_max_u32 v48, v34, v35\n"
"v_max3_u32 v48, v32, v33, v48\n"
"v_cmp_eq_u32 vcc, -1, v48\n"
"s_cbranch_vccz .Lsk_122\n"
"s_mov_b64 exec, vcc\n"
"buffer_load_dwordx4 v[32:35], v1, s[4:7], s46 offen offset:1024 sc1\n"
"s_mov_b64 exec, -1\n"
".Lsk_122:\n"
"v_max_u32 v48, v38, v39\n"
"v_max3_u32 v48, v36, v37, v48\n"
"v_cmp_eq_u32 vcc, -1, v48\n"
"s_cbranch_vccz .Lsk_123\n"
"s_mov_b64 exec, vcc\n"
"buffer_load_dwordx4 v[36:39], v1, s[4:7], s46 offen offset:2048 sc1\n"
"s_mov_b64 exec, -1\n"
".Lsk_123:\n"
"v_max_u32 v48, v42, v43\n"
"v_max3_u32 v48, v40, v41, v48\n"
"v_cmp_eq_u32 vcc, -1, v48\n"
"s_cbranch_vccz .Lsk_124\n"
"s_mov_b64 exec, vcc\n"
"buffer_load_dwordx4 v[40:43], v1, s[4:7], s46 offen offset:3072 sc1\n"
"s_mov_b64 exec, -1\n"
".Lsk_124:\n"
"v_max_u32 v48, v46, v47\n"
"v_max3_u32 v48, v44, v45, v48\n"
"v_cmp_eq_u32 vcc, -1, v48\n"
"s_cbranch_vccz .Lsk_125\n"
"s_mov_b64 exec, vcc\n"
"buffer_load_dwordx4 v[44:47], v1, s[4:7], s47 offen offset:0 sc1\n"
"s_mov_b64 exec, -1\n"
".Lsk_125:\n"
"s_branch .Ltp_119\n"
".Lou_120:\n"
"s_waitcnt vmcnt(0)\n"
"s_add_u32 m0, s29, 0x5000\n"
"s_add_u32 s31, s44, 0x5000\n"
"buffer_load_dwordx4 v1, s[4:7], s31 offen offset:0 sc1 lds\n"
"ds_write_b128 v2, v[28:31] offset:0\n"
"buffer_load_dwordx4 v1, s[4:7], s31 offen offset:1024 sc1 lds\n"
"ds_write_b128 v2, v[32:35] offset:1024\n"
"buffer_load_dwordx4 v1, s[4:7], s31 offen offset:2048 sc1 lds\n"
"ds_write_b128 v2, v[36:39] offset:2048\n"
"buffer_load_dwordx4 v1, s[4:7], s31 offen offset:3072 sc1 lds\n"
"ds_write_b128 v2, v[40:43] offset:3072\n"
"s_add_u32 m0, m0, 1024\n"
"s_add_u32 s31, s31, 1024\n"
"buffer_load_dwordx4 v1, s[4:7], s31 offen offset:3072 sc1 lds\n"
"ds_write_b128 v2, v[44:47] offset:4096\n"
"ds_write_b32 v13, v13 offset:272\n"
"s_mov_b32 s17, 0x100000\n"
".Ltp_126:\n"
"ds_read_b32 v90, v14 offset:272\n"
"s_sub_u32 s17, s17, 1\n"
"s_cbranch_scc1 .Lou_127\n"
"s_waitcnt lgkmcnt(0)\n"
"v_cmp_eq_u32 vcc, 0, v90\n"
"s_cbranch_vccnz .Ltp_126\n"
".Lou_127:\n"
"s_waitcnt lgkmcnt(0)\n"
"ds_read_b64 v[56:57], v3 offset:0\n"
"ds_read_b64 v[58:59], v3 offset:256\n"
"ds_read_b64 v[60:61], v3 offset:1024\n"
"ds_read_b64 v[62:63], v3 offset:1280\n"
"ds_read_b64 v[64:65], v3 offset:2048\n"
"ds_read_b64 v[66:67], v3 offset:2304\n"
"ds_read_b64 v[68:69], v3 offset:3072\n"
"ds_read_b64 v[70:71], v3 offset:3328\n"
"ds_read_b64 v[72:73], v3 offset:4096\n"
"ds_read_b64 v[74:75], v3 offset:4352\n"
"s_waitcnt lgkmcnt(6)\n"
"v_mfma_f32_16x16x32_bf16 v[28:31], v[96:99], v[56:59], 0\n"
"ds_read_b64 v[76:77], v3 offset:5120\n"
"s_add_u32 m0, s29, 0xa000\n"
"v_mfma_f32_16x16x32_bf16 v[32:35], v[100:103], v[56:59], 0\n"
"ds_read_b64 v[78:79], v3 offset:5376\n"
"s_add_u32 s31, s44, 0xa000\n"
"v_mfma_f32_16x16x32_bf16 v[52:55], v[56:59], v[56:59], 0\n"
"v_mfma_f32_16x16x32_bf16 v[28:31], v[104:107], v[60:63], v[28:31]\n"
"ds_read_b64 v[56:57], v3 offset:6144\n"
"buffer_load_dwordx4 v1, s[4:7], s31 offen offset:0 sc1 lds\n"
"v_mfma_f32_16x16x32_bf16 v[32:35], v[108:111], v[60:63], v[32:35]\n"
"ds_read_b64 v[58:59], v3 offset:6400\n"
"s_waitcnt lgkmcnt(6)\n"
"v_mfma_f32_16x16x32_bf16 v[28:31], v[112:115], v[64:67], v[28:31]\n"
"ds_read_b64 v[60:61], v3 offset:7168\n"
"v_mfma_f32_16x16x32_bf16 v[32:35], v[116:119], v[64:67], v[32:35]\n"
"ds_read_b64 v[62:63], v3 offset:7424\n"
"v_mfma_f32_16x16x32_bf16 v[28:31], v[120:123], v[68:71], v[28:31]\n"
"ds_read_b64 v[64:65], v3 offset:8192\n"
"buffer_load_dwordx4 v1, s[4:7], s31 offen offset:1024 sc1 lds\n"
"v_mfma_f32_16x16x32_bf16 v[32:35], v[124:127], v[68:71], v[32:35]\n"
"ds_read_b64 v[66:67], v3 offset:8448\n"
"s_waitcnt lgkmcnt(6)\n"
"v_mfma_f32_16x16x32_bf16 v[28:31], v[128:131], v[72:75], v[28:31]\n"
"ds_read_b64 v[68:69], v3 offset:9216\n"
"v_mfma_f32_16x16x32_bf16 v[32:35], v[132:135], v[72:75], v[32:35]\n"
"ds_read_b64 v[70:71], v3 offset:9472\n"
"v_mfma_f32_16x16x32_bf16 v[52:55], v[72:75], v[72:75], v[52:55]\n"
"buffer_load_dwordx4 v1, s[4:7], s31 offen offset:2048 sc1 lds\n"
"v_mfma_f32_16x16x32_bf16 v[28:31], v[136:139], v[76:79], v[28:31]\n"
"ds_read_b64 v[72:73], v3 offset:10240\n"
"v_mfma_f32_16x16x32_bf16 v[32:35], v[140:143], v[76:79], v[32:35]\n"
"ds_read_b64 v[74:75], v3 offset:10496\n"
"s_waitcnt lgkmcnt(6)\n"
"v_mfma_f32_16x16x32_bf16 v[28:31], v[144:147], v[56:59], v[28:31]\n"
"ds_read_b64 v[76:77], v3 offset:11264\n"
"v_mfma_f32_16x16x32_bf16 v[32:35], v[148:151], v[56:59], v[32:35]\n"
"ds_read_b64 v[78:79], v3 offset:11520\n"
"buffer_load_dwordx4 v1, s[4:7], s31 offen offset:3072 sc1 lds\n"
"v_mfma_f32_16x16x32_bf16 v[28:31], v[152:155], v[60:63], v[28:31]\n"
"ds_read_b64 v[56:57], v3 offset:12288\n"
"s_add_u32 m0, m0, 1024\n"
"v_mfma_f32_16x16x32_bf16 v[32:35], v[156:159], v[60:63], v[32:35]\n"
"ds_read_b64 v[58:59], v3 offset:12544\n"
"s_waitcnt lgkmcnt(6)\n"
"v_mfma_f32_16x16x32_bf16 v[28:31], v[160:163], v[64:67], v[28:31]\n"
"ds_read_b64 v[60:61], v3 offset:13312\n"
"s_add_u32 s31, s31, 1024\n"
"v_mfma_f32_16x16x32_bf16 v[32:35], v[164:167], v[64:67], v[32:35]\n"
"ds_read_b64 v[62:63], v3 offset:13568\n"
"buffer_load_dwordx4 v1, s[4:7], s31 offen offset:3072 sc1 lds\n"
"v_mfma_f32_16x16x32_bf16 v[52:55], v[64:67], v[64:67], v[52:55]\n"
"s_waitcnt vmcnt(5)\n"
"ds_write_b32 v13, v13 offset:288\n"
"v_mfma_f32_16x16x32_bf16 v[28:31], v[168:171], v[68:71], v[28:31]\n"
"ds_read_b64 v[64:65], v3 offset:14336\n"
"s_add_u32 m0, s29, 0xf000\n"
"s_add_u32 s31, s44, 0xf000\n"
"v_mfma_f32_16x16x32_bf16 v[32:35], v[172:175], v[68:71], v[32:35]\n"
"ds_read_b64 v[66:67], v3 offset:14592\n"
"s_waitcnt lgkmcnt(7)\n"
"v_mfma_f32_16x16x32_bf16 v[28:31], v[176:179], v[72:75], v[28:31]\n"
"ds_read_b64 v[68:69], v3 offset:15360\n"
"buffer_load_dwordx4 v1, s[4:7], s31 offen offset:0 sc1 lds\n"
"v_mfma_f32_16x16x32_bf16 v[32:35], v[180:183], v[72:75], v[32:35]\n"
"ds_read_b64 v[70:71], v3 offset:15616\n"
"v_mfma_f32_16x16x32_bf16 v[28:31], v[184:187], v[76:79], v[28:31]\n"
"ds_read_b64 v[72:73], v3 offset:16384\n"
"v_mfma_f32_16x16x32_bf16 v[32:35], v[188:191], v[76:79], v[32:35]\n"
"ds_read_b64 v[74:75], v3 offset:16640\n"
"s_waitcnt lgkmcnt(7)\n"
"v_mfma_f32_16x16x32_bf16 v[28:31], v[192:195], v[56:59], v[28:31]\n"
"ds_read_b64 v[76:77], v4 offset:0\n"
"buffer_load_dwordx4 v1, s[4:7], s31 offen offset:1024 sc1 lds\n"
"v_mfma_f32_16x16x32_bf16 v[32:35], v[196:199], v[56:59], v[32:35]\n"
"ds_read_b64 v[78:79], v4 offset:256\n"
"ds_read_b32 v90, v14 offset:288\n"
"v_mfma_f32_16x16x32_bf16 v[52:55], v[56:59], v[56:59], v[52:55]\n"
"v_mfma_f32_16x16x32_bf16 v[28:31], v[200:203], v[60:63], v[28:31]\n"
"ds_read_b64 v[56:57], v5 offset:0\n"
"v_mfma_f32_16x16x32_bf16 v[32:35], v[204:207], v[60:63], v[32:35]\n"
"ds_read_b64 v[58:59], v5 offset:256\n"
"buffer_load_dwordx4 v1, s[4:7], s31 offen offset:2048 sc1 lds\n"
"s_waitcnt lgkmcnt(7)\n"
"v_mfma_f32_16x16x32_bf16 v[28:31], v[208:211], v[64:67], v[28:31]\n"
"ds_read_b64 v[60:61], v6 offset:0\n"
"s_waitcnt lgkmcnt(3)\n"
"v_cmp_eq_u32 vcc, 0, v90\n"
"v_mfma_f32_16x16x32_bf16 v[32:35], v[212:215], v[64:67], v[32:35]\n"
"ds_read_b64 v[62:63], v6 offset:256\n"
"v_mfma_f32_16x16x32_bf16 v[28:31], v[216:219], v[68:71], v[28:31]\n"
"s_cbranch_vccnz .Lsl_128\n"
".Lbk_129:\n"
"ds_read_b64 v[64:65], v3 offset:20480\n"
"v_mfma_f32_16x16x32_bf16 v[32:35], v[220:223], v[68:71], v[32:35]\n"
"ds_read_b64 v[66:67], v3 offset:20736\n"
"buffer_load_dwordx4 v1, s[4:7], s31 offen offset:3072 sc1 lds\n"
"v_mfma_f32_16x16x32_bf16 v[28:31], v[224:227], v[72:75], v[28:31]\n"
"ds_read_b64 v[68:69], v3 offset:21504\n"
"s_add_u32 m0, m0, 1024\n"
"v_mfma_f32_16x16x32_bf16 v[32:35], v[228:231], v[72:75], v[32:35]\n"
"ds_read_b64 v[70:71], v3 offset:21760\n"
"s_add_u32 s31, s31, 1024\n"
"v_mfma_f32_16x16x32_bf16 v[52:55], v[72:75], v[72:75], v[52:55]\n"
"v_mfma_f32_16x16x32_bf16 v[28:31], v[232:235], v[76:79], v[28:31]\n"
"ds_read_b64 v[72:73], v3 offset:22528\n"
"buffer_load_dwordx4 v1, s[4:7], s31 offen offset:3072 sc1 lds\n"
"v_mfma_f32_16x16x32_bf16 v[32:35], v[236:239], v[76:79], v[32:35]\n"
"ds_read_b64 v[74:75], v3 offset:22784\n"
"s_waitcnt lgkmcnt(6)\n"
"v_mfma_f32_16x16x32_bf16 v[28:31], v[240:243], v[56:59], v[28:31]\n"
"ds_read_b64 v[76:77], v3 offset:23552\n"
"v_cndmask_b32 v94, v52, v53, s[34:35]\n"
"v_mfma_f32_16x16x32_bf16 v[32:35], v[244:247], v[56:59], v[32:35]\n"
"ds_read_b64 v[78:79], v3 offset:23808\n"
"v_cndmask_b32 v54, v54, v55, s[34:35]\n"
"v_mfma_f32_16x16x32_bf16 v[28:31], v[248:251], v[60:63], v[28:31]\n"
"ds_read_b64 v[56:57], v3 offset:24576\n"
"v_cndmask_b32 v94, v94, v54, s[20:21]\n"
"v_mfma_f32_16x16x32_bf16 v[32:35], v[252:255], v[60:63], v[32:35]\n"
"ds_read_b64 v[58:59], v3 offset:24832\n"
"s_waitcnt lgkmcnt(6)\n"
"v_mfma_f32_16x16x32_bf16 v[36:39], v[96:99], v[64:67], 0\n"
"ds_read_b64 v[60:61], v3 offset:25600\n"
"v_mfma_f32_16x16x32_bf16 v[40:43], v[100:103], v[64:67], 0\n"
"ds_read_b64 v[62:63], v3 offset:25856\n"
"v_cndmask_b32 v94, v28, v94, s[22:23]\n"
"s_waitcnt vmcnt(5)\n"
"v_mfma_f32_16x16x32_bf16 v[52:55], v[64:67], v[64:67], 0\n"
"ds_write_b32 v9, v94 offset:1856\n"
"v_mfma_f32_16x16x32_bf16 v[36:39], v[104:107], v[68:71], v[36:39]\n"
"ds_read_b64 v[64:65], v3 offset:26624\n"
"v_mfma_f32_16x16x32_bf16 v[40:43], v[108:111], v[68:71], v[40:43]\n"
"ds_read_b64 v[66:67], v3 offset:26880\n"
"s_waitcnt lgkmcnt(7)\n"
"v_mfma_f32_16x16x32_bf16 v[36:39], v[112:115], v[72:75], v[36:39]\n"
"ds_read_b64 v[68:69], v3 offset:27648\n"
"v_mfma_f32_16x16x32_bf16 v[40:43], v[116:119], v[72:75], v[40:43]\n"
"ds_read_b64 v[70:71], v3 offset:27904\n"
"v_mfma_f32_16x16x32_bf16 v[36:39], v[120:123], v[76:79], v[36:39]\n"
"ds_read_b64 v[72:73], v3 offset:28672\n"
"v_mfma_f32_16x16x32_bf16 v[40:43], v[124:127], v[76:79], v[40:43]\n"
"ds_read_b64 v[74:75], v3 offset:28928\n"
"s_waitcnt lgkmcnt(7)\n"
"v_mfma_f32_16x16x32_bf16 v[36:39], v[128:131], v[56:59], v[36:39]\n"
"ds_read_b64 v[76:77], v3 offset:29696\n"
"v_mfma_f32_16x16x32_bf16 v[40:43], v[132:135], v[56:59], v[40:43]\n"
"ds_read_b64 v[78:79], v3 offset:29952\n"
"v_mfma_f32_16x16x32_bf16 v[52:55], v[56:59], v[56:59], v[52:55]\n"
"v_mfma_f32_16x16x32_bf16 v[36:39], v[136:139], v[60:63], v[36:39]\n"
"ds_read_b64 v[56:57], v3 offset:30720\n"
"v_mfma_f32_16x16x32_bf16 v[40:43], v[140:143], v[60:63], v[40:43]\n"
"ds_read_b64 v[58:59], v3 offset:30976\n"
"s_waitcnt lgkmcnt(6)\n"
"v_mfma_f32_16x16x32_bf16 v[36:39], v[144:147], v[64:67], v[36:39]\n"
"ds_read_b64 v[60:61], v3 offset:31744\n"
"ds_read_b128 v[84:87], v11 offset:1856\n"
"ds_read_b32 v91, v12 offset:1856\n"
"v_mfma_f32_16x16x32_bf16 v[40:43], v[148:151], v[64:67], v[40:43]\n"
"ds_read_b64 v[62:63], v3 offset:32000\n"
"v_mfma_f32_16x16x32_bf16 v[36:39], v[152:155], v[68:71], v[36:39]\n"
"ds_read_b64 v[64:65], v3 offset:32768\n"
"v_mfma_f32_16x16x32_bf16 v[40:43], v[156:159], v[68:71], v[40:43]\n"
"ds_read_b64 v[66:67], v3 offset:33024\n"
"s_waitcnt lgkmcnt(8)\n"
"v_mfma_f32_16x16x32_bf16 v[36:39], v[160:163], v[72:75], v[36:39]\n"
"ds_read_b64 v[68:69], v3 offset:33792\n"
"v_mfma_f32_16x16x32_bf16 v[40:43], v[164:167], v[72:75], v[40:43]\n"
"ds_read_b64 v[70:71], v3 offset:34048\n"
"v_mfma_f32_16x16x32_bf16 v[52:55], v[72:75], v[72:75], v[52:55]\n"
"v_mfma_f32_16x16x32_bf16 v[36:39], v[168:171], v[76:79], v[36:39]\n"
"ds_read_b64 v[72:73], v3 offset:34816\n"
"s_waitcnt lgkmcnt(7)\n"
"v_add_f32 v84, v84, v85\n"
"v_add_f32 v86, v86, v87\n"
"v_mfma_f32_16x16x32_bf16 v[40:43], v[172:175], v[76:79], v[40:43]\n"
"ds_read_b64 v[74:75], v3 offset:35072\n"
"v_add_f32 v84, v84, v86\n"
"s_waitcnt lgkmcnt(6)\n"
"v_mfma_f32_16x16x32_bf16 v[36:39], v[176:179], v[56:59], v[36:39]\n"
"ds_read_b64 v[76:77], v3 offset:35840\n"
"v_mul_f32 v89, 0x3acccccd, v91\n"
"v_fma_f32 v84, -v91, v89, v84\n"
"v_mfma_f32_16x16x32_bf16 v[40:43], v[180:183], v[56:59], v[40:43]\n"
"ds_read_b64 v[78:79], v3 offset:36096\n"
"v_fmamk_f32 v84, v84, 0x3acccccd, v27\n"
"v_mfma_f32_16x16x32_bf16 v[36:39], v[184:187], v[60:63], v[36:39]\n"
"ds_read_b64 v[56:57], v3 offset:36864\n"
"v_cmp_u_f32 vcc, v84, v84\n"
"v_mfma_f32_16x16x32_bf16 v[40:43], v[188:191], v[60:63], v[40:43]\n"
"ds_read_b64 v[58:59], v3 offset:37120\n"
"s_waitcnt lgkmcnt(6)\n"
"v_mfma_f32_16x16x32_bf16 v[36:39], v[192:195], v[64:67], v[36:39]\n"
"ds_read_b64 v[60:61], v4 offset:20480\n"
"v_max_f32 v84, v84, v27\n"
"v_mfma_f32_16x16x32_bf16 v[40:43], v[196:199], v[64:67], v[40:43]\n"
"ds_read_b64 v[62:63], v4 offset:20736\n"
"v_rsq_f32 v88, v84\n"
"v_mfma_f32_16x16x32_bf16 v[52:55], v[64:67], v[64:67], v[52:55]\n"
"v_mul_f32 v89, -v89, v88\n"
"v_mfma_f32_16x16x32_bf16 v[36:39], v[200:203], v[68:71], v[36:39]\n"
"ds_read_b64 v[64:65], v5 offset:20480\n"
"s_cbranch_vccnz .Lsl_132\n"
".Lbk_133:\n"
"v_mfma_f32_16x16x32_bf16 v[40:43], v[204:207], v[68:71], v[40:43]\n"
"ds_read_b64 v[66:67], v5 offset:20736\n"
"s_add_u32 m0, s29, 0\n"
"s_waitcnt lgkmcnt(6)\n"
"v_mfma_f32_16x16x32_bf16 v[36:39], v[208:211], v[72:75], v[36:39]\n"
"ds_read_b64 v[68:69], v6 offset:20480\n"
"s_sub_u32 s79, s44, 0x5000\n"
"s_add_u32 s78, s29, 0xc28000\n"
"v_mfma_f32_16x16x32_bf16 v[40:43], v[212:215], v[72:75], v[40:43]\n"
"ds_read_b64 v[70:71], v6 offset:20736\n"
"s_max_i32 s31, s79, s78\n"
"v_mul_f32 v28, v88, v28\n"
"v_mfma_f32_16x16x32_bf16 v[36:39], v[216:219], v[76:79], v[36:39]\n"
"ds_read_b64 v[72:73], v3 offset:40960\n"
"buffer_load_dwordx4 v1, s[4:7], s31 offen offset:0 sc1 lds\n"
"v_mul_f32 v29, v88, v29\n"
"v_mfma_f32_16x16x32_bf16 v[40:43], v[220:223], v[76:79], v[40:43]\n"
"ds_read_b64 v[74:75], v3 offset:41216\n"
"s_waitcnt lgkmcnt(6)\n"
"v_mfma_f32_16x16x32_bf16 v[36:39], v[224:227], v[56:59], v[36:39]\n"
"ds_read_b64 v[76:77], v3 offset:41984\n"
"v_mul_f32 v30, v88, v30\n"
"v_mfma_f32_16x16x32_bf16 v[40:43], v[228:231], v[56:59], v[40:43]\n"
"ds_read_b64 v[78:79], v3 offset:42240\n"
"v_mul_f32 v31, v88, v31\n"
"v_mfma_f32_16x16x32_bf16 v[52:55], v[56:59], v[56:59], v[52:55]\n"
"buffer_load_dwordx4 v1, s[4:7], s31 offen offset:1024 sc1 lds\n"
"v_fmac_f32 v28, v89, v16\n"
"v_mfma_f32_16x16x32_bf16 v[36:39], v[232:235], v[60:63], v[36:39]\n"
"ds_read_b64 v[56:57], v3 offset:43008\n"
"v_fmac_f32 v29, v89, v17\n"
"v_mfma_f32_16x16x32_bf16 v[40:43], v[236:239], v[60:63], v[40:43]\n"
"ds_read_b64 v[58:59], v3 offset:43264\n"
"s_waitcnt lgkmcnt(6)\n"
"v_mfma_f32_16x16x32_bf16 v[36:39], v[240:243], v[64:67], v[36:39]\n"
"ds_read_b64 v[60:61], v3 offset:44032\n"
"v_cndmask_b32 v94, v52, v53, s[34:35]\n"
"v_mfma_f32_16x16x32_bf16 v[40:43], v[244:247], v[64:67], v[40:43]\n"
"ds_read_b64 v[62:63], v3 offset:44288\n"
"v_cndmask_b32 v54, v54, v55, s[34:35]\n"
"buffer_load_dwordx4 v1, s[4:7], s31 offen offset:2048 sc1 lds\n"
"v_mfma_f32_16x16x32_bf16 v[36:39], v[248:251], v[68:71], v[36:39]\n"
"ds_read_b64 v[64:65], v3 offset:45056\n"
"v_cndmask_b32 v94, v94, v54, s[20:21]\n"
"v_mfma_f32_16x16x32_bf16 v[40:43], v[252:255], v[68:71], v[40:43]\n"
"ds_read_b64 v[66:67], v3 offset:45312\n"
"s_waitcnt lgkmcnt(6)\n"
"v_mfma_f32_16x16x32_bf16 v[44:47], v[96:99], v[72:75], 0\n"
"ds_read_b64 v[68:69], v3 offset:46080\n"
"v_fmac_f32 v30, v89, v18\n"
"v_mfma_f32_16x16x32_bf16 v[48:51], v[100:103], v[72:75], 0\n"
"ds_read_b64 v[70:71], v3 offset:46336\n"
"v_cndmask_b32 v94, v36, v94, s[22:23]\n"
"s_waitcnt vmcnt(3)\n"
"buffer_load_dwordx4 v1, s[4:7], s31 offen offset:3072 sc1 lds\n"
"v_mfma_f32_16x16x32_bf16 v[52:55], v[72:75], v[72:75], 0\n"
"ds_write_b32 v9, v94 offset:2176\n"
"s_add_u32 m0, m0, 1024\n"
"v_mfma_f32_16x16x32_bf16 v[44:47], v[104:107], v[76:79], v[44:47]\n"
"ds_read_b64 v[72:73], v3 offset:47104\n"
"s_add_u32 s31, s31, 1024\n"
"v_mfma_f32_16x16x32_bf16 v[48:51], v[108:111], v[76:79], v[48:51]\n"
"ds_read_b64 v[74:75], v3 offset:47360\n"
"s_waitcnt lgkmcnt(7)\n"
"v_mfma_f32_16x16x32_bf16 v[44:47], v[112:115], v[56:59], v[44:47]\n"
"ds_read_b64 v[76:77], v3 offset:48128\n"
"buffer_load_dwordx4 v1, s[4:7], s31 offen offset:3072 sc1 lds\n"
"v_mfma_f32_16x16x32_bf16 v[48:51], v[116:119], v[56:59], v[48:51]\n"
"ds_read_b64 v[78:79], v3 offset:48384\n"
"v_fmac_f32 v31, v89, v19\n"
"v_mfma_f32_16x16x32_bf16 v[44:47], v[120:123], v[60:63], v[44:47]\n"
"ds_read_b64 v[56:57], v3 offset:49152\n"
"v_cvt_pk_bf16_f32 v28, v28, v29\n"
"v_mfma_f32_16x16x32_bf16 v[48:51], v[124:127], v[60:63], v[48:51]\n"
"ds_read_b64 v[58:59], v3 offset:49408\n"
"s_waitcnt lgkmcnt(7)\n"
"v_mfma_f32_16x16x32_bf16 v[44:47], v[128:131], v[64:67], v[44:47]\n"
"ds_read_b64 v[60:61], v3 offset:50176\n"
"v_cvt_pk_bf16_f32 v29, v30, v31\n"
"v_mfma_f32_16x16x32_bf16 v[48:51], v[132:135], v[64:67], v[48:51]\n"
"ds_read_b64 v[62:63], v3 offset:50432\n"
"v_mul_f32 v32, v88, v32\n"
"v_mfma_f32_16x16x32_bf16 v[52:55], v[64:67], v[64:67], v[52:55]\n"
"v_mul_f32 v33, v88, v33\n"
"v_mul_f32 v34, v88, v34\n"
"v_mfma_f32_16x16x32_bf16 v[44:47], v[136:139], v[68:71], v[44:47]\n"
"ds_read_b64 v[64:65], v3 offset:51200\n"
"v_mul_f32 v35, v88, v35\n"
"v_mfma_f32_16x16x32_bf16 v[48:51], v[140:143], v[68:71], v[48:51]\n"
"ds_read_b64 v[66:67], v3 offset:51456\n"
"s_waitcnt lgkmcnt(6)\n"
"v_mfma_f32_16x16x32_bf16 v[44:47], v[144:147], v[72:75], v[44:47]\n"
"ds_read_b64 v[68:69], v3 offset:52224\n"
"ds_read_b128 v[84:87], v11 offset:2176\n"
"ds_read_b32 v91, v12 offset:2176\n"
"v_mfma_f32_16x16x32_bf16 v[48:51], v[148:151], v[72:75], v[48:51]\n"
"ds_read_b64 v[70:71], v3 offset:52480\n"
"v_fmac_f32 v32, v89, v20\n"
"v_mfma_f32_16x16x32_bf16 v[44:47], v[152:155], v[76:79], v[44:47]\n"
"ds_read_b64 v[72:73], v3 offset:53248\n"
"v_fmac_f32 v33, v89, v21\n"
"v_mfma_f32_16x16x32_bf16 v[48:51], v[156:159], v[76:79], v[48:51]\n"
"ds_read_b64 v[74:75], v3 offset:53504\n"
"s_waitcnt lgkmcnt(8)\n"
"v_mfma_f32_16x16x32_bf16 v[44:47], v[160:163], v[56:59], v[44:47]\n"
"ds_read_b64 v[76:77], v3 offset:54272\n"
"v_fmac_f32 v34, v89, v22\n"
"v_mfma_f32_16x16x32_bf16 v[48:51], v[164:167], v[56:59], v[48:51]\n"
"ds_read_b64 v[78:79], v3 offset:54528\n"
"v_fmac_f32 v35, v89, v23\n"
"v_mfma_f32_16x16x32_bf16 v[52:55], v[56:59], v[56:59], v[52:55]\n"
"v_cvt_pk_bf16_f32 v32, v32, v33\n"
"v_cvt_pk_bf16_f32 v33, v34, v35\n"
"v_mfma_f32_16x16x32_bf16 v[44:47], v[168:171], v[60:63], v[44:47]\n"
"ds_read_b64 v[56:57], v3 offset:55296\n"
"s_waitcnt lgkmcnt(7)\n"
"v_add_f32 v84, v84, v85\n"
"v_add_f32 v86, v86, v87\n"
"v_mfma_f32_16x16x32_bf16 v[48:51], v[172:175], v[60:63], v[48:51]\n"
"ds_read_b64 v[58:59], v3 offset:55552\n"
"v_add_f32 v84, v84, v86\n"
"s_waitcnt lgkmcnt(6)\n"
"v_mfma_f32_16x16x32_bf16 v[44:47], v[176:179], v[64:67], v[44:47]\n"
"ds_read_b64 v[60:61], v3 offset:56320\n"
"v_mul_f32 v89, 0x3acccccd, v91\n"
"v_fma_f32 v84, -v91, v89, v84\n"
"v_mfma_f32_16x16x32_bf16 v[48:51], v[180:183], v[64:67], v[48:51]\n"
"ds_read_b64 v[62:63], v3 offset:56576\n"
"v_fmamk_f32 v84, v84, 0x3acccccd, v27\n"
"v_mfma_f32_16x16x32_bf16 v[44:47], v[184:187], v[68:71], v[44:47]\n"
"ds_read_b64 v[64:65], v3 offset:57344\n"
"v_cmp_u_f32 vcc, v84, v84\n"
"v_mfma_f32_16x16x32_bf16 v[48:51], v[188:191], v[68:71], v[48:51]\n"
"ds_read_b64 v[66:67], v3 offset:57600\n"
"s_waitcnt lgkmcnt(6)\n"
"v_mfma_f32_16x16x32_bf16 v[44:47], v[192:195], v[72:75], v[44:47]\n"
"ds_read_b64 v[68:69], v4 offset:40960\n"
"v_max_f32 v84, v84, v27\n"
"v_mfma_f32_16x16x32_bf16 v[48:51], v[196:199], v[72:75], v[48:51]\n"
"ds_read_b64 v[70:71], v4 offset:41216\n"
"v_rsq_f32 v88, v84\n"
"v_mfma_f32_16x16x32_bf16 v[52:55], v[72:75], v[72:75], v[52:55]\n"
"v_mul_f32 v89, -v89, v88\n"
"v_mfma_f32_16x16x32_bf16 v[44:47], v[200:203], v[76:79], v[44:47]\n"
"ds_read_b64 v[72:73], v5 offset:40960\n"
"s_cbranch_vccnz .Lsl_136\n"
".Lbk_137:\n"
"v_mfma_f32_16x16x32_bf16 v[48:51], v[204:207], v[76:79], v[48:51]\n"
"ds_read_b64 v[74:75], v5 offset:41216\n"
"ds_write_b64 v7, v[28:29] offset:3968\n"
"s_waitcnt lgkmcnt(7)\n"
"v_mfma_f32_16x16x32_bf16 v[44:47], v[208:211], v[56:59], v[44:47]\n"
"ds_read_b64 v[76:77], v6 offset:40960\n"
"v_mul_f32 v36, v88, v36\n"
"v_mfma_f32_16x16x32_bf16 v[48:51], v[212:215], v[56:59], v[48:51]\n"
"ds_read_b64 v[78:79], v6 offset:41216\n"
"v_mul_f32 v37, v88, v37\n"
"v_mfma_f32_16x16x32_bf16 v[44:47], v[216:219], v[60:63], v[44:47]\n"
"v_add_u32 v3, 0x5000, v3\n"
"ds_read_b64 v[56:57], v3 offset:40960\n"
"v_mfma_f32_16x16x32_bf16 v[48:51], v[220:223], v[60:63], v[48:51]\n"
"ds_read_b64 v[58:59], v3 offset:41216\n"
"s_waitcnt lgkmcnt(7)\n"
"v_mfma_f32_16x16x32_bf16 v[44:47], v[224:227], v[64:67], v[44:47]\n"
"ds_read_b64 v[60:61], v3 offset:41984\n"
"v_mul_f32 v38, v88, v38\n"
"v_mfma_f32_16x16x32_bf16 v[48:51], v[228:231], v[64:67], v[48:51]\n"
"ds_read_b64 v[62:63], v3 offset:42240\n"
"v_mov_b32 v84, 0x3f803f80\n"
"v_mfma_f32_16x16x32_bf16 v[52:55], v[64:67], v[64:67], v[52:55]\n"
"v_mov_b32 v85, 0x3f803f80\n"
"ds_write_b64 v8, v[32:33] offset:3968\n"
"v_mfma_f32_16x16x32_bf16 v[44:47], v[232:235], v[68:71], v[44:47]\n"
"ds_read_b64 v[64:65], v3 offset:43008\n"
"v_mul_f32 v39, v88, v39\n"
"v_mfma_f32_16x16x32_bf16 v[48:51], v[236:239], v[68:71], v[48:51]\n"
"ds_read_b64 v[66:67], v3 offset:43264\n"
"s_waitcnt lgkmcnt(7)\n"
"v_mfma_f32_16x16x32_bf16 v[44:47], v[240:243], v[72:75], v[44:47]\n"
"ds_read_b64 v[68:69], v3 offset:44032\n"
"v_cndmask_b32 v94, v52, v53, s[34:35]\n"
"v_mfma_f32_16x16x32_bf16 v[48:51], v[244:247], v[72:75], v[48:51]\n"
"ds_read_b64 v[70:71], v3 offset:44288\n"
"v_cndmask_b32 v54, v54, v55, s[34:35]\n"
"v_mfma_f32_16x16x32_bf16 v[44:47], v[248:251], v[76:79], v[44:47]\n"
"ds_read_b64 v[72:73], v3 offset:45056\n"
"v_cndmask_b32 v94, v94, v54, s[20:21]\n"
"v_mfma_f32_16x16x32_bf16 v[48:51], v[252:255], v[76:79], v[48:51]\n"
"ds_read_b64 v[74:75], v3 offset:45312\n"
"s_waitcnt lgkmcnt(7)\n"
"v_mfma_f32_16x16x32_bf16 v[28:31], v[96:99], v[56:59], 0\n"
"ds_read_b64 v[76:77], v3 offset:46080\n"
"v_fmac_f32 v36, v89, v16\n"
"v_mfma_f32_16x16x32_bf16 v[32:35], v[100:103], v[56:59], 0\n"
"ds_read_b64 v[78:79], v3 offset:46336\n"
"v_cndmask_b32 v94, v44, v94, s[22:23]\n"
"s_waitcnt vmcnt(0)\n"
"ds_write_b64 v93, v[84:85] offset:0\n"
"v_mfma_f32_16x16x32_bf16 v[52:55], v[56:59], v[56:59], 0\n"
"ds_write_b32 v9, v94 offset:2496\n"
"v_mfma_f32_16x16x32_bf16 v[28:31], v[104:107], v[60:63], v[28:31]\n"
"ds_read_b64 v[56:57], v3 offset:47104\n"
"v_fmac_f32 v37, v89, v17\n"
"v_mfma_f32_16x16x32_bf16 v[32:35], v[108:111], v[60:63], v[32:35]\n"
"ds_read_b64 v[58:59], v3 offset:47360\n"
"s_waitcnt lgkmcnt(8)\n"
"v_mfma_f32_16x16x32_bf16 v[28:31], v[112:115], v[64:67], v[28:31]\n"
"ds_read_b64 v[60:61], v3 offset:48128\n"
"v_fmac_f32 v38, v89, v18\n"
"v_mfma_f32_16x16x32_bf16 v[32:35], v[116:119], v[64:67], v[32:35]\n"
"ds_read_b64 v[62:63], v3 offset:48384\n"
"v_fmac_f32 v39, v89, v19\n"
"v_mfma_f32_16x16x32_bf16 v[28:31], v[120:123], v[68:71], v[28:31]\n"
"ds_read_b64 v[64:65], v3 offset:49152\n"
"v_cvt_pk_bf16_f32 v36, v36, v37\n"
"v_mfma_f32_16x16x32_bf16 v[32:35], v[124:127], v[68:71], v[32:35]\n"
"ds_read_b64 v[66:67], v3 offset:49408\n"
"s_waitcnt lgkmcnt(8)\n"
"v_mfma_f32_16x16x32_bf16 v[28:31], v[128:131], v[72:75], v[28:31]\n"
"ds_read_b64 v[68:69], v3 offset:50176\n"
"v_cvt_pk_bf16_f32 v37, v38, v39\n"
"v_mfma_f32_16x16x32_bf16 v[32:35], v[132:135], v[72:75], v[32:35]\n"
"ds_read_b64 v[70:71], v3 offset:50432\n"
"v_mul_f32 v40, v88, v40\n"
"v_mfma_f32_16x16x32_bf16 v[52:55], v[72:75], v[72:75], v[52:55]\n"
"v_mul_f32 v41, v88, v41\n"
"v_mul_f32 v42, v88, v42\n"
"v_mfma_f32_16x16x32_bf16 v[28:31], v[136:139], v[76:79], v[28:31]\n"
"ds_read_b64 v[72:73], v3 offset:51200\n"
"v_mul_f32 v43, v88, v43\n"
"v_mfma_f32_16x16x32_bf16 v[32:35], v[140:143], v[76:79], v[32:35]\n"
"ds_read_b64 v[74:75], v3 offset:51456\n"
"s_waitcnt lgkmcnt(6)\n"
"v_mfma_f32_16x16x32_bf16 v[28:31], v[144:147], v[56:59], v[28:31]\n"
"ds_read_b64 v[76:77], v3 offset:52224\n"
"ds_read_b128 v[84:87], v11 offset:2496\n"
"ds_read_b32 v91, v12 offset:2496\n"
"v_mfma_f32_16x16x32_bf16 v[32:35], v[148:151], v[56:59], v[32:35]\n"
"ds_read_b64 v[78:79], v3 offset:52480\n"
"v_fmac_f32 v40, v89, v20\n"
"v_mfma_f32_16x16x32_bf16 v[28:31], v[152:155], v[60:63], v[28:31]\n"
"ds_read_b64 v[56:57], v3 offset:53248\n"
"v_fmac_f32 v41, v89, v21\n"
"v_mfma_f32_16x16x32_bf16 v[32:35], v[156:159], v[60:63], v[32:35]\n"
"ds_read_b64 v[58:59], v3 offset:53504\n"
"s_waitcnt lgkmcnt(8)\n"
"v_mfma_f32_16x16x32_bf16 v[28:31], v[160:163], v[64:67], v[28:31]\n"
"ds_read_b64 v[60:61], v3 offset:54272\n"
"v_fmac_f32 v42, v89, v22\n"
"v_mfma_f32_16x16x32_bf16 v[32:35], v[164:167], v[64:67], v[32:35]\n"
"ds_read_b64 v[62:63], v3 offset:54528\n"
"v_fmac_f32 v43, v89, v23\n"
"v_mfma_f32_16x16x32_bf16 v[52:55], v[64:67], v[64:67], v[52:55]\n"
"v_cvt_pk_bf16_f32 v40, v40, v41\n"
"v_cvt_pk_bf16_f32 v41, v42, v43\n"
"v_mfma_f32_16x16x32_bf16 v[28:31], v[168:171], v[68:71], v[28:31]\n"
"ds_read_b64 v[64:65], v3 offset:55296\n"
"s_waitcnt lgkmcnt(7)\n"
"v_add_f32 v84, v84, v85\n"
"v_add_f32 v86, v86, v87\n"
"v_mfma_f32_16x16x32_bf16 v[32:35], v[172:175], v[68:71], v[32:35]\n"
"ds_read_b64 v[66:67], v3 offset:55552\n"
"v_add_f32 v84, v84, v86\n"
"s_waitcnt lgkmcnt(6)\n"
"v_mfma_f32_16x16x32_bf16 v[28:31], v[176:179], v[72:75], v[28:31]\n"
"ds_read_b64 v[68:69], v3 offset:56320\n"
"v_mul_f32 v89, 0x3acccccd, v91\n"
"v_fma_f32 v84, -v91, v89, v84\n"
"v_mfma_f32_16x16x32_bf16 v[32:35], v[180:183], v[72:75], v[32:35]\n"
"ds_read_b64 v[70:71], v3 offset:56576\n"
"v_fmamk_f32 v84, v84, 0x3acccccd, v27\n"
"v_mfma_f32_16x16x32_bf16 v[28:31], v[184:187], v[76:79], v[28:31]\n"
"ds_read_b64 v[72:73], v3 offset:57344\n"
"v_cmp_u_f32 vcc, v84, v84\n"
"v_mfma_f32_16x16x32_bf16 v[32:35], v[188:191], v[76:79], v[32:35]\n"
"ds_read_b64 v[74:75], v3 offset:57600\n"
"s_waitcnt lgkmcnt(6)\n"
"v_mfma_f32_16x16x32_bf16 v[28:31], v[192:195], v[56:59], v[28:31]\n"
"ds_read_b64 v[76:77], v4 offset:61440\n"
"v_max_f32 v84, v84, v27\n"
"v_mfma_f32_16x16x32_bf16 v[32:35], v[196:199], v[56:59], v[32:35]\n"
"ds_read_b64 v[78:79], v4 offset:61696\n"
"v_rsq_f32 v88, v84\n"
"v_mfma_f32_16x16x32_bf16 v[52:55], v[56:59], v[56:59], v[52:55]\n"
"v_mul_f32 v89, -v89, v88\n"
"v_mfma_f32_16x16x32_bf16 v[28:31], v[200:203], v[60:63], v[28:31]\n"
"ds_read_b64 v[56:57], v5 offset:61440\n"
"s_cbranch_vccnz .Lsl_140\n"
".Lbk_141:\n"
"v_mfma_f32_16x16x32_bf16 v[32:35], v[204:207], v[60:63], v[32:35]\n"
"ds_read_b64 v[58:59], v5 offset:61696\n"
"ds_write_b64 v7, v[36:37] offset:7936\n"
"s_waitcnt lgkmcnt(7)\n"
"v_mfma_f32_16x16x32_bf16 v[28:31], v[208:211], v[64:67], v[28:31]\n"
"ds_read_b64 v[60:61], v6 offset:61440\n"
"v_mul_f32 v44, v88, v44\n"
"v_mul_f32 v45, v88, v45\n"
"v_mfma_f32_16x16x32_bf16 v[32:35], v[212:215], v[64:67], v[32:35]\n"
"ds_read_b64 v[62:63], v6 offset:61696\n"
"v_mul_f32 v46, v88, v46\n"
"v_mul_f32 v47, v88, v47\n"
"v_mfma_f32_16x16x32_bf16 v[28:31], v[216:219], v[68:71], v[28:31]\n"
"v_add_u32 v3, 0xffffb000, v3\n"
"ds_read_b64 v[64:65], v3 offset:0\n"
"v_fmac_f32 v44, v89, v16\n"
"v_mfma_f32_16x16x32_bf16 v[32:35], v[220:223], v[68:71], v[32:35]\n"
"ds_read_b64 v[66:67], v3 offset:256\n"
"v_fmac_f32 v45, v89, v17\n"
"s_waitcnt lgkmcnt(7)\n"
"v_mfma_f32_16x16x32_bf16 v[28:31], v[224:227], v[72:75], v[28:31]\n"
"ds_read_b64 v[68:69], v3 offset:4096\n"
"v_fmac_f32 v46, v89, v18\n"
"v_fmac_f32 v47, v89, v19\n"
"v_mfma_f32_16x16x32_bf16 v[32:35], v[228:231], v[72:75], v[32:35]\n"
"ds_read_b64 v[70:71], v3 offset:4352\n"
"v_cvt_pk_bf16_f32 v44, v44, v45\n"
"v_cvt_pk_bf16_f32 v45, v46, v47\n"
"v_mfma_f32_16x16x32_bf16 v[52:55], v[72:75], v[72:75], v[52:55]\n"
"ds_write_b64 v8, v[40:41] offset:7936\n"
"v_mfma_f32_16x16x32_bf16 v[28:31], v[232:235], v[76:79], v[28:31]\n"
"ds_read_b64 v[72:73], v3 offset:8192\n"
"v_mul_f32 v48, v88, v48\n"
"v_mul_f32 v49, v88, v49\n"
"v_mfma_f32_16x16x32_bf16 v[32:35], v[236:239], v[76:79], v[32:35]\n"
"ds_read_b64 v[74:75], v3 offset:8448\n"
"v_mul_f32 v50, v88, v50\n"
"s_waitcnt lgkmcnt(7)\n"
"v_mfma_f32_16x16x32_bf16 v[28:31], v[240:243], v[56:59], v[28:31]\n"
"ds_read_b64 v[76:77], v3 offset:12288\n"
"v_cndmask_b32 v94, v52, v53, s[34:35]\n"
"v_mul_f32 v51, v88, v51\n"
"v_mfma_f32_16x16x32_bf16 v[32:35], v[244:247], v[56:59], v[32:35]\n"
"ds_read_b64 v[78:79], v3 offset:12544\n"
"v_cndmask_b32 v54, v54, v55, s[34:35]\n"
"v_fmac_f32 v48, v89, v20\n"
"v_mfma_f32_16x16x32_bf16 v[28:31], v[248:251], v[60:63], v[28:31]\n"
"ds_read_b64 v[56:57], v3 offset:16384\n"
"v_cndmask_b32 v94, v94, v54, s[20:21]\n"
"v_fmac_f32 v49, v89, v21\n"
"v_mfma_f32_16x16x32_bf16 v[32:35], v[252:255], v[60:63], v[32:35]\n"
"ds_read_b64 v[58:59], v3 offset:16640\n"
"v_fmac_f32 v50, v89, v22\n"
"s_waitcnt lgkmcnt(7)\n"
"s_cmp_eq_u32 s3, 3\n"
"s_cbranch_scc1 .Lfu_152\n"
"ds_read_b64 v[60:61], v3 offset:1024\n"
"v_fmac_f32 v51, v89, v23\n"
"v_mfma_f32_16x16x32_bf16 v[40:43], v[100:103], v[64:67], 0\n"
"ds_read_b64 v[62:63], v3 offset:1280\n"
"v_cndmask_b32 v94, v28, v94, s[22:23]\n"
"v_mfma_f32_16x16x32_bf16 v[52:55], v[64:67], v[64:67], 0\n"
"ds_write_b32 v9, v94 offset:2816\n"
"ds_read_b64 v[64:65], v3 offset:2048\n"
"v_cvt_pk_bf16_f32 v48, v48, v49\n"
"v_mfma_f32_16x16x32_bf16 v[40:43], v[132:135], v[68:71], v[40:43]\n"
"ds_read_b64 v[66:67], v3 offset:2304\n"
"v_cvt_pk_bf16_f32 v49, v50, v51\n"
"v_mfma_f32_16x16x32_bf16 v[52:55], v[68:71], v[68:71], v[52:55]\n"
"ds_write_b64 v7, v[44:45] offset:11904\n"
"s_waitcnt lgkmcnt(8)\n"
"ds_read_b64 v[68:69], v3 offset:3072\n"
"v_mfma_f32_16x16x32_bf16 v[40:43], v[164:167], v[72:75], v[40:43]\n"
"ds_read_b64 v[70:71], v3 offset:3328\n"
"v_mfma_f32_16x16x32_bf16 v[52:55], v[72:75], v[72:75], v[52:55]\n"
"ds_write_b64 v8, v[48:49] offset:11904\n"
"ds_read_b64 v[72:73], v3 offset:5120\n"
"v_mfma_f32_16x16x32_bf16 v[40:43], v[196:199], v[76:79], v[40:43]\n"
"ds_read_b64 v[74:75], v3 offset:5376\n"
"v_mfma_f32_16x16x32_bf16 v[52:55], v[76:79], v[76:79], v[52:55]\n"
"ds_read_b128 v[84:87], v11 offset:2816\n"
"ds_read_b32 v91, v12 offset:2816\n"
"s_waitcnt lgkmcnt(11)\n"
"ds_read_b64 v[76:77], v3 offset:6144\n"
"v_mfma_f32_16x16x32_bf16 v[40:43], v[228:231], v[56:59], v[40:43]\n"
"ds_read_b64 v[78:79], v3 offset:6400\n"
"v_mfma_f32_16x16x32_bf16 v[52:55], v[56:59], v[56:59], v[52:55]\n"
"ds_read_b64 v[56:57], v3 offset:7168\n"
"v_mfma_f32_16x16x32_bf16 v[40:43], v[108:111], v[60:63], v[40:43]\n"
"ds_read_b64 v[58:59], v3 offset:7424\n"
"s_waitcnt lgkmcnt(9)\n"
"ds_read_b64 v[60:61], v3 offset:9216\n"
"s_nop 1\n"
"ds_write_b32 v10, v52 offset:3456\n"
"v_cndmask_b32 v94, v52, v53, s[34:35]\n"
"v_cndmask_b32 v54, v54, v55, s[34:35]\n"
"v_mfma_f32_16x16x32_bf16 v[40:43], v[116:119], v[64:67], v[40:43]\n"
"ds_read_b64 v[62:63], v3 offset:9472\n"
"s_waitcnt lgkmcnt(8)\n"
"v_add_f32 v84, v84, v85\n"
"v_add_f32 v86, v86, v87\n"
"v_cndmask_b32 v94, v94, v54, s[20:21]\n"
"ds_write_b32 v9, v94 offset:3136\n"
"ds_read_b64 v[64:65], v3 offset:10240\n"
"v_add_f32 v84, v84, v86\n"
"s_waitcnt lgkmcnt(9)\n"
"v_mul_f32 v89, 0x3acccccd, v91\n"
"v_mfma_f32_16x16x32_bf16 v[40:43], v[124:127], v[68:71], v[40:43]\n"
"ds_read_b64 v[66:67], v3 offset:10496\n"
"v_fma_f32 v84, -v91, v89, v84\n"
"s_waitcnt lgkmcnt(8)\n"
"ds_read_b64 v[68:69], v3 offset:11264\n"
"v_fmamk_f32 v84, v84, 0x3acccccd, v27\n"
"v_cmp_u_f32 vcc, v84, v84\n"
"v_mfma_f32_16x16x32_bf16 v[40:43], v[140:143], v[72:75], v[40:43]\n"
"ds_read_b64 v[70:71], v3 offset:11520\n"
"v_max_f32 v84, v84, v27\n"
"v_rsq_f32 v88, v84\n"
"ds_read_b64 v[72:73], v3 offset:13312\n"
"v_mul_f32 v89, -v89, v88\n"
"v_mfma_f32_16x16x32_bf16 v[40:43], v[148:151], v[76:79], v[40:43]\n"
"ds_read_b64 v[74:75], v3 offset:13568\n"
"s_cbranch_vccnz .Lsl_144c\n"
".Lbk_145c:\n"
"s_waitcnt lgkmcnt(7)\n"
"ds_read_b64 v[76:77], v3 offset:14336\n"
"v_mul_f32 v28, v88, v28\n"
"v_mul_f32 v29, v88, v29\n"
"v_mfma_f32_16x16x32_bf16 v[40:43], v[156:159], v[56:59], v[40:43]\n"
"ds_read_b64 v[78:79], v3 offset:14592\n"
"ds_read_b128 v[44:47], v11 offset:3136\n"
"ds_read_b128 v[48:51], v11 offset:3456\n"
"ds_read_b64 v[56:57], v3 offset:15360\n"
"v_mul_f32 v30, v88, v30\n"
"v_mul_f32 v31, v88, v31\n"
"v_mfma_f32_16x16x32_bf16 v[40:43], v[172:175], v[60:63], v[40:43]\n"
"ds_read_b64 v[58:59], v3 offset:15616\n"
"v_fmac_f32 v28, v89, v16\n"
"s_waitcnt lgkmcnt(8)\n"
"ds_read_b64 v[60:61], v4 offset:0\n"
"v_fmac_f32 v29, v89, v17\n"
"v_fmac_f32 v30, v89, v18\n"
"v_mfma_f32_16x16x32_bf16 v[40:43], v[180:183], v[64:67], v[40:43]\n"
"ds_read_b64 v[62:63], v4 offset:256\n"
"v_fmac_f32 v31, v89, v19\n"
"v_cvt_pk_bf16_f32 v28, v28, v29\n"
"ds_read_b64 v[64:65], v5 offset:0\n"
"v_cvt_pk_bf16_f32 v29, v30, v31\n"
"ds_write_b64 v7, v[28:29] offset:15872\n"
"v_mfma_f32_16x16x32_bf16 v[40:43], v[188:191], v[68:71], v[40:43]\n"
"ds_read_b64 v[66:67], v5 offset:256\n"
"v_mul_f32 v32, v88, v32\n"
"s_waitcnt lgkmcnt(9)\n"
"ds_read_b64 v[68:69], v6 offset:0\n"
"s_waitcnt lgkmcnt(9)\n"
"v_add_f32 v44, v44, v45\n"
"v_mul_f32 v33, v88, v33\n"
"v_mfma_f32_16x16x32_bf16 v[40:43], v[204:207], v[72:75], v[40:43]\n"
"ds_read_b64 v[70:71], v6 offset:256\n"
"v_add_f32 v46, v46, v47\n"
"v_mul_f32 v34, v88, v34\n"
"s_waitcnt lgkmcnt(9)\n"
"v_add_f32 v48, v48, v49\n"
"v_add_f32 v50, v50, v51\n"
"v_mul_f32 v35, v88, v35\n"
"v_mfma_f32_16x16x32_bf16 v[40:43], v[212:215], v[76:79], v[40:43]\n"
"v_add_f32 v48, v48, v50\n"
"v_fmac_f32 v32, v89, v20\n"
"s_waitcnt lgkmcnt(5)\n"
"v_add_f32 v44, v44, v46\n"
"v_mul_f32 v53, 0x3acccccd, v48\n"
"v_fmac_f32 v33, v89, v21\n"
"v_mfma_f32_16x16x32_bf16 v[40:43], v[220:223], v[56:59], v[40:43]\n"
"v_fma_f32 v44, -v48, v53, v44\n"
"v_fmamk_f32 v44, v44, 0x3acccccd, v27\n"
"v_fmac_f32 v34, v89, v22\n"
"v_cmp_u_f32 vcc, v44, v44\n"
"v_max_f32 v44, v44, v27\n"
"v_fmac_f32 v35, v89, v23\n"
"v_mfma_f32_16x16x32_bf16 v[40:43], v[236:239], v[60:63], v[40:43]\n"
"v_rsq_f32 v52, v44\n"
"s_waitcnt lgkmcnt(0)\n"
"v_mul_f32 v53, -v53, v52\n"
"v_cvt_pk_bf16_f32 v32, v32, v33\n"
"v_cvt_pk_bf16_f32 v33, v34, v35\n"
"v_mfma_f32_16x16x32_bf16 v[40:43], v[244:247], v[64:67], v[40:43]\n"
"s_cbranch_vccnz .Lsl_148c\n"
".Lbk_149c:\n"
"ds_write_b64 v8, v[32:33] offset:15872\n"
"v_mfma_f32_16x16x32_bf16 v[40:43], v[252:255], v[68:71], v[40:43]\n"
"v_cmp_u_f32 vcc, v92, v92\n"
"s_nop 6\n"
"v_mul_f32 v40, v52, v40\n"
"v_mul_f32 v41, v52, v41\n"
"v_mul_f32 v42, v52, v42\n"
"v_mul_f32 v43, v52, v43\n"
"v_fmac_f32 v40, v53, v20\n"
"v_fmac_f32 v41, v53, v21\n"
"v_fmac_f32 v42, v53, v22\n"
"v_fmac_f32 v43, v53, v23\n"
"v_cvt_pk_bf16_f32 v40, v40, v41\n"
"v_cvt_pk_bf16_f32 v41, v42, v43\n"
"ds_write_b64 v8, v[40:41] offset:0\n"
"s_cbranch_vccnz .Lrd_118\n"
".Lgo_117c:\n"
"ds_write_b32 v13, v13 offset:304\n"
"s_branch .Ljn_153\n"
".Lfu_152:\n"
"s_nop 0\n"
"v_mfma_f32_16x16x32_bf16 v[36:39], v[96:99], v[64:67], 0\n"
"ds_read_b64 v[60:61], v3 offset:1024\n"
"v_fmac_f32 v51, v89, v23\n"
"v_mfma_f32_16x16x32_bf16 v[40:43], v[100:103], v[64:67], 0\n"
"ds_read_b64 v[62:63], v3 offset:1280\n"
"v_cndmask_b32 v94, v28, v94, s[22:23]\n"
"v_mfma_f32_16x16x32_bf16 v[52:55], v[64:67], v[64:67], 0\n"
"ds_write_b32 v9, v94 offset:2816\n"
"v_mfma_f32_16x16x32_bf16 v[36:39], v[128:131], v[68:71], v[36:39]\n"
"ds_read_b64 v[64:65], v3 offset:2048\n"
"v_cvt_pk_bf16_f32 v48, v48, v49\n"
"v_mfma_f32_16x16x32_bf16 v[40:43], v[132:135], v[68:71], v[40:43]\n"
"ds_read_b64 v[66:67], v3 offset:2304\n"
"v_cvt_pk_bf16_f32 v49, v50, v51\n"
"v_mfma_f32_16x16x32_bf16 v[52:55], v[68:71], v[68:71], v[52:55]\n"
"ds_write_b64 v7, v[44:45] offset:11904\n"
"s_waitcnt lgkmcnt(8)\n"
"v_mfma_f32_16x16x32_bf16 v[36:39], v[160:163], v[72:75], v[36:39]\n"
"ds_read_b64 v[68:69], v3 offset:3072\n"
"v_mfma_f32_16x16x32_bf16 v[40:43], v[164:167], v[72:75], v[40:43]\n"
"ds_read_b64 v[70:71], v3 offset:3328\n"
"v_mfma_f32_16x16x32_bf16 v[52:55], v[72:75], v[72:75], v[52:55]\n"
"ds_write_b64 v8, v[48:49] offset:11904\n"
"v_mfma_f32_16x16x32_bf16 v[36:39], v[192:195], v[76:79], v[36:39]\n"
"ds_read_b64 v[72:73], v3 offset:5120\n"
"v_mfma_f32_16x16x32_bf16 v[40:43], v[196:199], v[76:79], v[40:43]\n"
"ds_read_b64 v[74:75], v3 offset:5376\n"
"v_mfma_f32_16x16x32_bf16 v[52:55], v[76:79], v[76:79], v[52:55]\n"
"ds_read_b128 v[84:87], v11 offset:2816\n"
"ds_read_b32 v91, v12 offset:2816\n"
"s_waitcnt lgkmcnt(11)\n"
"v_mfma_f32_16x16x32_bf16 v[36:39], v[224:227], v[56:59], v[36:39]\n"
"ds_read_b64 v[76:77], v3 offset:6144\n"
"v_mfma_f32_16x16x32_bf16 v[40:43], v[228:231], v[56:59], v[40:43]\n"
"ds_read_b64 v[78:79], v3 offset:6400\n"
"v_mfma_f32_16x16x32_bf16 v[52:55], v[56:59], v[56:59], v[52:55]\n"
"v_mfma_f32_16x16x32_bf16 v[36:39], v[104:107], v[60:63], v[36:39]\n"
"ds_read_b64 v[56:57], v3 offset:7168\n"
"v_mfma_f32_16x16x32_bf16 v[40:43], v[108:111], v[60:63], v[40:43]\n"
"ds_read_b64 v[58:59], v3 offset:7424\n"
"s_waitcnt lgkmcnt(9)\n"
"v_mfma_f32_16x16x32_bf16 v[36:39], v[112:115], v[64:67], v[36:39]\n"
"ds_read_b64 v[60:61], v3 offset:9216\n"
"ds_write_b32 v10, v52 offset:3456\n"
"v_cndmask_b32 v94, v52, v53, s[34:35]\n"
"v_cndmask_b32 v54, v54, v55, s[34:35]\n"
"v_mfma_f32_16x16x32_bf16 v[40:43], v[116:119], v[64:67], v[40:43]\n"
"ds_read_b64 v[62:63], v3 offset:9472\n"
"s_waitcnt lgkmcnt(8)\n"
"v_add_f32 v84, v84, v85\n"
"v_add_f32 v86, v86, v87\n"
"v_cndmask_b32 v94, v94, v54, s[20:21]\n"
"ds_write_b32 v9, v94 offset:3136\n"
"v_mfma_f32_16x16x32_bf16 v[36:39], v[120:123], v[68:71], v[36:39]\n"
"ds_read_b64 v[64:65], v3 offset:10240\n"
"v_add_f32 v84, v84, v86\n"
"s_waitcnt lgkmcnt(9)\n"
"v_mul_f32 v89, 0x3acccccd, v91\n"
"v_mfma_f32_16x16x32_bf16 v[40:43], v[124:127], v[68:71], v[40:43]\n"
"ds_read_b64 v[66:67], v3 offset:10496\n"
"v_fma_f32 v84, -v91, v89, v84\n"
"s_waitcnt lgkmcnt(8)\n"
"v_mfma_f32_16x16x32_bf16 v[36:39], v[136:139], v[72:75], v[36:39]\n"
"ds_read_b64 v[68:69], v3 offset:11264\n"
"v_fmamk_f32 v84, v84, 0x3acccccd, v27\n"
"v_cmp_u_f32 vcc, v84, v84\n"
"v_mfma_f32_16x16x32_bf16 v[40:43], v[140:143], v[72:75], v[40:43]\n"
"ds_read_b64 v[70:71], v3 offset:11520\n"
"v_max_f32 v84, v84, v27\n"
"v_rsq_f32 v88, v84\n"
"v_mfma_f32_16x16x32_bf16 v[36:39], v[144:147], v[76:79], v[36:39]\n"
"ds_read_b64 v[72:73], v3 offset:13312\n"
"v_mul_f32 v89, -v89, v88\n"
"v_mfma_f32_16x16x32_bf16 v[40:43], v[148:151], v[76:79], v[40:43]\n"
"ds_read_b64 v[74:75], v3 offset:13568\n"
"s_cbranch_vccnz .Lsl_144\n"
".Lbk_145:\n"
"s_waitcnt lgkmcnt(7)\n"
"v_mfma_f32_16x16x32_bf16 v[36:39], v[152:155], v[56:59], v[36:39]\n"
"ds_read_b64 v[76:77], v3 offset:14336\n"
"v_mul_f32 v28, v88, v28\n"
"v_mul_f32 v29, v88, v29\n"
"v_mfma_f32_16x16x32_bf16 v[40:43], v[156:159], v[56:59], v[40:43]\n"
"ds_read_b64 v[78:79], v3 offset:14592\n"
"ds_read_b128 v[44:47], v11 offset:3136\n"
"ds_read_b128 v[48:51], v11 offset:3456\n"
"v_mfma_f32_16x16x32_bf16 v[36:39], v[168:171], v[60:63], v[36:39]\n"
"ds_read_b64 v[56:57], v3 offset:15360\n"
"v_mul_f32 v30, v88, v30\n"
"v_mul_f32 v31, v88, v31\n"
"v_mfma_f32_16x16x32_bf16 v[40:43], v[172:175], v[60:63], v[40:43]\n"
"ds_read_b64 v[58:59], v3 offset:15616\n"
"v_fmac_f32 v28, v89, v16\n"
"s_waitcnt lgkmcnt(8)\n"
"v_mfma_f32_16x16x32_bf16 v[36:39], v[176:179], v[64:67], v[36:39]\n"
"ds_read_b64 v[60:61], v4 offset:0\n"
"v_fmac_f32 v29, v89, v17\n"
"v_fmac_f32 v30, v89, v18\n"
"v_mfma_f32_16x16x32_bf16 v[40:43], v[180:183], v[64:67], v[40:43]\n"
"ds_read_b64 v[62:63], v4 offset:256\n"
"v_fmac_f32 v31, v89, v19\n"
"v_cvt_pk_bf16_f32 v28, v28, v29\n"
"v_mfma_f32_16x16x32_bf16 v[36:39], v[184:187], v[68:71], v[36:39]\n"
"ds_read_b64 v[64:65], v5 offset:0\n"
"v_cvt_pk_bf16_f32 v29, v30, v31\n"
"ds_write_b64 v7, v[28:29] offset:15872\n"
"v_mfma_f32_16x16x32_bf16 v[40:43], v[188:191], v[68:71], v[40:43]\n"
"ds_read_b64 v[66:67], v5 offset:256\n"
"v_mul_f32 v32, v88, v32\n"
"s_waitcnt lgkmcnt(9)\n"
"v_mfma_f32_16x16x32_bf16 v[36:39], v[200:203], v[72:75], v[36:39]\n"
"ds_read_b64 v[68:69], v6 offset:0\n"
"s_waitcnt lgkmcnt(9)\n"
"v_add_f32 v44, v44, v45\n"
"v_mul_f32 v33, v88, v33\n"
"v_mfma_f32_16x16x32_bf16 v[40:43], v[204:207], v[72:75], v[40:43]\n"
"ds_read_b64 v[70:71], v6 offset:256\n"
"v_add_f32 v46, v46, v47\n"
"v_mul_f32 v34, v88, v34\n"
"v_mfma_f32_16x16x32_bf16 v[36:39], v[208:211], v[76:79], v[36:39]\n"
"s_waitcnt lgkmcnt(9)\n"
"v_add_f32 v48, v48, v49\n"
"v_add_f32 v50, v50, v51\n"
"v_mul_f32 v35, v88, v35\n"
"v_mfma_f32_16x16x32_bf16 v[40:43], v[212:215], v[76:79], v[40:43]\n"
"v_add_f32 v48, v48, v50\n"
"v_fmac_f32 v32, v89, v20\n"
"s_waitcnt lgkmcnt(5)\n"
"v_mfma_f32_16x16x32_bf16 v[36:39], v[216:219], v[56:59], v[36:39]\n"
"v_add_f32 v44, v44, v46\n"
"v_mul_f32 v53, 0x3acccccd, v48\n"
"v_fmac_f32 v33, v89, v21\n"
"v_mfma_f32_16x16x32_bf16 v[40:43], v[220:223], v[56:59], v[40:43]\n"
"v_fma_f32 v44, -v48, v53, v44\n"
"v_fmamk_f32 v44, v44, 0x3acccccd, v27\n"
"v_fmac_f32 v34, v89, v22\n"
"v_mfma_f32_16x16x32_bf16 v[36:39], v[232:235], v[60:63], v[36:39]\n"
"v_cmp_u_f32 vcc, v44, v44\n"
"v_max_f32 v44, v44, v27\n"
"v_fmac_f32 v35, v89, v23\n"
"v_mfma_f32_16x16x32_bf16 v[40:43], v[236:239], v[60:63], v[40:43]\n"
"v_rsq_f32 v52, v44\n"
"s_waitcnt lgkmcnt(0)\n"
"v_mfma_f32_16x16x32_bf16 v[36:39], v[240:243], v[64:67], v[36:39]\n"
"v_mul_f32 v53, -v53, v52\n"
"v_cvt_pk_bf16_f32 v32, v32, v33\n"
"v_cvt_pk_bf16_f32 v33, v34, v35\n"
"v_mfma_f32_16x16x32_bf16 v[40:43], v[244:247], v[64:67], v[40:43]\n"
"s_cbranch_vccnz .Lsl_148\n"
".Lbk_149:\n"
"ds_write_b64 v8, v[32:33] offset:15872\n"
"v_mfma_f32_16x16x32_bf16 v[36:39], v[248:251], v[68:71], v[36:39]\n"
"v_mfma_f32_16x16x32_bf16 v[40:43], v[252:255], v[68:71], v[40:43]\n"
"v_cmp_u_f32 vcc, v92, v92\n"
"s_nop 3\n"
"v_mul_f32 v36, v52, v36\n"
"v_mul_f32 v37, v52, v37\n"
"s_cbranch_vccnz .Lrd_118\n"
".Lgo_117:\n"
"ds_write_b32 v13, v13 offset:304\n"
"v_mul_f32 v38, v52, v38\n"
"v_mul_f32 v39, v52, v39\n"
"v_fmac_f32 v36, v53, v16\n"
"v_fmac_f32 v37, v53, v17\n"
"v_fmac_f32 v38, v53, v18\n"
"v_fmac_f32 v39, v53, v19\n"
"v_cvt_pk_bf16_f32 v36, v36, v37\n"
"v_cvt_pk_bf16_f32 v37, v38, v39\n"
"ds_write_b64 v7, v[36:37] offset:0\n"
"v_mul_f32 v40, v52, v40\n"
"v_mul_f32 v41, v52, v41\n"
"v_mul_f32 v42, v52, v42\n"
"v_mul_f32 v43, v52, v43\n"
"v_fmac_f32 v40, v53, v20\n"
"v_fmac_f32 v41, v53, v21\n"
"v_fmac_f32 v42, v53, v22\n"
"v_fmac_f32 v43, v53, v23\n"
"v_cvt_pk_bf16_f32 v40, v40, v41\n"
"v_cvt_pk_bf16_f32 v41, v42, v43\n"
"ds_write_b64 v8, v[40:41] offset:0\n"
".Ljn_153:\n"
"v_mov_b32 v46, 0\n"
"v_mov_b32 v47, 0\n"
"v_mov_b32 v50, 0\n"
"v_mov_b32 v51, 0\n"
"v_mov_b32 v72, s42\n"
"v_mov_b32 v73, s42\n"
"v_mov_b32 v74, s42\n"
"v_mov_b32 v75, s42\n"
"s_cmp_lg_u32 s3, 2\n"
"s_cbranch_scc1 .Lnw_196\n"
"s_mov_b32 s17, 0x100000\n"
".Ltp_197:\n"
"ds_read_b32 v90, v14 offset:304\n"
"s_sub_u32 s17, s17, 1\n"
"s_cbranch_scc1 .Lou_198\n"
"s_waitcnt lgkmcnt(0)\n"
"v_cmp_eq_u32 vcc, 0, v90\n"
"s_cbranch_vccnz .Ltp_197\n"
".Lou_198:\n"
"s_waitcnt lgkmcnt(0)\n"
".Lnw_196:\n"
"ds_read_b32 v90, v14 offset:304\n"
"s_mov_b64 exec, 0xffff\n"
"ds_read_b64 v[46:47], v24 offset:4000\n"
"ds_read_b64 v[50:51], v24 offset:4040\n"
"s_mov_b64 exec, -1\n"
"ds_read_b64 v[44:45], v25 offset:3968\n"
"ds_read_b64 v[56:57], v25 offset:80\n"
"ds_read_b64 v[58:59], v24 offset:112\n"
"ds_read_b64 v[64:65], v25 offset:4048\n"
"ds_read_b64 v[66:67], v24 offset:4080\n"
"ds_read_b64 v[48:49], v25 offset:4008\n"
"ds_read_b64 v[60:61], v25 offset:120\n"
"ds_read_b64 v[62:63], v24 offset:152\n"
"ds_read_b64 v[68:69], v25 offset:4088\n"
"ds_read_b64 v[70:71], v24 offset:4120\n"
"s_waitcnt lgkmcnt(12)\n"
"v_or_b32 v90, s33, v90\n"
"s_mov_b32 s17, 0x100000\n"
"v_cmp_eq_u32 vcc, 0, v90\n"
"s_cbranch_vccz .Lok_200\n"
".Ltp_199:\n"
"ds_read_b32 v90, v14 offset:304\n"
"s_mov_b64 exec, 0xffff\n"
"ds_read_b64 v[46:47], v24 offset:4000\n"
"ds_read_b64 v[50:51], v24 offset:4040\n"
"s_mov_b64 exec, -1\n"
"ds_read_b64 v[44:45], v25 offset:3968\n"
"ds_read_b64 v[56:57], v25 offset:80\n"
"ds_read_b64 v[58:59], v24 offset:112\n"
"ds_read_b64 v[64:65], v25 offset:4048\n"
"ds_read_b64 v[66:67], v24 offset:4080\n"
"ds_read_b64 v[48:49], v25 offset:4008\n"
"ds_read_b64 v[60:61], v25 offset:120\n"
"ds_read_b64 v[62:63], v24 offset:152\n"
"ds_read_b64 v[68:69], v25 offset:4088\n"
"ds_read_b64 v[70:71], v24 offset:4120\n"
"s_sub_u32 s17, s17, 1\n"
"s_cbranch_scc1 .Lok_200\n"
"s_waitcnt lgkmcnt(12)\n"
"v_cmp_eq_u32 vcc, 0, v90\n"
"s_cbranch_vccnz .Ltp_199\n"
"s_waitcnt lgkmcnt(0)\n"
".Lok_200:\n"
"ds_read_b64_tr_b16 v[28:29], v26 offset:0\n"
"ds_read_b64_tr_b16 v[30:31], v26 offset:3968\n"
"ds_read_b64_tr_b16 v[32:33], v26 offset:40\n"
"s_waitcnt lgkmcnt(10)\n"
"v_mfma_f32_16x16x32_bf16 v[56:59], v[56:59], v[44:47], v[72:75]\n"
"s_waitcnt lgkmcnt(8)\n"
"v_mfma_f32_16x16x32_bf16 v[64:67], v[64:67], v[44:47], 0\n"
"ds_read_b64_tr_b16 v[34:35], v26 offset:4008\n"
"ds_read_b64_tr_b16 v[36:37], v26 offset:32\n"
"ds_read_b64_tr_b16 v[38:39], v26 offset:4000\n"
"s_waitcnt lgkmcnt(8)\n"
"v_mfma_f32_16x16x32_bf16 v[60:63], v[60:63], v[48:51], v[72:75]\n"
"s_waitcnt lgkmcnt(6)\n"
"v_mfma_f32_16x16x32_bf16 v[68:71], v[68:71], v[48:51], 0\n"
"ds_read_b64_tr_b16 v[40:41], v26 offset:72\n"
"ds_read_b64_tr_b16 v[42:43], v26 offset:4040\n"
"v_mov_b32 v84, 0x3f803f80\n"
"v_mov_b32 v85, 0x3f803f80\n"
"v_mov_b32 v86, 0x3f803f80\n"
"v_mov_b32 v87, 0x3f803f80\n"
"v_cndmask_b32 v76, v56, v57, s[34:35]\n"
"v_cndmask_b32 v72, v64, v65, s[34:35]\n"
"v_cndmask_b32 v76, v76, v72, s[36:37]\n"
"v_cndmask_b32 v77, v58, v59, s[34:35]\n"
"v_cndmask_b32 v72, v66, v67, s[34:35]\n"
"v_cndmask_b32 v77, v77, v72, s[38:39]\n"
"v_max_f32 v52, v76, v77\n"
"v_max_f32 v54, v76, v77\n"
"v_cndmask_b32 v78, v60, v61, s[34:35]\n"
"v_cndmask_b32 v72, v68, v69, s[34:35]\n"
"v_cndmask_b32 v78, v78, v72, s[36:37]\n"
"v_cndmask_b32 v79, v62, v63, s[34:35]\n"
"v_cndmask_b32 v72, v70, v71, s[34:35]\n"
"v_cndmask_b32 v79, v79, v72, s[38:39]\n"
"v_max_f32 v53, v78, v79\n"
"v_max_f32 v55, v78, v79\n"
"v_permlane16_swap_b32 v52, v54\n"
"s_nop 0\n"
"v_permlane16_swap_b32 v53, v55\n"
"v_max_f32 v72, v52, v54\n"
"v_max_f32 v52, v52, v54\n"
"v_max_f32 v73, v53, v55\n"
"v_max_f32 v53, v53, v55\n"
"v_permlane32_swap_b32 v52, v72\n"
"s_nop 0\n"
"v_permlane32_swap_b32 v53, v73\n"
"v_max_f32 v52, v52, v72\n"
"v_max_f32 v53, v53, v73\n"
"v_mul_f32 v52, 0xbea52b5b, v52\n"
"v_mul_f32 v53, 0xbea52b5b, v53\n"
"v_fmamk_f32 v76, v76, 0x3ea52b5b, v52\n"
"v_fmamk_f32 v77, v77, 0x3ea52b5b, v52\n"
"v_fmamk_f32 v78, v78, 0x3ea52b5b, v53\n"
"v_fmamk_f32 v79, v79, 0x3ea52b5b, v53\n"
"v_exp_f32 v76, v76\n"
"v_exp_f32 v77, v77\n"
"v_exp_f32 v78, v78\n"
"v_exp_f32 v79, v79\n"
"v_cvt_pk_bf16_f32 v52, v76, v77\n"
"v_cvt_pk_bf16_f32 v53, v78, v79\n"
"v_perm_b32 v44, v52, v52, v80\n"
"v_perm_b32 v45, v52, v52, v81\n"
"v_perm_b32 v46, v52, v52, v82\n"
"v_perm_b32 v47, v52, v52, v83\n"
"v_perm_b32 v48, v53, v53, v80\n"
"v_perm_b32 v49, v53, v53, v81\n"
"v_perm_b32 v50, v53, v53, v82\n"
"v_perm_b32 v51, v53, v53, v83\n"
"v_mfma_f32_16x16x32_bf16 v[56:59], v[84:87], v[44:47], 0\n"
"s_nop 0\n"
"v_mfma_f32_16x16x32_bf16 v[60:63], v[84:87], v[48:51], 0\n"
"s_waitcnt lgkmcnt(6)\n"
"v_mfma_f32_16x16x32_bf16 v[28:31], v[28:31], v[44:47], 0\n"
"s_waitcnt lgkmcnt(4)\n"
"v_mfma_f32_16x16x32_bf16 v[32:35], v[32:35], v[48:51], 0\n"
"s_waitcnt lgkmcnt(2)\n"
"v_mfma_f32_16x16x32_bf16 v[36:39], v[36:39], v[44:47], 0\n"
"s_waitcnt lgkmcnt(0)\n"
"v_mfma_f32_16x16x32_bf16 v[40:43], v[40:43], v[48:51], 0\n"
"v_cndmask_b32 v88, 2.0, v1, s[40:41]\n"
"v_rcp_f32 v52, v56\n"
"v_rcp_f32 v53, v60\n"
"ds_read_b32 v90, v14 offset:320\n"
"ds_read_b64 v[56:57], v3 offset:40960\n"
"ds_read_b64 v[58:59], v3 offset:41216\n"
"s_add_u32 s31, s30, 0xa60000\n"
"v_mul_f32 v28, v52, v28\n"
"v_mul_f32 v29, v52, v29\n"
"v_mul_f32 v30, v52, v30\n"
"v_mul_f32 v31, v52, v31\n"
"v_mul_f32 v32, v53, v32\n"
"ds_read_b64 v[60:61], v3 offset:41984\n"
"ds_read_b64 v[62:63], v3 offset:42240\n"
"v_mul_f32 v33, v53, v33\n"
"v_mul_f32 v34, v53, v34\n"
"v_mul_f32 v35, v53, v35\n"
"v_cvt_pk_bf16_f32 v28, v28, v29\n"
"v_cvt_pk_bf16_f32 v29, v30, v31\n"
"v_cvt_pk_bf16_f32 v30, v32, v33\n"
"ds_read_b64 v[64:65], v3 offset:43008\n"
"ds_read_b64 v[66:67], v3 offset:43264\n"
"v_cvt_pk_bf16_f32 v31, v34, v35\n"
"v_min_u32 v28, -2, v28\n"
"v_min_u32 v29, -2, v29\n"
"v_min_u32 v30, -2, v30\n"
"v_min_u32 v31, -2, v31\n"
"buffer_store_dwordx4 v[28:31], v1, s[4:7], s31 offen offset:0 sc1\n"
"ds_read_b64 v[68:69], v3 offset:44032\n"
"ds_read_b64 v[70:71], v3 offset:44288\n"
"v_mul_f32 v36, v52, v36\n"
"v_mul_f32 v37, v52, v37\n"
"v_mul_f32 v38, v52, v38\n"
"v_mul_f32 v39, v52, v39\n"
"v_mul_f32 v40, v53, v40\n"
"v_mul_f32 v41, v53, v41\n"
"ds_read_b64 v[72:73], v3 offset:45056\n"
"ds_read_b64 v[74:75], v3 offset:45312\n"
"v_mul_f32 v42, v53, v42\n"
"v_mul_f32 v43, v53, v43\n"
"v_cvt_pk_bf16_f32 v36, v36, v37\n"
"v_cvt_pk_bf16_f32 v37, v38, v39\n"
"v_cvt_pk_bf16_f32 v38, v40, v41\n"
"v_cvt_pk_bf16_f32 v39, v42, v43\n"
"v_min_u32 v36, -2, v36\n"
"v_min_u32 v37, -2, v37\n"
"v_min_u32 v38, -2, v38\n"
"v_min_u32 v39, -2, v39\n"
"buffer_store_dwordx4 v[36:39], v88, s[4:7], s31 offen offset:1024 sc1\n"
"ds_write_b32 v13, v13 offset:368\n"
"s_cmp_lg_u32 s54, 0\n"
"s_cbranch_scc1 .Lfx_204\n"
".Lbk_205:\n"
"s_mov_b32 s54, 0\n"
".Lag_201:\n"
"v_mov_b32 v92, 0\n"
"s_waitcnt lgkmcnt(11)\n"
"v_cmp_eq_u32 vcc, 0, v90\n"
"s_cbranch_vccnz .Lsl_238\n"
".Lbk_239:\n"
"s_waitcnt lgkmcnt(7)\n"
"v_mfma_f32_16x16x32_bf16 v[28:31], v[96:99], v[56:59], 0\n"
"ds_read_b64 v[76:77], v3 offset:46080\n"
"v_mfma_f32_16x16x32_bf16 v[32:35], v[100:103], v[56:59], 0\n"
"ds_read_b64 v[78:79], v3 offset:46336\n"
"v_mfma_f32_16x16x32_bf16 v[52:55], v[56:59], v[56:59], 0\n"
"v_mfma_f32_16x16x32_bf16 v[28:31], v[104:107], v[60:63], v[28:31]\n"
"ds_read_b64 v[56:57], v3 offset:47104\n"
"v_mfma_f32_16x16x32_bf16 v[32:35], v[108:111], v[60:63], v[32:35]\n"
"ds_read_b64 v[58:59], v3 offset:47360\n"
"s_waitcnt lgkmcnt(7)\n"
"v_mfma_f32_16x16x32_bf16 v[28:31], v[112:115], v[64:67], v[28:31]\n"
"ds_read_b64 v[60:61], v3 offset:48128\n"
"v_mfma_f32_16x16x32_bf16 v[32:35], v[116:119], v[64:67], v[32:35]\n"
"ds_read_b64 v[62:63], v3 offset:48384\n"
"v_mfma_f32_16x16x32_bf16 v[28:31], v[120:123], v[68:71], v[28:31]\n"
"ds_read_b64 v[64:65], v3 offset:49152\n"
"v_mfma_f32_16x16x32_bf16 v[32:35], v[124:127], v[68:71], v[32:35]\n"
"ds_read_b64 v[66:67], v3 offset:49408\n"
"s_waitcnt lgkmcnt(6)\n"
"v_mfma_f32_16x16x32_bf16 v[28:31], v[128:131], v[72:75], v[28:31]\n"
"ds_read_b64 v[68:69], v3 offset:50176\n"
"v_mfma_f32_16x16x32_bf16 v[32:35], v[132:135], v[72:75], v[32:35]\n"
"ds_read_b64 v[70:71], v3 offset:50432\n"
"v_mfma_f32_16x16x32_bf16 v[52:55], v[72:75], v[72:75], v[52:55]\n"
"v_mfma_f32_16x16x32_bf16 v[28:31], v[136:139], v[76:79], v[28:31]\n"
"ds_read_b64 v[72:73], v3 offset:51200\n"
"v_mfma_f32_16x16x32_bf16 v[32:35], v[140:143], v[76:79], v[32:35]\n"
"ds_read_b64 v[74:75], v3 offset:51456\n"
"s_waitcnt lgkmcnt(6)\n"
"v_mfma_f32_16x16x32_bf16 v[28:31], v[144:147], v[56:59], v[28:31]\n"
"ds_read_b64 v[76:77], v3 offset:52224\n"
"v_mfma_f32_16x16x32_bf16 v[32:35], v[148:151], v[56:59], v[32:35]\n"
"ds_read_b64 v[78:79], v3 offset:52480\n"
"v_mfma_f32_16x16x32_bf16 v[28:31], v[152:155], v[60:63], v[28:31]\n"
"ds_read_b64 v[56:57], v3 offset:53248\n"
"v_mfma_f32_16x16x32_bf16 v[32:35], v[156:159], v[60:63], v[32:35]\n"
"ds_read_b64 v[58:59], v3 offset:53504\n"
"s_waitcnt lgkmcnt(6)\n"
"v_mfma_f32_16x16x32_bf16 v[28:31], v[160:163], v[64:67], v[28:31]\n"
"ds_read_b64 v[60:61], v3 offset:54272\n"
"v_mfma_f32_16x16x32_bf16 v[32:35], v[164:167], v[64:67], v[32:35]\n"
"ds_read_b64 v[62:63], v3 offset:54528\n"
"v_mfma_f32_16x16x32_bf16 v[52:55], v[64:67], v[64:67], v[52:55]\n"
"v_mfma_f32_16x16x32_bf16 v[28:31], v[168:171], v[68:71], v[28:31]\n"
"ds_read_b64 v[64:65], v3 offset:55296\n"
"v_mfma_f32_16x16x32_bf16 v[32:35], v[172:175], v[68:71], v[32:35]\n"
"ds_read_b64 v[66:67], v3 offset:55552\n"
"s_waitcnt lgkmcnt(6)\n"
"v_mfma_f32_16x16x32_bf16 v[28:31], v[176:179], v[72:75], v[28:31]\n"
"ds_read_b64 v[68:69], v3 offset:56320\n"
"v_mfma_f32_16x16x32_bf16 v[32:35], v[180:183], v[72:75], v[32:35]\n"
"ds_read_b64 v[70:71], v3 offset:56576\n"
"v_mfma_f32_16x16x32_bf16 v[28:31], v[184:187], v[76:79], v[28:31]\n"
"ds_read_b64 v[72:73], v3 offset:57344\n"
"v_mfma_f32_16x16x32_bf16 v[32:35], v[188:191], v[76:79], v[32:35]\n"
"ds_read_b64 v[74:75], v3 offset:57600\n"
"s_waitcnt lgkmcnt(6)\n"
"v_mfma_f32_16x16x32_bf16 v[28:31], v[192:195], v[56:59], v[28:31]\n"
"ds_read_b64 v[76:77], v4 offset:40960\n"
"v_mfma_f32_16x16x32_bf16 v[32:35], v[196:199], v[56:59], v[32:35]\n"
"ds_read_b64 v[78:79], v4 offset:41216\n"
"v_mfma_f32_16x16x32_bf16 v[52:55], v[56:59], v[56:59], v[52:55]\n"
"v_mfma_f32_16x16x32_bf16 v[28:31], v[200:203], v[60:63], v[28:31]\n"
"ds_read_b64 v[56:57], v5 offset:40960\n"
"v_mfma_f32_16x16x32_bf16 v[32:35], v[204:207], v[60:63], v[32:35]\n"
"ds_read_b64 v[58:59], v5 offset:41216\n"
"s_waitcnt lgkmcnt(6)\n"
"v_mfma_f32_16x16x32_bf16 v[28:31], v[208:211], v[64:67], v[28:31]\n"
"ds_read_b64 v[60:61], v6 offset:40960\n"
"v_mfma_f32_16x16x32_bf16 v[32:35], v[212:215], v[64:67], v[32:35]\n"
"ds_read_b64 v[62:63], v6 offset:41216\n"
"v_mfma_f32_16x16x32_bf16 v[28:31], v[216:219], v[68:71], v[28:31]\n"
"v_add_u32 v3, 0x5000, v3\n"
"ds_read_b64 v[64:65], v3 offset:40960\n"
"v_mfma_f32_16x16x32_bf16 v[32:35], v[220:223], v[68:71], v[32:35]\n"
"ds_read_b64 v[66:67], v3 offset:41216\n"
"s_waitcnt lgkmcnt(6)\n"
"v_mfma_f32_16x16x32_bf16 v[28:31], v[224:227], v[72:75], v[28:31]\n"
"ds_read_b64 v[68:69], v3 offset:41984\n"
"v_mfma_f32_16x16x32_bf16 v[32:35], v[228:231], v[72:75], v[32:35]\n"
"ds_read_b64 v[70:71], v3 offset:42240\n"
"v_mfma_f32_16x16x32_bf16 v[52:55], v[72:75], v[72:75], v[52:55]\n"
"v_mfma_f32_16x16x32_bf16 v[28:31], v[232:235], v[76:79], v[28:31]\n"
"ds_read_b64 v[72:73], v3 offset:43008\n"
"v_mfma_f32_16x16x32_bf16 v[32:35], v[236:239], v[76:79], v[32:35]\n"
"ds_read_b64 v[74:75], v3 offset:43264\n"
"s_waitcnt lgkmcnt(6)\n"
"v_mfma_f32_16x16x32_bf16 v[28:31], v[240:243], v[56:59], v[28:31]\n"
"ds_read_b64 v[76:77], v3 offset:44032\n"
"v_cndmask_b32 v94, v52, v53, s[34:35]\n"
"v_mfma_f32_16x16x32_bf16 v[32:35], v[244:247], v[56:59], v[32:35]\n"
"ds_read_b64 v[78:79], v3 offset:44288\n"
"v_cndmask_b32 v54, v54, v55, s[34:35]\n"
"v_mfma_f32_16x16x32_bf16 v[28:31], v[248:251], v[60:63], v[28:31]\n"
"ds_read_b64 v[56:57], v3 offset:45056\n"
"v_cndmask_b32 v94, v94, v54, s[20:21]\n"
"v_mfma_f32_16x16x32_bf16 v[32:35], v[252:255], v[60:63], v[32:35]\n"
"ds_read_b64 v[58:59], v3 offset:45312\n"
"s_waitcnt lgkmcnt(6)\n"
"v_mfma_f32_16x16x32_bf16 v[36:39], v[96:99], v[64:67], 0\n"
"ds_read_b64 v[60:61], v3 offset:46080\n"
"v_mfma_f32_16x16x32_bf16 v[40:43], v[100:103], v[64:67], 0\n"
"ds_read_b64 v[62:63], v3 offset:46336\n"
"v_cndmask_b32 v94, v28, v94, s[22:23]\n"
"v_mfma_f32_16x16x32_bf16 v[52:55], v[64:67], v[64:67], 0\n"
"ds_write_b32 v9, v94 offset:3712\n"
"v_mfma_f32_16x16x32_bf16 v[36:39], v[104:107], v[68:71], v[36:39]\n"
"ds_read_b64 v[64:65], v3 offset:47104\n"
"v_mfma_f32_16x16x32_bf16 v[40:43], v[108:111], v[68:71], v[40:43]\n"
"ds_read_b64 v[66:67], v3 offset:47360\n"
"s_waitcnt lgkmcnt(7)\n"
"v_mfma_f32_16x16x32_bf16 v[36:39], v[112:115], v[72:75], v[36:39]\n"
"ds_read_b64 v[68:69], v3 offset:48128\n"
"v_mfma_f32_16x16x32_bf16 v[40:43], v[116:119], v[72:75], v[40:43]\n"
"ds_read_b64 v[70:71], v3 offset:48384\n"
"v_mfma_f32_16x16x32_bf16 v[36:39], v[120:123], v[76:79], v[36:39]\n"
"ds_read_b64 v[72:73], v3 offset:49152\n"
"v_mfma_f32_16x16x32_bf16 v[40:43], v[124:127], v[76:79], v[40:43]\n"
"ds_read_b64 v[74:75], v3 offset:49408\n"
"s_waitcnt lgkmcnt(7)\n"
"v_mfma_f32_16x16x32_bf16 v[36:39], v[128:131], v[56:59], v[36:39]\n"
"ds_read_b64 v[76:77], v3 offset:50176\n"
"v_mfma_f32_16x16x32_bf16 v[40:43], v[132:135], v[56:59], v[40:43]\n"
"ds_read_b64 v[78:79], v3 offset:50432\n"
"v_mfma_f32_16x16x32_bf16 v[52:55], v[56:59], v[56:59], v[52:55]\n"
"v_mfma_f32_16x16x32_bf16 v[36:39], v[136:139], v[60:63], v[36:39]\n"
"ds_read_b64 v[56:57], v3 offset:51200\n"
"v_mfma_f32_16x16x32_bf16 v[40:43], v[140:143], v[60:63], v[40:43]\n"
"ds_read_b64 v[58:59], v3 offset:51456\n"
"s_waitcnt lgkmcnt(6)\n"
"v_mfma_f32_16x16x32_bf16 v[36:39], v[144:147], v[64:67], v[36:39]\n"
"ds_read_b64 v[60:61], v3 offset:52224\n"
"ds_read_b128 v[84:87], v11 offset:3712\n"
"ds_read_b32 v91, v12 offset:3712\n"
"v_mfma_f32_16x16x32_bf16 v[40:43], v[148:151], v[64:67], v[40:43]\n"
"ds_read_b64 v[62:63], v3 offset:52480\n"
"v_mfma_f32_16x16x32_bf16 v[36:39], v[152:155], v[68:71], v[36:39]\n"
"ds_read_b64 v[64:65], v3 offset:53248\n"
"v_mfma_f32_16x16x32_bf16 v[40:43], v[156:159], v[68:71], v[40:43]\n"
"ds_read_b64 v[66:67], v3 offset:53504\n"
"s_waitcnt lgkmcnt(8)\n"
"v_mfma_f32_16x16x32_bf16 v[36:39], v[160:163], v[72:75], v[36:39]\n"
"ds_read_b64 v[68:69], v3 offset:54272\n"
"v_mfma_f32_16x16x32_bf16 v[40:43], v[164:167], v[72:75], v[40:43]\n"
"ds_read_b64 v[70:71], v3 offset:54528\n"
"v_mfma_f32_16x16x32_bf16 v[52:55], v[72:75], v[72:75], v[52:55]\n"
"v_mfma_f32_16x16x32_bf16 v[36:39], v[168:171], v[76:79], v[36:39]\n"
"ds_read_b64 v[72:73], v3 offset:55296\n"
"s_waitcnt lgkmcnt(7)\n"
"v_add_f32 v84, v84, v85\n"
"v_add_f32 v86, v86, v87\n"
"v_mfma_f32_16x16x32_bf16 v[40:43], v[172:175], v[76:79], v[40:43]\n"
"ds_read_b64 v[74:75], v3 offset:55552\n"
"v_add_f32 v84, v84, v86\n"
"s_waitcnt lgkmcnt(6)\n"
"v_mfma_f32_16x16x32_bf16 v[36:39], v[176:179], v[56:59], v[36:39]\n"
"ds_read_b64 v[76:77], v3 offset:56320\n"
"v_mul_f32 v89, 0x3acccccd, v91\n"
"v_fma_f32 v84, -v91, v89, v84\n"
"v_mfma_f32_16x16x32_bf16 v[40:43], v[180:183], v[56:59], v[40:43]\n"
"ds_read_b64 v[78:79], v3 offset:56576\n"
"v_fmamk_f32 v84, v84, 0x3acccccd, v27\n"
"v_cmp_u_f32 vcc, v84, v84\n"
"v_mfma_f32_16x16x32_bf16 v[36:39], v[184:187], v[60:63], v[36:39]\n"
"ds_read_b64 v[56:57], v3 offset:57344\n"
"v_max_f32 v84, v84, v27\n"
"v_mfma_f32_16x16x32_bf16 v[40:43], v[188:191], v[60:63], v[40:43]\n"
"ds_read_b64 v[58:59], v3 offset:57600\n"
"s_waitcnt lgkmcnt(6)\n"
"v_mfma_f32_16x16x32_bf16 v[36:39], v[192:195], v[64:67], v[36:39]\n"
"ds_read_b64 v[60:61], v4 offset:61440\n"
"v_rsq_f32 v88, v84\n"
"v_mfma_f32_16x16x32_bf16 v[40:43], v[196:199], v[64:67], v[40:43]\n"
"ds_read_b64 v[62:63], v4 offset:61696\n"
"ds_read_b32 v90, v14 offset:400\n"
"v_mfma_f32_16x16x32_bf16 v[52:55], v[64:67], v[64:67], v[52:55]\n"
"v_mul_f32 v89, -v89, v88\n"
"v_mfma_f32_16x16x32_bf16 v[36:39], v[200:203], v[68:71], v[36:39]\n"
"ds_read_b64 v[64:65], v5 offset:61440\n"
"s_cbranch_vccnz .Lsl_216\n"
".Lbk_217:\n"
"v_mfma_f32_16x16x32_bf16 v[40:43], v[204:207], v[68:71], v[40:43]\n"
"ds_read_b64 v[66:67], v5 offset:61696\n"
"s_add_u32 m0, s29, 0xa000\n"
"s_waitcnt lgkmcnt(7)\n"
"v_mfma_f32_16x16x32_bf16 v[36:39], v[208:211], v[72:75], v[36:39]\n"
"ds_read_b64 v[68:69], v6 offset:61440\n"
"s_waitcnt lgkmcnt(3)\n"
"v_cmp_eq_u32 vcc, 0, v90\n"
"v_mul_f32 v28, v88, v28\n"
"v_mfma_f32_16x16x32_bf16 v[40:43], v[212:215], v[72:75], v[40:43]\n"
"ds_read_b64 v[70:71], v6 offset:61696\n"
"s_add_u32 s31, s44, 0x13b000\n"
"v_mul_f32 v29, v88, v29\n"
"v_mfma_f32_16x16x32_bf16 v[36:39], v[216:219], v[76:79], v[36:39]\n"
"s_cbranch_vccnz .Lsl_208\n"
".Lbk_209:\n"
"v_add_u32 v3, 0xffffb000, v3\n"
"ds_read_b64 v[72:73], v3 offset:0\n"
"buffer_load_dwordx4 v1, s[4:7], s31 offen offset:0 sc1 lds\n"
"v_mfma_f32_16x16x32_bf16 v[40:43], v[220:223], v[76:79], v[40:43]\n"
"ds_read_b64 v[74:75], v3 offset:256\n"
"v_mfma_f32_16x16x32_bf16 v[36:39], v[224:227], v[56:59], v[36:39]\n"
"ds_read_b64 v[76:77], v3 offset:1024\n"
"v_mul_f32 v30, v88, v30\n"
"v_mfma_f32_16x16x32_bf16 v[40:43], v[228:231], v[56:59], v[40:43]\n"
"ds_read_b64 v[78:79], v3 offset:1280\n"
"v_mul_f32 v31, v88, v31\n"
"v_mfma_f32_16x16x32_bf16 v[52:55], v[56:59], v[56:59], v[52:55]\n"
"buffer_load_dwordx4 v1, s[4:7], s31 offen offset:1024 sc1 lds\n"
"v_fmac_f32 v28, v89, v16\n"
"v_mfma_f32_16x16x32_bf16 v[36:39], v[232:235], v[60:63], v[36:39]\n"
"ds_read_b64 v[56:57], v3 offset:2048\n"
"v_fmac_f32 v29, v89, v17\n"
"v_mfma_f32_16x16x32_bf16 v[40:43], v[236:239], v[60:63], v[40:43]\n"
"ds_read_b64 v[58:59], v3 offset:2304\n"
"s_waitcnt lgkmcnt(6)\n"
"v_mfma_f32_16x16x32_bf16 v[36:39], v[240:243], v[64:67], v[36:39]\n"
"ds_read_b64 v[60:61], v3 offset:3072\n"
"v_cndmask_b32 v94, v52, v53, s[34:35]\n"
"v_mfma_f32_16x16x32_bf16 v[40:43], v[244:247], v[64:67], v[40:43]\n"
"ds_read_b64 v[62:63], v3 offset:3328\n"
"v_cndmask_b32 v54, v54, v55, s[34:35]\n"
"buffer_load_dwordx4 v1, s[4:7], s31 offen offset:2048 sc1 lds\n"
"v_mfma_f32_16x16x32_bf16 v[36:39], v[248:251], v[68:71], v[36:39]\n"
"ds_read_b64 v[64:65], v3 offset:4096\n"
"v_cndmask_b32 v94, v94, v54, s[20:21]\n"
"v_mfma_f32_16x16x32_bf16 v[40:43], v[252:255], v[68:71], v[40:43]\n"
"ds_read_b64 v[66:67], v3 offset:4352\n"
"s_waitcnt lgkmcnt(6)\n"
"v_mfma_f32_16x16x32_bf16 v[44:47], v[96:99], v[72:75], 0\n"
"ds_read_b64 v[68:69], v3 offset:5120\n"
"v_fmac_f32 v30, v89, v18\n"
"v_mfma_f32_16x16x32_bf16 v[48:51], v[100:103], v[72:75], 0\n"
"ds_read_b64 v[70:71], v3 offset:5376\n"
"v_cndmask_b32 v94, v36, v94, s[22:23]\n"
"buffer_load_dwordx4 v1, s[4:7], s31 offen offset:3072 sc1 lds\n"
"v_mfma_f32_16x16x32_bf16 v[52:55], v[72:75], v[72:75], 0\n"
"ds_write_b32 v9, v94 offset:4032\n"
"s_add_u32 m0, m0, 1024\n"
"v_mfma_f32_16x16x32_bf16 v[44:47], v[104:107], v[76:79], v[44:47]\n"
"ds_read_b64 v[72:73], v3 offset:6144\n"
"s_add_u32 s31, s31, 1024\n"
"v_mfma_f32_16x16x32_bf16 v[48:51], v[108:111], v[76:79], v[48:51]\n"
"ds_read_b64 v[74:75], v3 offset:6400\n"
"s_waitcnt lgkmcnt(7)\n"
"v_mfma_f32_16x16x32_bf16 v[44:47], v[112:115], v[56:59], v[44:47]\n"
"ds_read_b64 v[76:77], v3 offset:7168\n"
"buffer_load_dwordx4 v1, s[4:7], s31 offen offset:3072 sc1 lds\n"
"v_mfma_f32_16x16x32_bf16 v[48:51], v[116:119], v[56:59], v[48:51]\n"
"ds_read_b64 v[78:79], v3 offset:7424\n"
"v_fmac_f32 v31, v89, v19\n"
"v_mfma_f32_16x16x32_bf16 v[44:47], v[120:123], v[60:63], v[44:47]\n"
"ds_read_b64 v[56:57], v3 offset:8192\n"
"v_cvt_pk_bf16_f32 v28, v28, v29\n"
"v_mfma_f32_16x16x32_bf16 v[48:51], v[124:127], v[60:63], v[48:51]\n"
"ds_read_b64 v[58:59], v3 offset:8448\n"
"s_waitcnt lgkmcnt(7)\n"
"v_mfma_f32_16x16x32_bf16 v[44:47], v[128:131], v[64:67], v[44:47]\n"
"ds_read_b64 v[60:61], v3 offset:9216\n"
"v_cvt_pk_bf16_f32 v29, v30, v31\n"
"v_mfma_f32_16x16x32_bf16 v[48:51], v[132:135], v[64:67], v[48:51]\n"
"ds_read_b64 v[62:63], v3 offset:9472\n"
"v_mul_f32 v32, v88, v32\n"
"v_mfma_f32_16x16x32_bf16 v[52:55], v[64:67], v[64:67], v[52:55]\n"
"v_mul_f32 v33, v88, v33\n"
"v_mul_f32 v34, v88, v34\n"
"v_mfma_f32_16x16x32_bf16 v[44:47], v[136:139], v[68:71], v[44:47]\n"
"ds_read_b64 v[64:65], v3 offset:10240\n"
"v_mul_f32 v35, v88, v35\n"
"v_mfma_f32_16x16x32_bf16 v[48:51], v[140:143], v[68:71], v[48:51]\n"
"ds_read_b64 v[66:67], v3 offset:10496\n"
"s_waitcnt lgkmcnt(6)\n"
"v_mfma_f32_16x16x32_bf16 v[44:47], v[144:147], v[72:75], v[44:47]\n"
"ds_read_b64 v[68:69], v3 offset:11264\n"
"ds_read_b128 v[84:87], v11 offset:4032\n"
"ds_read_b32 v91, v12 offset:4032\n"
"v_mfma_f32_16x16x32_bf16 v[48:51], v[148:151], v[72:75], v[48:51]\n"
"ds_read_b64 v[70:71], v3 offset:11520\n"
"v_fmac_f32 v32, v89, v20\n"
"v_mfma_f32_16x16x32_bf16 v[44:47], v[152:155], v[76:79], v[44:47]\n"
"ds_read_b64 v[72:73], v3 offset:12288\n"
"v_fmac_f32 v33, v89, v21\n"
"v_mfma_f32_16x16x32_bf16 v[48:51], v[156:159], v[76:79], v[48:51]\n"
"ds_read_b64 v[74:75], v3 offset:12544\n"
"s_waitcnt lgkmcnt(8)\n"
"v_mfma_f32_16x16x32_bf16 v[44:47], v[160:163], v[56:59], v[44:47]\n"
"ds_read_b64 v[76:77], v3 offset:13312\n"
"v_fmac_f32 v34, v89, v22\n"
"v_mfma_f32_16x16x32_bf16 v[48:51], v[164:167], v[56:59], v[48:51]\n"
"ds_read_b64 v[78:79], v3 offset:13568\n"
"v_fmac_f32 v35, v89, v23\n"
"v_mfma_f32_16x16x32_bf16 v[52:55], v[56:59], v[56:59], v[52:55]\n"
"v_cvt_pk_bf16_f32 v32, v32, v33\n"
"v_cvt_pk_bf16_f32 v33, v34, v35\n"
"v_mfma_f32_16x16x32_bf16 v[44:47], v[168:171], v[60:63], v[44:47]\n"
"ds_read_b64 v[56:57], v3 offset:14336\n"
"s_waitcnt lgkmcnt(7)\n"
"v_add_f32 v84, v84, v85\n"
"v_add_f32 v86, v86, v87\n"
"v_mfma_f32_16x16x32_bf16 v[48:51], v[172:175], v[60:63], v[48:51]\n"
"ds_read_b64 v[58:59], v3 offset:14592\n"
"v_add_f32 v84, v84, v86\n"
"s_waitcnt lgkmcnt(6)\n"
"v_mfma_f32_16x16x32_bf16 v[44:47], v[176:179], v[64:67], v[44:47]\n"
"ds_read_b64 v[60:61], v3 offset:15360\n"
"v_mul_f32 v89, 0x3acccccd, v91\n"
"v_fma_f32 v84, -v91, v89, v84\n"
"v_mfma_f32_16x16x32_bf16 v[48:51], v[180:183], v[64:67], v[48:51]\n"
"ds_read_b64 v[62:63], v3 offset:15616\n"
"v_fmamk_f32 v84, v84, 0x3acccccd, v27\n"
"v_cmp_u_f32 vcc, v84, v84\n"
"v_mfma_f32_16x16x32_bf16 v[44:47], v[184:187], v[68:71], v[44:47]\n"
"ds_read_b64 v[64:65], v3 offset:16384\n"
"v_max_f32 v84, v84, v27\n"
"v_mfma_f32_16x16x32_bf16 v[48:51], v[188:191], v[68:71], v[48:51]\n"
"ds_read_b64 v[66:67], v3 offset:16640\n"
"s_waitcnt lgkmcnt(6)\n"
"v_mfma_f32_16x16x32_bf16 v[44:47], v[192:195], v[72:75], v[44:47]\n"
"ds_read_b64 v[68:69], v4 offset:0\n"
"v_rsq_f32 v88, v84\n"
"v_mfma_f32_16x16x32_bf16 v[48:51], v[196:199], v[72:75], v[48:51]\n"
"ds_read_b64 v[70:71], v4 offset:256\n"
"ds_read_b32 v90, v14 offset:416\n"
"v_mfma_f32_16x16x32_bf16 v[52:55], v[72:75], v[72:75], v[52:55]\n"
"v_mul_f32 v89, -v89, v88\n"
"v_mfma_f32_16x16x32_bf16 v[44:47], v[200:203], v[76:79], v[44:47]\n"
"ds_read_b64 v[72:73], v5 offset:0\n"
"s_cbranch_vccnz .Lsl_220\n"
".Lbk_221:\n"
"v_mfma_f32_16x16x32_bf16 v[48:51], v[204:207], v[76:79], v[48:51]\n"
"ds_read_b64 v[74:75], v5 offset:256\n"
"ds_write_b64 v7, v[28:29] offset:44928\n"
"s_waitcnt lgkmcnt(8)\n"
"v_mfma_f32_16x16x32_bf16 v[44:47], v[208:211], v[56:59], v[44:47]\n"
"ds_read_b64 v[76:77], v6 offset:0\n"
"s_waitcnt lgkmcnt(4)\n"
"v_cmp_eq_u32 vcc, 0, v90\n"
"v_mul_f32 v36, v88, v36\n"
"v_mfma_f32_16x16x32_bf16 v[48:51], v[212:215], v[56:59], v[48:51]\n"
"ds_read_b64 v[78:79], v6 offset:256\n"
"v_mul_f32 v37, v88, v37\n"
"v_mfma_f32_16x16x32_bf16 v[44:47], v[216:219], v[60:63], v[44:47]\n"
"s_cbranch_vccnz .Lsl_212\n"
".Lbk_213:\n"
"ds_read_b64 v[56:57], v3 offset:20480\n"
"v_mfma_f32_16x16x32_bf16 v[48:51], v[220:223], v[60:63], v[48:51]\n"
"ds_read_b64 v[58:59], v3 offset:20736\n"
"v_mfma_f32_16x16x32_bf16 v[44:47], v[224:227], v[64:67], v[44:47]\n"
"ds_read_b64 v[60:61], v3 offset:21504\n"
"v_mul_f32 v38, v88, v38\n"
"v_mfma_f32_16x16x32_bf16 v[48:51], v[228:231], v[64:67], v[48:51]\n"
"ds_read_b64 v[62:63], v3 offset:21760\n"
"v_mov_b32 v84, 0x3f803f80\n"
"v_mfma_f32_16x16x32_bf16 v[52:55], v[64:67], v[64:67], v[52:55]\n"
"v_mov_b32 v85, 0x3f803f80\n"
"ds_write_b64 v8, v[32:33] offset:44928\n"
"v_mfma_f32_16x16x32_bf16 v[44:47], v[232:235], v[68:71], v[44:47]\n"
"ds_read_b64 v[64:65], v3 offset:22528\n"
"v_mul_f32 v39, v88, v39\n"
"v_mfma_f32_16x16x32_bf16 v[48:51], v[236:239], v[68:71], v[48:51]\n"
"ds_read_b64 v[66:67], v3 offset:22784\n"
"s_waitcnt lgkmcnt(7)\n"
"v_mfma_f32_16x16x32_bf16 v[44:47], v[240:243], v[72:75], v[44:47]\n"
"ds_read_b64 v[68:69], v3 offset:23552\n"
"v_cndmask_b32 v94, v52, v53, s[34:35]\n"
"v_mfma_f32_16x16x32_bf16 v[48:51], v[244:247], v[72:75], v[48:51]\n"
"ds_read_b64 v[70:71], v3 offset:23808\n"
"v_cndmask_b32 v54, v54, v55, s[34:35]\n"
"v_mfma_f32_16x16x32_bf16 v[44:47], v[248:251], v[76:79], v[44:47]\n"
"ds_read_b64 v[72:73], v3 offset:24576\n"
"v_cndmask_b32 v94, v94, v54, s[20:21]\n"
"v_mfma_f32_16x16x32_bf16 v[48:51], v[252:255], v[76:79], v[48:51]\n"
"ds_read_b64 v[74:75], v3 offset:24832\n"
"s_waitcnt lgkmcnt(7)\n"
"v_mfma_f32_16x16x32_bf16 v[28:31], v[96:99], v[56:59], 0\n"
"ds_read_b64 v[76:77], v3 offset:25600\n"
"v_fmac_f32 v36, v89, v16\n"
"v_mfma_f32_16x16x32_bf16 v[32:35], v[100:103], v[56:59], 0\n"
"ds_read_b64 v[78:79], v3 offset:25856\n"
"v_cndmask_b32 v94, v44, v94, s[22:23]\n"
"s_waitcnt vmcnt(0)\n"
"ds_write_b64 v93, v[84:85] offset:40960\n"
"v_mfma_f32_16x16x32_bf16 v[52:55], v[56:59], v[56:59], 0\n"
"ds_write_b32 v9, v94 offset:4352\n"
"v_mfma_f32_16x16x32_bf16 v[28:31], v[104:107], v[60:63], v[28:31]\n"
"ds_read_b64 v[56:57], v3 offset:26624\n"
"v_fmac_f32 v37, v89, v17\n"
"v_mfma_f32_16x16x32_bf16 v[32:35], v[108:111], v[60:63], v[32:35]\n"
"ds_read_b64 v[58:59], v3 offset:26880\n"
"s_waitcnt lgkmcnt(8)\n"
"v_mfma_f32_16x16x32_bf16 v[28:31], v[112:115], v[64:67], v[28:31]\n"
"ds_read_b64 v[60:61], v3 offset:27648\n"
"v_fmac_f32 v38, v89, v18\n"
"v_mfma_f32_16x16x32_bf16 v[32:35], v[116:119], v[64:67], v[32:35]\n"
"ds_read_b64 v[62:63], v3 offset:27904\n"
"v_fmac_f32 v39, v89, v19\n"
"v_mfma_f32_16x16x32_bf16 v[28:31], v[120:123], v[68:71], v[28:31]\n"
"ds_read_b64 v[64:65], v3 offset:28672\n"
"v_cvt_pk_bf16_f32 v36, v36, v37\n"
"v_mfma_f32_16x16x32_bf16 v[32:35], v[124:127], v[68:71], v[32:35]\n"
"ds_read_b64 v[66:67], v3 offset:28928\n"
"s_waitcnt lgkmcnt(8)\n"
"v_mfma_f32_16x16x32_bf16 v[28:31], v[128:131], v[72:75], v[28:31]\n"
"ds_read_b64 v[68:69], v3 offset:29696\n"
"v_cvt_pk_bf16_f32 v37, v38, v39\n"
"v_mfma_f32_16x16x32_bf16 v[32:35], v[132:135], v[72:75], v[32:35]\n"
"ds_read_b64 v[70:71], v3 offset:29952\n"
"v_mul_f32 v40, v88, v40\n"
"v_mfma_f32_16x16x32_bf16 v[52:55], v[72:75], v[72:75], v[52:55]\n"
"v_mul_f32 v41, v88, v41\n"
"v_mul_f32 v42, v88, v42\n"
"v_mfma_f32_16x16x32_bf16 v[28:31], v[136:139], v[76:79], v[28:31]\n"
"ds_read_b64 v[72:73], v3 offset:30720\n"
"v_mul_f32 v43, v88, v43\n"
"v_mfma_f32_16x16x32_bf16 v[32:35], v[140:143], v[76:79], v[32:35]\n"
"ds_read_b64 v[74:75], v3 offset:30976\n"
"s_waitcnt lgkmcnt(6)\n"
"v_mfma_f32_16x16x32_bf16 v[28:31], v[144:147], v[56:59], v[28:31]\n"
"ds_read_b64 v[76:77], v3 offset:31744\n"
"ds_read_b128 v[84:87], v11 offset:4352\n"
"ds_read_b32 v91, v12 offset:4352\n"
"v_mfma_f32_16x16x32_bf16 v[32:35], v[148:151], v[56:59], v[32:35]\n"
"ds_read_b64 v[78:79], v3 offset:32000\n"
"v_fmac_f32 v40, v89, v20\n"
"v_mfma_f32_16x16x32_bf16 v[28:31], v[152:155], v[60:63], v[28:31]\n"
"ds_read_b64 v[56:57], v3 offset:32768\n"
"v_fmac_f32 v41, v89, v21\n"
"v_mfma_f32_16x16x32_bf16 v[32:35], v[156:159], v[60:63], v[32:35]\n"
"ds_read_b64 v[58:59], v3 offset:33024\n"
"s_waitcnt lgkmcnt(8)\n"
"v_mfma_f32_16x16x32_bf16 v[28:31], v[160:163], v[64:67], v[28:31]\n"
"ds_read_b64 v[60:61], v3 offset:33792\n"
"v_fmac_f32 v42, v89, v22\n"
"v_mfma_f32_16x16x32_bf16 v[32:35], v[164:167], v[64:67], v[32:35]\n"
"ds_read_b64 v[62:63], v3 offset:34048\n"
"v_fmac_f32 v43, v89, v23\n"
"v_mfma_f32_16x16x32_bf16 v[52:55], v[64:67], v[64:67], v[52:55]\n"
"v_cvt_pk_bf16_f32 v40, v40, v41\n"
"v_cvt_pk_bf16_f32 v41, v42, v43\n"
"v_mfma_f32_16x16x32_bf16 v[28:31], v[168:171], v[68:71], v[28:31]\n"
"ds_read_b64 v[64:65], v3 offset:34816\n"
"s_waitcnt lgkmcnt(7)\n"
"v_add_f32 v84, v84, v85\n"
"v_add_f32 v86, v86, v87\n"
"v_mfma_f32_16x16x32_bf16 v[32:35], v[172:175], v[68:71], v[32:35]\n"
"ds_read_b64 v[66:67], v3 offset:35072\n"
"v_add_f32 v84, v84, v86\n"
"s_waitcnt lgkmcnt(6)\n"
"v_mfma_f32_16x16x32_bf16 v[28:31], v[176:179], v[72:75], v[28:31]\n"
"ds_read_b64 v[68:69], v3 offset:35840\n"
"v_mul_f32 v89, 0x3acccccd, v91\n"
"v_fma_f32 v84, -v91, v89, v84\n"
"v_mfma_f32_16x16x32_bf16 v[32:35], v[180:183], v[72:75], v[32:35]\n"
"ds_read_b64 v[70:71], v3 offset:36096\n"
"v_fmamk_f32 v84, v84, 0x3acccccd, v27\n"
"v_mfma_f32_16x16x32_bf16 v[28:31], v[184:187], v[76:79], v[28:31]\n"
"ds_read_b64 v[72:73], v3 offset:36864\n"
"v_cmp_u_f32 vcc, v84, v84\n"
"v_mfma_f32_16x16x32_bf16 v[32:35], v[188:191], v[76:79], v[32:35]\n"
"ds_read_b64 v[74:75], v3 offset:37120\n"
"s_waitcnt lgkmcnt(6)\n"
"v_mfma_f32_16x16x32_bf16 v[28:31], v[192:195], v[56:59], v[28:31]\n"
"ds_read_b64 v[76:77], v4 offset:20480\n"
"v_max_f32 v84, v84, v27\n"
"v_mfma_f32_16x16x32_bf16 v[32:35], v[196:199], v[56:59], v[32:35]\n"
"ds_read_b64 v[78:79], v4 offset:20736\n"
"v_rsq_f32 v88, v84\n"
"v_mfma_f32_16x16x32_bf16 v[52:55], v[56:59], v[56:59], v[52:55]\n"
"v_mul_f32 v89, -v89, v88\n"
"v_mfma_f32_16x16x32_bf16 v[28:31], v[200:203], v[60:63], v[28:31]\n"
"ds_read_b64 v[56:57], v5 offset:20480\n"
"s_cbranch_vccnz .Lsl_224\n"
".Lbk_225:\n"
"v_mfma_f32_16x16x32_bf16 v[32:35], v[204:207], v[60:63], v[32:35]\n"
"ds_read_b64 v[58:59], v5 offset:20736\n"
"ds_write_b64 v7, v[36:37] offset:48896\n"
"s_waitcnt lgkmcnt(7)\n"
"v_mfma_f32_16x16x32_bf16 v[28:31], v[208:211], v[64:67], v[28:31]\n"
"ds_read_b64 v[60:61], v6 offset:20480\n"
"v_mul_f32 v44, v88, v44\n"
"v_mul_f32 v45, v88, v45\n"
"v_mfma_f32_16x16x32_bf16 v[32:35], v[212:215], v[64:67], v[32:35]\n"
"ds_read_b64 v[62:63], v6 offset:20736\n"
"v_mul_f32 v46, v88, v46\n"
"v_mul_f32 v47, v88, v47\n"
"v_mfma_f32_16x16x32_bf16 v[28:31], v[216:219], v[68:71], v[28:31]\n"
"ds_read_b64 v[64:65], v3 offset:40960\n"
"v_fmac_f32 v44, v89, v16\n"
"v_fmac_f32 v45, v89, v17\n"
"v_mfma_f32_16x16x32_bf16 v[32:35], v[220:223], v[68:71], v[32:35]\n"
"ds_read_b64 v[66:67], v3 offset:41216\n"
"v_fmac_f32 v46, v89, v18\n"
"s_waitcnt lgkmcnt(7)\n"
"v_mfma_f32_16x16x32_bf16 v[28:31], v[224:227], v[72:75], v[28:31]\n"
"ds_read_b64 v[68:69], v3 offset:45056\n"
"v_fmac_f32 v47, v89, v19\n"
"v_cvt_pk_bf16_f32 v44, v44, v45\n"
"v_mfma_f32_16x16x32_bf16 v[32:35], v[228:231], v[72:75], v[32:35]\n"
"ds_read_b64 v[70:71], v3 offset:45312\n"
"v_cvt_pk_bf16_f32 v45, v46, v47\n"
"v_mul_f32 v48, v88, v48\n"
"v_mfma_f32_16x16x32_bf16 v[52:55], v[72:75], v[72:75], v[52:55]\n"
"ds_write_b64 v8, v[40:41] offset:48896\n"
"v_mfma_f32_16x16x32_bf16 v[28:31], v[232:235], v[76:79], v[28:31]\n"
"ds_read_b64 v[72:73], v3 offset:49152\n"
"v_mul_f32 v49, v88, v49\n"
"v_mul_f32 v50, v88, v50\n"
"v_mfma_f32_16x16x32_bf16 v[32:35], v[236:239], v[76:79], v[32:35]\n"
"ds_read_b64 v[74:75], v3 offset:49408\n"
"v_mul_f32 v51, v88, v51\n"
"s_waitcnt lgkmcnt(7)\n"
"v_mfma_f32_16x16x32_bf16 v[28:31], v[240:243], v[56:59], v[28:31]\n"
"ds_read_b64 v[76:77], v3 offset:53248\n"
"v_cndmask_b32 v94, v52, v53, s[34:35]\n"
"v_fmac_f32 v48, v89, v20\n"
"v_mfma_f32_16x16x32_bf16 v[32:35], v[244:247], v[56:59], v[32:35]\n"
"ds_read_b64 v[78:79], v3 offset:53504\n"
"v_cndmask_b32 v54, v54, v55, s[34:35]\n"
"v_fmac_f32 v49, v89, v21\n"
"v_mfma_f32_16x16x32_bf16 v[28:31], v[248:251], v[60:63], v[28:31]\n"
"ds_read_b64 v[56:57], v3 offset:57344\n"
"v_cndmask_b32 v94, v94, v54, s[20:21]\n"
"v_fmac_f32 v50, v89, v22\n"
"v_mfma_f32_16x16x32_bf16 v[32:35], v[252:255], v[60:63], v[32:35]\n"
"ds_read_b64 v[58:59], v3 offset:57600\n"
"s_waitcnt lgkmcnt(7)\n"
"s_cmp_eq_u32 s3, 3\n"
"s_cbranch_scc1 .Lfu_236\n"
"ds_read_b64 v[60:61], v3 offset:41984\n"
"v_fmac_f32 v51, v89, v23\n"
"v_mfma_f32_16x16x32_bf16 v[40:43], v[100:103], v[64:67], 0\n"
"ds_read_b64 v[62:63], v3 offset:42240\n"
"v_cndmask_b32 v94, v28, v94, s[22:23]\n"
"v_mfma_f32_16x16x32_bf16 v[52:55], v[64:67], v[64:67], 0\n"
"ds_write_b32 v9, v94 offset:4672\n"
"ds_read_b64 v[64:65], v3 offset:43008\n"
"v_cvt_pk_bf16_f32 v48, v48, v49\n"
"v_mfma_f32_16x16x32_bf16 v[40:43], v[132:135], v[68:71], v[40:43]\n"
"ds_read_b64 v[66:67], v3 offset:43264\n"
"v_cvt_pk_bf16_f32 v49, v50, v51\n"
"v_mfma_f32_16x16x32_bf16 v[52:55], v[68:71], v[68:71], v[52:55]\n"
"ds_write_b64 v7, v[44:45] offset:52864\n"
"s_waitcnt lgkmcnt(8)\n"
"ds_read_b64 v[68:69], v3 offset:44032\n"
"v_mfma_f32_16x16x32_bf16 v[40:43], v[164:167], v[72:75], v[40:43]\n"
"ds_read_b64 v[70:71], v3 offset:44288\n"
"v_mfma_f32_16x16x32_bf16 v[52:55], v[72:75], v[72:75], v[52:55]\n"
"ds_write_b64 v8, v[48:49] offset:52864\n"
"ds_read_b64 v[72:73], v3 offset:46080\n"
"v_mfma_f32_16x16x32_bf16 v[40:43], v[196:199], v[76:79], v[40:43]\n"
"ds_read_b64 v[74:75], v3 offset:46336\n"
"v_mfma_f32_16x16x32_bf16 v[52:55], v[76:79], v[76:79], v[52:55]\n"
"ds_read_b128 v[84:87], v11 offset:4672\n"
"ds_read_b32 v91, v12 offset:4672\n"
"s_waitcnt lgkmcnt(11)\n"
"ds_read_b64 v[76:77], v3 offset:47104\n"
"v_mfma_f32_16x16x32_bf16 v[40:43], v[228:231], v[56:59], v[40:43]\n"
"ds_read_b64 v[78:79], v3 offset:47360\n"
"v_mfma_f32_16x16x32_bf16 v[52:55], v[56:59], v[56:59], v[52:55]\n"
"ds_read_b64 v[56:57], v3 offset:48128\n"
"v_mfma_f32_16x16x32_bf16 v[40:43], v[108:111], v[60:63], v[40:43]\n"
"ds_read_b64 v[58:59], v3 offset:48384\n"
"s_waitcnt lgkmcnt(9)\n"
"ds_read_b64 v[60:61], v3 offset:50176\n"
"s_nop 1\n"
"ds_write_b32 v10, v52 offset:5312\n"
"v_cndmask_b32 v94, v52, v53, s[34:35]\n"
"v_cndmask_b32 v54, v54, v55, s[34:35]\n"
"v_mfma_f32_16x16x32_bf16 v[40:43], v[116:119], v[64:67], v[40:43]\n"
"ds_read_b64 v[62:63], v3 offset:50432\n"
"s_waitcnt lgkmcnt(8)\n"
"v_add_f32 v84, v84, v85\n"
"v_add_f32 v86, v86, v87\n"
"v_cndmask_b32 v94, v94, v54, s[20:21]\n"
"ds_write_b32 v9, v94 offset:4992\n"
"ds_read_b64 v[64:65], v3 offset:51200\n"
"v_add_f32 v84, v84, v86\n"
"s_waitcnt lgkmcnt(9)\n"
"v_mul_f32 v89, 0x3acccccd, v91\n"
"v_mfma_f32_16x16x32_bf16 v[40:43], v[124:127], v[68:71], v[40:43]\n"
"ds_read_b64 v[66:67], v3 offset:51456\n"
"v_fma_f32 v84, -v91, v89, v84\n"
"s_waitcnt lgkmcnt(8)\n"
"ds_read_b64 v[68:69], v3 offset:52224\n"
"v_fmamk_f32 v84, v84, 0x3acccccd, v27\n"
"v_cmp_u_f32 vcc, v84, v84\n"
"v_mfma_f32_16x16x32_bf16 v[40:43], v[140:143], v[72:75], v[40:43]\n"
"ds_read_b64 v[70:71], v3 offset:52480\n"
"v_max_f32 v84, v84, v27\n"
"v_rsq_f32 v88, v84\n"
"ds_read_b64 v[72:73], v3 offset:54272\n"
"v_mul_f32 v89, -v89, v88\n"
"v_mfma_f32_16x16x32_bf16 v[40:43], v[148:151], v[76:79], v[40:43]\n"
"ds_read_b64 v[74:75], v3 offset:54528\n"
"s_cbranch_vccnz .Lsl_228c\n"
".Lbk_229c:\n"
"s_waitcnt lgkmcnt(7)\n"
"ds_read_b64 v[76:77], v3 offset:55296\n"
"v_mul_f32 v28, v88, v28\n"
"v_mul_f32 v29, v88, v29\n"
"v_mfma_f32_16x16x32_bf16 v[40:43], v[156:159], v[56:59], v[40:43]\n"
"ds_read_b64 v[78:79], v3 offset:55552\n"
"ds_read_b128 v[44:47], v11 offset:4992\n"
"ds_read_b128 v[48:51], v11 offset:5312\n"
"ds_read_b64 v[56:57], v3 offset:56320\n"
"v_mul_f32 v30, v88, v30\n"
"v_mul_f32 v31, v88, v31\n"
"v_mfma_f32_16x16x32_bf16 v[40:43], v[172:175], v[60:63], v[40:43]\n"
"ds_read_b64 v[58:59], v3 offset:56576\n"
"v_fmac_f32 v28, v89, v16\n"
"s_waitcnt lgkmcnt(8)\n"
"ds_read_b64 v[60:61], v4 offset:40960\n"
"v_fmac_f32 v29, v89, v17\n"
"v_fmac_f32 v30, v89, v18\n"
"v_mfma_f32_16x16x32_bf16 v[40:43], v[180:183], v[64:67], v[40:43]\n"
"ds_read_b64 v[62:63], v4 offset:41216\n"
"v_fmac_f32 v31, v89, v19\n"
"v_cvt_pk_bf16_f32 v28, v28, v29\n"
"ds_read_b64 v[64:65], v5 offset:40960\n"
"v_cvt_pk_bf16_f32 v29, v30, v31\n"
"ds_write_b64 v7, v[28:29] offset:56832\n"
"v_mfma_f32_16x16x32_bf16 v[40:43], v[188:191], v[68:71], v[40:43]\n"
"ds_read_b64 v[66:67], v5 offset:41216\n"
"v_mul_f32 v32, v88, v32\n"
"s_waitcnt lgkmcnt(9)\n"
"ds_read_b64 v[68:69], v6 offset:40960\n"
"s_waitcnt lgkmcnt(9)\n"
"v_add_f32 v44, v44, v45\n"
"v_mul_f32 v33, v88, v33\n"
"v_mfma_f32_16x16x32_bf16 v[40:43], v[204:207], v[72:75], v[40:43]\n"
"ds_read_b64 v[70:71], v6 offset:41216\n"
"v_add_f32 v46, v46, v47\n"
"v_mul_f32 v34, v88, v34\n"
"s_waitcnt lgkmcnt(9)\n"
"v_add_f32 v48, v48, v49\n"
"v_add_f32 v50, v50, v51\n"
"v_mul_f32 v35, v88, v35\n"
"v_mfma_f32_16x16x32_bf16 v[40:43], v[212:215], v[76:79], v[40:43]\n"
"v_add_f32 v48, v48, v50\n"
"v_fmac_f32 v32, v89, v20\n"
"s_waitcnt lgkmcnt(5)\n"
"v_add_f32 v44, v44, v46\n"
"v_mul_f32 v53, 0x3acccccd, v48\n"
"v_fmac_f32 v33, v89, v21\n"
"v_mfma_f32_16x16x32_bf16 v[40:43], v[220:223], v[56:59], v[40:43]\n"
"v_fma_f32 v44, -v48, v53, v44\n"
"v_fmamk_f32 v44, v44, 0x3acccccd, v27\n"
"v_fmac_f32 v34, v89, v22\n"
"v_cmp_u_f32 vcc, v44, v44\n"
"v_max_f32 v44, v44, v27\n"
"v_fmac_f32 v35, v89, v23\n"
"v_mfma_f32_16x16x32_bf16 v[40:43], v[236:239], v[60:63], v[40:43]\n"
"v_rsq_f32 v52, v44\n"
"s_waitcnt lgkmcnt(0)\n"
"v_mul_f32 v53, -v53, v52\n"
"v_cvt_pk_bf16_f32 v32, v32, v33\n"
"v_cvt_pk_bf16_f32 v33, v34, v35\n"
"v_mfma_f32_16x16x32_bf16 v[40:43], v[244:247], v[64:67], v[40:43]\n"
"s_cbranch_vccnz .Lsl_232c\n"
".Lbk_233c:\n"
"ds_write_b64 v8, v[32:33] offset:56832\n"
"v_mfma_f32_16x16x32_bf16 v[40:43], v[252:255], v[68:71], v[40:43]\n"
"v_cmp_u_f32 vcc, v92, v92\n"
"s_nop 6\n"
"v_mul_f32 v40, v52, v40\n"
"v_mul_f32 v41, v52, v41\n"
"v_mul_f32 v42, v52, v42\n"
"v_mul_f32 v43, v52, v43\n"
"v_fmac_f32 v40, v53, v20\n"
"v_fmac_f32 v41, v53, v21\n"
"v_fmac_f32 v42, v53, v22\n"
"v_fmac_f32 v43, v53, v23\n"
"v_cvt_pk_bf16_f32 v40, v40, v41\n"
"v_cvt_pk_bf16_f32 v41, v42, v43\n"
"ds_write_b64 v8, v[40:41] offset:40960\n"
"s_cbranch_vccnz .Lrd_203\n"
".Lgo_202c:\n"
"ds_write_b32 v13, v13 offset:432\n"
"s_branch .Ljn_237\n"
".Lfu_236:\n"
"s_nop 0\n"
"v_mfma_f32_16x16x32_bf16 v[36:39], v[96:99], v[64:67], 0\n"
"ds_read_b64 v[60:61], v3 offset:41984\n"
"v_fmac_f32 v51, v89, v23\n"
"v_mfma_f32_16x16x32_bf16 v[40:43], v[100:103], v[64:67], 0\n"
"ds_read_b64 v[62:63], v3 offset:42240\n"
"v_cndmask_b32 v94, v28, v94, s[22:23]\n"
"v_mfma_f32_16x16x32_bf16 v[52:55], v[64:67], v[64:67], 0\n"
"ds_write_b32 v9, v94 offset:4672\n"
"v_mfma_f32_16x16x32_bf16 v[36:39], v[128:131], v[68:71], v[36:39]\n"
"ds_read_b64 v[64:65], v3 offset:43008\n"
"v_cvt_pk_bf16_f32 v48, v48, v49\n"
"v_mfma_f32_16x16x32_bf16 v[40:43], v[132:135], v[68:71], v[40:43]\n"
"ds_read_b64 v[66:67], v3 offset:43264\n"
"v_cvt_pk_bf16_f32 v49, v50, v51\n"
"v_mfma_f32_16x16x32_bf16 v[52:55], v[68:71], v[68:71], v[52:55]\n"
"ds_write_b64 v7, v[44:45] offset:52864\n"
"s_waitcnt lgkmcnt(8)\n"
"v_mfma_f32_16x16x32_bf16 v[36:39], v[160:163], v[72:75], v[36:39]\n"
"ds_read_b64 v[68:69], v3 offset:44032\n"
"v_mfma_f32_16x16x32_bf16 v[40:43], v[164:167], v[72:75], v[40:43]\n"
"ds_read_b64 v[70:71], v3 offset:44288\n"
"v_mfma_f32_16x16x32_bf16 v[52:55], v[72:75], v[72:75], v[52:55]\n"
"ds_write_b64 v8, v[48:49] offset:52864\n"
"v_mfma_f32_16x16x32_bf16 v[36:39], v[192:195], v[76:79], v[36:39]\n"
"ds_read_b64 v[72:73], v3 offset:46080\n"
"v_mfma_f32_16x16x32_bf16 v[40:43], v[196:199], v[76:79], v[40:43]\n"
"ds_read_b64 v[74:75], v3 offset:46336\n"
"v_mfma_f32_16x16x32_bf16 v[52:55], v[76:79], v[76:79], v[52:55]\n"
"ds_read_b128 v[84:87], v11 offset:4672\n"
"ds_read_b32 v91, v12 offset:4672\n"
"s_waitcnt lgkmcnt(11)\n"
"v_mfma_f32_16x16x32_bf16 v[36:39], v[224:227], v[56:59], v[36:39]\n"
"ds_read_b64 v[76:77], v3 offset:47104\n"
"v_mfma_f32_16x16x32_bf16 v[40:43], v[228:231], v[56:59], v[40:43]\n"
"ds_read_b64 v[78:79], v3 offset:47360\n"
"v_mfma_f32_16x16x32_bf16 v[52:55], v[56:59], v[56:59], v[52:55]\n"
"v_mfma_f32_16x16x32_bf16 v[36:39], v[104:107], v[60:63], v[36:39]\n"
"ds_read_b64 v[56:57], v3 offset:48128\n"
"v_mfma_f32_16x16x32_bf16 v[40:43], v[108:111], v[60:63], v[40:43]\n"
"ds_read_b64 v[58:59], v3 offset:48384\n"
"s_waitcnt lgkmcnt(9)\n"
"v_mfma_f32_16x16x32_bf16 v[36:39], v[112:115], v[64:67], v[36:39]\n"
"ds_read_b64 v[60:61], v3 offset:50176\n"
"ds_write_b32 v10, v52 offset:5312\n"
"v_cndmask_b32 v94, v52, v53, s[34:35]\n"
"v_cndmask_b32 v54, v54, v55, s[34:35]\n"
"v_mfma_f32_16x16x32_bf16 v[40:43], v[116:119], v[64:67], v[40:43]\n"
"ds_read_b64 v[62:63], v3 offset:50432\n"
"s_waitcnt lgkmcnt(8)\n"
"v_add_f32 v84, v84, v85\n"
"v_add_f32 v86, v86, v87\n"
"v_cndmask_b32 v94, v94, v54, s[20:21]\n"
"ds_write_b32 v9, v94 offset:4992\n"
"v_mfma_f32_16x16x32_bf16 v[36:39], v[120:123], v[68:71], v[36:39]\n"
"ds_read_b64 v[64:65], v3 offset:51200\n"
"v_add_f32 v84, v84, v86\n"
"s_waitcnt lgkmcnt(9)\n"
"v_mul_f32 v89, 0x3acccccd, v91\n"
"v_mfma_f32_16x16x32_bf16 v[40:43], v[124:127], v[68:71], v[40:43]\n"
"ds_read_b64 v[66:67], v3 offset:51456\n"
"v_fma_f32 v84, -v91, v89, v84\n"
"s_waitcnt lgkmcnt(8)\n"
"v_mfma_f32_16x16x32_bf16 v[36:39], v[136:139], v[72:75], v[36:39]\n"
"ds_read_b64 v[68:69], v3 offset:52224\n"
"v_fmamk_f32 v84, v84, 0x3acccccd, v27\n"
"v_cmp_u_f32 vcc, v84, v84\n"
"v_mfma_f32_16x16x32_bf16 v[40:43], v[140:143], v[72:75], v[40:43]\n"
"ds_read_b64 v[70:71], v3 offset:52480\n"
"v_max_f32 v84, v84, v27\n"
"v_rsq_f32 v88, v84\n"
"v_mfma_f32_16x16x32_bf16 v[36:39], v[144:147], v[76:79], v[36:39]\n"
"ds_read_b64 v[72:73], v3 offset:54272\n"
"v_mul_f32 v89, -v89, v88\n"
"v_mfma_f32_16x16x32_bf16 v[40:43], v[148:151], v[76:79], v[40:43]\n"
"ds_read_b64 v[74:75], v3 offset:54528\n"
"s_cbranch_vccnz .Lsl_228\n"
".Lbk_229:\n"
"s_waitcnt lgkmcnt(7)\n"
"v_mfma_f32_16x16x32_bf16 v[36:39], v[152:155], v[56:59], v[36:39]\n"
"ds_read_b64 v[76:77], v3 offset:55296\n"
"v_mul_f32 v28, v88, v28\n"
"v_mul_f32 v29, v88, v29\n"
"v_mfma_f32_16x16x32_bf16 v[40:43], v[156:159], v[56:59], v[40:43]\n"
"ds_read_b64 v[78:79], v3 offset:55552\n"
"ds_read_b128 v[44:47], v11 offset:4992\n"
"ds_read_b128 v[48:51], v11 offset:5312\n"
"v_mfma_f32_16x16x32_bf16 v[36:39], v[168:171], v[60:63], v[36:39]\n"
"ds_read_b64 v[56:57], v3 offset:56320\n"
"v_mul_f32 v30, v88, v30\n"
"v_mul_f32 v31, v88, v31\n"
"v_mfma_f32_16x16x32_bf16 v[40:43], v[172:175], v[60:63], v[40:43]\n"
"ds_read_b64 v[58:59], v3 offset:56576\n"
"v_fmac_f32 v28, v89, v16\n"
"s_waitcnt lgkmcnt(8)\n"
"v_mfma_f32_16x16x32_bf16 v[36:39], v[176:179], v[64:67], v[36:39]\n"
"ds_read_b64 v[60:61], v4 offset:40960\n"
"v_fmac_f32 v29, v89, v17\n"
"v_fmac_f32 v30, v89, v18\n"
"v_mfma_f32_16x16x32_bf16 v[40:43], v[180:183], v[64:67], v[40:43]\n"
"ds_read_b64 v[62:63], v4 offset:41216\n"
"v_fmac_f32 v31, v89, v19\n"
"v_cvt_pk_bf16_f32 v28, v28, v29\n"
"v_mfma_f32_16x16x32_bf16 v[36:39], v[184:187], v[68:71], v[36:39]\n"
"ds_read_b64 v[64:65], v5 offset:40960\n"
"v_cvt_pk_bf16_f32 v29, v30, v31\n"
"ds_write_b64 v7, v[28:29] offset:56832\n"
"v_mfma_f32_16x16x32_bf16 v[40:43], v[188:191], v[68:71], v[40:43]\n"
"ds_read_b64 v[66:67], v5 offset:41216\n"
"v_mul_f32 v32, v88, v32\n"
"s_waitcnt lgkmcnt(9)\n"
"v_mfma_f32_16x16x32_bf16 v[36:39], v[200:203], v[72:75], v[36:39]\n"
"ds_read_b64 v[68:69], v6 offset:40960\n"
"s_waitcnt lgkmcnt(9)\n"
"v_add_f32 v44, v44, v45\n"
"v_mul_f32 v33, v88, v33\n"
"v_mfma_f32_16x16x32_bf16 v[40:43], v[204:207], v[72:75], v[40:43]\n"
"ds_read_b64 v[70:71], v6 offset:41216\n"
"v_add_f32 v46, v46, v47\n"
"v_mul_f32 v34, v88, v34\n"
"v_mfma_f32_16x16x32_bf16 v[36:39], v[208:211], v[76:79], v[36:39]\n"
"s_waitcnt lgkmcnt(9)\n"
"v_add_f32 v48, v48, v49\n"
"v_add_f32 v50, v50, v51\n"
"v_mul_f32 v35, v88, v35\n"
"v_mfma_f32_16x16x32_bf16 v[40:43], v[212:215], v[76:79], v[40:43]\n"
"v_add_f32 v48, v48, v50\n"
"v_fmac_f32 v32, v89, v20\n"
"s_waitcnt lgkmcnt(5)\n"
"v_mfma_f32_16x16x32_bf16 v[36:39], v[216:219], v[56:59], v[36:39]\n"
"v_add_f32 v44, v44, v46\n"
"v_mul_f32 v53, 0x3acccccd, v48\n"
"v_fmac_f32 v33, v89, v21\n"
"v_mfma_f32_16x16x32_bf16 v[40:43], v[220:223], v[56:59], v[40:43]\n"
"v_fma_f32 v44, -v48, v53, v44\n"
"v_fmamk_f32 v44, v44, 0x3acccccd, v27\n"
"v_fmac_f32 v34, v89, v22\n"
"v_mfma_f32_16x16x32_bf16 v[36:39], v[232:235], v[60:63], v[36:39]\n"
"v_cmp_u_f32 vcc, v44, v44\n"
"v_max_f32 v44, v44, v27\n"
"v_fmac_f32 v35, v89, v23\n"
"v_mfma_f32_16x16x32_bf16 v[40:43], v[236:239], v[60:63], v[40:43]\n"
"v_rsq_f32 v52, v44\n"
"s_waitcnt lgkmcnt(0)\n"
"v_mfma_f32_16x16x32_bf16 v[36:39], v[240:243], v[64:67], v[36:39]\n"
"v_mul_f32 v53, -v53, v52\n"
"v_cvt_pk_bf16_f32 v32, v32, v33\n"
"v_cvt_pk_bf16_f32 v33, v34, v35\n"
"v_mfma_f32_16x16x32_bf16 v[40:43], v[244:247], v[64:67], v[40:43]\n"
"s_cbranch_vccnz .Lsl_232\n"
".Lbk_233:\n"
"ds_write_b64 v8, v[32:33] offset:56832\n"
"v_mfma_f32_16x16x32_bf16 v[36:39], v[248:251], v[68:71], v[36:39]\n"
"v_mfma_f32_16x16x32_bf16 v[40:43], v[252:255], v[68:71], v[40:43]\n"
"v_cmp_u_f32 vcc, v92, v92\n"
"s_nop 3\n"
"v_mul_f32 v36, v52, v36\n"
"v_mul_f32 v37, v52, v37\n"
"s_cbranch_vccnz .Lrd_203\n"
".Lgo_202:\n"
"ds_write_b32 v13, v13 offset:432\n"
"v_mul_f32 v38, v52, v38\n"
"v_mul_f32 v39, v52, v39\n"
"v_fmac_f32 v36, v53, v16\n"
"v_fmac_f32 v37, v53, v17\n"
"v_fmac_f32 v38, v53, v18\n"
"v_fmac_f32 v39, v53, v19\n"
"v_cvt_pk_bf16_f32 v36, v36, v37\n"
"v_cvt_pk_bf16_f32 v37, v38, v39\n"
"ds_write_b64 v7, v[36:37] offset:40960\n"
"v_mul_f32 v40, v52, v40\n"
"v_mul_f32 v41, v52, v41\n"
"v_mul_f32 v42, v52, v42\n"
"v_mul_f32 v43, v52, v43\n"
"v_fmac_f32 v40, v53, v20\n"
"v_fmac_f32 v41, v53, v21\n"
"v_fmac_f32 v42, v53, v22\n"
"v_fmac_f32 v43, v53, v23\n"
"v_cvt_pk_bf16_f32 v40, v40, v41\n"
"v_cvt_pk_bf16_f32 v41, v42, v43\n"
"ds_write_b64 v8, v[40:41] offset:40960\n"
".Ljn_237:\n"
"v_mov_b32 v46, 0\n"
"v_mov_b32 v47, 0\n"
"v_mov_b32 v50, 0\n"
"v_mov_b32 v51, 0\n"
"s_cmp_lg_u32 s3, 2\n"
"s_cbranch_scc1 .Lnw_282\n"
"s_mov_b32 s17, 0x100000\n"
".Ltp_283:\n"
"ds_read_b32 v90, v14 offset:432\n"
"s_sub_u32 s17, s17, 1\n"
"s_cbranch_scc1 .Lou_284\n"
"s_waitcnt lgkmcnt(0)\n"
"v_cmp_eq_u32 vcc, 0, v90\n"
"s_cbranch_vccnz .Ltp_283\n"
".Lou_284:\n"
"s_waitcnt lgkmcnt(0)\n"
".Lnw_282:\n"
"ds_read_b32 v90, v14 offset:432\n"
"s_mov_b64 exec, 0xffff\n"
"ds_read_b64 v[46:47], v24 offset:44960\n"
"ds_read_b64 v[50:51], v24 offset:45000\n"
"s_mov_b64 exec, -1\n"
"ds_read_b64 v[44:45], v25 offset:44928\n"
"ds_read_b64 v[56:57], v25 offset:41040\n"
"ds_read_b64 v[58:59], v24 offset:41072\n"
"ds_read_b64 v[64:65], v25 offset:45008\n"
"ds_read_b64 v[66:67], v24 offset:45040\n"
"ds_read_b64 v[48:49], v25 offset:44968\n"
"ds_read_b64 v[60:61], v25 offset:41080\n"
"ds_read_b64 v[62:63], v24 offset:41112\n"
"ds_read_b64 v[68:69], v25 offset:45048\n"
"ds_read_b64 v[70:71], v24 offset:45080\n"
"s_waitcnt lgkmcnt(12)\n"
"v_or_b32 v90, s33, v90\n"
"s_mov_b32 s17, 0x100000\n"
"v_cmp_eq_u32 vcc, 0, v90\n"
"s_cbranch_vccz .Lok_286\n"
".Ltp_285:\n"
"ds_read_b32 v90, v14 offset:432\n"
"s_mov_b64 exec, 0xffff\n"
"ds_read_b64 v[46:47], v24 offset:44960\n"
"ds_read_b64 v[50:51], v24 offset:45000\n"
"s_mov_b64 exec, -1\n"
"ds_read_b64 v[44:45], v25 offset:44928\n"
"ds_read_b64 v[56:57], v25 offset:41040\n"
"ds_read_b64 v[58:59], v24 offset:41072\n"
"ds_read_b64 v[64:65], v25 offset:45008\n"
"ds_read_b64 v[66:67], v24 offset:45040\n"
"ds_read_b64 v[48:49], v25 offset:44968\n"
"ds_read_b64 v[60:61], v25 offset:41080\n"
"ds_read_b64 v[62:63], v24 offset:41112\n"
"ds_read_b64 v[68:69], v25 offset:45048\n"
"ds_read_b64 v[70:71], v24 offset:45080\n"
"s_sub_u32 s17, s17, 1\n"
"s_cbranch_scc1 .Lok_286\n"
"s_waitcnt lgkmcnt(12)\n"
"v_cmp_eq_u32 vcc, 0, v90\n"
"s_cbranch_vccnz .Ltp_285\n"
"s_waitcnt lgkmcnt(0)\n"
".Lok_286:\n"
"ds_read_b64_tr_b16 v[28:29], v26 offset:40960\n"
"ds_read_b64_tr_b16 v[30:31], v26 offset:44928\n"
"ds_read_b64_tr_b16 v[32:33], v26 offset:41000\n"
"s_waitcnt lgkmcnt(10)\n"
"v_mfma_f32_16x16x32_bf16 v[56:59], v[56:59], v[44:47], 0\n"
"s_waitcnt lgkmcnt(8)\n"
"v_mfma_f32_16x16x32_bf16 v[64:67], v[64:67], v[44:47], 0\n"
"ds_read_b64_tr_b16 v[34:35], v26 offset:44968\n"
"ds_read_b64_tr_b16 v[36:37], v26 offset:40992\n"
"ds_read_b64_tr_b16 v[38:39], v26 offset:44960\n"
"s_waitcnt lgkmcnt(8)\n"
"v_mfma_f32_16x16x32_bf16 v[60:63], v[60:63], v[48:51], 0\n"
"s_waitcnt lgkmcnt(6)\n"
"v_mfma_f32_16x16x32_bf16 v[68:71], v[68:71], v[48:51], 0\n"
"ds_read_b64_tr_b16 v[40:41], v26 offset:41032\n"
"ds_read_b64_tr_b16 v[42:43], v26 offset:45000\n"
"v_mov_b32 v84, 0x3f803f80\n"
"v_mov_b32 v85, 0x3f803f80\n"
"v_mov_b32 v86, 0x3f803f80\n"
"v_mov_b32 v87, 0x3f803f80\n"
"v_cndmask_b32 v76, v56, v57, s[34:35]\n"
"v_cndmask_b32 v72, v64, v65, s[34:35]\n"
"v_cndmask_b32 v76, v76, v72, s[36:37]\n"
"v_cndmask_b32 v77, v58, v59, s[34:35]\n"
"v_cndmask_b32 v72, v66, v67, s[34:35]\n"
"v_cndmask_b32 v77, v77, v72, s[38:39]\n"
"v_max_f32 v52, v76, v77\n"
"v_max_f32 v54, v76, v77\n"
"v_cndmask_b32 v78, v60, v61, s[34:35]\n"
"v_cndmask_b32 v72, v68, v69, s[34:35]\n"
"v_cndmask_b32 v78, v78, v72, s[36:37]\n"
"v_cndmask_b32 v79, v62, v63, s[34:35]\n"
"v_cndmask_b32 v72, v70, v71, s[34:35]\n"
"v_cndmask_b32 v79, v79, v72, s[38:39]\n"
"v_max_f32 v53, v78, v79\n"
"v_max_f32 v55, v78, v79\n"
"v_permlane16_swap_b32 v52, v54\n"
"s_nop 0\n"
"v_permlane16_swap_b32 v53, v55\n"
"v_max_f32 v72, v52, v54\n"
"v_max_f32 v52, v52, v54\n"
"v_max_f32 v73, v53, v55\n"
"v_max_f32 v53, v53, v55\n"
"v_permlane32_swap_b32 v52, v72\n"
"s_nop 0\n"
"v_permlane32_swap_b32 v53, v73\n"
"v_max_f32 v52, v52, v72\n"
"v_max_f32 v53, v53, v73\n"
"v_mul_f32 v52, 0xbea52b5b, v52\n"
"v_mul_f32 v53, 0xbea52b5b, v53\n"
"v_fmamk_f32 v76, v76, 0x3ea52b5b, v52\n"
"v_fmamk_f32 v77, v77, 0x3ea52b5b, v52\n"
"v_fmamk_f32 v78, v78, 0x3ea52b5b, v53\n"
"v_fmamk_f32 v79, v79, 0x3ea52b5b, v53\n"
"v_exp_f32 v76, v76\n"
"v_exp_f32 v77, v77\n"
"v_exp_f32 v78, v78\n"
"v_exp_f32 v79, v79\n"
"v_cvt_pk_bf16_f32 v52, v76, v77\n"
"v_cvt_pk_bf16_f32 v53, v78, v79\n"
"v_perm_b32 v44, v52, v52, v80\n"
"v_perm_b32 v45, v52, v52, v81\n"
"v_perm_b32 v46, v52, v52, v82\n"
"v_perm_b32 v47, v52, v52, v83\n"
"v_perm_b32 v48, v53, v53, v80\n"
"v_perm_b32 v49, v53, v53, v81\n"
"v_perm_b32 v50, v53, v53, v82\n"
"v_perm_b32 v51, v53, v53, v83\n"
"v_mfma_f32_16x16x32_bf16 v[56:59], v[84:87], v[44:47], 0\n"
"s_nop 0\n"
"v_mfma_f32_16x16x32_bf16 v[60:63], v[84:87], v[48:51], 0\n"
"s_waitcnt lgkmcnt(6)\n"
"v_mfma_f32_16x16x32_bf16 v[28:31], v[28:31], v[44:47], 0\n"
"s_waitcnt lgkmcnt(4)\n"
"v_mfma_f32_16x16x32_bf16 v[32:35], v[32:35], v[48:51], 0\n"
"s_waitcnt lgkmcnt(2)\n"
"v_mfma_f32_16x16x32_bf16 v[36:39], v[36:39], v[44:47], 0\n"
"s_waitcnt lgkmcnt(0)\n"
"v_mfma_f32_16x16x32_bf16 v[40:43], v[40:43], v[48:51], 0\n"
"v_cndmask_b32 v88, 2.0, v1, s[40:41]\n"
"v_rcp_f32 v52, v56\n"
"v_rcp_f32 v53, v60\n"
"s_add_u32 s31, s30, 0xba0000\n"
"v_mul_f32 v28, v52, v28\n"
"v_mul_f32 v29, v52, v29\n"
"v_mul_f32 v30, v52, v30\n"
"v_mul_f32 v31, v52, v31\n"
"v_mul_f32 v32, v53, v32\n"
"v_mul_f32 v33, v53, v33\n"
"v_mul_f32 v34, v53, v34\n"
"v_mul_f32 v35, v53, v35\n"
"v_cvt_pk_bf16_f32 v28, v28, v29\n"
"v_cvt_pk_bf16_f32 v29, v30, v31\n"
"v_cvt_pk_bf16_f32 v30, v32, v33\n"
"v_cvt_pk_bf16_f32 v31, v34, v35\n"
"v_min_u32 v28, -2, v28\n"
"v_min_u32 v29, -2, v29\n"
"v_min_u32 v30, -2, v30\n"
"v_min_u32 v31, -2, v31\n"
"buffer_store_dwordx4 v[28:31], v1, s[4:7], s31 offen offset:0 sc1\n"
"v_mul_f32 v36, v52, v36\n"
"v_mul_f32 v37, v52, v37\n"
"v_mul_f32 v38, v52, v38\n"
"v_mul_f32 v39, v52, v39\n"
"v_mul_f32 v40, v53, v40\n"
"v_mul_f32 v41, v53, v41\n"
"v_mul_f32 v42, v53, v42\n"
"v_mul_f32 v43, v53, v43\n"
"v_cvt_pk_bf16_f32 v36, v36, v37\n"
"v_cvt_pk_bf16_f32 v37, v38, v39\n"
"v_cvt_pk_bf16_f32 v38, v40, v41\n"
"v_cvt_pk_bf16_f32 v39, v42, v43\n"
"v_min_u32 v36, -2, v36\n"
"v_min_u32 v37, -2, v37\n"
"v_min_u32 v38, -2, v38\n"
"v_min_u32 v39, -2, v39\n"
"buffer_store_dwordx4 v[36:39], v88, s[4:7], s31 offen offset:1024 sc1\n"
"ds_write_b32 v13, v13 offset:480\n"
"s_nop 2\n"
"s_cmp_lt_i32 s45, 0\n"
"s_cbranch_scc1 .Lnc_287\n"
"s_setprio 3\n"
"s_lshl_b32 s79, s3, 10\n"
"v_lshl_add_u32 v34, v0, 4, s79\n"
"v_lshrrev_b32 v59, 7, v34\n"
"v_and_b32 v58, 0x70, v34\n"
"s_mov_b32 s79, 0x5000\n"
"v_mad_i32_i24 v33, v59, s79, v58\n"
"s_mov_b32 s17, 0x100000\n"
".Ltp_288:\n"
"s_sleep 1\n"
"ds_read_b32 v42, v14 offset:432\n"
"s_sub_u32 s17, s17, 1\n"
"s_cbranch_scc1 .Lou_289\n"
"s_waitcnt lgkmcnt(0)\n"
"v_cmp_eq_u32 vcc, 0, v42\n"
"s_cbranch_vccnz .Ltp_288\n"
".Lou_289:\n"
"s_waitcnt lgkmcnt(0)\n"
"s_mov_b32 s17, 0x100000\n"
".Ltp_290:\n"
"s_sleep 1\n"
"ds_read_b32 v42, v14 offset:496\n"
"s_sub_u32 s17, s17, 1\n"
"s_cbranch_scc1 .Lou_291\n"
"s_waitcnt lgkmcnt(0)\n"
"v_cmp_eq_u32 vcc, 0, v42\n"
"s_cbranch_vccnz .Ltp_290\n"
".Lou_291:\n"
"s_waitcnt lgkmcnt(0)\n"
"s_lshl_b32 s79, s45, 8\n"
"s_add_u32 s46, s79, 0x1128000\n"
"s_add_u32 s47, s46, 0x140000\n"
"s_and_b32 s79, s2, 7\n"
"s_lshl_b32 s79, s79, 14\n"
"s_add_u32 s48, s79, 0x1668000\n"
"s_add_u32 s49, s48, 0x1000\n"
"v_and_b32 v94, 0xffffff80, v33\n"
"v_add_u32 v94, v94, v33\n"
"buffer_load_dwordx4 v[78:81], v94, s[4:7], s46 offen offset:0 sc1\n"
"buffer_load_dwordx4 v[82:85], v94, s[4:7], s46 offen offset:128 sc1\n"
"buffer_load_dwordx4 v[86:89], v94, s[4:7], s47 offen offset:0 sc1\n"
"buffer_load_dwordx4 v[90:93], v94, s[4:7], s47 offen offset:128 sc1\n"
"buffer_load_dwordx4 v[70:73], v34, s[4:7], s48 offen sc1\n"
"buffer_load_dwordx4 v[74:77], v34, s[4:7], s49 offen sc1\n"
"v_lshrrev_b32 v44, 4, v0\n"
"v_mov_b32 v59, 0xffff0000\n"
"v_mov_b32 v58, 0xffff\n"
"s_mov_b32 vcc_lo, 0xaaaaaaaa\n"
"s_mov_b32 vcc_hi, 0xaaaaaaaa\n"
"v_cndmask_b32 v45, v58, v59, vcc\n"
"v_and_b32 v59, 15, v0\n"
"v_lshlrev_b32 v46, 5, v59\n"
"v_and_b32 v59, 15, v0\n"
"v_lshrrev_b32 v47, 2, v59\n"
"s_lshl_b32 s79, s3, 10\n"
"v_lshl_add_u32 v48, v0, 4, s79\n"
"v_lshrrev_b32 v49, 2, v48\n"
"v_lshrrev_b32 v50, 6, v48\n"
"v_lshlrev_b32 v59, 3, v44\n"
"v_and_b32 v58, 15, v0\n"
"v_sub_u32 v59, v59, v58\n"
"v_add_u32 v51, 0x3ef, v59\n"
"v_and_b32 v59, 24, v50\n"
"v_xor_b32 v59, v49, v59\n"
"v_add_u32 v36, 0xa000, v59\n"
"s_lshl_b32 s79, s3, 8\n"
"s_add_u32 s79, s79, 0xa000\n"
"v_and_b32 v59, 15, v0\n"
"v_xor_b32 v59, s3, v59\n"
"v_and_b32 v59, 1, v59\n"
"v_lshrrev_b32 v58, 1, v44\n"
"v_xor_b32 v58, v47, v58\n"
"v_and_b32 v58, 1, v58\n"
"v_lshl_add_u32 v59, v59, 1, v58\n"
"v_and_b32 v58, 15, v0\n"
"v_and_b32 v58, 2, v58\n"
"v_and_b32 v57, 15, v0\n"
"v_lshrrev_b32 v57, 3, v57\n"
"v_lshlrev_b32 v57, 7, v57\n"
"v_lshl_add_u32 v57, v47, 11, v57\n"
"v_lshl_add_u32 v57, v44, 6, v57\n"
"v_lshl_add_u32 v58, v58, 4, v57\n"
"v_lshl_add_u32 v59, v59, 3, v58\n"
"v_add_u32 v31, s79, v59\n"
"s_mul_i32 s79, s3, 0x2100\n"
"s_add_u32 s79, s79, 0x13840\n"
"v_lshrrev_b32 v59, 2, v51\n"
"v_and_b32 v58, 3, v51\n"
"v_mul_i32_i24 v58, 0x840, v58\n"
"v_lshl_add_u32 v59, v59, 3, v58\n"
"v_add_u32 v28, s79, v59\n"
"s_mul_i32 s79, s3, 0x1200\n"
"s_add_u32 s79, s79, 0x1c420\n"
"v_lshl_add_u32 v59, v44, 4, v46\n"
"v_add_u32 v29, s79, v59\n"
"s_lshl_b32 s79, s3, 11\n"
"s_lshr_b32 s78, s3, 1\n"
"s_lshl_b32 s78, s78, 7\n"
"s_add_u32 s79, s79, s78\n"
"s_add_u32 s79, s79, 0xa000\n"
"s_and_b32 s78, s3, 1\n"
"v_xor_b32 v59, v44, v47\n"
"v_xor_b32 v59, s78, v59\n"
"v_and_b32 v59, 3, v59\n"
"v_lshl_add_u32 v59, v59, 3, v46\n"
"v_add_u32 v30, s79, v59\n"
"v_xor_b32 v32, 8, v31\n"
"v_add_u32 v35, 0x1c600, v49\n"
"v_xor_b32 v37, 8, v36\n"
"s_mov_b32 vcc_lo, 0x300003\n"
"s_mov_b32 vcc_hi, 0x30000300\n"
"v_cndmask_b32 v38, 0, v45, vcc\n"
"s_mov_b32 vcc_lo, 0xc0000c\n"
"s_mov_b32 vcc_hi, 0xc0000c00\n"
"v_cndmask_b32 v39, 0, v45, vcc\n"
"s_lshl_b32 s79, s3, 2\n"
"s_add_u32 s79, s79, 0x26800\n"
"v_mov_b32 v40, s79\n"
"v_and_b32 v59, 48, v48\n"
"v_mul_i32_i24 v59, 0x50, v59\n"
"s_mov_b32 s79, 0x2800\n"
"v_mad_i32_i24 v41, v50, s79, v59\n"
"ds_read_b32 v40, v40\n"
"s_waitcnt lgkmcnt(0)\n"
"v_sub_u32 v44, 0x7f000000, v40\n"
"v_lshrrev_b32 v45, 16, v44\n"
"v_or_b32 v44, v44, v45\n"
"v_and_b32 v38, v44, v38\n"
"v_and_b32 v39, v44, v39\n"
"s_mov_b32 s52, 0x05040100\n"
"s_mov_b32 s53, 0x07060302\n"
"s_mov_b32 s17, 32768\n"
".Ltp_292:\n"
"s_waitcnt vmcnt(0)\n"
"s_sub_u32 s17, s17, 1\n"
"s_cbranch_scc1 .Lou_293\n"
"v_bitop3_b32 v54, v79, v81, s16 bitop3:0x7e\n"
"v_bitop3_b32 v55, v83, v85, s16 bitop3:0x7e\n"
"v_bitop3_b32 v56, v87, v89, s16 bitop3:0x7e\n"
"v_bitop3_b32 v57, v91, v93, s16 bitop3:0x7e\n"
"v_or3_b32 v58, v54, v55, v56\n"
"v_or_b32 v58, v58, v57\n"
"v_cmp_ne_u32 vcc, 0, v58\n"
"s_cbranch_vccz .Lou_293\n"
"s_sleep 2\n"
"v_cmp_ne_u32 vcc, 0, v54\n"
"s_cbranch_vccz .Lsk_294\n"
"s_mov_b64 exec, vcc\n"
"buffer_load_dwordx4 v[78:81], v94, s[4:7], s46 offen offset:0 sc1\n"
"s_mov_b64 exec, -1\n"
".Lsk_294:\n"
"v_cmp_ne_u32 vcc, 0, v55\n"
"s_cbranch_vccz .Lsk_295\n"
"s_mov_b64 exec, vcc\n"
"buffer_load_dwordx4 v[82:85], v94, s[4:7], s46 offen offset:128 sc1\n"
"s_mov_b64 exec, -1\n"
".Lsk_295:\n"
"v_cmp_ne_u32 vcc, 0, v56\n"
"s_cbranch_vccz .Lsk_296\n"
"s_mov_b64 exec, vcc\n"
"buffer_load_dwordx4 v[86:89], v94, s[4:7], s47 offen offset:0 sc1\n"
"s_mov_b64 exec, -1\n"
".Lsk_296:\n"
"v_cmp_ne_u32 vcc, 0, v57\n"
"s_cbranch_vccz .Lsk_297\n"
"s_mov_b64 exec, vcc\n"
"buffer_load_dwordx4 v[90:93], v94, s[4:7], s47 offen offset:128 sc1\n"
"s_mov_b64 exec, -1\n"
".Lsk_297:\n"
"s_branch .Ltp_292\n"
".Lou_293:\n"
"s_waitcnt vmcnt(0)\n"
"v_perm_b32 v54, v82, v78, s52\n"
"v_perm_b32 v55, v82, v78, s53\n"
"v_perm_b32 v56, v84, v80, s52\n"
"v_perm_b32 v57, v84, v80, s53\n"
"ds_write_b32 v36, v54 offset:0\n"
"ds_write_b32 v37, v55 offset:2048\n"
"ds_write_b32 v36, v56 offset:4224\n"
"ds_write_b32 v37, v57 offset:6272\n"
"v_perm_b32 v54, v90, v86, s52\n"
"v_perm_b32 v55, v90, v86, s53\n"
"v_perm_b32 v56, v92, v88, s52\n"
"v_perm_b32 v57, v92, v88, s53\n"
"ds_write_b32 v36, v54 offset:1024\n"
"ds_write_b32 v37, v55 offset:3072\n"
"ds_write_b32 v36, v56 offset:5248\n"
"ds_write_b32 v37, v57 offset:7296\n"
"v_lshlrev_b32 v54, 16, v78\n"
"v_lshlrev_b32 v55, 16, v82\n"
"v_and_b32 v56, 0xffff0000, v78\n"
"v_and_b32 v57, 0xffff0000, v82\n"
"v_lshlrev_b32 v58, 16, v80\n"
"v_lshlrev_b32 v59, 16, v84\n"
"v_and_b32 v60, 0xffff0000, v80\n"
"v_and_b32 v61, 0xffff0000, v84\n"
"v_lshlrev_b32 v62, 16, v86\n"
"v_lshlrev_b32 v63, 16, v90\n"
"v_and_b32 v64, 0xffff0000, v86\n"
"v_and_b32 v65, 0xffff0000, v90\n"
"v_lshlrev_b32 v66, 16, v88\n"
"v_lshlrev_b32 v67, 16, v92\n"
"v_and_b32 v68, 0xffff0000, v88\n"
"v_and_b32 v69, 0xffff0000, v92\n"
"s_mov_b32 s17, 32768\n"
".Ltp_298:\n"
"s_waitcnt vmcnt(0)\n"
"s_sub_u32 s17, s17, 1\n"
"s_cbranch_scc1 .Lou_299\n"
"v_max_u32 v78, v70, v71\n"
"v_max3_u32 v78, v72, v73, v78\n"
"v_max3_u32 v78, v74, v75, v78\n"
"v_max3_u32 v78, v76, v77, v78\n"
"v_cmp_eq_u32 vcc, -1, v78\n"
"s_cbranch_vccz .Lou_299\n"
"v_max_u32 v78, v72, v73\n"
"v_max3_u32 v78, v70, v71, v78\n"
"v_cmp_eq_u32 vcc, -1, v78\n"
"s_cbranch_vccz .Lsk_300\n"
"s_mov_b64 exec, vcc\n"
"buffer_load_dwordx4 v[70:73], v34, s[4:7], s48 offen sc1\n"
"s_mov_b64 exec, -1\n"
".Lsk_300:\n"
"v_max_u32 v78, v76, v77\n"
"v_max3_u32 v78, v74, v75, v78\n"
"v_cmp_eq_u32 vcc, -1, v78\n"
"s_cbranch_vccz .Lsk_301\n"
"s_mov_b64 exec, vcc\n"
"buffer_load_dwordx4 v[74:77], v34, s[4:7], s49 offen sc1\n"
"s_mov_b64 exec, -1\n"
".Lsk_301:\n"
"s_branch .Ltp_298\n"
".Lou_299:\n"
"s_waitcnt vmcnt(0)\n"
"v_fma_f32 v54, v54, v71, v70\n"
"v_fma_f32 v55, v55, v73, v72\n"
"v_cvt_pk_f16_f32 v54, v54, v55\n"
"v_fma_f32 v56, v56, v71, v70\n"
"v_fma_f32 v57, v57, v73, v72\n"
"v_cvt_pk_f16_f32 v56, v56, v57\n"
"v_fma_f32 v58, v58, v71, v70\n"
"v_fma_f32 v59, v59, v73, v72\n"
"v_cvt_pk_f16_f32 v58, v58, v59\n"
"v_fma_f32 v60, v60, v71, v70\n"
"v_fma_f32 v61, v61, v73, v72\n"
"v_cvt_pk_f16_f32 v60, v60, v61\n"
"ds_write2st64_b32 v35, v54, v56 offset0:0 offset1:18\n"
"ds_write2st64_b32 v35, v58, v60 offset0:36 offset1:54\n"
"v_fma_f32 v62, v62, v75, v74\n"
"v_fma_f32 v63, v63, v77, v76\n"
"v_cvt_pk_f16_f32 v62, v62, v63\n"
"v_fma_f32 v64, v64, v75, v74\n"
"v_fma_f32 v65, v65, v77, v76\n"
"v_cvt_pk_f16_f32 v64, v64, v65\n"
"v_fma_f32 v66, v66, v75, v74\n"
"v_fma_f32 v67, v67, v77, v76\n"
"v_cvt_pk_f16_f32 v66, v66, v67\n"
"v_fma_f32 v68, v68, v75, v74\n"
"v_fma_f32 v69, v69, v77, v76\n"
"v_cvt_pk_f16_f32 v68, v68, v69\n"
"ds_write2st64_b32 v35, v62, v64 offset0:4 offset1:22\n"
"ds_write2st64_b32 v35, v66, v68 offset0:40 offset1:58\n"
"ds_write_b32 v13, v13 offset:512\n"
"s_mov_b32 s17, 0x100000\n"
".Ltp_302:\n"
"ds_read_b32 v42, v14 offset:512\n"
"s_sub_u32 s17, s17, 1\n"
"s_cbranch_scc1 .Lou_303\n"
"s_waitcnt lgkmcnt(0)\n"
"v_cmp_eq_u32 vcc, 0, v42\n"
"s_cbranch_vccnz .Ltp_302\n"
".Lou_303:\n"
"s_waitcnt lgkmcnt(0)\n"
"s_setprio 0\n"
"s_mov_b32 s50, 0xfffefffe\n"
"ds_read_b64 v[60:61], v28 offset:1984\n"
"ds_read_b64 v[62:63], v28 offset:1992\n"
"ds_read_b128 v[64:67], v29 offset:1984\n"
"ds_read_b128 v[68:71], v29 offset:1472\n"
"ds_read_b64 v[72:73], v28 offset:1472\n"
"ds_read_b64 v[74:75], v28 offset:1480\n"
"ds_read_b128 v[76:79], v29 offset:960\n"
"s_waitcnt lgkmcnt(0)\n"
"v_mfma_f32_16x16x32_f16 v[56:59], v[60:63], v[64:67], 0\n"
"ds_read_b64 v[80:81], v28 offset:960\n"
"ds_read_b64 v[82:83], v28 offset:968\n"
"v_mfma_f32_16x16x32_f16 v[52:55], v[60:63], v[68:71], 0\n"
"ds_read_b128 v[84:87], v29 offset:448\n"
"ds_read_b64 v[88:89], v28 offset:448\n"
"v_mfma_f32_16x16x32_f16 v[56:59], v[72:75], v[68:71], v[56:59]\n"
"ds_read_b64 v[90:91], v28 offset:456\n"
"ds_read_b64 v[92:93], v28 offset:1920\n"
"v_mfma_f32_16x16x32_f16 v[48:51], v[60:63], v[76:79], 0\n"
"ds_read_b64 v[94:95], v28 offset:1928\n"
"s_waitcnt lgkmcnt(4)\n"
"v_mfma_f32_16x16x32_f16 v[52:55], v[72:75], v[76:79], v[52:55]\n"
"ds_read_b128 v[2:5], v29 offset:1920\n"
"ds_read_b128 v[34:37], v29 offset:1408\n"
"v_mfma_f32_16x16x32_f16 v[56:59], v[80:83], v[76:79], v[56:59]\n"
"ds_read_b64 v[64:65], v28 offset:1408\n"
"ds_read_b64 v[66:67], v28 offset:1416\n"
"v_mfma_f32_16x16x32_f16 v[44:47], v[60:63], v[84:87], 0\n"
"ds_read_b128 v[68:71], v29 offset:896\n"
"ds_read_b64 v[76:77], v28 offset:896\n"
"v_mfma_f32_16x16x32_f16 v[48:51], v[72:75], v[84:87], v[48:51]\n"
"ds_read_b64 v[78:79], v28 offset:904\n"
"s_waitcnt lgkmcnt(5)\n"
"v_mfma_f32_16x16x32_f16 v[52:55], v[80:83], v[84:87], v[52:55]\n"
"ds_read_b128 v[60:63], v29 offset:384\n"
"ds_read_b64 v[72:73], v28 offset:384\n"
"v_mfma_f32_16x16x32_f16 v[56:59], v[88:91], v[84:87], v[56:59]\n"
"ds_read_b64 v[74:75], v28 offset:392\n"
"ds_read_b64 v[80:81], v28 offset:1856\n"
"v_mfma_f32_16x16x32_f16 v[56:59], v[92:95], v[2:5], v[56:59]\n"
"ds_read_b64 v[82:83], v28 offset:1864\n"
"ds_read_b128 v[84:87], v29 offset:1856\n"
"v_mfma_f32_16x16x32_f16 v[52:55], v[92:95], v[34:37], v[52:55]\n"
"ds_read_b128 v[88:91], v29 offset:1344\n"
"s_waitcnt lgkmcnt(7)\n"
"v_mfma_f32_16x16x32_f16 v[56:59], v[64:67], v[34:37], v[56:59]\n"
"ds_read_b64 v[2:3], v30 offset:0\n"
"ds_read_b64 v[34:35], v28 offset:1344\n"
"v_mfma_f32_16x16x32_f16 v[48:51], v[92:95], v[68:71], v[48:51]\n"
"ds_read_b64 v[36:37], v28 offset:1352\n"
"v_mfma_f32_16x16x32_f16 v[52:55], v[64:67], v[68:71], v[52:55]\n"
"v_mfma_f32_16x16x32_f16 v[56:59], v[76:79], v[68:71], v[56:59]\n"
"ds_read_b128 v[68:71], v29 offset:832\n"
"s_waitcnt lgkmcnt(8)\n"
"v_mfma_f32_16x16x32_f16 v[44:47], v[92:95], v[60:63], v[44:47]\n"
"ds_read_b64 v[92:93], v28 offset:832\n"
"v_mfma_f32_16x16x32_f16 v[48:51], v[64:67], v[60:63], v[48:51]\n"
"ds_read_b64 v[94:95], v28 offset:840\n"
"v_mfma_f32_16x16x32_f16 v[52:55], v[76:79], v[60:63], v[52:55]\n"
"ds_read_b128 v[64:67], v29 offset:320\n"
"ds_read_b64 v[76:77], v28 offset:320\n"
"v_mfma_f32_16x16x32_f16 v[56:59], v[72:75], v[60:63], v[56:59]\n"
"ds_read_b64 v[78:79], v28 offset:328\n"
"s_waitcnt lgkmcnt(6)\n"
"v_mfma_f32_16x16x32_f16 v[56:59], v[80:83], v[84:87], v[56:59]\n"
"ds_read_b64 v[60:61], v30 offset:512\n"
"ds_read_b64 v[72:73], v28 offset:1792\n"
"v_mfma_f32_16x16x32_f16 v[52:55], v[80:83], v[88:91], v[52:55]\n"
"ds_read_b64 v[74:75], v28 offset:1800\n"
"s_lshl_b32 s46, s45, 3\n"
"v_mfma_f32_16x16x16_bf16 v[44:47], v[38:39], v[2:3], v[44:47]\n"
"ds_read_b128 v[84:87], v29 offset:1792\n"
"v_mfma_f32_16x16x32_f16 v[56:59], v[34:37], v[88:91], v[56:59]\n"
"ds_read_b128 v[2:5], v29 offset:1280\n"
"s_waitcnt lgkmcnt(7)\n"
"v_mfma_f32_16x16x32_f16 v[48:51], v[80:83], v[68:71], v[48:51]\n"
"ds_read_b64 v[88:89], v28 offset:1280\n"
"v_mfma_f32_16x16x32_f16 v[52:55], v[34:37], v[68:71], v[52:55]\n"
"ds_read_b64 v[90:91], v28 offset:1288\n"
"v_mfma_f32_16x16x32_f16 v[56:59], v[92:95], v[68:71], v[56:59]\n"
"ds_read_b128 v[68:71], v29 offset:768\n"
"v_mfma_f32_16x16x32_f16 v[44:47], v[80:83], v[64:67], v[44:47]\n"
"ds_read_b64 v[80:81], v28 offset:768\n"
"s_waitcnt lgkmcnt(8)\n"
"v_mfma_f32_16x16x32_f16 v[48:51], v[34:37], v[64:67], v[48:51]\n"
"ds_read_b64 v[82:83], v28 offset:776\n"
"v_mfma_f32_16x16x32_f16 v[52:55], v[92:95], v[64:67], v[52:55]\n"
"ds_read_b128 v[34:37], v29 offset:256\n"
"ds_read_b64 v[92:93], v28 offset:256\n"
"v_mfma_f32_16x16x32_f16 v[56:59], v[76:79], v[64:67], v[56:59]\n"
"ds_read_b64 v[94:95], v28 offset:264\n"
"ds_read_b64 v[64:65], v28 offset:1728\n"
"v_mfma_f32_16x16x16_bf16 v[48:51], v[38:39], v[60:61], v[48:51]\n"
"ds_read_b64 v[66:67], v28 offset:1736\n"
"s_waitcnt lgkmcnt(7)\n"
"v_mfma_f32_16x16x32_f16 v[56:59], v[72:75], v[84:87], v[56:59]\n"
"ds_read_b128 v[76:79], v29 offset:1728\n"
"ds_read_b128 v[60:63], v29 offset:1216\n"
"v_mfma_f32_16x16x32_f16 v[52:55], v[72:75], v[2:5], v[52:55]\n"
"ds_read_b64 v[84:85], v28 offset:1216\n"
"v_mfma_f32_16x16x32_f16 v[56:59], v[88:91], v[2:5], v[56:59]\n"
"ds_read_b64 v[86:87], v28 offset:1224\n"
"v_mfma_f32_16x16x32_f16 v[48:51], v[72:75], v[68:71], v[48:51]\n"
"ds_read_b128 v[2:5], v29 offset:704\n"
"s_waitcnt lgkmcnt(9)\n"
"v_mfma_f32_16x16x32_f16 v[52:55], v[88:91], v[68:71], v[52:55]\n"
"v_mfma_f32_16x16x32_f16 v[56:59], v[80:83], v[68:71], v[56:59]\n"
"ds_read_b64 v[68:69], v30 offset:1024\n"
"v_mfma_f32_16x16x32_f16 v[44:47], v[72:75], v[34:37], v[44:47]\n"
"ds_read_b64 v[72:73], v28 offset:704\n"
"v_mfma_f32_16x16x32_f16 v[48:51], v[88:91], v[34:37], v[48:51]\n"
"ds_read_b64 v[74:75], v28 offset:712\n"
"s_waitcnt lgkmcnt(6)\n"
"v_mfma_f32_16x16x32_f16 v[52:55], v[80:83], v[34:37], v[52:55]\n"
"ds_read_b128 v[88:91], v29 offset:192\n"
"ds_read_b64 v[80:81], v28 offset:192\n"
"v_mfma_f32_16x16x32_f16 v[56:59], v[92:95], v[34:37], v[56:59]\n"
"ds_read_b64 v[82:83], v28 offset:200\n"
"ds_read_b64 v[34:35], v28 offset:1664\n"
"v_mfma_f32_16x16x32_f16 v[56:59], v[64:67], v[76:79], v[56:59]\n"
"ds_read_b64 v[36:37], v28 offset:1672\n"
"ds_read_b128 v[92:95], v29 offset:1664\n"
"v_mfma_f32_16x16x32_f16 v[52:55], v[64:67], v[60:63], v[52:55]\n"
"ds_read_b128 v[76:79], v29 offset:1152\n"
"s_waitcnt lgkmcnt(9)\n"
"v_mfma_f32_16x16x32_f16 v[56:59], v[84:87], v[60:63], v[56:59]\n"
"ds_read_b64 v[60:61], v28 offset:1152\n"
"v_mfma_f32_16x16x32_f16 v[48:51], v[64:67], v[2:5], v[48:51]\n"
"ds_read_b64 v[62:63], v28 offset:1160\n"
"v_mfma_f32_16x16x16_bf16 v[52:55], v[38:39], v[68:69], v[52:55]\n"
"ds_read_b128 v[68:71], v29 offset:640\n"
"s_nop 4\n"
"v_mfma_f32_16x16x32_f16 v[52:55], v[84:87], v[2:5], v[52:55]\n"
"s_waitcnt lgkmcnt(9)\n"
"v_mfma_f32_16x16x32_f16 v[56:59], v[72:75], v[2:5], v[56:59]\n"
"ds_read_b64 v[2:3], v28 offset:640\n"
"v_mfma_f32_16x16x32_f16 v[44:47], v[64:67], v[88:91], v[44:47]\n"
"ds_read_b64 v[4:5], v28 offset:648\n"
"v_mfma_f32_16x16x32_f16 v[48:51], v[84:87], v[88:91], v[48:51]\n"
"ds_read_b128 v[64:67], v29 offset:128\n"
"v_mfma_f32_16x16x32_f16 v[52:55], v[72:75], v[88:91], v[52:55]\n"
"ds_read_b64 v[84:85], v30 offset:1536\n"
"s_waitcnt lgkmcnt(5)\n"
"v_mfma_f32_16x16x32_f16 v[56:59], v[80:83], v[88:91], v[56:59]\n"
"ds_read_b64 v[72:73], v28 offset:128\n"
"ds_read_b64 v[74:75], v28 offset:136\n"
"v_mfma_f32_16x16x32_f16 v[56:59], v[34:37], v[92:95], v[56:59]\n"
"ds_read_b64 v[88:89], v28 offset:1600\n"
"ds_read_b64 v[90:91], v28 offset:1608\n"
"v_mfma_f32_16x16x32_f16 v[52:55], v[34:37], v[76:79], v[52:55]\n"
"ds_read_b128 v[80:83], v29 offset:1600\n"
"v_mfma_f32_16x16x32_f16 v[56:59], v[60:63], v[76:79], v[56:59]\n"
"ds_read_b128 v[92:95], v29 offset:1088\n"
"s_waitcnt lgkmcnt(7)\n"
"v_mfma_f32_16x16x32_f16 v[48:51], v[34:37], v[68:71], v[48:51]\n"
"ds_read_b64 v[76:77], v28 offset:1088\n"
"v_mfma_f32_16x16x32_f16 v[52:55], v[60:63], v[68:71], v[52:55]\n"
"ds_read_b64 v[78:79], v28 offset:1096\n"
"v_mfma_f32_16x16x32_f16 v[56:59], v[2:5], v[68:71], v[56:59]\n"
"ds_read_b128 v[68:71], v29 offset:576\n"
"v_mfma_f32_16x16x32_f16 v[44:47], v[34:37], v[64:67], v[44:47]\n"
"ds_read_b64 v[34:35], v28 offset:576\n"
"s_waitcnt lgkmcnt(8)\n"
"s_nop 0\n"
"v_mfma_f32_16x16x16_bf16 v[56:59], v[38:39], v[84:85], v[56:59]\n"
"ds_read_b64 v[36:37], v28 offset:584\n"
"v_mfma_f32_16x16x32_f16 v[48:51], v[60:63], v[64:67], v[48:51]\n"
"ds_read_b128 v[84:87], v29 offset:64\n"
"ds_read_b64 v[60:61], v28 offset:64\n"
"v_mfma_f32_16x16x32_f16 v[52:55], v[2:5], v[64:67], v[52:55]\n"
"ds_read_b64 v[62:63], v28 offset:72\n"
"ds_read_b64 v[2:3], v28 offset:1536\n"
"v_mfma_f32_16x16x32_f16 v[56:59], v[72:75], v[64:67], v[56:59]\n"
"ds_read_b64 v[4:5], v28 offset:1544\n"
"s_waitcnt lgkmcnt(7)\n"
"v_mfma_f32_16x16x32_f16 v[56:59], v[88:91], v[80:83], v[56:59]\n"
"ds_read_b128 v[64:67], v29 offset:0\n"
"ds_read_b128 v[72:75], v29 offset:512\n"
"v_mfma_f32_16x16x32_f16 v[52:55], v[88:91], v[92:95], v[52:55]\n"
"ds_read_b64 v[80:81], v28 offset:1024\n"
"v_mfma_f32_16x16x32_f16 v[56:59], v[76:79], v[92:95], v[56:59]\n"
"ds_read_b64 v[82:83], v28 offset:1032\n"
"v_mfma_f32_16x16x32_f16 v[48:51], v[88:91], v[68:71], v[48:51]\n"
"ds_read_b128 v[92:95], v29 offset:1024\n"
"s_waitcnt lgkmcnt(9)\n"
"v_mfma_f32_16x16x32_f16 v[52:55], v[76:79], v[68:71], v[52:55]\n"
"v_mfma_f32_16x16x32_f16 v[56:59], v[34:37], v[68:71], v[56:59]\n"
"ds_read_b64 v[68:69], v28 offset:512\n"
"v_mfma_f32_16x16x32_f16 v[44:47], v[88:91], v[84:87], v[44:47]\n"
"ds_read_b64 v[70:71], v28 offset:520\n"
"v_mfma_f32_16x16x32_f16 v[48:51], v[76:79], v[84:87], v[48:51]\n"
"ds_read_b128 v[88:91], v29 offset:1536\n"
"s_waitcnt lgkmcnt(6)\n"
"v_mfma_f32_16x16x32_f16 v[52:55], v[34:37], v[84:87], v[52:55]\n"
"ds_read_b64 v[76:77], v28 offset:0\n"
"v_mfma_f32_16x16x32_f16 v[56:59], v[60:63], v[84:87], v[56:59]\n"
"ds_read_b64 v[78:79], v28 offset:8\n"
"v_mfma_f32_16x16x32_f16 v[44:47], v[2:5], v[64:67], v[44:47]\n"
"v_mfma_f32_16x16x32_f16 v[48:51], v[2:5], v[72:75], v[48:51]\n"
"s_waitcnt lgkmcnt(3)\n"
"v_mfma_f32_16x16x32_f16 v[48:51], v[80:83], v[64:67], v[48:51]\n"
"v_mfma_f32_16x16x32_f16 v[52:55], v[2:5], v[92:95], v[52:55]\n"
"v_mul_f32 v44, v40, v44\n"
"v_mul_f32 v45, v40, v45\n"
"v_mul_f32 v46, v40, v46\n"
"v_mfma_f32_16x16x32_f16 v[52:55], v[80:83], v[72:75], v[52:55]\n"
"v_mul_f32 v47, v40, v47\n"
"v_cvt_pk_bf16_f32 v44, v44, v45\n"
"v_cvt_pk_bf16_f32 v45, v46, v47\n"
"v_mfma_f32_16x16x32_f16 v[52:55], v[68:71], v[64:67], v[52:55]\n"
"v_pk_min_u16 v44, v44, s50\n"
"v_pk_min_u16 v45, v45, s50\n"
"s_waitcnt lgkmcnt(0)\n"
"v_mfma_f32_16x16x32_f16 v[56:59], v[2:5], v[88:91], v[56:59]\n"
"ds_write_b64 v30, v[44:45] offset:0\n"
"v_mfma_f32_16x16x32_f16 v[56:59], v[80:83], v[92:95], v[56:59]\n"
"v_mul_f32 v48, v40, v48\n"
"v_mul_f32 v49, v40, v49\n"
"v_mfma_f32_16x16x32_f16 v[56:59], v[68:71], v[72:75], v[56:59]\n"
"v_mul_f32 v50, v40, v50\n"
"v_mul_f32 v51, v40, v51\n"
"v_mfma_f32_16x16x32_f16 v[56:59], v[76:79], v[64:67], v[56:59]\n"
"v_cvt_pk_bf16_f32 v48, v48, v49\n"
"v_cvt_pk_bf16_f32 v49, v50, v51\n"
"v_pk_min_u16 v48, v48, s50\n"
"v_pk_min_u16 v49, v49, s50\n"
"ds_write_b64 v30, v[48:49] offset:512\n"
"v_mul_f32 v52, v40, v52\n"
"v_mul_f32 v53, v40, v53\n"
"v_mul_f32 v54, v40, v54\n"
"v_mul_f32 v55, v40, v55\n"
"v_cvt_pk_bf16_f32 v52, v52, v53\n"
"v_cvt_pk_bf16_f32 v53, v54, v55\n"
"v_pk_min_u16 v52, v52, s50\n"
"v_pk_min_u16 v53, v53, s50\n"
"ds_write_b64 v30, v[52:53] offset:1024\n"
"v_mul_f32 v56, v40, v56\n"
"v_mul_f32 v57, v40, v57\n"
"v_mul_f32 v58, v40, v58\n"
"v_mul_f32 v59, v40, v59\n"
"v_cvt_pk_bf16_f32 v56, v56, v57\n"
"v_cvt_pk_bf16_f32 v57, v58, v59\n"
"v_pk_min_u16 v56, v56, s50\n"
"v_pk_min_u16 v57, v57, s50\n"
"ds_write_b64 v30, v[56:57] offset:1536\n"
"ds_write_b32 v13, v13 offset:528\n"
"s_mov_b32 s17, 0x100000\n"
".Ltp_304:\n"
"ds_read_b32 v42, v14 offset:528\n"
"s_sub_u32 s17, s17, 1\n"
"s_cbranch_scc1 .Lou_305\n"
"s_waitcnt lgkmcnt(0)\n"
"v_cmp_eq_u32 vcc, 0, v42\n"
"s_cbranch_vccnz .Ltp_304\n"
".Lou_305:\n"
"s_waitcnt lgkmcnt(0)\n"
"ds_read_b64_tr_b16 v[60:61], v31 offset:0\n"
"ds_read_b64_tr_b16 v[62:63], v32 offset:0\n"
"ds_read_b64_tr_b16 v[64:65], v31 offset:1024\n"
"ds_read_b64_tr_b16 v[66:67], v32 offset:1024\n"
"s_add_u32 s47, s46, 0x1400\n"
"s_add_u32 s48, s46, 0xa0000\n"
"s_add_u32 s49, s46, 0xa1400\n"
"s_waitcnt lgkmcnt(3)\n"
"buffer_store_dwordx2 v[60:61], v41, s[12:15], s46 offen\n"
"s_mov_b32 s89, 18\n"
".Ldl_306:\n"
"s_sub_u32 s89, s89, 1\n"
"s_cbranch_scc1 .Lde_307\n"
"s_sleep 1\n"
"s_branch .Ldl_306\n"
".Lde_307:\n"
"s_waitcnt lgkmcnt(2)\n"
"buffer_store_dwordx2 v[62:63], v41, s[12:15], s47 offen\n"
"s_mov_b32 s89, 18\n"
".Ldl_308:\n"
"s_sub_u32 s89, s89, 1\n"
"s_cbranch_scc1 .Lde_309\n"
"s_sleep 1\n"
"s_branch .Ldl_308\n"
".Lde_309:\n"
"s_waitcnt lgkmcnt(1)\n"
"buffer_store_dwordx2 v[64:65], v41, s[12:15], s48 offen\n"
"s_mov_b32 s89, 18\n"
".Ldl_310:\n"
"s_sub_u32 s89, s89, 1\n"
"s_cbranch_scc1 .Lde_311\n"
"s_sleep 1\n"
"s_branch .Ldl_310\n"
".Lde_311:\n"
"s_waitcnt lgkmcnt(0)\n"
"buffer_store_dwordx2 v[66:67], v41, s[12:15], s49 offen\n"
"v_add_u32 v31, 0x16c00, v31\n"
"v_add_u32 v32, 0x16c00, v32\n"
"s_add_u32 s46, s46, 0x140000\n"
"s_add_u32 s47, s47, 0x140000\n"
"s_add_u32 s48, s48, 0x140000\n"
"s_add_u32 s49, s49, 0x140000\n"
"s_lshr_b32 s79, s3, 1\n"
"s_mul_i32 s79, s79, -16\n"
"v_add_u32 v76, s79, v14\n"
"s_mov_b32 s17, 0x100000\n"
".Ltp_312:\n"
"s_sleep 1\n"
"ds_read_b32 v42, v14 offset:576\n"
"s_sub_u32 s17, s17, 1\n"
"s_cbranch_scc1 .Lou_313\n"
"s_waitcnt lgkmcnt(0)\n"
"v_cmp_eq_u32 vcc, 0, v42\n"
"s_cbranch_vccnz .Ltp_312\n"
".Lou_313:\n"
"s_waitcnt lgkmcnt(0)\n"
"ds_read_b64_tr_b16 v[68:69], v31 offset:0\n"
"ds_read_b64_tr_b16 v[70:71], v32 offset:0\n"
"s_waitcnt lgkmcnt(1)\n"
"buffer_store_dwordx2 v[68:69], v41, s[12:15], s46 offen\n"
"s_waitcnt lgkmcnt(0)\n"
"buffer_store_dwordx2 v[70:71], v41, s[12:15], s47 offen\n"
"s_mov_b32 s17, 0x100000\n"
".Ltp_314:\n"
"s_sleep 1\n"
"ds_read_b32 v42, v76 offset:560\n"
"s_sub_u32 s17, s17, 1\n"
"s_cbranch_scc1 .Lou_315\n"
"s_waitcnt lgkmcnt(0)\n"
"v_cmp_eq_u32 vcc, 0, v42\n"
"s_cbranch_vccnz .Ltp_314\n"
".Lou_315:\n"
"s_waitcnt lgkmcnt(0)\n"
"ds_read_b64_tr_b16 v[72:73], v31 offset:1024\n"
"ds_read_b64_tr_b16 v[74:75], v32 offset:1024\n"
"s_waitcnt lgkmcnt(1)\n"
"buffer_store_dwordx2 v[72:73], v41, s[12:15], s48 offen\n"
"s_waitcnt lgkmcnt(0)\n"
"buffer_store_dwordx2 v[74:75], v41, s[12:15], s49 offen\n"
"s_setprio 1\n"
"s_nop 0\n"
".Lnc_287:\n"
"s_add_u32 s31, s2, s3\n"
"s_cmp_lg_u32 s31, 0\n"
"s_cbranch_scc1 .Lfin_316\n"
"v_mov_b32 v32, s16\n"
"s_mov_b64 exec, 1\n"
"buffer_store_dword v32, off, s[4:7], 0 sc1\n"
"s_mov_b64 exec, -1\n"
".Lfin_316:\n"
"s_endpgm\n"
"s_branch .L_xend\n"
".Lsl_7:\n"
"s_nop 7\n"
"s_mov_b32 s17, 0x100000\n"
".Ltp_9:\n"
"ds_read_b32 v90, v14 offset:0\n"
"s_sub_u32 s17, s17, 1\n"
"s_cbranch_scc1 .Lou_10\n"
"s_waitcnt lgkmcnt(0)\n"
"v_cmp_eq_u32 vcc, 0, v90\n"
"s_cbranch_vccnz .Ltp_9\n"
".Lou_10:\n"
"s_waitcnt lgkmcnt(0)\n"
"s_branch .Lbk_8\n"
".Lsl_18:\n"
"s_nop 7\n"
"s_mov_b32 s17, 0x100000\n"
".Ltp_20:\n"
"ds_read_b32 v90, v14 offset:48\n"
"s_sub_u32 s17, s17, 1\n"
"s_cbranch_scc1 .Lou_21\n"
"s_waitcnt lgkmcnt(0)\n"
"v_cmp_eq_u32 vcc, 0, v90\n"
"s_cbranch_vccnz .Ltp_20\n"
".Lou_21:\n"
"s_waitcnt lgkmcnt(0)\n"
"s_branch .Lbk_19\n"
".Lsl_22:\n"
"s_nop 7\n"
"s_mov_b32 s17, 0x100000\n"
".Ltp_24:\n"
"ds_read_b32 v90, v14 offset:64\n"
"s_sub_u32 s17, s17, 1\n"
"s_cbranch_scc1 .Lou_25\n"
"s_waitcnt lgkmcnt(0)\n"
"v_cmp_eq_u32 vcc, 0, v90\n"
"s_cbranch_vccnz .Ltp_24\n"
".Lou_25:\n"
"s_waitcnt lgkmcnt(0)\n"
"s_branch .Lbk_23\n"
".Lsl_26:\n"
"s_nop 7\n"
"s_mov_b32 s17, 0x100000\n"
".Ltp_28:\n"
"ds_read_b32 v90, v14 offset:16\n"
"s_sub_u32 s17, s17, 1\n"
"s_cbranch_scc1 .Lou_29\n"
"s_waitcnt lgkmcnt(0)\n"
"v_cmp_eq_u32 vcc, 0, v90\n"
"s_cbranch_vccnz .Ltp_28\n"
".Lou_29:\n"
"s_waitcnt lgkmcnt(0)\n"
"s_branch .Lbk_27\n"
".Lsl_30:\n"
"s_nop 7\n"
"s_mov_b32 s17, 0x100000\n"
".Ltp_32:\n"
"ds_read_b32 v90, v14 offset:80\n"
"s_sub_u32 s17, s17, 1\n"
"s_cbranch_scc1 .Lou_33\n"
"s_waitcnt lgkmcnt(0)\n"
"v_cmp_eq_u32 vcc, 0, v90\n"
"s_cbranch_vccnz .Ltp_32\n"
".Lou_33:\n"
"s_waitcnt lgkmcnt(0)\n"
"s_branch .Lbk_31\n"
".Lsl_38:\n"
"s_nop 7\n"
"s_mov_b32 s17, 0x100000\n"
".Ltp_40:\n"
"ds_read_b128 v[84:87], v11 offset:0\n"
"ds_read_b32 v91, v12 offset:0\n"
"s_sub_u32 s17, s17, 1\n"
"s_cbranch_scc1 .Lou_41\n"
"s_waitcnt lgkmcnt(0)\n"
"v_max3_u32 v88, v84, v85, v91\n"
"v_max3_u32 v88, v86, v87, v88\n"
"v_cmp_eq_u32 vcc, -1, v88\n"
"s_cbranch_vccnz .Ltp_40\n"
".Lou_41:\n"
"s_waitcnt lgkmcnt(0)\n"
"v_add_f32 v84, v84, v85\n"
"v_add_f32 v86, v86, v87\n"
"v_add_f32 v84, v84, v86\n"
"v_mul_f32 v89, 0x3acccccd, v91\n"
"v_fma_f32 v84, -v91, v89, v84\n"
"v_fmamk_f32 v84, v84, 0x3acccccd, v27\n"
"v_max_f32 v84, v84, v27\n"
"v_rsq_f32 v88, v84\n"
"s_nop 0\n"
"v_mul_f32 v89, -v89, v88\n"
"s_nop 1\n"
"s_branch .Lbk_39\n"
".Lsl_42:\n"
"s_nop 7\n"
"s_mov_b32 s17, 0x100000\n"
".Ltp_44:\n"
"ds_read_b128 v[84:87], v11 offset:320\n"
"ds_read_b32 v91, v12 offset:320\n"
"s_sub_u32 s17, s17, 1\n"
"s_cbranch_scc1 .Lou_45\n"
"s_waitcnt lgkmcnt(0)\n"
"v_max3_u32 v88, v84, v85, v91\n"
"v_max3_u32 v88, v86, v87, v88\n"
"v_cmp_eq_u32 vcc, -1, v88\n"
"s_cbranch_vccnz .Ltp_44\n"
".Lou_45:\n"
"s_waitcnt lgkmcnt(0)\n"
"v_add_f32 v84, v84, v85\n"
"v_add_f32 v86, v86, v87\n"
"v_add_f32 v84, v84, v86\n"
"v_mul_f32 v89, 0x3acccccd, v91\n"
"v_fma_f32 v84, -v91, v89, v84\n"
"v_fmamk_f32 v84, v84, 0x3acccccd, v27\n"
"v_max_f32 v84, v84, v27\n"
"v_rsq_f32 v88, v84\n"
"s_nop 0\n"
"v_mul_f32 v89, -v89, v88\n"
"s_nop 1\n"
"s_branch .Lbk_43\n"
".Lsl_46:\n"
"s_nop 7\n"
"s_mov_b32 s17, 0x100000\n"
".Ltp_48:\n"
"ds_read_b128 v[84:87], v11 offset:640\n"
"ds_read_b32 v91, v12 offset:640\n"
"s_sub_u32 s17, s17, 1\n"
"s_cbranch_scc1 .Lou_49\n"
"s_waitcnt lgkmcnt(0)\n"
"v_max3_u32 v88, v84, v85, v91\n"
"v_max3_u32 v88, v86, v87, v88\n"
"v_cmp_eq_u32 vcc, -1, v88\n"
"s_cbranch_vccnz .Ltp_48\n"
".Lou_49:\n"
"s_waitcnt lgkmcnt(0)\n"
"v_add_f32 v84, v84, v85\n"
"v_add_f32 v86, v86, v87\n"
"v_add_f32 v84, v84, v86\n"
"v_mul_f32 v89, 0x3acccccd, v91\n"
"v_fma_f32 v84, -v91, v89, v84\n"
"v_fmamk_f32 v84, v84, 0x3acccccd, v27\n"
"v_max_f32 v84, v84, v27\n"
"v_rsq_f32 v88, v84\n"
"s_nop 0\n"
"v_mul_f32 v89, -v89, v88\n"
"s_nop 1\n"
"s_branch .Lbk_47\n"
".Lsl_50:\n"
"s_nop 7\n"
"s_mov_b32 s17, 0x100000\n"
".Ltp_52:\n"
"ds_read_b128 v[84:87], v11 offset:960\n"
"ds_read_b32 v91, v12 offset:960\n"
"s_sub_u32 s17, s17, 1\n"
"s_cbranch_scc1 .Lou_53\n"
"s_waitcnt lgkmcnt(0)\n"
"v_max3_u32 v88, v84, v85, v91\n"
"v_max3_u32 v88, v86, v87, v88\n"
"v_cmp_eq_u32 vcc, -1, v88\n"
"s_cbranch_vccnz .Ltp_52\n"
".Lou_53:\n"
"s_waitcnt lgkmcnt(0)\n"
"v_add_f32 v84, v84, v85\n"
"v_add_f32 v86, v86, v87\n"
"v_add_f32 v84, v84, v86\n"
"v_mul_f32 v89, 0x3acccccd, v91\n"
"v_fma_f32 v84, -v91, v89, v84\n"
"v_fmamk_f32 v84, v84, 0x3acccccd, v27\n"
"v_max_f32 v84, v84, v27\n"
"v_rsq_f32 v88, v84\n"
"s_nop 0\n"
"v_mul_f32 v89, -v89, v88\n"
"s_nop 1\n"
"s_branch .Lbk_51\n"
".Lsl_54:\n"
"s_nop 7\n"
"s_mov_b32 s17, 0x100000\n"
".Ltp_56:\n"
"ds_read_b128 v[44:47], v11 offset:1280\n"
"ds_read_b128 v[48:51], v11 offset:1600\n"
"s_sub_u32 s17, s17, 1\n"
"s_cbranch_scc1 .Lou_57\n"
"s_waitcnt lgkmcnt(0)\n"
"v_max3_u32 v52, v44, v45, v51\n"
"v_max3_u32 v52, v46, v47, v52\n"
"v_max3_u32 v52, v48, v49, v52\n"
"v_max3_u32 v52, v50, v51, v52\n"
"v_cmp_eq_u32 vcc, -1, v52\n"
"s_cbranch_vccnz .Ltp_56\n"
".Lou_57:\n"
"s_waitcnt lgkmcnt(0)\n"
"v_add_f32 v44, v44, v45\n"
"v_add_f32 v46, v46, v47\n"
"v_add_f32 v48, v48, v49\n"
"v_add_f32 v50, v50, v51\n"
"v_add_f32 v48, v48, v50\n"
"v_add_f32 v44, v44, v46\n"
"v_mul_f32 v53, 0x3acccccd, v48\n"
"v_fma_f32 v44, -v48, v53, v44\n"
"v_fmamk_f32 v44, v44, 0x3acccccd, v27\n"
"v_max_f32 v44, v44, v27\n"
"v_rsq_f32 v52, v44\n"
"s_nop 0\n"
"v_mul_f32 v53, -v53, v52\n"
"s_nop 1\n"
"s_branch .Lbk_55\n"
".Lsl_50c:\n"
"s_nop 7\n"
"s_mov_b32 s17, 0x100000\n"
".Ltp_52c:\n"
"ds_read_b128 v[84:87], v11 offset:960\n"
"ds_read_b32 v91, v12 offset:960\n"
"s_sub_u32 s17, s17, 1\n"
"s_cbranch_scc1 .Lou_53c\n"
"s_waitcnt lgkmcnt(0)\n"
"v_max3_u32 v88, v84, v85, v91\n"
"v_max3_u32 v88, v86, v87, v88\n"
"v_cmp_eq_u32 vcc, -1, v88\n"
"s_cbranch_vccnz .Ltp_52c\n"
".Lou_53c:\n"
"s_waitcnt lgkmcnt(0)\n"
"v_add_f32 v84, v84, v85\n"
"v_add_f32 v86, v86, v87\n"
"v_add_f32 v84, v84, v86\n"
"v_mul_f32 v89, 0x3acccccd, v91\n"
"v_fma_f32 v84, -v91, v89, v84\n"
"v_fmamk_f32 v84, v84, 0x3acccccd, v27\n"
"v_max_f32 v84, v84, v27\n"
"v_rsq_f32 v88, v84\n"
"s_nop 0\n"
"v_mul_f32 v89, -v89, v88\n"
"s_nop 1\n"
"s_branch .Lbk_51c\n"
".Lsl_54c:\n"
"s_nop 7\n"
"s_mov_b32 s17, 0x100000\n"
".Ltp_56c:\n"
"ds_read_b128 v[44:47], v11 offset:1280\n"
"ds_read_b128 v[48:51], v11 offset:1600\n"
"s_sub_u32 s17, s17, 1\n"
"s_cbranch_scc1 .Lou_57c\n"
"s_waitcnt lgkmcnt(0)\n"
"v_max3_u32 v52, v44, v45, v51\n"
"v_max3_u32 v52, v46, v47, v52\n"
"v_max3_u32 v52, v48, v49, v52\n"
"v_max3_u32 v52, v50, v51, v52\n"
"v_cmp_eq_u32 vcc, -1, v52\n"
"s_cbranch_vccnz .Ltp_56c\n"
".Lou_57c:\n"
"s_waitcnt lgkmcnt(0)\n"
"v_add_f32 v44, v44, v45\n"
"v_add_f32 v46, v46, v47\n"
"v_add_f32 v48, v48, v49\n"
"v_add_f32 v50, v50, v51\n"
"v_add_f32 v48, v48, v50\n"
"v_add_f32 v44, v44, v46\n"
"v_mul_f32 v53, 0x3acccccd, v48\n"
"v_fma_f32 v44, -v48, v53, v44\n"
"v_fmamk_f32 v44, v44, 0x3acccccd, v27\n"
"v_max_f32 v44, v44, v27\n"
"v_rsq_f32 v52, v44\n"
"s_nop 0\n"
"v_mul_f32 v53, -v53, v52\n"
"s_nop 1\n"
"s_branch .Lbk_55c\n"
".Lsl_128:\n"
"s_nop 7\n"
"s_mov_b32 s17, 0x100000\n"
".Ltp_130:\n"
"ds_read_b32 v90, v14 offset:288\n"
"s_sub_u32 s17, s17, 1\n"
"s_cbranch_scc1 .Lou_131\n"
"s_waitcnt lgkmcnt(0)\n"
"v_cmp_eq_u32 vcc, 0, v90\n"
"s_cbranch_vccnz .Ltp_130\n"
".Lou_131:\n"
"s_waitcnt lgkmcnt(0)\n"
"s_branch .Lbk_129\n"
".Lsl_132:\n"
"s_nop 7\n"
"s_mov_b32 s17, 0x100000\n"
".Ltp_134:\n"
"ds_read_b128 v[84:87], v11 offset:1856\n"
"ds_read_b32 v91, v12 offset:1856\n"
"s_sub_u32 s17, s17, 1\n"
"s_cbranch_scc1 .Lou_135\n"
"s_waitcnt lgkmcnt(0)\n"
"v_max3_u32 v88, v84, v85, v91\n"
"v_max3_u32 v88, v86, v87, v88\n"
"v_cmp_eq_u32 vcc, -1, v88\n"
"s_cbranch_vccnz .Ltp_134\n"
".Lou_135:\n"
"s_waitcnt lgkmcnt(0)\n"
"v_add_f32 v84, v84, v85\n"
"v_add_f32 v86, v86, v87\n"
"v_add_f32 v84, v84, v86\n"
"v_mul_f32 v89, 0x3acccccd, v91\n"
"v_fma_f32 v84, -v91, v89, v84\n"
"v_fmamk_f32 v84, v84, 0x3acccccd, v27\n"
"v_add_f32 v92, v92, v84\n"
"v_max_f32 v84, v84, v27\n"
"v_rsq_f32 v88, v84\n"
"s_nop 0\n"
"v_mul_f32 v89, -v89, v88\n"
"s_nop 1\n"
"s_branch .Lbk_133\n"
".Lsl_136:\n"
"s_nop 7\n"
"s_mov_b32 s17, 0x100000\n"
".Ltp_138:\n"
"ds_read_b128 v[84:87], v11 offset:2176\n"
"ds_read_b32 v91, v12 offset:2176\n"
"s_sub_u32 s17, s17, 1\n"
"s_cbranch_scc1 .Lou_139\n"
"s_waitcnt lgkmcnt(0)\n"
"v_max3_u32 v88, v84, v85, v91\n"
"v_max3_u32 v88, v86, v87, v88\n"
"v_cmp_eq_u32 vcc, -1, v88\n"
"s_cbranch_vccnz .Ltp_138\n"
".Lou_139:\n"
"s_waitcnt lgkmcnt(0)\n"
"v_add_f32 v84, v84, v85\n"
"v_add_f32 v86, v86, v87\n"
"v_add_f32 v84, v84, v86\n"
"v_mul_f32 v89, 0x3acccccd, v91\n"
"v_fma_f32 v84, -v91, v89, v84\n"
"v_fmamk_f32 v84, v84, 0x3acccccd, v27\n"
"v_add_f32 v92, v92, v84\n"
"v_max_f32 v84, v84, v27\n"
"v_rsq_f32 v88, v84\n"
"s_nop 0\n"
"v_mul_f32 v89, -v89, v88\n"
"s_nop 1\n"
"s_branch .Lbk_137\n"
".Lsl_140:\n"
"s_nop 7\n"
"s_mov_b32 s17, 0x100000\n"
".Ltp_142:\n"
"ds_read_b128 v[84:87], v11 offset:2496\n"
"ds_read_b32 v91, v12 offset:2496\n"
"s_sub_u32 s17, s17, 1\n"
"s_cbranch_scc1 .Lou_143\n"
"s_waitcnt lgkmcnt(0)\n"
"v_max3_u32 v88, v84, v85, v91\n"
"v_max3_u32 v88, v86, v87, v88\n"
"v_cmp_eq_u32 vcc, -1, v88\n"
"s_cbranch_vccnz .Ltp_142\n"
".Lou_143:\n"
"s_waitcnt lgkmcnt(0)\n"
"v_add_f32 v84, v84, v85\n"
"v_add_f32 v86, v86, v87\n"
"v_add_f32 v84, v84, v86\n"
"v_mul_f32 v89, 0x3acccccd, v91\n"
"v_fma_f32 v84, -v91, v89, v84\n"
"v_fmamk_f32 v84, v84, 0x3acccccd, v27\n"
"v_add_f32 v92, v92, v84\n"
"v_max_f32 v84, v84, v27\n"
"v_rsq_f32 v88, v84\n"
"s_nop 0\n"
"v_mul_f32 v89, -v89, v88\n"
"s_nop 1\n"
"s_branch .Lbk_141\n"
".Lsl_144:\n"
"s_nop 7\n"
"s_mov_b32 s17, 0x100000\n"
".Ltp_146:\n"
"ds_read_b128 v[84:87], v11 offset:2816\n"
"ds_read_b32 v91, v12 offset:2816\n"
"s_sub_u32 s17, s17, 1\n"
"s_cbranch_scc1 .Lou_147\n"
"s_waitcnt lgkmcnt(0)\n"
"v_max3_u32 v88, v84, v85, v91\n"
"v_max3_u32 v88, v86, v87, v88\n"
"v_cmp_eq_u32 vcc, -1, v88\n"
"s_cbranch_vccnz .Ltp_146\n"
".Lou_147:\n"
"s_waitcnt lgkmcnt(0)\n"
"v_add_f32 v84, v84, v85\n"
"v_add_f32 v86, v86, v87\n"
"v_add_f32 v84, v84, v86\n"
"v_mul_f32 v89, 0x3acccccd, v91\n"
"v_fma_f32 v84, -v91, v89, v84\n"
"v_fmamk_f32 v84, v84, 0x3acccccd, v27\n"
"v_add_f32 v92, v92, v84\n"
"v_max_f32 v84, v84, v27\n"
"v_rsq_f32 v88, v84\n"
"s_nop 0\n"
"v_mul_f32 v89, -v89, v88\n"
"s_nop 1\n"
"s_branch .Lbk_145\n"
".Lsl_148:\n"
"s_nop 7\n"
"s_mov_b32 s17, 0x100000\n"
".Ltp_150:\n"
"ds_read_b128 v[44:47], v11 offset:3136\n"
"ds_read_b128 v[48:51], v11 offset:3456\n"
"s_sub_u32 s17, s17, 1\n"
"s_cbranch_scc1 .Lou_151\n"
"s_waitcnt lgkmcnt(0)\n"
"v_max3_u32 v52, v44, v45, v51\n"
"v_max3_u32 v52, v46, v47, v52\n"
"v_max3_u32 v52, v48, v49, v52\n"
"v_max3_u32 v52, v50, v51, v52\n"
"v_cmp_eq_u32 vcc, -1, v52\n"
"s_cbranch_vccnz .Ltp_150\n"
".Lou_151:\n"
"s_waitcnt lgkmcnt(0)\n"
"v_add_f32 v44, v44, v45\n"
"v_add_f32 v46, v46, v47\n"
"v_add_f32 v48, v48, v49\n"
"v_add_f32 v50, v50, v51\n"
"v_add_f32 v48, v48, v50\n"
"v_add_f32 v44, v44, v46\n"
"v_mul_f32 v53, 0x3acccccd, v48\n"
"v_fma_f32 v44, -v48, v53, v44\n"
"v_fmamk_f32 v44, v44, 0x3acccccd, v27\n"
"v_add_f32 v92, v92, v44\n"
"v_max_f32 v44, v44, v27\n"
"v_rsq_f32 v52, v44\n"
"s_nop 0\n"
"v_mul_f32 v53, -v53, v52\n"
"s_nop 1\n"
"s_branch .Lbk_149\n"
".Lsl_144c:\n"
"s_nop 7\n"
"s_mov_b32 s17, 0x100000\n"
".Ltp_146c:\n"
"ds_read_b128 v[84:87], v11 offset:2816\n"
"ds_read_b32 v91, v12 offset:2816\n"
"s_sub_u32 s17, s17, 1\n"
"s_cbranch_scc1 .Lou_147c\n"
"s_waitcnt lgkmcnt(0)\n"
"v_max3_u32 v88, v84, v85, v91\n"
"v_max3_u32 v88, v86, v87, v88\n"
"v_cmp_eq_u32 vcc, -1, v88\n"
"s_cbranch_vccnz .Ltp_146c\n"
".Lou_147c:\n"
"s_waitcnt lgkmcnt(0)\n"
"v_add_f32 v84, v84, v85\n"
"v_add_f32 v86, v86, v87\n"
"v_add_f32 v84, v84, v86\n"
"v_mul_f32 v89, 0x3acccccd, v91\n"
"v_fma_f32 v84, -v91, v89, v84\n"
"v_fmamk_f32 v84, v84, 0x3acccccd, v27\n"
"v_add_f32 v92, v92, v84\n"
"v_max_f32 v84, v84, v27\n"
"v_rsq_f32 v88, v84\n"
"s_nop 0\n"
"v_mul_f32 v89, -v89, v88\n"
"s_nop 1\n"
"s_branch .Lbk_145c\n"
".Lsl_148c:\n"
"s_nop 7\n"
"s_mov_b32 s17, 0x100000\n"
".Ltp_150c:\n"
"ds_read_b128 v[44:47], v11 offset:3136\n"
"ds_read_b128 v[48:51], v11 offset:3456\n"
"s_sub_u32 s17, s17, 1\n"
"s_cbranch_scc1 .Lou_151c\n"
"s_waitcnt lgkmcnt(0)\n"
"v_max3_u32 v52, v44, v45, v51\n"
"v_max3_u32 v52, v46, v47, v52\n"
"v_max3_u32 v52, v48, v49, v52\n"
"v_max3_u32 v52, v50, v51, v52\n"
"v_cmp_eq_u32 vcc, -1, v52\n"
"s_cbranch_vccnz .Ltp_150c\n"
".Lou_151c:\n"
"s_waitcnt lgkmcnt(0)\n"
"v_add_f32 v44, v44, v45\n"
"v_add_f32 v46, v46, v47\n"
"v_add_f32 v48, v48, v49\n"
"v_add_f32 v50, v50, v51\n"
"v_add_f32 v48, v48, v50\n"
"v_add_f32 v44, v44, v46\n"
"v_mul_f32 v53, 0x3acccccd, v48\n"
"v_fma_f32 v44, -v48, v53, v44\n"
"v_fmamk_f32 v44, v44, 0x3acccccd, v27\n"
"v_add_f32 v92, v92, v44\n"
"v_max_f32 v44, v44, v27\n"
"v_rsq_f32 v52, v44\n"
"s_nop 0\n"
"v_mul_f32 v53, -v53, v52\n"
"s_nop 1\n"
"s_branch .Lbk_149c\n"
".Lrd_118:\n"
"s_nop 7\n"
"s_cmp_eq_u32 s54, 0\n"
"s_cbranch_scc1 .Lfi_154\n"
"s_cmp_lg_u32 s3, 3\n"
"s_cbranch_scc1 .Lgo_117c\n"
"s_branch .Lgo_117\n"
".Lfi_154:\n"
"s_mov_b32 s54, 1\n"
"s_sub_u32 s79, s44, 0x5000\n"
"s_add_u32 s78, s29, 0xc28000\n"
"s_max_i32 s46, s79, s78\n"
"s_add_u32 s47, s46, 0x1000\n"
"buffer_load_dwordx4 v[28:31], v1, s[4:7], s46 offen offset:0 sc1\n"
"buffer_load_dwordx4 v[32:35], v1, s[4:7], s46 offen offset:1024 sc1\n"
"buffer_load_dwordx4 v[36:39], v1, s[4:7], s46 offen offset:2048 sc1\n"
"buffer_load_dwordx4 v[40:43], v1, s[4:7], s46 offen offset:3072 sc1\n"
"buffer_load_dwordx4 v[44:47], v1, s[4:7], s47 offen offset:0 sc1\n"
"s_mov_b32 s17, 32768\n"
".Ltp_155:\n"
"s_waitcnt vmcnt(0)\n"
"s_sub_u32 s17, s17, 1\n"
"s_cbranch_scc1 .Lou_156\n"
"v_max_u32 v48, v28, v29\n"
"v_max3_u32 v48, v30, v31, v48\n"
"v_max3_u32 v48, v32, v33, v48\n"
"v_max3_u32 v48, v34, v35, v48\n"
"v_max3_u32 v48, v36, v37, v48\n"
"v_max3_u32 v48, v38, v39, v48\n"
"v_max3_u32 v48, v40, v41, v48\n"
"v_max3_u32 v48, v42, v43, v48\n"
"v_max3_u32 v48, v44, v45, v48\n"
"v_max3_u32 v48, v46, v47, v48\n"
"v_cmp_eq_u32 vcc, -1, v48\n"
"s_cbranch_vccz .Lou_156\n"
"v_max_u32 v48, v30, v31\n"
"v_max3_u32 v48, v28, v29, v48\n"
"v_cmp_eq_u32 vcc, -1, v48\n"
"s_cbranch_vccz .Lsk_157\n"
"s_mov_b64 exec, vcc\n"
"buffer_load_dwordx4 v[28:31], v1, s[4:7], s46 offen offset:0 sc1\n"
"s_mov_b64 exec, -1\n"
".Lsk_157:\n"
"v_max_u32 v48, v34, v35\n"
"v_max3_u32 v48, v32, v33, v48\n"
"v_cmp_eq_u32 vcc, -1, v48\n"
"s_cbranch_vccz .Lsk_158\n"
"s_mov_b64 exec, vcc\n"
"buffer_load_dwordx4 v[32:35], v1, s[4:7], s46 offen offset:1024 sc1\n"
"s_mov_b64 exec, -1\n"
".Lsk_158:\n"
"v_max_u32 v48, v38, v39\n"
"v_max3_u32 v48, v36, v37, v48\n"
"v_cmp_eq_u32 vcc, -1, v48\n"
"s_cbranch_vccz .Lsk_159\n"
"s_mov_b64 exec, vcc\n"
"buffer_load_dwordx4 v[36:39], v1, s[4:7], s46 offen offset:2048 sc1\n"
"s_mov_b64 exec, -1\n"
".Lsk_159:\n"
"v_max_u32 v48, v42, v43\n"
"v_max3_u32 v48, v40, v41, v48\n"
"v_cmp_eq_u32 vcc, -1, v48\n"
"s_cbranch_vccz .Lsk_160\n"
"s_mov_b64 exec, vcc\n"
"buffer_load_dwordx4 v[40:43], v1, s[4:7], s46 offen offset:3072 sc1\n"
"s_mov_b64 exec, -1\n"
".Lsk_160:\n"
"v_max_u32 v48, v46, v47\n"
"v_max3_u32 v48, v44, v45, v48\n"
"v_cmp_eq_u32 vcc, -1, v48\n"
"s_cbranch_vccz .Lsk_161\n"
"s_mov_b64 exec, vcc\n"
"buffer_load_dwordx4 v[44:47], v1, s[4:7], s47 offen offset:0 sc1\n"
"s_mov_b64 exec, -1\n"
".Lsk_161:\n"
"s_branch .Ltp_155\n"
".Lou_156:\n"
"s_waitcnt vmcnt(0)\n"
"s_mov_b32 s46, s44\n"
"s_add_u32 s47, s46, 0x1000\n"
"buffer_load_dwordx4 v[28:31], v1, s[4:7], s46 offen offset:0 sc1\n"
"buffer_load_dwordx4 v[32:35], v1, s[4:7], s46 offen offset:1024 sc1\n"
"buffer_load_dwordx4 v[36:39], v1, s[4:7], s46 offen offset:2048 sc1\n"
"buffer_load_dwordx4 v[40:43], v1, s[4:7], s46 offen offset:3072 sc1\n"
"buffer_load_dwordx4 v[44:47], v1, s[4:7], s47 offen offset:0 sc1\n"
"s_mov_b32 s17, 32768\n"
".Ltp_162:\n"
"s_waitcnt vmcnt(0)\n"
"s_sub_u32 s17, s17, 1\n"
"s_cbranch_scc1 .Lou_163\n"
"v_max_u32 v48, v28, v29\n"
"v_max3_u32 v48, v30, v31, v48\n"
"v_max3_u32 v48, v32, v33, v48\n"
"v_max3_u32 v48, v34, v35, v48\n"
"v_max3_u32 v48, v36, v37, v48\n"
"v_max3_u32 v48, v38, v39, v48\n"
"v_max3_u32 v48, v40, v41, v48\n"
"v_max3_u32 v48, v42, v43, v48\n"
"v_max3_u32 v48, v44, v45, v48\n"
"v_max3_u32 v48, v46, v47, v48\n"
"v_cmp_eq_u32 vcc, -1, v48\n"
"s_cbranch_vccz .Lou_163\n"
"v_max_u32 v48, v30, v31\n"
"v_max3_u32 v48, v28, v29, v48\n"
"v_cmp_eq_u32 vcc, -1, v48\n"
"s_cbranch_vccz .Lsk_164\n"
"s_mov_b64 exec, vcc\n"
"buffer_load_dwordx4 v[28:31], v1, s[4:7], s46 offen offset:0 sc1\n"
"s_mov_b64 exec, -1\n"
".Lsk_164:\n"
"v_max_u32 v48, v34, v35\n"
"v_max3_u32 v48, v32, v33, v48\n"
"v_cmp_eq_u32 vcc, -1, v48\n"
"s_cbranch_vccz .Lsk_165\n"
"s_mov_b64 exec, vcc\n"
"buffer_load_dwordx4 v[32:35], v1, s[4:7], s46 offen offset:1024 sc1\n"
"s_mov_b64 exec, -1\n"
".Lsk_165:\n"
"v_max_u32 v48, v38, v39\n"
"v_max3_u32 v48, v36, v37, v48\n"
"v_cmp_eq_u32 vcc, -1, v48\n"
"s_cbranch_vccz .Lsk_166\n"
"s_mov_b64 exec, vcc\n"
"buffer_load_dwordx4 v[36:39], v1, s[4:7], s46 offen offset:2048 sc1\n"
"s_mov_b64 exec, -1\n"
".Lsk_166:\n"
"v_max_u32 v48, v42, v43\n"
"v_max3_u32 v48, v40, v41, v48\n"
"v_cmp_eq_u32 vcc, -1, v48\n"
"s_cbranch_vccz .Lsk_167\n"
"s_mov_b64 exec, vcc\n"
"buffer_load_dwordx4 v[40:43], v1, s[4:7], s46 offen offset:3072 sc1\n"
"s_mov_b64 exec, -1\n"
".Lsk_167:\n"
"v_max_u32 v48, v46, v47\n"
"v_max3_u32 v48, v44, v45, v48\n"
"v_cmp_eq_u32 vcc, -1, v48\n"
"s_cbranch_vccz .Lsk_168\n"
"s_mov_b64 exec, vcc\n"
"buffer_load_dwordx4 v[44:47], v1, s[4:7], s47 offen offset:0 sc1\n"
"s_mov_b64 exec, -1\n"
".Lsk_168:\n"
"s_branch .Ltp_162\n"
".Lou_163:\n"
"s_waitcnt vmcnt(0)\n"
"s_add_u32 s46, s44, 0x5000\n"
"s_add_u32 s47, s46, 0x1000\n"
"buffer_load_dwordx4 v[28:31], v1, s[4:7], s46 offen offset:0 sc1\n"
"buffer_load_dwordx4 v[32:35], v1, s[4:7], s46 offen offset:1024 sc1\n"
"buffer_load_dwordx4 v[36:39], v1, s[4:7], s46 offen offset:2048 sc1\n"
"buffer_load_dwordx4 v[40:43], v1, s[4:7], s46 offen offset:3072 sc1\n"
"buffer_load_dwordx4 v[44:47], v1, s[4:7], s47 offen offset:0 sc1\n"
"s_mov_b32 s17, 32768\n"
".Ltp_169:\n"
"s_waitcnt vmcnt(0)\n"
"s_sub_u32 s17, s17, 1\n"
"s_cbranch_scc1 .Lou_170\n"
"v_max_u32 v48, v28, v29\n"
"v_max3_u32 v48, v30, v31, v48\n"
"v_max3_u32 v48, v32, v33, v48\n"
"v_max3_u32 v48, v34, v35, v48\n"
"v_max3_u32 v48, v36, v37, v48\n"
"v_max3_u32 v48, v38, v39, v48\n"
"v_max3_u32 v48, v40, v41, v48\n"
"v_max3_u32 v48, v42, v43, v48\n"
"v_max3_u32 v48, v44, v45, v48\n"
"v_max3_u32 v48, v46, v47, v48\n"
"v_cmp_eq_u32 vcc, -1, v48\n"
"s_cbranch_vccz .Lou_170\n"
"v_max_u32 v48, v30, v31\n"
"v_max3_u32 v48, v28, v29, v48\n"
"v_cmp_eq_u32 vcc, -1, v48\n"
"s_cbranch_vccz .Lsk_171\n"
"s_mov_b64 exec, vcc\n"
"buffer_load_dwordx4 v[28:31], v1, s[4:7], s46 offen offset:0 sc1\n"
"s_mov_b64 exec, -1\n"
".Lsk_171:\n"
"v_max_u32 v48, v34, v35\n"
"v_max3_u32 v48, v32, v33, v48\n"
"v_cmp_eq_u32 vcc, -1, v48\n"
"s_cbranch_vccz .Lsk_172\n"
"s_mov_b64 exec, vcc\n"
"buffer_load_dwordx4 v[32:35], v1, s[4:7], s46 offen offset:1024 sc1\n"
"s_mov_b64 exec, -1\n"
".Lsk_172:\n"
"v_max_u32 v48, v38, v39\n"
"v_max3_u32 v48, v36, v37, v48\n"
"v_cmp_eq_u32 vcc, -1, v48\n"
"s_cbranch_vccz .Lsk_173\n"
"s_mov_b64 exec, vcc\n"
"buffer_load_dwordx4 v[36:39], v1, s[4:7], s46 offen offset:2048 sc1\n"
"s_mov_b64 exec, -1\n"
".Lsk_173:\n"
"v_max_u32 v48, v42, v43\n"
"v_max3_u32 v48, v40, v41, v48\n"
"v_cmp_eq_u32 vcc, -1, v48\n"
"s_cbranch_vccz .Lsk_174\n"
"s_mov_b64 exec, vcc\n"
"buffer_load_dwordx4 v[40:43], v1, s[4:7], s46 offen offset:3072 sc1\n"
"s_mov_b64 exec, -1\n"
".Lsk_174:\n"
"v_max_u32 v48, v46, v47\n"
"v_max3_u32 v48, v44, v45, v48\n"
"v_cmp_eq_u32 vcc, -1, v48\n"
"s_cbranch_vccz .Lsk_175\n"
"s_mov_b64 exec, vcc\n"
"buffer_load_dwordx4 v[44:47], v1, s[4:7], s47 offen offset:0 sc1\n"
"s_mov_b64 exec, -1\n"
".Lsk_175:\n"
"s_branch .Ltp_169\n"
".Lou_170:\n"
"s_waitcnt vmcnt(0)\n"
"s_add_u32 s46, s44, 0xa000\n"
"s_add_u32 s47, s46, 0x1000\n"
"buffer_load_dwordx4 v[28:31], v1, s[4:7], s46 offen offset:0 sc1\n"
"buffer_load_dwordx4 v[32:35], v1, s[4:7], s46 offen offset:1024 sc1\n"
"buffer_load_dwordx4 v[36:39], v1, s[4:7], s46 offen offset:2048 sc1\n"
"buffer_load_dwordx4 v[40:43], v1, s[4:7], s46 offen offset:3072 sc1\n"
"buffer_load_dwordx4 v[44:47], v1, s[4:7], s47 offen offset:0 sc1\n"
"s_mov_b32 s17, 32768\n"
".Ltp_176:\n"
"s_waitcnt vmcnt(0)\n"
"s_sub_u32 s17, s17, 1\n"
"s_cbranch_scc1 .Lou_177\n"
"v_max_u32 v48, v28, v29\n"
"v_max3_u32 v48, v30, v31, v48\n"
"v_max3_u32 v48, v32, v33, v48\n"
"v_max3_u32 v48, v34, v35, v48\n"
"v_max3_u32 v48, v36, v37, v48\n"
"v_max3_u32 v48, v38, v39, v48\n"
"v_max3_u32 v48, v40, v41, v48\n"
"v_max3_u32 v48, v42, v43, v48\n"
"v_max3_u32 v48, v44, v45, v48\n"
"v_max3_u32 v48, v46, v47, v48\n"
"v_cmp_eq_u32 vcc, -1, v48\n"
"s_cbranch_vccz .Lou_177\n"
"v_max_u32 v48, v30, v31\n"
"v_max3_u32 v48, v28, v29, v48\n"
"v_cmp_eq_u32 vcc, -1, v48\n"
"s_cbranch_vccz .Lsk_178\n"
"s_mov_b64 exec, vcc\n"
"buffer_load_dwordx4 v[28:31], v1, s[4:7], s46 offen offset:0 sc1\n"
"s_mov_b64 exec, -1\n"
".Lsk_178:\n"
"v_max_u32 v48, v34, v35\n"
"v_max3_u32 v48, v32, v33, v48\n"
"v_cmp_eq_u32 vcc, -1, v48\n"
"s_cbranch_vccz .Lsk_179\n"
"s_mov_b64 exec, vcc\n"
"buffer_load_dwordx4 v[32:35], v1, s[4:7], s46 offen offset:1024 sc1\n"
"s_mov_b64 exec, -1\n"
".Lsk_179:\n"
"v_max_u32 v48, v38, v39\n"
"v_max3_u32 v48, v36, v37, v48\n"
"v_cmp_eq_u32 vcc, -1, v48\n"
"s_cbranch_vccz .Lsk_180\n"
"s_mov_b64 exec, vcc\n"
"buffer_load_dwordx4 v[36:39], v1, s[4:7], s46 offen offset:2048 sc1\n"
"s_mov_b64 exec, -1\n"
".Lsk_180:\n"
"v_max_u32 v48, v42, v43\n"
"v_max3_u32 v48, v40, v41, v48\n"
"v_cmp_eq_u32 vcc, -1, v48\n"
"s_cbranch_vccz .Lsk_181\n"
"s_mov_b64 exec, vcc\n"
"buffer_load_dwordx4 v[40:43], v1, s[4:7], s46 offen offset:3072 sc1\n"
"s_mov_b64 exec, -1\n"
".Lsk_181:\n"
"v_max_u32 v48, v46, v47\n"
"v_max3_u32 v48, v44, v45, v48\n"
"v_cmp_eq_u32 vcc, -1, v48\n"
"s_cbranch_vccz .Lsk_182\n"
"s_mov_b64 exec, vcc\n"
"buffer_load_dwordx4 v[44:47], v1, s[4:7], s47 offen offset:0 sc1\n"
"s_mov_b64 exec, -1\n"
".Lsk_182:\n"
"s_branch .Ltp_176\n"
".Lou_177:\n"
"s_waitcnt vmcnt(0)\n"
"s_add_u32 s46, s44, 0xf000\n"
"s_add_u32 s47, s46, 0x1000\n"
"buffer_load_dwordx4 v[28:31], v1, s[4:7], s46 offen offset:0 sc1\n"
"buffer_load_dwordx4 v[32:35], v1, s[4:7], s46 offen offset:1024 sc1\n"
"buffer_load_dwordx4 v[36:39], v1, s[4:7], s46 offen offset:2048 sc1\n"
"buffer_load_dwordx4 v[40:43], v1, s[4:7], s46 offen offset:3072 sc1\n"
"buffer_load_dwordx4 v[44:47], v1, s[4:7], s47 offen offset:0 sc1\n"
"s_mov_b32 s17, 32768\n"
".Ltp_183:\n"
"s_waitcnt vmcnt(0)\n"
"s_sub_u32 s17, s17, 1\n"
"s_cbranch_scc1 .Lou_184\n"
"v_max_u32 v48, v28, v29\n"
"v_max3_u32 v48, v30, v31, v48\n"
"v_max3_u32 v48, v32, v33, v48\n"
"v_max3_u32 v48, v34, v35, v48\n"
"v_max3_u32 v48, v36, v37, v48\n"
"v_max3_u32 v48, v38, v39, v48\n"
"v_max3_u32 v48, v40, v41, v48\n"
"v_max3_u32 v48, v42, v43, v48\n"
"v_max3_u32 v48, v44, v45, v48\n"
"v_max3_u32 v48, v46, v47, v48\n"
"v_cmp_eq_u32 vcc, -1, v48\n"
"s_cbranch_vccz .Lou_184\n"
"v_max_u32 v48, v30, v31\n"
"v_max3_u32 v48, v28, v29, v48\n"
"v_cmp_eq_u32 vcc, -1, v48\n"
"s_cbranch_vccz .Lsk_185\n"
"s_mov_b64 exec, vcc\n"
"buffer_load_dwordx4 v[28:31], v1, s[4:7], s46 offen offset:0 sc1\n"
"s_mov_b64 exec, -1\n"
".Lsk_185:\n"
"v_max_u32 v48, v34, v35\n"
"v_max3_u32 v48, v32, v33, v48\n"
"v_cmp_eq_u32 vcc, -1, v48\n"
"s_cbranch_vccz .Lsk_186\n"
"s_mov_b64 exec, vcc\n"
"buffer_load_dwordx4 v[32:35], v1, s[4:7], s46 offen offset:1024 sc1\n"
"s_mov_b64 exec, -1\n"
".Lsk_186:\n"
"v_max_u32 v48, v38, v39\n"
"v_max3_u32 v48, v36, v37, v48\n"
"v_cmp_eq_u32 vcc, -1, v48\n"
"s_cbranch_vccz .Lsk_187\n"
"s_mov_b64 exec, vcc\n"
"buffer_load_dwordx4 v[36:39], v1, s[4:7], s46 offen offset:2048 sc1\n"
"s_mov_b64 exec, -1\n"
".Lsk_187:\n"
"v_max_u32 v48, v42, v43\n"
"v_max3_u32 v48, v40, v41, v48\n"
"v_cmp_eq_u32 vcc, -1, v48\n"
"s_cbranch_vccz .Lsk_188\n"
"s_mov_b64 exec, vcc\n"
"buffer_load_dwordx4 v[40:43], v1, s[4:7], s46 offen offset:3072 sc1\n"
"s_mov_b64 exec, -1\n"
".Lsk_188:\n"
"v_max_u32 v48, v46, v47\n"
"v_max3_u32 v48, v44, v45, v48\n"
"v_cmp_eq_u32 vcc, -1, v48\n"
"s_cbranch_vccz .Lsk_189\n"
"s_mov_b64 exec, vcc\n"
"buffer_load_dwordx4 v[44:47], v1, s[4:7], s47 offen offset:0 sc1\n"
"s_mov_b64 exec, -1\n"
".Lsk_189:\n"
"s_branch .Ltp_183\n"
".Lou_184:\n"
"s_waitcnt vmcnt(0)\n"
"s_mov_b32 s17, 0x100000\n"
".Ltp_190:\n"
"ds_read_b32 v90, v14 offset:320\n"
"s_sub_u32 s17, s17, 1\n"
"s_cbranch_scc1 .Lou_191\n"
"s_waitcnt lgkmcnt(0)\n"
"v_cmp_eq_u32 vcc, 0, v90\n"
"s_cbranch_vccnz .Ltp_190\n"
".Lou_191:\n"
"s_waitcnt lgkmcnt(0)\n"
"ds_write_b32 v13, v13 offset:336\n"
"s_mov_b32 s17, 0x100000\n"
".Ltp_192:\n"
"ds_read_b32 v90, v14 offset:336\n"
"s_sub_u32 s17, s17, 1\n"
"s_cbranch_scc1 .Lou_193\n"
"s_waitcnt lgkmcnt(0)\n"
"v_cmp_eq_u32 vcc, 0, v90\n"
"s_cbranch_vccnz .Ltp_192\n"
".Lou_193:\n"
"s_waitcnt lgkmcnt(0)\n"
"v_mov_b32 v90, 0\n"
"ds_write_b32 v13, v90 offset:272\n"
"ds_write_b32 v13, v90 offset:288\n"
"v_mov_b32 v90, -1\n"
"ds_write_b32 v9, v90 offset:1856\n"
"ds_write_b32 v9, v90 offset:2176\n"
"ds_write_b32 v9, v90 offset:2496\n"
"ds_write_b32 v9, v90 offset:2816\n"
"ds_write_b32 v9, v90 offset:3136\n"
"ds_write_b32 v10, v90 offset:3456\n"
"ds_write_b32 v13, v13 offset:352\n"
"s_mov_b32 s17, 0x100000\n"
".Ltp_194:\n"
"ds_read_b32 v90, v14 offset:352\n"
"s_sub_u32 s17, s17, 1\n"
"s_cbranch_scc1 .Lou_195\n"
"s_waitcnt lgkmcnt(0)\n"
"v_cmp_eq_u32 vcc, 0, v90\n"
"s_cbranch_vccnz .Ltp_194\n"
".Lou_195:\n"
"s_waitcnt lgkmcnt(0)\n"
"s_branch .Lag_116\n"
".Lfx_204:\n"
"s_nop 7\n"
"s_add_u32 m0, s29, 0xa000\n"
"s_add_u32 s31, s44, 0x140000\n"
"buffer_load_dwordx4 v1, s[4:7], s31 offen offset:0 sc1 lds\n"
"buffer_load_dwordx4 v1, s[4:7], s31 offen offset:1024 sc1 lds\n"
"buffer_load_dwordx4 v1, s[4:7], s31 offen offset:2048 sc1 lds\n"
"buffer_load_dwordx4 v1, s[4:7], s31 offen offset:3072 sc1 lds\n"
"s_add_u32 m0, m0, 1024\n"
"s_add_u32 s31, s31, 1024\n"
"buffer_load_dwordx4 v1, s[4:7], s31 offen offset:3072 sc1 lds\n"
"s_add_u32 m0, s29, 0xf000\n"
"s_add_u32 s31, s44, 0x145000\n"
"buffer_load_dwordx4 v1, s[4:7], s31 offen offset:0 sc1 lds\n"
"buffer_load_dwordx4 v1, s[4:7], s31 offen offset:1024 sc1 lds\n"
"buffer_load_dwordx4 v1, s[4:7], s31 offen offset:2048 sc1 lds\n"
"buffer_load_dwordx4 v1, s[4:7], s31 offen offset:3072 sc1 lds\n"
"s_add_u32 m0, m0, 1024\n"
"s_add_u32 s31, s31, 1024\n"
"buffer_load_dwordx4 v1, s[4:7], s31 offen offset:3072 sc1 lds\n"
"s_waitcnt vmcnt(0)\n"
"ds_write_b32 v13, v13 offset:384\n"
"s_mov_b32 s17, 0x100000\n"
".Ltp_206:\n"
"ds_read_b32 v90, v14 offset:384\n"
"s_sub_u32 s17, s17, 1\n"
"s_cbranch_scc1 .Lou_207\n"
"s_waitcnt lgkmcnt(0)\n"
"v_cmp_eq_u32 vcc, 0, v90\n"
"s_cbranch_vccnz .Ltp_206\n"
".Lou_207:\n"
"s_waitcnt lgkmcnt(0)\n"
"v_mov_b32 v90, 0\n"
"s_branch .Lbk_205\n"
".Lsl_208:\n"
"s_nop 7\n"
"s_mov_b32 s17, 0x100000\n"
".Ltp_210:\n"
"ds_read_b32 v90, v14 offset:400\n"
"s_sub_u32 s17, s17, 1\n"
"s_cbranch_scc1 .Lou_211\n"
"s_waitcnt lgkmcnt(0)\n"
"v_cmp_eq_u32 vcc, 0, v90\n"
"s_cbranch_vccnz .Ltp_210\n"
".Lou_211:\n"
"s_waitcnt lgkmcnt(0)\n"
"s_branch .Lbk_209\n"
".Lsl_212:\n"
"s_nop 7\n"
"s_mov_b32 s17, 0x100000\n"
".Ltp_214:\n"
"ds_read_b32 v90, v14 offset:416\n"
"s_sub_u32 s17, s17, 1\n"
"s_cbranch_scc1 .Lou_215\n"
"s_waitcnt lgkmcnt(0)\n"
"v_cmp_eq_u32 vcc, 0, v90\n"
"s_cbranch_vccnz .Ltp_214\n"
".Lou_215:\n"
"s_waitcnt lgkmcnt(0)\n"
"s_branch .Lbk_213\n"
".Lsl_216:\n"
"s_nop 7\n"
"s_mov_b32 s17, 0x100000\n"
".Ltp_218:\n"
"ds_read_b128 v[84:87], v11 offset:3712\n"
"ds_read_b32 v91, v12 offset:3712\n"
"s_sub_u32 s17, s17, 1\n"
"s_cbranch_scc1 .Lou_219\n"
"s_waitcnt lgkmcnt(0)\n"
"v_max3_u32 v88, v84, v85, v91\n"
"v_max3_u32 v88, v86, v87, v88\n"
"v_cmp_eq_u32 vcc, -1, v88\n"
"s_cbranch_vccnz .Ltp_218\n"
".Lou_219:\n"
"s_waitcnt lgkmcnt(0)\n"
"v_add_f32 v84, v84, v85\n"
"v_add_f32 v86, v86, v87\n"
"v_add_f32 v84, v84, v86\n"
"v_mul_f32 v89, 0x3acccccd, v91\n"
"v_fma_f32 v84, -v91, v89, v84\n"
"v_fmamk_f32 v84, v84, 0x3acccccd, v27\n"
"v_add_f32 v92, v92, v84\n"
"v_max_f32 v84, v84, v27\n"
"v_rsq_f32 v88, v84\n"
"s_nop 0\n"
"v_mul_f32 v89, -v89, v88\n"
"s_nop 1\n"
"s_branch .Lbk_217\n"
".Lsl_220:\n"
"s_nop 7\n"
"s_mov_b32 s17, 0x100000\n"
".Ltp_222:\n"
"ds_read_b128 v[84:87], v11 offset:4032\n"
"ds_read_b32 v91, v12 offset:4032\n"
"s_sub_u32 s17, s17, 1\n"
"s_cbranch_scc1 .Lou_223\n"
"s_waitcnt lgkmcnt(0)\n"
"v_max3_u32 v88, v84, v85, v91\n"
"v_max3_u32 v88, v86, v87, v88\n"
"v_cmp_eq_u32 vcc, -1, v88\n"
"s_cbranch_vccnz .Ltp_222\n"
".Lou_223:\n"
"s_waitcnt lgkmcnt(0)\n"
"v_add_f32 v84, v84, v85\n"
"v_add_f32 v86, v86, v87\n"
"v_add_f32 v84, v84, v86\n"
"v_mul_f32 v89, 0x3acccccd, v91\n"
"v_fma_f32 v84, -v91, v89, v84\n"
"v_fmamk_f32 v84, v84, 0x3acccccd, v27\n"
"v_add_f32 v92, v92, v84\n"
"v_max_f32 v84, v84, v27\n"
"v_rsq_f32 v88, v84\n"
"s_nop 0\n"
"v_mul_f32 v89, -v89, v88\n"
"s_nop 1\n"
"s_branch .Lbk_221\n"
".Lsl_224:\n"
"s_nop 7\n"
"s_mov_b32 s17, 0x100000\n"
".Ltp_226:\n"
"ds_read_b128 v[84:87], v11 offset:4352\n"
"ds_read_b32 v91, v12 offset:4352\n"
"s_sub_u32 s17, s17, 1\n"
"s_cbranch_scc1 .Lou_227\n"
"s_waitcnt lgkmcnt(0)\n"
"v_max3_u32 v88, v84, v85, v91\n"
"v_max3_u32 v88, v86, v87, v88\n"
"v_cmp_eq_u32 vcc, -1, v88\n"
"s_cbranch_vccnz .Ltp_226\n"
".Lou_227:\n"
"s_waitcnt lgkmcnt(0)\n"
"v_add_f32 v84, v84, v85\n"
"v_add_f32 v86, v86, v87\n"
"v_add_f32 v84, v84, v86\n"
"v_mul_f32 v89, 0x3acccccd, v91\n"
"v_fma_f32 v84, -v91, v89, v84\n"
"v_fmamk_f32 v84, v84, 0x3acccccd, v27\n"
"v_add_f32 v92, v92, v84\n"
"v_max_f32 v84, v84, v27\n"
"v_rsq_f32 v88, v84\n"
"s_nop 0\n"
"v_mul_f32 v89, -v89, v88\n"
"s_nop 1\n"
"s_branch .Lbk_225\n"
".Lsl_228:\n"
"s_nop 7\n"
"s_mov_b32 s17, 0x100000\n"
".Ltp_230:\n"
"ds_read_b128 v[84:87], v11 offset:4672\n"
"ds_read_b32 v91, v12 offset:4672\n"
"s_sub_u32 s17, s17, 1\n"
"s_cbranch_scc1 .Lou_231\n"
"s_waitcnt lgkmcnt(0)\n"
"v_max3_u32 v88, v84, v85, v91\n"
"v_max3_u32 v88, v86, v87, v88\n"
"v_cmp_eq_u32 vcc, -1, v88\n"
"s_cbranch_vccnz .Ltp_230\n"
".Lou_231:\n"
"s_waitcnt lgkmcnt(0)\n"
"v_add_f32 v84, v84, v85\n"
"v_add_f32 v86, v86, v87\n"
"v_add_f32 v84, v84, v86\n"
"v_mul_f32 v89, 0x3acccccd, v91\n"
"v_fma_f32 v84, -v91, v89, v84\n"
"v_fmamk_f32 v84, v84, 0x3acccccd, v27\n"
"v_add_f32 v92, v92, v84\n"
"v_max_f32 v84, v84, v27\n"
"v_rsq_f32 v88, v84\n"
"s_nop 0\n"
"v_mul_f32 v89, -v89, v88\n"
"s_nop 1\n"
"s_branch .Lbk_229\n"
".Lsl_232:\n"
"s_nop 7\n"
"s_mov_b32 s17, 0x100000\n"
".Ltp_234:\n"
"ds_read_b128 v[44:47], v11 offset:4992\n"
"ds_read_b128 v[48:51], v11 offset:5312\n"
"s_sub_u32 s17, s17, 1\n"
"s_cbranch_scc1 .Lou_235\n"
"s_waitcnt lgkmcnt(0)\n"
"v_max3_u32 v52, v44, v45, v51\n"
"v_max3_u32 v52, v46, v47, v52\n"
"v_max3_u32 v52, v48, v49, v52\n"
"v_max3_u32 v52, v50, v51, v52\n"
"v_cmp_eq_u32 vcc, -1, v52\n"
"s_cbranch_vccnz .Ltp_234\n"
".Lou_235:\n"
"s_waitcnt lgkmcnt(0)\n"
"v_add_f32 v44, v44, v45\n"
"v_add_f32 v46, v46, v47\n"
"v_add_f32 v48, v48, v49\n"
"v_add_f32 v50, v50, v51\n"
"v_add_f32 v48, v48, v50\n"
"v_add_f32 v44, v44, v46\n"
"v_mul_f32 v53, 0x3acccccd, v48\n"
"v_fma_f32 v44, -v48, v53, v44\n"
"v_fmamk_f32 v44, v44, 0x3acccccd, v27\n"
"v_add_f32 v92, v92, v44\n"
"v_max_f32 v44, v44, v27\n"
"v_rsq_f32 v52, v44\n"
"s_nop 0\n"
"v_mul_f32 v53, -v53, v52\n"
"s_nop 1\n"
"s_branch .Lbk_233\n"
".Lsl_228c:\n"
"s_nop 7\n"
"s_mov_b32 s17, 0x100000\n"
".Ltp_230c:\n"
"ds_read_b128 v[84:87], v11 offset:4672\n"
"ds_read_b32 v91, v12 offset:4672\n"
"s_sub_u32 s17, s17, 1\n"
"s_cbranch_scc1 .Lou_231c\n"
"s_waitcnt lgkmcnt(0)\n"
"v_max3_u32 v88, v84, v85, v91\n"
"v_max3_u32 v88, v86, v87, v88\n"
"v_cmp_eq_u32 vcc, -1, v88\n"
"s_cbranch_vccnz .Ltp_230c\n"
".Lou_231c:\n"
"s_waitcnt lgkmcnt(0)\n"
"v_add_f32 v84, v84, v85\n"
"v_add_f32 v86, v86, v87\n"
"v_add_f32 v84, v84, v86\n"
"v_mul_f32 v89, 0x3acccccd, v91\n"
"v_fma_f32 v84, -v91, v89, v84\n"
"v_fmamk_f32 v84, v84, 0x3acccccd, v27\n"
"v_add_f32 v92, v92, v84\n"
"v_max_f32 v84, v84, v27\n"
"v_rsq_f32 v88, v84\n"
"s_nop 0\n"
"v_mul_f32 v89, -v89, v88\n"
"s_nop 1\n"
"s_branch .Lbk_229c\n"
".Lsl_232c:\n"
"s_nop 7\n"
"s_mov_b32 s17, 0x100000\n"
".Ltp_234c:\n"
"ds_read_b128 v[44:47], v11 offset:4992\n"
"ds_read_b128 v[48:51], v11 offset:5312\n"
"s_sub_u32 s17, s17, 1\n"
"s_cbranch_scc1 .Lou_235c\n"
"s_waitcnt lgkmcnt(0)\n"
"v_max3_u32 v52, v44, v45, v51\n"
"v_max3_u32 v52, v46, v47, v52\n"
"v_max3_u32 v52, v48, v49, v52\n"
"v_max3_u32 v52, v50, v51, v52\n"
"v_cmp_eq_u32 vcc, -1, v52\n"
"s_cbranch_vccnz .Ltp_234c\n"
".Lou_235c:\n"
"s_waitcnt lgkmcnt(0)\n"
"v_add_f32 v44, v44, v45\n"
"v_add_f32 v46, v46, v47\n"
"v_add_f32 v48, v48, v49\n"
"v_add_f32 v50, v50, v51\n"
"v_add_f32 v48, v48, v50\n"
"v_add_f32 v44, v44, v46\n"
"v_mul_f32 v53, 0x3acccccd, v48\n"
"v_fma_f32 v44, -v48, v53, v44\n"
"v_fmamk_f32 v44, v44, 0x3acccccd, v27\n"
"v_add_f32 v92, v92, v44\n"
"v_max_f32 v44, v44, v27\n"
"v_rsq_f32 v52, v44\n"
"s_nop 0\n"
"v_mul_f32 v53, -v53, v52\n"
"s_nop 1\n"
"s_branch .Lbk_233c\n"
".Lsl_238:\n"
"s_nop 7\n"
"s_mov_b32 s17, 0x100000\n"
".Ltp_240:\n"
"ds_read_b32 v90, v14 offset:320\n"
"s_sub_u32 s17, s17, 1\n"
"s_cbranch_scc1 .Lou_241\n"
"s_waitcnt lgkmcnt(0)\n"
"v_cmp_eq_u32 vcc, 0, v90\n"
"s_cbranch_vccnz .Ltp_240\n"
".Lou_241:\n"
"ds_read_b64 v[56:57], v3 offset:40960\n"
"ds_read_b64 v[58:59], v3 offset:41216\n"
"ds_read_b64 v[60:61], v3 offset:41984\n"
"ds_read_b64 v[62:63], v3 offset:42240\n"
"ds_read_b64 v[64:65], v3 offset:43008\n"
"ds_read_b64 v[66:67], v3 offset:43264\n"
"ds_read_b64 v[68:69], v3 offset:44032\n"
"ds_read_b64 v[70:71], v3 offset:44288\n"
"ds_read_b64 v[72:73], v3 offset:45056\n"
"ds_read_b64 v[74:75], v3 offset:45312\n"
"s_waitcnt lgkmcnt(0)\n"
"s_branch .Lbk_239\n"
".Lrd_203:\n"
"s_nop 7\n"
"s_cmp_eq_u32 s54, 0\n"
"s_cbranch_scc1 .Lfi_242\n"
"s_cmp_lg_u32 s3, 3\n"
"s_cbranch_scc1 .Lgo_202c\n"
"s_branch .Lgo_202\n"
".Lfi_242:\n"
"s_mov_b32 s54, 1\n"
"s_add_u32 s46, s44, 0x13b000\n"
"s_add_u32 s47, s46, 0x1000\n"
"buffer_load_dwordx4 v[28:31], v1, s[4:7], s46 offen offset:0 sc1\n"
"buffer_load_dwordx4 v[32:35], v1, s[4:7], s46 offen offset:1024 sc1\n"
"buffer_load_dwordx4 v[36:39], v1, s[4:7], s46 offen offset:2048 sc1\n"
"buffer_load_dwordx4 v[40:43], v1, s[4:7], s46 offen offset:3072 sc1\n"
"buffer_load_dwordx4 v[44:47], v1, s[4:7], s47 offen offset:0 sc1\n"
"s_mov_b32 s17, 32768\n"
".Ltp_243:\n"
"s_waitcnt vmcnt(0)\n"
"s_sub_u32 s17, s17, 1\n"
"s_cbranch_scc1 .Lou_244\n"
"v_max_u32 v48, v28, v29\n"
"v_max3_u32 v48, v30, v31, v48\n"
"v_max3_u32 v48, v32, v33, v48\n"
"v_max3_u32 v48, v34, v35, v48\n"
"v_max3_u32 v48, v36, v37, v48\n"
"v_max3_u32 v48, v38, v39, v48\n"
"v_max3_u32 v48, v40, v41, v48\n"
"v_max3_u32 v48, v42, v43, v48\n"
"v_max3_u32 v48, v44, v45, v48\n"
"v_max3_u32 v48, v46, v47, v48\n"
"v_cmp_eq_u32 vcc, -1, v48\n"
"s_cbranch_vccz .Lou_244\n"
"v_max_u32 v48, v30, v31\n"
"v_max3_u32 v48, v28, v29, v48\n"
"v_cmp_eq_u32 vcc, -1, v48\n"
"s_cbranch_vccz .Lsk_245\n"
"s_mov_b64 exec, vcc\n"
"buffer_load_dwordx4 v[28:31], v1, s[4:7], s46 offen offset:0 sc1\n"
"s_mov_b64 exec, -1\n"
".Lsk_245:\n"
"v_max_u32 v48, v34, v35\n"
"v_max3_u32 v48, v32, v33, v48\n"
"v_cmp_eq_u32 vcc, -1, v48\n"
"s_cbranch_vccz .Lsk_246\n"
"s_mov_b64 exec, vcc\n"
"buffer_load_dwordx4 v[32:35], v1, s[4:7], s46 offen offset:1024 sc1\n"
"s_mov_b64 exec, -1\n"
".Lsk_246:\n"
"v_max_u32 v48, v38, v39\n"
"v_max3_u32 v48, v36, v37, v48\n"
"v_cmp_eq_u32 vcc, -1, v48\n"
"s_cbranch_vccz .Lsk_247\n"
"s_mov_b64 exec, vcc\n"
"buffer_load_dwordx4 v[36:39], v1, s[4:7], s46 offen offset:2048 sc1\n"
"s_mov_b64 exec, -1\n"
".Lsk_247:\n"
"v_max_u32 v48, v42, v43\n"
"v_max3_u32 v48, v40, v41, v48\n"
"v_cmp_eq_u32 vcc, -1, v48\n"
"s_cbranch_vccz .Lsk_248\n"
"s_mov_b64 exec, vcc\n"
"buffer_load_dwordx4 v[40:43], v1, s[4:7], s46 offen offset:3072 sc1\n"
"s_mov_b64 exec, -1\n"
".Lsk_248:\n"
"v_max_u32 v48, v46, v47\n"
"v_max3_u32 v48, v44, v45, v48\n"
"v_cmp_eq_u32 vcc, -1, v48\n"
"s_cbranch_vccz .Lsk_249\n"
"s_mov_b64 exec, vcc\n"
"buffer_load_dwordx4 v[44:47], v1, s[4:7], s47 offen offset:0 sc1\n"
"s_mov_b64 exec, -1\n"
".Lsk_249:\n"
"s_branch .Ltp_243\n"
".Lou_244:\n"
"s_waitcnt vmcnt(0)\n"
"s_add_u32 s46, s44, 0x140000\n"
"s_add_u32 s47, s46, 0x1000\n"
"buffer_load_dwordx4 v[28:31], v1, s[4:7], s46 offen offset:0 sc1\n"
"buffer_load_dwordx4 v[32:35], v1, s[4:7], s46 offen offset:1024 sc1\n"
"buffer_load_dwordx4 v[36:39], v1, s[4:7], s46 offen offset:2048 sc1\n"
"buffer_load_dwordx4 v[40:43], v1, s[4:7], s46 offen offset:3072 sc1\n"
"buffer_load_dwordx4 v[44:47], v1, s[4:7], s47 offen offset:0 sc1\n"
"s_mov_b32 s17, 32768\n"
".Ltp_250:\n"
"s_waitcnt vmcnt(0)\n"
"s_sub_u32 s17, s17, 1\n"
"s_cbranch_scc1 .Lou_251\n"
"v_max_u32 v48, v28, v29\n"
"v_max3_u32 v48, v30, v31, v48\n"
"v_max3_u32 v48, v32, v33, v48\n"
"v_max3_u32 v48, v34, v35, v48\n"
"v_max3_u32 v48, v36, v37, v48\n"
"v_max3_u32 v48, v38, v39, v48\n"
"v_max3_u32 v48, v40, v41, v48\n"
"v_max3_u32 v48, v42, v43, v48\n"
"v_max3_u32 v48, v44, v45, v48\n"
"v_max3_u32 v48, v46, v47, v48\n"
"v_cmp_eq_u32 vcc, -1, v48\n"
"s_cbranch_vccz .Lou_251\n"
"v_max_u32 v48, v30, v31\n"
"v_max3_u32 v48, v28, v29, v48\n"
"v_cmp_eq_u32 vcc, -1, v48\n"
"s_cbranch_vccz .Lsk_252\n"
"s_mov_b64 exec, vcc\n"
"buffer_load_dwordx4 v[28:31], v1, s[4:7], s46 offen offset:0 sc1\n"
"s_mov_b64 exec, -1\n"
".Lsk_252:\n"
"v_max_u32 v48, v34, v35\n"
"v_max3_u32 v48, v32, v33, v48\n"
"v_cmp_eq_u32 vcc, -1, v48\n"
"s_cbranch_vccz .Lsk_253\n"
"s_mov_b64 exec, vcc\n"
"buffer_load_dwordx4 v[32:35], v1, s[4:7], s46 offen offset:1024 sc1\n"
"s_mov_b64 exec, -1\n"
".Lsk_253:\n"
"v_max_u32 v48, v38, v39\n"
"v_max3_u32 v48, v36, v37, v48\n"
"v_cmp_eq_u32 vcc, -1, v48\n"
"s_cbranch_vccz .Lsk_254\n"
"s_mov_b64 exec, vcc\n"
"buffer_load_dwordx4 v[36:39], v1, s[4:7], s46 offen offset:2048 sc1\n"
"s_mov_b64 exec, -1\n"
".Lsk_254:\n"
"v_max_u32 v48, v42, v43\n"
"v_max3_u32 v48, v40, v41, v48\n"
"v_cmp_eq_u32 vcc, -1, v48\n"
"s_cbranch_vccz .Lsk_255\n"
"s_mov_b64 exec, vcc\n"
"buffer_load_dwordx4 v[40:43], v1, s[4:7], s46 offen offset:3072 sc1\n"
"s_mov_b64 exec, -1\n"
".Lsk_255:\n"
"v_max_u32 v48, v46, v47\n"
"v_max3_u32 v48, v44, v45, v48\n"
"v_cmp_eq_u32 vcc, -1, v48\n"
"s_cbranch_vccz .Lsk_256\n"
"s_mov_b64 exec, vcc\n"
"buffer_load_dwordx4 v[44:47], v1, s[4:7], s47 offen offset:0 sc1\n"
"s_mov_b64 exec, -1\n"
".Lsk_256:\n"
"s_branch .Ltp_250\n"
".Lou_251:\n"
"s_waitcnt vmcnt(0)\n"
"s_add_u32 s46, s44, 0x145000\n"
"s_add_u32 s47, s46, 0x1000\n"
"buffer_load_dwordx4 v[28:31], v1, s[4:7], s46 offen offset:0 sc1\n"
"buffer_load_dwordx4 v[32:35], v1, s[4:7], s46 offen offset:1024 sc1\n"
"buffer_load_dwordx4 v[36:39], v1, s[4:7], s46 offen offset:2048 sc1\n"
"buffer_load_dwordx4 v[40:43], v1, s[4:7], s46 offen offset:3072 sc1\n"
"buffer_load_dwordx4 v[44:47], v1, s[4:7], s47 offen offset:0 sc1\n"
"s_mov_b32 s17, 32768\n"
".Ltp_257:\n"
"s_waitcnt vmcnt(0)\n"
"s_sub_u32 s17, s17, 1\n"
"s_cbranch_scc1 .Lou_258\n"
"v_max_u32 v48, v28, v29\n"
"v_max3_u32 v48, v30, v31, v48\n"
"v_max3_u32 v48, v32, v33, v48\n"
"v_max3_u32 v48, v34, v35, v48\n"
"v_max3_u32 v48, v36, v37, v48\n"
"v_max3_u32 v48, v38, v39, v48\n"
"v_max3_u32 v48, v40, v41, v48\n"
"v_max3_u32 v48, v42, v43, v48\n"
"v_max3_u32 v48, v44, v45, v48\n"
"v_max3_u32 v48, v46, v47, v48\n"
"v_cmp_eq_u32 vcc, -1, v48\n"
"s_cbranch_vccz .Lou_258\n"
"v_max_u32 v48, v30, v31\n"
"v_max3_u32 v48, v28, v29, v48\n"
"v_cmp_eq_u32 vcc, -1, v48\n"
"s_cbranch_vccz .Lsk_259\n"
"s_mov_b64 exec, vcc\n"
"buffer_load_dwordx4 v[28:31], v1, s[4:7], s46 offen offset:0 sc1\n"
"s_mov_b64 exec, -1\n"
".Lsk_259:\n"
"v_max_u32 v48, v34, v35\n"
"v_max3_u32 v48, v32, v33, v48\n"
"v_cmp_eq_u32 vcc, -1, v48\n"
"s_cbranch_vccz .Lsk_260\n"
"s_mov_b64 exec, vcc\n"
"buffer_load_dwordx4 v[32:35], v1, s[4:7], s46 offen offset:1024 sc1\n"
"s_mov_b64 exec, -1\n"
".Lsk_260:\n"
"v_max_u32 v48, v38, v39\n"
"v_max3_u32 v48, v36, v37, v48\n"
"v_cmp_eq_u32 vcc, -1, v48\n"
"s_cbranch_vccz .Lsk_261\n"
"s_mov_b64 exec, vcc\n"
"buffer_load_dwordx4 v[36:39], v1, s[4:7], s46 offen offset:2048 sc1\n"
"s_mov_b64 exec, -1\n"
".Lsk_261:\n"
"v_max_u32 v48, v42, v43\n"
"v_max3_u32 v48, v40, v41, v48\n"
"v_cmp_eq_u32 vcc, -1, v48\n"
"s_cbranch_vccz .Lsk_262\n"
"s_mov_b64 exec, vcc\n"
"buffer_load_dwordx4 v[40:43], v1, s[4:7], s46 offen offset:3072 sc1\n"
"s_mov_b64 exec, -1\n"
".Lsk_262:\n"
"v_max_u32 v48, v46, v47\n"
"v_max3_u32 v48, v44, v45, v48\n"
"v_cmp_eq_u32 vcc, -1, v48\n"
"s_cbranch_vccz .Lsk_263\n"
"s_mov_b64 exec, vcc\n"
"buffer_load_dwordx4 v[44:47], v1, s[4:7], s47 offen offset:0 sc1\n"
"s_mov_b64 exec, -1\n"
".Lsk_263:\n"
"s_branch .Ltp_257\n"
".Lou_258:\n"
"s_waitcnt vmcnt(0)\n"
"s_add_u32 s46, s44, 0x14a000\n"
"s_add_u32 s47, s46, 0x1000\n"
"buffer_load_dwordx4 v[28:31], v1, s[4:7], s46 offen offset:0 sc1\n"
"buffer_load_dwordx4 v[32:35], v1, s[4:7], s46 offen offset:1024 sc1\n"
"buffer_load_dwordx4 v[36:39], v1, s[4:7], s46 offen offset:2048 sc1\n"
"buffer_load_dwordx4 v[40:43], v1, s[4:7], s46 offen offset:3072 sc1\n"
"buffer_load_dwordx4 v[44:47], v1, s[4:7], s47 offen offset:0 sc1\n"
"s_mov_b32 s17, 32768\n"
".Ltp_264:\n"
"s_waitcnt vmcnt(0)\n"
"s_sub_u32 s17, s17, 1\n"
"s_cbranch_scc1 .Lou_265\n"
"v_max_u32 v48, v28, v29\n"
"v_max3_u32 v48, v30, v31, v48\n"
"v_max3_u32 v48, v32, v33, v48\n"
"v_max3_u32 v48, v34, v35, v48\n"
"v_max3_u32 v48, v36, v37, v48\n"
"v_max3_u32 v48, v38, v39, v48\n"
"v_max3_u32 v48, v40, v41, v48\n"
"v_max3_u32 v48, v42, v43, v48\n"
"v_max3_u32 v48, v44, v45, v48\n"
"v_max3_u32 v48, v46, v47, v48\n"
"v_cmp_eq_u32 vcc, -1, v48\n"
"s_cbranch_vccz .Lou_265\n"
"v_max_u32 v48, v30, v31\n"
"v_max3_u32 v48, v28, v29, v48\n"
"v_cmp_eq_u32 vcc, -1, v48\n"
"s_cbranch_vccz .Lsk_266\n"
"s_mov_b64 exec, vcc\n"
"buffer_load_dwordx4 v[28:31], v1, s[4:7], s46 offen offset:0 sc1\n"
"s_mov_b64 exec, -1\n"
".Lsk_266:\n"
"v_max_u32 v48, v34, v35\n"
"v_max3_u32 v48, v32, v33, v48\n"
"v_cmp_eq_u32 vcc, -1, v48\n"
"s_cbranch_vccz .Lsk_267\n"
"s_mov_b64 exec, vcc\n"
"buffer_load_dwordx4 v[32:35], v1, s[4:7], s46 offen offset:1024 sc1\n"
"s_mov_b64 exec, -1\n"
".Lsk_267:\n"
"v_max_u32 v48, v38, v39\n"
"v_max3_u32 v48, v36, v37, v48\n"
"v_cmp_eq_u32 vcc, -1, v48\n"
"s_cbranch_vccz .Lsk_268\n"
"s_mov_b64 exec, vcc\n"
"buffer_load_dwordx4 v[36:39], v1, s[4:7], s46 offen offset:2048 sc1\n"
"s_mov_b64 exec, -1\n"
".Lsk_268:\n"
"v_max_u32 v48, v42, v43\n"
"v_max3_u32 v48, v40, v41, v48\n"
"v_cmp_eq_u32 vcc, -1, v48\n"
"s_cbranch_vccz .Lsk_269\n"
"s_mov_b64 exec, vcc\n"
"buffer_load_dwordx4 v[40:43], v1, s[4:7], s46 offen offset:3072 sc1\n"
"s_mov_b64 exec, -1\n"
".Lsk_269:\n"
"v_max_u32 v48, v46, v47\n"
"v_max3_u32 v48, v44, v45, v48\n"
"v_cmp_eq_u32 vcc, -1, v48\n"
"s_cbranch_vccz .Lsk_270\n"
"s_mov_b64 exec, vcc\n"
"buffer_load_dwordx4 v[44:47], v1, s[4:7], s47 offen offset:0 sc1\n"
"s_mov_b64 exec, -1\n"
".Lsk_270:\n"
"s_branch .Ltp_264\n"
".Lou_265:\n"
"s_waitcnt vmcnt(0)\n"
"s_add_u32 s46, s44, 0x14f000\n"
"s_add_u32 s47, s46, 0x1000\n"
"buffer_load_dwordx4 v[28:31], v1, s[4:7], s46 offen offset:0 sc1\n"
"buffer_load_dwordx4 v[32:35], v1, s[4:7], s46 offen offset:1024 sc1\n"
"buffer_load_dwordx4 v[36:39], v1, s[4:7], s46 offen offset:2048 sc1\n"
"buffer_load_dwordx4 v[40:43], v1, s[4:7], s46 offen offset:3072 sc1\n"
"buffer_load_dwordx4 v[44:47], v1, s[4:7], s47 offen offset:0 sc1\n"
"s_mov_b32 s17, 32768\n"
".Ltp_271:\n"
"s_waitcnt vmcnt(0)\n"
"s_sub_u32 s17, s17, 1\n"
"s_cbranch_scc1 .Lou_272\n"
"v_max_u32 v48, v28, v29\n"
"v_max3_u32 v48, v30, v31, v48\n"
"v_max3_u32 v48, v32, v33, v48\n"
"v_max3_u32 v48, v34, v35, v48\n"
"v_max3_u32 v48, v36, v37, v48\n"
"v_max3_u32 v48, v38, v39, v48\n"
"v_max3_u32 v48, v40, v41, v48\n"
"v_max3_u32 v48, v42, v43, v48\n"
"v_max3_u32 v48, v44, v45, v48\n"
"v_max3_u32 v48, v46, v47, v48\n"
"v_cmp_eq_u32 vcc, -1, v48\n"
"s_cbranch_vccz .Lou_272\n"
"v_max_u32 v48, v30, v31\n"
"v_max3_u32 v48, v28, v29, v48\n"
"v_cmp_eq_u32 vcc, -1, v48\n"
"s_cbranch_vccz .Lsk_273\n"
"s_mov_b64 exec, vcc\n"
"buffer_load_dwordx4 v[28:31], v1, s[4:7], s46 offen offset:0 sc1\n"
"s_mov_b64 exec, -1\n"
".Lsk_273:\n"
"v_max_u32 v48, v34, v35\n"
"v_max3_u32 v48, v32, v33, v48\n"
"v_cmp_eq_u32 vcc, -1, v48\n"
"s_cbranch_vccz .Lsk_274\n"
"s_mov_b64 exec, vcc\n"
"buffer_load_dwordx4 v[32:35], v1, s[4:7], s46 offen offset:1024 sc1\n"
"s_mov_b64 exec, -1\n"
".Lsk_274:\n"
"v_max_u32 v48, v38, v39\n"
"v_max3_u32 v48, v36, v37, v48\n"
"v_cmp_eq_u32 vcc, -1, v48\n"
"s_cbranch_vccz .Lsk_275\n"
"s_mov_b64 exec, vcc\n"
"buffer_load_dwordx4 v[36:39], v1, s[4:7], s46 offen offset:2048 sc1\n"
"s_mov_b64 exec, -1\n"
".Lsk_275:\n"
"v_max_u32 v48, v42, v43\n"
"v_max3_u32 v48, v40, v41, v48\n"
"v_cmp_eq_u32 vcc, -1, v48\n"
"s_cbranch_vccz .Lsk_276\n"
"s_mov_b64 exec, vcc\n"
"buffer_load_dwordx4 v[40:43], v1, s[4:7], s46 offen offset:3072 sc1\n"
"s_mov_b64 exec, -1\n"
".Lsk_276:\n"
"v_max_u32 v48, v46, v47\n"
"v_max3_u32 v48, v44, v45, v48\n"
"v_cmp_eq_u32 vcc, -1, v48\n"
"s_cbranch_vccz .Lsk_277\n"
"s_mov_b64 exec, vcc\n"
"buffer_load_dwordx4 v[44:47], v1, s[4:7], s47 offen offset:0 sc1\n"
"s_mov_b64 exec, -1\n"
".Lsk_277:\n"
"s_branch .Ltp_271\n"
".Lou_272:\n"
"s_waitcnt vmcnt(0)\n"
"ds_write_b32 v13, v13 offset:448\n"
"s_mov_b32 s17, 0x100000\n"
".Ltp_278:\n"
"ds_read_b32 v90, v14 offset:448\n"
"s_sub_u32 s17, s17, 1\n"
"s_cbranch_scc1 .Lou_279\n"
"s_waitcnt lgkmcnt(0)\n"
"v_cmp_eq_u32 vcc, 0, v90\n"
"s_cbranch_vccnz .Ltp_278\n"
".Lou_279:\n"
"s_waitcnt lgkmcnt(0)\n"
"v_mov_b32 v90, 0\n"
"v_mov_b32 v90, -1\n"
"ds_write_b32 v9, v90 offset:3712\n"
"ds_write_b32 v9, v90 offset:4032\n"
"ds_write_b32 v9, v90 offset:4352\n"
"ds_write_b32 v9, v90 offset:4672\n"
"ds_write_b32 v9, v90 offset:4992\n"
"ds_write_b32 v10, v90 offset:5312\n"
"s_add_u32 m0, s29, 0xa000\n"
"s_add_u32 s31, s44, 0x140000\n"
"buffer_load_dwordx4 v1, s[4:7], s31 offen offset:0 sc1 lds\n"
"buffer_load_dwordx4 v1, s[4:7], s31 offen offset:1024 sc1 lds\n"
"buffer_load_dwordx4 v1, s[4:7], s31 offen offset:2048 sc1 lds\n"
"buffer_load_dwordx4 v1, s[4:7], s31 offen offset:3072 sc1 lds\n"
"s_add_u32 m0, m0, 1024\n"
"s_add_u32 s31, s31, 1024\n"
"buffer_load_dwordx4 v1, s[4:7], s31 offen offset:3072 sc1 lds\n"
"s_add_u32 m0, s29, 0xf000\n"
"s_add_u32 s31, s44, 0x145000\n"
"buffer_load_dwordx4 v1, s[4:7], s31 offen offset:0 sc1 lds\n"
"buffer_load_dwordx4 v1, s[4:7], s31 offen offset:1024 sc1 lds\n"
"buffer_load_dwordx4 v1, s[4:7], s31 offen offset:2048 sc1 lds\n"
"buffer_load_dwordx4 v1, s[4:7], s31 offen offset:3072 sc1 lds\n"
"s_add_u32 m0, m0, 1024\n"
"s_add_u32 s31, s31, 1024\n"
"buffer_load_dwordx4 v1, s[4:7], s31 offen offset:3072 sc1 lds\n"
"s_add_u32 m0, s29, 0\n"
"s_add_u32 s31, s44, 0x14a000\n"
"buffer_load_dwordx4 v1, s[4:7], s31 offen offset:0 sc1 lds\n"
"buffer_load_dwordx4 v1, s[4:7], s31 offen offset:1024 sc1 lds\n"
"buffer_load_dwordx4 v1, s[4:7], s31 offen offset:2048 sc1 lds\n"
"buffer_load_dwordx4 v1, s[4:7], s31 offen offset:3072 sc1 lds\n"
"s_add_u32 m0, m0, 1024\n"
"s_add_u32 s31, s31, 1024\n"
"buffer_load_dwordx4 v1, s[4:7], s31 offen offset:3072 sc1 lds\n"
"s_add_u32 m0, s29, 0x5000\n"
"s_add_u32 s31, s44, 0x14f000\n"
"buffer_load_dwordx4 v1, s[4:7], s31 offen offset:0 sc1 lds\n"
"buffer_load_dwordx4 v1, s[4:7], s31 offen offset:1024 sc1 lds\n"
"buffer_load_dwordx4 v1, s[4:7], s31 offen offset:2048 sc1 lds\n"
"buffer_load_dwordx4 v1, s[4:7], s31 offen offset:3072 sc1 lds\n"
"s_add_u32 m0, m0, 1024\n"
"s_add_u32 s31, s31, 1024\n"
"buffer_load_dwordx4 v1, s[4:7], s31 offen offset:3072 sc1 lds\n"
"s_waitcnt vmcnt(0)\n"
"ds_write_b32 v13, v13 offset:464\n"
"s_mov_b32 s17, 0x100000\n"
".Ltp_280:\n"
"ds_read_b32 v90, v14 offset:464\n"
"s_sub_u32 s17, s17, 1\n"
"s_cbranch_scc1 .Lou_281\n"
"s_waitcnt lgkmcnt(0)\n"
"v_cmp_eq_u32 vcc, 0, v90\n"
"s_cbranch_vccnz .Ltp_280\n"
".Lou_281:\n"
"s_waitcnt lgkmcnt(0)\n"
"v_mov_b32 v90, 0\n"
"s_branch .Lag_201\n"
".L_xend:\n"
"v_and_b32 v0, 63, v0\n"
"s_and_b32 s3, s3, 3\n"
"s_and_b32 s79, s2, 7\n"
"s_lshl_b32 s79, s79, 4\n"
"s_lshr_b32 s78, s2, 3\n"
"s_add_u32 s79, s79, s78\n"
"s_and_b32 s78, s2, 3\n"
"s_lshl_b32 s78, s78, 3\n"
"s_and_b32 s77, s2, 4\n"
"s_cmp_eq_i32 s77, 4\n"
"s_cselect_b32 s78, 32, s78\n"
"s_add_u32 s79, s79, s78\n"
"s_and_b32 s78, s2, 4\n"
"s_lshl_b32 s78, s78, 4\n"
"s_add_u32 s78, s2, s78\n"
"s_cmp_lt_i32 s78, 0xc0\n"
"s_cselect_b32 s18, s79, -1\n"
"s_and_b32 s79, s2, 7\n"
"s_lshl_b32 s79, s79, 4\n"
"s_lshr_b32 s78, s2, 3\n"
"s_add_u32 s79, s79, s78\n"
"s_and_b32 s78, s2, 3\n"
"s_lshl_b32 s78, s78, 3\n"
"s_add_u32 s78, s78, 24\n"
"s_and_b32 s77, s2, 4\n"
"s_cmp_eq_i32 s77, 4\n"
"s_cselect_b32 s78, 48, s78\n"
"s_sub_u32 s79, s79, s78\n"
"s_and_b32 s78, s2, 4\n"
"s_lshl_b32 s78, s78, 4\n"
"s_add_u32 s78, s2, s78\n"
"s_cmp_lt_i32 s78, 0xc0\n"
"s_cselect_b32 s19, 0x3e8, s79\n"
"v_lshlrev_b32 v2, 4, v0\n"
"s_mov_b32 s4, s26\n"
"s_mov_b32 s5, s27\n"
"s_mov_b32 s6, 0x1688000\n"
"s_mov_b32 s7, 0x20000\n"
"s_mul_i32 s79, s3, 0x2100\n"
"s_add_u32 s74, s79, 0x14000\n"
"v_lshl_add_u32 v152, v0, 2, s74\n"
"v_add_u32 v153, 0x440, v152\n"
"v_add_u32 v154, 0x880, v152\n"
"v_add_u32 v155, 0xcc0, v152\n"
"v_and_b32 v99, 15, v0\n"
"v_and_b32 v98, 48, v0\n"
"s_mov_b32 s79, 0x110\n"
"v_mad_i32_i24 v99, v99, s79, v98\n"
"v_add_u32 v141, s74, v99\n"
"s_lshl_b32 s79, s3, 2\n"
"s_add_u32 s79, s79, 0x26400\n"
"v_mov_b32 v99, s79\n"
"s_mov_b32 vcc_lo, 1\n"
"s_mov_b32 vcc_hi, 0\n"
"v_cndmask_b32 v4, 2.0, v99, vcc\n"
"v_and_b32 v99, 3, v0\n"
"v_lshlrev_b32 v99, 2, v99\n"
"v_add_u32 v5, 0x26400, v99\n"
"v_min_i32 v99, 49, v0\n"
"v_lshlrev_b32 v38, 5, v99\n"
"s_and_b32 s79, s2, 1\n"
"s_lshl_b32 s79, s79, 2\n"
"s_lshr_b32 s78, s2, 3\n"
"s_and_b32 s78, s78, 3\n"
"s_add_u32 s28, s79, s78\n"
"s_lshr_b32 s79, s2, 1\n"
"s_and_b32 s79, s79, 3\n"
"s_lshl_b32 s79, s79, 3\n"
"s_lshr_b32 s78, s2, 3\n"
"s_lshr_b32 s78, s78, 2\n"
"s_add_u32 s29, s79, s78\n"
"s_and_b32 s79, s2, 7\n"
"s_lshl_b32 s79, s79, 14\n"
"s_add_u32 s48, s79, 0x4000\n"
"s_mov_b32 s8, s22\n"
"s_mov_b32 s9, s23\n"
"s_mov_b32 s10, 0x280000\n"
"s_mov_b32 s11, 0x20000\n"
"s_mov_b32 s12, s24\n"
"s_mov_b32 s13, s25\n"
"s_mov_b32 s14, 0x280000\n"
"s_mov_b32 s15, 0x20000\n"
"s_mov_b32 s44, 0xcccccccc\n"
"s_mov_b32 s45, 0xcccccccc\n"
"s_mov_b32 s56, 0\n"
"s_mov_b32 s57, -2\n"
"s_mov_b32 s58, 0\n"
"s_mov_b32 s59, -1\n"
"s_lshl_b32 s79, s29, 1\n"
"s_lshr_b32 s78, s3, 1\n"
"s_add_u32 s79, s79, s78\n"
"s_mul_i32 s30, s79, 0x5000\n"
"s_and_b32 s79, s2, 7\n"
"s_lshl_b32 s79, s79, 14\n"
"s_add_u32 s33, s79, 0x3800\n"
"s_lshl_b32 s79, s28, 2\n"
"s_add_u32 s79, s79, s3\n"
"s_mul_i32 s79, s79, 0x6400\n"
"s_add_u32 s32, s79, 0x380000\n"
"s_lshr_b32 s79, s2, 1\n"
"s_and_b32 s79, s79, 3\n"
"s_lshl_b32 s79, s79, 2\n"
"s_lshr_b32 s78, s2, 3\n"
"s_lshr_b32 s78, s78, 3\n"
"s_add_u32 s79, s79, s78\n"
"s_lshl_b32 s79, s79, 2\n"
"s_add_u32 s79, s79, s3\n"
"s_mul_i32 s79, s79, 0x5000\n"
"s_and_b32 s78, s2, 1\n"
"s_lshl_b32 s78, s78, 3\n"
"s_lshr_b32 s77, s2, 3\n"
"s_and_b32 s77, s77, 7\n"
"s_add_u32 s78, s78, s77\n"
"s_mul_i32 s78, s78, 0x500\n"
"s_add_u32 s79, s79, s78\n"
"s_add_u32 s31, s79, 0x448000\n"
"v_mov_b32 v128, 0\n"
"v_mov_b32 v129, 0\n"
"v_mov_b32 v130, 0\n"
"v_mov_b32 v131, 0\n"
"v_mov_b32 v132, 0\n"
"v_mov_b32 v133, 0\n"
"v_mov_b32 v134, 0\n"
"v_mov_b32 v135, 0\n"
"v_mov_b32 v136, 0\n"
"v_mov_b32 v137, 0\n"
"v_mov_b32 v138, 0\n"
"v_mov_b32 v139, 0\n"
"s_waitcnt lgkmcnt(0)\n"
"s_and_b32 s79, s3, 1\n"
"s_lshl_b32 s60, s79, 4\n"
"s_lshr_b32 s39, s39, s60\n"
"s_lshr_b32 s40, s40, s60\n"
"s_lshr_b32 s41, s41, s60\n"
"v_mov_b32 v142, s40\n"
"v_mov_b32 v140, s41\n"
"s_mul_i32 s16, s16, 0x2c9277b5\n"
"s_add_u32 s16, s16, 0xac564b05\n"
"s_cmp_lt_i32 s18, 0\n"
"s_cbranch_scc1 .Lnk_317\n"
"s_setprio 0\n"
"s_mov_b32 s55, 2\n"
"s_mov_b32 s52, 0xc2f00000\n"
"s_mov_b32 s53, 0x3fb8aa3b\n"
"s_mov_b32 s54, 0x3f317218\n"
"v_mov_b32 v31, 0x42f00000\n"
"v_lshlrev_b32 v142, 16, v142\n"
"v_med3_f32 v142, v142, s52, v31\n"
"v_mul_f32 v28, s53, v142\n"
"v_exp_f32 v29, v28\n"
"v_fma_f32 v28, v142, s53, -v28\n"
"v_fmac_f32 v28, 0x32a57060, v142\n"
"v_fma_f32 v28, v28, s54, 1.0\n"
"v_mul_f32 v142, v29, v28\n"
"v_cvt_pk_bf16_f32 v142, v142, v142\n"
"v_lshlrev_b32 v142, 16, v142\n"
"s_waitcnt vmcnt(8)\n"
"s_nop 3\n"
".Lkg_324:\n"
"v_lshlrev_b32 v32, 16, v32\n"
"v_lshlrev_b32 v33, 16, v33\n"
"v_lshlrev_b32 v34, 16, v34\n"
"v_lshlrev_b32 v35, 16, v35\n"
"v_mul_f32 v26, v142, v34\n"
"v_mul_f32 v27, v142, v35\n"
"v_max3_f32 v30, |v26|, |v27|, 0\n"
"s_nop 1\n"
"v_max_u32_dpp v30, v30, v30 row_ror:8 row_mask:0xf bank_mask:0xf\n"
"v_med3_f32 v32, v32, s52, v31\n"
"s_nop 0\n"
"v_max_u32_dpp v30, v30, v30 row_ror:4 row_mask:0xf bank_mask:0xf\n"
"v_mul_f32 v28, s53, v32\n"
"s_nop 0\n"
"v_max_u32_dpp v30, v30, v30 row_ror:2 row_mask:0xf bank_mask:0xf\n"
"v_exp_f32 v29, v28\n"
"s_nop 0\n"
"v_max_u32_dpp v30, v30, v30 row_ror:1 row_mask:0xf bank_mask:0xf\n"
"v_fma_f32 v28, v32, s53, -v28\n"
"v_fmac_f32 v28, 0x32a57060, v32\n"
"v_mov_b32 v34, v30\n"
"v_fma_f32 v28, v28, s54, 1.0\n"
"s_nop 0\n"
"v_permlane16_swap_b32 v30, v34\n"
"v_mul_f32 v32, v29, v28\n"
"v_max_u32 v30, v30, v34\n"
"v_cvt_pk_bf16_f32 v32, v32, v32\n"
"v_lshlrev_b32 v32, 16, v32\n"
"v_mov_b32 v34, v30\n"
"v_mul_f32 v25, v142, v33\n"
"s_nop 0\n"
"v_permlane32_swap_b32 v30, v34\n"
"v_mul_f32 v24, v142, -v32\n"
"v_max_u32 v30, v30, v34\n"
"v_mul_f32 v25, 0.15915494, v25\n"
"v_mul_f32 v24, s53, v24\n"
"v_readfirstlane_b32 s51, v30\n"
"s_nop 2\n"
"s_bfe_u32 s50, s51, 0x80017\n"
"s_max_i32 s50, s50, 37\n"
"s_min_i32 s50, s50, 217\n"
"s_cmp_gt_i32 s51, 0\n"
"s_cselect_b32 s50, s50, 127\n"
"s_sub_u32 s51, 267, s50\n"
"s_lshl_b32 s51, s51, 23\n"
"s_sub_u32 s50, s50, 27\n"
"s_lshl_b32 s50, s50, 23\n"
"v_mul_f32 v26, s51, v26\n"
"v_mul_f32 v27, s51, v27\n"
"v_mul_f32 v111, 0x41800000, v25\n"
"v_mul_f32 v100, 0x41800000, v24\n"
"v_fract_f32 v111, v111\n"
"v_exp_f32 v100, v100\n"
"v_cos_f32 v110, v111\n"
"v_sin_f32 v111, -v111\n"
"v_mul_f32 v110, v110, v100\n"
"v_mul_f32 v111, v111, v100\n"
"v_fract_f32 v113, v25\n"
"v_exp_f32 v100, v24\n"
"v_cos_f32 v112, v113\n"
"v_sin_f32 v113, v113\n"
"v_mul_f32 v112, v112, v100\n"
"v_mul_f32 v113, v113, v100\n"
"v_mul_f32 v115, 0x43800000, v25\n"
"v_mul_f32 v100, 0x43800000, v24\n"
"v_fract_f32 v115, v115\n"
"v_exp_f32 v100, v100\n"
"v_cos_f32 v114, v115\n"
"v_sin_f32 v115, v115\n"
"v_mul_f32 v114, v114, v100\n"
"v_mul_f32 v115, v115, v100\n"
"v_mul_f32 v116, v111, v111\n"
"v_add_f32 v117, v110, v110\n"
"v_fma_f32 v116, v110, v110, -v116\n"
"v_mul_f32 v117, v117, v111\n"
"v_mul_f32 v120, v117, v117\n"
"v_add_f32 v121, v116, v116\n"
"v_fma_f32 v120, v116, v116, -v120\n"
"v_mul_f32 v121, v121, v117\n"
"v_mul_f32 v118, v117, v111\n"
"v_mul_f32 v119, v116, v111\n"
"v_fma_f32 v118, v116, v110, -v118\n"
"v_fmac_f32 v119, v117, v110\n"
"v_mul_f32 v122, v121, v121\n"
"v_add_f32 v123, v120, v120\n"
"v_fma_f32 v122, v120, v120, -v122\n"
"v_mul_f32 v123, v123, v121\n"
"v_mov_b32 v84, 0x7400\n"
"v_mul_f32 v124, 0x46800000, v110\n"
"v_mul_f32 v125, 0x46800000, v111\n"
"v_cvt_pk_f16_f32 v100, v124, v125\n"
"v_mul_f32 v124, 0x46800000, v116\n"
"v_mul_f32 v125, 0x46800000, v117\n"
"v_cvt_pk_f16_f32 v85, v124, v125\n"
"v_mul_f32 v124, 0x46800000, v118\n"
"v_mul_f32 v125, 0x46800000, v119\n"
"v_cvt_pk_f16_f32 v101, v124, v125\n"
"v_cvt_pk_f16_f32 v93, v120, v121\n"
"v_cvt_pk_f16_f32 v94, v122, v123\n"
"v_mul_f32 v124, v123, v121\n"
"v_mul_f32 v125, v122, v121\n"
"v_fma_f32 v124, v122, v120, -v124\n"
"v_fmac_f32 v125, v123, v120\n"
"v_cvt_pk_f16_f32 v95, v124, v125\n"
"ds_write2_b32 v152, v84, v100 offset0:0 offset1:68\n"
"v_pk_mul_f16 v86, v84, v93 op_sel:[0,0] op_sel_hi:[1,0]\n"
"v_pk_mul_f16 v102, v100, v93 op_sel:[0,0] op_sel_hi:[1,0]\n"
"v_pk_fma_f16 v86, v84, v93, v86 op_sel:[1,1,0] op_sel_hi:[0,1,1] neg_lo:[1,0,0]\n"
"v_pk_fma_f16 v102, v100, v93, v102 op_sel:[1,1,0] op_sel_hi:[0,1,1] neg_lo:[1,0,0]\n"
"ds_write2_b32 v152, v85, v101 offset0:136 offset1:204\n"
"v_pk_mul_f16 v87, v85, v93 op_sel:[0,0] op_sel_hi:[1,0]\n"
"v_pk_mul_f16 v103, v101, v93 op_sel:[0,0] op_sel_hi:[1,0]\n"
"v_pk_fma_f16 v87, v85, v93, v87 op_sel:[1,1,0] op_sel_hi:[0,1,1] neg_lo:[1,0,0]\n"
"v_pk_fma_f16 v103, v101, v93, v103 op_sel:[1,1,0] op_sel_hi:[0,1,1] neg_lo:[1,0,0]\n"
"ds_write2_b32 v153, v86, v102 offset0:0 offset1:68\n"
"v_pk_mul_f16 v88, v84, v94 op_sel:[0,0] op_sel_hi:[1,0]\n"
"v_pk_mul_f16 v102, v100, v94 op_sel:[0,0] op_sel_hi:[1,0]\n"
"v_pk_fma_f16 v88, v84, v94, v88 op_sel:[1,1,0] op_sel_hi:[0,1,1] neg_lo:[1,0,0]\n"
"v_pk_fma_f16 v102, v100, v94, v102 op_sel:[1,1,0] op_sel_hi:[0,1,1] neg_lo:[1,0,0]\n"
"ds_write2_b32 v153, v87, v103 offset0:136 offset1:204\n"
"v_pk_mul_f16 v89, v85, v94 op_sel:[0,0] op_sel_hi:[1,0]\n"
"v_pk_mul_f16 v103, v101, v94 op_sel:[0,0] op_sel_hi:[1,0]\n"
"v_pk_fma_f16 v89, v85, v94, v89 op_sel:[1,1,0] op_sel_hi:[0,1,1] neg_lo:[1,0,0]\n"
"v_pk_fma_f16 v103, v101, v94, v103 op_sel:[1,1,0] op_sel_hi:[0,1,1] neg_lo:[1,0,0]\n"
"ds_write2_b32 v154, v88, v102 offset0:0 offset1:68\n"
"v_pk_mul_f16 v90, v84, v95 op_sel:[0,0] op_sel_hi:[1,0]\n"
"v_pk_mul_f16 v102, v100, v95 op_sel:[0,0] op_sel_hi:[1,0]\n"
"v_pk_fma_f16 v90, v84, v95, v90 op_sel:[1,1,0] op_sel_hi:[0,1,1] neg_lo:[1,0,0]\n"
"v_pk_fma_f16 v102, v100, v95, v102 op_sel:[1,1,0] op_sel_hi:[0,1,1] neg_lo:[1,0,0]\n"
"ds_write2_b32 v154, v89, v103 offset0:136 offset1:204\n"
"v_pk_mul_f16 v91, v85, v95 op_sel:[0,0] op_sel_hi:[1,0]\n"
"v_pk_mul_f16 v103, v101, v95 op_sel:[0,0] op_sel_hi:[1,0]\n"
"v_pk_fma_f16 v91, v85, v95, v91 op_sel:[1,1,0] op_sel_hi:[0,1,1] neg_lo:[1,0,0]\n"
"v_pk_fma_f16 v103, v101, v95, v103 op_sel:[1,1,0] op_sel_hi:[0,1,1] neg_lo:[1,0,0]\n"
"ds_write2_b32 v155, v90, v102 offset0:0 offset1:68\n"
"ds_write2_b32 v155, v91, v103 offset0:136 offset1:204\n"
"ds_read_b128 v[52:55], v141 offset:0\n"
"ds_read_b128 v[56:59], v141 offset:64\n"
"ds_read_b128 v[60:63], v141 offset:128\n"
"ds_read_b128 v[64:67], v141 offset:192\n"
"v_mul_f32 v116, v113, v113\n"
"v_add_f32 v117, v112, v112\n"
"v_fma_f32 v116, v112, v112, -v116\n"
"v_mul_f32 v117, v117, v113\n"
"v_mul_f32 v120, v117, v117\n"
"v_add_f32 v121, v116, v116\n"
"v_fma_f32 v120, v116, v116, -v120\n"
"v_mul_f32 v121, v121, v117\n"
"v_mul_f32 v118, v117, v113\n"
"v_mul_f32 v119, v116, v113\n"
"v_fma_f32 v118, v116, v112, -v118\n"
"v_fmac_f32 v119, v117, v112\n"
"v_mul_f32 v122, v121, v121\n"
"v_add_f32 v123, v120, v120\n"
"v_fma_f32 v122, v120, v120, -v122\n"
"v_mul_f32 v123, v123, v121\n"
"v_cvt_pk_f16_f32 v84, v26, v27\n"
"v_mul_f32 v124, v27, v113\n"
"v_mul_f32 v125, v26, v113\n"
"v_fma_f32 v124, v26, v112, -v124\n"
"v_fmac_f32 v125, v27, v112\n"
"v_cvt_pk_f16_f32 v100, v124, v125\n"
"v_mul_f32 v124, v27, v117\n"
"v_mul_f32 v125, v26, v117\n"
"v_fma_f32 v124, v26, v116, -v124\n"
"v_fmac_f32 v125, v27, v116\n"
"v_cvt_pk_f16_f32 v85, v124, v125\n"
"v_mul_f32 v124, v27, v119\n"
"v_mul_f32 v125, v26, v119\n"
"v_fma_f32 v124, v26, v118, -v124\n"
"v_fmac_f32 v125, v27, v118\n"
"v_cvt_pk_f16_f32 v101, v124, v125\n"
"v_cvt_pk_f16_f32 v93, v120, v121\n"
"v_cvt_pk_f16_f32 v94, v122, v123\n"
"v_mul_f32 v124, v123, v121\n"
"v_mul_f32 v125, v122, v121\n"
"v_fma_f32 v124, v122, v120, -v124\n"
"v_fmac_f32 v125, v123, v120\n"
"v_cvt_pk_f16_f32 v95, v124, v125\n"
"ds_write2_b32 v152, v84, v100 offset0:0 offset1:68\n"
"v_pk_mul_f16 v86, v84, v93 op_sel:[0,0] op_sel_hi:[1,0]\n"
"v_pk_mul_f16 v102, v100, v93 op_sel:[0,0] op_sel_hi:[1,0]\n"
"v_pk_fma_f16 v86, v84, v93, v86 op_sel:[1,1,0] op_sel_hi:[0,1,1] neg_lo:[1,0,0]\n"
"v_pk_fma_f16 v102, v100, v93, v102 op_sel:[1,1,0] op_sel_hi:[0,1,1] neg_lo:[1,0,0]\n"
"ds_write2_b32 v152, v85, v101 offset0:136 offset1:204\n"
"v_pk_mul_f16 v87, v85, v93 op_sel:[0,0] op_sel_hi:[1,0]\n"
"v_pk_mul_f16 v103, v101, v93 op_sel:[0,0] op_sel_hi:[1,0]\n"
"v_pk_fma_f16 v87, v85, v93, v87 op_sel:[1,1,0] op_sel_hi:[0,1,1] neg_lo:[1,0,0]\n"
"v_pk_fma_f16 v103, v101, v93, v103 op_sel:[1,1,0] op_sel_hi:[0,1,1] neg_lo:[1,0,0]\n"
"ds_write2_b32 v153, v86, v102 offset0:0 offset1:68\n"
"v_pk_mul_f16 v88, v84, v94 op_sel:[0,0] op_sel_hi:[1,0]\n"
"v_pk_mul_f16 v102, v100, v94 op_sel:[0,0] op_sel_hi:[1,0]\n"
"v_pk_fma_f16 v88, v84, v94, v88 op_sel:[1,1,0] op_sel_hi:[0,1,1] neg_lo:[1,0,0]\n"
"v_pk_fma_f16 v102, v100, v94, v102 op_sel:[1,1,0] op_sel_hi:[0,1,1] neg_lo:[1,0,0]\n"
"ds_write2_b32 v153, v87, v103 offset0:136 offset1:204\n"
"v_pk_mul_f16 v89, v85, v94 op_sel:[0,0] op_sel_hi:[1,0]\n"
"v_pk_mul_f16 v103, v101, v94 op_sel:[0,0] op_sel_hi:[1,0]\n"
"v_pk_fma_f16 v89, v85, v94, v89 op_sel:[1,1,0] op_sel_hi:[0,1,1] neg_lo:[1,0,0]\n"
"v_pk_fma_f16 v103, v101, v94, v103 op_sel:[1,1,0] op_sel_hi:[0,1,1] neg_lo:[1,0,0]\n"
"ds_write2_b32 v154, v88, v102 offset0:0 offset1:68\n"
"v_pk_mul_f16 v90, v84, v95 op_sel:[0,0] op_sel_hi:[1,0]\n"
"v_pk_mul_f16 v102, v100, v95 op_sel:[0,0] op_sel_hi:[1,0]\n"
"v_pk_fma_f16 v90, v84, v95, v90 op_sel:[1,1,0] op_sel_hi:[0,1,1] neg_lo:[1,0,0]\n"
"v_pk_fma_f16 v102, v100, v95, v102 op_sel:[1,1,0] op_sel_hi:[0,1,1] neg_lo:[1,0,0]\n"
"ds_write2_b32 v154, v89, v103 offset0:136 offset1:204\n"
"v_pk_mul_f16 v91, v85, v95 op_sel:[0,0] op_sel_hi:[1,0]\n"
"v_pk_mul_f16 v103, v101, v95 op_sel:[0,0] op_sel_hi:[1,0]\n"
"v_pk_fma_f16 v91, v85, v95, v91 op_sel:[1,1,0] op_sel_hi:[0,1,1] neg_lo:[1,0,0]\n"
"v_pk_fma_f16 v103, v101, v95, v103 op_sel:[1,1,0] op_sel_hi:[0,1,1] neg_lo:[1,0,0]\n"
"ds_write2_b32 v155, v90, v102 offset0:0 offset1:68\n"
"ds_write2_b32 v155, v91, v103 offset0:136 offset1:204\n"
"ds_read_b128 v[68:71], v141 offset:0\n"
"ds_read_b128 v[72:75], v141 offset:64\n"
"ds_read_b128 v[76:79], v141 offset:128\n"
"ds_read_b128 v[80:83], v141 offset:192\n"
"v_mul_f32 v116, v115, v115\n"
"v_add_f32 v117, v114, v114\n"
"v_fma_f32 v116, v114, v114, -v116\n"
"v_mul_f32 v117, v117, v115\n"
"v_mul_f32 v120, v117, v117\n"
"v_add_f32 v121, v116, v116\n"
"v_fma_f32 v120, v116, v116, -v120\n"
"v_mul_f32 v121, v121, v117\n"
"v_mul_f32 v118, v117, v115\n"
"v_mul_f32 v119, v116, v115\n"
"v_fma_f32 v118, v116, v114, -v118\n"
"v_fmac_f32 v119, v117, v114\n"
"v_cvt_pk_f16_f32 v92, v114, v115\n"
"v_cvt_pk_f16_f32 v93, v116, v117\n"
"v_cvt_pk_f16_f32 v95, v120, v121\n"
"v_cvt_pk_f16_f32 v94, v118, v119\n"
"v_mul_f32 v124, v121, v115\n"
"v_mul_f32 v125, v120, v115\n"
"v_fma_f32 v124, v120, v114, -v124\n"
"v_fmac_f32 v125, v121, v114\n"
"v_cvt_pk_f16_f32 v96, v124, v125\n"
"v_mul_f32 v126, v121, v117\n"
"v_mul_f32 v127, v120, v117\n"
"v_fma_f32 v126, v120, v116, -v126\n"
"v_fmac_f32 v127, v121, v116\n"
"v_cvt_pk_f16_f32 v97, v126, v127\n"
"v_mul_f32 v124, v121, v119\n"
"v_mul_f32 v125, v120, v119\n"
"v_fma_f32 v124, v120, v118, -v124\n"
"v_fmac_f32 v125, v121, v118\n"
"v_cvt_pk_f16_f32 v98, v124, v125\n"
"v_mul_f32 v126, v121, v121\n"
"v_mul_f32 v127, v120, v121\n"
"v_fma_f32 v126, v120, v120, -v126\n"
"v_fmac_f32 v127, v121, v120\n"
"v_cvt_pk_f16_f32 v99, v126, v127\n"
"s_setprio 3\n"
"s_waitcnt lgkmcnt(3)\n"
"v_mfma_f32_16x16x32_f16 v[144:147], v[68:71], v[52:55], 0\n"
"v_pk_mul_f16 v100, v84, v92 op_sel:[0,0] op_sel_hi:[1,0]\n"
"v_pk_mul_f16 v101, v85, v92 op_sel:[0,0] op_sel_hi:[1,0]\n"
"s_waitcnt lgkmcnt(2)\n"
"v_mfma_f32_16x16x32_f16 v[144:147], v[72:75], v[56:59], v[144:147]\n"
"v_pk_fma_f16 v100, v84, v92, v100 op_sel:[1,1,0] op_sel_hi:[0,1,1] neg_lo:[1,0,0]\n"
"v_pk_fma_f16 v101, v85, v92, v101 op_sel:[1,1,0] op_sel_hi:[0,1,1] neg_lo:[1,0,0]\n"
"s_waitcnt lgkmcnt(1)\n"
"v_mfma_f32_16x16x32_f16 v[144:147], v[76:79], v[60:63], v[144:147]\n"
"v_pk_mul_f16 v102, v86, v92 op_sel:[0,0] op_sel_hi:[1,0]\n"
"v_pk_mul_f16 v103, v87, v92 op_sel:[0,0] op_sel_hi:[1,0]\n"
"s_waitcnt lgkmcnt(0)\n"
"v_mfma_f32_16x16x32_f16 v[144:147], v[80:83], v[64:67], v[144:147]\n"
"s_setprio 0\n"
"v_pk_fma_f16 v102, v86, v92, v102 op_sel:[1,1,0] op_sel_hi:[0,1,1] neg_lo:[1,0,0]\n"
"v_pk_fma_f16 v103, v87, v92, v103 op_sel:[1,1,0] op_sel_hi:[0,1,1] neg_lo:[1,0,0]\n"
"ds_write2_b32 v152, v100, v101 offset0:0 offset1:68\n"
"v_pk_mul_f16 v100, v88, v92 op_sel:[0,0] op_sel_hi:[1,0]\n"
"v_pk_mul_f16 v101, v89, v92 op_sel:[0,0] op_sel_hi:[1,0]\n"
"v_pk_fma_f16 v100, v88, v92, v100 op_sel:[1,1,0] op_sel_hi:[0,1,1] neg_lo:[1,0,0]\n"
"v_pk_fma_f16 v101, v89, v92, v101 op_sel:[1,1,0] op_sel_hi:[0,1,1] neg_lo:[1,0,0]\n"
"ds_write2_b32 v152, v102, v103 offset0:136 offset1:204\n"
"v_pk_mul_f16 v102, v90, v92 op_sel:[0,0] op_sel_hi:[1,0]\n"
"v_pk_mul_f16 v103, v91, v92 op_sel:[0,0] op_sel_hi:[1,0]\n"
"v_pk_fma_f16 v102, v90, v92, v102 op_sel:[1,1,0] op_sel_hi:[0,1,1] neg_lo:[1,0,0]\n"
"v_pk_fma_f16 v103, v91, v92, v103 op_sel:[1,1,0] op_sel_hi:[0,1,1] neg_lo:[1,0,0]\n"
"ds_write2_b32 v153, v100, v101 offset0:0 offset1:68\n"
"v_pk_mul_f16 v100, v84, v93 op_sel:[0,0] op_sel_hi:[1,0]\n"
"v_pk_mul_f16 v101, v86, v93 op_sel:[0,0] op_sel_hi:[1,0]\n"
"v_pk_fma_f16 v100, v84, v93, v100 op_sel:[1,1,0] op_sel_hi:[0,1,1] neg_lo:[1,0,0]\n"
"v_pk_fma_f16 v101, v86, v93, v101 op_sel:[1,1,0] op_sel_hi:[0,1,1] neg_lo:[1,0,0]\n"
"ds_write2_b32 v153, v102, v103 offset0:136 offset1:204\n"
"v_pk_mul_f16 v102, v88, v93 op_sel:[0,0] op_sel_hi:[1,0]\n"
"v_pk_mul_f16 v103, v90, v93 op_sel:[0,0] op_sel_hi:[1,0]\n"
"v_pk_fma_f16 v102, v88, v93, v102 op_sel:[1,1,0] op_sel_hi:[0,1,1] neg_lo:[1,0,0]\n"
"v_pk_fma_f16 v103, v90, v93, v103 op_sel:[1,1,0] op_sel_hi:[0,1,1] neg_lo:[1,0,0]\n"
"ds_write2_b32 v154, v100, v101 offset0:0 offset1:68\n"
"v_pk_mul_f16 v100, v84, v94 op_sel:[0,0] op_sel_hi:[1,0]\n"
"v_pk_mul_f16 v101, v86, v94 op_sel:[0,0] op_sel_hi:[1,0]\n"
"v_pk_fma_f16 v100, v84, v94, v100 op_sel:[1,1,0] op_sel_hi:[0,1,1] neg_lo:[1,0,0]\n"
"v_pk_fma_f16 v101, v86, v94, v101 op_sel:[1,1,0] op_sel_hi:[0,1,1] neg_lo:[1,0,0]\n"
"ds_write2_b32 v154, v102, v103 offset0:136 offset1:204\n"
"v_pk_mul_f16 v102, v88, v94 op_sel:[0,0] op_sel_hi:[1,0]\n"
"v_pk_mul_f16 v103, v90, v94 op_sel:[0,0] op_sel_hi:[1,0]\n"
"v_pk_fma_f16 v102, v88, v94, v102 op_sel:[1,1,0] op_sel_hi:[0,1,1] neg_lo:[1,0,0]\n"
"v_pk_fma_f16 v103, v90, v94, v103 op_sel:[1,1,0] op_sel_hi:[0,1,1] neg_lo:[1,0,0]\n"
"ds_write2_b32 v155, v100, v101 offset0:0 offset1:68\n"
"ds_write2_b32 v155, v102, v103 offset0:136 offset1:204\n"
"ds_read_b128 v[68:71], v141 offset:0\n"
"ds_read_b128 v[72:75], v141 offset:64\n"
"ds_read_b128 v[76:79], v141 offset:128\n"
"ds_read_b128 v[80:83], v141 offset:192\n"
"v_pk_mul_f16 v100, v84, v95 op_sel:[0,0] op_sel_hi:[1,0]\n"
"v_pk_mul_f16 v101, v88, v95 op_sel:[0,0] op_sel_hi:[1,0]\n"
"v_pk_fma_f16 v100, v84, v95, v100 op_sel:[1,1,0] op_sel_hi:[0,1,1] neg_lo:[1,0,0]\n"
"v_pk_fma_f16 v101, v88, v95, v101 op_sel:[1,1,0] op_sel_hi:[0,1,1] neg_lo:[1,0,0]\n"
"v_pk_mul_f16 v102, v84, v96 op_sel:[0,0] op_sel_hi:[1,0]\n"
"v_pk_mul_f16 v103, v88, v96 op_sel:[0,0] op_sel_hi:[1,0]\n"
"v_pk_fma_f16 v102, v84, v96, v102 op_sel:[1,1,0] op_sel_hi:[0,1,1] neg_lo:[1,0,0]\n"
"v_pk_fma_f16 v103, v88, v96, v103 op_sel:[1,1,0] op_sel_hi:[0,1,1] neg_lo:[1,0,0]\n"
"v_pk_mul_f16 v104, v84, v97 op_sel:[0,0] op_sel_hi:[1,0]\n"
"v_pk_mul_f16 v105, v88, v97 op_sel:[0,0] op_sel_hi:[1,0]\n"
"v_pk_fma_f16 v104, v84, v97, v104 op_sel:[1,1,0] op_sel_hi:[0,1,1] neg_lo:[1,0,0]\n"
"v_pk_fma_f16 v105, v88, v97, v105 op_sel:[1,1,0] op_sel_hi:[0,1,1] neg_lo:[1,0,0]\n"
"v_pk_mul_f16 v106, v84, v98 op_sel:[0,0] op_sel_hi:[1,0]\n"
"v_pk_mul_f16 v107, v88, v98 op_sel:[0,0] op_sel_hi:[1,0]\n"
"v_pk_fma_f16 v106, v84, v98, v106 op_sel:[1,1,0] op_sel_hi:[0,1,1] neg_lo:[1,0,0]\n"
"v_pk_fma_f16 v107, v88, v98, v107 op_sel:[1,1,0] op_sel_hi:[0,1,1] neg_lo:[1,0,0]\n"
"v_pk_mul_f16 v108, v84, v99 op_sel:[0,0] op_sel_hi:[1,0]\n"
"v_pk_mul_f16 v109, v88, v99 op_sel:[0,0] op_sel_hi:[1,0]\n"
"v_pk_fma_f16 v108, v84, v99, v108 op_sel:[1,1,0] op_sel_hi:[0,1,1] neg_lo:[1,0,0]\n"
"v_pk_fma_f16 v109, v88, v99, v109 op_sel:[1,1,0] op_sel_hi:[0,1,1] neg_lo:[1,0,0]\n"
"s_setprio 3\n"
"s_waitcnt lgkmcnt(3)\n"
"v_mfma_f32_16x16x32_f16 v[148:151], v[68:71], v[52:55], 0\n"
"ds_write2_b32 v152, v100, v101 offset0:0 offset1:68\n"
"s_waitcnt lgkmcnt(3)\n"
"v_mfma_f32_16x16x32_f16 v[148:151], v[72:75], v[56:59], v[148:151]\n"
"ds_write2_b32 v152, v102, v103 offset0:136 offset1:204\n"
"s_waitcnt lgkmcnt(3)\n"
"v_mfma_f32_16x16x32_f16 v[148:151], v[76:79], v[60:63], v[148:151]\n"
"ds_write2_b32 v153, v104, v105 offset0:0 offset1:68\n"
"s_waitcnt lgkmcnt(3)\n"
"v_mfma_f32_16x16x32_f16 v[148:151], v[80:83], v[64:67], v[148:151]\n"
"s_setprio 0\n"
"ds_write2_b32 v153, v106, v107 offset0:136 offset1:204\n"
"ds_write2_b32 v154, v108, v109 offset0:0 offset1:68\n"
"ds_read_b128 v[68:71], v141 offset:0\n"
"ds_read_b128 v[72:75], v141 offset:64\n"
"ds_read_b128 v[76:79], v141 offset:128\n"
"ds_read_b128 v[80:83], v141 offset:192\n"
"v_fmac_f32 v128, s50, v144\n"
"v_fmac_f32 v129, s50, v145\n"
"v_fmac_f32 v130, s50, v146\n"
"v_fmac_f32 v131, s50, v147\n"
"v_fmac_f32 v132, s50, v148\n"
"v_fmac_f32 v133, s50, v149\n"
"v_fmac_f32 v134, s50, v150\n"
"v_fmac_f32 v135, s50, v151\n"
"s_setprio 3\n"
"s_waitcnt lgkmcnt(3)\n"
"v_mfma_f32_16x16x32_f16 v[48:51], v[68:71], v[52:55], 0\n"
"s_waitcnt lgkmcnt(2)\n"
"v_mfma_f32_16x16x32_f16 v[48:51], v[72:75], v[56:59], v[48:51]\n"
"s_waitcnt lgkmcnt(1)\n"
"v_mfma_f32_16x16x32_f16 v[48:51], v[76:79], v[60:63], v[48:51]\n"
"s_waitcnt lgkmcnt(0)\n"
"v_mfma_f32_16x16x32_f16 v[48:51], v[80:83], v[64:67], v[48:51]\n"
"s_setprio 0\n"
"s_sub_u32 s55, s55, 1\n"
"s_nop 5\n"
"v_fmac_f32 v136, s50, v48\n"
"v_fmac_f32 v137, s50, v49\n"
"v_fmac_f32 v138, s50, v50\n"
"v_fmac_f32 v139, s50, v51\n"
"s_cbranch_scc0 .Lgo_325\n"
"s_setpc_b64 s[60:61]\n"
".Lgo_325:\n"
"s_waitcnt vmcnt(0)\n"
"v_mov_b32 v32, v40\n"
"v_mov_b32 v40, v44\n"
"v_mov_b32 v33, v41\n"
"v_mov_b32 v41, v45\n"
"v_mov_b32 v34, v42\n"
"v_mov_b32 v42, v46\n"
"v_mov_b32 v35, v43\n"
"v_mov_b32 v43, v47\n"
"s_cmp_lg_u32 s55, 0\n"
"s_cbranch_scc1 .Lkg_324\n"
"s_mov_b32 s40, 0\n"
"s_mov_b32 s40, 1\n"
"v_lshlrev_b32 v32, 16, v32\n"
"v_lshlrev_b32 v33, 16, v33\n"
"v_lshlrev_b32 v34, 16, v34\n"
"v_lshlrev_b32 v35, 16, v35\n"
"v_mul_f32 v26, v142, v34\n"
"v_mul_f32 v27, v142, v35\n"
"v_max3_f32 v30, |v26|, |v27|, 0\n"
"s_nop 1\n"
"v_max_u32_dpp v30, v30, v30 row_ror:8 row_mask:0xf bank_mask:0xf\n"
"v_med3_f32 v32, v32, s52, v31\n"
"s_nop 0\n"
"v_max_u32_dpp v30, v30, v30 row_ror:4 row_mask:0xf bank_mask:0xf\n"
"v_mul_f32 v28, s53, v32\n"
"s_nop 0\n"
"v_max_u32_dpp v30, v30, v30 row_ror:2 row_mask:0xf bank_mask:0xf\n"
"v_exp_f32 v29, v28\n"
"s_nop 0\n"
"v_max_u32_dpp v30, v30, v30 row_ror:1 row_mask:0xf bank_mask:0xf\n"
"v_fma_f32 v28, v32, s53, -v28\n"
"v_fmac_f32 v28, 0x32a57060, v32\n"
"v_mov_b32 v34, v30\n"
"v_fma_f32 v28, v28, s54, 1.0\n"
"s_nop 0\n"
"v_permlane16_swap_b32 v30, v34\n"
"v_mul_f32 v32, v29, v28\n"
"v_max_u32 v30, v30, v34\n"
"v_cvt_pk_bf16_f32 v32, v32, v32\n"
"v_lshlrev_b32 v32, 16, v32\n"
"v_mov_b32 v34, v30\n"
"v_mul_f32 v25, v142, v33\n"
"s_nop 0\n"
"v_permlane32_swap_b32 v30, v34\n"
"v_mul_f32 v24, v142, -v32\n"
"v_max_u32 v30, v30, v34\n"
"v_mul_f32 v25, 0.15915494, v25\n"
"v_mul_f32 v24, s53, v24\n"
"v_readfirstlane_b32 s51, v30\n"
"s_nop 2\n"
"s_bfe_u32 s50, s51, 0x80017\n"
"s_max_i32 s50, s50, 37\n"
"s_min_i32 s50, s50, 217\n"
"s_cmp_gt_i32 s51, 0\n"
"s_cselect_b32 s50, s50, 127\n"
"s_sub_u32 s51, 267, s50\n"
"s_lshl_b32 s51, s51, 23\n"
"s_sub_u32 s50, s50, 27\n"
"s_lshl_b32 s50, s50, 23\n"
"v_mul_f32 v26, s51, v26\n"
"v_mul_f32 v27, s51, v27\n"
"v_mul_f32 v111, 0x41800000, v25\n"
"v_mul_f32 v100, 0x41800000, v24\n"
"v_fract_f32 v111, v111\n"
"v_exp_f32 v100, v100\n"
"v_cos_f32 v110, v111\n"
"v_sin_f32 v111, -v111\n"
"v_mul_f32 v110, v110, v100\n"
"v_mul_f32 v111, v111, v100\n"
"v_fract_f32 v113, v25\n"
"v_exp_f32 v100, v24\n"
"v_cos_f32 v112, v113\n"
"v_sin_f32 v113, v113\n"
"v_mul_f32 v112, v112, v100\n"
"v_mul_f32 v113, v113, v100\n"
"v_mul_f32 v115, 0x43800000, v25\n"
"v_mul_f32 v100, 0x43800000, v24\n"
"v_fract_f32 v115, v115\n"
"v_exp_f32 v100, v100\n"
"v_cos_f32 v114, v115\n"
"v_sin_f32 v115, v115\n"
"v_mul_f32 v114, v114, v100\n"
"v_mul_f32 v115, v115, v100\n"
"v_mul_f32 v116, v111, v111\n"
"v_add_f32 v117, v110, v110\n"
"v_fma_f32 v116, v110, v110, -v116\n"
"v_mul_f32 v117, v117, v111\n"
"v_mul_f32 v120, v117, v117\n"
"v_add_f32 v121, v116, v116\n"
"v_fma_f32 v120, v116, v116, -v120\n"
"v_mul_f32 v121, v121, v117\n"
"v_mul_f32 v118, v117, v111\n"
"v_mul_f32 v119, v116, v111\n"
"v_fma_f32 v118, v116, v110, -v118\n"
"v_fmac_f32 v119, v117, v110\n"
"v_mul_f32 v122, v121, v121\n"
"v_add_f32 v123, v120, v120\n"
"v_fma_f32 v122, v120, v120, -v122\n"
"v_mul_f32 v123, v123, v121\n"
"v_mov_b32 v84, 0x7400\n"
"v_mul_f32 v124, 0x46800000, v110\n"
"v_mul_f32 v125, 0x46800000, v111\n"
"v_cvt_pk_f16_f32 v100, v124, v125\n"
"v_mul_f32 v124, 0x46800000, v116\n"
"v_mul_f32 v125, 0x46800000, v117\n"
"v_cvt_pk_f16_f32 v85, v124, v125\n"
"v_mul_f32 v124, 0x46800000, v118\n"
"v_mul_f32 v125, 0x46800000, v119\n"
"v_cvt_pk_f16_f32 v101, v124, v125\n"
"v_cvt_pk_f16_f32 v93, v120, v121\n"
"v_cvt_pk_f16_f32 v94, v122, v123\n"
"v_mul_f32 v124, v123, v121\n"
"v_mul_f32 v125, v122, v121\n"
"v_fma_f32 v124, v122, v120, -v124\n"
"v_fmac_f32 v125, v123, v120\n"
"v_cvt_pk_f16_f32 v95, v124, v125\n"
"ds_write2_b32 v152, v84, v100 offset0:0 offset1:68\n"
"v_pk_mul_f16 v86, v84, v93 op_sel:[0,0] op_sel_hi:[1,0]\n"
"v_pk_mul_f16 v102, v100, v93 op_sel:[0,0] op_sel_hi:[1,0]\n"
"v_pk_fma_f16 v86, v84, v93, v86 op_sel:[1,1,0] op_sel_hi:[0,1,1] neg_lo:[1,0,0]\n"
"v_pk_fma_f16 v102, v100, v93, v102 op_sel:[1,1,0] op_sel_hi:[0,1,1] neg_lo:[1,0,0]\n"
"ds_write2_b32 v152, v85, v101 offset0:136 offset1:204\n"
"v_pk_mul_f16 v87, v85, v93 op_sel:[0,0] op_sel_hi:[1,0]\n"
"v_pk_mul_f16 v103, v101, v93 op_sel:[0,0] op_sel_hi:[1,0]\n"
"v_pk_fma_f16 v87, v85, v93, v87 op_sel:[1,1,0] op_sel_hi:[0,1,1] neg_lo:[1,0,0]\n"
"v_pk_fma_f16 v103, v101, v93, v103 op_sel:[1,1,0] op_sel_hi:[0,1,1] neg_lo:[1,0,0]\n"
"ds_write2_b32 v153, v86, v102 offset0:0 offset1:68\n"
"v_pk_mul_f16 v88, v84, v94 op_sel:[0,0] op_sel_hi:[1,0]\n"
"v_pk_mul_f16 v102, v100, v94 op_sel:[0,0] op_sel_hi:[1,0]\n"
"v_pk_fma_f16 v88, v84, v94, v88 op_sel:[1,1,0] op_sel_hi:[0,1,1] neg_lo:[1,0,0]\n"
"v_pk_fma_f16 v102, v100, v94, v102 op_sel:[1,1,0] op_sel_hi:[0,1,1] neg_lo:[1,0,0]\n"
"ds_write2_b32 v153, v87, v103 offset0:136 offset1:204\n"
"v_pk_mul_f16 v89, v85, v94 op_sel:[0,0] op_sel_hi:[1,0]\n"
"v_pk_mul_f16 v103, v101, v94 op_sel:[0,0] op_sel_hi:[1,0]\n"
"v_pk_fma_f16 v89, v85, v94, v89 op_sel:[1,1,0] op_sel_hi:[0,1,1] neg_lo:[1,0,0]\n"
"v_pk_fma_f16 v103, v101, v94, v103 op_sel:[1,1,0] op_sel_hi:[0,1,1] neg_lo:[1,0,0]\n"
"ds_write2_b32 v154, v88, v102 offset0:0 offset1:68\n"
"v_pk_mul_f16 v90, v84, v95 op_sel:[0,0] op_sel_hi:[1,0]\n"
"v_pk_mul_f16 v102, v100, v95 op_sel:[0,0] op_sel_hi:[1,0]\n"
"v_pk_fma_f16 v90, v84, v95, v90 op_sel:[1,1,0] op_sel_hi:[0,1,1] neg_lo:[1,0,0]\n"
"v_pk_fma_f16 v102, v100, v95, v102 op_sel:[1,1,0] op_sel_hi:[0,1,1] neg_lo:[1,0,0]\n"
"ds_write2_b32 v154, v89, v103 offset0:136 offset1:204\n"
"v_pk_mul_f16 v91, v85, v95 op_sel:[0,0] op_sel_hi:[1,0]\n"
"v_pk_mul_f16 v103, v101, v95 op_sel:[0,0] op_sel_hi:[1,0]\n"
"v_pk_fma_f16 v91, v85, v95, v91 op_sel:[1,1,0] op_sel_hi:[0,1,1] neg_lo:[1,0,0]\n"
"v_pk_fma_f16 v103, v101, v95, v103 op_sel:[1,1,0] op_sel_hi:[0,1,1] neg_lo:[1,0,0]\n"
"ds_write2_b32 v155, v90, v102 offset0:0 offset1:68\n"
"ds_write2_b32 v155, v91, v103 offset0:136 offset1:204\n"
"ds_read_b128 v[52:55], v141 offset:0\n"
"ds_read_b128 v[56:59], v141 offset:64\n"
"ds_read_b128 v[60:63], v141 offset:128\n"
"ds_read_b128 v[64:67], v141 offset:192\n"
"v_mul_f32 v116, v113, v113\n"
"v_add_f32 v117, v112, v112\n"
"v_fma_f32 v116, v112, v112, -v116\n"
"v_mul_f32 v117, v117, v113\n"
"v_mul_f32 v120, v117, v117\n"
"v_add_f32 v121, v116, v116\n"
"v_fma_f32 v120, v116, v116, -v120\n"
"v_mul_f32 v121, v121, v117\n"
"v_mul_f32 v118, v117, v113\n"
"s_setprio 3\n"
"buffer_load_dwordx4 v[44:47], v38, s[4:7], s33 offen sc1\n"
"s_setprio 0\n"
"v_mul_f32 v119, v116, v113\n"
"v_fma_f32 v118, v116, v112, -v118\n"
"v_fmac_f32 v119, v117, v112\n"
"v_mul_f32 v122, v121, v121\n"
"v_add_f32 v123, v120, v120\n"
"v_fma_f32 v122, v120, v120, -v122\n"
"v_mul_f32 v123, v123, v121\n"
"v_cvt_pk_f16_f32 v84, v26, v27\n"
"v_mul_f32 v124, v27, v113\n"
"v_mul_f32 v125, v26, v113\n"
"v_fma_f32 v124, v26, v112, -v124\n"
"v_fmac_f32 v125, v27, v112\n"
"v_cvt_pk_f16_f32 v100, v124, v125\n"
"v_mul_f32 v124, v27, v117\n"
"v_mul_f32 v125, v26, v117\n"
"v_fma_f32 v124, v26, v116, -v124\n"
"v_fmac_f32 v125, v27, v116\n"
"v_cvt_pk_f16_f32 v85, v124, v125\n"
"v_mul_f32 v124, v27, v119\n"
"v_mul_f32 v125, v26, v119\n"
"v_fma_f32 v124, v26, v118, -v124\n"
"v_fmac_f32 v125, v27, v118\n"
"v_cvt_pk_f16_f32 v101, v124, v125\n"
"v_cvt_pk_f16_f32 v93, v120, v121\n"
"v_cvt_pk_f16_f32 v94, v122, v123\n"
"v_mul_f32 v124, v123, v121\n"
"v_mul_f32 v125, v122, v121\n"
"v_fma_f32 v124, v122, v120, -v124\n"
"v_fmac_f32 v125, v123, v120\n"
"v_cvt_pk_f16_f32 v95, v124, v125\n"
"ds_write2_b32 v152, v84, v100 offset0:0 offset1:68\n"
"v_pk_mul_f16 v86, v84, v93 op_sel:[0,0] op_sel_hi:[1,0]\n"
"v_pk_mul_f16 v102, v100, v93 op_sel:[0,0] op_sel_hi:[1,0]\n"
"v_pk_fma_f16 v86, v84, v93, v86 op_sel:[1,1,0] op_sel_hi:[0,1,1] neg_lo:[1,0,0]\n"
"v_pk_fma_f16 v102, v100, v93, v102 op_sel:[1,1,0] op_sel_hi:[0,1,1] neg_lo:[1,0,0]\n"
"ds_write2_b32 v152, v85, v101 offset0:136 offset1:204\n"
"v_pk_mul_f16 v87, v85, v93 op_sel:[0,0] op_sel_hi:[1,0]\n"
"v_pk_mul_f16 v103, v101, v93 op_sel:[0,0] op_sel_hi:[1,0]\n"
"v_pk_fma_f16 v87, v85, v93, v87 op_sel:[1,1,0] op_sel_hi:[0,1,1] neg_lo:[1,0,0]\n"
"v_pk_fma_f16 v103, v101, v93, v103 op_sel:[1,1,0] op_sel_hi:[0,1,1] neg_lo:[1,0,0]\n"
"ds_write2_b32 v153, v86, v102 offset0:0 offset1:68\n"
"v_pk_mul_f16 v88, v84, v94 op_sel:[0,0] op_sel_hi:[1,0]\n"
"v_pk_mul_f16 v102, v100, v94 op_sel:[0,0] op_sel_hi:[1,0]\n"
"v_pk_fma_f16 v88, v84, v94, v88 op_sel:[1,1,0] op_sel_hi:[0,1,1] neg_lo:[1,0,0]\n"
"v_pk_fma_f16 v102, v100, v94, v102 op_sel:[1,1,0] op_sel_hi:[0,1,1] neg_lo:[1,0,0]\n"
"ds_write2_b32 v153, v87, v103 offset0:136 offset1:204\n"
"v_pk_mul_f16 v89, v85, v94 op_sel:[0,0] op_sel_hi:[1,0]\n"
"v_pk_mul_f16 v103, v101, v94 op_sel:[0,0] op_sel_hi:[1,0]\n"
"v_pk_fma_f16 v89, v85, v94, v89 op_sel:[1,1,0] op_sel_hi:[0,1,1] neg_lo:[1,0,0]\n"
"v_pk_fma_f16 v103, v101, v94, v103 op_sel:[1,1,0] op_sel_hi:[0,1,1] neg_lo:[1,0,0]\n"
"ds_write2_b32 v154, v88, v102 offset0:0 offset1:68\n"
"v_pk_mul_f16 v90, v84, v95 op_sel:[0,0] op_sel_hi:[1,0]\n"
"v_pk_mul_f16 v102, v100, v95 op_sel:[0,0] op_sel_hi:[1,0]\n"
"v_pk_fma_f16 v90, v84, v95, v90 op_sel:[1,1,0] op_sel_hi:[0,1,1] neg_lo:[1,0,0]\n"
"s_waitcnt vmcnt(0)\n"
"v_xor_b32 v44, s16, v44\n"
"v_xor_b32 v45, s16, v45\n"
"v_xor_b32 v46, s16, v46\n"
"v_xor_b32 v47, s16, v47\n"
"v_or_b32 v44, v44, v45\n"
"v_or3_b32 v44, v44, v46, v47\n"
"v_cmp_ne_u32 vcc, 0, v44\n"
"s_cbranch_vccnz .Lsl_320\n"
".Lbk_321:\n"
"v_pk_fma_f16 v102, v100, v95, v102 op_sel:[1,1,0] op_sel_hi:[0,1,1] neg_lo:[1,0,0]\n"
"ds_write2_b32 v154, v89, v103 offset0:136 offset1:204\n"
"v_pk_mul_f16 v91, v85, v95 op_sel:[0,0] op_sel_hi:[1,0]\n"
"v_pk_mul_f16 v103, v101, v95 op_sel:[0,0] op_sel_hi:[1,0]\n"
"v_pk_fma_f16 v91, v85, v95, v91 op_sel:[1,1,0] op_sel_hi:[0,1,1] neg_lo:[1,0,0]\n"
"v_pk_fma_f16 v103, v101, v95, v103 op_sel:[1,1,0] op_sel_hi:[0,1,1] neg_lo:[1,0,0]\n"
"buffer_load_dwordx4 v[156:159], v2, s[4:7], s32 offen offset:0 sc1\n"
"ds_write2_b32 v155, v90, v102 offset0:0 offset1:68\n"
"ds_write2_b32 v155, v91, v103 offset0:136 offset1:204\n"
"ds_read_b128 v[68:71], v141 offset:0\n"
"ds_read_b128 v[72:75], v141 offset:64\n"
"ds_read_b128 v[76:79], v141 offset:128\n"
"ds_read_b128 v[80:83], v141 offset:192\n"
"buffer_load_dwordx4 v[160:163], v2, s[4:7], s32 offen offset:1024 sc1\n"
"v_mul_f32 v116, v115, v115\n"
"v_add_f32 v117, v114, v114\n"
"v_fma_f32 v116, v114, v114, -v116\n"
"v_mul_f32 v117, v117, v115\n"
"v_mul_f32 v120, v117, v117\n"
"v_add_f32 v121, v116, v116\n"
"buffer_load_dwordx4 v[164:167], v2, s[4:7], s32 offen offset:2048 sc1\n"
"v_fma_f32 v120, v116, v116, -v120\n"
"v_mul_f32 v121, v121, v117\n"
"v_mul_f32 v118, v117, v115\n"
"v_mul_f32 v119, v116, v115\n"
"v_fma_f32 v118, v116, v114, -v118\n"
"v_fmac_f32 v119, v117, v114\n"
"buffer_load_dwordx4 v[168:171], v2, s[4:7], s32 offen offset:3072 sc1\n"
"v_cvt_pk_f16_f32 v92, v114, v115\n"
"v_cvt_pk_f16_f32 v93, v116, v117\n"
"v_cvt_pk_f16_f32 v95, v120, v121\n"
"v_cvt_pk_f16_f32 v94, v118, v119\n"
"v_mul_f32 v124, v121, v115\n"
"v_mul_f32 v125, v120, v115\n"
"s_add_u32 s32, s32, 4096\n"
"buffer_load_dwordx4 v[172:175], v2, s[4:7], s32 offen offset:0 sc1\n"
"v_fma_f32 v124, v120, v114, -v124\n"
"v_fmac_f32 v125, v121, v114\n"
"v_cvt_pk_f16_f32 v96, v124, v125\n"
"v_mul_f32 v126, v121, v117\n"
"v_mul_f32 v127, v120, v117\n"
"v_fma_f32 v126, v120, v116, -v126\n"
"buffer_load_dwordx4 v[176:179], v2, s[4:7], s32 offen offset:1024 sc1\n"
"v_fmac_f32 v127, v121, v116\n"
"v_cvt_pk_f16_f32 v97, v126, v127\n"
"v_mul_f32 v124, v121, v119\n"
"v_mul_f32 v125, v120, v119\n"
"v_fma_f32 v124, v120, v118, -v124\n"
"v_fmac_f32 v125, v121, v118\n"
"s_waitcnt vmcnt(5)\n"
"buffer_load_dwordx4 v[180:183], v2, s[4:7], s32 offen offset:2048 sc1\n"
"v_cvt_pk_f16_f32 v98, v124, v125\n"
"v_mul_f32 v126, v121, v121\n"
"v_mul_f32 v127, v120, v121\n"
"v_fma_f32 v126, v120, v120, -v126\n"
"v_fmac_f32 v127, v121, v120\n"
"v_cvt_pk_f16_f32 v99, v126, v127\n"
"s_waitcnt vmcnt(5)\n"
"buffer_load_dwordx4 v[184:187], v2, s[4:7], s32 offen offset:3072 sc1\n"
"s_setprio 3\n"
"s_waitcnt lgkmcnt(3)\n"
"v_mfma_f32_16x16x32_f16 v[144:147], v[68:71], v[52:55], 0\n"
"v_pk_mul_f16 v100, v84, v92 op_sel:[0,0] op_sel_hi:[1,0]\n"
"v_pk_mul_f16 v101, v85, v92 op_sel:[0,0] op_sel_hi:[1,0]\n"
"s_waitcnt lgkmcnt(2)\n"
"v_mfma_f32_16x16x32_f16 v[144:147], v[72:75], v[56:59], v[144:147]\n"
"v_pk_fma_f16 v100, v84, v92, v100 op_sel:[1,1,0] op_sel_hi:[0,1,1] neg_lo:[1,0,0]\n"
"s_add_u32 s32, s32, 4096\n"
"s_waitcnt vmcnt(5)\n"
"buffer_load_dwordx4 v[188:191], v2, s[4:7], s32 offen offset:0 sc1\n"
"v_pk_fma_f16 v101, v85, v92, v101 op_sel:[1,1,0] op_sel_hi:[0,1,1] neg_lo:[1,0,0]\n"
"s_waitcnt lgkmcnt(1)\n"
"v_mfma_f32_16x16x32_f16 v[144:147], v[76:79], v[60:63], v[144:147]\n"
"v_pk_mul_f16 v102, v86, v92 op_sel:[0,0] op_sel_hi:[1,0]\n"
"v_pk_mul_f16 v103, v87, v92 op_sel:[0,0] op_sel_hi:[1,0]\n"
"s_waitcnt lgkmcnt(0)\n"
"v_mfma_f32_16x16x32_f16 v[144:147], v[80:83], v[64:67], v[144:147]\n"
"s_setprio 0\n"
"s_waitcnt vmcnt(5)\n"
"buffer_load_dwordx4 v[192:195], v2, s[4:7], s32 offen offset:1024 sc1\n"
"v_pk_fma_f16 v102, v86, v92, v102 op_sel:[1,1,0] op_sel_hi:[0,1,1] neg_lo:[1,0,0]\n"
"v_pk_fma_f16 v103, v87, v92, v103 op_sel:[1,1,0] op_sel_hi:[0,1,1] neg_lo:[1,0,0]\n"
"ds_write2_b32 v152, v100, v101 offset0:0 offset1:68\n"
"v_pk_mul_f16 v100, v88, v92 op_sel:[0,0] op_sel_hi:[1,0]\n"
"v_pk_mul_f16 v101, v89, v92 op_sel:[0,0] op_sel_hi:[1,0]\n"
"v_pk_fma_f16 v100, v88, v92, v100 op_sel:[1,1,0] op_sel_hi:[0,1,1] neg_lo:[1,0,0]\n"
"s_waitcnt vmcnt(5)\n"
"buffer_load_dwordx4 v[196:199], v2, s[4:7], s32 offen offset:2048 sc1\n"
"v_pk_fma_f16 v101, v89, v92, v101 op_sel:[1,1,0] op_sel_hi:[0,1,1] neg_lo:[1,0,0]\n"
"ds_write2_b32 v152, v102, v103 offset0:136 offset1:204\n"
"v_pk_mul_f16 v102, v90, v92 op_sel:[0,0] op_sel_hi:[1,0]\n"
"v_pk_mul_f16 v103, v91, v92 op_sel:[0,0] op_sel_hi:[1,0]\n"
"v_pk_fma_f16 v102, v90, v92, v102 op_sel:[1,1,0] op_sel_hi:[0,1,1] neg_lo:[1,0,0]\n"
"v_pk_fma_f16 v103, v91, v92, v103 op_sel:[1,1,0] op_sel_hi:[0,1,1] neg_lo:[1,0,0]\n"
"s_waitcnt vmcnt(5)\n"
"buffer_load_dwordx4 v[200:203], v2, s[4:7], s32 offen offset:3072 sc1\n"
"ds_write2_b32 v153, v100, v101 offset0:0 offset1:68\n"
"v_pk_mul_f16 v100, v84, v93 op_sel:[0,0] op_sel_hi:[1,0]\n"
"v_pk_mul_f16 v101, v86, v93 op_sel:[0,0] op_sel_hi:[1,0]\n"
"v_pk_fma_f16 v100, v84, v93, v100 op_sel:[1,1,0] op_sel_hi:[0,1,1] neg_lo:[1,0,0]\n"
"v_pk_fma_f16 v101, v86, v93, v101 op_sel:[1,1,0] op_sel_hi:[0,1,1] neg_lo:[1,0,0]\n"
"ds_write2_b32 v153, v102, v103 offset0:136 offset1:204\n"
"s_add_u32 s32, s32, 4096\n"
"s_waitcnt vmcnt(5)\n"
"buffer_load_dwordx4 v[204:207], v2, s[4:7], s32 offen offset:0 sc1\n"
"v_pk_mul_f16 v102, v88, v93 op_sel:[0,0] op_sel_hi:[1,0]\n"
"v_pk_mul_f16 v103, v90, v93 op_sel:[0,0] op_sel_hi:[1,0]\n"
"v_pk_fma_f16 v102, v88, v93, v102 op_sel:[1,1,0] op_sel_hi:[0,1,1] neg_lo:[1,0,0]\n"
"v_pk_fma_f16 v103, v90, v93, v103 op_sel:[1,1,0] op_sel_hi:[0,1,1] neg_lo:[1,0,0]\n"
"ds_write2_b32 v154, v100, v101 offset0:0 offset1:68\n"
"v_pk_mul_f16 v100, v84, v94 op_sel:[0,0] op_sel_hi:[1,0]\n"
"s_waitcnt vmcnt(5)\n"
"buffer_load_dwordx4 v[208:211], v2, s[4:7], s32 offen offset:1024 sc1\n"
"v_pk_mul_f16 v101, v86, v94 op_sel:[0,0] op_sel_hi:[1,0]\n"
"v_pk_fma_f16 v100, v84, v94, v100 op_sel:[1,1,0] op_sel_hi:[0,1,1] neg_lo:[1,0,0]\n"
"v_pk_fma_f16 v101, v86, v94, v101 op_sel:[1,1,0] op_sel_hi:[0,1,1] neg_lo:[1,0,0]\n"
"ds_write2_b32 v154, v102, v103 offset0:136 offset1:204\n"
"v_pk_mul_f16 v102, v88, v94 op_sel:[0,0] op_sel_hi:[1,0]\n"
"v_pk_mul_f16 v103, v90, v94 op_sel:[0,0] op_sel_hi:[1,0]\n"
"s_waitcnt vmcnt(5)\n"
"buffer_load_dwordx4 v[212:215], v2, s[4:7], s32 offen offset:2048 sc1\n"
"v_pk_fma_f16 v102, v88, v94, v102 op_sel:[1,1,0] op_sel_hi:[0,1,1] neg_lo:[1,0,0]\n"
"v_pk_fma_f16 v103, v90, v94, v103 op_sel:[1,1,0] op_sel_hi:[0,1,1] neg_lo:[1,0,0]\n"
"ds_write2_b32 v155, v100, v101 offset0:0 offset1:68\n"
"ds_write2_b32 v155, v102, v103 offset0:136 offset1:204\n"
"ds_read_b128 v[68:71], v141 offset:0\n"
"ds_read_b128 v[72:75], v141 offset:64\n"
"s_waitcnt vmcnt(5)\n"
"buffer_load_dwordx4 v[216:219], v2, s[4:7], s32 offen offset:3072 sc1\n"
"ds_read_b128 v[76:79], v141 offset:128\n"
"ds_read_b128 v[80:83], v141 offset:192\n"
"v_pk_mul_f16 v100, v84, v95 op_sel:[0,0] op_sel_hi:[1,0]\n"
"v_pk_mul_f16 v101, v88, v95 op_sel:[0,0] op_sel_hi:[1,0]\n"
"v_pk_fma_f16 v100, v84, v95, v100 op_sel:[1,1,0] op_sel_hi:[0,1,1] neg_lo:[1,0,0]\n"
"v_pk_fma_f16 v101, v88, v95, v101 op_sel:[1,1,0] op_sel_hi:[0,1,1] neg_lo:[1,0,0]\n"
"s_add_u32 s32, s32, 4096\n"
"s_waitcnt vmcnt(5)\n"
"buffer_load_dwordx4 v[220:223], v2, s[4:7], s32 offen offset:0 sc1\n"
"v_pk_mul_f16 v102, v84, v96 op_sel:[0,0] op_sel_hi:[1,0]\n"
"v_pk_mul_f16 v103, v88, v96 op_sel:[0,0] op_sel_hi:[1,0]\n"
"v_pk_fma_f16 v102, v84, v96, v102 op_sel:[1,1,0] op_sel_hi:[0,1,1] neg_lo:[1,0,0]\n"
"v_pk_fma_f16 v103, v88, v96, v103 op_sel:[1,1,0] op_sel_hi:[0,1,1] neg_lo:[1,0,0]\n"
"v_pk_mul_f16 v104, v84, v97 op_sel:[0,0] op_sel_hi:[1,0]\n"
"v_pk_mul_f16 v105, v88, v97 op_sel:[0,0] op_sel_hi:[1,0]\n"
"s_waitcnt vmcnt(5)\n"
"buffer_load_dwordx4 v[224:227], v2, s[4:7], s32 offen offset:1024 sc1\n"
"v_pk_fma_f16 v104, v84, v97, v104 op_sel:[1,1,0] op_sel_hi:[0,1,1] neg_lo:[1,0,0]\n"
"v_pk_fma_f16 v105, v88, v97, v105 op_sel:[1,1,0] op_sel_hi:[0,1,1] neg_lo:[1,0,0]\n"
"v_pk_mul_f16 v106, v84, v98 op_sel:[0,0] op_sel_hi:[1,0]\n"
"v_pk_mul_f16 v107, v88, v98 op_sel:[0,0] op_sel_hi:[1,0]\n"
"v_pk_fma_f16 v106, v84, v98, v106 op_sel:[1,1,0] op_sel_hi:[0,1,1] neg_lo:[1,0,0]\n"
"v_pk_fma_f16 v107, v88, v98, v107 op_sel:[1,1,0] op_sel_hi:[0,1,1] neg_lo:[1,0,0]\n"
"s_waitcnt vmcnt(5)\n"
"buffer_load_dwordx4 v[228:231], v2, s[4:7], s32 offen offset:2048 sc1\n"
"v_pk_mul_f16 v108, v84, v99 op_sel:[0,0] op_sel_hi:[1,0]\n"
"v_pk_mul_f16 v109, v88, v99 op_sel:[0,0] op_sel_hi:[1,0]\n"
"v_pk_fma_f16 v108, v84, v99, v108 op_sel:[1,1,0] op_sel_hi:[0,1,1] neg_lo:[1,0,0]\n"
"v_pk_fma_f16 v109, v88, v99, v109 op_sel:[1,1,0] op_sel_hi:[0,1,1] neg_lo:[1,0,0]\n"
"s_setprio 3\n"
"s_waitcnt lgkmcnt(3)\n"
"v_mfma_f32_16x16x32_f16 v[148:151], v[68:71], v[52:55], 0\n"
"s_waitcnt vmcnt(5)\n"
"buffer_load_dwordx4 v[232:235], v2, s[4:7], s32 offen offset:3072 sc1\n"
"ds_write2_b32 v152, v100, v101 offset0:0 offset1:68\n"
"s_waitcnt lgkmcnt(3)\n"
"v_mfma_f32_16x16x32_f16 v[148:151], v[72:75], v[56:59], v[148:151]\n"
"ds_write2_b32 v152, v102, v103 offset0:136 offset1:204\n"
"s_waitcnt lgkmcnt(3)\n"
"v_mfma_f32_16x16x32_f16 v[148:151], v[76:79], v[60:63], v[148:151]\n"
"ds_write2_b32 v153, v104, v105 offset0:0 offset1:68\n"
"s_waitcnt lgkmcnt(3)\n"
"v_mfma_f32_16x16x32_f16 v[148:151], v[80:83], v[64:67], v[148:151]\n"
"s_add_u32 s32, s32, 4096\n"
"s_waitcnt vmcnt(5)\n"
"buffer_load_dwordx4 v[236:239], v2, s[4:7], s32 offen offset:0 sc1\n"
"s_setprio 0\n"
"ds_write2_b32 v153, v106, v107 offset0:136 offset1:204\n"
"ds_write2_b32 v154, v108, v109 offset0:0 offset1:68\n"
"ds_read_b128 v[68:71], v141 offset:0\n"
"ds_read_b128 v[72:75], v141 offset:64\n"
"ds_read_b128 v[76:79], v141 offset:128\n"
"s_waitcnt vmcnt(5)\n"
"buffer_load_dwordx4 v[240:243], v2, s[4:7], s32 offen offset:1024 sc1\n"
"ds_read_b128 v[80:83], v141 offset:192\n"
"v_fmac_f32 v128, s50, v144\n"
"v_fmac_f32 v129, s50, v145\n"
"v_fmac_f32 v130, s50, v146\n"
"v_fmac_f32 v131, s50, v147\n"
"v_fmac_f32 v132, s50, v148\n"
"s_waitcnt vmcnt(5)\n"
"buffer_load_dwordx4 v[244:247], v2, s[4:7], s32 offen offset:2048 sc1\n"
"v_fmac_f32 v133, s50, v149\n"
"v_fmac_f32 v134, s50, v150\n"
"v_fmac_f32 v135, s50, v151\n"
"s_setprio 3\n"
"s_waitcnt lgkmcnt(3)\n"
"v_mfma_f32_16x16x32_f16 v[48:51], v[68:71], v[52:55], 0\n"
"s_waitcnt lgkmcnt(2)\n"
"v_mfma_f32_16x16x32_f16 v[48:51], v[72:75], v[56:59], v[48:51]\n"
"s_waitcnt vmcnt(5)\n"
"buffer_load_dwordx4 v[248:251], v2, s[4:7], s32 offen offset:3072 sc1\n"
"s_waitcnt lgkmcnt(1)\n"
"v_mfma_f32_16x16x32_f16 v[48:51], v[76:79], v[60:63], v[48:51]\n"
"s_waitcnt lgkmcnt(0)\n"
"v_mfma_f32_16x16x32_f16 v[48:51], v[80:83], v[64:67], v[48:51]\n"
"s_setprio 0\n"
"s_nop 6\n"
"v_fmac_f32 v136, s50, v48\n"
"v_fmac_f32 v137, s50, v49\n"
"v_fmac_f32 v138, s50, v50\n"
"s_add_u32 s32, s32, 4096\n"
"s_waitcnt vmcnt(5)\n"
"buffer_load_dwordx4 v[252:255], v2, s[4:7], s32 offen offset:0 sc1\n"
"v_fmac_f32 v139, s50, v51\n"
"s_and_b32 s73, s3, 1\n"
"v_and_b32 v78, 15, v0\n"
"v_lshrrev_b32 v79, 4, v0\n"
"s_lshl_b32 s74, s73, 30\n"
"v_mul_i32_i24 v99, 0x500, v78\n"
"v_lshl_add_u32 v14, v79, 3, v99\n"
"v_lshrrev_b32 v99, 1, v78\n"
"v_and_b32 v98, 1, v78\n"
"v_lshlrev_b32 v98, 7, v98\n"
"v_lshl_add_u32 v98, v79, 8, v98\n"
"v_lshl_add_u32 v17, v99, 4, v98\n"
"s_mov_b32 s79, 0x25400\n"
"s_cmp_eq_i32 s3, 0\n"
"s_cselect_b32 s79, s79, 0x21000\n"
"s_mov_b32 s78, 0x25c00\n"
"s_cmp_eq_i32 s3, 2\n"
"s_cselect_b32 s78, s78, 0x23400\n"
"s_cmp_lt_i32 s3, 2\n"
"s_cselect_b32 s79, s79, s78\n"
"v_add_u32 v6, s79, v2\n"
"s_mov_b32 s79, 0x40000000\n"
"s_cmp_eq_i32 s3, 0\n"
"s_cselect_b32 s79, s79, 0x20c00\n"
"s_mov_b32 s78, 0x40000000\n"
"s_cmp_eq_i32 s3, 2\n"
"s_cselect_b32 s78, s78, 0x23000\n"
"s_cmp_lt_i32 s3, 2\n"
"s_cselect_b32 s79, s79, s78\n"
"v_add_u32 v7, s79, v2\n"
"s_mov_b32 s79, 0x23c00\n"
"s_cmp_eq_i32 s3, 0\n"
"s_cselect_b32 s79, s79, 0x1f200\n"
"s_mov_b32 s78, 0x21800\n"
"s_cmp_eq_i32 s3, 2\n"
"s_cselect_b32 s78, s78, 0x1ce00\n"
"s_cmp_lt_i32 s3, 2\n"
"s_cselect_b32 s79, s79, s78\n"
"v_add_u32 v8, s79, v2\n"
"s_mov_b32 s79, 0x20400\n"
"s_cmp_eq_i32 s3, 0\n"
"s_cselect_b32 s79, s79, 0x24c00\n"
"s_mov_b32 s78, 0x1e000\n"
"s_cmp_eq_i32 s3, 2\n"
"s_cselect_b32 s78, s78, 0x22800\n"
"s_cmp_lt_i32 s3, 2\n"
"s_cselect_b32 s79, s79, s78\n"
"v_add_u32 v9, s79, v2\n"
"s_mov_b32 s79, 0x24400\n"
"s_cmp_eq_i32 s3, 0\n"
"s_cselect_b32 s79, s79, 0x24800\n"
"s_mov_b32 s78, 0x22000\n"
"s_cmp_eq_i32 s3, 2\n"
"s_cselect_b32 s78, s78, 0x22400\n"
"s_cmp_lt_i32 s3, 2\n"
"s_cselect_b32 s79, s79, s78\n"
"v_add_u32 v10, s79, v2\n"
"v_add_u32 v15, s74, v14\n"
"v_lshlrev_b32 v99, 3, v78\n"
"s_mov_b32 vcc_lo, 0x500005\n"
"s_mov_b32 vcc_hi, 0x50000500\n"
"v_cndmask_b32 v16, 2.0, v99, vcc\n"
"v_add_u32 v18, s74, v17\n"
"s_sub_u32 s79, s73, 1\n"
"v_mov_b32 v19, s79\n"
"v_lshrrev_b32 v99, 1, v79\n"
"v_and_b32 v98, 1, v79\n"
"v_and_b32 v97, 4, v78\n"
"v_and_b32 v96, 3, v78\n"
"v_lshrrev_b32 v95, 3, v78\n"
"v_lshlrev_b32 v95, 6, v95\n"
"v_lshl_add_u32 v96, v96, 4, v95\n"
"v_lshl_add_u32 v97, v97, 1, v96\n"
"v_lshl_add_u32 v98, v98, 8, v97\n"
"v_lshl_add_u32 v99, v99, 7, v98\n"
"v_sub_u32 v39, v99, v14\n"
"s_mov_b32 s79, 0x20c00\n"
"s_cmp_eq_i32 s3, 0\n"
"s_cselect_b32 s79, s79, 0x25400\n"
"s_mov_b32 s78, 0x23000\n"
"s_cmp_eq_i32 s3, 2\n"
"s_cselect_b32 s78, s78, 0x25c00\n"
"s_cmp_lt_i32 s3, 2\n"
"s_cselect_b32 s79, s79, s78\n"
"v_add_u32 v11, s79, v2\n"
"s_mov_b32 s79, 0x21800\n"
"s_cmp_eq_i32 s3, 0\n"
"s_cselect_b32 s79, s79, 0x1ce00\n"
"s_mov_b32 s78, 0x23c00\n"
"s_cmp_eq_i32 s3, 2\n"
"s_cselect_b32 s78, s78, 0x1f200\n"
"s_cmp_lt_i32 s3, 2\n"
"s_cselect_b32 s79, s79, s78\n"
"v_add_u32 v12, s79, v2\n"
"s_mov_b32 s79, 0x22400\n"
"s_cmp_eq_i32 s3, 0\n"
"s_cselect_b32 s79, s79, 0x1e000\n"
"s_mov_b32 s78, 0x24800\n"
"s_cmp_eq_i32 s3, 2\n"
"s_cselect_b32 s78, s78, 0x20400\n"
"s_cmp_lt_i32 s3, 2\n"
"s_cselect_b32 s79, s79, s78\n"
"v_add_u32 v13, s79, v2\n"
"v_mov_b32 v20, 0x3f803f80\n"
"v_mov_b32 v21, 0x3f803f80\n"
"v_mov_b32 v22, 0x3f803f80\n"
"v_mov_b32 v23, 0x3f803f80\n"
"s_branch .Ljk_318\n"
".Lnk_317:\n"
"s_and_b32 s73, s3, 1\n"
"v_and_b32 v78, 15, v0\n"
"v_lshrrev_b32 v79, 4, v0\n"
"s_lshl_b32 s74, s73, 30\n"
"v_mul_i32_i24 v99, 0x500, v78\n"
"v_lshl_add_u32 v14, v79, 3, v99\n"
"v_lshrrev_b32 v99, 1, v78\n"
"v_and_b32 v98, 1, v78\n"
"v_lshlrev_b32 v98, 7, v98\n"
"v_lshl_add_u32 v98, v79, 8, v98\n"
"v_lshl_add_u32 v17, v99, 4, v98\n"
"s_mov_b32 s79, 0x25400\n"
"s_cmp_eq_i32 s3, 0\n"
"s_cselect_b32 s79, s79, 0x21000\n"
"s_mov_b32 s78, 0x25c00\n"
"s_cmp_eq_i32 s3, 2\n"
"s_cselect_b32 s78, s78, 0x23400\n"
"s_cmp_lt_i32 s3, 2\n"
"s_cselect_b32 s79, s79, s78\n"
"v_add_u32 v6, s79, v2\n"
"s_mov_b32 s79, 0x40000000\n"
"s_cmp_eq_i32 s3, 0\n"
"s_cselect_b32 s79, s79, 0x20c00\n"
"s_mov_b32 s78, 0x40000000\n"
"s_cmp_eq_i32 s3, 2\n"
"s_cselect_b32 s78, s78, 0x23000\n"
"s_cmp_lt_i32 s3, 2\n"
"s_cselect_b32 s79, s79, s78\n"
"v_add_u32 v7, s79, v2\n"
"s_mov_b32 s79, 0x23c00\n"
"s_cmp_eq_i32 s3, 0\n"
"s_cselect_b32 s79, s79, 0x1f200\n"
"s_mov_b32 s78, 0x21800\n"
"s_cmp_eq_i32 s3, 2\n"
"s_cselect_b32 s78, s78, 0x1ce00\n"
"s_cmp_lt_i32 s3, 2\n"
"s_cselect_b32 s79, s79, s78\n"
"v_add_u32 v8, s79, v2\n"
"s_mov_b32 s79, 0x20400\n"
"s_cmp_eq_i32 s3, 0\n"
"s_cselect_b32 s79, s79, 0x24c00\n"
"s_mov_b32 s78, 0x1e000\n"
"s_cmp_eq_i32 s3, 2\n"
"s_cselect_b32 s78, s78, 0x22800\n"
"s_cmp_lt_i32 s3, 2\n"
"s_cselect_b32 s79, s79, s78\n"
"v_add_u32 v9, s79, v2\n"
"s_mov_b32 s79, 0x24400\n"
"s_cmp_eq_i32 s3, 0\n"
"s_cselect_b32 s79, s79, 0x24800\n"
"s_mov_b32 s78, 0x22000\n"
"s_cmp_eq_i32 s3, 2\n"
"s_cselect_b32 s78, s78, 0x22400\n"
"s_cmp_lt_i32 s3, 2\n"
"s_cselect_b32 s79, s79, s78\n"
"v_add_u32 v10, s79, v2\n"
"v_add_u32 v15, s74, v14\n"
"v_lshlrev_b32 v99, 3, v78\n"
"s_mov_b32 vcc_lo, 0x500005\n"
"s_mov_b32 vcc_hi, 0x50000500\n"
"v_cndmask_b32 v16, 2.0, v99, vcc\n"
"v_add_u32 v18, s74, v17\n"
"s_sub_u32 s79, s73, 1\n"
"v_mov_b32 v19, s79\n"
"v_lshrrev_b32 v99, 1, v79\n"
"v_and_b32 v98, 1, v79\n"
"v_and_b32 v97, 4, v78\n"
"v_and_b32 v96, 3, v78\n"
"v_lshrrev_b32 v95, 3, v78\n"
"v_lshlrev_b32 v95, 6, v95\n"
"v_lshl_add_u32 v96, v96, 4, v95\n"
"v_lshl_add_u32 v97, v97, 1, v96\n"
"v_lshl_add_u32 v98, v98, 8, v97\n"
"v_lshl_add_u32 v99, v99, 7, v98\n"
"v_sub_u32 v39, v99, v14\n"
"s_mov_b32 s79, 0x20c00\n"
"s_cmp_eq_i32 s3, 0\n"
"s_cselect_b32 s79, s79, 0x25400\n"
"s_mov_b32 s78, 0x23000\n"
"s_cmp_eq_i32 s3, 2\n"
"s_cselect_b32 s78, s78, 0x25c00\n"
"s_cmp_lt_i32 s3, 2\n"
"s_cselect_b32 s79, s79, s78\n"
"v_add_u32 v11, s79, v2\n"
"s_mov_b32 s79, 0x21800\n"
"s_cmp_eq_i32 s3, 0\n"
"s_cselect_b32 s79, s79, 0x1ce00\n"
"s_mov_b32 s78, 0x23c00\n"
"s_cmp_eq_i32 s3, 2\n"
"s_cselect_b32 s78, s78, 0x1f200\n"
"s_cmp_lt_i32 s3, 2\n"
"s_cselect_b32 s79, s79, s78\n"
"v_add_u32 v12, s79, v2\n"
"s_mov_b32 s79, 0x22400\n"
"s_cmp_eq_i32 s3, 0\n"
"s_cselect_b32 s79, s79, 0x1e000\n"
"s_mov_b32 s78, 0x24800\n"
"s_cmp_eq_i32 s3, 2\n"
"s_cselect_b32 s78, s78, 0x20400\n"
"s_cmp_lt_i32 s3, 2\n"
"s_cselect_b32 s79, s79, s78\n"
"v_add_u32 v13, s79, v2\n"
"v_mov_b32 v20, 0x3f803f80\n"
"v_mov_b32 v21, 0x3f803f80\n"
"v_mov_b32 v22, 0x3f803f80\n"
"v_mov_b32 v23, 0x3f803f80\n"
"s_mov_b32 s17, 0x100000\n"
".Ltp_326:\n"
"s_sleep 2\n"
"ds_read_b32 v3, v5 offset:16\n"
"s_sub_u32 s17, s17, 1\n"
"s_cbranch_scc1 .Lou_327\n"
"s_waitcnt lgkmcnt(0)\n"
"v_cmp_eq_u32 vcc, 0, v3\n"
"s_cbranch_vccnz .Ltp_326\n"
".Lou_327:\n"
"s_waitcnt lgkmcnt(0)\n"
"s_nop 2\n"
"s_mov_b32 s17, 32768\n"
".Ltp_328:\n"
"buffer_load_dwordx4 v[40:43], v38, s[4:7], s33 offen sc1\n"
"s_sub_u32 s17, s17, 1\n"
"s_cbranch_scc1 .Lou_329\n"
"s_waitcnt vmcnt(0)\n"
"v_xor_b32 v40, s16, v40\n"
"v_xor_b32 v41, s16, v41\n"
"v_xor_b32 v42, s16, v42\n"
"v_xor_b32 v43, s16, v43\n"
"v_or_b32 v40, v40, v41\n"
"v_or3_b32 v40, v40, v42, v43\n"
"v_cmp_ne_u32 vcc, 0, v40\n"
"s_cbranch_vccz .Lou_329\n"
"s_sleep 1\n"
"s_branch .Ltp_328\n"
".Lou_329:\n"
"s_waitcnt vmcnt(0)\n"
".Lpl_319:\n"
"s_mov_b32 s17, 0x100000\n"
".Ltp_330:\n"
"s_sleep 2\n"
"ds_read_b32 v3, v5 offset:32\n"
"s_sub_u32 s17, s17, 1\n"
"s_cbranch_scc1 .Lou_331\n"
"s_waitcnt lgkmcnt(0)\n"
"v_cmp_eq_u32 vcc, 0, v3\n"
"s_cbranch_vccnz .Ltp_330\n"
".Lou_331:\n"
"s_waitcnt lgkmcnt(0)\n"
"buffer_load_dwordx4 v[156:159], v2, s[4:7], s32 offen offset:0 sc1\n"
"buffer_load_dwordx4 v[160:163], v2, s[4:7], s32 offen offset:1024 sc1\n"
"buffer_load_dwordx4 v[164:167], v2, s[4:7], s32 offen offset:2048 sc1\n"
"buffer_load_dwordx4 v[168:171], v2, s[4:7], s32 offen offset:3072 sc1\n"
"s_add_u32 s32, s32, 4096\n"
"buffer_load_dwordx4 v[172:175], v2, s[4:7], s32 offen offset:0 sc1\n"
"buffer_load_dwordx4 v[176:179], v2, s[4:7], s32 offen offset:1024 sc1\n"
"s_waitcnt vmcnt(5)\n"
"buffer_load_dwordx4 v[180:183], v2, s[4:7], s32 offen offset:2048 sc1\n"
"s_waitcnt vmcnt(5)\n"
"buffer_load_dwordx4 v[184:187], v2, s[4:7], s32 offen offset:3072 sc1\n"
"s_add_u32 s32, s32, 4096\n"
"s_waitcnt vmcnt(5)\n"
"buffer_load_dwordx4 v[188:191], v2, s[4:7], s32 offen offset:0 sc1\n"
"s_waitcnt vmcnt(5)\n"
"buffer_load_dwordx4 v[192:195], v2, s[4:7], s32 offen offset:1024 sc1\n"
"s_waitcnt vmcnt(5)\n"
"buffer_load_dwordx4 v[196:199], v2, s[4:7], s32 offen offset:2048 sc1\n"
"s_waitcnt vmcnt(5)\n"
"buffer_load_dwordx4 v[200:203], v2, s[4:7], s32 offen offset:3072 sc1\n"
"s_add_u32 s32, s32, 4096\n"
"s_waitcnt vmcnt(5)\n"
"buffer_load_dwordx4 v[204:207], v2, s[4:7], s32 offen offset:0 sc1\n"
"s_waitcnt vmcnt(5)\n"
"buffer_load_dwordx4 v[208:211], v2, s[4:7], s32 offen offset:1024 sc1\n"
"s_waitcnt vmcnt(5)\n"
"buffer_load_dwordx4 v[212:215], v2, s[4:7], s32 offen offset:2048 sc1\n"
"s_waitcnt vmcnt(5)\n"
"buffer_load_dwordx4 v[216:219], v2, s[4:7], s32 offen offset:3072 sc1\n"
"s_add_u32 s32, s32, 4096\n"
"s_waitcnt vmcnt(5)\n"
"buffer_load_dwordx4 v[220:223], v2, s[4:7], s32 offen offset:0 sc1\n"
"s_waitcnt vmcnt(5)\n"
"buffer_load_dwordx4 v[224:227], v2, s[4:7], s32 offen offset:1024 sc1\n"
"s_waitcnt vmcnt(5)\n"
"buffer_load_dwordx4 v[228:231], v2, s[4:7], s32 offen offset:2048 sc1\n"
"s_waitcnt vmcnt(5)\n"
"buffer_load_dwordx4 v[232:235], v2, s[4:7], s32 offen offset:3072 sc1\n"
"s_add_u32 s32, s32, 4096\n"
"s_waitcnt vmcnt(5)\n"
"buffer_load_dwordx4 v[236:239], v2, s[4:7], s32 offen offset:0 sc1\n"
"s_waitcnt vmcnt(5)\n"
"buffer_load_dwordx4 v[240:243], v2, s[4:7], s32 offen offset:1024 sc1\n"
"s_waitcnt vmcnt(5)\n"
"buffer_load_dwordx4 v[244:247], v2, s[4:7], s32 offen offset:2048 sc1\n"
"s_waitcnt vmcnt(5)\n"
"buffer_load_dwordx4 v[248:251], v2, s[4:7], s32 offen offset:3072 sc1\n"
"s_add_u32 s32, s32, 4096\n"
"s_waitcnt vmcnt(5)\n"
"buffer_load_dwordx4 v[252:255], v2, s[4:7], s32 offen offset:0 sc1\n"
".Ljk_318:\n"
"s_cmp_lt_i32 s18, 0\n"
"s_cselect_b32 s43, 3, 0\n"
"s_setprio 2\n"
"v_mov_b32 v40, -1\n"
"v_mov_b32 v41, -1\n"
"v_mov_b32 v42, -1\n"
"v_mov_b32 v43, -1\n"
"s_lshl_b32 s79, s2, 2\n"
"s_add_u32 s79, s79, s3\n"
"s_lshl_b32 s79, s79, 10\n"
"s_add_u32 s49, s79, 0xc28000\n"
"buffer_store_dwordx4 v[40:43], v2, s[4:7], s49 offen sc1\n"
"s_add_u32 s49, s49, 0x100000\n"
"buffer_store_dwordx4 v[40:43], v2, s[4:7], s49 offen sc1\n"
"s_add_u32 s49, s49, 0x100000\n"
"s_sub_u32 s79, 0xea7fff, s49\n"
"s_and_b32 s79, s79, 0x80000000\n"
"s_or_b32 s49, s49, s79\n"
"buffer_store_dwordx4 v[40:43], v2, s[4:7], s49 offen sc1\n"
"s_nop 1\n"
"s_setprio 0\n"
"s_cmp_lg_u32 s43, 0\n"
"s_cbranch_scc1 .Lsk_350\n"
"ds_read_b32 v3, v5 offset:0\n"
"s_waitcnt lgkmcnt(0)\n"
"v_cmp_eq_u32 vcc, 0, v3\n"
"s_cbranch_vccz .Len_349\n"
"s_call_b64 s[46:47], .Lst_332\n"
".Lsk_350:\n"
"s_cmp_lg_u32 s43, 1\n"
"s_cbranch_scc1 .Lsk_351\n"
"ds_read_b32 v3, v5 offset:128\n"
"s_waitcnt lgkmcnt(0)\n"
"v_cmp_eq_u32 vcc, 0, v3\n"
"s_cbranch_vccz .Len_349\n"
"s_call_b64 s[46:47], .Lst_333\n"
".Lsk_351:\n"
"s_cmp_lg_u32 s43, 2\n"
"s_cbranch_scc1 .Lsk_352\n"
"ds_read_b32 v3, v5 offset:128\n"
"s_waitcnt lgkmcnt(0)\n"
"v_cmp_eq_u32 vcc, 0, v3\n"
"s_cbranch_vccz .Len_349\n"
"s_call_b64 s[46:47], .Lst_334\n"
".Lsk_352:\n"
".Len_349:\n"
"s_setprio 2\n"
"s_mul_i32 s79, s28, 0xa0\n"
"s_add_u32 s79, s30, s79\n"
"s_and_b32 s78, s3, 1\n"
"s_mul_i32 s78, s78, 0x60\n"
"s_add_u32 s36, s79, s78\n"
"s_mul_i32 s79, s3, 0x1400\n"
"s_add_u32 s79, s30, s79\n"
"s_add_u32 s32, s79, 0x448000\n"
"s_add_u32 s33, s32, 0x1000\n"
"s_lshl_b32 s79, s29, 1\n"
"s_lshr_b32 s78, s3, 1\n"
"s_xor_b32 s78, s78, 1\n"
"s_add_u32 s79, s79, s78\n"
"s_mul_i32 s79, s79, 0x5000\n"
"s_mul_i32 s78, s3, 0x1400\n"
"s_add_u32 s79, s79, s78\n"
"s_add_u32 s34, s79, 0x448000\n"
"s_add_u32 s35, s34, 0x1000\n"
"s_lshl_b32 s79, s30, 1\n"
"s_mul_i32 s78, s28, 0x1400\n"
"s_add_u32 s79, s79, s78\n"
"s_and_b32 s78, s3, 1\n"
"s_mul_i32 s78, s78, 0xc00\n"
"s_add_u32 s79, s79, s78\n"
"s_add_u32 s37, s79, 0x6c8000\n"
"s_lshl_b32 s79, s29, 1\n"
"s_lshr_b32 s78, s3, 1\n"
"s_add_u32 s79, s79, s78\n"
"s_lshl_b32 s79, s79, 11\n"
"s_lshl_b32 s78, s28, 8\n"
"s_add_u32 s79, s79, s78\n"
"s_and_b32 s78, s3, 1\n"
"s_lshl_b32 s78, s78, 7\n"
"s_add_u32 s79, s79, s78\n"
"s_add_u32 s38, s79, 0xbc8000\n"
"buffer_load_dwordx2 v[40:41], v14, s[8:11], s36 offen offset:0\n"
"buffer_load_dwordx2 v[42:43], v14, s[8:11], s36 offen offset:32\n"
"buffer_load_dwordx2 v[44:45], v15, s[8:11], s36 offen offset:64\n"
"s_mov_b32 s17, 0x100000\n"
".Ltp_353:\n"
"s_sleep 2\n"
"ds_read_b32 v3, v5 offset:96\n"
"s_sub_u32 s17, s17, 1\n"
"s_cbranch_scc1 .Lou_354\n"
"s_waitcnt lgkmcnt(0)\n"
"v_cmp_eq_u32 vcc, 0, v3\n"
"s_cbranch_vccnz .Ltp_353\n"
".Lou_354:\n"
"s_waitcnt lgkmcnt(0)\n"
"s_cmp_eq_u32 s17, 0xfffff\n"
"s_cbranch_scc1 .Lsk_355\n"
".Lsk_355:\n"
"v_lshlrev_b32 v38, 5, v0\n"
"s_waitcnt vmcnt(0)\n"
"s_nop 3\n"
"s_mov_b32 s17, 32768\n"
".Ltp_357:\n"
"buffer_load_dwordx4 v[88:91], v38, s[4:7], s48 offen sc1\n"
"s_sub_u32 s17, s17, 1\n"
"s_cbranch_scc1 .Lou_358\n"
"s_waitcnt vmcnt(0)\n"
"v_xor_b32 v88, s16, v88\n"
"v_xor_b32 v89, s16, v89\n"
"v_xor_b32 v90, s16, v90\n"
"v_xor_b32 v91, s16, v91\n"
"v_or_b32 v88, v88, v89\n"
"v_or3_b32 v88, v88, v90, v91\n"
"v_cmp_ne_u32 vcc, 0, v88\n"
"s_cbranch_vccz .Lou_358\n"
"s_mov_b32 s89, 6\n"
".Ldl_359:\n"
"s_sub_u32 s89, s89, 1\n"
"s_cbranch_scc1 .Lde_360\n"
"s_sleep 1\n"
"s_branch .Ldl_359\n"
".Lde_360:\n"
"s_branch .Ltp_357\n"
".Lou_358:\n"
"s_waitcnt vmcnt(0)\n"
"v_lshlrev_b32 v48, 16, v40\n"
"v_and_b32 v49, 0xffff0000, v40\n"
"v_lshlrev_b32 v50, 16, v41\n"
"v_and_b32 v51, 0xffff0000, v41\n"
"v_lshlrev_b32 v52, 16, v42\n"
"v_and_b32 v53, 0xffff0000, v42\n"
"v_lshlrev_b32 v54, 16, v43\n"
"v_and_b32 v55, 0xffff0000, v43\n"
"v_lshlrev_b32 v56, 16, v44\n"
"v_and_b32 v57, 0xffff0000, v44\n"
"v_lshlrev_b32 v58, 16, v45\n"
"v_and_b32 v59, 0xffff0000, v45\n"
"v_mov_b32 v46, 0\n"
"v_mov_b32 v47, 0\n"
"s_mov_b32 s17, 0x100000\n"
".Ltp_361:\n"
"s_sleep 1\n"
"ds_read_b32 v3, v5 offset:128\n"
"s_sub_u32 s17, s17, 1\n"
"s_cbranch_scc1 .Lou_362\n"
"s_waitcnt lgkmcnt(0)\n"
"v_cmp_eq_u32 vcc, 0, v3\n"
"s_cbranch_vccnz .Ltp_361\n"
".Lou_362:\n"
"s_waitcnt lgkmcnt(0)\n"
"s_cmp_eq_u32 s17, 0xfffff\n"
"s_cbranch_scc1 .Lsk_363\n"
"s_mov_b32 s89, 18\n"
".Ldl_365:\n"
"s_sub_u32 s89, s89, 1\n"
"s_cbranch_scc1 .Lde_366\n"
"s_sleep 1\n"
"s_branch .Ldl_365\n"
".Lde_366:\n"
"s_branch .Ljn_364\n"
".Lsk_363:\n"
".Ljn_364:\n"
"buffer_load_dwordx4 v[108:111], v2, s[4:7], s34 offen offset:0 sc1\n"
"s_nop 9\n"
"buffer_load_dwordx4 v[112:115], v2, s[4:7], s34 offen offset:1024 sc1\n"
"s_nop 9\n"
"buffer_load_dwordx4 v[116:119], v2, s[4:7], s34 offen offset:2048 sc1\n"
"s_nop 9\n"
"buffer_load_dwordx4 v[120:123], v2, s[4:7], s34 offen offset:3072 sc1\n"
"s_nop 9\n"
"buffer_load_dwordx4 v[124:127], v2, s[4:7], s35 offen offset:0 sc1\n"
"s_nop 9\n"
"buffer_load_dwordx4 v[88:91], v2, s[4:7], s32 offen offset:0 sc1\n"
"s_nop 9\n"
"buffer_load_dwordx4 v[92:95], v2, s[4:7], s32 offen offset:1024 sc1\n"
"s_nop 9\n"
"buffer_load_dwordx4 v[96:99], v2, s[4:7], s32 offen offset:2048 sc1\n"
"s_nop 9\n"
"buffer_load_dwordx4 v[100:103], v2, s[4:7], s32 offen offset:3072 sc1\n"
"s_nop 9\n"
"buffer_load_dwordx4 v[104:107], v2, s[4:7], s33 offen offset:0 sc1\n"
"s_waitcnt vmcnt(9)\n"
"v_max_u32 v38, v108, v109\n"
"v_max3_u32 v38, v110, v111, v38\n"
"v_cmp_eq_u32 vcc, -1, v38\n"
"s_cbranch_vccnz .Lsl_367\n"
".Lbk_368:\n"
"v_mfma_f32_16x16x32_bf16 v[68:71], v[156:159], v[108:111], 0\n"
"v_mfma_f32_16x16x32_bf16 v[72:75], v[160:163], v[108:111], 0\n"
"v_mfma_f32_16x16x32_bf16 v[76:79], v[164:167], v[108:111], 0\n"
"s_waitcnt vmcnt(8)\n"
"v_max_u32 v38, v112, v113\n"
"v_max3_u32 v38, v114, v115, v38\n"
"v_mfma_f32_16x16x32_bf16 v[80:83], v[168:171], v[108:111], 0\n"
"v_cmp_eq_u32 vcc, -1, v38\n"
"v_mfma_f32_16x16x32_bf16 v[84:87], v[172:175], v[108:111], 0\n"
"s_cbranch_vccnz .Lsl_369\n"
".Lbk_370:\n"
"v_mfma_f32_16x16x32_bf16 v[68:71], v[176:179], v[112:115], v[68:71]\n"
"v_mfma_f32_16x16x32_bf16 v[72:75], v[180:183], v[112:115], v[72:75]\n"
"v_mfma_f32_16x16x32_bf16 v[76:79], v[184:187], v[112:115], v[76:79]\n"
"s_waitcnt vmcnt(7)\n"
"v_max_u32 v38, v116, v117\n"
"v_max3_u32 v38, v118, v119, v38\n"
"v_mfma_f32_16x16x32_bf16 v[80:83], v[188:191], v[112:115], v[80:83]\n"
"v_cmp_eq_u32 vcc, -1, v38\n"
"v_mfma_f32_16x16x32_bf16 v[84:87], v[192:195], v[112:115], v[84:87]\n"
"s_cbranch_vccnz .Lsl_371\n"
".Lbk_372:\n"
"v_mfma_f32_16x16x32_bf16 v[68:71], v[196:199], v[116:119], v[68:71]\n"
"v_mfma_f32_16x16x32_bf16 v[72:75], v[200:203], v[116:119], v[72:75]\n"
"v_mfma_f32_16x16x32_bf16 v[76:79], v[204:207], v[116:119], v[76:79]\n"
"s_waitcnt vmcnt(6)\n"
"v_max_u32 v38, v120, v121\n"
"v_max3_u32 v38, v122, v123, v38\n"
"v_mfma_f32_16x16x32_bf16 v[80:83], v[208:211], v[116:119], v[80:83]\n"
"v_cmp_eq_u32 vcc, -1, v38\n"
"v_mfma_f32_16x16x32_bf16 v[84:87], v[212:215], v[116:119], v[84:87]\n"
"s_cbranch_vccnz .Lsl_373\n"
".Lbk_374:\n"
"v_mfma_f32_16x16x32_bf16 v[68:71], v[216:219], v[120:123], v[68:71]\n"
"v_mfma_f32_16x16x32_bf16 v[72:75], v[220:223], v[120:123], v[72:75]\n"
"v_mfma_f32_16x16x32_bf16 v[76:79], v[224:227], v[120:123], v[76:79]\n"
"s_waitcnt vmcnt(5)\n"
"v_max_u32 v38, v124, v125\n"
"v_max3_u32 v38, v126, v127, v38\n"
"v_mfma_f32_16x16x32_bf16 v[80:83], v[228:231], v[120:123], v[80:83]\n"
"v_cmp_eq_u32 vcc, -1, v38\n"
"v_mfma_f32_16x16x32_bf16 v[84:87], v[232:235], v[120:123], v[84:87]\n"
"s_cbranch_vccnz .Lsl_375\n"
".Lbk_376:\n"
"v_mfma_f32_16x16x32_bf16 v[68:71], v[236:239], v[124:127], v[68:71]\n"
"v_mfma_f32_16x16x32_bf16 v[72:75], v[240:243], v[124:127], v[72:75]\n"
"v_mfma_f32_16x16x32_bf16 v[76:79], v[244:247], v[124:127], v[76:79]\n"
"s_waitcnt vmcnt(4)\n"
"v_max_u32 v38, v88, v89\n"
"v_max3_u32 v38, v90, v91, v38\n"
"v_mfma_f32_16x16x32_bf16 v[80:83], v[248:251], v[124:127], v[80:83]\n"
"v_cmp_eq_u32 vcc, -1, v38\n"
"v_mfma_f32_16x16x32_bf16 v[84:87], v[252:255], v[124:127], v[84:87]\n"
"ds_write_b128 v8, v[68:71] offset:0\n"
"s_cbranch_vccnz .Lsl_377\n"
".Lbk_378:\n"
"v_mfma_f32_16x16x32_bf16 v[56:59], v[164:167], v[88:91], v[56:59]\n"
"s_waitcnt vmcnt(3)\n"
"v_max_u32 v38, v92, v93\n"
"v_max3_u32 v38, v94, v95, v38\n"
"ds_write_b128 v8, v[72:75] offset:1024\n"
"v_mfma_f32_16x16x32_bf16 v[60:63], v[168:171], v[88:91], 0\n"
"v_cmp_eq_u32 vcc, -1, v38\n"
"ds_write_b128 v10, v[76:79] offset:0\n"
"v_mfma_f32_16x16x32_bf16 v[64:67], v[172:175], v[88:91], 0\n"
"v_mov_b32 v117, s16\n"
"s_cbranch_vccnz .Lsl_379\n"
".Lbk_380:\n"
"v_mfma_f32_16x16x32_bf16 v[56:59], v[184:187], v[92:95], v[56:59]\n"
"s_waitcnt vmcnt(2)\n"
"v_max_u32 v38, v96, v97\n"
"v_max3_u32 v38, v98, v99, v38\n"
"v_mov_b32 v119, s16\n"
"v_mfma_f32_16x16x32_bf16 v[60:63], v[188:191], v[92:95], v[60:63]\n"
"v_cmp_eq_u32 vcc, -1, v38\n"
"ds_write_b128 v9, v[80:83] offset:0\n"
"v_mfma_f32_16x16x32_bf16 v[64:67], v[192:195], v[92:95], v[64:67]\n"
"ds_write_b128 v9, v[84:87] offset:1024\n"
"s_cbranch_vccnz .Lsl_381\n"
".Lbk_382:\n"
"v_mfma_f32_16x16x32_bf16 v[56:59], v[204:207], v[96:99], v[56:59]\n"
"s_waitcnt vmcnt(1)\n"
"v_max_u32 v38, v100, v101\n"
"v_max3_u32 v38, v102, v103, v38\n"
"ds_write_b32 v4, v4 offset:592\n"
"v_mfma_f32_16x16x32_bf16 v[60:63], v[208:211], v[96:99], v[60:63]\n"
"v_cmp_eq_u32 vcc, -1, v38\n"
"v_mov_b32 v121, s16\n"
"v_mov_b32 v123, s16\n"
"v_mfma_f32_16x16x32_bf16 v[64:67], v[212:215], v[96:99], v[64:67]\n"
"v_mov_b32 v125, s16\n"
"s_cbranch_vccnz .Lsl_383\n"
".Lbk_384:\n"
"v_mfma_f32_16x16x32_bf16 v[56:59], v[224:227], v[100:103], v[56:59]\n"
"s_waitcnt vmcnt(0)\n"
"v_max_u32 v38, v104, v105\n"
"v_max3_u32 v38, v106, v107, v38\n"
"v_mov_b32 v127, s16\n"
"v_mfma_f32_16x16x32_bf16 v[60:63], v[228:231], v[100:103], v[60:63]\n"
"v_cmp_eq_u32 vcc, -1, v38\n"
"ds_read_b32 v3, v5 offset:592\n"
"v_mfma_f32_16x16x32_bf16 v[64:67], v[232:235], v[100:103], v[64:67]\n"
"s_cbranch_vccnz .Lsl_385\n"
".Lbk_386:\n"
"v_mfma_f32_16x16x32_bf16 v[56:59], v[244:247], v[104:107], v[56:59]\n"
"ds_read_b128 v[76:79], v12 offset:2048\n"
"ds_read_b128 v[108:111], v13 offset:2048\n"
"v_mfma_f32_16x16x32_bf16 v[60:63], v[248:251], v[104:107], v[60:63]\n"
"ds_read_b128 v[68:71], v12 offset:0\n"
"ds_read_b128 v[80:83], v13 offset:0\n"
"v_mfma_f32_16x16x32_bf16 v[64:67], v[252:255], v[104:107], v[64:67]\n"
"ds_read_b128 v[72:75], v12 offset:1024\n"
"ds_read_b128 v[84:87], v13 offset:1024\n"
"v_mfma_f32_16x16x32_bf16 v[48:51], v[156:159], v[88:91], v[48:51]\n"
"ds_write_b128 v7, v[56:59] offset:0\n"
"v_mfma_f32_16x16x32_bf16 v[52:55], v[160:163], v[88:91], v[52:55]\n"
"ds_write_b128 v6, v[60:63] offset:0\n"
"v_mfma_f32_16x16x32_bf16 v[48:51], v[176:179], v[92:95], v[48:51]\n"
"ds_write_b128 v6, v[64:67] offset:1024\n"
"v_mfma_f32_16x16x32_bf16 v[52:55], v[180:183], v[92:95], v[52:55]\n"
"ds_write_b32 v4, v4 offset:608\n"
"v_mfma_f32_16x16x32_bf16 v[48:51], v[196:199], v[96:99], v[48:51]\n"
"s_mov_b32 s17, 0x100000\n"
"s_waitcnt lgkmcnt(10)\n"
"v_cmp_eq_u32 vcc, 0, v3\n"
"s_cbranch_vccz .Lok_388\n"
".Ltp_387:\n"
"ds_read_b32 v3, v5 offset:592\n"
"ds_read_b128 v[76:79], v12 offset:2048\n"
"ds_read_b128 v[108:111], v13 offset:2048\n"
"ds_read_b128 v[68:71], v12 offset:0\n"
"ds_read_b128 v[80:83], v13 offset:0\n"
"ds_read_b128 v[72:75], v12 offset:1024\n"
"ds_read_b128 v[84:87], v13 offset:1024\n"
"s_sub_u32 s17, s17, 1\n"
"s_cbranch_scc1 .Lok_388\n"
"s_waitcnt lgkmcnt(6)\n"
"v_cmp_eq_u32 vcc, 0, v3\n"
"s_cbranch_vccnz .Ltp_387\n"
"s_waitcnt lgkmcnt(0)\n"
".Lok_388:\n"
"v_mfma_f32_16x16x32_bf16 v[52:55], v[200:203], v[96:99], v[52:55]\n"
"s_waitcnt lgkmcnt(8)\n"
"v_add_f32 v76, v76, v108\n"
"v_add_f32 v77, v77, v109\n"
"v_add_f32 v78, v78, v110\n"
"v_mfma_f32_16x16x32_bf16 v[48:51], v[216:219], v[100:103], v[48:51]\n"
"v_add_f32 v79, v79, v111\n"
"s_waitcnt lgkmcnt(6)\n"
"v_add_f32 v68, v68, v80\n"
"v_add_f32 v69, v69, v81\n"
"v_mfma_f32_16x16x32_bf16 v[52:55], v[220:223], v[100:103], v[52:55]\n"
"v_add_f32 v70, v70, v82\n"
"v_add_f32 v71, v71, v83\n"
"s_waitcnt lgkmcnt(4)\n"
"v_add_f32 v72, v72, v84\n"
"v_mfma_f32_16x16x32_bf16 v[48:51], v[236:239], v[104:107], v[48:51]\n"
"v_add_f32 v73, v73, v85\n"
"v_add_f32 v74, v74, v86\n"
"v_add_f32 v75, v75, v87\n"
"v_mfma_f32_16x16x32_bf16 v[52:55], v[240:243], v[104:107], v[52:55]\n"
"ds_read_b32 v3, v5 offset:608\n"
"ds_read_b128 v[112:115], v11 offset:2048\n"
"ds_read_b128 v[60:63], v11 offset:0\n"
"ds_read_b128 v[64:67], v11 offset:1024\n"
"v_add_f32 v56, v56, v76\n"
"v_add_f32 v57, v57, v77\n"
"v_add_f32 v58, v58, v78\n"
"v_add_f32 v59, v59, v79\n"
"v_add_f32 v48, v48, v68\n"
"v_add_f32 v49, v49, v69\n"
"v_add_f32 v50, v50, v70\n"
"v_add_f32 v51, v51, v71\n"
"v_add_f32 v52, v52, v72\n"
"v_add_f32 v53, v53, v73\n"
"v_add_f32 v54, v54, v74\n"
"v_add_f32 v55, v55, v75\n"
"s_mov_b32 s17, 0x100000\n"
"s_waitcnt lgkmcnt(3)\n"
"v_cmp_eq_u32 vcc, 0, v3\n"
"s_cbranch_vccz .Lok_390\n"
".Ltp_389:\n"
"ds_read_b32 v3, v5 offset:608\n"
"ds_read_b128 v[112:115], v11 offset:2048\n"
"ds_read_b128 v[60:63], v11 offset:0\n"
"ds_read_b128 v[64:67], v11 offset:1024\n"
"s_sub_u32 s17, s17, 1\n"
"s_cbranch_scc1 .Lok_390\n"
"s_waitcnt lgkmcnt(3)\n"
"v_cmp_eq_u32 vcc, 0, v3\n"
"s_cbranch_vccnz .Ltp_389\n"
"s_waitcnt lgkmcnt(0)\n"
".Lok_390:\n"
"s_waitcnt lgkmcnt(2)\n"
"v_add_f32 v56, v56, v112\n"
"v_add_f32 v57, v57, v113\n"
"v_add_f32 v58, v58, v114\n"
"v_add_f32 v59, v59, v115\n"
"v_cvt_pk_bf16_f32 v44, v56, v57\n"
"v_cvt_pk_bf16_f32 v45, v58, v59\n"
"v_and_b32 v44, v19, v44\n"
"v_and_b32 v45, v19, v45\n"
"s_waitcnt lgkmcnt(1)\n"
"v_add_f32 v48, v48, v60\n"
"v_add_f32 v49, v49, v61\n"
"v_add_f32 v50, v50, v62\n"
"v_add_f32 v51, v51, v63\n"
"v_cvt_pk_bf16_f32 v40, v48, v49\n"
"v_cvt_pk_bf16_f32 v41, v50, v51\n"
"v_mfma_f32_16x16x32_bf16 v[88:91], v[44:47], v[44:47], 0\n"
"v_mfma_f32_16x16x32_bf16 v[92:95], v[20:23], v[44:47], 0\n"
"s_waitcnt lgkmcnt(0)\n"
"v_add_f32 v52, v52, v64\n"
"v_add_f32 v53, v53, v65\n"
"v_add_f32 v54, v54, v66\n"
"v_add_f32 v55, v55, v67\n"
"v_cvt_pk_bf16_f32 v42, v52, v53\n"
"v_cvt_pk_bf16_f32 v43, v54, v55\n"
"v_cvt_pk_bf16_f32 v116, v48, v49\n"
"v_cvt_pk_bf16_f32 v118, v50, v51\n"
"v_mfma_f32_16x16x32_bf16 v[88:91], v[40:43], v[40:43], v[88:91]\n"
"v_mfma_f32_16x16x32_bf16 v[92:95], v[20:23], v[40:43], v[92:95]\n"
"v_cvt_pk_bf16_f32 v120, v52, v53\n"
"v_cvt_pk_bf16_f32 v122, v54, v55\n"
"v_cvt_pk_bf16_f32 v124, v56, v57\n"
"v_cvt_pk_bf16_f32 v126, v58, v59\n"
"s_nop 0\n"
"v_cndmask_b32 v101, v88, v90, s[44:45]\n"
"v_cndmask_b32 v88, v89, v91, s[44:45]\n"
"s_nop 0\n"
"v_min_u32 v100, -2, v92\n"
"v_min_u32 v88, -2, v88\n"
"v_min_u32 v101, -2, v101\n"
"v_mov_b32_dpp v102, v100 quad_perm:[1,0,3,2] row_mask:0xf bank_mask:0xf\n"
"v_mov_b32_dpp v103, v88 quad_perm:[1,0,3,2] row_mask:0xf bank_mask:0xf\n"
"buffer_store_dwordx4 v[100:103], v16, s[4:7], s38 offen sc1\n"
"buffer_store_dwordx4 v[116:119], v17, s[4:7], s37 offen offset:0 sc1\n"
"buffer_store_dwordx4 v[120:123], v17, s[4:7], s37 offen offset:1024 sc1\n"
"buffer_store_dwordx4 v[124:127], v18, s[4:7], s37 offen offset:2048 sc1\n"
"ds_write_b32 v4, v4 offset:176\n"
"s_xor_b32 s79, s3, 1\n"
"s_lshl_b32 s79, s79, 8\n"
"s_add_u32 s32, s19, s79\n"
"s_cmp_ge_u32 s32, 64\n"
"s_cbranch_scc1 .Lnr_391\n"
"s_lshl_b32 s79, s19, 11\n"
"s_add_u32 s32, s79, 0xbc8000\n"
"s_lshl_b32 s79, s19, 7\n"
"s_add_u32 s33, s79, 0xc08000\n"
"v_lshrrev_b32 v99, 3, v0\n"
"v_and_b32 v98, 7, v0\n"
"v_lshlrev_b32 v98, 4, v98\n"
"v_lshl_add_u32 v40, v99, 14, v98\n"
"v_mov_b32 v41, 0x3727c5ac\n"
"s_mov_b32 s89, 15\n"
".Ldl_392:\n"
"s_sub_u32 s89, s89, 1\n"
"s_cbranch_scc1 .Lde_393\n"
"s_sleep 1\n"
"s_branch .Ldl_392\n"
".Lde_393:\n"
"buffer_load_dwordx4 v[48:51], v2, s[4:7], s32 offen offset:0 sc1\n"
"buffer_load_dwordx4 v[52:55], v2, s[4:7], s32 offen offset:1024 sc1\n"
"s_mov_b32 s17, 32768\n"
".Ltp_394:\n"
"s_waitcnt vmcnt(0)\n"
"s_sub_u32 s17, s17, 1\n"
"s_cbranch_scc1 .Lou_395\n"
"v_max_u32 v38, v48, v49\n"
"v_max3_u32 v38, v50, v51, v38\n"
"v_max3_u32 v38, v52, v53, v38\n"
"v_max3_u32 v38, v54, v55, v38\n"
"v_cmp_eq_u32 vcc, -1, v38\n"
"s_cbranch_vccz .Lou_395\n"
"v_max_u32 v38, v50, v51\n"
"v_max3_u32 v38, v48, v49, v38\n"
"v_cmp_eq_u32 vcc, -1, v38\n"
"s_cbranch_vccz .Lsk_396\n"
"s_mov_b64 exec, vcc\n"
"buffer_load_dwordx4 v[48:51], v2, s[4:7], s32 offen offset:0 sc1\n"
"s_mov_b64 exec, -1\n"
".Lsk_396:\n"
"v_max_u32 v38, v54, v55\n"
"v_max3_u32 v38, v52, v53, v38\n"
"v_cmp_eq_u32 vcc, -1, v38\n"
"s_cbranch_vccz .Lsk_397\n"
"s_mov_b64 exec, vcc\n"
"buffer_load_dwordx4 v[52:55], v2, s[4:7], s32 offen offset:1024 sc1\n"
"s_mov_b64 exec, -1\n"
".Lsk_397:\n"
"s_branch .Ltp_394\n"
".Lou_395:\n"
"s_waitcnt vmcnt(0)\n"
"v_add_f32 v48, v48, v52\n"
"v_add_f32 v49, v49, v53\n"
"v_add_f32 v50, v50, v54\n"
"v_add_f32 v51, v51, v55\n"
"v_add_f32_dpp v52, v48, v48 row_ror:8 row_mask:0xf bank_mask:0xf\n"
"v_add_f32_dpp v53, v49, v49 row_ror:8 row_mask:0xf bank_mask:0xf\n"
"v_add_f32_dpp v54, v50, v50 row_ror:8 row_mask:0xf bank_mask:0xf\n"
"v_add_f32_dpp v55, v51, v51 row_ror:8 row_mask:0xf bank_mask:0xf\n"
"v_mov_b32 v48, v52\n"
"v_mov_b32 v49, v53\n"
"v_mov_b32 v50, v54\n"
"v_mov_b32 v51, v55\n"
"v_permlane16_swap_b32 v48, v52\n"
"v_permlane16_swap_b32 v49, v53\n"
"v_permlane16_swap_b32 v50, v54\n"
"v_permlane16_swap_b32 v51, v55\n"
"v_add_f32 v52, v48, v52\n"
"v_add_f32 v53, v49, v53\n"
"v_add_f32 v54, v50, v54\n"
"v_add_f32 v55, v51, v55\n"
"v_mov_b32 v48, v52\n"
"v_mov_b32 v49, v53\n"
"v_mov_b32 v50, v54\n"
"v_mov_b32 v51, v55\n"
"v_permlane32_swap_b32 v48, v52\n"
"v_permlane32_swap_b32 v49, v53\n"
"v_permlane32_swap_b32 v50, v54\n"
"v_permlane32_swap_b32 v51, v55\n"
"v_add_f32 v52, v48, v52\n"
"v_add_f32 v53, v49, v53\n"
"v_add_f32 v54, v50, v54\n"
"v_add_f32 v55, v51, v55\n"
"v_mul_f32 v48, 0x3acccccd, v52\n"
"v_mul_f32 v50, 0x3acccccd, v54\n"
"v_fma_f32 v53, -v52, v48, v53\n"
"v_fma_f32 v55, -v54, v50, v55\n"
"v_fmamk_f32 v53, v53, 0x3acccccd, v41\n"
"v_fmamk_f32 v55, v55, 0x3acccccd, v41\n"
"v_max_f32 v53, v53, v41\n"
"v_max_f32 v55, v55, v41\n"
"v_rsq_f32 v49, v53\n"
"v_rsq_f32 v51, v55\n"
"v_mul_f32 v48, -v48, v49\n"
"v_mul_f32 v50, -v50, v51\n"
"v_min_u32 v49, -2, v49\n"
"v_min_u32 v51, -2, v51\n"
"v_min_u32 v48, -2, v48\n"
"v_min_u32 v50, -2, v50\n"
"buffer_store_dwordx4 v[48:51], v40, s[4:7], s33 offen sc1\n"
"s_nop 3\n"
".Lnr_391:\n"
"v_lshlrev_b32 v99, 4, v0\n"
"v_mov_b32 v98, 0x40000000\n"
"v_cmp_gt_i32 vcc, 16, v0\n"
"s_nop 1\n"
"v_cndmask_b32 v43, v98, v99, vcc\n"
"v_mov_b32 v44, -1\n"
"v_mov_b32 v45, -1\n"
"v_mov_b32 v46, -1\n"
"v_mov_b32 v47, -1\n"
"s_add_u32 s49, s31, 0xa60000\n"
"buffer_store_dwordx4 v[44:47], v2, s[4:7], s49 offen offset:0 sc1\n"
"buffer_store_dwordx4 v[44:47], v43, s[4:7], s49 offen offset:1024 sc1\n"
"s_add_u32 s49, s31, 0xba0000\n"
"buffer_store_dwordx4 v[44:47], v2, s[4:7], s49 offen offset:0 sc1\n"
"buffer_store_dwordx4 v[44:47], v43, s[4:7], s49 offen offset:1024 sc1\n"
"s_lshl_b32 s79, s2, 2\n"
"s_add_u32 s79, s79, s3\n"
"s_lshl_b32 s79, s79, 10\n"
"s_add_u32 s49, s79, 0x1628000\n"
"s_sub_u32 s79, 0x1687fff, s49\n"
"s_and_b32 s79, s79, 0x80000000\n"
"s_or_b32 s49, s49, s79\n"
"buffer_store_dwordx4 v[44:47], v2, s[4:7], s49 offen sc1\n"
"s_nop 1\n"
"s_setprio 0\n"
"s_cmp_lg_u32 s43, 0\n"
"s_cbranch_scc1 .Lsk_399\n"
"s_call_b64 s[46:47], .Lst_332\n"
".Lsk_399:\n"
"s_cmp_lg_u32 s43, 1\n"
"s_cbranch_scc1 .Lsk_400\n"
"s_call_b64 s[46:47], .Lst_333\n"
".Lsk_400:\n"
"s_cmp_lg_u32 s43, 2\n"
"s_cbranch_scc1 .Lsk_401\n"
"ds_read_b32 v3, v5 offset:160\n"
"s_waitcnt lgkmcnt(0)\n"
"v_cmp_eq_u32 vcc, 0, v3\n"
"s_cbranch_vccz .Len_398\n"
"s_call_b64 s[46:47], .Lst_334\n"
".Lsk_401:\n"
".Len_398:\n"
"s_setprio 2\n"
"s_mul_i32 s79, s28, 0xa0\n"
"s_add_u32 s79, s30, s79\n"
"s_and_b32 s78, s3, 1\n"
"s_mul_i32 s78, s78, 0x60\n"
"s_add_u32 s79, s79, s78\n"
"s_add_u32 s36, s79, 0x140000\n"
"s_mul_i32 s79, s3, 0x1400\n"
"s_add_u32 s79, s30, s79\n"
"s_add_u32 s32, s79, 0x588000\n"
"s_add_u32 s33, s32, 0x1000\n"
"s_lshl_b32 s79, s29, 1\n"
"s_lshr_b32 s78, s3, 1\n"
"s_xor_b32 s78, s78, 1\n"
"s_add_u32 s79, s79, s78\n"
"s_mul_i32 s79, s79, 0x5000\n"
"s_mul_i32 s78, s3, 0x1400\n"
"s_add_u32 s79, s79, s78\n"
"s_add_u32 s34, s79, 0x588000\n"
"s_add_u32 s35, s34, 0x1000\n"
"s_lshl_b32 s79, s30, 1\n"
"s_mul_i32 s78, s28, 0x1400\n"
"s_add_u32 s79, s79, s78\n"
"s_and_b32 s78, s3, 1\n"
"s_mul_i32 s78, s78, 0xc00\n"
"s_add_u32 s79, s79, s78\n"
"s_add_u32 s37, s79, 0x948000\n"
"s_lshl_b32 s79, s29, 1\n"
"s_lshr_b32 s78, s3, 1\n"
"s_add_u32 s79, s79, s78\n"
"s_lshl_b32 s79, s79, 11\n"
"s_lshl_b32 s78, s28, 8\n"
"s_add_u32 s79, s79, s78\n"
"s_and_b32 s78, s3, 1\n"
"s_lshl_b32 s78, s78, 7\n"
"s_add_u32 s79, s79, s78\n"
"s_add_u32 s38, s79, 0xbe8000\n"
"buffer_load_dwordx2 v[40:41], v14, s[8:11], s36 offen offset:0\n"
"buffer_load_dwordx2 v[42:43], v14, s[8:11], s36 offen offset:32\n"
"buffer_load_dwordx2 v[44:45], v15, s[8:11], s36 offen offset:64\n"
"s_mov_b32 s17, 0x100000\n"
".Ltp_402:\n"
"ds_read_b32 v3, v5 offset:176\n"
"s_sub_u32 s17, s17, 1\n"
"s_cbranch_scc1 .Lou_403\n"
"s_waitcnt lgkmcnt(0)\n"
"v_cmp_eq_u32 vcc, 0, v3\n"
"s_cbranch_vccnz .Ltp_402\n"
".Lou_403:\n"
"s_waitcnt lgkmcnt(0)\n"
"s_waitcnt vmcnt(2)\n"
"v_lshlrev_b32 v48, 16, v40\n"
"v_and_b32 v49, 0xffff0000, v40\n"
"v_lshlrev_b32 v50, 16, v41\n"
"v_and_b32 v51, 0xffff0000, v41\n"
"s_waitcnt vmcnt(1)\n"
"v_lshlrev_b32 v52, 16, v42\n"
"v_and_b32 v53, 0xffff0000, v42\n"
"v_lshlrev_b32 v54, 16, v43\n"
"v_and_b32 v55, 0xffff0000, v43\n"
"s_waitcnt vmcnt(0)\n"
"v_lshlrev_b32 v56, 16, v44\n"
"v_and_b32 v57, 0xffff0000, v44\n"
"v_lshlrev_b32 v58, 16, v45\n"
"v_and_b32 v59, 0xffff0000, v45\n"
"v_mov_b32 v46, 0\n"
"v_mov_b32 v47, 0\n"
"s_mov_b32 s17, 0x100000\n"
".Ltp_404:\n"
"s_sleep 1\n"
"ds_read_b32 v3, v5 offset:160\n"
"s_sub_u32 s17, s17, 1\n"
"s_cbranch_scc1 .Lou_405\n"
"s_waitcnt lgkmcnt(0)\n"
"v_cmp_eq_u32 vcc, 0, v3\n"
"s_cbranch_vccnz .Ltp_404\n"
".Lou_405:\n"
"s_waitcnt lgkmcnt(0)\n"
"s_cmp_eq_u32 s17, 0xfffff\n"
"s_cbranch_scc1 .Lsk_406\n"
"s_mov_b32 s89, 24\n"
".Ldl_408:\n"
"s_sub_u32 s89, s89, 1\n"
"s_cbranch_scc1 .Lde_409\n"
"s_sleep 1\n"
"s_branch .Ldl_408\n"
".Lde_409:\n"
"s_branch .Ljn_407\n"
".Lsk_406:\n"
".Ljn_407:\n"
"buffer_load_dwordx4 v[108:111], v2, s[4:7], s34 offen offset:0 sc1\n"
"s_nop 9\n"
"buffer_load_dwordx4 v[112:115], v2, s[4:7], s34 offen offset:1024 sc1\n"
"s_nop 9\n"
"buffer_load_dwordx4 v[116:119], v2, s[4:7], s34 offen offset:2048 sc1\n"
"s_nop 9\n"
"buffer_load_dwordx4 v[120:123], v2, s[4:7], s34 offen offset:3072 sc1\n"
"s_nop 9\n"
"buffer_load_dwordx4 v[124:127], v2, s[4:7], s35 offen offset:0 sc1\n"
"s_nop 9\n"
"buffer_load_dwordx4 v[88:91], v2, s[4:7], s32 offen offset:0 sc1\n"
"s_nop 9\n"
"buffer_load_dwordx4 v[92:95], v2, s[4:7], s32 offen offset:1024 sc1\n"
"s_nop 9\n"
"buffer_load_dwordx4 v[96:99], v2, s[4:7], s32 offen offset:2048 sc1\n"
"s_nop 9\n"
"buffer_load_dwordx4 v[100:103], v2, s[4:7], s32 offen offset:3072 sc1\n"
"s_nop 9\n"
"buffer_load_dwordx4 v[104:107], v2, s[4:7], s33 offen offset:0 sc1\n"
"s_waitcnt vmcnt(9)\n"
"v_max_u32 v38, v108, v109\n"
"v_max3_u32 v38, v110, v111, v38\n"
"v_cmp_eq_u32 vcc, -1, v38\n"
"s_cbranch_vccnz .Lsl_410\n"
".Lbk_411:\n"
"v_mfma_f32_16x16x32_bf16 v[68:71], v[156:159], v[108:111], 0\n"
"v_mfma_f32_16x16x32_bf16 v[72:75], v[160:163], v[108:111], 0\n"
"v_mfma_f32_16x16x32_bf16 v[76:79], v[164:167], v[108:111], 0\n"
"s_waitcnt vmcnt(8)\n"
"v_max_u32 v38, v112, v113\n"
"v_max3_u32 v38, v114, v115, v38\n"
"v_mfma_f32_16x16x32_bf16 v[80:83], v[168:171], v[108:111], 0\n"
"v_cmp_eq_u32 vcc, -1, v38\n"
"v_mfma_f32_16x16x32_bf16 v[84:87], v[172:175], v[108:111], 0\n"
"s_cbranch_vccnz .Lsl_412\n"
".Lbk_413:\n"
"v_mfma_f32_16x16x32_bf16 v[68:71], v[176:179], v[112:115], v[68:71]\n"
"v_mfma_f32_16x16x32_bf16 v[72:75], v[180:183], v[112:115], v[72:75]\n"
"v_mfma_f32_16x16x32_bf16 v[76:79], v[184:187], v[112:115], v[76:79]\n"
"s_waitcnt vmcnt(7)\n"
"v_max_u32 v38, v116, v117\n"
"v_max3_u32 v38, v118, v119, v38\n"
"v_mfma_f32_16x16x32_bf16 v[80:83], v[188:191], v[112:115], v[80:83]\n"
"v_cmp_eq_u32 vcc, -1, v38\n"
"v_mfma_f32_16x16x32_bf16 v[84:87], v[192:195], v[112:115], v[84:87]\n"
"s_cbranch_vccnz .Lsl_414\n"
".Lbk_415:\n"
"v_mfma_f32_16x16x32_bf16 v[68:71], v[196:199], v[116:119], v[68:71]\n"
"v_mfma_f32_16x16x32_bf16 v[72:75], v[200:203], v[116:119], v[72:75]\n"
"v_mfma_f32_16x16x32_bf16 v[76:79], v[204:207], v[116:119], v[76:79]\n"
"s_waitcnt vmcnt(6)\n"
"v_max_u32 v38, v120, v121\n"
"v_max3_u32 v38, v122, v123, v38\n"
"v_mfma_f32_16x16x32_bf16 v[80:83], v[208:211], v[116:119], v[80:83]\n"
"v_cmp_eq_u32 vcc, -1, v38\n"
"v_mfma_f32_16x16x32_bf16 v[84:87], v[212:215], v[116:119], v[84:87]\n"
"s_cbranch_vccnz .Lsl_416\n"
".Lbk_417:\n"
"v_mfma_f32_16x16x32_bf16 v[68:71], v[216:219], v[120:123], v[68:71]\n"
"v_mfma_f32_16x16x32_bf16 v[72:75], v[220:223], v[120:123], v[72:75]\n"
"v_mfma_f32_16x16x32_bf16 v[76:79], v[224:227], v[120:123], v[76:79]\n"
"s_waitcnt vmcnt(5)\n"
"v_max_u32 v38, v124, v125\n"
"v_max3_u32 v38, v126, v127, v38\n"
"v_mfma_f32_16x16x32_bf16 v[80:83], v[228:231], v[120:123], v[80:83]\n"
"v_cmp_eq_u32 vcc, -1, v38\n"
"v_mfma_f32_16x16x32_bf16 v[84:87], v[232:235], v[120:123], v[84:87]\n"
"s_cbranch_vccnz .Lsl_418\n"
".Lbk_419:\n"
"v_mfma_f32_16x16x32_bf16 v[68:71], v[236:239], v[124:127], v[68:71]\n"
"v_mfma_f32_16x16x32_bf16 v[72:75], v[240:243], v[124:127], v[72:75]\n"
"v_mfma_f32_16x16x32_bf16 v[76:79], v[244:247], v[124:127], v[76:79]\n"
"s_waitcnt vmcnt(4)\n"
"v_max_u32 v38, v88, v89\n"
"v_max3_u32 v38, v90, v91, v38\n"
"v_mfma_f32_16x16x32_bf16 v[80:83], v[248:251], v[124:127], v[80:83]\n"
"v_cmp_eq_u32 vcc, -1, v38\n"
"v_mfma_f32_16x16x32_bf16 v[84:87], v[252:255], v[124:127], v[84:87]\n"
"ds_write_b128 v8, v[68:71] offset:0\n"
"s_cbranch_vccnz .Lsl_420\n"
".Lbk_421:\n"
"v_mfma_f32_16x16x32_bf16 v[56:59], v[164:167], v[88:91], v[56:59]\n"
"s_waitcnt vmcnt(3)\n"
"v_max_u32 v38, v92, v93\n"
"v_max3_u32 v38, v94, v95, v38\n"
"ds_write_b128 v8, v[72:75] offset:1024\n"
"v_mfma_f32_16x16x32_bf16 v[60:63], v[168:171], v[88:91], 0\n"
"v_cmp_eq_u32 vcc, -1, v38\n"
"ds_write_b128 v10, v[76:79] offset:0\n"
"v_mfma_f32_16x16x32_bf16 v[64:67], v[172:175], v[88:91], 0\n"
"v_mov_b32 v117, s16\n"
"s_cbranch_vccnz .Lsl_422\n"
".Lbk_423:\n"
"v_mfma_f32_16x16x32_bf16 v[56:59], v[184:187], v[92:95], v[56:59]\n"
"s_waitcnt vmcnt(2)\n"
"v_max_u32 v38, v96, v97\n"
"v_max3_u32 v38, v98, v99, v38\n"
"v_mov_b32 v119, s16\n"
"v_mfma_f32_16x16x32_bf16 v[60:63], v[188:191], v[92:95], v[60:63]\n"
"v_cmp_eq_u32 vcc, -1, v38\n"
"ds_write_b128 v9, v[80:83] offset:0\n"
"v_mfma_f32_16x16x32_bf16 v[64:67], v[192:195], v[92:95], v[64:67]\n"
"ds_write_b128 v9, v[84:87] offset:1024\n"
"s_cbranch_vccnz .Lsl_424\n"
".Lbk_425:\n"
"v_mfma_f32_16x16x32_bf16 v[56:59], v[204:207], v[96:99], v[56:59]\n"
"s_waitcnt vmcnt(1)\n"
"v_max_u32 v38, v100, v101\n"
"v_max3_u32 v38, v102, v103, v38\n"
"ds_write_b32 v4, v4 offset:624\n"
"v_mfma_f32_16x16x32_bf16 v[60:63], v[208:211], v[96:99], v[60:63]\n"
"v_cmp_eq_u32 vcc, -1, v38\n"
"v_mov_b32 v121, s16\n"
"v_mov_b32 v123, s16\n"
"v_mfma_f32_16x16x32_bf16 v[64:67], v[212:215], v[96:99], v[64:67]\n"
"v_mov_b32 v125, s16\n"
"s_cbranch_vccnz .Lsl_426\n"
".Lbk_427:\n"
"v_mfma_f32_16x16x32_bf16 v[56:59], v[224:227], v[100:103], v[56:59]\n"
"s_waitcnt vmcnt(0)\n"
"v_max_u32 v38, v104, v105\n"
"v_max3_u32 v38, v106, v107, v38\n"
"v_mov_b32 v127, s16\n"
"v_mfma_f32_16x16x32_bf16 v[60:63], v[228:231], v[100:103], v[60:63]\n"
"v_cmp_eq_u32 vcc, -1, v38\n"
"ds_read_b32 v3, v5 offset:624\n"
"v_mfma_f32_16x16x32_bf16 v[64:67], v[232:235], v[100:103], v[64:67]\n"
"s_cbranch_vccnz .Lsl_428\n"
".Lbk_429:\n"
"v_mfma_f32_16x16x32_bf16 v[56:59], v[244:247], v[104:107], v[56:59]\n"
"ds_read_b128 v[76:79], v12 offset:2048\n"
"ds_read_b128 v[108:111], v13 offset:2048\n"
"v_mfma_f32_16x16x32_bf16 v[60:63], v[248:251], v[104:107], v[60:63]\n"
"ds_read_b128 v[68:71], v12 offset:0\n"
"ds_read_b128 v[80:83], v13 offset:0\n"
"v_mfma_f32_16x16x32_bf16 v[64:67], v[252:255], v[104:107], v[64:67]\n"
"ds_read_b128 v[72:75], v12 offset:1024\n"
"ds_read_b128 v[84:87], v13 offset:1024\n"
"v_mfma_f32_16x16x32_bf16 v[48:51], v[156:159], v[88:91], v[48:51]\n"
"ds_write_b128 v7, v[56:59] offset:0\n"
"v_mfma_f32_16x16x32_bf16 v[52:55], v[160:163], v[88:91], v[52:55]\n"
"ds_write_b128 v6, v[60:63] offset:0\n"
"v_mfma_f32_16x16x32_bf16 v[48:51], v[176:179], v[92:95], v[48:51]\n"
"ds_write_b128 v6, v[64:67] offset:1024\n"
"v_mfma_f32_16x16x32_bf16 v[52:55], v[180:183], v[92:95], v[52:55]\n"
"ds_write_b32 v4, v4 offset:640\n"
"v_mfma_f32_16x16x32_bf16 v[48:51], v[196:199], v[96:99], v[48:51]\n"
"s_mov_b32 s17, 0x100000\n"
"s_waitcnt lgkmcnt(10)\n"
"v_cmp_eq_u32 vcc, 0, v3\n"
"s_cbranch_vccz .Lok_431\n"
".Ltp_430:\n"
"ds_read_b32 v3, v5 offset:624\n"
"ds_read_b128 v[76:79], v12 offset:2048\n"
"ds_read_b128 v[108:111], v13 offset:2048\n"
"ds_read_b128 v[68:71], v12 offset:0\n"
"ds_read_b128 v[80:83], v13 offset:0\n"
"ds_read_b128 v[72:75], v12 offset:1024\n"
"ds_read_b128 v[84:87], v13 offset:1024\n"
"s_sub_u32 s17, s17, 1\n"
"s_cbranch_scc1 .Lok_431\n"
"s_waitcnt lgkmcnt(6)\n"
"v_cmp_eq_u32 vcc, 0, v3\n"
"s_cbranch_vccnz .Ltp_430\n"
"s_waitcnt lgkmcnt(0)\n"
".Lok_431:\n"
"v_mfma_f32_16x16x32_bf16 v[52:55], v[200:203], v[96:99], v[52:55]\n"
"s_waitcnt lgkmcnt(8)\n"
"v_add_f32 v76, v76, v108\n"
"v_add_f32 v77, v77, v109\n"
"v_add_f32 v78, v78, v110\n"
"v_mfma_f32_16x16x32_bf16 v[48:51], v[216:219], v[100:103], v[48:51]\n"
"v_add_f32 v79, v79, v111\n"
"s_waitcnt lgkmcnt(6)\n"
"v_add_f32 v68, v68, v80\n"
"v_add_f32 v69, v69, v81\n"
"v_mfma_f32_16x16x32_bf16 v[52:55], v[220:223], v[100:103], v[52:55]\n"
"v_add_f32 v70, v70, v82\n"
"v_add_f32 v71, v71, v83\n"
"s_waitcnt lgkmcnt(4)\n"
"v_add_f32 v72, v72, v84\n"
"v_mfma_f32_16x16x32_bf16 v[48:51], v[236:239], v[104:107], v[48:51]\n"
"v_add_f32 v73, v73, v85\n"
"v_add_f32 v74, v74, v86\n"
"v_add_f32 v75, v75, v87\n"
"v_mfma_f32_16x16x32_bf16 v[52:55], v[240:243], v[104:107], v[52:55]\n"
"ds_read_b32 v3, v5 offset:640\n"
"ds_read_b128 v[112:115], v11 offset:2048\n"
"ds_read_b128 v[60:63], v11 offset:0\n"
"ds_read_b128 v[64:67], v11 offset:1024\n"
"v_add_f32 v56, v56, v76\n"
"v_add_f32 v57, v57, v77\n"
"v_add_f32 v58, v58, v78\n"
"v_add_f32 v59, v59, v79\n"
"v_add_f32 v48, v48, v68\n"
"v_add_f32 v49, v49, v69\n"
"v_add_f32 v50, v50, v70\n"
"v_add_f32 v51, v51, v71\n"
"v_add_f32 v52, v52, v72\n"
"v_add_f32 v53, v53, v73\n"
"v_add_f32 v54, v54, v74\n"
"v_add_f32 v55, v55, v75\n"
"s_mov_b32 s17, 0x100000\n"
"s_waitcnt lgkmcnt(3)\n"
"v_cmp_eq_u32 vcc, 0, v3\n"
"s_cbranch_vccz .Lok_433\n"
".Ltp_432:\n"
"ds_read_b32 v3, v5 offset:640\n"
"ds_read_b128 v[112:115], v11 offset:2048\n"
"ds_read_b128 v[60:63], v11 offset:0\n"
"ds_read_b128 v[64:67], v11 offset:1024\n"
"s_sub_u32 s17, s17, 1\n"
"s_cbranch_scc1 .Lok_433\n"
"s_waitcnt lgkmcnt(3)\n"
"v_cmp_eq_u32 vcc, 0, v3\n"
"s_cbranch_vccnz .Ltp_432\n"
"s_waitcnt lgkmcnt(0)\n"
".Lok_433:\n"
"s_waitcnt lgkmcnt(2)\n"
"v_add_f32 v56, v56, v112\n"
"v_add_f32 v57, v57, v113\n"
"v_add_f32 v58, v58, v114\n"
"v_add_f32 v59, v59, v115\n"
"v_cvt_pk_bf16_f32 v44, v56, v57\n"
"v_cvt_pk_bf16_f32 v45, v58, v59\n"
"v_and_b32 v44, v19, v44\n"
"v_and_b32 v45, v19, v45\n"
"s_waitcnt lgkmcnt(1)\n"
"v_add_f32 v48, v48, v60\n"
"v_add_f32 v49, v49, v61\n"
"v_add_f32 v50, v50, v62\n"
"v_add_f32 v51, v51, v63\n"
"v_cvt_pk_bf16_f32 v40, v48, v49\n"
"v_cvt_pk_bf16_f32 v41, v50, v51\n"
"v_mfma_f32_16x16x32_bf16 v[88:91], v[44:47], v[44:47], 0\n"
"v_mfma_f32_16x16x32_bf16 v[92:95], v[20:23], v[44:47], 0\n"
"s_waitcnt lgkmcnt(0)\n"
"v_add_f32 v52, v52, v64\n"
"v_add_f32 v53, v53, v65\n"
"v_add_f32 v54, v54, v66\n"
"v_add_f32 v55, v55, v67\n"
"v_cvt_pk_bf16_f32 v42, v52, v53\n"
"v_cvt_pk_bf16_f32 v43, v54, v55\n"
"v_cvt_pk_bf16_f32 v116, v48, v49\n"
"v_cvt_pk_bf16_f32 v118, v50, v51\n"
"v_mfma_f32_16x16x32_bf16 v[88:91], v[40:43], v[40:43], v[88:91]\n"
"v_mfma_f32_16x16x32_bf16 v[92:95], v[20:23], v[40:43], v[92:95]\n"
"v_cvt_pk_bf16_f32 v120, v52, v53\n"
"v_cvt_pk_bf16_f32 v122, v54, v55\n"
"v_cvt_pk_bf16_f32 v124, v56, v57\n"
"v_cvt_pk_bf16_f32 v126, v58, v59\n"
"s_nop 0\n"
"v_cndmask_b32 v101, v88, v90, s[44:45]\n"
"v_cndmask_b32 v88, v89, v91, s[44:45]\n"
"s_nop 0\n"
"v_min_u32 v100, -2, v92\n"
"v_min_u32 v88, -2, v88\n"
"v_min_u32 v101, -2, v101\n"
"v_mov_b32_dpp v102, v100 quad_perm:[1,0,3,2] row_mask:0xf bank_mask:0xf\n"
"v_mov_b32_dpp v103, v88 quad_perm:[1,0,3,2] row_mask:0xf bank_mask:0xf\n"
"buffer_store_dwordx4 v[100:103], v16, s[4:7], s38 offen sc1\n"
"buffer_store_dwordx4 v[116:119], v17, s[4:7], s37 offen offset:0 sc1\n"
"buffer_store_dwordx4 v[120:123], v17, s[4:7], s37 offen offset:1024 sc1\n"
"buffer_store_dwordx4 v[124:127], v18, s[4:7], s37 offen offset:2048 sc1\n"
"ds_write_b32 v4, v4 offset:256\n"
"s_xor_b32 s79, s3, 1\n"
"s_lshl_b32 s79, s79, 8\n"
"s_add_u32 s32, s19, s79\n"
"s_cmp_ge_u32 s32, 64\n"
"s_cbranch_scc1 .Lnr_434\n"
"s_lshl_b32 s79, s19, 11\n"
"s_add_u32 s32, s79, 0xbe8000\n"
"s_lshl_b32 s79, s19, 7\n"
"s_add_u32 s33, s79, 0xc0a000\n"
"v_lshrrev_b32 v99, 3, v0\n"
"v_and_b32 v98, 7, v0\n"
"v_lshlrev_b32 v98, 4, v98\n"
"v_lshl_add_u32 v40, v99, 14, v98\n"
"v_mov_b32 v41, 0x3727c5ac\n"
"s_mov_b32 s89, 12\n"
".Ldl_435:\n"
"s_sub_u32 s89, s89, 1\n"
"s_cbranch_scc1 .Lde_436\n"
"s_sleep 1\n"
"s_branch .Ldl_435\n"
".Lde_436:\n"
"buffer_load_dwordx4 v[48:51], v2, s[4:7], s32 offen offset:0 sc1\n"
"buffer_load_dwordx4 v[52:55], v2, s[4:7], s32 offen offset:1024 sc1\n"
"s_mov_b32 s17, 32768\n"
".Ltp_437:\n"
"s_waitcnt vmcnt(0)\n"
"s_sub_u32 s17, s17, 1\n"
"s_cbranch_scc1 .Lou_438\n"
"v_max_u32 v38, v48, v49\n"
"v_max3_u32 v38, v50, v51, v38\n"
"v_max3_u32 v38, v52, v53, v38\n"
"v_max3_u32 v38, v54, v55, v38\n"
"v_cmp_eq_u32 vcc, -1, v38\n"
"s_cbranch_vccz .Lou_438\n"
"v_max_u32 v38, v50, v51\n"
"v_max3_u32 v38, v48, v49, v38\n"
"v_cmp_eq_u32 vcc, -1, v38\n"
"s_cbranch_vccz .Lsk_439\n"
"s_mov_b64 exec, vcc\n"
"buffer_load_dwordx4 v[48:51], v2, s[4:7], s32 offen offset:0 sc1\n"
"s_mov_b64 exec, -1\n"
".Lsk_439:\n"
"v_max_u32 v38, v54, v55\n"
"v_max3_u32 v38, v52, v53, v38\n"
"v_cmp_eq_u32 vcc, -1, v38\n"
"s_cbranch_vccz .Lsk_440\n"
"s_mov_b64 exec, vcc\n"
"buffer_load_dwordx4 v[52:55], v2, s[4:7], s32 offen offset:1024 sc1\n"
"s_mov_b64 exec, -1\n"
".Lsk_440:\n"
"s_branch .Ltp_437\n"
".Lou_438:\n"
"s_waitcnt vmcnt(0)\n"
"v_add_f32 v48, v48, v52\n"
"v_add_f32 v49, v49, v53\n"
"v_add_f32 v50, v50, v54\n"
"v_add_f32 v51, v51, v55\n"
"v_add_f32_dpp v52, v48, v48 row_ror:8 row_mask:0xf bank_mask:0xf\n"
"v_add_f32_dpp v53, v49, v49 row_ror:8 row_mask:0xf bank_mask:0xf\n"
"v_add_f32_dpp v54, v50, v50 row_ror:8 row_mask:0xf bank_mask:0xf\n"
"v_add_f32_dpp v55, v51, v51 row_ror:8 row_mask:0xf bank_mask:0xf\n"
"v_mov_b32 v48, v52\n"
"v_mov_b32 v49, v53\n"
"v_mov_b32 v50, v54\n"
"v_mov_b32 v51, v55\n"
"v_permlane16_swap_b32 v48, v52\n"
"v_permlane16_swap_b32 v49, v53\n"
"v_permlane16_swap_b32 v50, v54\n"
"v_permlane16_swap_b32 v51, v55\n"
"v_add_f32 v52, v48, v52\n"
"v_add_f32 v53, v49, v53\n"
"v_add_f32 v54, v50, v54\n"
"v_add_f32 v55, v51, v55\n"
"v_mov_b32 v48, v52\n"
"v_mov_b32 v49, v53\n"
"v_mov_b32 v50, v54\n"
"v_mov_b32 v51, v55\n"
"v_permlane32_swap_b32 v48, v52\n"
"v_permlane32_swap_b32 v49, v53\n"
"v_permlane32_swap_b32 v50, v54\n"
"v_permlane32_swap_b32 v51, v55\n"
"v_add_f32 v52, v48, v52\n"
"v_add_f32 v53, v49, v53\n"
"v_add_f32 v54, v50, v54\n"
"v_add_f32 v55, v51, v55\n"
"v_mul_f32 v48, 0x3acccccd, v52\n"
"v_mul_f32 v50, 0x3acccccd, v54\n"
"v_fma_f32 v53, -v52, v48, v53\n"
"v_fma_f32 v55, -v54, v50, v55\n"
"v_fmamk_f32 v53, v53, 0x3acccccd, v41\n"
"v_fmamk_f32 v55, v55, 0x3acccccd, v41\n"
"v_max_f32 v53, v53, v41\n"
"v_max_f32 v55, v55, v41\n"
"v_rsq_f32 v49, v53\n"
"v_rsq_f32 v51, v55\n"
"v_mul_f32 v48, -v48, v49\n"
"v_mul_f32 v50, -v50, v51\n"
"v_min_u32 v49, -2, v49\n"
"v_min_u32 v51, -2, v51\n"
"v_min_u32 v48, -2, v48\n"
"v_min_u32 v50, -2, v50\n"
"buffer_store_dwordx4 v[48:51], v40, s[4:7], s33 offen sc1\n"
"s_nop 3\n"
".Lnr_434:\n"
"s_cmp_lt_i32 s18, 0\n"
"s_cbranch_scc1 .Lnc_441\n"
"s_nop 0\n"
"s_setprio 0\n"
"s_cmp_lg_u32 s43, 0\n"
"s_cbranch_scc1 .Lsk_443\n"
"s_call_b64 s[46:47], .Lst_332\n"
".Lsk_443:\n"
"s_cmp_lg_u32 s43, 1\n"
"s_cbranch_scc1 .Lsk_444\n"
"s_call_b64 s[46:47], .Lst_333\n"
".Lsk_444:\n"
"s_cmp_lg_u32 s43, 2\n"
"s_cbranch_scc1 .Lsk_445\n"
"s_call_b64 s[46:47], .Lst_334\n"
".Lsk_445:\n"
".Len_442:\n"
"s_setprio 2\n"
"s_mov_b32 s17, 0x100000\n"
".Ltp_446:\n"
"s_sleep 1\n"
"ds_read_b32 v3, v5 offset:208\n"
"s_sub_u32 s17, s17, 1\n"
"s_cbranch_scc1 .Lou_447\n"
"s_waitcnt lgkmcnt(0)\n"
"v_cmp_eq_u32 vcc, 0, v3\n"
"s_cbranch_vccnz .Ltp_446\n"
".Lou_447:\n"
"s_waitcnt lgkmcnt(0)\n"
"s_setprio 0\n"
"s_lshl_b32 s79, s18, 8\n"
"s_add_u32 s32, s79, 0x948000\n"
"s_add_u32 s33, s32, 0x140000\n"
"s_and_b32 s79, s2, 7\n"
"s_lshl_b32 s79, s79, 14\n"
"s_add_u32 s34, s79, 0xc0a000\n"
"s_add_u32 s35, s34, 0x1000\n"
"v_and_b32 v126, 0xffffff80, v29\n"
"v_add_u32 v126, v126, v29\n"
"ds_read_b128 v[88:91], v25 offset:448\n"
"ds_read_b128 v[92:95], v25 offset:432\n"
"ds_read_b64 v[96:97], v24 offset:448\n"
"ds_read_b64 v[98:99], v24 offset:456\n"
"ds_read_b128 v[100:103], v25 offset:960\n"
"ds_read_b128 v[104:107], v25 offset:1472\n"
"s_waitcnt lgkmcnt(1)\n"
"v_mfma_f32_16x16x32_f16 v[64:67], v[88:91], v[144:147], 0\n"
"ds_read_b128 v[40:43], v25 offset:1984\n"
"ds_read_b64 v[44:45], v24 offset:960\n"
"v_mfma_f32_16x16x32_f16 v[68:71], v[92:95], v[148:151], 0\n"
"ds_read_b64 v[46:47], v24 offset:968\n"
"ds_read_b64 v[72:73], v24 offset:1472\n"
"v_mfma_f32_16x16x32_f16 v[48:51], v[152:155], v[88:91], 0\n"
"ds_read_b64 v[74:75], v24 offset:1480\n"
"ds_read_b64 v[76:77], v24 offset:384\n"
"v_mfma_f32_16x16x32_f16 v[48:51], v[96:99], v[100:103], v[48:51]\n"
"ds_read_b64 v[78:79], v24 offset:392\n"
"s_waitcnt lgkmcnt(4)\n"
"v_mfma_f32_16x16x32_f16 v[52:55], v[96:99], v[104:107], 0\n"
"ds_read_b128 v[84:87], v25 offset:896\n"
"ds_read_b128 v[92:95], v25 offset:1408\n"
"v_mfma_f32_16x16x32_f16 v[56:59], v[96:99], v[40:43], 0\n"
"ds_read_b128 v[88:91], v25 offset:1920\n"
"ds_read_b64 v[100:101], v24 offset:896\n"
"v_mfma_f32_16x16x32_f16 v[52:55], v[44:47], v[40:43], v[52:55]\n"
"ds_read_b64 v[102:103], v24 offset:904\n"
"ds_read_b64 v[96:97], v24 offset:1408\n"
"v_cvt_pk_f16_f32 v80, v64, v65\n"
"v_mfma_f32_16x16x32_f16 v[48:51], v[44:47], v[104:107], v[48:51]\n"
"ds_read_b64 v[98:99], v24 offset:1416\n"
"s_waitcnt lgkmcnt(4)\n"
"v_mfma_f32_16x16x32_f16 v[48:51], v[72:75], v[40:43], v[48:51]\n"
"ds_read_b128 v[104:107], v25 offset:960\n"
"v_cvt_pk_f16_f32 v81, v66, v67\n"
"v_mfma_f32_16x16x32_f16 v[48:51], v[76:79], v[84:87], v[48:51]\n"
"ds_read_b128 v[44:47], v25 offset:944\n"
"v_cvt_pk_f16_f32 v82, v68, v69\n"
"v_mfma_f32_16x16x32_f16 v[52:55], v[76:79], v[92:95], v[52:55]\n"
"ds_read_b64 v[40:41], v24 offset:320\n"
"ds_read_b64 v[42:43], v24 offset:328\n"
"v_mfma_f32_16x16x32_f16 v[56:59], v[76:79], v[88:91], v[56:59]\n"
"ds_read_b128 v[72:75], v25 offset:832\n"
"s_waitcnt lgkmcnt(5)\n"
"v_mfma_f32_16x16x32_f16 v[52:55], v[100:103], v[88:91], v[52:55]\n"
"ds_read_b128 v[84:87], v25 offset:1344\n"
"v_cvt_pk_f16_f32 v83, v70, v71\n"
"v_mfma_f32_16x16x32_f16 v[48:51], v[100:103], v[92:95], v[48:51]\n"
"ds_read_b128 v[76:79], v25 offset:1856\n"
"ds_read_b64 v[92:93], v24 offset:832\n"
"v_mfma_f32_16x16x32_f16 v[48:51], v[96:99], v[88:91], v[48:51]\n"
"ds_read_b64 v[94:95], v24 offset:840\n"
"v_mfma_f32_16x16x32_f16 v[48:51], v[80:83], v[128:131], v[48:51]\n"
"ds_read_b64 v[100:101], v24 offset:1344\n"
"s_waitcnt lgkmcnt(9)\n"
"v_mfma_f32_16x16x32_f16 v[52:55], v[80:83], v[132:135], v[52:55]\n"
"ds_read_b64 v[102:103], v24 offset:1352\n"
"ds_read_b64 v[88:89], v24 offset:256\n"
"v_mfma_f32_16x16x32_f16 v[56:59], v[80:83], v[136:139], v[56:59]\n"
"ds_read_b64 v[90:91], v24 offset:264\n"
"v_mfma_f32_16x16x32_f16 v[60:63], v[80:83], v[140:143], 0\n"
"ds_read_b128 v[96:99], v25 offset:768\n"
"v_mfma_f32_16x16x32_f16 v[64:67], v[104:107], v[144:147], 0\n"
"s_waitcnt lgkmcnt(8)\n"
"v_mfma_f32_16x16x32_f16 v[68:71], v[44:47], v[148:151], 0\n"
"ds_read_b128 v[44:47], v25 offset:1280\n"
"v_mfma_f32_16x16x32_f16 v[52:55], v[152:155], v[104:107], v[52:55]\n"
"ds_read_b128 v[104:107], v25 offset:1792\n"
"v_mfma_f32_16x16x32_f16 v[48:51], v[40:43], v[72:75], v[48:51]\n"
"ds_read_b64 v[72:73], v24 offset:768\n"
"v_mfma_f32_16x16x32_f16 v[52:55], v[40:43], v[84:87], v[52:55]\n"
"ds_read_b64 v[74:75], v24 offset:776\n"
"s_waitcnt lgkmcnt(7)\n"
"v_mfma_f32_16x16x32_f16 v[56:59], v[40:43], v[76:79], v[56:59]\n"
"ds_read_b64 v[40:41], v24 offset:1280\n"
"v_mfma_f32_16x16x32_f16 v[52:55], v[92:95], v[76:79], v[52:55]\n"
"ds_read_b64 v[42:43], v24 offset:1288\n"
"v_cvt_pk_f16_f32 v80, v64, v65\n"
"v_mfma_f32_16x16x32_f16 v[48:51], v[92:95], v[84:87], v[48:51]\n"
"ds_read_b128 v[84:87], v25 offset:1472\n"
"v_cvt_pk_f16_f32 v81, v66, v67\n"
"v_mfma_f32_16x16x32_f16 v[48:51], v[100:103], v[76:79], v[48:51]\n"
"ds_read_b128 v[92:95], v25 offset:1456\n"
"s_waitcnt lgkmcnt(4)\n"
"v_mfma_f32_16x16x32_f16 v[48:51], v[88:91], v[96:99], v[48:51]\n"
"ds_read_b64 v[76:77], v24 offset:192\n"
"ds_read_b64 v[78:79], v24 offset:200\n"
"v_mfma_f32_16x16x32_f16 v[52:55], v[88:91], v[44:47], v[52:55]\n"
"ds_read_b128 v[100:103], v25 offset:704\n"
"v_cvt_pk_f16_f32 v82, v68, v69\n"
"v_mfma_f32_16x16x32_f16 v[56:59], v[88:91], v[104:107], v[56:59]\n"
"ds_read_b128 v[96:99], v25 offset:1216\n"
"v_cvt_pk_f16_f32 v83, v70, v71\n"
"v_mfma_f32_16x16x32_f16 v[52:55], v[72:75], v[104:107], v[52:55]\n"
"ds_read_b128 v[88:91], v25 offset:1728\n"
"s_waitcnt lgkmcnt(7)\n"
"v_mfma_f32_16x16x32_f16 v[48:51], v[72:75], v[44:47], v[48:51]\n"
"ds_read_b64 v[44:45], v24 offset:704\n"
"v_mfma_f32_16x16x32_f16 v[48:51], v[40:43], v[104:107], v[48:51]\n"
"ds_read_b64 v[46:47], v24 offset:712\n"
"ds_read_b64 v[72:73], v24 offset:1216\n"
"v_mfma_f32_16x16x32_f16 v[52:55], v[80:83], v[128:131], v[52:55]\n"
"ds_read_b64 v[74:75], v24 offset:1224\n"
"ds_read_b64 v[104:105], v24 offset:128\n"
"v_mfma_f32_16x16x32_f16 v[56:59], v[80:83], v[132:135], v[56:59]\n"
"ds_read_b64 v[106:107], v24 offset:136\n"
"s_waitcnt lgkmcnt(11)\n"
"v_mfma_f32_16x16x32_f16 v[60:63], v[80:83], v[136:139], v[60:63]\n"
"ds_read_b128 v[40:43], v25 offset:640\n"
"v_mfma_f32_16x16x32_f16 v[64:67], v[84:87], v[144:147], 0\n"
"v_mfma_f32_16x16x32_f16 v[68:71], v[92:95], v[148:151], 0\n"
"ds_read_b128 v[92:95], v25 offset:1152\n"
"v_mfma_f32_16x16x32_f16 v[56:59], v[152:155], v[84:87], v[56:59]\n"
"ds_read_b128 v[84:87], v25 offset:1664\n"
"s_waitcnt lgkmcnt(7)\n"
"v_mfma_f32_16x16x32_f16 v[48:51], v[76:79], v[100:103], v[48:51]\n"
"ds_read_b64 v[100:101], v24 offset:640\n"
"v_mfma_f32_16x16x32_f16 v[52:55], v[76:79], v[96:99], v[52:55]\n"
"ds_read_b64 v[102:103], v24 offset:648\n"
"v_mfma_f32_16x16x32_f16 v[56:59], v[76:79], v[88:91], v[56:59]\n"
"ds_read_b64 v[76:77], v24 offset:1152\n"
"v_mfma_f32_16x16x32_f16 v[52:55], v[44:47], v[88:91], v[52:55]\n"
"ds_read_b64 v[78:79], v24 offset:1160\n"
"s_waitcnt lgkmcnt(5)\n"
"v_mfma_f32_16x16x32_f16 v[48:51], v[44:47], v[96:99], v[48:51]\n"
"ds_read_b128 v[96:99], v25 offset:1984\n"
"v_cvt_pk_f16_f32 v80, v64, v65\n"
"v_mfma_f32_16x16x32_f16 v[48:51], v[72:75], v[88:91], v[48:51]\n"
"ds_read_b128 v[44:47], v25 offset:1968\n"
"ds_read_b64 v[88:89], v24 offset:64\n"
"v_mfma_f32_16x16x32_f16 v[48:51], v[104:107], v[40:43], v[48:51]\n"
"ds_read_b64 v[90:91], v24 offset:72\n"
"ds_read_b128 v[72:75], v25 offset:576\n"
"v_mfma_f32_16x16x32_f16 v[52:55], v[104:107], v[92:95], v[52:55]\n"
"ds_read_b128 v[40:43], v25 offset:1088\n"
"s_waitcnt lgkmcnt(6)\n"
"v_mfma_f32_16x16x32_f16 v[56:59], v[104:107], v[84:87], v[56:59]\n"
"ds_read_b128 v[104:107], v25 offset:1600\n"
"v_cvt_pk_f16_f32 v81, v66, v67\n"
"v_mfma_f32_16x16x32_f16 v[52:55], v[100:103], v[84:87], v[52:55]\n"
"v_cvt_pk_f16_f32 v82, v68, v69\n"
"v_cvt_pk_f16_f32 v83, v70, v71\n"
"v_mfma_f32_16x16x32_f16 v[48:51], v[100:103], v[92:95], v[48:51]\n"
"ds_read_b64 v[92:93], v24 offset:576\n"
"ds_read_b64 v[94:95], v24 offset:584\n"
"v_mfma_f32_16x16x32_f16 v[48:51], v[76:79], v[84:87], v[48:51]\n"
"ds_read_b64 v[100:101], v24 offset:1088\n"
"s_waitcnt lgkmcnt(8)\n"
"v_mfma_f32_16x16x32_f16 v[56:59], v[80:83], v[128:131], v[56:59]\n"
"ds_read_b64 v[102:103], v24 offset:1096\n"
"ds_read_b64 v[84:85], v24 offset:0\n"
"v_mfma_f32_16x16x32_f16 v[60:63], v[80:83], v[132:135], v[60:63]\n"
"ds_read_b64 v[86:87], v24 offset:8\n"
"v_mfma_f32_16x16x32_f16 v[64:67], v[96:99], v[144:147], 0\n"
"ds_read_b128 v[76:79], v25 offset:512\n"
"v_mfma_f32_16x16x32_f16 v[68:71], v[44:47], v[148:151], 0\n"
"ds_read_b128 v[44:47], v25 offset:1024\n"
"s_waitcnt lgkmcnt(8)\n"
"v_mfma_f32_16x16x32_f16 v[60:63], v[152:155], v[96:99], v[60:63]\n"
"ds_read_b128 v[96:99], v25 offset:1536\n"
"v_mfma_f32_16x16x32_f16 v[48:51], v[88:91], v[72:75], v[48:51]\n"
"ds_read_b64 v[72:73], v24 offset:512\n"
"v_mfma_f32_16x16x32_f16 v[52:55], v[88:91], v[40:43], v[52:55]\n"
"ds_read_b64 v[74:75], v24 offset:520\n"
"v_mfma_f32_16x16x32_f16 v[56:59], v[88:91], v[104:107], v[56:59]\n"
"ds_read_b64 v[88:89], v24 offset:1024\n"
"s_waitcnt lgkmcnt(5)\n"
"v_mfma_f32_16x16x32_f16 v[52:55], v[92:95], v[104:107], v[52:55]\n"
"ds_read_b64 v[90:91], v24 offset:1032\n"
"v_cvt_pk_f16_f32 v80, v64, v65\n"
"v_mfma_f32_16x16x32_f16 v[48:51], v[92:95], v[40:43], v[48:51]\n"
"v_cvt_pk_f16_f32 v81, v66, v67\n"
"v_cvt_pk_f16_f32 v82, v68, v69\n"
"v_mfma_f32_16x16x32_f16 v[48:51], v[100:103], v[104:107], v[48:51]\n"
"v_cvt_pk_f16_f32 v83, v70, v71\n"
"v_mfma_f32_16x16x32_f16 v[48:51], v[84:87], v[76:79], v[48:51]\n"
"s_waitcnt lgkmcnt(2)\n"
"v_mfma_f32_16x16x32_f16 v[52:55], v[84:87], v[44:47], v[52:55]\n"
"v_mfma_f32_16x16x32_f16 v[56:59], v[84:87], v[96:99], v[56:59]\n"
"v_mfma_f32_16x16x32_f16 v[52:55], v[72:75], v[96:99], v[52:55]\n"
"v_mfma_f32_16x16x32_f16 v[48:51], v[72:75], v[44:47], v[48:51]\n"
"s_waitcnt lgkmcnt(0)\n"
"v_mfma_f32_16x16x32_f16 v[48:51], v[88:91], v[96:99], v[48:51]\n"
"v_mfma_f32_16x16x32_f16 v[60:63], v[80:83], v[128:131], v[60:63]\n"
"s_setprio 2\n"
"s_mov_b32 s17, 0x100000\n"
".Ltp_448:\n"
"ds_read_b32 v3, v5 offset:256\n"
"s_sub_u32 s17, s17, 1\n"
"s_cbranch_scc1 .Lou_449\n"
"s_waitcnt lgkmcnt(0)\n"
"v_cmp_eq_u32 vcc, 0, v3\n"
"s_cbranch_vccnz .Ltp_448\n"
".Lou_449:\n"
"s_waitcnt lgkmcnt(0)\n"
"buffer_load_dwordx4 v[110:113], v126, s[4:7], s32 offen offset:0 sc1\n"
"buffer_load_dwordx4 v[114:117], v126, s[4:7], s32 offen offset:128 sc1\n"
"buffer_load_dwordx4 v[118:121], v126, s[4:7], s33 offen offset:0 sc1\n"
"buffer_load_dwordx4 v[122:125], v126, s[4:7], s33 offen offset:128 sc1\n"
"s_mov_b32 s38, 0x05040100\n"
"s_mov_b32 s39, 0x07060302\n"
"s_mov_b32 s17, 32768\n"
".Ltp_450:\n"
"s_waitcnt vmcnt(0)\n"
"s_sub_u32 s17, s17, 1\n"
"s_cbranch_scc1 .Lou_451\n"
"v_bitop3_b32 v64, v111, v113, s16 bitop3:0x7e\n"
"v_bitop3_b32 v65, v115, v117, s16 bitop3:0x7e\n"
"v_bitop3_b32 v66, v119, v121, s16 bitop3:0x7e\n"
"v_bitop3_b32 v67, v123, v125, s16 bitop3:0x7e\n"
"v_or3_b32 v68, v64, v65, v66\n"
"v_or_b32 v68, v68, v67\n"
"v_cmp_ne_u32 vcc, 0, v68\n"
"s_cbranch_vccz .Lou_451\n"
"s_sleep 2\n"
"v_cmp_ne_u32 vcc, 0, v64\n"
"s_cbranch_vccz .Lsk_452\n"
"s_mov_b64 exec, vcc\n"
"buffer_load_dwordx4 v[110:113], v126, s[4:7], s32 offen offset:0 sc1\n"
"s_mov_b64 exec, -1\n"
".Lsk_452:\n"
"v_cmp_ne_u32 vcc, 0, v65\n"
"s_cbranch_vccz .Lsk_453\n"
"s_mov_b64 exec, vcc\n"
"buffer_load_dwordx4 v[114:117], v126, s[4:7], s32 offen offset:128 sc1\n"
"s_mov_b64 exec, -1\n"
".Lsk_453:\n"
"v_cmp_ne_u32 vcc, 0, v66\n"
"s_cbranch_vccz .Lsk_454\n"
"s_mov_b64 exec, vcc\n"
"buffer_load_dwordx4 v[118:121], v126, s[4:7], s33 offen offset:0 sc1\n"
"s_mov_b64 exec, -1\n"
".Lsk_454:\n"
"v_cmp_ne_u32 vcc, 0, v67\n"
"s_cbranch_vccz .Lsk_455\n"
"s_mov_b64 exec, vcc\n"
"buffer_load_dwordx4 v[122:125], v126, s[4:7], s33 offen offset:128 sc1\n"
"s_mov_b64 exec, -1\n"
".Lsk_455:\n"
"s_branch .Ltp_450\n"
".Lou_451:\n"
"s_waitcnt vmcnt(0)\n"
"s_mov_b32 s89, 2\n"
".Ldl_456:\n"
"s_sub_u32 s89, s89, 1\n"
"s_cbranch_scc1 .Lde_457\n"
"s_sleep 1\n"
"s_branch .Ldl_456\n"
".Lde_457:\n"
"buffer_load_dwordx4 v[88:91], v30, s[4:7], s34 offen sc1\n"
"buffer_load_dwordx4 v[92:95], v30, s[4:7], s35 offen sc1\n"
"v_perm_b32 v64, v114, v110, s38\n"
"v_perm_b32 v65, v114, v110, s39\n"
"v_perm_b32 v66, v116, v112, s38\n"
"v_perm_b32 v67, v116, v112, s39\n"
"ds_write_b32 v32, v64 offset:0\n"
"ds_write_b32 v33, v65 offset:2048\n"
"ds_write_b32 v32, v66 offset:4224\n"
"ds_write_b32 v33, v67 offset:6272\n"
"v_perm_b32 v64, v122, v118, s38\n"
"v_perm_b32 v65, v122, v118, s39\n"
"v_perm_b32 v66, v124, v120, s38\n"
"v_perm_b32 v67, v124, v120, s39\n"
"ds_write_b32 v32, v64 offset:1024\n"
"ds_write_b32 v33, v65 offset:3072\n"
"ds_write_b32 v32, v66 offset:5248\n"
"ds_write_b32 v33, v67 offset:7296\n"
"v_lshlrev_b32 v64, 16, v110\n"
"v_lshlrev_b32 v65, 16, v114\n"
"v_and_b32 v66, 0xffff0000, v110\n"
"v_and_b32 v67, 0xffff0000, v114\n"
"v_lshlrev_b32 v68, 16, v112\n"
"v_lshlrev_b32 v69, 16, v116\n"
"v_and_b32 v70, 0xffff0000, v112\n"
"v_and_b32 v71, 0xffff0000, v116\n"
"v_lshlrev_b32 v72, 16, v118\n"
"v_lshlrev_b32 v73, 16, v122\n"
"v_and_b32 v74, 0xffff0000, v118\n"
"v_and_b32 v75, 0xffff0000, v122\n"
"v_lshlrev_b32 v76, 16, v120\n"
"v_lshlrev_b32 v77, 16, v124\n"
"v_and_b32 v78, 0xffff0000, v120\n"
"v_and_b32 v79, 0xffff0000, v124\n"
"s_mov_b32 s17, 32768\n"
".Ltp_458:\n"
"s_waitcnt vmcnt(0)\n"
"s_sub_u32 s17, s17, 1\n"
"s_cbranch_scc1 .Lou_459\n"
"v_max_u32 v110, v88, v89\n"
"v_max3_u32 v110, v90, v91, v110\n"
"v_max3_u32 v110, v92, v93, v110\n"
"v_max3_u32 v110, v94, v95, v110\n"
"v_cmp_eq_u32 vcc, -1, v110\n"
"s_cbranch_vccz .Lou_459\n"
"v_max_u32 v110, v90, v91\n"
"v_max3_u32 v110, v88, v89, v110\n"
"v_cmp_eq_u32 vcc, -1, v110\n"
"s_cbranch_vccz .Lsk_460\n"
"s_mov_b64 exec, vcc\n"
"buffer_load_dwordx4 v[88:91], v30, s[4:7], s34 offen sc1\n"
"s_mov_b64 exec, -1\n"
".Lsk_460:\n"
"v_max_u32 v110, v94, v95\n"
"v_max3_u32 v110, v92, v93, v110\n"
"v_cmp_eq_u32 vcc, -1, v110\n"
"s_cbranch_vccz .Lsk_461\n"
"s_mov_b64 exec, vcc\n"
"buffer_load_dwordx4 v[92:95], v30, s[4:7], s35 offen sc1\n"
"s_mov_b64 exec, -1\n"
".Lsk_461:\n"
"s_branch .Ltp_458\n"
".Lou_459:\n"
"s_waitcnt vmcnt(0)\n"
"v_fma_f32 v64, v64, v89, v88\n"
"v_fma_f32 v65, v65, v91, v90\n"
"v_cvt_pk_f16_f32 v64, v64, v65\n"
"v_fma_f32 v66, v66, v89, v88\n"
"v_fma_f32 v67, v67, v91, v90\n"
"v_cvt_pk_f16_f32 v66, v66, v67\n"
"v_fma_f32 v68, v68, v89, v88\n"
"v_fma_f32 v69, v69, v91, v90\n"
"v_cvt_pk_f16_f32 v68, v68, v69\n"
"v_fma_f32 v70, v70, v89, v88\n"
"v_fma_f32 v71, v71, v91, v90\n"
"v_cvt_pk_f16_f32 v70, v70, v71\n"
"ds_write2st64_b32 v31, v64, v66 offset0:0 offset1:18\n"
"ds_write2st64_b32 v31, v68, v70 offset0:36 offset1:54\n"
"v_fma_f32 v72, v72, v93, v92\n"
"v_fma_f32 v73, v73, v95, v94\n"
"v_cvt_pk_f16_f32 v72, v72, v73\n"
"v_fma_f32 v74, v74, v93, v92\n"
"v_fma_f32 v75, v75, v95, v94\n"
"v_cvt_pk_f16_f32 v74, v74, v75\n"
"v_fma_f32 v76, v76, v93, v92\n"
"v_fma_f32 v77, v77, v95, v94\n"
"v_cvt_pk_f16_f32 v76, v76, v77\n"
"v_fma_f32 v78, v78, v93, v92\n"
"v_fma_f32 v79, v79, v95, v94\n"
"v_cvt_pk_f16_f32 v78, v78, v79\n"
"ds_write2st64_b32 v31, v72, v74 offset0:4 offset1:22\n"
"ds_write2st64_b32 v31, v76, v78 offset0:40 offset1:58\n"
"ds_write_b32 v4, v4 offset:656\n"
"s_mov_b32 s17, 0x100000\n"
".Ltp_462:\n"
"ds_read_b32 v3, v5 offset:656\n"
"s_sub_u32 s17, s17, 1\n"
"s_cbranch_scc1 .Lou_463\n"
"s_waitcnt lgkmcnt(0)\n"
"v_cmp_eq_u32 vcc, 0, v3\n"
"s_cbranch_vccnz .Ltp_462\n"
".Lou_463:\n"
"s_waitcnt lgkmcnt(0)\n"
"s_mov_b32 s36, 0xfffefffe\n"
"ds_read_b64 v[88:89], v24 offset:1984\n"
"ds_read_b64 v[90:91], v24 offset:1992\n"
"ds_read_b128 v[92:95], v25 offset:4032\n"
"ds_read_b128 v[96:99], v25 offset:3520\n"
"ds_read_b64 v[100:101], v24 offset:1472\n"
"ds_read_b64 v[102:103], v24 offset:1480\n"
"ds_read_b128 v[104:107], v25 offset:3008\n"
"s_waitcnt lgkmcnt(0)\n"
"v_mfma_f32_16x16x32_f16 v[60:63], v[88:91], v[92:95], v[60:63]\n"
"ds_read_b64 v[108:109], v24 offset:960\n"
"ds_read_b64 v[110:111], v24 offset:968\n"
"v_mfma_f32_16x16x32_f16 v[56:59], v[88:91], v[96:99], v[56:59]\n"
"ds_read_b128 v[112:115], v25 offset:2496\n"
"ds_read_b64 v[116:117], v24 offset:448\n"
"v_mfma_f32_16x16x32_f16 v[60:63], v[100:103], v[96:99], v[60:63]\n"
"ds_read_b64 v[118:119], v24 offset:456\n"
"ds_read_b64 v[120:121], v24 offset:1920\n"
"v_mfma_f32_16x16x32_f16 v[52:55], v[88:91], v[104:107], v[52:55]\n"
"ds_read_b64 v[122:123], v24 offset:1928\n"
"s_waitcnt lgkmcnt(4)\n"
"v_mfma_f32_16x16x32_f16 v[56:59], v[100:103], v[104:107], v[56:59]\n"
"ds_read_b128 v[124:127], v25 offset:3968\n"
"ds_read_b128 v[40:43], v25 offset:3456\n"
"v_mfma_f32_16x16x32_f16 v[60:63], v[108:111], v[104:107], v[60:63]\n"
"ds_read_b64 v[44:45], v24 offset:1408\n"
"ds_read_b64 v[46:47], v24 offset:1416\n"
"v_mfma_f32_16x16x32_f16 v[48:51], v[88:91], v[112:115], v[48:51]\n"
"ds_read_b128 v[92:95], v25 offset:2944\n"
"ds_read_b64 v[96:97], v24 offset:896\n"
"v_mfma_f32_16x16x32_f16 v[52:55], v[100:103], v[112:115], v[52:55]\n"
"ds_read_b64 v[98:99], v24 offset:904\n"
"s_waitcnt lgkmcnt(5)\n"
"v_mfma_f32_16x16x32_f16 v[56:59], v[108:111], v[112:115], v[56:59]\n"
"ds_read_b128 v[104:107], v25 offset:2432\n"
"ds_read_b64 v[88:89], v24 offset:384\n"
"v_mfma_f32_16x16x32_f16 v[60:63], v[116:119], v[112:115], v[60:63]\n"
"ds_read_b64 v[90:91], v24 offset:392\n"
"ds_read_b64 v[100:101], v24 offset:1856\n"
"v_mfma_f32_16x16x32_f16 v[60:63], v[120:123], v[124:127], v[60:63]\n"
"ds_read_b64 v[102:103], v24 offset:1864\n"
"ds_read_b128 v[108:111], v25 offset:3904\n"
"v_mfma_f32_16x16x32_f16 v[56:59], v[120:123], v[40:43], v[56:59]\n"
"ds_read_b128 v[112:115], v25 offset:3392\n"
"s_waitcnt lgkmcnt(7)\n"
"v_mfma_f32_16x16x32_f16 v[60:63], v[44:47], v[40:43], v[60:63]\n"
"ds_read_b64 v[116:117], v26 offset:0\n"
"ds_read_b64 v[124:125], v24 offset:1344\n"
"v_mfma_f32_16x16x32_f16 v[52:55], v[120:123], v[92:95], v[52:55]\n"
"ds_read_b64 v[126:127], v24 offset:1352\n"
"v_mfma_f32_16x16x32_f16 v[56:59], v[44:47], v[92:95], v[56:59]\n"
"ds_read_b128 v[40:43], v25 offset:2880\n"
"v_mfma_f32_16x16x32_f16 v[60:63], v[96:99], v[92:95], v[60:63]\n"
"ds_read_b64 v[92:93], v24 offset:832\n"
"s_waitcnt lgkmcnt(9)\n"
"v_mfma_f32_16x16x32_f16 v[48:51], v[120:123], v[104:107], v[48:51]\n"
"ds_read_b64 v[94:95], v24 offset:840\n"
"v_mfma_f32_16x16x32_f16 v[52:55], v[44:47], v[104:107], v[52:55]\n"
"ds_read_b128 v[120:123], v25 offset:2368\n"
"v_mfma_f32_16x16x32_f16 v[56:59], v[96:99], v[104:107], v[56:59]\n"
"ds_read_b64 v[44:45], v24 offset:320\n"
"v_mfma_f32_16x16x32_f16 v[60:63], v[88:91], v[104:107], v[60:63]\n"
"ds_read_b64 v[46:47], v24 offset:328\n"
"s_waitcnt lgkmcnt(6)\n"
"v_mfma_f32_16x16x32_f16 v[60:63], v[100:103], v[108:111], v[60:63]\n"
"ds_read_b64 v[96:97], v26 offset:512\n"
"ds_read_b64 v[104:105], v24 offset:1792\n"
"v_mfma_f32_16x16x32_f16 v[56:59], v[100:103], v[112:115], v[56:59]\n"
"ds_read_b64 v[106:107], v24 offset:1800\n"
"ds_read_b128 v[88:91], v25 offset:3840\n"
"v_mfma_f32_16x16x16_bf16 v[48:51], v[34:35], v[116:117], v[48:51]\n"
"ds_read_b128 v[108:111], v25 offset:3328\n"
"ds_read_b64 v[116:117], v24 offset:1280\n"
"v_mfma_f32_16x16x32_f16 v[60:63], v[124:127], v[112:115], v[60:63]\n"
"ds_read_b64 v[118:119], v24 offset:1288\n"
"s_waitcnt lgkmcnt(9)\n"
"v_mfma_f32_16x16x32_f16 v[52:55], v[100:103], v[40:43], v[52:55]\n"
"ds_read_b128 v[112:115], v25 offset:2816\n"
"s_and_b32 s79, s18, 0xfc\n"
"v_mfma_f32_16x16x32_f16 v[56:59], v[124:127], v[40:43], v[56:59]\n"
"s_and_b32 s78, s18, 1\n"
"s_lshl_b32 s78, s78, 1\n"
"v_mfma_f32_16x16x32_f16 v[60:63], v[92:95], v[40:43], v[60:63]\n"
"ds_read_b64 v[40:41], v24 offset:768\n"
"s_add_u32 s79, s79, s78\n"
"v_mfma_f32_16x16x32_f16 v[48:51], v[100:103], v[120:123], v[48:51]\n"
"ds_read_b64 v[42:43], v24 offset:776\n"
"s_waitcnt lgkmcnt(9)\n"
"v_mfma_f32_16x16x32_f16 v[52:55], v[124:127], v[120:123], v[52:55]\n"
"ds_read_b128 v[100:103], v25 offset:2304\n"
"s_lshr_b32 s78, s18, 1\n"
"v_mfma_f32_16x16x32_f16 v[56:59], v[92:95], v[120:123], v[56:59]\n"
"ds_read_b64 v[124:125], v24 offset:256\n"
"s_and_b32 s78, s78, 1\n"
"v_mfma_f32_16x16x32_f16 v[60:63], v[44:47], v[120:123], v[60:63]\n"
"ds_read_b64 v[126:127], v24 offset:264\n"
"ds_read_b64 v[92:93], v24 offset:1728\n"
"v_mfma_f32_16x16x16_bf16 v[52:55], v[34:35], v[96:97], v[52:55]\n"
"ds_read_b64 v[94:95], v24 offset:1736\n"
"s_waitcnt lgkmcnt(7)\n"
"v_mfma_f32_16x16x32_f16 v[60:63], v[104:107], v[88:91], v[60:63]\n"
"ds_read_b128 v[120:123], v25 offset:3776\n"
"ds_read_b128 v[44:47], v25 offset:3264\n"
"v_mfma_f32_16x16x32_f16 v[56:59], v[104:107], v[108:111], v[56:59]\n"
"ds_read_b64 v[96:97], v24 offset:1216\n"
"ds_read_b64 v[98:99], v24 offset:1224\n"
"v_mfma_f32_16x16x32_f16 v[60:63], v[116:119], v[108:111], v[60:63]\n"
"ds_read_b128 v[88:91], v25 offset:2752\n"
"s_add_u32 s79, s79, s78\n"
"v_mfma_f32_16x16x32_f16 v[52:55], v[104:107], v[112:115], v[52:55]\n"
"ds_read_b64 v[108:109], v26 offset:1024\n"
"s_waitcnt lgkmcnt(10)\n"
"v_mfma_f32_16x16x32_f16 v[56:59], v[116:119], v[112:115], v[56:59]\n"
"s_lshl_b32 s79, s79, 7\n"
"s_add_u32 s32, s79, 0xd68000\n"
"v_mfma_f32_16x16x32_f16 v[60:63], v[40:43], v[112:115], v[60:63]\n"
"ds_read_b64 v[112:113], v24 offset:704\n"
"v_mfma_f32_16x16x32_f16 v[48:51], v[104:107], v[100:103], v[48:51]\n"
"ds_read_b64 v[114:115], v24 offset:712\n"
"v_mfma_f32_16x16x32_f16 v[52:55], v[116:119], v[100:103], v[52:55]\n"
"ds_read_b128 v[104:107], v25 offset:2240\n"
"s_waitcnt lgkmcnt(7)\n"
"v_mfma_f32_16x16x32_f16 v[56:59], v[40:43], v[100:103], v[56:59]\n"
"ds_read_b64 v[116:117], v24 offset:192\n"
"v_mfma_f32_16x16x32_f16 v[60:63], v[124:127], v[100:103], v[60:63]\n"
"ds_read_b64 v[118:119], v24 offset:200\n"
"ds_read_b64 v[40:41], v24 offset:1664\n"
"v_mfma_f32_16x16x32_f16 v[60:63], v[92:95], v[120:123], v[60:63]\n"
"ds_read_b64 v[42:43], v24 offset:1672\n"
"ds_read_b128 v[100:103], v25 offset:3712\n"
"v_mfma_f32_16x16x32_f16 v[56:59], v[92:95], v[44:47], v[56:59]\n"
"ds_read_b128 v[124:127], v25 offset:3200\n"
"s_waitcnt lgkmcnt(9)\n"
"v_mfma_f32_16x16x32_f16 v[60:63], v[96:99], v[44:47], v[60:63]\n"
"ds_read_b64 v[120:121], v24 offset:1152\n"
"ds_read_b64 v[122:123], v24 offset:1160\n"
"v_mfma_f32_16x16x32_f16 v[52:55], v[92:95], v[88:91], v[52:55]\n"
"ds_read_b128 v[44:47], v25 offset:2688\n"
"v_mfma_f32_16x16x16_bf16 v[56:59], v[34:35], v[108:109], v[56:59]\n"
"ds_read_b64 v[108:109], v24 offset:640\n"
"s_nop 4\n"
"v_mfma_f32_16x16x32_f16 v[56:59], v[96:99], v[88:91], v[56:59]\n"
"ds_read_b64 v[110:111], v24 offset:648\n"
"s_waitcnt lgkmcnt(11)\n"
"v_mfma_f32_16x16x32_f16 v[60:63], v[112:115], v[88:91], v[60:63]\n"
"ds_read_b128 v[88:91], v25 offset:2176\n"
"v_mfma_f32_16x16x32_f16 v[48:51], v[92:95], v[104:107], v[48:51]\n"
"ds_read_b64 v[92:93], v26 offset:1536\n"
"v_mfma_f32_16x16x32_f16 v[52:55], v[96:99], v[104:107], v[52:55]\n"
"v_mfma_f32_16x16x32_f16 v[56:59], v[112:115], v[104:107], v[56:59]\n"
"ds_read_b64 v[96:97], v24 offset:128\n"
"s_waitcnt lgkmcnt(6)\n"
"v_mfma_f32_16x16x32_f16 v[60:63], v[116:119], v[104:107], v[60:63]\n"
"ds_read_b64 v[98:99], v24 offset:136\n"
"ds_read_b64 v[112:113], v24 offset:1600\n"
"v_mfma_f32_16x16x32_f16 v[60:63], v[40:43], v[100:103], v[60:63]\n"
"ds_read_b64 v[114:115], v24 offset:1608\n"
"ds_read_b128 v[104:107], v25 offset:3648\n"
"v_mfma_f32_16x16x32_f16 v[56:59], v[40:43], v[124:127], v[56:59]\n"
"ds_read_b128 v[116:119], v25 offset:3136\n"
"ds_read_b64 v[100:101], v24 offset:1088\n"
"v_mfma_f32_16x16x32_f16 v[60:63], v[120:123], v[124:127], v[60:63]\n"
"ds_read_b64 v[102:103], v24 offset:1096\n"
"s_waitcnt lgkmcnt(9)\n"
"v_mfma_f32_16x16x32_f16 v[52:55], v[40:43], v[44:47], v[52:55]\n"
"ds_read_b128 v[124:127], v25 offset:2624\n"
"v_mfma_f32_16x16x32_f16 v[56:59], v[120:123], v[44:47], v[56:59]\n"
"v_mfma_f32_16x16x32_f16 v[60:63], v[108:111], v[44:47], v[60:63]\n"
"ds_read_b64 v[44:45], v24 offset:576\n"
"v_mfma_f32_16x16x32_f16 v[48:51], v[40:43], v[88:91], v[48:51]\n"
"ds_read_b64 v[46:47], v24 offset:584\n"
"s_waitcnt lgkmcnt(9)\n"
"s_nop 0\n"
"v_mfma_f32_16x16x16_bf16 v[60:63], v[34:35], v[92:93], v[60:63]\n"
"ds_read_b128 v[40:43], v25 offset:2112\n"
"v_mfma_f32_16x16x32_f16 v[52:55], v[120:123], v[88:91], v[52:55]\n"
"ds_read_b64 v[92:93], v24 offset:64\n"
"v_mfma_f32_16x16x32_f16 v[56:59], v[108:111], v[88:91], v[56:59]\n"
"ds_read_b64 v[94:95], v24 offset:72\n"
"ds_read_b64 v[120:121], v24 offset:1536\n"
"v_mfma_f32_16x16x32_f16 v[60:63], v[96:99], v[88:91], v[60:63]\n"
"ds_read_b64 v[122:123], v24 offset:1544\n"
"s_waitcnt lgkmcnt(7)\n"
"v_mfma_f32_16x16x32_f16 v[60:63], v[112:115], v[104:107], v[60:63]\n"
"ds_read_b128 v[108:111], v25 offset:2048\n"
"ds_read_b128 v[88:91], v25 offset:2560\n"
"v_mfma_f32_16x16x32_f16 v[56:59], v[112:115], v[116:119], v[56:59]\n"
"ds_read_b64 v[96:97], v24 offset:1024\n"
"ds_read_b64 v[98:99], v24 offset:1032\n"
"v_mfma_f32_16x16x32_f16 v[60:63], v[100:103], v[116:119], v[60:63]\n"
"ds_read_b128 v[104:107], v25 offset:3072\n"
"v_mfma_f32_16x16x32_f16 v[52:55], v[112:115], v[124:127], v[52:55]\n"
"ds_read_b64 v[116:117], v24 offset:512\n"
"s_waitcnt lgkmcnt(10)\n"
"v_mfma_f32_16x16x32_f16 v[56:59], v[100:103], v[124:127], v[56:59]\n"
"ds_read_b64 v[118:119], v24 offset:520\n"
"v_mfma_f32_16x16x32_f16 v[60:63], v[44:47], v[124:127], v[60:63]\n"
"ds_read_b128 v[124:127], v25 offset:3584\n"
"v_mfma_f32_16x16x32_f16 v[48:51], v[112:115], v[40:43], v[48:51]\n"
"v_mfma_f32_16x16x32_f16 v[52:55], v[100:103], v[40:43], v[52:55]\n"
"ds_read_b64 v[112:113], v24 offset:0\n"
"s_waitcnt lgkmcnt(7)\n"
"v_mfma_f32_16x16x32_f16 v[56:59], v[44:47], v[40:43], v[56:59]\n"
"ds_read_b64 v[114:115], v24 offset:8\n"
"v_mfma_f32_16x16x32_f16 v[60:63], v[92:95], v[40:43], v[60:63]\n"
"v_mfma_f32_16x16x32_f16 v[48:51], v[120:123], v[108:111], v[48:51]\n"
"v_mfma_f32_16x16x32_f16 v[52:55], v[120:123], v[88:91], v[52:55]\n"
"s_waitcnt lgkmcnt(3)\n"
"v_mfma_f32_16x16x32_f16 v[52:55], v[96:99], v[108:111], v[52:55]\n"
"v_mfma_f32_16x16x32_f16 v[56:59], v[120:123], v[104:107], v[56:59]\n"
"v_mul_f32 v48, v36, v48\n"
"v_mul_f32 v49, v36, v49\n"
"v_mul_f32 v50, v36, v50\n"
"v_mfma_f32_16x16x32_f16 v[56:59], v[96:99], v[88:91], v[56:59]\n"
"v_mul_f32 v51, v36, v51\n"
"v_cvt_pk_bf16_f32 v48, v48, v49\n"
"v_cvt_pk_bf16_f32 v49, v50, v51\n"
"v_mfma_f32_16x16x32_f16 v[56:59], v[116:119], v[108:111], v[56:59]\n"
"v_pk_min_u16 v48, v48, s36\n"
"v_pk_min_u16 v49, v49, s36\n"
"s_waitcnt lgkmcnt(0)\n"
"v_mfma_f32_16x16x32_f16 v[60:63], v[120:123], v[124:127], v[60:63]\n"
"ds_write_b64 v26, v[48:49] offset:0\n"
"v_mfma_f32_16x16x32_f16 v[60:63], v[96:99], v[104:107], v[60:63]\n"
"v_mul_f32 v52, v36, v52\n"
"v_mul_f32 v53, v36, v53\n"
"v_mfma_f32_16x16x32_f16 v[60:63], v[116:119], v[88:91], v[60:63]\n"
"v_mul_f32 v54, v36, v54\n"
"v_mul_f32 v55, v36, v55\n"
"v_mfma_f32_16x16x32_f16 v[60:63], v[112:115], v[108:111], v[60:63]\n"
"v_cvt_pk_bf16_f32 v52, v52, v53\n"
"v_cvt_pk_bf16_f32 v53, v54, v55\n"
"v_pk_min_u16 v52, v52, s36\n"
"v_pk_min_u16 v53, v53, s36\n"
"ds_write_b64 v26, v[52:53] offset:512\n"
"v_mul_f32 v56, v36, v56\n"
"v_mul_f32 v57, v36, v57\n"
"v_mul_f32 v58, v36, v58\n"
"v_mul_f32 v59, v36, v59\n"
"v_cvt_pk_bf16_f32 v56, v56, v57\n"
"v_cvt_pk_bf16_f32 v57, v58, v59\n"
"v_pk_min_u16 v56, v56, s36\n"
"v_pk_min_u16 v57, v57, s36\n"
"ds_write_b64 v26, v[56:57] offset:1024\n"
"v_mul_f32 v60, v36, v60\n"
"v_mul_f32 v61, v36, v61\n"
"v_mul_f32 v62, v36, v62\n"
"v_mul_f32 v63, v36, v63\n"
"v_cvt_pk_bf16_f32 v60, v60, v61\n"
"v_cvt_pk_bf16_f32 v61, v62, v63\n"
"v_pk_min_u16 v60, v60, s36\n"
"v_pk_min_u16 v61, v61, s36\n"
"ds_write_b64 v26, v[60:61] offset:1536\n"
"ds_write_b32 v4, v4 offset:672\n"
"s_mov_b32 s17, 0x100000\n"
".Ltp_464:\n"
"ds_read_b32 v3, v5 offset:672\n"
"s_sub_u32 s17, s17, 1\n"
"s_cbranch_scc1 .Lou_465\n"
"s_waitcnt lgkmcnt(0)\n"
"v_cmp_eq_u32 vcc, 0, v3\n"
"s_cbranch_vccnz .Ltp_464\n"
".Lou_465:\n"
"s_waitcnt lgkmcnt(0)\n"
"ds_read_b64_tr_b16 v[64:65], v27 offset:0\n"
"ds_read_b64_tr_b16 v[66:67], v28 offset:0\n"
"ds_read_b64_tr_b16 v[68:69], v27 offset:1024\n"
"ds_read_b64_tr_b16 v[70:71], v28 offset:1024\n"
"s_add_u32 s33, s32, 0xa0000\n"
"s_waitcnt lgkmcnt(2)\n"
"buffer_store_dwordx4 v[64:67], v29, s[4:7], s32 offen sc1\n"
"s_waitcnt lgkmcnt(0)\n"
"buffer_store_dwordx4 v[68:71], v29, s[4:7], s33 offen sc1\n"
"ds_write_b32 v4, v4 offset:688\n"
"s_nop 0\n"
"s_branch .Ljc_466\n"
".Lnc_441:\n"
"s_mov_b32 s17, 0x100000\n"
".Ltp_467:\n"
"s_sleep 2\n"
"ds_read_b32 v3, v5 offset:272\n"
"s_sub_u32 s17, s17, 1\n"
"s_cbranch_scc1 .Lou_468\n"
"s_waitcnt lgkmcnt(0)\n"
"v_cmp_eq_u32 vcc, 0, v3\n"
"s_cbranch_vccnz .Ltp_467\n"
".Lou_468:\n"
"s_waitcnt lgkmcnt(0)\n"
"s_and_b32 s79, s2, 7\n"
"s_lshl_b32 s79, s79, 14\n"
"s_add_u32 s32, s79, 0xc0a000\n"
"s_add_u32 s33, s32, 0x1000\n"
"buffer_load_dwordx4 v[48:51], v2, s[4:7], s32 offen offset:0 sc1\n"
"buffer_load_dwordx4 v[52:55], v2, s[4:7], s32 offen offset:1024 sc1\n"
"buffer_load_dwordx4 v[56:59], v2, s[4:7], s32 offen offset:2048 sc1\n"
"buffer_load_dwordx4 v[60:63], v2, s[4:7], s32 offen offset:3072 sc1\n"
"buffer_load_dwordx4 v[64:67], v2, s[4:7], s33 offen offset:0 sc1\n"
"buffer_load_dwordx4 v[68:71], v2, s[4:7], s33 offen offset:1024 sc1\n"
"buffer_load_dwordx4 v[72:75], v2, s[4:7], s33 offen offset:2048 sc1\n"
"buffer_load_dwordx4 v[76:79], v2, s[4:7], s33 offen offset:3072 sc1\n"
"s_mov_b32 s17, 32768\n"
".Ltp_469:\n"
"s_waitcnt vmcnt(0)\n"
"s_sub_u32 s17, s17, 1\n"
"s_cbranch_scc1 .Lou_470\n"
"v_max_u32 v80, v48, v49\n"
"v_max3_u32 v80, v50, v51, v80\n"
"v_max3_u32 v80, v52, v53, v80\n"
"v_max3_u32 v80, v54, v55, v80\n"
"v_max3_u32 v80, v56, v57, v80\n"
"v_max3_u32 v80, v58, v59, v80\n"
"v_max3_u32 v80, v60, v61, v80\n"
"v_max3_u32 v80, v62, v63, v80\n"
"v_max3_u32 v80, v64, v65, v80\n"
"v_max3_u32 v80, v66, v67, v80\n"
"v_max3_u32 v80, v68, v69, v80\n"
"v_max3_u32 v80, v70, v71, v80\n"
"v_max3_u32 v80, v72, v73, v80\n"
"v_max3_u32 v80, v74, v75, v80\n"
"v_max3_u32 v80, v76, v77, v80\n"
"v_max3_u32 v80, v78, v79, v80\n"
"v_cmp_eq_u32 vcc, -1, v80\n"
"s_cbranch_vccz .Lou_470\n"
"s_sleep 4\n"
"v_max_u32 v80, v50, v51\n"
"v_max3_u32 v80, v48, v49, v80\n"
"v_cmp_eq_u32 vcc, -1, v80\n"
"s_cbranch_vccz .Lsk_471\n"
"s_mov_b64 exec, vcc\n"
"buffer_load_dwordx4 v[48:51], v2, s[4:7], s32 offen offset:0 sc1\n"
"s_mov_b64 exec, -1\n"
".Lsk_471:\n"
"v_max_u32 v80, v54, v55\n"
"v_max3_u32 v80, v52, v53, v80\n"
"v_cmp_eq_u32 vcc, -1, v80\n"
"s_cbranch_vccz .Lsk_472\n"
"s_mov_b64 exec, vcc\n"
"buffer_load_dwordx4 v[52:55], v2, s[4:7], s32 offen offset:1024 sc1\n"
"s_mov_b64 exec, -1\n"
".Lsk_472:\n"
"v_max_u32 v80, v58, v59\n"
"v_max3_u32 v80, v56, v57, v80\n"
"v_cmp_eq_u32 vcc, -1, v80\n"
"s_cbranch_vccz .Lsk_473\n"
"s_mov_b64 exec, vcc\n"
"buffer_load_dwordx4 v[56:59], v2, s[4:7], s32 offen offset:2048 sc1\n"
"s_mov_b64 exec, -1\n"
".Lsk_473:\n"
"v_max_u32 v80, v62, v63\n"
"v_max3_u32 v80, v60, v61, v80\n"
"v_cmp_eq_u32 vcc, -1, v80\n"
"s_cbranch_vccz .Lsk_474\n"
"s_mov_b64 exec, vcc\n"
"buffer_load_dwordx4 v[60:63], v2, s[4:7], s32 offen offset:3072 sc1\n"
"s_mov_b64 exec, -1\n"
".Lsk_474:\n"
"v_max_u32 v80, v66, v67\n"
"v_max3_u32 v80, v64, v65, v80\n"
"v_cmp_eq_u32 vcc, -1, v80\n"
"s_cbranch_vccz .Lsk_475\n"
"s_mov_b64 exec, vcc\n"
"buffer_load_dwordx4 v[64:67], v2, s[4:7], s33 offen offset:0 sc1\n"
"s_mov_b64 exec, -1\n"
".Lsk_475:\n"
"v_max_u32 v80, v70, v71\n"
"v_max3_u32 v80, v68, v69, v80\n"
"v_cmp_eq_u32 vcc, -1, v80\n"
"s_cbranch_vccz .Lsk_476\n"
"s_mov_b64 exec, vcc\n"
"buffer_load_dwordx4 v[68:71], v2, s[4:7], s33 offen offset:1024 sc1\n"
"s_mov_b64 exec, -1\n"
".Lsk_476:\n"
"v_max_u32 v80, v74, v75\n"
"v_max3_u32 v80, v72, v73, v80\n"
"v_cmp_eq_u32 vcc, -1, v80\n"
"s_cbranch_vccz .Lsk_477\n"
"s_mov_b64 exec, vcc\n"
"buffer_load_dwordx4 v[72:75], v2, s[4:7], s33 offen offset:2048 sc1\n"
"s_mov_b64 exec, -1\n"
".Lsk_477:\n"
"v_max_u32 v80, v78, v79\n"
"v_max3_u32 v80, v76, v77, v80\n"
"v_cmp_eq_u32 vcc, -1, v80\n"
"s_cbranch_vccz .Lsk_478\n"
"s_mov_b64 exec, vcc\n"
"buffer_load_dwordx4 v[76:79], v2, s[4:7], s33 offen offset:3072 sc1\n"
"s_mov_b64 exec, -1\n"
".Lsk_478:\n"
"s_branch .Ltp_469\n"
".Lou_470:\n"
"s_waitcnt vmcnt(0)\n"
".Ljc_466:\n"
"s_setprio 0\n"
"s_lshr_b32 s79, s2, 1\n"
"s_and_b32 s79, s79, 3\n"
"s_lshl_b32 s79, s79, 2\n"
"s_lshr_b32 s78, s2, 3\n"
"s_lshr_b32 s78, s78, 3\n"
"s_add_u32 s79, s79, s78\n"
"s_lshl_b32 s79, s79, 2\n"
"s_add_u32 s79, s79, 64\n"
"s_mul_i32 s79, s79, 0x5000\n"
"s_mul_i32 s78, s3, 0x1400\n"
"s_add_u32 s79, s79, s78\n"
"s_add_u32 s50, s79, 0xc28000\n"
"s_add_u32 s51, s50, 0x1000\n"
"v_min_i32 v99, 3, v0\n"
"v_mul_i32_i24 v48, 0x500, v99\n"
"s_mul_i32 s79, s3, 0x1400\n"
"s_add_u32 s79, s79, 0xa000\n"
"v_add_u32 v50, s79, v2\n"
"s_and_b32 s79, s2, 7\n"
"s_lshl_b32 s79, s79, 3\n"
"s_add_u32 s79, s79, 0x47\n"
"s_mul_i32 s79, s79, 0x5000\n"
"s_and_b32 s78, s2, 7\n"
"s_lshr_b32 s78, s78, 1\n"
"s_lshl_b32 s78, s78, 7\n"
"s_add_u32 s79, s79, s78\n"
"s_mul_i32 s78, s3, 0x1400\n"
"s_add_u32 s79, s79, s78\n"
"s_add_u32 s49, s79, 0xc28000\n"
"s_mov_b32 s17, 0x100000\n"
".Ltp_479:\n"
"s_sleep 2\n"
"ds_read_b32 v3, v5 offset:3264\n"
"s_sub_u32 s17, s17, 1\n"
"s_cbranch_scc1 .Lou_480\n"
"s_waitcnt lgkmcnt(0)\n"
"v_cmp_eq_u32 vcc, -1, v3\n"
"s_cbranch_vccnz .Ltp_479\n"
".Lou_480:\n"
"s_waitcnt lgkmcnt(0)\n"
"s_mov_b32 s89, 8\n"
".Ldl_481:\n"
"s_sub_u32 s89, s89, 1\n"
"s_cbranch_scc1 .Lde_482\n"
"s_sleep 1\n"
"s_branch .Ldl_481\n"
".Lde_482:\n"
"s_mov_b32 s17, 32768\n"
".Ltp_483:\n"
"buffer_load_dword v49, v48, s[4:7], s49 offen sc1\n"
"s_sub_u32 s17, s17, 1\n"
"s_cbranch_scc1 .Lou_484\n"
"s_waitcnt vmcnt(0)\n"
"v_cmp_eq_u32 vcc, -1, v49\n"
"s_cbranch_vccz .Lou_484\n"
"s_mov_b32 s89, 2\n"
".Ldl_485:\n"
"s_sub_u32 s89, s89, 1\n"
"s_cbranch_scc1 .Lde_486\n"
"s_sleep 1\n"
"s_branch .Ldl_485\n"
".Lde_486:\n"
"s_branch .Ltp_483\n"
".Lou_484:\n"
"s_waitcnt vmcnt(0)\n"
"buffer_load_dwordx4 v[52:55], v2, s[4:7], s50 offen offset:0 sc1\n"
"buffer_load_dwordx4 v[56:59], v2, s[4:7], s50 offen offset:1024 sc1\n"
"buffer_load_dwordx4 v[60:63], v2, s[4:7], s50 offen offset:2048 sc1\n"
"buffer_load_dwordx4 v[64:67], v2, s[4:7], s50 offen offset:3072 sc1\n"
"buffer_load_dwordx4 v[68:71], v2, s[4:7], s51 offen offset:0 sc1\n"
"s_mov_b32 s17, 32768\n"
".Ltp_487:\n"
"s_waitcnt vmcnt(0)\n"
"s_sub_u32 s17, s17, 1\n"
"s_cbranch_scc1 .Lou_488\n"
"v_max_u32 v38, v52, v53\n"
"v_max3_u32 v38, v54, v55, v38\n"
"v_max3_u32 v38, v56, v57, v38\n"
"v_max3_u32 v38, v58, v59, v38\n"
"v_max3_u32 v38, v60, v61, v38\n"
"v_max3_u32 v38, v62, v63, v38\n"
"v_max3_u32 v38, v64, v65, v38\n"
"v_max3_u32 v38, v66, v67, v38\n"
"v_max3_u32 v38, v68, v69, v38\n"
"v_max3_u32 v38, v70, v71, v38\n"
"v_cmp_eq_u32 vcc, -1, v38\n"
"s_cbranch_vccz .Lou_488\n"
"s_sleep 1\n"
"v_max_u32 v38, v54, v55\n"
"v_max3_u32 v38, v52, v53, v38\n"
"v_cmp_eq_u32 vcc, -1, v38\n"
"s_cbranch_vccz .Lsk_489\n"
"s_mov_b64 exec, vcc\n"
"buffer_load_dwordx4 v[52:55], v2, s[4:7], s50 offen offset:0 sc1\n"
"s_mov_b64 exec, -1\n"
".Lsk_489:\n"
"v_max_u32 v38, v58, v59\n"
"v_max3_u32 v38, v56, v57, v38\n"
"v_cmp_eq_u32 vcc, -1, v38\n"
"s_cbranch_vccz .Lsk_490\n"
"s_mov_b64 exec, vcc\n"
"buffer_load_dwordx4 v[56:59], v2, s[4:7], s50 offen offset:1024 sc1\n"
"s_mov_b64 exec, -1\n"
".Lsk_490:\n"
"v_max_u32 v38, v62, v63\n"
"v_max3_u32 v38, v60, v61, v38\n"
"v_cmp_eq_u32 vcc, -1, v38\n"
"s_cbranch_vccz .Lsk_491\n"
"s_mov_b64 exec, vcc\n"
"buffer_load_dwordx4 v[60:63], v2, s[4:7], s50 offen offset:2048 sc1\n"
"s_mov_b64 exec, -1\n"
".Lsk_491:\n"
"v_max_u32 v38, v66, v67\n"
"v_max3_u32 v38, v64, v65, v38\n"
"v_cmp_eq_u32 vcc, -1, v38\n"
"s_cbranch_vccz .Lsk_492\n"
"s_mov_b64 exec, vcc\n"
"buffer_load_dwordx4 v[64:67], v2, s[4:7], s50 offen offset:3072 sc1\n"
"s_mov_b64 exec, -1\n"
".Lsk_492:\n"
"v_max_u32 v38, v70, v71\n"
"v_max3_u32 v38, v68, v69, v38\n"
"v_cmp_eq_u32 vcc, -1, v38\n"
"s_cbranch_vccz .Lsk_493\n"
"s_mov_b64 exec, vcc\n"
"buffer_load_dwordx4 v[68:71], v2, s[4:7], s51 offen offset:0 sc1\n"
"s_mov_b64 exec, -1\n"
".Lsk_493:\n"
"s_branch .Ltp_487\n"
".Lou_488:\n"
"s_waitcnt vmcnt(0)\n"
"s_mov_b32 s17, 0x100000\n"
".Ltp_494:\n"
"s_sleep 2\n"
"ds_read_b32 v3, v5 offset:3584\n"
"s_sub_u32 s17, s17, 1\n"
"s_cbranch_scc1 .Lou_495\n"
"s_waitcnt lgkmcnt(0)\n"
"v_cmp_eq_u32 vcc, -1, v3\n"
"s_cbranch_vccnz .Ltp_494\n"
".Lou_495:\n"
"s_waitcnt lgkmcnt(0)\n"
"ds_write_b128 v50, v[52:55] offset:0\n"
"ds_write_b128 v50, v[56:59] offset:1024\n"
"ds_write_b128 v50, v[60:63] offset:2048\n"
"ds_write_b128 v50, v[64:67] offset:3072\n"
"ds_write_b128 v50, v[68:71] offset:4096\n"
"ds_write_b32 v4, v4 offset:704\n"
"s_mov_b32 s17, 0x100000\n"
".Ltp_496:\n"
"s_sleep 2\n"
"ds_read_b32 v3, v5 offset:3904\n"
"s_sub_u32 s17, s17, 1\n"
"s_cbranch_scc1 .Lou_497\n"
"s_waitcnt lgkmcnt(0)\n"
"v_cmp_eq_u32 vcc, -1, v3\n"
"s_cbranch_vccnz .Ltp_496\n"
".Lou_497:\n"
"s_waitcnt lgkmcnt(0)\n"
"s_mul_i32 s79, s3, 0x1400\n"
"s_add_u32 m0, s79, 0xf000\n"
"s_add_u32 s49, s50, 0x5000\n"
"buffer_load_dwordx4 v2, s[4:7], s49 offen offset:0 sc1 lds\n"
"buffer_load_dwordx4 v2, s[4:7], s49 offen offset:1024 sc1 lds\n"
"buffer_load_dwordx4 v2, s[4:7], s49 offen offset:2048 sc1 lds\n"
"buffer_load_dwordx4 v2, s[4:7], s49 offen offset:3072 sc1 lds\n"
"s_add_u32 m0, m0, 1024\n"
"s_add_u32 s49, s49, 1024\n"
"buffer_load_dwordx4 v2, s[4:7], s49 offen offset:3072 sc1 lds\n"
"s_setprio 2\n"
"v_add_u32 v14, v39, v14\n"
"v_add_u32 v15, v39, v15\n"
"s_mul_i32 s79, s28, 0xa00\n"
"s_add_u32 s79, s30, s79\n"
"s_and_b32 s78, s3, 1\n"
"s_mul_i32 s78, s78, 0x600\n"
"s_add_u32 s79, s79, s78\n"
"s_add_u32 s36, s79, 0xc28000\n"
"s_mul_i32 s79, s3, 0x1400\n"
"s_add_u32 s79, s30, s79\n"
"s_add_u32 s32, s79, 0xea8000\n"
"s_add_u32 s33, s32, 0x1000\n"
"s_lshl_b32 s79, s29, 1\n"
"s_lshr_b32 s78, s3, 1\n"
"s_xor_b32 s78, s78, 1\n"
"s_add_u32 s79, s79, s78\n"
"s_mul_i32 s79, s79, 0x5000\n"
"s_mul_i32 s78, s3, 0x1400\n"
"s_add_u32 s79, s79, s78\n"
"s_add_u32 s34, s79, 0xea8000\n"
"s_add_u32 s35, s34, 0x1000\n"
"s_lshl_b32 s79, s30, 1\n"
"s_mul_i32 s78, s28, 0x1400\n"
"s_add_u32 s79, s79, s78\n"
"s_and_b32 s78, s3, 1\n"
"s_mul_i32 s78, s78, 0xc00\n"
"s_add_u32 s79, s79, s78\n"
"s_add_u32 s37, s79, 0x1128000\n"
"s_lshl_b32 s79, s29, 1\n"
"s_lshr_b32 s78, s3, 1\n"
"s_add_u32 s79, s79, s78\n"
"s_lshl_b32 s79, s79, 11\n"
"s_lshl_b32 s78, s28, 8\n"
"s_add_u32 s79, s79, s78\n"
"s_and_b32 s78, s3, 1\n"
"s_lshl_b32 s78, s78, 7\n"
"s_add_u32 s79, s79, s78\n"
"s_add_u32 s38, s79, 0x1628000\n"
"s_mov_b32 s17, 0x100000\n"
".Ltp_498:\n"
"ds_read_b32 v3, v5 offset:256\n"
"s_sub_u32 s17, s17, 1\n"
"s_cbranch_scc1 .Lou_499\n"
"s_waitcnt lgkmcnt(0)\n"
"v_cmp_eq_u32 vcc, 0, v3\n"
"s_cbranch_vccnz .Ltp_498\n"
".Lou_499:\n"
"s_waitcnt lgkmcnt(0)\n"
"s_cmp_lt_i32 s18, 0\n"
"s_cbranch_scc1 .Lcw_500\n"
"s_mov_b32 s17, 0x100000\n"
".Ltp_501:\n"
"ds_read_b32 v3, v5 offset:688\n"
"s_sub_u32 s17, s17, 1\n"
"s_cbranch_scc1 .Lou_502\n"
"s_waitcnt lgkmcnt(0)\n"
"v_cmp_eq_u32 vcc, 0, v3\n"
"s_cbranch_vccnz .Ltp_501\n"
".Lou_502:\n"
"s_waitcnt lgkmcnt(0)\n"
".Lcw_500:\n"
"buffer_load_dwordx2 v[40:41], v14, s[4:7], s36 offen offset:0 sc1\n"
"buffer_load_dwordx2 v[42:43], v14, s[4:7], s36 offen offset:512 sc1\n"
"buffer_load_dwordx2 v[44:45], v15, s[4:7], s36 offen offset:1024 sc1\n"
"s_waitcnt vmcnt(3)\n"
"ds_write_b32 v4, v4 offset:320\n"
"s_mov_b32 s17, 0x100000\n"
".Ltp_506:\n"
"s_sleep 1\n"
"ds_read_b32 v3, v5 offset:304\n"
"s_sub_u32 s17, s17, 1\n"
"s_cbranch_scc1 .Lou_507\n"
"s_waitcnt lgkmcnt(0)\n"
"v_cmp_eq_u32 vcc, 0, v3\n"
"s_cbranch_vccnz .Ltp_506\n"
".Lou_507:\n"
"s_waitcnt lgkmcnt(0)\n"
"s_mul_i32 m0, s3, 0x1400\n"
"s_add_u32 s49, s50, 0xa000\n"
"buffer_load_dwordx4 v2, s[4:7], s49 offen offset:0 sc1 lds\n"
"buffer_load_dwordx4 v2, s[4:7], s49 offen offset:1024 sc1 lds\n"
"buffer_load_dwordx4 v2, s[4:7], s49 offen offset:2048 sc1 lds\n"
"buffer_load_dwordx4 v2, s[4:7], s49 offen offset:3072 sc1 lds\n"
"s_add_u32 m0, m0, 1024\n"
"s_add_u32 s49, s49, 1024\n"
"buffer_load_dwordx4 v2, s[4:7], s49 offen offset:3072 sc1 lds\n"
"s_waitcnt vmcnt(5)\n"
"s_mov_b32 s17, 32768\n"
"s_branch .Lmd_505\n"
".Ltp_503:\n"
"buffer_load_dwordx2 v[40:41], v14, s[4:7], s36 offen offset:0 sc1\n"
"buffer_load_dwordx2 v[42:43], v14, s[4:7], s36 offen offset:512 sc1\n"
"buffer_load_dwordx2 v[44:45], v15, s[4:7], s36 offen offset:1024 sc1\n"
"s_waitcnt vmcnt(0)\n"
".Lmd_505:\n"
"s_sub_u32 s17, s17, 1\n"
"s_cbranch_scc1 .Lou_504\n"
"v_max3_u32 v38, v40, v41, v42\n"
"v_max3_u32 v38, v43, v44, v38\n"
"v_max_u32 v38, v45, v38\n"
"v_cmp_eq_u32 vcc, -1, v38\n"
"s_cbranch_vccnz .Ltp_503\n"
".Lou_504:\n"
"v_lshlrev_b32 v48, 16, v40\n"
"v_and_b32 v49, 0xffff0000, v40\n"
"v_lshlrev_b32 v50, 16, v41\n"
"v_and_b32 v51, 0xffff0000, v41\n"
"v_lshlrev_b32 v52, 16, v42\n"
"v_and_b32 v53, 0xffff0000, v42\n"
"v_lshlrev_b32 v54, 16, v43\n"
"v_and_b32 v55, 0xffff0000, v43\n"
"v_lshlrev_b32 v56, 16, v44\n"
"v_and_b32 v57, 0xffff0000, v44\n"
"v_lshlrev_b32 v58, 16, v45\n"
"v_and_b32 v59, 0xffff0000, v45\n"
"v_mov_b32 v46, 0\n"
"v_mov_b32 v47, 0\n"
"s_mov_b32 s17, 0x100000\n"
".Ltp_508:\n"
"s_sleep 1\n"
"ds_read_b32 v3, v5 offset:368\n"
"s_sub_u32 s17, s17, 1\n"
"s_cbranch_scc1 .Lou_509\n"
"s_waitcnt lgkmcnt(0)\n"
"v_cmp_eq_u32 vcc, 0, v3\n"
"s_cbranch_vccnz .Ltp_508\n"
".Lou_509:\n"
"s_waitcnt lgkmcnt(0)\n"
"s_mul_i32 s79, s3, 0x1400\n"
"s_add_u32 m0, s79, 0x5000\n"
"s_add_u32 s49, s50, 0xf000\n"
"buffer_load_dwordx4 v2, s[4:7], s49 offen offset:0 sc1 lds\n"
"buffer_load_dwordx4 v2, s[4:7], s49 offen offset:1024 sc1 lds\n"
"buffer_load_dwordx4 v2, s[4:7], s49 offen offset:2048 sc1 lds\n"
"buffer_load_dwordx4 v2, s[4:7], s49 offen offset:3072 sc1 lds\n"
"s_add_u32 m0, m0, 1024\n"
"s_add_u32 s49, s49, 1024\n"
"buffer_load_dwordx4 v2, s[4:7], s49 offen offset:3072 sc1 lds\n"
"s_waitcnt vmcnt(5)\n"
"ds_write_b32 v4, v4 offset:400\n"
"s_waitcnt vmcnt(0)\n"
"ds_write_b32 v4, v4 offset:416\n"
"buffer_load_dwordx4 v[108:111], v2, s[4:7], s34 offen offset:0 sc1\n"
"s_nop 9\n"
"buffer_load_dwordx4 v[112:115], v2, s[4:7], s34 offen offset:1024 sc1\n"
"s_nop 9\n"
"buffer_load_dwordx4 v[116:119], v2, s[4:7], s34 offen offset:2048 sc1\n"
"s_nop 9\n"
"buffer_load_dwordx4 v[120:123], v2, s[4:7], s34 offen offset:3072 sc1\n"
"s_nop 9\n"
"buffer_load_dwordx4 v[124:127], v2, s[4:7], s35 offen offset:0 sc1\n"
"s_nop 9\n"
"buffer_load_dwordx4 v[88:91], v2, s[4:7], s32 offen offset:0 sc1\n"
"s_nop 9\n"
"buffer_load_dwordx4 v[92:95], v2, s[4:7], s32 offen offset:1024 sc1\n"
"s_nop 9\n"
"buffer_load_dwordx4 v[96:99], v2, s[4:7], s32 offen offset:2048 sc1\n"
"s_nop 9\n"
"buffer_load_dwordx4 v[100:103], v2, s[4:7], s32 offen offset:3072 sc1\n"
"s_nop 9\n"
"buffer_load_dwordx4 v[104:107], v2, s[4:7], s33 offen offset:0 sc1\n"
"s_waitcnt vmcnt(9)\n"
"v_max_u32 v38, v108, v109\n"
"v_max3_u32 v38, v110, v111, v38\n"
"v_cmp_eq_u32 vcc, -1, v38\n"
"s_cbranch_vccnz .Lsl_510\n"
".Lbk_511:\n"
"v_mfma_f32_16x16x32_bf16 v[68:71], v[156:159], v[108:111], 0\n"
"v_mfma_f32_16x16x32_bf16 v[72:75], v[160:163], v[108:111], 0\n"
"v_mfma_f32_16x16x32_bf16 v[76:79], v[164:167], v[108:111], 0\n"
"s_waitcnt vmcnt(8)\n"
"v_max_u32 v38, v112, v113\n"
"v_max3_u32 v38, v114, v115, v38\n"
"v_mfma_f32_16x16x32_bf16 v[80:83], v[168:171], v[108:111], 0\n"
"v_cmp_eq_u32 vcc, -1, v38\n"
"v_mfma_f32_16x16x32_bf16 v[84:87], v[172:175], v[108:111], 0\n"
"s_cbranch_vccnz .Lsl_512\n"
".Lbk_513:\n"
"v_mfma_f32_16x16x32_bf16 v[68:71], v[176:179], v[112:115], v[68:71]\n"
"v_mfma_f32_16x16x32_bf16 v[72:75], v[180:183], v[112:115], v[72:75]\n"
"v_mfma_f32_16x16x32_bf16 v[76:79], v[184:187], v[112:115], v[76:79]\n"
"s_waitcnt vmcnt(7)\n"
"v_max_u32 v38, v116, v117\n"
"v_max3_u32 v38, v118, v119, v38\n"
"v_mfma_f32_16x16x32_bf16 v[80:83], v[188:191], v[112:115], v[80:83]\n"
"v_cmp_eq_u32 vcc, -1, v38\n"
"v_mfma_f32_16x16x32_bf16 v[84:87], v[192:195], v[112:115], v[84:87]\n"
"s_cbranch_vccnz .Lsl_514\n"
".Lbk_515:\n"
"v_mfma_f32_16x16x32_bf16 v[68:71], v[196:199], v[116:119], v[68:71]\n"
"v_mfma_f32_16x16x32_bf16 v[72:75], v[200:203], v[116:119], v[72:75]\n"
"v_mfma_f32_16x16x32_bf16 v[76:79], v[204:207], v[116:119], v[76:79]\n"
"s_waitcnt vmcnt(6)\n"
"v_max_u32 v38, v120, v121\n"
"v_max3_u32 v38, v122, v123, v38\n"
"v_mfma_f32_16x16x32_bf16 v[80:83], v[208:211], v[116:119], v[80:83]\n"
"v_cmp_eq_u32 vcc, -1, v38\n"
"v_mfma_f32_16x16x32_bf16 v[84:87], v[212:215], v[116:119], v[84:87]\n"
"s_cbranch_vccnz .Lsl_516\n"
".Lbk_517:\n"
"v_mfma_f32_16x16x32_bf16 v[68:71], v[216:219], v[120:123], v[68:71]\n"
"v_mfma_f32_16x16x32_bf16 v[72:75], v[220:223], v[120:123], v[72:75]\n"
"v_mfma_f32_16x16x32_bf16 v[76:79], v[224:227], v[120:123], v[76:79]\n"
"s_waitcnt vmcnt(5)\n"
"v_max_u32 v38, v124, v125\n"
"v_max3_u32 v38, v126, v127, v38\n"
"v_mfma_f32_16x16x32_bf16 v[80:83], v[228:231], v[120:123], v[80:83]\n"
"v_cmp_eq_u32 vcc, -1, v38\n"
"v_mfma_f32_16x16x32_bf16 v[84:87], v[232:235], v[120:123], v[84:87]\n"
"s_cbranch_vccnz .Lsl_518\n"
".Lbk_519:\n"
"v_mfma_f32_16x16x32_bf16 v[68:71], v[236:239], v[124:127], v[68:71]\n"
"v_mfma_f32_16x16x32_bf16 v[72:75], v[240:243], v[124:127], v[72:75]\n"
"v_mfma_f32_16x16x32_bf16 v[76:79], v[244:247], v[124:127], v[76:79]\n"
"s_waitcnt vmcnt(4)\n"
"v_max_u32 v38, v88, v89\n"
"v_max3_u32 v38, v90, v91, v38\n"
"v_mfma_f32_16x16x32_bf16 v[80:83], v[248:251], v[124:127], v[80:83]\n"
"v_cmp_eq_u32 vcc, -1, v38\n"
"v_mfma_f32_16x16x32_bf16 v[84:87], v[252:255], v[124:127], v[84:87]\n"
"ds_write_b128 v8, v[68:71] offset:0\n"
"s_cbranch_vccnz .Lsl_520\n"
".Lbk_521:\n"
"v_mfma_f32_16x16x32_bf16 v[56:59], v[164:167], v[88:91], v[56:59]\n"
"s_waitcnt vmcnt(3)\n"
"v_max_u32 v38, v92, v93\n"
"v_max3_u32 v38, v94, v95, v38\n"
"ds_write_b128 v8, v[72:75] offset:1024\n"
"v_mfma_f32_16x16x32_bf16 v[60:63], v[168:171], v[88:91], 0\n"
"v_cmp_eq_u32 vcc, -1, v38\n"
"ds_write_b128 v10, v[76:79] offset:0\n"
"v_mfma_f32_16x16x32_bf16 v[64:67], v[172:175], v[88:91], 0\n"
"v_mov_b32 v117, s16\n"
"s_cbranch_vccnz .Lsl_522\n"
".Lbk_523:\n"
"v_mfma_f32_16x16x32_bf16 v[56:59], v[184:187], v[92:95], v[56:59]\n"
"s_waitcnt vmcnt(2)\n"
"v_max_u32 v38, v96, v97\n"
"v_max3_u32 v38, v98, v99, v38\n"
"v_mov_b32 v119, s16\n"
"v_mfma_f32_16x16x32_bf16 v[60:63], v[188:191], v[92:95], v[60:63]\n"
"v_cmp_eq_u32 vcc, -1, v38\n"
"ds_write_b128 v9, v[80:83] offset:0\n"
"v_mfma_f32_16x16x32_bf16 v[64:67], v[192:195], v[92:95], v[64:67]\n"
"ds_write_b128 v9, v[84:87] offset:1024\n"
"s_cbranch_vccnz .Lsl_524\n"
".Lbk_525:\n"
"v_mfma_f32_16x16x32_bf16 v[56:59], v[204:207], v[96:99], v[56:59]\n"
"s_waitcnt vmcnt(1)\n"
"v_max_u32 v38, v100, v101\n"
"v_max3_u32 v38, v102, v103, v38\n"
"ds_write_b32 v4, v4 offset:720\n"
"v_mfma_f32_16x16x32_bf16 v[60:63], v[208:211], v[96:99], v[60:63]\n"
"v_cmp_eq_u32 vcc, -1, v38\n"
"v_mov_b32 v121, s16\n"
"v_mov_b32 v123, s16\n"
"v_mfma_f32_16x16x32_bf16 v[64:67], v[212:215], v[96:99], v[64:67]\n"
"v_mov_b32 v125, s16\n"
"s_cbranch_vccnz .Lsl_526\n"
".Lbk_527:\n"
"v_mfma_f32_16x16x32_bf16 v[56:59], v[224:227], v[100:103], v[56:59]\n"
"s_waitcnt vmcnt(0)\n"
"v_max_u32 v38, v104, v105\n"
"v_max3_u32 v38, v106, v107, v38\n"
"v_mov_b32 v127, s16\n"
"v_mfma_f32_16x16x32_bf16 v[60:63], v[228:231], v[100:103], v[60:63]\n"
"v_cmp_eq_u32 vcc, -1, v38\n"
"ds_read_b32 v3, v5 offset:720\n"
"v_mfma_f32_16x16x32_bf16 v[64:67], v[232:235], v[100:103], v[64:67]\n"
"s_cbranch_vccnz .Lsl_528\n"
".Lbk_529:\n"
"v_mfma_f32_16x16x32_bf16 v[56:59], v[244:247], v[104:107], v[56:59]\n"
"ds_read_b128 v[76:79], v12 offset:2048\n"
"ds_read_b128 v[108:111], v13 offset:2048\n"
"v_mfma_f32_16x16x32_bf16 v[60:63], v[248:251], v[104:107], v[60:63]\n"
"ds_read_b128 v[68:71], v12 offset:0\n"
"ds_read_b128 v[80:83], v13 offset:0\n"
"v_mfma_f32_16x16x32_bf16 v[64:67], v[252:255], v[104:107], v[64:67]\n"
"ds_read_b128 v[72:75], v12 offset:1024\n"
"ds_read_b128 v[84:87], v13 offset:1024\n"
"v_mfma_f32_16x16x32_bf16 v[48:51], v[156:159], v[88:91], v[48:51]\n"
"ds_write_b128 v7, v[56:59] offset:0\n"
"v_mfma_f32_16x16x32_bf16 v[52:55], v[160:163], v[88:91], v[52:55]\n"
"ds_write_b128 v6, v[60:63] offset:0\n"
"v_mfma_f32_16x16x32_bf16 v[48:51], v[176:179], v[92:95], v[48:51]\n"
"ds_write_b128 v6, v[64:67] offset:1024\n"
"v_mfma_f32_16x16x32_bf16 v[52:55], v[180:183], v[92:95], v[52:55]\n"
"ds_write_b32 v4, v4 offset:736\n"
"v_mfma_f32_16x16x32_bf16 v[48:51], v[196:199], v[96:99], v[48:51]\n"
"s_mov_b32 s17, 0x100000\n"
"s_waitcnt lgkmcnt(10)\n"
"v_cmp_eq_u32 vcc, 0, v3\n"
"s_cbranch_vccz .Lok_531\n"
".Ltp_530:\n"
"ds_read_b32 v3, v5 offset:720\n"
"ds_read_b128 v[76:79], v12 offset:2048\n"
"ds_read_b128 v[108:111], v13 offset:2048\n"
"ds_read_b128 v[68:71], v12 offset:0\n"
"ds_read_b128 v[80:83], v13 offset:0\n"
"ds_read_b128 v[72:75], v12 offset:1024\n"
"ds_read_b128 v[84:87], v13 offset:1024\n"
"s_sub_u32 s17, s17, 1\n"
"s_cbranch_scc1 .Lok_531\n"
"s_waitcnt lgkmcnt(6)\n"
"v_cmp_eq_u32 vcc, 0, v3\n"
"s_cbranch_vccnz .Ltp_530\n"
"s_waitcnt lgkmcnt(0)\n"
".Lok_531:\n"
"v_mfma_f32_16x16x32_bf16 v[52:55], v[200:203], v[96:99], v[52:55]\n"
"s_waitcnt lgkmcnt(8)\n"
"v_add_f32 v76, v76, v108\n"
"v_add_f32 v77, v77, v109\n"
"v_add_f32 v78, v78, v110\n"
"v_mfma_f32_16x16x32_bf16 v[48:51], v[216:219], v[100:103], v[48:51]\n"
"v_add_f32 v79, v79, v111\n"
"s_waitcnt lgkmcnt(6)\n"
"v_add_f32 v68, v68, v80\n"
"v_add_f32 v69, v69, v81\n"
"v_mfma_f32_16x16x32_bf16 v[52:55], v[220:223], v[100:103], v[52:55]\n"
"v_add_f32 v70, v70, v82\n"
"v_add_f32 v71, v71, v83\n"
"s_waitcnt lgkmcnt(4)\n"
"v_add_f32 v72, v72, v84\n"
"v_mfma_f32_16x16x32_bf16 v[48:51], v[236:239], v[104:107], v[48:51]\n"
"v_add_f32 v73, v73, v85\n"
"v_add_f32 v74, v74, v86\n"
"v_add_f32 v75, v75, v87\n"
"v_mfma_f32_16x16x32_bf16 v[52:55], v[240:243], v[104:107], v[52:55]\n"
"ds_read_b32 v3, v5 offset:736\n"
"ds_read_b128 v[112:115], v11 offset:2048\n"
"ds_read_b128 v[60:63], v11 offset:0\n"
"ds_read_b128 v[64:67], v11 offset:1024\n"
"v_add_f32 v56, v56, v76\n"
"v_add_f32 v57, v57, v77\n"
"v_add_f32 v58, v58, v78\n"
"v_add_f32 v59, v59, v79\n"
"v_add_f32 v48, v48, v68\n"
"v_add_f32 v49, v49, v69\n"
"v_add_f32 v50, v50, v70\n"
"v_add_f32 v51, v51, v71\n"
"v_add_f32 v52, v52, v72\n"
"v_add_f32 v53, v53, v73\n"
"v_add_f32 v54, v54, v74\n"
"v_add_f32 v55, v55, v75\n"
"s_mov_b32 s17, 0x100000\n"
"s_waitcnt lgkmcnt(3)\n"
"v_cmp_eq_u32 vcc, 0, v3\n"
"s_cbranch_vccz .Lok_533\n"
".Ltp_532:\n"
"ds_read_b32 v3, v5 offset:736\n"
"ds_read_b128 v[112:115], v11 offset:2048\n"
"ds_read_b128 v[60:63], v11 offset:0\n"
"ds_read_b128 v[64:67], v11 offset:1024\n"
"s_sub_u32 s17, s17, 1\n"
"s_cbranch_scc1 .Lok_533\n"
"s_waitcnt lgkmcnt(3)\n"
"v_cmp_eq_u32 vcc, 0, v3\n"
"s_cbranch_vccnz .Ltp_532\n"
"s_waitcnt lgkmcnt(0)\n"
".Lok_533:\n"
"s_waitcnt lgkmcnt(2)\n"
"v_add_f32 v56, v56, v112\n"
"v_add_f32 v57, v57, v113\n"
"v_add_f32 v58, v58, v114\n"
"v_add_f32 v59, v59, v115\n"
"v_cvt_pk_bf16_f32 v44, v56, v57\n"
"v_cvt_pk_bf16_f32 v45, v58, v59\n"
"v_and_b32 v44, v19, v44\n"
"v_and_b32 v45, v19, v45\n"
"s_waitcnt lgkmcnt(1)\n"
"v_add_f32 v48, v48, v60\n"
"v_add_f32 v49, v49, v61\n"
"v_add_f32 v50, v50, v62\n"
"v_add_f32 v51, v51, v63\n"
"v_cvt_pk_bf16_f32 v40, v48, v49\n"
"v_cvt_pk_bf16_f32 v41, v50, v51\n"
"v_mfma_f32_16x16x32_bf16 v[88:91], v[44:47], v[44:47], 0\n"
"v_mfma_f32_16x16x32_bf16 v[92:95], v[20:23], v[44:47], 0\n"
"s_waitcnt lgkmcnt(0)\n"
"v_add_f32 v52, v52, v64\n"
"v_add_f32 v53, v53, v65\n"
"v_add_f32 v54, v54, v66\n"
"v_add_f32 v55, v55, v67\n"
"v_cvt_pk_bf16_f32 v42, v52, v53\n"
"v_cvt_pk_bf16_f32 v43, v54, v55\n"
"v_cvt_pk_bf16_f32 v116, v48, v49\n"
"v_cvt_pk_bf16_f32 v118, v50, v51\n"
"v_mfma_f32_16x16x32_bf16 v[88:91], v[40:43], v[40:43], v[88:91]\n"
"v_mfma_f32_16x16x32_bf16 v[92:95], v[20:23], v[40:43], v[92:95]\n"
"v_cvt_pk_bf16_f32 v120, v52, v53\n"
"v_cvt_pk_bf16_f32 v122, v54, v55\n"
"v_cvt_pk_bf16_f32 v124, v56, v57\n"
"v_cvt_pk_bf16_f32 v126, v58, v59\n"
"s_nop 0\n"
"v_cndmask_b32 v101, v88, v90, s[44:45]\n"
"v_cndmask_b32 v88, v89, v91, s[44:45]\n"
"s_nop 0\n"
"v_min_u32 v100, -2, v92\n"
"v_min_u32 v88, -2, v88\n"
"v_min_u32 v101, -2, v101\n"
"v_mov_b32_dpp v102, v100 quad_perm:[1,0,3,2] row_mask:0xf bank_mask:0xf\n"
"v_mov_b32_dpp v103, v88 quad_perm:[1,0,3,2] row_mask:0xf bank_mask:0xf\n"
"buffer_store_dwordx4 v[100:103], v16, s[4:7], s38 offen sc1\n"
"buffer_store_dwordx4 v[116:119], v17, s[4:7], s37 offen offset:0 sc1\n"
"buffer_store_dwordx4 v[120:123], v17, s[4:7], s37 offen offset:1024 sc1\n"
"buffer_store_dwordx4 v[124:127], v18, s[4:7], s37 offen offset:2048 sc1\n"
"ds_write_b32 v4, v4 offset:496\n"
"s_xor_b32 s79, s3, 1\n"
"s_lshl_b32 s79, s79, 8\n"
"s_add_u32 s32, s19, s79\n"
"s_cmp_ge_u32 s32, 64\n"
"s_cbranch_scc1 .Lnr_534\n"
"s_lshl_b32 s79, s19, 11\n"
"s_add_u32 s32, s79, 0x1628000\n"
"s_lshl_b32 s79, s19, 7\n"
"s_add_u32 s33, s79, 0x1668000\n"
"v_lshrrev_b32 v99, 3, v0\n"
"v_and_b32 v98, 7, v0\n"
"v_lshlrev_b32 v98, 4, v98\n"
"v_lshl_add_u32 v40, v99, 14, v98\n"
"v_mov_b32 v41, 0x3727c5ac\n"
"s_mov_b32 s89, 8\n"
".Ldl_535:\n"
"s_sub_u32 s89, s89, 1\n"
"s_cbranch_scc1 .Lde_536\n"
"s_sleep 1\n"
"s_branch .Ldl_535\n"
".Lde_536:\n"
"buffer_load_dwordx4 v[48:51], v2, s[4:7], s32 offen offset:0 sc1\n"
"buffer_load_dwordx4 v[52:55], v2, s[4:7], s32 offen offset:1024 sc1\n"
"s_mov_b32 s17, 32768\n"
".Ltp_537:\n"
"s_waitcnt vmcnt(0)\n"
"s_sub_u32 s17, s17, 1\n"
"s_cbranch_scc1 .Lou_538\n"
"v_max_u32 v38, v48, v49\n"
"v_max3_u32 v38, v50, v51, v38\n"
"v_max3_u32 v38, v52, v53, v38\n"
"v_max3_u32 v38, v54, v55, v38\n"
"v_cmp_eq_u32 vcc, -1, v38\n"
"s_cbranch_vccz .Lou_538\n"
"v_max_u32 v38, v50, v51\n"
"v_max3_u32 v38, v48, v49, v38\n"
"v_cmp_eq_u32 vcc, -1, v38\n"
"s_cbranch_vccz .Lsk_539\n"
"s_mov_b64 exec, vcc\n"
"buffer_load_dwordx4 v[48:51], v2, s[4:7], s32 offen offset:0 sc1\n"
"s_mov_b64 exec, -1\n"
".Lsk_539:\n"
"v_max_u32 v38, v54, v55\n"
"v_max3_u32 v38, v52, v53, v38\n"
"v_cmp_eq_u32 vcc, -1, v38\n"
"s_cbranch_vccz .Lsk_540\n"
"s_mov_b64 exec, vcc\n"
"buffer_load_dwordx4 v[52:55], v2, s[4:7], s32 offen offset:1024 sc1\n"
"s_mov_b64 exec, -1\n"
".Lsk_540:\n"
"s_branch .Ltp_537\n"
".Lou_538:\n"
"s_waitcnt vmcnt(0)\n"
"v_add_f32 v48, v48, v52\n"
"v_add_f32 v49, v49, v53\n"
"v_add_f32 v50, v50, v54\n"
"v_add_f32 v51, v51, v55\n"
"v_add_f32_dpp v52, v48, v48 row_ror:8 row_mask:0xf bank_mask:0xf\n"
"v_add_f32_dpp v53, v49, v49 row_ror:8 row_mask:0xf bank_mask:0xf\n"
"v_add_f32_dpp v54, v50, v50 row_ror:8 row_mask:0xf bank_mask:0xf\n"
"v_add_f32_dpp v55, v51, v51 row_ror:8 row_mask:0xf bank_mask:0xf\n"
"v_mov_b32 v48, v52\n"
"v_mov_b32 v49, v53\n"
"v_mov_b32 v50, v54\n"
"v_mov_b32 v51, v55\n"
"v_permlane16_swap_b32 v48, v52\n"
"v_permlane16_swap_b32 v49, v53\n"
"v_permlane16_swap_b32 v50, v54\n"
"v_permlane16_swap_b32 v51, v55\n"
"v_add_f32 v52, v48, v52\n"
"v_add_f32 v53, v49, v53\n"
"v_add_f32 v54, v50, v54\n"
"v_add_f32 v55, v51, v55\n"
"v_mov_b32 v48, v52\n"
"v_mov_b32 v49, v53\n"
"v_mov_b32 v50, v54\n"
"v_mov_b32 v51, v55\n"
"v_permlane32_swap_b32 v48, v52\n"
"v_permlane32_swap_b32 v49, v53\n"
"v_permlane32_swap_b32 v50, v54\n"
"v_permlane32_swap_b32 v51, v55\n"
"v_add_f32 v52, v48, v52\n"
"v_add_f32 v53, v49, v53\n"
"v_add_f32 v54, v50, v54\n"
"v_add_f32 v55, v51, v55\n"
"v_mul_f32 v48, 0x3acccccd, v52\n"
"v_mul_f32 v50, 0x3acccccd, v54\n"
"v_fma_f32 v53, -v52, v48, v53\n"
"v_fma_f32 v55, -v54, v50, v55\n"
"v_fmamk_f32 v53, v53, 0x3acccccd, v41\n"
"v_fmamk_f32 v55, v55, 0x3acccccd, v41\n"
"v_max_f32 v53, v53, v41\n"
"v_max_f32 v55, v55, v41\n"
"v_rsq_f32 v49, v53\n"
"v_rsq_f32 v51, v55\n"
"v_mul_f32 v48, -v48, v49\n"
"v_mul_f32 v50, -v50, v51\n"
"v_min_u32 v49, -2, v49\n"
"v_min_u32 v51, -2, v51\n"
"v_min_u32 v48, -2, v48\n"
"v_min_u32 v50, -2, v50\n"
"buffer_store_dwordx4 v[48:51], v40, s[4:7], s33 offen sc1\n"
"s_nop 3\n"
".Lnr_534:\n"
"s_mul_i32 s79, s28, 0xa00\n"
"s_add_u32 s79, s30, s79\n"
"s_and_b32 s78, s3, 1\n"
"s_mul_i32 s78, s78, 0x600\n"
"s_add_u32 s79, s79, s78\n"
"s_add_u32 s36, s79, 0xd68000\n"
"s_mul_i32 s79, s3, 0x1400\n"
"s_add_u32 s79, s30, s79\n"
"s_add_u32 s32, s79, 0xfe8000\n"
"s_add_u32 s33, s32, 0x1000\n"
"s_lshl_b32 s79, s29, 1\n"
"s_lshr_b32 s78, s3, 1\n"
"s_xor_b32 s78, s78, 1\n"
"s_add_u32 s79, s79, s78\n"
"s_mul_i32 s79, s79, 0x5000\n"
"s_mul_i32 s78, s3, 0x1400\n"
"s_add_u32 s79, s79, s78\n"
"s_add_u32 s34, s79, 0xfe8000\n"
"s_add_u32 s35, s34, 0x1000\n"
"s_lshl_b32 s79, s30, 1\n"
"s_mul_i32 s78, s28, 0x1400\n"
"s_add_u32 s79, s79, s78\n"
"s_and_b32 s78, s3, 1\n"
"s_mul_i32 s78, s78, 0xc00\n"
"s_add_u32 s79, s79, s78\n"
"s_add_u32 s37, s79, 0x13a8000\n"
"s_lshl_b32 s79, s29, 1\n"
"s_lshr_b32 s78, s3, 1\n"
"s_add_u32 s79, s79, s78\n"
"s_lshl_b32 s79, s79, 11\n"
"s_lshl_b32 s78, s28, 8\n"
"s_add_u32 s79, s79, s78\n"
"s_and_b32 s78, s3, 1\n"
"s_lshl_b32 s78, s78, 7\n"
"s_add_u32 s79, s79, s78\n"
"s_add_u32 s38, s79, 0x1648000\n"
"s_mov_b32 s17, 0x100000\n"
".Ltp_541:\n"
"ds_read_b32 v3, v5 offset:496\n"
"s_sub_u32 s17, s17, 1\n"
"s_cbranch_scc1 .Lou_542\n"
"s_waitcnt lgkmcnt(0)\n"
"v_cmp_eq_u32 vcc, 0, v3\n"
"s_cbranch_vccnz .Ltp_541\n"
".Lou_542:\n"
"s_waitcnt lgkmcnt(0)\n"
"s_mov_b32 s17, 0x100000\n"
".Ltp_546:\n"
"s_sleep 2\n"
"ds_read_b32 v3, v5 offset:4800\n"
"s_sub_u32 s17, s17, 1\n"
"s_cbranch_scc1 .Lou_547\n"
"s_waitcnt lgkmcnt(0)\n"
"v_cmp_eq_u32 vcc, -1, v3\n"
"s_cbranch_vccnz .Ltp_546\n"
".Lou_547:\n"
"s_waitcnt lgkmcnt(0)\n"
"s_mov_b32 s17, 32768\n"
".Ltp_543:\n"
"buffer_load_dwordx2 v[40:41], v14, s[4:7], s36 offen offset:0 sc1\n"
"buffer_load_dwordx2 v[42:43], v14, s[4:7], s36 offen offset:512 sc1\n"
"buffer_load_dwordx2 v[44:45], v15, s[4:7], s36 offen offset:1024 sc1\n"
"s_waitcnt vmcnt(0)\n"
".Lmd_545:\n"
"s_sub_u32 s17, s17, 1\n"
"s_cbranch_scc1 .Lou_544\n"
"v_max3_u32 v38, v40, v41, v42\n"
"v_max3_u32 v38, v43, v44, v38\n"
"v_max_u32 v38, v45, v38\n"
"v_cmp_eq_u32 vcc, -1, v38\n"
"s_cbranch_vccnz .Ltp_543\n"
".Lou_544:\n"
"v_lshlrev_b32 v48, 16, v40\n"
"v_and_b32 v49, 0xffff0000, v40\n"
"v_lshlrev_b32 v50, 16, v41\n"
"v_and_b32 v51, 0xffff0000, v41\n"
"v_lshlrev_b32 v52, 16, v42\n"
"v_and_b32 v53, 0xffff0000, v42\n"
"v_lshlrev_b32 v54, 16, v43\n"
"v_and_b32 v55, 0xffff0000, v43\n"
"v_lshlrev_b32 v56, 16, v44\n"
"v_and_b32 v57, 0xffff0000, v44\n"
"v_lshlrev_b32 v58, 16, v45\n"
"v_and_b32 v59, 0xffff0000, v45\n"
"v_mov_b32 v46, 0\n"
"v_mov_b32 v47, 0\n"
"s_mov_b32 s17, 0x100000\n"
".Ltp_548:\n"
"s_sleep 1\n"
"ds_read_b32 v3, v5 offset:480\n"
"s_sub_u32 s17, s17, 1\n"
"s_cbranch_scc1 .Lou_549\n"
"s_waitcnt lgkmcnt(0)\n"
"v_cmp_eq_u32 vcc, 0, v3\n"
"s_cbranch_vccnz .Ltp_548\n"
".Lou_549:\n"
"s_waitcnt lgkmcnt(0)\n"
"s_cmp_eq_u32 s17, 0xfffff\n"
"s_cbranch_scc1 .Lsk_550\n"
"s_mov_b32 s89, 19\n"
".Ldl_552:\n"
"s_sub_u32 s89, s89, 1\n"
"s_cbranch_scc1 .Lde_553\n"
"s_sleep 1\n"
"s_branch .Ldl_552\n"
".Lde_553:\n"
"s_branch .Ljn_551\n"
".Lsk_550:\n"
"s_mov_b32 s89, 12\n"
".Ldl_554:\n"
"s_sub_u32 s89, s89, 1\n"
"s_cbranch_scc1 .Lde_555\n"
"s_sleep 1\n"
"s_branch .Ldl_554\n"
".Lde_555:\n"
".Ljn_551:\n"
"buffer_load_dwordx4 v[108:111], v2, s[4:7], s34 offen offset:0 sc1\n"
"s_nop 9\n"
"buffer_load_dwordx4 v[112:115], v2, s[4:7], s34 offen offset:1024 sc1\n"
"s_nop 9\n"
"buffer_load_dwordx4 v[116:119], v2, s[4:7], s34 offen offset:2048 sc1\n"
"s_nop 9\n"
"buffer_load_dwordx4 v[120:123], v2, s[4:7], s34 offen offset:3072 sc1\n"
"s_nop 9\n"
"buffer_load_dwordx4 v[124:127], v2, s[4:7], s35 offen offset:0 sc1\n"
"s_nop 9\n"
"buffer_load_dwordx4 v[88:91], v2, s[4:7], s32 offen offset:0 sc1\n"
"s_nop 9\n"
"buffer_load_dwordx4 v[92:95], v2, s[4:7], s32 offen offset:1024 sc1\n"
"s_nop 9\n"
"buffer_load_dwordx4 v[96:99], v2, s[4:7], s32 offen offset:2048 sc1\n"
"s_nop 9\n"
"buffer_load_dwordx4 v[100:103], v2, s[4:7], s32 offen offset:3072 sc1\n"
"s_nop 9\n"
"buffer_load_dwordx4 v[104:107], v2, s[4:7], s33 offen offset:0 sc1\n"
"s_waitcnt vmcnt(9)\n"
"v_max_u32 v38, v108, v109\n"
"v_max3_u32 v38, v110, v111, v38\n"
"v_cmp_eq_u32 vcc, -1, v38\n"
"s_cbranch_vccnz .Lsl_556\n"
".Lbk_557:\n"
"v_mfma_f32_16x16x32_bf16 v[68:71], v[156:159], v[108:111], 0\n"
"v_mfma_f32_16x16x32_bf16 v[72:75], v[160:163], v[108:111], 0\n"
"v_mfma_f32_16x16x32_bf16 v[76:79], v[164:167], v[108:111], 0\n"
"s_waitcnt vmcnt(8)\n"
"v_max_u32 v38, v112, v113\n"
"v_max3_u32 v38, v114, v115, v38\n"
"v_mfma_f32_16x16x32_bf16 v[80:83], v[168:171], v[108:111], 0\n"
"v_cmp_eq_u32 vcc, -1, v38\n"
"v_mfma_f32_16x16x32_bf16 v[84:87], v[172:175], v[108:111], 0\n"
"s_cbranch_vccnz .Lsl_558\n"
".Lbk_559:\n"
"v_mfma_f32_16x16x32_bf16 v[68:71], v[176:179], v[112:115], v[68:71]\n"
"v_mfma_f32_16x16x32_bf16 v[72:75], v[180:183], v[112:115], v[72:75]\n"
"v_mfma_f32_16x16x32_bf16 v[76:79], v[184:187], v[112:115], v[76:79]\n"
"s_waitcnt vmcnt(7)\n"
"v_max_u32 v38, v116, v117\n"
"v_max3_u32 v38, v118, v119, v38\n"
"v_mfma_f32_16x16x32_bf16 v[80:83], v[188:191], v[112:115], v[80:83]\n"
"v_cmp_eq_u32 vcc, -1, v38\n"
"v_mfma_f32_16x16x32_bf16 v[84:87], v[192:195], v[112:115], v[84:87]\n"
"s_cbranch_vccnz .Lsl_560\n"
".Lbk_561:\n"
"v_mfma_f32_16x16x32_bf16 v[68:71], v[196:199], v[116:119], v[68:71]\n"
"v_mfma_f32_16x16x32_bf16 v[72:75], v[200:203], v[116:119], v[72:75]\n"
"v_mfma_f32_16x16x32_bf16 v[76:79], v[204:207], v[116:119], v[76:79]\n"
"s_waitcnt vmcnt(6)\n"
"v_max_u32 v38, v120, v121\n"
"v_max3_u32 v38, v122, v123, v38\n"
"v_mfma_f32_16x16x32_bf16 v[80:83], v[208:211], v[116:119], v[80:83]\n"
"v_cmp_eq_u32 vcc, -1, v38\n"
"v_mfma_f32_16x16x32_bf16 v[84:87], v[212:215], v[116:119], v[84:87]\n"
"s_cbranch_vccnz .Lsl_562\n"
".Lbk_563:\n"
"v_mfma_f32_16x16x32_bf16 v[68:71], v[216:219], v[120:123], v[68:71]\n"
"v_mfma_f32_16x16x32_bf16 v[72:75], v[220:223], v[120:123], v[72:75]\n"
"v_mfma_f32_16x16x32_bf16 v[76:79], v[224:227], v[120:123], v[76:79]\n"
"s_waitcnt vmcnt(5)\n"
"v_max_u32 v38, v124, v125\n"
"v_max3_u32 v38, v126, v127, v38\n"
"v_mfma_f32_16x16x32_bf16 v[80:83], v[228:231], v[120:123], v[80:83]\n"
"v_cmp_eq_u32 vcc, -1, v38\n"
"v_mfma_f32_16x16x32_bf16 v[84:87], v[232:235], v[120:123], v[84:87]\n"
"s_cbranch_vccnz .Lsl_564\n"
".Lbk_565:\n"
"v_mfma_f32_16x16x32_bf16 v[68:71], v[236:239], v[124:127], v[68:71]\n"
"v_mfma_f32_16x16x32_bf16 v[72:75], v[240:243], v[124:127], v[72:75]\n"
"v_mfma_f32_16x16x32_bf16 v[76:79], v[244:247], v[124:127], v[76:79]\n"
"s_waitcnt vmcnt(4)\n"
"v_max_u32 v38, v88, v89\n"
"v_max3_u32 v38, v90, v91, v38\n"
"v_mfma_f32_16x16x32_bf16 v[80:83], v[248:251], v[124:127], v[80:83]\n"
"v_cmp_eq_u32 vcc, -1, v38\n"
"v_mfma_f32_16x16x32_bf16 v[84:87], v[252:255], v[124:127], v[84:87]\n"
"ds_write_b128 v8, v[68:71] offset:0\n"
"s_cbranch_vccnz .Lsl_566\n"
".Lbk_567:\n"
"v_mfma_f32_16x16x32_bf16 v[56:59], v[164:167], v[88:91], v[56:59]\n"
"s_waitcnt vmcnt(3)\n"
"v_max_u32 v38, v92, v93\n"
"v_max3_u32 v38, v94, v95, v38\n"
"ds_write_b128 v8, v[72:75] offset:1024\n"
"v_mfma_f32_16x16x32_bf16 v[60:63], v[168:171], v[88:91], 0\n"
"v_cmp_eq_u32 vcc, -1, v38\n"
"ds_write_b128 v10, v[76:79] offset:0\n"
"v_mfma_f32_16x16x32_bf16 v[64:67], v[172:175], v[88:91], 0\n"
"v_mov_b32 v117, s16\n"
"s_cbranch_vccnz .Lsl_568\n"
".Lbk_569:\n"
"v_mfma_f32_16x16x32_bf16 v[56:59], v[184:187], v[92:95], v[56:59]\n"
"s_waitcnt vmcnt(2)\n"
"v_max_u32 v38, v96, v97\n"
"v_max3_u32 v38, v98, v99, v38\n"
"v_mov_b32 v119, s16\n"
"v_mfma_f32_16x16x32_bf16 v[60:63], v[188:191], v[92:95], v[60:63]\n"
"v_cmp_eq_u32 vcc, -1, v38\n"
"ds_write_b128 v9, v[80:83] offset:0\n"
"v_mfma_f32_16x16x32_bf16 v[64:67], v[192:195], v[92:95], v[64:67]\n"
"ds_write_b128 v9, v[84:87] offset:1024\n"
"s_cbranch_vccnz .Lsl_570\n"
".Lbk_571:\n"
"v_mfma_f32_16x16x32_bf16 v[56:59], v[204:207], v[96:99], v[56:59]\n"
"s_waitcnt vmcnt(1)\n"
"v_max_u32 v38, v100, v101\n"
"v_max3_u32 v38, v102, v103, v38\n"
"ds_write_b32 v4, v4 offset:752\n"
"v_mfma_f32_16x16x32_bf16 v[60:63], v[208:211], v[96:99], v[60:63]\n"
"v_cmp_eq_u32 vcc, -1, v38\n"
"v_mov_b32 v121, s16\n"
"v_mov_b32 v123, s16\n"
"v_mfma_f32_16x16x32_bf16 v[64:67], v[212:215], v[96:99], v[64:67]\n"
"v_mov_b32 v125, s16\n"
"s_cbranch_vccnz .Lsl_572\n"
".Lbk_573:\n"
"v_mfma_f32_16x16x32_bf16 v[56:59], v[224:227], v[100:103], v[56:59]\n"
"s_waitcnt vmcnt(0)\n"
"v_max_u32 v38, v104, v105\n"
"v_max3_u32 v38, v106, v107, v38\n"
"v_mov_b32 v127, s16\n"
"v_mfma_f32_16x16x32_bf16 v[60:63], v[228:231], v[100:103], v[60:63]\n"
"v_cmp_eq_u32 vcc, -1, v38\n"
"ds_read_b32 v3, v5 offset:752\n"
"v_mfma_f32_16x16x32_bf16 v[64:67], v[232:235], v[100:103], v[64:67]\n"
"s_cbranch_vccnz .Lsl_574\n"
".Lbk_575:\n"
"v_mfma_f32_16x16x32_bf16 v[56:59], v[244:247], v[104:107], v[56:59]\n"
"ds_read_b128 v[76:79], v12 offset:2048\n"
"ds_read_b128 v[108:111], v13 offset:2048\n"
"v_mfma_f32_16x16x32_bf16 v[60:63], v[248:251], v[104:107], v[60:63]\n"
"ds_read_b128 v[68:71], v12 offset:0\n"
"ds_read_b128 v[80:83], v13 offset:0\n"
"v_mfma_f32_16x16x32_bf16 v[64:67], v[252:255], v[104:107], v[64:67]\n"
"ds_read_b128 v[72:75], v12 offset:1024\n"
"ds_read_b128 v[84:87], v13 offset:1024\n"
"v_mfma_f32_16x16x32_bf16 v[48:51], v[156:159], v[88:91], v[48:51]\n"
"ds_write_b128 v7, v[56:59] offset:0\n"
"v_mfma_f32_16x16x32_bf16 v[52:55], v[160:163], v[88:91], v[52:55]\n"
"ds_write_b128 v6, v[60:63] offset:0\n"
"v_mfma_f32_16x16x32_bf16 v[48:51], v[176:179], v[92:95], v[48:51]\n"
"ds_write_b128 v6, v[64:67] offset:1024\n"
"v_mfma_f32_16x16x32_bf16 v[52:55], v[180:183], v[92:95], v[52:55]\n"
"ds_write_b32 v4, v4 offset:768\n"
"v_mfma_f32_16x16x32_bf16 v[48:51], v[196:199], v[96:99], v[48:51]\n"
"s_mov_b32 s17, 0x100000\n"
"s_waitcnt lgkmcnt(10)\n"
"v_cmp_eq_u32 vcc, 0, v3\n"
"s_cbranch_vccz .Lok_577\n"
".Ltp_576:\n"
"ds_read_b32 v3, v5 offset:752\n"
"ds_read_b128 v[76:79], v12 offset:2048\n"
"ds_read_b128 v[108:111], v13 offset:2048\n"
"ds_read_b128 v[68:71], v12 offset:0\n"
"ds_read_b128 v[80:83], v13 offset:0\n"
"ds_read_b128 v[72:75], v12 offset:1024\n"
"ds_read_b128 v[84:87], v13 offset:1024\n"
"s_sub_u32 s17, s17, 1\n"
"s_cbranch_scc1 .Lok_577\n"
"s_waitcnt lgkmcnt(6)\n"
"v_cmp_eq_u32 vcc, 0, v3\n"
"s_cbranch_vccnz .Ltp_576\n"
"s_waitcnt lgkmcnt(0)\n"
".Lok_577:\n"
"v_mfma_f32_16x16x32_bf16 v[52:55], v[200:203], v[96:99], v[52:55]\n"
"s_waitcnt lgkmcnt(8)\n"
"v_add_f32 v76, v76, v108\n"
"v_add_f32 v77, v77, v109\n"
"v_add_f32 v78, v78, v110\n"
"v_mfma_f32_16x16x32_bf16 v[48:51], v[216:219], v[100:103], v[48:51]\n"
"v_add_f32 v79, v79, v111\n"
"s_waitcnt lgkmcnt(6)\n"
"v_add_f32 v68, v68, v80\n"
"v_add_f32 v69, v69, v81\n"
"v_mfma_f32_16x16x32_bf16 v[52:55], v[220:223], v[100:103], v[52:55]\n"
"v_add_f32 v70, v70, v82\n"
"v_add_f32 v71, v71, v83\n"
"s_waitcnt lgkmcnt(4)\n"
"v_add_f32 v72, v72, v84\n"
"v_mfma_f32_16x16x32_bf16 v[48:51], v[236:239], v[104:107], v[48:51]\n"
"v_add_f32 v73, v73, v85\n"
"v_add_f32 v74, v74, v86\n"
"v_add_f32 v75, v75, v87\n"
"v_mfma_f32_16x16x32_bf16 v[52:55], v[240:243], v[104:107], v[52:55]\n"
"ds_read_b32 v3, v5 offset:768\n"
"ds_read_b128 v[112:115], v11 offset:2048\n"
"ds_read_b128 v[60:63], v11 offset:0\n"
"ds_read_b128 v[64:67], v11 offset:1024\n"
"v_add_f32 v56, v56, v76\n"
"v_add_f32 v57, v57, v77\n"
"v_add_f32 v58, v58, v78\n"
"v_add_f32 v59, v59, v79\n"
"v_add_f32 v48, v48, v68\n"
"v_add_f32 v49, v49, v69\n"
"v_add_f32 v50, v50, v70\n"
"v_add_f32 v51, v51, v71\n"
"v_add_f32 v52, v52, v72\n"
"v_add_f32 v53, v53, v73\n"
"v_add_f32 v54, v54, v74\n"
"v_add_f32 v55, v55, v75\n"
"s_mov_b32 s17, 0x100000\n"
"s_waitcnt lgkmcnt(3)\n"
"v_cmp_eq_u32 vcc, 0, v3\n"
"s_cbranch_vccz .Lok_579\n"
".Ltp_578:\n"
"ds_read_b32 v3, v5 offset:768\n"
"ds_read_b128 v[112:115], v11 offset:2048\n"
"ds_read_b128 v[60:63], v11 offset:0\n"
"ds_read_b128 v[64:67], v11 offset:1024\n"
"s_sub_u32 s17, s17, 1\n"
"s_cbranch_scc1 .Lok_579\n"
"s_waitcnt lgkmcnt(3)\n"
"v_cmp_eq_u32 vcc, 0, v3\n"
"s_cbranch_vccnz .Ltp_578\n"
"s_waitcnt lgkmcnt(0)\n"
".Lok_579:\n"
"s_waitcnt lgkmcnt(2)\n"
"v_add_f32 v56, v56, v112\n"
"v_add_f32 v57, v57, v113\n"
"v_add_f32 v58, v58, v114\n"
"v_add_f32 v59, v59, v115\n"
"v_cvt_pk_bf16_f32 v44, v56, v57\n"
"v_cvt_pk_bf16_f32 v45, v58, v59\n"
"v_and_b32 v44, v19, v44\n"
"v_and_b32 v45, v19, v45\n"
"s_waitcnt lgkmcnt(1)\n"
"v_add_f32 v48, v48, v60\n"
"v_add_f32 v49, v49, v61\n"
"v_add_f32 v50, v50, v62\n"
"v_add_f32 v51, v51, v63\n"
"v_cvt_pk_bf16_f32 v40, v48, v49\n"
"v_cvt_pk_bf16_f32 v41, v50, v51\n"
"v_mfma_f32_16x16x32_bf16 v[88:91], v[44:47], v[44:47], 0\n"
"v_mfma_f32_16x16x32_bf16 v[92:95], v[20:23], v[44:47], 0\n"
"s_waitcnt lgkmcnt(0)\n"
"v_add_f32 v52, v52, v64\n"
"v_add_f32 v53, v53, v65\n"
"v_add_f32 v54, v54, v66\n"
"v_add_f32 v55, v55, v67\n"
"v_cvt_pk_bf16_f32 v42, v52, v53\n"
"v_cvt_pk_bf16_f32 v43, v54, v55\n"
"v_cvt_pk_bf16_f32 v116, v48, v49\n"
"v_cvt_pk_bf16_f32 v118, v50, v51\n"
"v_mfma_f32_16x16x32_bf16 v[88:91], v[40:43], v[40:43], v[88:91]\n"
"v_mfma_f32_16x16x32_bf16 v[92:95], v[20:23], v[40:43], v[92:95]\n"
"v_cvt_pk_bf16_f32 v120, v52, v53\n"
"v_cvt_pk_bf16_f32 v122, v54, v55\n"
"v_cvt_pk_bf16_f32 v124, v56, v57\n"
"v_cvt_pk_bf16_f32 v126, v58, v59\n"
"s_nop 0\n"
"v_cndmask_b32 v101, v88, v90, s[44:45]\n"
"v_cndmask_b32 v88, v89, v91, s[44:45]\n"
"s_nop 0\n"
"v_min_u32 v100, -2, v92\n"
"v_min_u32 v88, -2, v88\n"
"v_min_u32 v101, -2, v101\n"
"v_mov_b32_dpp v102, v100 quad_perm:[1,0,3,2] row_mask:0xf bank_mask:0xf\n"
"v_mov_b32_dpp v103, v88 quad_perm:[1,0,3,2] row_mask:0xf bank_mask:0xf\n"
"buffer_store_dwordx4 v[100:103], v16, s[4:7], s38 offen sc1\n"
"buffer_store_dwordx4 v[116:119], v17, s[4:7], s37 offen offset:0 sc1\n"
"buffer_store_dwordx4 v[120:123], v17, s[4:7], s37 offen offset:1024 sc1\n"
"buffer_store_dwordx4 v[124:127], v18, s[4:7], s37 offen offset:2048 sc1\n"
"ds_write_b32 v4, v4 offset:784\n"
"s_xor_b32 s79, s3, 1\n"
"s_lshl_b32 s79, s79, 8\n"
"s_add_u32 s32, s19, s79\n"
"s_cmp_ge_u32 s32, 64\n"
"s_cbranch_scc1 .Lnr_580\n"
"s_lshl_b32 s79, s19, 11\n"
"s_add_u32 s32, s79, 0x1648000\n"
"s_lshl_b32 s79, s19, 7\n"
"s_add_u32 s33, s79, 0x166a000\n"
"v_lshrrev_b32 v99, 3, v0\n"
"v_and_b32 v98, 7, v0\n"
"v_lshlrev_b32 v98, 4, v98\n"
"v_lshl_add_u32 v40, v99, 14, v98\n"
"v_mov_b32 v41, 0x3727c5ac\n"
"s_mov_b32 s89, 16\n"
".Ldl_581:\n"
"s_sub_u32 s89, s89, 1\n"
"s_cbranch_scc1 .Lde_582\n"
"s_sleep 1\n"
"s_branch .Ldl_581\n"
".Lde_582:\n"
"buffer_load_dwordx4 v[48:51], v2, s[4:7], s32 offen offset:0 sc1\n"
"buffer_load_dwordx4 v[52:55], v2, s[4:7], s32 offen offset:1024 sc1\n"
"s_mov_b32 s17, 32768\n"
".Ltp_583:\n"
"s_waitcnt vmcnt(0)\n"
"s_sub_u32 s17, s17, 1\n"
"s_cbranch_scc1 .Lou_584\n"
"v_max_u32 v38, v48, v49\n"
"v_max3_u32 v38, v50, v51, v38\n"
"v_max3_u32 v38, v52, v53, v38\n"
"v_max3_u32 v38, v54, v55, v38\n"
"v_cmp_eq_u32 vcc, -1, v38\n"
"s_cbranch_vccz .Lou_584\n"
"v_max_u32 v38, v50, v51\n"
"v_max3_u32 v38, v48, v49, v38\n"
"v_cmp_eq_u32 vcc, -1, v38\n"
"s_cbranch_vccz .Lsk_585\n"
"s_mov_b64 exec, vcc\n"
"buffer_load_dwordx4 v[48:51], v2, s[4:7], s32 offen offset:0 sc1\n"
"s_mov_b64 exec, -1\n"
".Lsk_585:\n"
"v_max_u32 v38, v54, v55\n"
"v_max3_u32 v38, v52, v53, v38\n"
"v_cmp_eq_u32 vcc, -1, v38\n"
"s_cbranch_vccz .Lsk_586\n"
"s_mov_b64 exec, vcc\n"
"buffer_load_dwordx4 v[52:55], v2, s[4:7], s32 offen offset:1024 sc1\n"
"s_mov_b64 exec, -1\n"
".Lsk_586:\n"
"s_branch .Ltp_583\n"
".Lou_584:\n"
"s_waitcnt vmcnt(0)\n"
"v_add_f32 v48, v48, v52\n"
"v_add_f32 v49, v49, v53\n"
"v_add_f32 v50, v50, v54\n"
"v_add_f32 v51, v51, v55\n"
"v_add_f32_dpp v52, v48, v48 row_ror:8 row_mask:0xf bank_mask:0xf\n"
"v_add_f32_dpp v53, v49, v49 row_ror:8 row_mask:0xf bank_mask:0xf\n"
"v_add_f32_dpp v54, v50, v50 row_ror:8 row_mask:0xf bank_mask:0xf\n"
"v_add_f32_dpp v55, v51, v51 row_ror:8 row_mask:0xf bank_mask:0xf\n"
"v_mov_b32 v48, v52\n"
"v_mov_b32 v49, v53\n"
"v_mov_b32 v50, v54\n"
"v_mov_b32 v51, v55\n"
"v_permlane16_swap_b32 v48, v52\n"
"v_permlane16_swap_b32 v49, v53\n"
"v_permlane16_swap_b32 v50, v54\n"
"v_permlane16_swap_b32 v51, v55\n"
"v_add_f32 v52, v48, v52\n"
"v_add_f32 v53, v49, v53\n"
"v_add_f32 v54, v50, v54\n"
"v_add_f32 v55, v51, v55\n"
"v_mov_b32 v48, v52\n"
"v_mov_b32 v49, v53\n"
"v_mov_b32 v50, v54\n"
"v_mov_b32 v51, v55\n"
"v_permlane32_swap_b32 v48, v52\n"
"v_permlane32_swap_b32 v49, v53\n"
"v_permlane32_swap_b32 v50, v54\n"
"v_permlane32_swap_b32 v51, v55\n"
"v_add_f32 v52, v48, v52\n"
"v_add_f32 v53, v49, v53\n"
"v_add_f32 v54, v50, v54\n"
"v_add_f32 v55, v51, v55\n"
"v_mul_f32 v48, 0x3acccccd, v52\n"
"v_mul_f32 v50, 0x3acccccd, v54\n"
"v_fma_f32 v53, -v52, v48, v53\n"
"v_fma_f32 v55, -v54, v50, v55\n"
"v_fmamk_f32 v53, v53, 0x3acccccd, v41\n"
"v_fmamk_f32 v55, v55, 0x3acccccd, v41\n"
"v_max_f32 v53, v53, v41\n"
"v_max_f32 v55, v55, v41\n"
"v_rsq_f32 v49, v53\n"
"v_rsq_f32 v51, v55\n"
"v_mul_f32 v48, -v48, v49\n"
"v_mul_f32 v50, -v50, v51\n"
"v_min_u32 v49, -2, v49\n"
"v_min_u32 v51, -2, v51\n"
"v_min_u32 v48, -2, v48\n"
"v_min_u32 v50, -2, v50\n"
"buffer_store_dwordx4 v[48:51], v40, s[4:7], s33 offen sc1\n"
"s_nop 3\n"
".Lnr_580:\n"
"s_cmp_lt_i32 s18, 0\n"
"s_cbranch_scc1 .Lnc_587\n"
"s_mov_b32 s17, 0x100000\n"
".Ltp_588:\n"
"s_sleep 1\n"
"ds_read_b32 v3, v5 offset:512\n"
"s_sub_u32 s17, s17, 1\n"
"s_cbranch_scc1 .Lou_589\n"
"s_waitcnt lgkmcnt(0)\n"
"v_cmp_eq_u32 vcc, 0, v3\n"
"s_cbranch_vccnz .Ltp_588\n"
".Lou_589:\n"
"s_waitcnt lgkmcnt(0)\n"
"s_setprio 0\n"
"s_lshl_b32 s79, s18, 8\n"
"s_add_u32 s32, s79, 0x13a8000\n"
"s_add_u32 s33, s32, 0x140000\n"
"s_and_b32 s79, s2, 7\n"
"s_lshl_b32 s79, s79, 14\n"
"s_add_u32 s34, s79, 0x166a000\n"
"s_add_u32 s35, s34, 0x1000\n"
"v_and_b32 v126, 0xffffff80, v29\n"
"v_add_u32 v126, v126, v29\n"
"ds_read_b128 v[88:91], v25 offset:448\n"
"ds_read_b128 v[92:95], v25 offset:432\n"
"ds_read_b64 v[96:97], v24 offset:448\n"
"ds_read_b64 v[98:99], v24 offset:456\n"
"ds_read_b128 v[100:103], v25 offset:960\n"
"ds_read_b128 v[104:107], v25 offset:1472\n"
"s_waitcnt lgkmcnt(1)\n"
"v_mfma_f32_16x16x32_f16 v[64:67], v[88:91], v[144:147], 0\n"
"ds_read_b128 v[40:43], v25 offset:1984\n"
"ds_read_b64 v[44:45], v24 offset:960\n"
"v_mfma_f32_16x16x32_f16 v[68:71], v[92:95], v[148:151], 0\n"
"ds_read_b64 v[46:47], v24 offset:968\n"
"ds_read_b64 v[72:73], v24 offset:1472\n"
"v_mfma_f32_16x16x32_f16 v[48:51], v[152:155], v[88:91], 0\n"
"ds_read_b64 v[74:75], v24 offset:1480\n"
"ds_read_b64 v[76:77], v24 offset:384\n"
"v_mfma_f32_16x16x32_f16 v[48:51], v[96:99], v[100:103], v[48:51]\n"
"ds_read_b64 v[78:79], v24 offset:392\n"
"s_waitcnt lgkmcnt(4)\n"
"v_mfma_f32_16x16x32_f16 v[52:55], v[96:99], v[104:107], 0\n"
"ds_read_b128 v[84:87], v25 offset:896\n"
"ds_read_b128 v[92:95], v25 offset:1408\n"
"v_mfma_f32_16x16x32_f16 v[56:59], v[96:99], v[40:43], 0\n"
"ds_read_b128 v[88:91], v25 offset:1920\n"
"ds_read_b64 v[100:101], v24 offset:896\n"
"v_mfma_f32_16x16x32_f16 v[52:55], v[44:47], v[40:43], v[52:55]\n"
"ds_read_b64 v[102:103], v24 offset:904\n"
"ds_read_b64 v[96:97], v24 offset:1408\n"
"v_cvt_pk_f16_f32 v80, v64, v65\n"
"v_mfma_f32_16x16x32_f16 v[48:51], v[44:47], v[104:107], v[48:51]\n"
"ds_read_b64 v[98:99], v24 offset:1416\n"
"s_waitcnt lgkmcnt(4)\n"
"v_mfma_f32_16x16x32_f16 v[48:51], v[72:75], v[40:43], v[48:51]\n"
"ds_read_b128 v[104:107], v25 offset:960\n"
"v_cvt_pk_f16_f32 v81, v66, v67\n"
"v_mfma_f32_16x16x32_f16 v[48:51], v[76:79], v[84:87], v[48:51]\n"
"ds_read_b128 v[44:47], v25 offset:944\n"
"v_cvt_pk_f16_f32 v82, v68, v69\n"
"v_mfma_f32_16x16x32_f16 v[52:55], v[76:79], v[92:95], v[52:55]\n"
"ds_read_b64 v[40:41], v24 offset:320\n"
"ds_read_b64 v[42:43], v24 offset:328\n"
"v_mfma_f32_16x16x32_f16 v[56:59], v[76:79], v[88:91], v[56:59]\n"
"ds_read_b128 v[72:75], v25 offset:832\n"
"s_waitcnt lgkmcnt(5)\n"
"v_mfma_f32_16x16x32_f16 v[52:55], v[100:103], v[88:91], v[52:55]\n"
"ds_read_b128 v[84:87], v25 offset:1344\n"
"v_cvt_pk_f16_f32 v83, v70, v71\n"
"v_mfma_f32_16x16x32_f16 v[48:51], v[100:103], v[92:95], v[48:51]\n"
"ds_read_b128 v[76:79], v25 offset:1856\n"
"ds_read_b64 v[92:93], v24 offset:832\n"
"v_mfma_f32_16x16x32_f16 v[48:51], v[96:99], v[88:91], v[48:51]\n"
"ds_read_b64 v[94:95], v24 offset:840\n"
"v_mfma_f32_16x16x32_f16 v[48:51], v[80:83], v[128:131], v[48:51]\n"
"ds_read_b64 v[100:101], v24 offset:1344\n"
"s_waitcnt lgkmcnt(9)\n"
"v_mfma_f32_16x16x32_f16 v[52:55], v[80:83], v[132:135], v[52:55]\n"
"ds_read_b64 v[102:103], v24 offset:1352\n"
"ds_read_b64 v[88:89], v24 offset:256\n"
"v_mfma_f32_16x16x32_f16 v[56:59], v[80:83], v[136:139], v[56:59]\n"
"ds_read_b64 v[90:91], v24 offset:264\n"
"v_mfma_f32_16x16x32_f16 v[60:63], v[80:83], v[140:143], 0\n"
"ds_read_b128 v[96:99], v25 offset:768\n"
"v_mfma_f32_16x16x32_f16 v[64:67], v[104:107], v[144:147], 0\n"
"s_waitcnt lgkmcnt(8)\n"
"v_mfma_f32_16x16x32_f16 v[68:71], v[44:47], v[148:151], 0\n"
"ds_read_b128 v[44:47], v25 offset:1280\n"
"v_mfma_f32_16x16x32_f16 v[52:55], v[152:155], v[104:107], v[52:55]\n"
"ds_read_b128 v[104:107], v25 offset:1792\n"
"v_mfma_f32_16x16x32_f16 v[48:51], v[40:43], v[72:75], v[48:51]\n"
"ds_read_b64 v[72:73], v24 offset:768\n"
"v_mfma_f32_16x16x32_f16 v[52:55], v[40:43], v[84:87], v[52:55]\n"
"ds_read_b64 v[74:75], v24 offset:776\n"
"s_waitcnt lgkmcnt(7)\n"
"v_mfma_f32_16x16x32_f16 v[56:59], v[40:43], v[76:79], v[56:59]\n"
"ds_read_b64 v[40:41], v24 offset:1280\n"
"v_mfma_f32_16x16x32_f16 v[52:55], v[92:95], v[76:79], v[52:55]\n"
"ds_read_b64 v[42:43], v24 offset:1288\n"
"v_cvt_pk_f16_f32 v80, v64, v65\n"
"v_mfma_f32_16x16x32_f16 v[48:51], v[92:95], v[84:87], v[48:51]\n"
"ds_read_b128 v[84:87], v25 offset:1472\n"
"v_cvt_pk_f16_f32 v81, v66, v67\n"
"v_mfma_f32_16x16x32_f16 v[48:51], v[100:103], v[76:79], v[48:51]\n"
"ds_read_b128 v[92:95], v25 offset:1456\n"
"s_waitcnt lgkmcnt(4)\n"
"v_mfma_f32_16x16x32_f16 v[48:51], v[88:91], v[96:99], v[48:51]\n"
"ds_read_b64 v[76:77], v24 offset:192\n"
"ds_read_b64 v[78:79], v24 offset:200\n"
"v_mfma_f32_16x16x32_f16 v[52:55], v[88:91], v[44:47], v[52:55]\n"
"ds_read_b128 v[100:103], v25 offset:704\n"
"v_cvt_pk_f16_f32 v82, v68, v69\n"
"v_mfma_f32_16x16x32_f16 v[56:59], v[88:91], v[104:107], v[56:59]\n"
"ds_read_b128 v[96:99], v25 offset:1216\n"
"v_cvt_pk_f16_f32 v83, v70, v71\n"
"v_mfma_f32_16x16x32_f16 v[52:55], v[72:75], v[104:107], v[52:55]\n"
"ds_read_b128 v[88:91], v25 offset:1728\n"
"s_waitcnt lgkmcnt(7)\n"
"v_mfma_f32_16x16x32_f16 v[48:51], v[72:75], v[44:47], v[48:51]\n"
"ds_read_b64 v[44:45], v24 offset:704\n"
"v_mfma_f32_16x16x32_f16 v[48:51], v[40:43], v[104:107], v[48:51]\n"
"ds_read_b64 v[46:47], v24 offset:712\n"
"ds_read_b64 v[72:73], v24 offset:1216\n"
"v_mfma_f32_16x16x32_f16 v[52:55], v[80:83], v[128:131], v[52:55]\n"
"ds_read_b64 v[74:75], v24 offset:1224\n"
"ds_read_b64 v[104:105], v24 offset:128\n"
"v_mfma_f32_16x16x32_f16 v[56:59], v[80:83], v[132:135], v[56:59]\n"
"ds_read_b64 v[106:107], v24 offset:136\n"
"s_waitcnt lgkmcnt(11)\n"
"v_mfma_f32_16x16x32_f16 v[60:63], v[80:83], v[136:139], v[60:63]\n"
"ds_read_b128 v[40:43], v25 offset:640\n"
"v_mfma_f32_16x16x32_f16 v[64:67], v[84:87], v[144:147], 0\n"
"v_mfma_f32_16x16x32_f16 v[68:71], v[92:95], v[148:151], 0\n"
"ds_read_b128 v[92:95], v25 offset:1152\n"
"v_mfma_f32_16x16x32_f16 v[56:59], v[152:155], v[84:87], v[56:59]\n"
"ds_read_b128 v[84:87], v25 offset:1664\n"
"s_waitcnt lgkmcnt(7)\n"
"v_mfma_f32_16x16x32_f16 v[48:51], v[76:79], v[100:103], v[48:51]\n"
"ds_read_b64 v[100:101], v24 offset:640\n"
"v_mfma_f32_16x16x32_f16 v[52:55], v[76:79], v[96:99], v[52:55]\n"
"ds_read_b64 v[102:103], v24 offset:648\n"
"v_mfma_f32_16x16x32_f16 v[56:59], v[76:79], v[88:91], v[56:59]\n"
"ds_read_b64 v[76:77], v24 offset:1152\n"
"v_mfma_f32_16x16x32_f16 v[52:55], v[44:47], v[88:91], v[52:55]\n"
"ds_read_b64 v[78:79], v24 offset:1160\n"
"s_waitcnt lgkmcnt(5)\n"
"v_mfma_f32_16x16x32_f16 v[48:51], v[44:47], v[96:99], v[48:51]\n"
"ds_read_b128 v[96:99], v25 offset:1984\n"
"v_cvt_pk_f16_f32 v80, v64, v65\n"
"v_mfma_f32_16x16x32_f16 v[48:51], v[72:75], v[88:91], v[48:51]\n"
"ds_read_b128 v[44:47], v25 offset:1968\n"
"ds_read_b64 v[88:89], v24 offset:64\n"
"v_mfma_f32_16x16x32_f16 v[48:51], v[104:107], v[40:43], v[48:51]\n"
"ds_read_b64 v[90:91], v24 offset:72\n"
"ds_read_b128 v[72:75], v25 offset:576\n"
"v_mfma_f32_16x16x32_f16 v[52:55], v[104:107], v[92:95], v[52:55]\n"
"ds_read_b128 v[40:43], v25 offset:1088\n"
"s_waitcnt lgkmcnt(6)\n"
"v_mfma_f32_16x16x32_f16 v[56:59], v[104:107], v[84:87], v[56:59]\n"
"ds_read_b128 v[104:107], v25 offset:1600\n"
"v_cvt_pk_f16_f32 v81, v66, v67\n"
"v_mfma_f32_16x16x32_f16 v[52:55], v[100:103], v[84:87], v[52:55]\n"
"v_cvt_pk_f16_f32 v82, v68, v69\n"
"v_cvt_pk_f16_f32 v83, v70, v71\n"
"v_mfma_f32_16x16x32_f16 v[48:51], v[100:103], v[92:95], v[48:51]\n"
"ds_read_b64 v[92:93], v24 offset:576\n"
"ds_read_b64 v[94:95], v24 offset:584\n"
"v_mfma_f32_16x16x32_f16 v[48:51], v[76:79], v[84:87], v[48:51]\n"
"ds_read_b64 v[100:101], v24 offset:1088\n"
"s_waitcnt lgkmcnt(8)\n"
"v_mfma_f32_16x16x32_f16 v[56:59], v[80:83], v[128:131], v[56:59]\n"
"ds_read_b64 v[102:103], v24 offset:1096\n"
"ds_read_b64 v[84:85], v24 offset:0\n"
"v_mfma_f32_16x16x32_f16 v[60:63], v[80:83], v[132:135], v[60:63]\n"
"ds_read_b64 v[86:87], v24 offset:8\n"
"v_mfma_f32_16x16x32_f16 v[64:67], v[96:99], v[144:147], 0\n"
"ds_read_b128 v[76:79], v25 offset:512\n"
"v_mfma_f32_16x16x32_f16 v[68:71], v[44:47], v[148:151], 0\n"
"ds_read_b128 v[44:47], v25 offset:1024\n"
"s_waitcnt lgkmcnt(8)\n"
"v_mfma_f32_16x16x32_f16 v[60:63], v[152:155], v[96:99], v[60:63]\n"
"ds_read_b128 v[96:99], v25 offset:1536\n"
"v_mfma_f32_16x16x32_f16 v[48:51], v[88:91], v[72:75], v[48:51]\n"
"ds_read_b64 v[72:73], v24 offset:512\n"
"v_mfma_f32_16x16x32_f16 v[52:55], v[88:91], v[40:43], v[52:55]\n"
"ds_read_b64 v[74:75], v24 offset:520\n"
"v_mfma_f32_16x16x32_f16 v[56:59], v[88:91], v[104:107], v[56:59]\n"
"ds_read_b64 v[88:89], v24 offset:1024\n"
"s_waitcnt lgkmcnt(5)\n"
"v_mfma_f32_16x16x32_f16 v[52:55], v[92:95], v[104:107], v[52:55]\n"
"ds_read_b64 v[90:91], v24 offset:1032\n"
"v_cvt_pk_f16_f32 v80, v64, v65\n"
"v_mfma_f32_16x16x32_f16 v[48:51], v[92:95], v[40:43], v[48:51]\n"
"v_cvt_pk_f16_f32 v81, v66, v67\n"
"v_cvt_pk_f16_f32 v82, v68, v69\n"
"v_mfma_f32_16x16x32_f16 v[48:51], v[100:103], v[104:107], v[48:51]\n"
"v_cvt_pk_f16_f32 v83, v70, v71\n"
"v_mfma_f32_16x16x32_f16 v[48:51], v[84:87], v[76:79], v[48:51]\n"
"s_waitcnt lgkmcnt(2)\n"
"v_mfma_f32_16x16x32_f16 v[52:55], v[84:87], v[44:47], v[52:55]\n"
"v_mfma_f32_16x16x32_f16 v[56:59], v[84:87], v[96:99], v[56:59]\n"
"v_mfma_f32_16x16x32_f16 v[52:55], v[72:75], v[96:99], v[52:55]\n"
"v_mfma_f32_16x16x32_f16 v[48:51], v[72:75], v[44:47], v[48:51]\n"
"s_waitcnt lgkmcnt(0)\n"
"v_mfma_f32_16x16x32_f16 v[48:51], v[88:91], v[96:99], v[48:51]\n"
"v_mfma_f32_16x16x32_f16 v[60:63], v[80:83], v[128:131], v[60:63]\n"
"s_setprio 2\n"
"s_mov_b32 s17, 0x100000\n"
".Ltp_590:\n"
"ds_read_b32 v3, v5 offset:784\n"
"s_sub_u32 s17, s17, 1\n"
"s_cbranch_scc1 .Lou_591\n"
"s_waitcnt lgkmcnt(0)\n"
"v_cmp_eq_u32 vcc, 0, v3\n"
"s_cbranch_vccnz .Ltp_590\n"
".Lou_591:\n"
"s_waitcnt lgkmcnt(0)\n"
"buffer_load_dwordx4 v[110:113], v126, s[4:7], s32 offen offset:0 sc1\n"
"buffer_load_dwordx4 v[114:117], v126, s[4:7], s32 offen offset:128 sc1\n"
"buffer_load_dwordx4 v[118:121], v126, s[4:7], s33 offen offset:0 sc1\n"
"buffer_load_dwordx4 v[122:125], v126, s[4:7], s33 offen offset:128 sc1\n"
"ds_read_b64 v[128:129], v24 offset:1984\n"
"ds_read_b64 v[130:131], v24 offset:1992\n"
"ds_read_b64 v[132:133], v24 offset:1920\n"
"ds_read_b64 v[134:135], v24 offset:1928\n"
"ds_read_b64 v[136:137], v24 offset:1856\n"
"ds_read_b64 v[138:139], v24 offset:1864\n"
"ds_read_b64 v[140:141], v24 offset:1792\n"
"ds_read_b64 v[142:143], v24 offset:1800\n"
"ds_read_b64 v[144:145], v24 offset:1728\n"
"ds_read_b64 v[146:147], v24 offset:1736\n"
"ds_read_b64 v[148:149], v24 offset:1664\n"
"ds_read_b64 v[150:151], v24 offset:1672\n"
"ds_read_b64 v[152:153], v24 offset:1600\n"
"ds_read_b64 v[154:155], v24 offset:1608\n"
"ds_read_b64 v[156:157], v24 offset:1536\n"
"ds_read_b64 v[158:159], v24 offset:1544\n"
"ds_read_b64 v[160:161], v24 offset:1472\n"
"ds_read_b64 v[162:163], v24 offset:1480\n"
"ds_read_b64 v[164:165], v24 offset:1408\n"
"ds_read_b64 v[166:167], v24 offset:1416\n"
"ds_read_b64 v[168:169], v24 offset:1344\n"
"ds_read_b64 v[170:171], v24 offset:1352\n"
"ds_read_b64 v[172:173], v24 offset:1280\n"
"ds_read_b64 v[174:175], v24 offset:1288\n"
"ds_read_b64 v[176:177], v24 offset:1216\n"
"ds_read_b64 v[178:179], v24 offset:1224\n"
"ds_read_b64 v[180:181], v24 offset:1152\n"
"ds_read_b64 v[182:183], v24 offset:1160\n"
"ds_read_b64 v[184:185], v24 offset:1088\n"
"ds_read_b64 v[186:187], v24 offset:1096\n"
"ds_read_b64 v[188:189], v24 offset:1024\n"
"ds_read_b64 v[190:191], v24 offset:1032\n"
"ds_read_b64 v[192:193], v24 offset:960\n"
"ds_read_b64 v[194:195], v24 offset:968\n"
"ds_read_b64 v[196:197], v24 offset:896\n"
"ds_read_b64 v[198:199], v24 offset:904\n"
"ds_read_b64 v[200:201], v24 offset:832\n"
"ds_read_b64 v[202:203], v24 offset:840\n"
"ds_read_b64 v[204:205], v24 offset:768\n"
"ds_read_b64 v[206:207], v24 offset:776\n"
"ds_read_b64 v[208:209], v24 offset:704\n"
"ds_read_b64 v[210:211], v24 offset:712\n"
"ds_read_b64 v[212:213], v24 offset:640\n"
"ds_read_b64 v[214:215], v24 offset:648\n"
"ds_read_b64 v[216:217], v24 offset:576\n"
"ds_read_b64 v[218:219], v24 offset:584\n"
"ds_read_b64 v[220:221], v24 offset:512\n"
"ds_read_b64 v[222:223], v24 offset:520\n"
"ds_read_b64 v[224:225], v24 offset:448\n"
"ds_read_b64 v[226:227], v24 offset:456\n"
"ds_read_b64 v[228:229], v24 offset:384\n"
"ds_read_b64 v[230:231], v24 offset:392\n"
"ds_read_b64 v[232:233], v24 offset:320\n"
"ds_read_b64 v[234:235], v24 offset:328\n"
"ds_read_b64 v[236:237], v24 offset:256\n"
"ds_read_b64 v[238:239], v24 offset:264\n"
"ds_read_b64 v[240:241], v24 offset:192\n"
"ds_read_b64 v[242:243], v24 offset:200\n"
"ds_read_b64 v[244:245], v24 offset:128\n"
"ds_read_b64 v[246:247], v24 offset:136\n"
"ds_read_b64 v[248:249], v24 offset:64\n"
"ds_read_b64 v[250:251], v24 offset:72\n"
"ds_read_b64 v[252:253], v24 offset:0\n"
"ds_read_b64 v[254:255], v24 offset:8\n"
"s_mov_b32 s38, 0x05040100\n"
"s_mov_b32 s39, 0x07060302\n"
"s_mov_b32 s17, 32768\n"
".Ltp_592:\n"
"s_waitcnt vmcnt(0)\n"
"s_sub_u32 s17, s17, 1\n"
"s_cbranch_scc1 .Lou_593\n"
"v_bitop3_b32 v64, v111, v113, s16 bitop3:0x7e\n"
"v_bitop3_b32 v65, v115, v117, s16 bitop3:0x7e\n"
"v_bitop3_b32 v66, v119, v121, s16 bitop3:0x7e\n"
"v_bitop3_b32 v67, v123, v125, s16 bitop3:0x7e\n"
"v_or3_b32 v68, v64, v65, v66\n"
"v_or_b32 v68, v68, v67\n"
"v_cmp_ne_u32 vcc, 0, v68\n"
"s_cbranch_vccz .Lou_593\n"
"s_sleep 2\n"
"v_cmp_ne_u32 vcc, 0, v64\n"
"s_cbranch_vccz .Lsk_594\n"
"s_mov_b64 exec, vcc\n"
"buffer_load_dwordx4 v[110:113], v126, s[4:7], s32 offen offset:0 sc1\n"
"s_mov_b64 exec, -1\n"
".Lsk_594:\n"
"v_cmp_ne_u32 vcc, 0, v65\n"
"s_cbranch_vccz .Lsk_595\n"
"s_mov_b64 exec, vcc\n"
"buffer_load_dwordx4 v[114:117], v126, s[4:7], s32 offen offset:128 sc1\n"
"s_mov_b64 exec, -1\n"
".Lsk_595:\n"
"v_cmp_ne_u32 vcc, 0, v66\n"
"s_cbranch_vccz .Lsk_596\n"
"s_mov_b64 exec, vcc\n"
"buffer_load_dwordx4 v[118:121], v126, s[4:7], s33 offen offset:0 sc1\n"
"s_mov_b64 exec, -1\n"
".Lsk_596:\n"
"v_cmp_ne_u32 vcc, 0, v67\n"
"s_cbranch_vccz .Lsk_597\n"
"s_mov_b64 exec, vcc\n"
"buffer_load_dwordx4 v[122:125], v126, s[4:7], s33 offen offset:128 sc1\n"
"s_mov_b64 exec, -1\n"
".Lsk_597:\n"
"s_branch .Ltp_592\n"
".Lou_593:\n"
"s_waitcnt vmcnt(0)\n"
"buffer_load_dwordx4 v[88:91], v30, s[4:7], s34 offen sc1\n"
"buffer_load_dwordx4 v[92:95], v30, s[4:7], s35 offen sc1\n"
"v_perm_b32 v64, v114, v110, s38\n"
"v_perm_b32 v65, v114, v110, s39\n"
"v_perm_b32 v66, v116, v112, s38\n"
"v_perm_b32 v67, v116, v112, s39\n"
"ds_write_b32 v32, v64 offset:0\n"
"ds_write_b32 v33, v65 offset:2048\n"
"ds_write_b32 v32, v66 offset:4224\n"
"ds_write_b32 v33, v67 offset:6272\n"
"v_perm_b32 v64, v122, v118, s38\n"
"v_perm_b32 v65, v122, v118, s39\n"
"v_perm_b32 v66, v124, v120, s38\n"
"v_perm_b32 v67, v124, v120, s39\n"
"ds_write_b32 v32, v64 offset:1024\n"
"ds_write_b32 v33, v65 offset:3072\n"
"ds_write_b32 v32, v66 offset:5248\n"
"ds_write_b32 v33, v67 offset:7296\n"
"v_lshlrev_b32 v64, 16, v110\n"
"v_lshlrev_b32 v65, 16, v114\n"
"v_and_b32 v66, 0xffff0000, v110\n"
"v_and_b32 v67, 0xffff0000, v114\n"
"v_lshlrev_b32 v68, 16, v112\n"
"v_lshlrev_b32 v69, 16, v116\n"
"v_and_b32 v70, 0xffff0000, v112\n"
"v_and_b32 v71, 0xffff0000, v116\n"
"v_lshlrev_b32 v72, 16, v118\n"
"v_lshlrev_b32 v73, 16, v122\n"
"v_and_b32 v74, 0xffff0000, v118\n"
"v_and_b32 v75, 0xffff0000, v122\n"
"v_lshlrev_b32 v76, 16, v120\n"
"v_lshlrev_b32 v77, 16, v124\n"
"v_and_b32 v78, 0xffff0000, v120\n"
"v_and_b32 v79, 0xffff0000, v124\n"
"s_mov_b32 s17, 32768\n"
".Ltp_598:\n"
"s_waitcnt vmcnt(0)\n"
"s_sub_u32 s17, s17, 1\n"
"s_cbranch_scc1 .Lou_599\n"
"v_max_u32 v110, v88, v89\n"
"v_max3_u32 v110, v90, v91, v110\n"
"v_max3_u32 v110, v92, v93, v110\n"
"v_max3_u32 v110, v94, v95, v110\n"
"v_cmp_eq_u32 vcc, -1, v110\n"
"s_cbranch_vccz .Lou_599\n"
"v_max_u32 v110, v90, v91\n"
"v_max3_u32 v110, v88, v89, v110\n"
"v_cmp_eq_u32 vcc, -1, v110\n"
"s_cbranch_vccz .Lsk_600\n"
"s_mov_b64 exec, vcc\n"
"buffer_load_dwordx4 v[88:91], v30, s[4:7], s34 offen sc1\n"
"s_mov_b64 exec, -1\n"
".Lsk_600:\n"
"v_max_u32 v110, v94, v95\n"
"v_max3_u32 v110, v92, v93, v110\n"
"v_cmp_eq_u32 vcc, -1, v110\n"
"s_cbranch_vccz .Lsk_601\n"
"s_mov_b64 exec, vcc\n"
"buffer_load_dwordx4 v[92:95], v30, s[4:7], s35 offen sc1\n"
"s_mov_b64 exec, -1\n"
".Lsk_601:\n"
"s_branch .Ltp_598\n"
".Lou_599:\n"
"s_waitcnt vmcnt(0)\n"
"v_fma_f32 v64, v64, v89, v88\n"
"v_fma_f32 v65, v65, v91, v90\n"
"v_cvt_pk_f16_f32 v64, v64, v65\n"
"v_fma_f32 v66, v66, v89, v88\n"
"v_fma_f32 v67, v67, v91, v90\n"
"v_cvt_pk_f16_f32 v66, v66, v67\n"
"v_fma_f32 v68, v68, v89, v88\n"
"v_fma_f32 v69, v69, v91, v90\n"
"v_cvt_pk_f16_f32 v68, v68, v69\n"
"v_fma_f32 v70, v70, v89, v88\n"
"v_fma_f32 v71, v71, v91, v90\n"
"v_cvt_pk_f16_f32 v70, v70, v71\n"
"ds_write2st64_b32 v31, v64, v66 offset0:0 offset1:18\n"
"ds_write2st64_b32 v31, v68, v70 offset0:36 offset1:54\n"
"v_fma_f32 v72, v72, v93, v92\n"
"v_fma_f32 v73, v73, v95, v94\n"
"v_cvt_pk_f16_f32 v72, v72, v73\n"
"v_fma_f32 v74, v74, v93, v92\n"
"v_fma_f32 v75, v75, v95, v94\n"
"v_cvt_pk_f16_f32 v74, v74, v75\n"
"v_fma_f32 v76, v76, v93, v92\n"
"v_fma_f32 v77, v77, v95, v94\n"
"v_cvt_pk_f16_f32 v76, v76, v77\n"
"v_fma_f32 v78, v78, v93, v92\n"
"v_fma_f32 v79, v79, v95, v94\n"
"v_cvt_pk_f16_f32 v78, v78, v79\n"
"ds_write2st64_b32 v31, v72, v74 offset0:4 offset1:22\n"
"ds_write2st64_b32 v31, v76, v78 offset0:40 offset1:58\n"
"ds_write_b32 v4, v4 offset:800\n"
"s_mov_b32 s17, 0x100000\n"
".Ltp_602:\n"
"ds_read_b32 v3, v5 offset:800\n"
"s_sub_u32 s17, s17, 1\n"
"s_cbranch_scc1 .Lou_603\n"
"s_waitcnt lgkmcnt(0)\n"
"v_cmp_eq_u32 vcc, 0, v3\n"
"s_cbranch_vccnz .Ltp_602\n"
".Lou_603:\n"
"s_waitcnt lgkmcnt(0)\n"
"s_mov_b32 s36, 0xfffefffe\n"
"ds_read_b64 v[88:89], v26 offset:0\n"
"ds_read_b64 v[92:93], v26 offset:512\n"
"ds_read_b64 v[96:97], v26 offset:1024\n"
"ds_read_b64 v[100:101], v26 offset:1536\n"
"ds_read_b128 v[104:107], v25 offset:2496\n"
"s_waitcnt lgkmcnt(1)\n"
"v_mfma_f32_16x16x16_bf16 v[48:51], v[34:35], v[88:89], v[48:51]\n"
"ds_read_b128 v[108:111], v25 offset:3008\n"
"ds_read_b128 v[112:115], v25 offset:2432\n"
"v_mfma_f32_16x16x16_bf16 v[52:55], v[34:35], v[92:93], v[52:55]\n"
"ds_read_b128 v[116:119], v25 offset:2944\n"
"ds_read_b128 v[120:123], v25 offset:2368\n"
"v_mfma_f32_16x16x16_bf16 v[56:59], v[34:35], v[96:97], v[56:59]\n"
"ds_read_b128 v[124:127], v25 offset:2880\n"
"v_mfma_f32_16x16x16_bf16 v[60:63], v[34:35], v[100:101], v[60:63]\n"
"ds_read_b128 v[40:43], v25 offset:2304\n"
"s_waitcnt lgkmcnt(4)\n"
"v_mfma_f32_16x16x32_f16 v[48:51], v[128:131], v[104:107], v[48:51]\n"
"ds_read_b128 v[44:47], v25 offset:2816\n"
"v_mfma_f32_16x16x32_f16 v[52:55], v[160:163], v[104:107], v[52:55]\n"
"ds_read_b128 v[88:91], v25 offset:2240\n"
"v_mfma_f32_16x16x32_f16 v[52:55], v[128:131], v[108:111], v[52:55]\n"
"ds_read_b128 v[92:95], v25 offset:2752\n"
"v_mfma_f32_16x16x32_f16 v[48:51], v[132:135], v[112:115], v[48:51]\n"
"ds_read_b128 v[96:99], v25 offset:2176\n"
"s_waitcnt lgkmcnt(6)\n"
"v_mfma_f32_16x16x32_f16 v[52:55], v[164:167], v[112:115], v[52:55]\n"
"v_mfma_f32_16x16x32_f16 v[52:55], v[132:135], v[116:119], v[52:55]\n"
"ds_read_b128 v[100:103], v25 offset:2688\n"
"v_mfma_f32_16x16x32_f16 v[48:51], v[136:139], v[120:123], v[48:51]\n"
"ds_read_b128 v[104:107], v25 offset:2112\n"
"v_mfma_f32_16x16x32_f16 v[52:55], v[168:171], v[120:123], v[52:55]\n"
"s_waitcnt lgkmcnt(5)\n"
"v_mfma_f32_16x16x32_f16 v[52:55], v[136:139], v[124:127], v[52:55]\n"
"ds_read_b128 v[108:111], v25 offset:2624\n"
"v_mfma_f32_16x16x32_f16 v[48:51], v[140:143], v[40:43], v[48:51]\n"
"ds_read_b128 v[112:115], v25 offset:2048\n"
"v_mfma_f32_16x16x32_f16 v[52:55], v[172:175], v[40:43], v[52:55]\n"
"v_mfma_f32_16x16x32_f16 v[52:55], v[140:143], v[44:47], v[52:55]\n"
"ds_read_b128 v[116:119], v25 offset:2560\n"
"s_waitcnt lgkmcnt(5)\n"
"v_mfma_f32_16x16x32_f16 v[48:51], v[144:147], v[88:91], v[48:51]\n"
"ds_read_b128 v[120:123], v25 offset:2496\n"
"v_mfma_f32_16x16x32_f16 v[52:55], v[176:179], v[88:91], v[52:55]\n"
"ds_read_b128 v[124:127], v25 offset:3008\n"
"v_mfma_f32_16x16x32_f16 v[52:55], v[144:147], v[92:95], v[52:55]\n"
"ds_read_b128 v[40:43], v25 offset:3520\n"
"v_mfma_f32_16x16x32_f16 v[48:51], v[148:151], v[96:99], v[48:51]\n"
"ds_read_b128 v[44:47], v25 offset:2432\n"
"s_waitcnt lgkmcnt(7)\n"
"v_mfma_f32_16x16x32_f16 v[52:55], v[180:183], v[96:99], v[52:55]\n"
"ds_read_b128 v[88:91], v25 offset:2944\n"
"v_mfma_f32_16x16x32_f16 v[52:55], v[148:151], v[100:103], v[52:55]\n"
"ds_read_b128 v[92:95], v25 offset:3456\n"
"v_mfma_f32_16x16x32_f16 v[48:51], v[152:155], v[104:107], v[48:51]\n"
"ds_read_b128 v[96:99], v25 offset:2368\n"
"v_mfma_f32_16x16x32_f16 v[52:55], v[184:187], v[104:107], v[52:55]\n"
"ds_read_b128 v[100:103], v25 offset:2880\n"
"s_waitcnt lgkmcnt(8)\n"
"v_mfma_f32_16x16x32_f16 v[52:55], v[152:155], v[108:111], v[52:55]\n"
"ds_read_b128 v[104:107], v25 offset:3392\n"
"v_mfma_f32_16x16x32_f16 v[48:51], v[156:159], v[112:115], v[48:51]\n"
"ds_read_b128 v[108:111], v25 offset:2304\n"
"v_mfma_f32_16x16x32_f16 v[52:55], v[188:191], v[112:115], v[52:55]\n"
"ds_read_b128 v[112:115], v25 offset:2816\n"
"v_mfma_f32_16x16x32_f16 v[52:55], v[156:159], v[116:119], v[52:55]\n"
"ds_read_b128 v[116:119], v25 offset:3328\n"
"s_waitcnt lgkmcnt(8)\n"
"v_mfma_f32_16x16x32_f16 v[56:59], v[192:195], v[120:123], v[56:59]\n"
"ds_read_b128 v[120:123], v25 offset:2240\n"
"v_mfma_f32_16x16x32_f16 v[56:59], v[160:163], v[124:127], v[56:59]\n"
"ds_read_b128 v[124:127], v25 offset:2752\n"
"v_mfma_f32_16x16x32_f16 v[56:59], v[128:131], v[40:43], v[56:59]\n"
"ds_read_b128 v[40:43], v25 offset:3264\n"
"v_mul_f32 v48, v36, v48\n"
"v_mul_f32 v49, v36, v49\n"
"v_mul_f32 v50, v36, v50\n"
"v_mfma_f32_16x16x32_f16 v[56:59], v[196:199], v[44:47], v[56:59]\n"
"ds_read_b128 v[44:47], v25 offset:2176\n"
"v_mul_f32 v51, v36, v51\n"
"v_cvt_pk_bf16_f32 v48, v48, v49\n"
"s_waitcnt lgkmcnt(8)\n"
"v_mfma_f32_16x16x32_f16 v[56:59], v[164:167], v[88:91], v[56:59]\n"
"ds_read_b128 v[88:91], v25 offset:2688\n"
"v_cvt_pk_bf16_f32 v49, v50, v51\n"
"v_pk_min_u16 v48, v48, s36\n"
"v_pk_min_u16 v49, v49, s36\n"
"v_mfma_f32_16x16x32_f16 v[56:59], v[132:135], v[92:95], v[56:59]\n"
"ds_read_b128 v[92:95], v25 offset:3200\n"
"ds_write_b64 v26, v[48:49] offset:0\n"
"v_mfma_f32_16x16x32_f16 v[56:59], v[200:203], v[96:99], v[56:59]\n"
"ds_read_b128 v[96:99], v25 offset:2112\n"
"v_mul_f32 v52, v36, v52\n"
"v_mul_f32 v53, v36, v53\n"
"v_mul_f32 v54, v36, v54\n"
"v_mfma_f32_16x16x32_f16 v[56:59], v[168:171], v[100:103], v[56:59]\n"
"ds_read_b128 v[100:103], v25 offset:2624\n"
"v_mul_f32 v55, v36, v55\n"
"v_cvt_pk_bf16_f32 v52, v52, v53\n"
"s_waitcnt lgkmcnt(9)\n"
"v_mfma_f32_16x16x32_f16 v[56:59], v[136:139], v[104:107], v[56:59]\n"
"ds_read_b128 v[104:107], v25 offset:3136\n"
"v_cvt_pk_bf16_f32 v53, v54, v55\n"
"v_pk_min_u16 v52, v52, s36\n"
"v_pk_min_u16 v53, v53, s36\n"
"v_mfma_f32_16x16x32_f16 v[56:59], v[204:207], v[108:111], v[56:59]\n"
"ds_read_b128 v[108:111], v25 offset:2048\n"
"ds_write_b64 v26, v[52:53] offset:512\n"
"v_mfma_f32_16x16x32_f16 v[56:59], v[172:175], v[112:115], v[56:59]\n"
"ds_read_b128 v[112:115], v25 offset:2560\n"
"ds_write_b32 v4, v4 offset:576\n"
"v_mfma_f32_16x16x32_f16 v[56:59], v[140:143], v[116:119], v[56:59]\n"
"ds_read_b128 v[116:119], v25 offset:3072\n"
"s_waitcnt lgkmcnt(11)\n"
"v_mfma_f32_16x16x32_f16 v[56:59], v[208:211], v[120:123], v[56:59]\n"
"ds_read_b128 v[120:123], v25 offset:2496\n"
"v_mfma_f32_16x16x32_f16 v[56:59], v[176:179], v[124:127], v[56:59]\n"
"ds_read_b128 v[124:127], v25 offset:3008\n"
"v_mfma_f32_16x16x32_f16 v[56:59], v[144:147], v[40:43], v[56:59]\n"
"ds_read_b128 v[40:43], v25 offset:3520\n"
"v_mfma_f32_16x16x32_f16 v[56:59], v[212:215], v[44:47], v[56:59]\n"
"ds_read_b128 v[44:47], v25 offset:4032\n"
"s_waitcnt lgkmcnt(10)\n"
"v_mfma_f32_16x16x32_f16 v[56:59], v[180:183], v[88:91], v[56:59]\n"
"ds_read_b128 v[88:91], v25 offset:2432\n"
"v_mfma_f32_16x16x32_f16 v[56:59], v[148:151], v[92:95], v[56:59]\n"
"ds_read_b128 v[92:95], v25 offset:2944\n"
"v_mfma_f32_16x16x32_f16 v[56:59], v[216:219], v[96:99], v[56:59]\n"
"ds_read_b128 v[96:99], v25 offset:3456\n"
"v_mfma_f32_16x16x32_f16 v[56:59], v[184:187], v[100:103], v[56:59]\n"
"ds_read_b128 v[100:103], v25 offset:3968\n"
"s_waitcnt lgkmcnt(8)\n"
"v_mfma_f32_16x16x32_f16 v[56:59], v[152:155], v[104:107], v[56:59]\n"
"ds_read_b128 v[104:107], v25 offset:2368\n"
"v_mfma_f32_16x16x32_f16 v[56:59], v[220:223], v[108:111], v[56:59]\n"
"ds_read_b128 v[108:111], v25 offset:2880\n"
"v_mfma_f32_16x16x32_f16 v[56:59], v[188:191], v[112:115], v[56:59]\n"
"ds_read_b128 v[112:115], v25 offset:3392\n"
"v_mfma_f32_16x16x32_f16 v[56:59], v[156:159], v[116:119], v[56:59]\n"
"ds_read_b128 v[116:119], v25 offset:3904\n"
"s_waitcnt lgkmcnt(8)\n"
"v_mfma_f32_16x16x32_f16 v[60:63], v[224:227], v[120:123], v[60:63]\n"
"ds_read_b128 v[120:123], v25 offset:2304\n"
"v_mfma_f32_16x16x32_f16 v[60:63], v[192:195], v[124:127], v[60:63]\n"
"ds_read_b128 v[124:127], v25 offset:2816\n"
"v_mfma_f32_16x16x32_f16 v[60:63], v[160:163], v[40:43], v[60:63]\n"
"ds_read_b128 v[40:43], v25 offset:3328\n"
"v_mul_f32 v56, v36, v56\n"
"v_mul_f32 v57, v36, v57\n"
"v_mul_f32 v58, v36, v58\n"
"v_mul_f32 v59, v36, v59\n"
"v_mfma_f32_16x16x32_f16 v[60:63], v[128:131], v[44:47], v[60:63]\n"
"ds_read_b128 v[44:47], v25 offset:3840\n"
"v_cvt_pk_bf16_f32 v56, v56, v57\n"
"v_cvt_pk_bf16_f32 v57, v58, v59\n"
"v_pk_min_u16 v56, v56, s36\n"
"s_waitcnt lgkmcnt(8)\n"
"v_mfma_f32_16x16x32_f16 v[60:63], v[228:231], v[88:91], v[60:63]\n"
"ds_read_b128 v[88:91], v25 offset:2240\n"
"v_pk_min_u16 v57, v57, s36\n"
"ds_write_b64 v26, v[56:57] offset:1024\n"
"v_mfma_f32_16x16x32_f16 v[60:63], v[196:199], v[92:95], v[60:63]\n"
"ds_read_b128 v[92:95], v25 offset:2752\n"
"ds_write_b32 v4, v4 offset:560\n"
"v_mfma_f32_16x16x32_f16 v[60:63], v[164:167], v[96:99], v[60:63]\n"
"ds_read_b128 v[96:99], v25 offset:3264\n"
"v_mfma_f32_16x16x32_f16 v[60:63], v[132:135], v[100:103], v[60:63]\n"
"ds_read_b128 v[100:103], v25 offset:3776\n"
"s_waitcnt lgkmcnt(10)\n"
"v_mfma_f32_16x16x32_f16 v[60:63], v[232:235], v[104:107], v[60:63]\n"
"ds_read_b128 v[104:107], v25 offset:2176\n"
"v_mfma_f32_16x16x32_f16 v[60:63], v[200:203], v[108:111], v[60:63]\n"
"ds_read_b128 v[108:111], v25 offset:2688\n"
"v_mfma_f32_16x16x32_f16 v[60:63], v[168:171], v[112:115], v[60:63]\n"
"ds_read_b128 v[112:115], v25 offset:3200\n"
"v_mfma_f32_16x16x32_f16 v[60:63], v[136:139], v[116:119], v[60:63]\n"
"ds_read_b128 v[116:119], v25 offset:3712\n"
"s_waitcnt lgkmcnt(10)\n"
"v_mfma_f32_16x16x32_f16 v[60:63], v[236:239], v[120:123], v[60:63]\n"
"ds_read_b128 v[120:123], v25 offset:2112\n"
"v_mfma_f32_16x16x32_f16 v[60:63], v[204:207], v[124:127], v[60:63]\n"
"ds_read_b128 v[124:127], v25 offset:2624\n"
"v_mfma_f32_16x16x32_f16 v[60:63], v[172:175], v[40:43], v[60:63]\n"
"ds_read_b128 v[40:43], v25 offset:3136\n"
"v_mfma_f32_16x16x32_f16 v[60:63], v[140:143], v[44:47], v[60:63]\n"
"ds_read_b128 v[44:47], v25 offset:3648\n"
"s_waitcnt lgkmcnt(8)\n"
"v_mfma_f32_16x16x32_f16 v[60:63], v[240:243], v[88:91], v[60:63]\n"
"ds_read_b128 v[88:91], v25 offset:2048\n"
"v_mfma_f32_16x16x32_f16 v[60:63], v[208:211], v[92:95], v[60:63]\n"
"ds_read_b128 v[92:95], v25 offset:2560\n"
"v_mfma_f32_16x16x32_f16 v[60:63], v[176:179], v[96:99], v[60:63]\n"
"ds_read_b128 v[96:99], v25 offset:3072\n"
"v_mfma_f32_16x16x32_f16 v[60:63], v[144:147], v[100:103], v[60:63]\n"
"ds_read_b128 v[100:103], v25 offset:3584\n"
"s_waitcnt lgkmcnt(8)\n"
"v_mfma_f32_16x16x32_f16 v[60:63], v[244:247], v[104:107], v[60:63]\n"
"v_mfma_f32_16x16x32_f16 v[60:63], v[212:215], v[108:111], v[60:63]\n"
"v_mfma_f32_16x16x32_f16 v[60:63], v[180:183], v[112:115], v[60:63]\n"
"v_mfma_f32_16x16x32_f16 v[60:63], v[148:151], v[116:119], v[60:63]\n"
"s_waitcnt lgkmcnt(4)\n"
"v_mfma_f32_16x16x32_f16 v[60:63], v[248:251], v[120:123], v[60:63]\n"
"v_mfma_f32_16x16x32_f16 v[60:63], v[216:219], v[124:127], v[60:63]\n"
"v_mfma_f32_16x16x32_f16 v[60:63], v[184:187], v[40:43], v[60:63]\n"
"v_mfma_f32_16x16x32_f16 v[60:63], v[152:155], v[44:47], v[60:63]\n"
"s_waitcnt lgkmcnt(0)\n"
"v_mfma_f32_16x16x32_f16 v[60:63], v[252:255], v[88:91], v[60:63]\n"
"v_mfma_f32_16x16x32_f16 v[60:63], v[220:223], v[92:95], v[60:63]\n"
"v_mfma_f32_16x16x32_f16 v[60:63], v[188:191], v[96:99], v[60:63]\n"
"v_mfma_f32_16x16x32_f16 v[60:63], v[156:159], v[100:103], v[60:63]\n"
"s_nop 7\n"
"v_mul_f32 v60, v36, v60\n"
"v_mul_f32 v61, v36, v61\n"
"v_mul_f32 v62, v36, v62\n"
"v_mul_f32 v63, v36, v63\n"
"v_cvt_pk_bf16_f32 v60, v60, v61\n"
"v_cvt_pk_bf16_f32 v61, v62, v63\n"
"v_pk_min_u16 v60, v60, s36\n"
"v_pk_min_u16 v61, v61, s36\n"
"ds_write_b64 v26, v[60:61] offset:1536\n"
"ds_write_b32 v4, v4 offset:544\n"
"s_nop 2\n"
".Lnc_587:\n"
"s_branch .L_yend\n"
".Lsl_320:\n"
"s_nop 7\n"
"s_mov_b32 s17, 32768\n"
".Ltp_322:\n"
"buffer_load_dwordx4 v[44:47], v38, s[4:7], s33 offen sc1\n"
"s_sub_u32 s17, s17, 1\n"
"s_cbranch_scc1 .Lou_323\n"
"s_waitcnt vmcnt(0)\n"
"v_xor_b32 v44, s16, v44\n"
"v_xor_b32 v45, s16, v45\n"
"v_xor_b32 v46, s16, v46\n"
"v_xor_b32 v47, s16, v47\n"
"v_or_b32 v44, v44, v45\n"
"v_or3_b32 v44, v44, v46, v47\n"
"v_cmp_ne_u32 vcc, 0, v44\n"
"s_cbranch_vccz .Lou_323\n"
"s_sleep 16\n"
"s_branch .Ltp_322\n"
".Lou_323:\n"
"s_waitcnt vmcnt(0)\n"
"s_branch .Lbk_321\n"
".Lst_332:\n"
"s_nop 7\n"
"s_lshl_b32 s79, s3, 2\n"
"s_add_u32 s79, s79, 0x26800\n"
"v_mov_b32 v36, s79\n"
"s_cmp_lg_u32 s40, 0\n"
"s_cbranch_scc1 .Lhv_335\n"
"s_call_b64 s[60:61], .Lkg_324\n"
"s_setprio 0\n"
".Lhv_335:\n"
"v_and_b32 v73, 15, v0\n"
"v_lshlrev_b32 v74, 2, v0\n"
"v_lshrrev_b32 v75, 4, v0\n"
"s_mul_i32 s79, s3, 0x2100\n"
"s_add_u32 s72, s79, 0x15100\n"
"v_lshlrev_b32 v76, 3, v73\n"
"v_lshlrev_b32 v77, 4, v75\n"
"v_lshlrev_b32 v78, 8, v75\n"
"s_add_u32 s73, s72, 0x400\n"
"s_sub_u32 s74, s72, 0x1100\n"
"s_add_u32 s79, s72, 0x800\n"
"v_add3_u32 v79, v78, v76, s79\n"
"v_lshl_add_u32 v99, v73, 6, v77\n"
"v_add_u32 v80, s72, v99\n"
"v_lshl_add_u32 v99, v73, 5, v77\n"
"v_lshl_add_u32 v98, v73, 4, v78\n"
"s_mov_b32 vcc_lo, -1\n"
"s_mov_b32 vcc_hi, 0\n"
"v_cndmask_b32 v99, v98, v99, vcc\n"
"v_add_u32 v81, s73, v99\n"
"s_mov_b32 vcc_lo, -1\n"
"s_mov_b32 vcc_hi, 1\n"
"v_cndmask_b32 v82, 2.0, v79, vcc\n"
"s_mov_b32 vcc_lo, -1\n"
"s_mov_b32 vcc_hi, 0\n"
"v_cndmask_b32 v83, 2.0, v79, vcc\n"
"s_add_u32 s79, s72, 0x7ec\n"
"s_add_u32 s79, s79, 0x2c0\n"
"v_lshlrev_b32 v99, 5, v75\n"
"v_sub_u32 v99, v76, v99\n"
"v_add_u32 v84, s79, v99\n"
"s_add_u32 s79, s72, 0xa40\n"
"v_add_u32 v99, s79, v74\n"
"s_mov_b32 vcc_lo, -1\n"
"s_mov_b32 vcc_hi, 1\n"
"v_cndmask_b32 v100, 2.0, v99, vcc\n"
"v_sub_u32 v99, 0x420, v0\n"
"v_sub_u32 v98, 0x41f, v0\n"
"v_bfe_u32 v98, v98, 2, 1\n"
"v_add_u32 v99, v99, v98\n"
"v_and_b32 v99, 0xffc, v99\n"
"v_min_i32 v99, 0x400, v99\n"
"v_mov_b32 v98, 0x640\n"
"s_mov_b32 vcc_lo, 0xf8000000\n"
"s_mov_b32 vcc_hi, -1\n"
"v_cndmask_b32 v99, v98, v99, vcc\n"
"v_add_u32 v101, s73, v99\n"
"v_add_u32 v102, s74, v74\n"
"v_lshlrev_b32 v99, 3, v75\n"
"v_sub_u32 v99, v99, v73\n"
"s_add_u32 s79, s74, 60\n"
"v_lshl_add_u32 v103, v99, 2, s79\n"
"v_mov_b32 v99, 0x3f800000\n"
"s_mov_b32 vcc_lo, 0x7f00000\n"
"s_mov_b32 vcc_hi, 0\n"
"v_cndmask_b32 v104, 0, v99, vcc\n"
"v_mov_b32 v99, 0x3f800000\n"
"s_mov_b32 vcc_lo, 0xff80000\n"
"s_mov_b32 vcc_hi, 0\n"
"v_cndmask_b32 v105, 0, v99, vcc\n"
"v_mov_b32 v106, 0\n"
"v_cmp_eq_u32 vcc, 0, v0\n"
"v_lshlrev_b32 v140, 16, v140\n"
"v_mul_f32 v140, 0.5, v140\n"
"v_cndmask_b32 v140, 0, v140, vcc\n"
"v_add_f32 v128, v128, v140\n"
"v_cndmask_b32 v136, v136, 0, s[56:57]\n"
"v_cndmask_b32 v137, v137, 0, s[58:59]\n"
"v_cndmask_b32 v138, v138, 0, s[58:59]\n"
"v_cndmask_b32 v139, v139, 0, s[58:59]\n"
"v_max3_f32 v85, |v128|, |v129|, 0\n"
"v_max3_f32 v85, |v130|, |v131|, v85\n"
"v_max3_f32 v85, |v132|, |v133|, v85\n"
"v_max3_f32 v85, |v134|, |v135|, v85\n"
"v_max3_f32 v85, |v136|, |v137|, v85\n"
"v_max3_f32 v85, |v138|, |v139|, v85\n"
"s_nop 1\n"
"v_max_u32_dpp v85, v85, v85 row_ror:8 row_mask:0xf bank_mask:0xf\n"
"s_nop 1\n"
"v_max_u32_dpp v85, v85, v85 row_ror:4 row_mask:0xf bank_mask:0xf\n"
"s_nop 1\n"
"v_max_u32_dpp v85, v85, v85 row_ror:2 row_mask:0xf bank_mask:0xf\n"
"s_nop 1\n"
"v_max_u32_dpp v85, v85, v85 row_ror:1 row_mask:0xf bank_mask:0xf\n"
"v_mov_b32 v86, v85\n"
"s_nop 1\n"
"v_permlane16_swap_b32 v85, v86\n"
"v_max_u32 v85, v85, v86\n"
"v_mov_b32 v86, v85\n"
"s_nop 1\n"
"v_permlane32_swap_b32 v85, v86\n"
"v_max_u32 v85, v85, v86\n"
"s_nop 0\n"
"v_readfirstlane_b32 s51, v85\n"
"s_nop 2\n"
"s_bfe_u32 s50, s51, 0x80017\n"
"s_max_i32 s50, s50, 77\n"
"s_min_i32 s50, s50, 227\n"
"s_cmp_gt_i32 s51, 0\n"
"s_cselect_b32 s50, s50, 127\n"
"s_sub_u32 s51, 268, s50\n"
"s_lshl_b32 s51, s51, 23\n"
"s_sub_u32 s50, s50, 13\n"
"s_lshl_b32 s50, s50, 23\n"
"v_mul_f32 v128, s51, v128\n"
"v_mul_f32 v129, s51, v129\n"
"v_mul_f32 v130, s51, v130\n"
"v_mul_f32 v131, s51, v131\n"
"v_mul_f32 v132, s51, v132\n"
"v_mul_f32 v133, s51, v133\n"
"v_mul_f32 v134, s51, v134\n"
"v_mul_f32 v135, s51, v135\n"
"v_mul_f32 v136, s51, v136\n"
"v_mul_f32 v137, s51, v137\n"
"v_mul_f32 v138, s51, v138\n"
"v_mul_f32 v139, s51, v139\n"
"ds_write_b128 v80, v[128:131]\n"
"ds_write_b128 v81, v[132:135]\n"
"ds_write_b64 v82, v[136:137]\n"
"ds_write_b64 v83, v[138:139] offset:128\n"
"v_mov_b32 v87, s50\n"
"ds_write_b32 v36, v87\n"
"ds_write_b64 v82, v[136:137] offset:704\n"
"ds_write_b64 v83, v[138:139] offset:832\n"
"ds_write_b32 v100, v106\n"
"ds_read_b32 v101, v101\n"
"ds_write2st64_b32 v102, v104, v105 offset0:1 offset1:2\n"
"ds_read2_b32 v[48:49], v84 offset0:6 offset1:4\n"
"ds_read2_b32 v[50:51], v84 offset0:2 offset1:0\n"
"ds_read2_b32 v[52:53], v84 offset0:7 offset1:5\n"
"ds_read2_b32 v[54:55], v84 offset0:3 offset1:1\n"
"ds_read2_b32 v[56:57], v84 offset0:38 offset1:36\n"
"ds_read2_b32 v[58:59], v84 offset0:34 offset1:32\n"
"ds_read2_b32 v[60:61], v84 offset0:39 offset1:37\n"
"ds_read2_b32 v[62:63], v84 offset0:35 offset1:33\n"
"s_waitcnt lgkmcnt(9)\n"
"ds_write_b32 v102, v101\n"
"ds_read2_b32 v[64:65], v84 offset0:70 offset1:68\n"
"ds_read2_b32 v[66:67], v84 offset0:66 offset1:64\n"
"ds_read2_b32 v[68:69], v84 offset0:71 offset1:69\n"
"ds_read2_b32 v[70:71], v84 offset0:67 offset1:65\n"
"ds_read2_b32 v[72:73], v84 offset0:102 offset1:100\n"
"ds_read2_b32 v[74:75], v84 offset0:98 offset1:96\n"
"ds_read2_b32 v[76:77], v84 offset0:103 offset1:101\n"
"ds_read2_b32 v[78:79], v84 offset0:99 offset1:97\n"
"ds_read2_b32 v[88:89], v103 offset0:0 offset1:1\n"
"s_waitcnt lgkmcnt(15)\n"
"v_cvt_pk_f16_f32 v128, v48, v49\n"
"ds_read2_b32 v[90:91], v103 offset0:2 offset1:3\n"
"v_cvt_pk_f16_f32 v129, v50, v51\n"
"ds_read2_b32 v[92:93], v103 offset0:4 offset1:5\n"
"v_cvt_pk_f16_f32 v130, v52, v53\n"
"ds_read2_b32 v[94:95], v103 offset0:6 offset1:7\n"
"s_waitcnt lgkmcnt(15)\n"
"v_cvt_pk_f16_f32 v131, v54, v55\n"
"ds_read2_b32 v[96:97], v103 offset0:64 offset1:65\n"
"v_cvt_pk_f16_f32 v132, v56, v57\n"
"ds_read2_b32 v[98:99], v103 offset0:66 offset1:67\n"
"v_cvt_pk_f16_f32 v133, v58, v59\n"
"ds_read2_b32 v[108:109], v103 offset0:68 offset1:69\n"
"s_waitcnt lgkmcnt(15)\n"
"v_cvt_pk_f16_f32 v134, v60, v61\n"
"ds_read2_b32 v[110:111], v103 offset0:70 offset1:71\n"
"v_cvt_pk_f16_f32 v135, v62, v63\n"
"ds_read2_b32 v[112:113], v103 offset0:128 offset1:129\n"
"s_waitcnt lgkmcnt(15)\n"
"v_cvt_pk_f16_f32 v136, v64, v65\n"
"ds_read2_b32 v[114:115], v103 offset0:130 offset1:131\n"
"v_cvt_pk_f16_f32 v137, v66, v67\n"
"ds_read2_b32 v[116:117], v103 offset0:132 offset1:133\n"
"s_waitcnt lgkmcnt(15)\n"
"v_cvt_pk_f16_f32 v138, v68, v69\n"
"ds_read2_b32 v[118:119], v103 offset0:134 offset1:135\n"
"v_cvt_pk_f16_f32 v139, v70, v71\n"
"s_waitcnt lgkmcnt(15)\n"
"v_cvt_pk_f16_f32 v140, v72, v73\n"
"s_waitcnt lgkmcnt(14)\n"
"v_cvt_pk_f16_f32 v141, v74, v75\n"
"s_waitcnt lgkmcnt(13)\n"
"v_cvt_pk_f16_f32 v142, v76, v77\n"
"s_waitcnt lgkmcnt(12)\n"
"v_cvt_pk_f16_f32 v143, v78, v79\n"
"s_waitcnt lgkmcnt(11)\n"
"v_cvt_pk_f16_f32 v152, v88, v89\n"
"s_waitcnt lgkmcnt(10)\n"
"v_cvt_pk_f16_f32 v153, v90, v91\n"
"s_waitcnt lgkmcnt(9)\n"
"v_cvt_pk_f16_f32 v154, v92, v93\n"
"s_waitcnt lgkmcnt(8)\n"
"v_cvt_pk_f16_f32 v155, v94, v95\n"
"s_waitcnt lgkmcnt(7)\n"
"v_cvt_pk_f16_f32 v144, v96, v97\n"
"s_waitcnt lgkmcnt(6)\n"
"v_cvt_pk_f16_f32 v145, v98, v99\n"
"s_waitcnt lgkmcnt(5)\n"
"v_cvt_pk_f16_f32 v146, v108, v109\n"
"s_waitcnt lgkmcnt(4)\n"
"v_cvt_pk_f16_f32 v147, v110, v111\n"
"s_waitcnt lgkmcnt(3)\n"
"v_cvt_pk_f16_f32 v148, v112, v113\n"
"s_waitcnt lgkmcnt(2)\n"
"v_cvt_pk_f16_f32 v149, v114, v115\n"
"s_waitcnt lgkmcnt(1)\n"
"v_cvt_pk_f16_f32 v150, v116, v117\n"
"s_waitcnt lgkmcnt(0)\n"
"v_cvt_pk_f16_f32 v151, v118, v119\n"
"ds_write_b32 v4, v4 offset:192\n"
"s_mov_b32 s43, 1\n"
"s_nop 2\n"
"s_setpc_b64 s[46:47]\n"
".Lst_333:\n"
"s_nop 7\n"
"s_mov_b32 s43, 2\n"
"s_nop 0\n"
"s_setpc_b64 s[46:47]\n"
".Lst_334:\n"
"s_nop 7\n"
"v_and_b32 v72, 15, v0\n"
"v_lshrrev_b32 v73, 4, v0\n"
"v_mov_b32 v99, 0xffff0000\n"
"v_mov_b32 v98, 0xffff\n"
"s_mov_b32 vcc_lo, 0xaaaaaaaa\n"
"s_mov_b32 vcc_hi, 0xaaaaaaaa\n"
"v_cndmask_b32 v74, v98, v99, vcc\n"
"v_lshlrev_b32 v75, 5, v72\n"
"v_lshrrev_b32 v76, 2, v72\n"
"s_lshl_b32 s79, s3, 10\n"
"v_lshl_add_u32 v30, v0, 4, s79\n"
"v_lshrrev_b32 v77, 2, v30\n"
"v_lshrrev_b32 v78, 6, v30\n"
"v_lshlrev_b32 v99, 3, v73\n"
"v_sub_u32 v99, v99, v72\n"
"v_add_u32 v79, 0x3ef, v99\n"
"v_and_b32 v99, 24, v78\n"
"v_xor_b32 v99, v77, v99\n"
"v_add_u32 v32, 0x20c00, v99\n"
"s_lshl_b32 s79, s3, 8\n"
"s_add_u32 s79, s79, 0x20c00\n"
"v_xor_b32 v99, s3, v72\n"
"v_and_b32 v99, 1, v99\n"
"v_lshrrev_b32 v98, 1, v73\n"
"v_xor_b32 v98, v76, v98\n"
"v_and_b32 v98, 1, v98\n"
"v_lshl_add_u32 v99, v99, 1, v98\n"
"v_and_b32 v98, 2, v72\n"
"v_lshrrev_b32 v97, 3, v72\n"
"v_lshlrev_b32 v97, 7, v97\n"
"v_lshl_add_u32 v97, v76, 11, v97\n"
"v_lshl_add_u32 v97, v73, 6, v97\n"
"v_lshl_add_u32 v98, v98, 4, v97\n"
"v_lshl_add_u32 v99, v99, 3, v98\n"
"v_add_u32 v27, s79, v99\n"
"s_mul_i32 s79, s3, 0x2100\n"
"s_add_u32 s79, s79, 0x13840\n"
"v_lshrrev_b32 v99, 2, v79\n"
"v_and_b32 v98, 3, v79\n"
"v_mul_i32_i24 v98, 0x840, v98\n"
"v_lshl_add_u32 v99, v99, 3, v98\n"
"v_add_u32 v24, s79, v99\n"
"s_mul_i32 s79, s3, 0x1200\n"
"s_add_u32 s79, s79, 0x1c420\n"
"v_lshl_add_u32 v99, v73, 4, v75\n"
"v_add_u32 v25, s79, v99\n"
"s_lshl_b32 s79, s3, 11\n"
"s_lshr_b32 s78, s3, 1\n"
"s_lshl_b32 s78, s78, 7\n"
"s_add_u32 s79, s79, s78\n"
"s_add_u32 s79, s79, 0x20c00\n"
"s_and_b32 s78, s3, 1\n"
"v_xor_b32 v99, v73, v76\n"
"v_xor_b32 v99, s78, v99\n"
"v_and_b32 v99, 3, v99\n"
"v_lshl_add_u32 v99, v99, 3, v75\n"
"v_add_u32 v26, s79, v99\n"
"v_xor_b32 v28, 8, v27\n"
"v_lshrrev_b32 v99, 7, v30\n"
"v_and_b32 v98, 0x70, v30\n"
"s_mov_b32 s79, 0x5000\n"
"v_mad_i32_i24 v29, v99, s79, v98\n"
"v_add_u32 v31, 0x1ce00, v77\n"
"v_xor_b32 v33, 8, v32\n"
"s_mov_b32 vcc_lo, 0x300003\n"
"s_mov_b32 vcc_hi, 0x30000300\n"
"v_cndmask_b32 v34, 0, v74, vcc\n"
"s_mov_b32 vcc_lo, 0xc0000c\n"
"s_mov_b32 vcc_hi, 0xc0000c00\n"
"v_cndmask_b32 v35, 0, v74, vcc\n"
"s_lshl_b32 s79, s3, 2\n"
"s_add_u32 s79, s79, 0x26800\n"
"v_mov_b32 v36, s79\n"
"v_and_b32 v99, 48, v30\n"
"v_mul_i32_i24 v99, 0x50, v99\n"
"s_mov_b32 s79, 0x2800\n"
"v_mad_i32_i24 v37, v78, s79, v99\n"
"ds_read_b32 v36, v36\n"
"s_waitcnt lgkmcnt(0)\n"
"v_sub_u32 v76, 0x7f000000, v36\n"
"v_lshrrev_b32 v77, 16, v76\n"
"v_or_b32 v76, v76, v77\n"
"v_and_b32 v34, v76, v34\n"
"v_and_b32 v35, v76, v35\n"
"s_mov_b32 s43, 3\n"
"s_nop 3\n"
"s_setpc_b64 s[46:47]\n"
".Loa_336:\n"
"s_nop 7\n"
"s_mov_b32 s17, 32768\n"
".Ltp_337:\n"
"s_waitcnt vmcnt(0)\n"
"s_sub_u32 s17, s17, 1\n"
"s_cbranch_scc1 .Lou_338\n"
"v_max_u32 v38, v88, v89\n"
"v_max3_u32 v38, v90, v91, v38\n"
"v_max3_u32 v38, v92, v93, v38\n"
"v_max3_u32 v38, v94, v95, v38\n"
"v_max3_u32 v38, v96, v97, v38\n"
"v_max3_u32 v38, v98, v99, v38\n"
"v_max3_u32 v38, v100, v101, v38\n"
"v_max3_u32 v38, v102, v103, v38\n"
"v_max3_u32 v38, v104, v105, v38\n"
"v_max3_u32 v38, v106, v107, v38\n"
"v_max3_u32 v38, v108, v109, v38\n"
"v_max3_u32 v38, v110, v111, v38\n"
"v_max3_u32 v38, v112, v113, v38\n"
"v_max3_u32 v38, v114, v115, v38\n"
"v_max3_u32 v38, v116, v117, v38\n"
"v_max3_u32 v38, v118, v119, v38\n"
"v_max3_u32 v38, v120, v121, v38\n"
"v_max3_u32 v38, v122, v123, v38\n"
"v_max3_u32 v38, v124, v125, v38\n"
"v_max3_u32 v38, v126, v127, v38\n"
"v_cmp_eq_u32 vcc, -1, v38\n"
"s_cbranch_vccz .Lou_338\n"
"s_sleep 1\n"
"v_max_u32 v38, v90, v91\n"
"v_max3_u32 v38, v88, v89, v38\n"
"v_cmp_eq_u32 vcc, -1, v38\n"
"s_cbranch_vccz .Lsk_339\n"
"s_mov_b64 exec, vcc\n"
"buffer_load_dwordx4 v[88:91], v2, s[4:7], s32 offen offset:0 sc1\n"
"s_mov_b64 exec, -1\n"
".Lsk_339:\n"
"v_max_u32 v38, v94, v95\n"
"v_max3_u32 v38, v92, v93, v38\n"
"v_cmp_eq_u32 vcc, -1, v38\n"
"s_cbranch_vccz .Lsk_340\n"
"s_mov_b64 exec, vcc\n"
"buffer_load_dwordx4 v[92:95], v2, s[4:7], s32 offen offset:1024 sc1\n"
"s_mov_b64 exec, -1\n"
".Lsk_340:\n"
"v_max_u32 v38, v98, v99\n"
"v_max3_u32 v38, v96, v97, v38\n"
"v_cmp_eq_u32 vcc, -1, v38\n"
"s_cbranch_vccz .Lsk_341\n"
"s_mov_b64 exec, vcc\n"
"buffer_load_dwordx4 v[96:99], v2, s[4:7], s32 offen offset:2048 sc1\n"
"s_mov_b64 exec, -1\n"
".Lsk_341:\n"
"v_max_u32 v38, v102, v103\n"
"v_max3_u32 v38, v100, v101, v38\n"
"v_cmp_eq_u32 vcc, -1, v38\n"
"s_cbranch_vccz .Lsk_342\n"
"s_mov_b64 exec, vcc\n"
"buffer_load_dwordx4 v[100:103], v2, s[4:7], s32 offen offset:3072 sc1\n"
"s_mov_b64 exec, -1\n"
".Lsk_342:\n"
"v_max_u32 v38, v106, v107\n"
"v_max3_u32 v38, v104, v105, v38\n"
"v_cmp_eq_u32 vcc, -1, v38\n"
"s_cbranch_vccz .Lsk_343\n"
"s_mov_b64 exec, vcc\n"
"buffer_load_dwordx4 v[104:107], v2, s[4:7], s33 offen offset:0 sc1\n"
"s_mov_b64 exec, -1\n"
".Lsk_343:\n"
"v_max_u32 v38, v110, v111\n"
"v_max3_u32 v38, v108, v109, v38\n"
"v_cmp_eq_u32 vcc, -1, v38\n"
"s_cbranch_vccz .Lsk_344\n"
"s_mov_b64 exec, vcc\n"
"buffer_load_dwordx4 v[108:111], v2, s[4:7], s34 offen offset:0 sc1\n"
"s_mov_b64 exec, -1\n"
".Lsk_344:\n"
"v_max_u32 v38, v114, v115\n"
"v_max3_u32 v38, v112, v113, v38\n"
"v_cmp_eq_u32 vcc, -1, v38\n"
"s_cbranch_vccz .Lsk_345\n"
"s_mov_b64 exec, vcc\n"
"buffer_load_dwordx4 v[112:115], v2, s[4:7], s34 offen offset:1024 sc1\n"
"s_mov_b64 exec, -1\n"
".Lsk_345:\n"
"v_max_u32 v38, v118, v119\n"
"v_max3_u32 v38, v116, v117, v38\n"
"v_cmp_eq_u32 vcc, -1, v38\n"
"s_cbranch_vccz .Lsk_346\n"
"s_mov_b64 exec, vcc\n"
"buffer_load_dwordx4 v[116:119], v2, s[4:7], s34 offen offset:2048 sc1\n"
"s_mov_b64 exec, -1\n"
".Lsk_346:\n"
"v_max_u32 v38, v122, v123\n"
"v_max3_u32 v38, v120, v121, v38\n"
"v_cmp_eq_u32 vcc, -1, v38\n"
"s_cbranch_vccz .Lsk_347\n"
"s_mov_b64 exec, vcc\n"
"buffer_load_dwordx4 v[120:123], v2, s[4:7], s34 offen offset:3072 sc1\n"
"s_mov_b64 exec, -1\n"
".Lsk_347:\n"
"v_max_u32 v38, v126, v127\n"
"v_max3_u32 v38, v124, v125, v38\n"
"v_cmp_eq_u32 vcc, -1, v38\n"
"s_cbranch_vccz .Lsk_348\n"
"s_mov_b64 exec, vcc\n"
"buffer_load_dwordx4 v[124:127], v2, s[4:7], s35 offen offset:0 sc1\n"
"s_mov_b64 exec, -1\n"
".Lsk_348:\n"
"s_branch .Ltp_337\n"
".Lou_338:\n"
"s_waitcnt vmcnt(0)\n"
"s_setpc_b64 s[46:47]\n"
".Lsl_367:\n"
"s_nop 7\n"
"s_call_b64 s[46:47], .Loa_336\n"
"s_branch .Lbk_368\n"
".Lsl_369:\n"
"s_nop 7\n"
"s_call_b64 s[46:47], .Loa_336\n"
"s_branch .Lbk_370\n"
".Lsl_371:\n"
"s_nop 7\n"
"s_call_b64 s[46:47], .Loa_336\n"
"s_branch .Lbk_372\n"
".Lsl_373:\n"
"s_nop 7\n"
"s_call_b64 s[46:47], .Loa_336\n"
"s_branch .Lbk_374\n"
".Lsl_375:\n"
"s_nop 7\n"
"s_call_b64 s[46:47], .Loa_336\n"
"s_branch .Lbk_376\n"
".Lsl_377:\n"
"s_nop 7\n"
"s_call_b64 s[46:47], .Loa_336\n"
"s_branch .Lbk_378\n"
".Lsl_379:\n"
"s_nop 7\n"
"s_call_b64 s[46:47], .Loa_336\n"
"s_branch .Lbk_380\n"
".Lsl_381:\n"
"s_nop 7\n"
"s_call_b64 s[46:47], .Loa_336\n"
"s_branch .Lbk_382\n"
".Lsl_383:\n"
"s_nop 7\n"
"s_call_b64 s[46:47], .Loa_336\n"
"s_branch .Lbk_384\n"
".Lsl_385:\n"
"s_nop 7\n"
"s_call_b64 s[46:47], .Loa_336\n"
"s_branch .Lbk_386\n"
".Lsl_410:\n"
"s_nop 7\n"
"s_call_b64 s[46:47], .Loa_336\n"
"s_branch .Lbk_411\n"
".Lsl_412:\n"
"s_nop 7\n"
"s_call_b64 s[46:47], .Loa_336\n"
"s_branch .Lbk_413\n"
".Lsl_414:\n"
"s_nop 7\n"
"s_call_b64 s[46:47], .Loa_336\n"
"s_branch .Lbk_415\n"
".Lsl_416:\n"
"s_nop 7\n"
"s_call_b64 s[46:47], .Loa_336\n"
"s_branch .Lbk_417\n"
".Lsl_418:\n"
"s_nop 7\n"
"s_call_b64 s[46:47], .Loa_336\n"
"s_branch .Lbk_419\n"
".Lsl_420:\n"
"s_nop 7\n"
"s_call_b64 s[46:47], .Loa_336\n"
"s_branch .Lbk_421\n"
".Lsl_422:\n"
"s_nop 7\n"
"s_call_b64 s[46:47], .Loa_336\n"
"s_branch .Lbk_423\n"
".Lsl_424:\n"
"s_nop 7\n"
"s_call_b64 s[46:47], .Loa_336\n"
"s_branch .Lbk_425\n"
".Lsl_426:\n"
"s_nop 7\n"
"s_call_b64 s[46:47], .Loa_336\n"
"s_branch .Lbk_427\n"
".Lsl_428:\n"
"s_nop 7\n"
"s_call_b64 s[46:47], .Loa_336\n"
"s_branch .Lbk_429\n"
".Lsl_510:\n"
"s_nop 7\n"
"s_call_b64 s[46:47], .Loa_336\n"
"s_branch .Lbk_511\n"
".Lsl_512:\n"
"s_nop 7\n"
"s_call_b64 s[46:47], .Loa_336\n"
"s_branch .Lbk_513\n"
".Lsl_514:\n"
"s_nop 7\n"
"s_call_b64 s[46:47], .Loa_336\n"
"s_branch .Lbk_515\n"
".Lsl_516:\n"
"s_nop 7\n"
"s_call_b64 s[46:47], .Loa_336\n"
"s_branch .Lbk_517\n"
".Lsl_518:\n"
"s_nop 7\n"
"s_call_b64 s[46:47], .Loa_336\n"
"s_branch .Lbk_519\n"
".Lsl_520:\n"
"s_nop 7\n"
"s_call_b64 s[46:47], .Loa_336\n"
"s_branch .Lbk_521\n"
".Lsl_522:\n"
"s_nop 7\n"
"s_call_b64 s[46:47], .Loa_336\n"
"s_branch .Lbk_523\n"
".Lsl_524:\n"
"s_nop 7\n"
"s_call_b64 s[46:47], .Loa_336\n"
"s_branch .Lbk_525\n"
".Lsl_526:\n"
"s_nop 7\n"
"s_call_b64 s[46:47], .Loa_336\n"
"s_branch .Lbk_527\n"
".Lsl_528:\n"
"s_nop 7\n"
"s_call_b64 s[46:47], .Loa_336\n"
"s_branch .Lbk_529\n"
".Lsl_556:\n"
"s_nop 7\n"
"s_call_b64 s[46:47], .Loa_336\n"
"s_branch .Lbk_557\n"
".Lsl_558:\n"
"s_nop 7\n"
"s_call_b64 s[46:47], .Loa_336\n"
"s_branch .Lbk_559\n"
".Lsl_560:\n"
"s_nop 7\n"
"s_call_b64 s[46:47], .Loa_336\n"
"s_branch .Lbk_561\n"
".Lsl_562:\n"
"s_nop 7\n"
"s_call_b64 s[46:47], .Loa_336\n"
"s_branch .Lbk_563\n"
".Lsl_564:\n"
"s_nop 7\n"
"s_call_b64 s[46:47], .Loa_336\n"
"s_branch .Lbk_565\n"
".Lsl_566:\n"
"s_nop 7\n"
"s_call_b64 s[46:47], .Loa_336\n"
"s_branch .Lbk_567\n"
".Lsl_568:\n"
"s_nop 7\n"
"s_call_b64 s[46:47], .Loa_336\n"
"s_branch .Lbk_569\n"
".Lsl_570:\n"
"s_nop 7\n"
"s_call_b64 s[46:47], .Loa_336\n"
"s_branch .Lbk_571\n"
".Lsl_572:\n"
"s_nop 7\n"
"s_call_b64 s[46:47], .Loa_336\n"
"s_branch .Lbk_573\n"
".Lsl_574:\n"
"s_nop 7\n"
"s_call_b64 s[46:47], .Loa_336\n"
"s_branch .Lbk_575\n"
".L_yend:\n"
      :: "{s[0:1]}"(__builtin_amdgcn_kernarg_segment_ptr()), "{s2}"((int)__builtin_amdgcn_workgroup_id_x()),
         "{s3}"(__builtin_amdgcn_readfirstlane(tid >> 6)), "{v0}"(tid),
         "{v1}"((unsigned)(size_t)(__attribute__((address_space(3))) unsigned char*)smem)
      : "memory", "vcc", "scc", "m0", "v255", "s95");
}

extern "C" void kernel_launch(void* const* d_in, const int* in_sizes, int n_in,
                              void* d_out, int out_size, void* d_ws, size_t ws_size,
                              hipStream_t stream) {
  mega_kernel<<<256, 512, 0, stream>>>((const u16*)d_in[0], (const u16*)d_in[1], (const u16*)d_in[2],
                                       (const u16*)d_in[4], (const u16*)d_in[5], (const u16*)d_in[6],
                                       (const u16*)d_in[7], (const u16*)d_in[8], (const u16*)d_in[9],
                                       (u16*)d_out, (unsigned char*)d_ws);
}
```

```cpp
#include <hip/hip_runtime.h>
typedef unsigned short u16;

__global__ __launch_bounds__(512, 2) void mega_kernel(
    const u16* __restrict__ x_, const u16* __restrict__ wqkv_, const u16* __restrict__ wo_,
    const u16* __restrict__ log_A_real_, const u16* __restrict__ A_imag_,
    const u16* __restrict__ C_real_, const u16* __restrict__ C_imag_, const u16* __restrict__ log_dt_,
    const u16* __restrict__ D_skip, u16* __restrict__ out, unsigned char* __restrict__ ws_) {
  __shared__ __attribute__((aligned(16))) unsigned char smem[163840];
  const int tid = (int)__builtin_amdgcn_workitem_id_x();
  asm volatile(
"s_cmp_lt_u32 s3, 4\n"
"s_cbranch_scc1 .Lxs_1\n"
"s_load_dwordx8 s[8:15], s[0:1], 0x18\n"
"s_load_dwordx4 s[16:19], s[0:1], 0x38\n"
"s_load_dwordx2 s[22:23], s[0:1], 0x0\n"
"s_load_dwordx4 s[24:27], s[0:1], 0x48\n"
"s_getpc_b64 s[4:5]\n"
".Lmk_a:\n"
"v_lshrrev_b32 v79, 6, v0\n"
"v_and_b32 v78, 7, v0\n"
"v_lshl_add_u32 v79, v79, 3, v78\n"
"s_lshr_b32 s79, s2, 3\n"
"s_lshl_b32 s79, s79, 7\n"
"s_sub_u32 s79, s79, 0x20000\n"
"v_lshl_add_u32 v2, v79, 12, s79\n"
"v_min_u32 v2, .L_yend-.Lmk_a-4, v2\n"
"s_mov_b32 m0, 163328\n"
"v_mov_b32 v4, 0\n"
"global_load_lds_dword v2, s[4:5]\n"
"v_lshlrev_b32 v79, 2, v0\n"
"v_add_u32 v2, 0x26000, v79\n"
"ds_write_b32 v2, v4\n"
"v_lshlrev_b32 v79, 4, v0\n"
"v_add_u32 v2, 0x25840, v79\n"
"v_mov_b32 v8, -1\n"
"v_mov_b32 v9, -1\n"
"v_mov_b32 v10, -1\n"
"v_mov_b32 v11, -1\n"
"ds_write_b128 v2, v[8:11]\n"
"ds_write_b128 v2, v[8:11] offset:4096\n"
"s_and_b32 s79, s2, 7\n"
"s_lshl_b32 s79, s79, 4\n"
"s_lshr_b32 s78, s2, 3\n"
"s_add_u32 s79, s79, s78\n"
"s_and_b32 s78, s2, 3\n"
"s_lshl_b32 s78, s78, 3\n"
"s_and_b32 s77, s2, 4\n"
"s_cmp_eq_i32 s77, 4\n"
"s_cselect_b32 s78, 32, s78\n"
"s_add_u32 s79, s79, s78\n"
"s_lshl_b32 s79, s79, 2\n"
"s_and_b32 s78, s3, 3\n"
"s_add_u32 s6, s79, s78\n"
"v_and_b32 v79, 63, v0\n"
"s_mul_i32 s79, s6, 0x180\n"
"v_lshl_add_u32 v2, v79, 1, s79\n"
"s_lshl_b32 s79, s6, 1\n"
"s_and_b32 s20, s79, 0xffc\n"
"s_and_b32 s79, s2, 4\n"
"s_lshl_b32 s79, s79, 4\n"
"s_add_u32 s7, s2, s79\n"
"s_waitcnt lgkmcnt(0)\n"
"s_cmp_ge_u32 s7, 192\n"
"s_cbranch_scc1 .Lnl_2\n"
"s_load_dword s40, s[16:17], s20\n"
"global_load_ushort v32, v2, s[8:9] offset:0\n"
"global_load_ushort v33, v2, s[10:11] offset:0\n"
"global_load_ushort v34, v2, s[12:13] offset:0\n"
"global_load_ushort v35, v2, s[14:15] offset:0\n"
"global_load_ushort v40, v2, s[8:9] offset:128\n"
"global_load_ushort v41, v2, s[10:11] offset:128\n"
"global_load_ushort v42, v2, s[12:13] offset:128\n"
"global_load_ushort v43, v2, s[14:15] offset:128\n"
"global_load_ushort v44, v2, s[8:9] offset:256\n"
"global_load_ushort v45, v2, s[10:11] offset:256\n"
"global_load_ushort v46, v2, s[12:13] offset:256\n"
"global_load_ushort v47, v2, s[14:15] offset:256\n"
"s_load_dword s41, s[18:19], s20\n"
".Lnl_2:\n"
"s_load_dword s16, s[26:27], 0x0\n"
"s_barrier\n"
"s_branch .L_xend\n"
".Lxs_1:\n"
"s_load_dwordx4 s[20:23], s[0:1], 0x0\n"
"s_load_dwordx2 s[56:57], s[0:1], 0x10\n"
"s_load_dwordx4 s[24:27], s[0:1], 0x48\n"
"v_and_b32 v0, 63, v0\n"
"s_lshl_b32 s79, s2, 2\n"
"s_add_u32 s31, s79, s3\n"
"s_mul_i32 s79, s31, 0xcd\n"
"s_lshr_b32 s46, s79, 11\n"
"s_mul_i32 s79, s46, 10\n"
"s_sub_u32 s47, s31, s79\n"
"s_lshl_b32 s79, s47, 6\n"
"v_add_u32 v68, s79, v0\n"
"v_lshlrev_b32 v80, 1, v68\n"
"s_mul_i32 s48, s46, 0x7800\n"
"s_mov_b32 s14, 0x258000\n"
"s_mov_b32 s15, 0x20000\n"
"s_waitcnt lgkmcnt(0)\n"
"s_mov_b64 s[12:13], s[22:23]\n"
"s_cmp_ge_u32 s2, 200\n"
"s_cbranch_scc1 .Lnorep_3\n"
"buffer_load_ushort v28, v80, s[12:15], s48 offen offset:0\n"
"buffer_load_ushort v36, v80, s[12:15], s48 offen offset:1280\n"
"buffer_load_ushort v44, v80, s[12:15], s48 offen offset:2560\n"
"s_add_u32 s48, s48, 3840\n"
"buffer_load_ushort v29, v80, s[12:15], s48 offen offset:0\n"
"buffer_load_ushort v37, v80, s[12:15], s48 offen offset:1280\n"
"buffer_load_ushort v45, v80, s[12:15], s48 offen offset:2560\n"
"s_add_u32 s48, s48, 3840\n"
"buffer_load_ushort v30, v80, s[12:15], s48 offen offset:0\n"
"buffer_load_ushort v38, v80, s[12:15], s48 offen offset:1280\n"
"buffer_load_ushort v46, v80, s[12:15], s48 offen offset:2560\n"
"s_add_u32 s48, s48, 3840\n"
"buffer_load_ushort v31, v80, s[12:15], s48 offen offset:0\n"
"buffer_load_ushort v39, v80, s[12:15], s48 offen offset:1280\n"
"buffer_load_ushort v47, v80, s[12:15], s48 offen offset:2560\n"
"s_add_u32 s48, s48, 3840\n"
"buffer_load_ushort v32, v80, s[12:15], s48 offen offset:0\n"
"buffer_load_ushort v40, v80, s[12:15], s48 offen offset:1280\n"
"buffer_load_ushort v48, v80, s[12:15], s48 offen offset:2560\n"
"s_add_u32 s48, s48, 3840\n"
"buffer_load_ushort v33, v80, s[12:15], s48 offen offset:0\n"
"buffer_load_ushort v41, v80, s[12:15], s48 offen offset:1280\n"
"buffer_load_ushort v49, v80, s[12:15], s48 offen offset:2560\n"
"s_add_u32 s48, s48, 3840\n"
"buffer_load_ushort v34, v80, s[12:15], s48 offen offset:0\n"
"buffer_load_ushort v42, v80, s[12:15], s48 offen offset:1280\n"
"buffer_load_ushort v50, v80, s[12:15], s48 offen offset:2560\n"
"s_add_u32 s48, s48, 3840\n"
"buffer_load_ushort v35, v80, s[12:15], s48 offen offset:0\n"
"buffer_load_ushort v43, v80, s[12:15], s48 offen offset:1280\n"
"buffer_load_ushort v51, v80, s[12:15], s48 offen offset:2560\n"
"s_barrier\n"
"s_branch .Lrep_4\n"
".Lnorep_3:\n"
"s_barrier\n"
"s_sleep 12\n"
"s_mov_b64 s[12:13], s[56:57]\n"
"s_cmp_lt_i32 s31, 0x3e8\n"
"s_cselect_b32 s14, 0xc8000, 0\n"
"v_lshlrev_b32 v80, 1, v0\n"
"s_lshl_b32 s79, s31, 2\n"
"s_add_u32 s79, s79, 0xfffff380\n"
"s_mul_i32 s79, s79, 0xcd\n"
"s_lshr_b32 s80, s79, 11\n"
"s_lshl_b32 s79, s31, 2\n"
"s_add_u32 s79, s79, 0xfffff380\n"
"s_mul_i32 s78, s80, 10\n"
"s_sub_u32 s84, s79, s78\n"
"s_mul_i32 s79, s80, 0x67\n"
"s_lshr_b32 s79, s79, 9\n"
"s_mul_i32 s79, s79, 20\n"
"s_lshl_b32 s78, s80, 2\n"
"s_add_u32 s79, s79, s78\n"
"s_mul_i32 s79, s79, 0x500\n"
"s_lshl_b32 s78, s84, 7\n"
"s_add_u32 s48, s79, s78\n"
"s_add_u32 s49, s48, 0x6400\n"
"buffer_load_ushort v100, v80, s[12:15], s48 offen offset:0\n"
"buffer_load_ushort v101, v80, s[12:15], s48 offen offset:1280\n"
"buffer_load_ushort v102, v80, s[12:15], s48 offen offset:2560\n"
"buffer_load_ushort v103, v80, s[12:15], s48 offen offset:3840\n"
"buffer_load_ushort v104, v80, s[12:15], s49 offen offset:0\n"
"buffer_load_ushort v105, v80, s[12:15], s49 offen offset:1280\n"
"buffer_load_ushort v106, v80, s[12:15], s49 offen offset:2560\n"
"buffer_load_ushort v107, v80, s[12:15], s49 offen offset:3840\n"
"s_lshl_b32 s79, s31, 2\n"
"s_add_u32 s79, s79, 0xfffff381\n"
"s_mul_i32 s79, s79, 0xcd\n"
"s_lshr_b32 s81, s79, 11\n"
"s_lshl_b32 s79, s31, 2\n"
"s_add_u32 s79, s79, 0xfffff381\n"
"s_mul_i32 s78, s81, 10\n"
"s_sub_u32 s85, s79, s78\n"
"s_mul_i32 s79, s81, 0x67\n"
"s_lshr_b32 s79, s79, 9\n"
"s_mul_i32 s79, s79, 20\n"
"s_lshl_b32 s78, s81, 2\n"
"s_add_u32 s79, s79, s78\n"
"s_mul_i32 s79, s79, 0x500\n"
"s_lshl_b32 s78, s85, 7\n"
"s_add_u32 s48, s79, s78\n"
"s_add_u32 s49, s48, 0x6400\n"
"buffer_load_ushort v108, v80, s[12:15], s48 offen offset:0\n"
"buffer_load_ushort v109, v80, s[12:15], s48 offen offset:1280\n"
"buffer_load_ushort v110, v80, s[12:15], s48 offen offset:2560\n"
"buffer_load_ushort v111, v80, s[12:15], s48 offen offset:3840\n"
"buffer_load_ushort v112, v80, s[12:15], s49 offen offset:0\n"
"buffer_load_ushort v113, v80, s[12:15], s49 offen offset:1280\n"
"buffer_load_ushort v114, v80, s[12:15], s49 offen offset:2560\n"
"buffer_load_ushort v115, v80, s[12:15], s49 offen offset:3840\n"
"s_lshl_b32 s79, s31, 2\n"
"s_add_u32 s79, s79, 0xfffff382\n"
"s_mul_i32 s79, s79, 0xcd\n"
"s_lshr_b32 s82, s79, 11\n"
"s_lshl_b32 s79, s31, 2\n"
"s_add_u32 s79, s79, 0xfffff382\n"
"s_mul_i32 s78, s82, 10\n"
"s_sub_u32 s86, s79, s78\n"
"s_mul_i32 s79, s82, 0x67\n"
"s_lshr_b32 s79, s79, 9\n"
"s_mul_i32 s79, s79, 20\n"
"s_lshl_b32 s78, s82, 2\n"
"s_add_u32 s79, s79, s78\n"
"s_mul_i32 s79, s79, 0x500\n"
"s_lshl_b32 s78, s86, 7\n"
"s_add_u32 s48, s79, s78\n"
"s_add_u32 s49, s48, 0x6400\n"
"buffer_load_ushort v116, v80, s[12:15], s48 offen offset:0\n"
"buffer_load_ushort v117, v80, s[12:15], s48 offen offset:1280\n"
"buffer_load_ushort v118, v80, s[12:15], s48 offen offset:2560\n"
"buffer_load_ushort v119, v80, s[12:15], s48 offen offset:3840\n"
"buffer_load_ushort v120, v80, s[12:15], s49 offen offset:0\n"
"buffer_load_ushort v121, v80, s[12:15], s49 offen offset:1280\n"
"buffer_load_ushort v122, v80, s[12:15], s49 offen offset:2560\n"
"buffer_load_ushort v123, v80, s[12:15], s49 offen offset:3840\n"
"s_lshl_b32 s79, s31, 2\n"
"s_add_u32 s79, s79, 0xfffff383\n"
"s_mul_i32 s79, s79, 0xcd\n"
"s_lshr_b32 s83, s79, 11\n"
"s_lshl_b32 s79, s31, 2\n"
"s_add_u32 s79, s79, 0xfffff383\n"
"s_mul_i32 s78, s83, 10\n"
"s_sub_u32 s87, s79, s78\n"
"s_mul_i32 s79, s83, 0x67\n"
"s_lshr_b32 s79, s79, 9\n"
"s_mul_i32 s79, s79, 20\n"
"s_lshl_b32 s78, s83, 2\n"
"s_add_u32 s79, s79, s78\n"
"s_mul_i32 s79, s79, 0x500\n"
"s_lshl_b32 s78, s87, 7\n"
"s_add_u32 s48, s79, s78\n"
"s_add_u32 s49, s48, 0x6400\n"
"buffer_load_ushort v124, v80, s[12:15], s48 offen offset:0\n"
"buffer_load_ushort v125, v80, s[12:15], s48 offen offset:1280\n"
"buffer_load_ushort v126, v80, s[12:15], s48 offen offset:2560\n"
"buffer_load_ushort v127, v80, s[12:15], s48 offen offset:3840\n"
"buffer_load_ushort v128, v80, s[12:15], s49 offen offset:0\n"
"buffer_load_ushort v129, v80, s[12:15], s49 offen offset:1280\n"
"buffer_load_ushort v130, v80, s[12:15], s49 offen offset:2560\n"
"buffer_load_ushort v131, v80, s[12:15], s49 offen offset:3840\n"
".Lrep_4:\n"
"s_mov_b32 s4, s26\n"
"s_mov_b32 s5, s27\n"
"s_mov_b32 s6, 0x1688000\n"
"s_mov_b32 s7, 0x20000\n"
"s_load_dword s16, s[26:27], 0x0\n"
"v_lshlrev_b32 v1, 4, v0\n"
"s_and_b32 s79, s2, 7\n"
"s_lshl_b32 s79, s79, 4\n"
"s_lshr_b32 s78, s2, 3\n"
"s_add_u32 s79, s79, s78\n"
"s_and_b32 s78, s2, 3\n"
"s_lshl_b32 s78, s78, 3\n"
"s_and_b32 s77, s2, 4\n"
"s_cmp_eq_i32 s77, 4\n"
"s_cselect_b32 s78, 32, s78\n"
"s_add_u32 s79, s79, s78\n"
"s_and_b32 s78, s2, 4\n"
"s_lshl_b32 s78, s78, 4\n"
"s_add_u32 s78, s2, s78\n"
"s_cmp_lt_i32 s78, 0xc0\n"
"s_cselect_b32 s45, s79, -1\n"
"s_mov_b32 s8, s20\n"
"s_mov_b32 s9, s21\n"
"s_mov_b32 s10, 0x280000\n"
"s_mov_b32 s11, 0x20000\n"
"s_mov_b32 s12, s24\n"
"s_mov_b32 s13, s25\n"
"s_mov_b32 s14, 0x280000\n"
"s_mov_b32 s15, 0x20000\n"
"s_and_b32 s79, s2, 1\n"
"s_lshl_b32 s79, s79, 3\n"
"s_lshr_b32 s78, s2, 3\n"
"s_and_b32 s78, s78, 7\n"
"s_add_u32 s18, s79, s78\n"
"s_lshr_b32 s79, s2, 1\n"
"s_and_b32 s79, s79, 3\n"
"s_lshl_b32 s79, s79, 2\n"
"s_lshr_b32 s78, s2, 3\n"
"s_lshr_b32 s78, s78, 3\n"
"s_add_u32 s19, s79, s78\n"
"s_mul_i32 s79, s19, 0\n"
"s_add_u32 s79, s3, s79\n"
"s_and_b32 s43, s79, 3\n"
"v_mul_i32_i24 v79, 0xcd, v68\n"
"v_lshrrev_b32 v84, 13, v79\n"
"v_mul_i32_i24 v79, 40, v84\n"
"v_sub_u32 v85, v68, v79\n"
"v_mul_i32_i24 v79, 0xa0, v84\n"
"v_lshlrev_b32 v84, 10, v79\n"
"v_subrev_u32 v79, 24, v85\n"
"v_and_b32 v78, 15, v85\n"
"v_cmp_le_i32 vcc, 32, v85\n"
"s_nop 1\n"
"v_cndmask_b32 v79, v78, v79, vcc\n"
"v_lshrrev_b32 v78, 4, v85\n"
"s_mov_b32 s79, 0xa000\n"
"v_mad_i32_i24 v78, v78, s79, v84\n"
"v_lshl_add_u32 v64, v79, 4, v78\n"
"v_mov_b32 v86, v85\n"
"v_and_b32 v79, 15, v86\n"
"v_lshrrev_b32 v78, 4, v86\n"
"v_min_i32 v78, 3, v78\n"
"s_mov_b32 s79, 0xa000\n"
"v_mad_i32_i24 v78, v78, s79, v84\n"
"v_mov_b32 v77, 0\n"
"v_mov_b32 v76, 0x400\n"
"v_lshrrev_b32 v75, 4, v86\n"
"v_cmp_eq_i32 vcc, 3, v75\n"
"s_nop 1\n"
"v_cndmask_b32 v77, v76, v77, vcc\n"
"v_add_u32 v78, v78, v77\n"
"v_lshl_add_u32 v65, v79, 4, v78\n"
"v_add_u32 v86, 40, v85\n"
"v_and_b32 v79, 15, v86\n"
"v_lshrrev_b32 v78, 4, v86\n"
"v_min_i32 v78, 3, v78\n"
"s_mov_b32 s79, 0xa000\n"
"v_mad_i32_i24 v78, v78, s79, v84\n"
"v_mov_b32 v77, 0\n"
"v_mov_b32 v76, 0x400\n"
"v_lshrrev_b32 v75, 4, v86\n"
"v_cmp_eq_i32 vcc, 3, v75\n"
"s_nop 1\n"
"v_cndmask_b32 v77, v76, v77, vcc\n"
"v_add_u32 v78, v78, v77\n"
"v_lshl_add_u32 v66, v79, 4, v78\n"
"v_subrev_u32 v79, 0x80, v64\n"
"v_mov_b32 v78, 0x40000000\n"
"v_cmp_le_i32 vcc, 32, v85\n"
"s_nop 1\n"
"v_cndmask_b32 v67, v78, v79, vcc\n"
"s_lshr_b32 s79, s46, 2\n"
"s_lshl_b32 s79, s79, 11\n"
"s_and_b32 s78, s46, 3\n"
"s_lshl_b32 s78, s78, 8\n"
"s_add_u32 s79, s79, s78\n"
"s_add_u32 s48, s79, 0x100000\n"
"s_lshl_b32 s79, s47, 10\n"
"s_lshr_b32 s78, s46, 2\n"
"s_lshl_b32 s78, s78, 5\n"
"s_add_u32 s79, s79, s78\n"
"s_and_b32 s78, s46, 3\n"
"s_lshl_b32 s78, s78, 2\n"
"s_add_u32 s79, s79, s78\n"
"s_lshl_b32 s78, s2, 5\n"
"s_lshl_b32 s77, s3, 2\n"
"s_add_u32 s78, s78, s77\n"
"s_add_u32 s78, s78, 0xf00\n"
"s_cmp_lt_i32 s31, 0x320\n"
"s_cselect_b32 s79, s79, s78\n"
"s_add_u32 s32, s79, 0x1000\n"
"v_mov_b32 v20, 0x3f803f80\n"
"v_mov_b32 v21, 0x3f803f80\n"
"v_mov_b32 v22, 0x3f803f80\n"
"v_mov_b32 v23, 0x3f803f80\n"
"s_mov_b32 s79, 0x55\n"
"s_cmp_lt_i32 s47, 5\n"
"s_cselect_b32 s79, s79, 0xaa\n"
"s_cmp_lt_i32 s31, 0x320\n"
"s_cselect_b32 s58, s79, 0xff\n"
"s_mov_b32 s59, 0\n"
"s_mul_i32 s79, s19, 0x14000\n"
"s_mul_i32 s78, s43, 0x140\n"
"s_add_u32 s28, s79, s78\n"
"s_mul_i32 s29, s43, 0x1400\n"
"s_lshr_b32 s79, s2, 3\n"
"s_and_b32 s31, s79, 7\n"
"s_mul_i32 s33, s43, 0x140\n"
"s_sub_u32 s79, s31, 5\n"
"s_cmp_ge_i32 s31, 5\n"
"s_cselect_b32 s31, s79, s31\n"
"s_mul_i32 s79, s31, 0x5000\n"
"s_add_u32 s79, s79, s28\n"
"s_sub_u32 s79, s79, 0x5000\n"
"s_max_i32 s60, s79, s33\n"
"s_mul_i32 s79, s31, 0x5000\n"
"s_mov_b32 s78, 0x12400\n"
"s_cmp_ge_i32 s31, 2\n"
"s_cselect_b32 s78, s78, 0xa000\n"
"s_add_u32 s79, s79, s78\n"
"s_cmp_eq_i32 s31, 4\n"
"s_cselect_b32 s79, 0, s79\n"
"s_add_u32 s61, s29, s79\n"
"s_sub_u32 s79, s31, 4\n"
"s_add_u32 s78, s31, 1\n"
"s_cmp_ge_i32 s31, 4\n"
"s_cselect_b32 s31, s79, s78\n"
"s_mul_i32 s79, s31, 0x5000\n"
"s_add_u32 s79, s79, s28\n"
"s_sub_u32 s79, s79, 0x5000\n"
"s_max_i32 s62, s79, s33\n"
"s_mul_i32 s79, s31, 0x5000\n"
"s_mov_b32 s78, 0x12400\n"
"s_cmp_ge_i32 s31, 2\n"
"s_cselect_b32 s78, s78, 0xa000\n"
"s_add_u32 s79, s79, s78\n"
"s_cmp_eq_i32 s31, 4\n"
"s_cselect_b32 s79, 0, s79\n"
"s_add_u32 s63, s29, s79\n"
"s_sub_u32 s79, s31, 4\n"
"s_add_u32 s78, s31, 1\n"
"s_cmp_ge_i32 s31, 4\n"
"s_cselect_b32 s31, s79, s78\n"
"s_mul_i32 s79, s31, 0x5000\n"
"s_add_u32 s79, s79, s28\n"
"s_sub_u32 s79, s79, 0x5000\n"
"s_max_i32 s64, s79, s33\n"
"s_mul_i32 s79, s31, 0x5000\n"
"s_mov_b32 s78, 0x12400\n"
"s_cmp_ge_i32 s31, 2\n"
"s_cselect_b32 s78, s78, 0xa000\n"
"s_add_u32 s79, s79, s78\n"
"s_cmp_eq_i32 s31, 4\n"
"s_cselect_b32 s79, 0, s79\n"
"s_add_u32 s65, s29, s79\n"
"s_sub_u32 s79, s31, 4\n"
"s_add_u32 s78, s31, 1\n"
"s_cmp_ge_i32 s31, 4\n"
"s_cselect_b32 s31, s79, s78\n"
"s_mul_i32 s79, s31, 0x5000\n"
"s_add_u32 s79, s79, s28\n"
"s_sub_u32 s79, s79, 0x5000\n"
"s_max_i32 s66, s79, s33\n"
"s_mul_i32 s79, s31, 0x5000\n"
"s_mov_b32 s78, 0x12400\n"
"s_cmp_ge_i32 s31, 2\n"
"s_cselect_b32 s78, s78, 0xa000\n"
"s_add_u32 s79, s79, s78\n"
"s_cmp_eq_i32 s31, 4\n"
"s_cselect_b32 s79, 0, s79\n"
"s_add_u32 s67, s29, s79\n"
"s_sub_u32 s79, s31, 4\n"
"s_add_u32 s78, s31, 1\n"
"s_cmp_ge_i32 s31, 4\n"
"s_cselect_b32 s31, s79, s78\n"
"s_mul_i32 s79, s31, 0x5000\n"
"s_add_u32 s79, s79, s28\n"
"s_sub_u32 s79, s79, 0x5000\n"
"s_max_i32 s68, s79, s33\n"
"s_mul_i32 s79, s31, 0x5000\n"
"s_mov_b32 s78, 0x12400\n"
"s_cmp_ge_i32 s31, 2\n"
"s_cselect_b32 s78, s78, 0xa000\n"
"s_add_u32 s79, s79, s78\n"
"s_cmp_eq_i32 s31, 4\n"
"s_cselect_b32 s79, 0, s79\n"
"s_add_u32 s69, s29, s79\n"
"v_and_b32 v79, 3, v0\n"
"v_lshrrev_b32 v78, 5, v0\n"
"v_lshlrev_b32 v78, 1, v78\n"
"v_xor_b32 v79, v79, v78\n"
"v_lshrrev_b32 v78, 2, v0\n"
"v_mul_i32_i24 v78, 0x500, v78\n"
"v_lshl_add_u32 v2, v79, 4, v78\n"
"v_min_i32 v79, 24, v0\n"
"s_lshr_b32 s79, s2, 3\n"
"s_and_b32 s79, s79, 7\n"
"s_lshl_b32 s79, s79, 2\n"
"s_add_u32 s79, s79, s3\n"
"s_mul_i32 s79, s79, 0xc80\n"
"s_lshl_b32 s78, s19, 6\n"
"s_add_u32 s78, s78, 0x3f0\n"
"s_mul_i32 s78, s78, 0x500\n"
"s_add_u32 s79, s79, s78\n"
"v_lshl_add_u32 v89, v79, 7, s79\n"
"s_mov_b32 s31, 0x05040100\n"
"s_cmp_ge_u32 s2, 200\n"
"s_cbranch_scc1 .Lnorep_5\n"
"buffer_store_dwordx4 v[20:23], v67, s[4:7], s48 offen sc1\n"
"s_waitcnt vmcnt(1)\n"
"v_perm_b32 v52, v29, v28, s31\n"
"v_perm_b32 v53, v31, v30, s31\n"
"v_perm_b32 v54, v33, v32, s31\n"
"v_perm_b32 v55, v35, v34, s31\n"
"buffer_store_dwordx4 v[52:55], v64, s[4:7], s48 offen sc1\n"
"v_perm_b32 v56, v37, v36, s31\n"
"v_perm_b32 v57, v39, v38, s31\n"
"v_perm_b32 v58, v41, v40, s31\n"
"v_perm_b32 v59, v43, v42, s31\n"
"buffer_store_dwordx4 v[56:59], v65, s[4:7], s48 offen sc1\n"
"v_perm_b32 v60, v45, v44, s31\n"
"v_perm_b32 v61, v47, v46, s31\n"
"v_perm_b32 v62, v49, v48, s31\n"
"v_perm_b32 v63, v51, v50, s31\n"
"buffer_store_dwordx4 v[60:63], v66, s[4:7], s48 offen sc1\n"
"s_branch .Lrep_6\n"
".Lnorep_5:\n"
"s_lshl_b32 s79, s84, 6\n"
"v_add_u32 v68, s79, v0\n"
"v_mul_i32_i24 v79, 0xcd, v68\n"
"v_lshrrev_b32 v81, 14, v79\n"
"s_lshr_b32 s79, s80, 2\n"
"s_mul_i32 s79, s79, 13\n"
"s_lshr_b32 s79, s79, 6\n"
"s_and_b32 s79, s79, 1\n"
"s_lshl_b32 s79, s79, 1\n"
"v_mul_i32_i24 v79, 0x50, v81\n"
"v_sub_u32 v79, v68, v79\n"
"v_lshrrev_b32 v79, 4, v79\n"
"v_add_u32 v82, s79, v79\n"
"v_subrev_u32 v79, 5, v82\n"
"v_cmp_le_i32 vcc, 5, v82\n"
"s_nop 1\n"
"v_cndmask_b32 v79, v82, v79, vcc\n"
"s_mov_b32 s79, 0x64\n"
"v_mad_i32_i24 v79, v81, s79, v79\n"
"v_and_b32 v78, 15, v68\n"
"v_lshlrev_b32 v78, 4, v78\n"
"v_lshl_add_u32 v64, v79, 10, v78\n"
"s_lshr_b32 s79, s80, 2\n"
"s_mul_i32 s79, s79, 0x1400\n"
"s_and_b32 s78, s80, 3\n"
"s_lshl_b32 s78, s78, 8\n"
"s_add_u32 s79, s79, s78\n"
"s_add_u32 s79, s79, 0x380000\n"
"s_cmp_lt_i32 s80, 0x50\n"
"s_cselect_b32 s80, s79, 0x40000000\n"
"s_lshl_b32 s79, s85, 6\n"
"v_add_u32 v68, s79, v0\n"
"v_mul_i32_i24 v79, 0xcd, v68\n"
"v_lshrrev_b32 v81, 14, v79\n"
"s_lshr_b32 s79, s81, 2\n"
"s_mul_i32 s79, s79, 13\n"
"s_lshr_b32 s79, s79, 6\n"
"s_and_b32 s79, s79, 1\n"
"s_lshl_b32 s79, s79, 1\n"
"v_mul_i32_i24 v79, 0x50, v81\n"
"v_sub_u32 v79, v68, v79\n"
"v_lshrrev_b32 v79, 4, v79\n"
"v_add_u32 v82, s79, v79\n"
"v_subrev_u32 v79, 5, v82\n"
"v_cmp_le_i32 vcc, 5, v82\n"
"s_nop 1\n"
"v_cndmask_b32 v79, v82, v79, vcc\n"
"s_mov_b32 s79, 0x64\n"
"v_mad_i32_i24 v79, v81, s79, v79\n"
"v_and_b32 v78, 15, v68\n"
"v_lshlrev_b32 v78, 4, v78\n"
"v_lshl_add_u32 v65, v79, 10, v78\n"
"s_lshr_b32 s79, s81, 2\n"
"s_mul_i32 s79, s79, 0x1400\n"
"s_and_b32 s78, s81, 3\n"
"s_lshl_b32 s78, s78, 8\n"
"s_add_u32 s79, s79, s78\n"
"s_add_u32 s79, s79, 0x380000\n"
"s_cmp_lt_i32 s81, 0x50\n"
"s_cselect_b32 s81, s79, 0x40000000\n"
"s_lshl_b32 s79, s86, 6\n"
"v_add_u32 v68, s79, v0\n"
"v_mul_i32_i24 v79, 0xcd, v68\n"
"v_lshrrev_b32 v81, 14, v79\n"
"s_lshr_b32 s79, s82, 2\n"
"s_mul_i32 s79, s79, 13\n"
"s_lshr_b32 s79, s79, 6\n"
"s_and_b32 s79, s79, 1\n"
"s_lshl_b32 s79, s79, 1\n"
"v_mul_i32_i24 v79, 0x50, v81\n"
"v_sub_u32 v79, v68, v79\n"
"v_lshrrev_b32 v79, 4, v79\n"
"v_add_u32 v82, s79, v79\n"
"v_subrev_u32 v79, 5, v82\n"
"v_cmp_le_i32 vcc, 5, v82\n"
"s_nop 1\n"
"v_cndmask_b32 v79, v82, v79, vcc\n"
"s_mov_b32 s79, 0x64\n"
"v_mad_i32_i24 v79, v81, s79, v79\n"
"v_and_b32 v78, 15, v68\n"
"v_lshlrev_b32 v78, 4, v78\n"
"v_lshl_add_u32 v66, v79, 10, v78\n"
"s_lshr_b32 s79, s82, 2\n"
"s_mul_i32 s79, s79, 0x1400\n"
"s_and_b32 s78, s82, 3\n"
"s_lshl_b32 s78, s78, 8\n"
"s_add_u32 s79, s79, s78\n"
"s_add_u32 s79, s79, 0x380000\n"
"s_cmp_lt_i32 s82, 0x50\n"
"s_cselect_b32 s82, s79, 0x40000000\n"
"s_lshl_b32 s79, s87, 6\n"
"v_add_u32 v68, s79, v0\n"
"v_mul_i32_i24 v79, 0xcd, v68\n"
"v_lshrrev_b32 v81, 14, v79\n"
"s_lshr_b32 s79, s83, 2\n"
"s_mul_i32 s79, s79, 13\n"
"s_lshr_b32 s79, s79, 6\n"
"s_and_b32 s79, s79, 1\n"
"s_lshl_b32 s79, s79, 1\n"
"v_mul_i32_i24 v79, 0x50, v81\n"
"v_sub_u32 v79, v68, v79\n"
"v_lshrrev_b32 v79, 4, v79\n"
"v_add_u32 v82, s79, v79\n"
"v_subrev_u32 v79, 5, v82\n"
"v_cmp_le_i32 vcc, 5, v82\n"
"s_nop 1\n"
"v_cndmask_b32 v79, v82, v79, vcc\n"
"s_mov_b32 s79, 0x64\n"
"v_mad_i32_i24 v79, v81, s79, v79\n"
"v_and_b32 v78, 15, v68\n"
"v_lshlrev_b32 v78, 4, v78\n"
"v_lshl_add_u32 v67, v79, 10, v78\n"
"s_lshr_b32 s79, s83, 2\n"
"s_mul_i32 s79, s79, 0x1400\n"
"s_and_b32 s78, s83, 3\n"
"s_lshl_b32 s78, s78, 8\n"
"s_add_u32 s79, s79, s78\n"
"s_add_u32 s79, s79, 0x380000\n"
"s_cmp_lt_i32 s83, 0x50\n"
"s_cselect_b32 s83, s79, 0x40000000\n"
"s_waitcnt vmcnt(0)\n"
"v_perm_b32 v28, v101, v100, s31\n"
"v_perm_b32 v29, v103, v102, s31\n"
"v_perm_b32 v30, v105, v104, s31\n"
"v_perm_b32 v31, v107, v106, s31\n"
"buffer_store_dwordx4 v[28:31], v64, s[4:7], s80 offen sc1\n"
"v_perm_b32 v32, v109, v108, s31\n"
"v_perm_b32 v33, v111, v110, s31\n"
"v_perm_b32 v34, v113, v112, s31\n"
"v_perm_b32 v35, v115, v114, s31\n"
"buffer_store_dwordx4 v[32:35], v65, s[4:7], s81 offen sc1\n"
"v_perm_b32 v36, v117, v116, s31\n"
"v_perm_b32 v37, v119, v118, s31\n"
"v_perm_b32 v38, v121, v120, s31\n"
"v_perm_b32 v39, v123, v122, s31\n"
"buffer_store_dwordx4 v[36:39], v66, s[4:7], s82 offen sc1\n"
"v_perm_b32 v40, v125, v124, s31\n"
"v_perm_b32 v41, v127, v126, s31\n"
"v_perm_b32 v42, v129, v128, s31\n"
"v_perm_b32 v43, v131, v130, s31\n"
"buffer_store_dwordx4 v[40:43], v67, s[4:7], s83 offen sc1\n"
".Lrep_6:\n"
"v_lshlrev_b32 v80, 14, v0\n"
"s_mov_b32 s34, 0xaaaaaaaa\n"
"s_mov_b32 s35, 0xaaaaaaaa\n"
"s_mov_b32 s40, 0xffff\n"
"s_mov_b32 s41, 0\n"
"s_mov_b32 s36, 0xfff0ffff\n"
"s_mov_b32 s37, 0xf000ff00\n"
"s_mov_b32 s38, 0xffc0fffc\n"
"s_mov_b32 s39, 0xc000fc00\n"
"s_lshl_b32 s79, s19, 2\n"
"s_add_u32 s78, s3, 1\n"
"s_and_b32 s78, s78, 3\n"
"s_add_u32 s79, s79, s78\n"
"s_mul_i32 s79, s79, 0x5000\n"
"s_mul_i32 s78, s18, 0x500\n"
"s_add_u32 s79, s79, s78\n"
"s_add_u32 s30, s79, 0x448000\n"
"s_mul_i32 s79, s18, 40\n"
"s_lshr_b32 s79, s79, 6\n"
"s_lshl_b32 s79, s79, 10\n"
"v_lshl_add_u32 v79, v0, 5, s79\n"
"v_min_i32 v78, 39, v0\n"
"v_subrev_u32 v78, 20, v78\n"
"s_mul_i32 s79, s18, 40\n"
"s_add_u32 s79, s79, 39\n"
"s_lshr_b32 s79, s79, 6\n"
"s_lshl_b32 s79, s79, 10\n"
"v_lshl_add_u32 v78, v78, 5, s79\n"
"v_cmp_gt_i32 vcc, 20, v0\n"
"s_nop 1\n"
"v_cndmask_b32 v19, v78, v79, vcc\n"
"s_and_b32 s79, s2, 7\n"
"s_lshl_b32 s79, s79, 14\n"
"s_add_u32 s50, s79, 0x1000\n"
"s_mul_i32 s79, s19, 0x14000\n"
"s_mul_i32 s78, s43, 0x1400\n"
"s_add_u32 s79, s79, s78\n"
"s_add_u32 s44, s79, 0xc28000\n"
"s_add_u32 s79, s3, 1\n"
"s_and_b32 s79, s79, 3\n"
"s_add_u32 s79, s19, s79\n"
"s_cmp_eq_i32 s79, 0\n"
"s_cselect_b32 s42, 0xfe967699, 0\n"
"s_lshl_b32 s79, s18, 2\n"
"s_add_u32 s79, s79, s3\n"
"s_mul_i32 s79, s79, 0xa000\n"
"s_add_u32 s31, s79, 0x100000\n"
"s_lshl_b32 s79, s43, 11\n"
"s_add_u32 s52, s31, s79\n"
"s_mov_b32 s79, 0xffffe800\n"
"s_cmp_ge_i32 s43, 3\n"
"s_cselect_b32 s79, s79, 0x8800\n"
"s_add_u32 s53, s52, s79\n"
"s_mov_b32 s79, 0xfffff000\n"
"s_cmp_ge_i32 s43, 2\n"
"s_cselect_b32 s79, s79, 0x9000\n"
"s_add_u32 s54, s52, s79\n"
"s_mov_b32 s79, 0xfffff800\n"
"s_cmp_ge_i32 s43, 1\n"
"s_cselect_b32 s79, s79, 0x9800\n"
"s_add_u32 s55, s52, s79\n"
"s_mov_b32 s20, 0xcccccccc\n"
"s_mov_b32 s21, 0xcccccccc\n"
"s_mov_b32 s22, 0xf0000f\n"
"s_mov_b32 s23, 0xf0000f00\n"
"v_and_b32 v161, 15, v0\n"
"v_and_b32 v162, 3, v0\n"
"s_mul_i32 s70, s43, 0x1400\n"
"s_lshl_b32 s71, s3, 2\n"
"s_lshl_b32 s72, s3, 5\n"
"s_lshl_b32 s73, s43, 10\n"
"v_lshrrev_b32 v163, 4, v0\n"
"v_and_b32 v164, 3, v161\n"
"v_mul_i32_i24 v165, 0xf8, v161\n"
"v_lshlrev_b32 v166, 4, v161\n"
"v_lshlrev_b32 v167, 3, v163\n"
"v_lshlrev_b32 v79, 2, v161\n"
"v_add_u32 v12, 0x26940, v79\n"
"s_add_u32 s79, s3, 1\n"
"s_and_b32 s79, s79, 3\n"
"s_lshl_b32 s74, s79, 4\n"
"s_add_u32 s79, s71, 0x26840\n"
"v_add_u32 v168, s79, v166\n"
"s_lshl_b32 s79, s3, 11\n"
"s_add_u32 s79, s79, 0x5000\n"
"v_add_u32 v79, s79, v166\n"
"s_mov_b32 vcc_lo, 0xf0000f\n"
"s_mov_b32 vcc_hi, 0xf0000f00\n"
"v_cndmask_b32 v92, 2.0, v79, vcc\n"
"v_add_u32 v79, s74, v161\n"
"v_mul_i32_i24 v79, 0xf8, v79\n"
"v_add_u32 v24, 0x5000, v79\n"
"v_lshrrev_b32 v79, 3, v161\n"
"v_lshlrev_b32 v79, 1, v79\n"
"v_xor_b32 v79, v163, v79\n"
"v_lshlrev_b32 v79, 4, v79\n"
"v_lshl_add_u32 v169, v161, 6, v79\n"
"v_lshl_add_u32 v79, v164, 2, v166\n"
"v_lshl_add_u32 v79, v163, 8, v79\n"
"v_add_u32 v17, 0x5000, v79\n"
"v_add_u32 v79, 48, v167\n"
"v_mov_b32 v78, 0xf0\n"
"s_mov_b32 vcc_lo, 0\n"
"s_mov_b32 vcc_hi, -1\n"
"v_cndmask_b32 v79, v78, v79, vcc\n"
"s_cmp_eq_i32 s3, 3\n"
"s_cselect_b32 s79, 0xb0, s72\n"
"v_add_u32 v78, s79, v167\n"
"v_mov_b32 v77, s3\n"
"v_cmp_eq_i32 vcc, 2, v77\n"
"s_nop 1\n"
"v_cndmask_b32 v79, v78, v79, vcc\n"
"s_mov_b32 s79, 0x5000\n"
"v_add3_u32 v7, v165, v79, s79\n"
"s_add_u32 s79, s72, 0x50\n"
"s_cmp_eq_i32 s3, 3\n"
"s_cselect_b32 s79, 0xd0, s79\n"
"s_add_u32 s79, s79, 0x5000\n"
"v_add3_u32 v8, v165, v167, s79\n"
"v_mov_b32 v79, 0x40000000\n"
"s_xor_b32 s79, s3, 2\n"
"s_lshl_b32 s79, s79, 2\n"
"v_add_u32 v78, s79, v163\n"
"v_mov_b32 v77, 1\n"
"s_mov_b32 vcc_lo, 0xf000f\n"
"s_mov_b32 vcc_hi, 0xf000f\n"
"v_cndmask_b32 v77, 0, v77, vcc\n"
"v_cmp_eq_i32 vcc, v78, v77\n"
"s_nop 1\n"
"v_cndmask_b32 v79, v79, v12, vcc\n"
"s_mov_b32 vcc_lo, 0xf0000f\n"
"s_mov_b32 vcc_hi, 0xf0000f00\n"
"v_cndmask_b32 v9, v79, v168, vcc\n"
"v_add_u32 v11, 0x26840, v166\n"
"s_mov_b32 vcc_lo, 0xffff\n"
"s_mov_b32 vcc_hi, 0\n"
"v_cndmask_b32 v10, 2.0, v168, vcc\n"
"s_add_u32 s79, s71, 0x26400\n"
"v_mov_b32 v79, s79\n"
"s_mov_b32 vcc_lo, 1\n"
"s_mov_b32 vcc_hi, 0\n"
"v_cndmask_b32 v13, 2.0, v79, vcc\n"
"v_lshlrev_b32 v79, 2, v162\n"
"v_add_u32 v14, 0x26400, v79\n"
"v_add_u32 v25, v24, v167\n"
"v_lshl_add_u32 v79, v163, 2, s74\n"
"v_lshrrev_b32 v78, 2, v161\n"
"v_add_u32 v79, v79, v78\n"
"v_mul_i32_i24 v79, 0xf8, v79\n"
"v_lshl_add_u32 v79, v164, 3, v79\n"
"v_add_u32 v26, 0x50a0, v79\n"
"v_mov_b32 v27, 0x3727c5ac\n"
"v_xor_b32 v18, 4, v17\n"
"v_xor_b32 v79, 1, v164\n"
"v_lshl_add_u32 v91, v79, 2, v92\n"
"v_lshrrev_b32 v79, 2, v0\n"
"v_lshlrev_b32 v78, 4, v162\n"
"v_lshl_add_u32 v79, v79, 10, v78\n"
"v_add_u32 v79, s70, v79\n"
"s_mov_b32 vcc_lo, 0xfffff\n"
"s_mov_b32 vcc_hi, 0\n"
"v_cndmask_b32 v93, 2.0, v79, vcc\n"
"v_lshlrev_b32 v79, 3, v161\n"
"v_add_u32 v94, 0x9d80, v79\n"
"v_lshlrev_b32 v79, 3, v0\n"
"v_add_u32 v16, 0x9d80, v79\n"
"v_add_u32 v88, s70, v169\n"
"v_add_u32 v3, s73, v169\n"
"s_add_u32 s79, s73, 0xfffff400\n"
"s_add_u32 s78, s73, 0x4400\n"
"s_cmp_ge_i32 s43, 3\n"
"s_cselect_b32 s79, s79, s78\n"
"v_add_u32 v4, s79, v169\n"
"s_add_u32 s79, s73, 0xfffff800\n"
"s_add_u32 s78, s73, 0x4800\n"
"s_cmp_ge_i32 s43, 2\n"
"s_cselect_b32 s79, s79, s78\n"
"v_add_u32 v5, s79, v169\n"
"s_add_u32 s79, s73, 0xfffffc00\n"
"s_add_u32 s78, s73, 0x4c00\n"
"s_cmp_ge_i32 s43, 1\n"
"s_cselect_b32 s79, s79, s78\n"
"v_add_u32 v6, s79, v169\n"
"s_waitcnt lgkmcnt(0)\n"
"s_mul_i32 s16, s16, 0x2c9277b5\n"
"s_add_u32 s16, s16, 0xac564b05\n"
"v_mov_b32 v82, s16\n"
"s_waitcnt vmcnt(0)\n"
"s_mov_b64 exec, s[58:59]\n"
"buffer_store_dword v82, v80, s[4:7], s32 offen sc1\n"
"s_mov_b64 exec, -1\n"
"s_setprio 1\n"
"s_add_u32 m0, s61, 0\n"
"s_nop 0\n"
"buffer_load_dwordx4 v2, s[8:11], s60 offen offset:0 lds\n"
"s_add_u32 m0, s61, 0x3c0\n"
"s_nop 0\n"
"buffer_load_dwordx4 v2, s[8:11], s60 offen offset:64 lds\n"
"s_add_u32 m0, s61, 0x780\n"
"s_nop 0\n"
"buffer_load_dwordx4 v2, s[8:11], s60 offen offset:128 lds\n"
"s_add_u32 m0, s61, 0xb40\n"
"s_nop 0\n"
"buffer_load_dwordx4 v2, s[8:11], s60 offen offset:192 lds\n"
"s_add_u32 m0, s61, 0xf00\n"
"s_nop 0\n"
"buffer_load_dwordx4 v2, s[8:11], s60 offen offset:256 lds\n"
"s_add_u32 m0, s63, 0\n"
"s_nop 0\n"
"buffer_load_dwordx4 v2, s[8:11], s62 offen offset:0 lds\n"
"s_add_u32 m0, s63, 0x3c0\n"
"s_nop 0\n"
"buffer_load_dwordx4 v2, s[8:11], s62 offen offset:64 lds\n"
"s_add_u32 m0, s63, 0x780\n"
"s_nop 0\n"
"buffer_load_dwordx4 v2, s[8:11], s62 offen offset:128 lds\n"
"s_add_u32 m0, s63, 0xb40\n"
"s_nop 0\n"
"buffer_load_dwordx4 v2, s[8:11], s62 offen offset:192 lds\n"
"s_add_u32 m0, s63, 0xf00\n"
"s_nop 0\n"
"buffer_load_dwordx4 v2, s[8:11], s62 offen offset:256 lds\n"
"s_add_u32 m0, s65, 0\n"
"s_nop 0\n"
"buffer_load_dwordx4 v2, s[8:11], s64 offen offset:0 lds\n"
"s_add_u32 m0, s65, 0x3c0\n"
"s_nop 0\n"
"buffer_load_dwordx4 v2, s[8:11], s64 offen offset:64 lds\n"
"s_add_u32 m0, s65, 0x780\n"
"s_nop 0\n"
"buffer_load_dwordx4 v2, s[8:11], s64 offen offset:128 lds\n"
"s_add_u32 m0, s65, 0xb40\n"
"s_nop 0\n"
"buffer_load_dwordx4 v2, s[8:11], s64 offen offset:192 lds\n"
"s_add_u32 m0, s65, 0xf00\n"
"s_nop 0\n"
"buffer_load_dwordx4 v2, s[8:11], s64 offen offset:256 lds\n"
"s_add_u32 m0, s67, 0\n"
"s_nop 0\n"
"buffer_load_dwordx4 v2, s[8:11], s66 offen offset:0 lds\n"
"s_add_u32 m0, s67, 0x3c0\n"
"s_nop 0\n"
"buffer_load_dwordx4 v2, s[8:11], s66 offen offset:64 lds\n"
"s_add_u32 m0, s67, 0x780\n"
"s_nop 0\n"
"buffer_load_dwordx4 v2, s[8:11], s66 offen offset:128 lds\n"
"s_add_u32 m0, s67, 0xb40\n"
"s_nop 0\n"
"buffer_load_dwordx4 v2, s[8:11], s66 offen offset:192 lds\n"
"s_add_u32 m0, s67, 0xf00\n"
"s_nop 0\n"
"buffer_load_dwordx4 v2, s[8:11], s66 offen offset:256 lds\n"
"s_add_u32 m0, s69, 0\n"
"s_nop 0\n"
"buffer_load_dwordx4 v2, s[8:11], s68 offen offset:0 lds\n"
"s_add_u32 m0, s69, 0x3c0\n"
"s_nop 0\n"
"buffer_load_dwordx4 v2, s[8:11], s68 offen offset:64 lds\n"
"s_add_u32 m0, s69, 0x780\n"
"s_nop 0\n"
"buffer_load_dwordx4 v2, s[8:11], s68 offen offset:128 lds\n"
"s_add_u32 m0, s69, 0xb40\n"
"s_nop 0\n"
"buffer_load_dwordx4 v2, s[8:11], s68 offen offset:192 lds\n"
"s_add_u32 m0, s69, 0xf00\n"
"s_nop 0\n"
"buffer_load_dwordx4 v2, s[8:11], s68 offen offset:256 lds\n"
"buffer_load_dwordx4 v[96:99], v19, s[4:7], s50 offen sc1\n"
"s_waitcnt vmcnt(1)\n"
"ds_write_b128 v93, v[20:23] offset:40960\n"
"ds_write_b32 v13, v13 offset:0\n"
"v_add_u32 v88, 0xa000, v88\n"
"ds_read_b128 v[68:71], v88 offset:0\n"
"ds_read_b128 v[72:75], v88 offset:1024\n"
"ds_read_b128 v[76:79], v88 offset:2048\n"
"ds_read_b128 v[80:83], v88 offset:3072\n"
"s_waitcnt vmcnt(0)\n"
"v_xor_b32 v96, s16, v96\n"
"v_xor_b32 v97, s16, v97\n"
"v_xor_b32 v98, s16, v98\n"
"v_xor_b32 v99, s16, v99\n"
"v_or_b32 v96, v96, v97\n"
"v_or3_b32 v96, v96, v98, v99\n"
"v_cmp_ne_u32 vcc, 0, v96\n"
"s_cbranch_vccnz .Lwt_11\n"
"buffer_load_dwordx4 v[96:99], v1, s[4:7], s52 offen offset:0 sc1\n"
"s_waitcnt lgkmcnt(3)\n"
"v_mfma_f32_16x16x32_bf16 v[28:31], v[68:71], v[68:71], 0\n"
"ds_read_b128 v[84:87], v88 offset:4096\n"
"v_mfma_f32_16x16x32_bf16 v[32:35], v[20:23], v[68:71], 0\n"
"s_waitcnt lgkmcnt(3)\n"
"v_mfma_f32_16x16x32_bf16 v[28:31], v[72:75], v[72:75], v[28:31]\n"
"buffer_load_dwordx4 v[100:103], v1, s[4:7], s52 offen offset:1024 sc1\n"
"ds_read_b128 v[68:71], v88 offset:20480\n"
"v_mfma_f32_16x16x32_bf16 v[32:35], v[20:23], v[72:75], v[32:35]\n"
"s_waitcnt lgkmcnt(3)\n"
"v_mfma_f32_16x16x32_bf16 v[28:31], v[76:79], v[76:79], v[28:31]\n"
"ds_read_b128 v[72:75], v88 offset:21504\n"
"v_mfma_f32_16x16x32_bf16 v[32:35], v[20:23], v[76:79], v[32:35]\n"
"buffer_load_dwordx4 v[104:107], v1, s[4:7], s52 offen offset:2048 sc1\n"
"s_waitcnt lgkmcnt(3)\n"
"v_mfma_f32_16x16x32_bf16 v[28:31], v[80:83], v[80:83], v[28:31]\n"
"ds_read_b128 v[76:79], v88 offset:22528\n"
"v_mfma_f32_16x16x32_bf16 v[32:35], v[20:23], v[80:83], v[32:35]\n"
"s_waitcnt lgkmcnt(3)\n"
"v_mfma_f32_16x16x32_bf16 v[28:31], v[84:87], v[84:87], v[28:31]\n"
"ds_read_b128 v[80:83], v88 offset:23552\n"
"buffer_load_dwordx4 v[108:111], v1, s[4:7], s52 offen offset:3072 sc1\n"
"v_mfma_f32_16x16x32_bf16 v[32:35], v[20:23], v[84:87], v[32:35]\n"
"s_waitcnt lgkmcnt(3)\n"
"v_mfma_f32_16x16x32_bf16 v[36:39], v[68:71], v[68:71], 0\n"
"ds_read_b128 v[84:87], v88 offset:24576\n"
"v_mfma_f32_16x16x32_bf16 v[40:43], v[20:23], v[68:71], 0\n"
"s_waitcnt lgkmcnt(3)\n"
"v_mfma_f32_16x16x32_bf16 v[36:39], v[72:75], v[72:75], v[36:39]\n"
"s_add_u32 s52, s52, 4096\n"
"buffer_load_dwordx4 v[112:115], v1, s[4:7], s52 offen offset:0 sc1\n"
"v_add_u32 v88, 0x12400, v88\n"
"ds_read_b128 v[68:71], v88 offset:0\n"
"v_mfma_f32_16x16x32_bf16 v[40:43], v[20:23], v[72:75], v[40:43]\n"
"ds_write_b128 v92, v[28:31] offset:0\n"
"buffer_load_dwordx4 v[116:119], v1, s[4:7], s52 offen offset:1024 sc1\n"
"s_waitcnt lgkmcnt(4)\n"
"v_mfma_f32_16x16x32_bf16 v[36:39], v[76:79], v[76:79], v[36:39]\n"
"ds_read_b128 v[72:75], v88 offset:1024\n"
"v_mfma_f32_16x16x32_bf16 v[40:43], v[20:23], v[76:79], v[40:43]\n"
"ds_write_b32 v91, v32 offset:0\n"
"s_waitcnt lgkmcnt(5)\n"
"v_mfma_f32_16x16x32_bf16 v[36:39], v[80:83], v[80:83], v[36:39]\n"
"buffer_load_dwordx4 v[120:123], v1, s[4:7], s52 offen offset:2048 sc1\n"
"ds_read_b128 v[76:79], v88 offset:2048\n"
"v_mfma_f32_16x16x32_bf16 v[40:43], v[20:23], v[80:83], v[40:43]\n"
"s_waitcnt lgkmcnt(5)\n"
"v_mfma_f32_16x16x32_bf16 v[36:39], v[84:87], v[84:87], v[36:39]\n"
"ds_read_b128 v[80:83], v88 offset:3072\n"
"v_mfma_f32_16x16x32_bf16 v[40:43], v[20:23], v[84:87], v[40:43]\n"
"buffer_load_dwordx4 v[124:127], v1, s[4:7], s52 offen offset:3072 sc1\n"
"s_waitcnt lgkmcnt(5)\n"
"v_mfma_f32_16x16x32_bf16 v[44:47], v[68:71], v[68:71], 0\n"
"ds_read_b128 v[84:87], v88 offset:4096\n"
"v_mfma_f32_16x16x32_bf16 v[48:51], v[20:23], v[68:71], 0\n"
"s_waitcnt lgkmcnt(4)\n"
"v_mfma_f32_16x16x32_bf16 v[44:47], v[72:75], v[72:75], v[44:47]\n"
"ds_read_b128 v[68:71], v88 offset:20480\n"
"s_add_u32 s52, s52, 4096\n"
"buffer_load_dwordx4 v[128:131], v1, s[4:7], s52 offen offset:0 sc1\n"
"v_mfma_f32_16x16x32_bf16 v[48:51], v[20:23], v[72:75], v[48:51]\n"
"ds_write_b128 v92, v[36:39] offset:256\n"
"s_waitcnt lgkmcnt(4)\n"
"v_mfma_f32_16x16x32_bf16 v[44:47], v[76:79], v[76:79], v[44:47]\n"
"ds_read_b128 v[72:75], v88 offset:21504\n"
"buffer_load_dwordx4 v[132:135], v1, s[4:7], s52 offen offset:1024 sc1\n"
"v_mfma_f32_16x16x32_bf16 v[48:51], v[20:23], v[76:79], v[48:51]\n"
"ds_write_b32 v91, v40 offset:256\n"
"s_waitcnt lgkmcnt(5)\n"
"v_mfma_f32_16x16x32_bf16 v[44:47], v[80:83], v[80:83], v[44:47]\n"
"ds_read_b128 v[76:79], v88 offset:22528\n"
"v_mfma_f32_16x16x32_bf16 v[48:51], v[20:23], v[80:83], v[48:51]\n"
"buffer_load_dwordx4 v[136:139], v1, s[4:7], s52 offen offset:2048 sc1\n"
"s_waitcnt lgkmcnt(5)\n"
"v_mfma_f32_16x16x32_bf16 v[44:47], v[84:87], v[84:87], v[44:47]\n"
"ds_read_b128 v[80:83], v88 offset:23552\n"
"v_mfma_f32_16x16x32_bf16 v[48:51], v[20:23], v[84:87], v[48:51]\n"
"s_waitcnt lgkmcnt(5)\n"
"v_mfma_f32_16x16x32_bf16 v[52:55], v[68:71], v[68:71], 0\n"
"ds_read_b128 v[84:87], v88 offset:24576\n"
"buffer_load_dwordx4 v[140:143], v1, s[4:7], s52 offen offset:3072 sc1\n"
"v_mfma_f32_16x16x32_bf16 v[56:59], v[20:23], v[68:71], 0\n"
"s_waitcnt lgkmcnt(4)\n"
"v_mfma_f32_16x16x32_bf16 v[52:55], v[72:75], v[72:75], v[52:55]\n"
"v_add_u32 v88, 0xfffe3c00, v88\n"
"ds_read_b128 v[68:71], v88 offset:0\n"
"v_mfma_f32_16x16x32_bf16 v[56:59], v[20:23], v[72:75], v[56:59]\n"
"s_add_u32 s52, s52, 4096\n"
"buffer_load_dwordx4 v[144:147], v1, s[4:7], s52 offen offset:0 sc1\n"
"ds_write_b128 v92, v[44:47] offset:512\n"
"s_waitcnt lgkmcnt(4)\n"
"v_mfma_f32_16x16x32_bf16 v[52:55], v[76:79], v[76:79], v[52:55]\n"
"ds_read_b128 v[72:75], v88 offset:1024\n"
"v_mfma_f32_16x16x32_bf16 v[56:59], v[20:23], v[76:79], v[56:59]\n"
"buffer_load_dwordx4 v[148:151], v1, s[4:7], s52 offen offset:1024 sc1\n"
"ds_write_b32 v91, v48 offset:512\n"
"ds_read_b32 v90, v14 offset:0\n"
"s_waitcnt lgkmcnt(6)\n"
"v_mfma_f32_16x16x32_bf16 v[52:55], v[80:83], v[80:83], v[52:55]\n"
"ds_read_b128 v[76:79], v88 offset:2048\n"
"v_mfma_f32_16x16x32_bf16 v[56:59], v[20:23], v[80:83], v[56:59]\n"
"buffer_load_dwordx4 v[152:155], v1, s[4:7], s52 offen offset:2048 sc1\n"
"s_waitcnt lgkmcnt(6)\n"
"v_mfma_f32_16x16x32_bf16 v[52:55], v[84:87], v[84:87], v[52:55]\n"
"ds_read_b128 v[80:83], v88 offset:3072\n"
"v_mfma_f32_16x16x32_bf16 v[56:59], v[20:23], v[84:87], v[56:59]\n"
"s_waitcnt lgkmcnt(6)\n"
"v_mfma_f32_16x16x32_bf16 v[60:63], v[68:71], v[68:71], 0\n"
"ds_read_b128 v[84:87], v88 offset:4096\n"
"buffer_load_dwordx4 v[156:159], v1, s[4:7], s52 offen offset:3072 sc1\n"
"v_mfma_f32_16x16x32_bf16 v[64:67], v[20:23], v[68:71], 0\n"
"s_waitcnt lgkmcnt(3)\n"
"v_cmp_eq_u32 vcc, 0, v90\n"
"v_mfma_f32_16x16x32_bf16 v[60:63], v[72:75], v[72:75], v[60:63]\n"
"v_mfma_f32_16x16x32_bf16 v[64:67], v[20:23], v[72:75], v[64:67]\n"
"ds_write_b128 v92, v[52:55] offset:768\n"
"s_cbranch_vccnz .Lsl_7\n"
".Lbk_8:\n"
"s_waitcnt lgkmcnt(3)\n"
"v_mfma_f32_16x16x32_bf16 v[60:63], v[76:79], v[76:79], v[60:63]\n"
"v_add_u32 v3, 0x5000, v3\n"
"v_mfma_f32_16x16x32_bf16 v[64:67], v[20:23], v[76:79], v[64:67]\n"
"ds_read_b128 v[68:71], v3 offset:20480\n"
"ds_write_b32 v91, v56 offset:768\n"
"ds_read_b128 v[72:75], v3 offset:40960\n"
"s_waitcnt lgkmcnt(5)\n"
"v_mfma_f32_16x16x32_bf16 v[60:63], v[80:83], v[80:83], v[60:63]\n"
"v_add_u32 v95, 0x17400, v3\n"
"v_mfma_f32_16x16x32_bf16 v[64:67], v[20:23], v[80:83], v[64:67]\n"
"ds_read_b128 v[76:79], v95 offset:0\n"
"s_waitcnt lgkmcnt(5)\n"
"v_mfma_f32_16x16x32_bf16 v[60:63], v[84:87], v[84:87], v[60:63]\n"
"ds_read_b128 v[80:83], v95 offset:20480\n"
"v_mfma_f32_16x16x32_bf16 v[64:67], v[20:23], v[84:87], v[64:67]\n"
"s_nop 4\n"
"ds_write_b128 v92, v[60:63] offset:1024\n"
"s_nop 1\n"
"ds_write_b32 v91, v64 offset:1024\n"
"s_branch .Ljn_12\n"
".Lwt_11:\n"
"s_waitcnt lgkmcnt(0)\n"
"v_mfma_f32_16x16x32_bf16 v[28:31], v[68:71], v[68:71], 0\n"
"ds_read_b128 v[84:87], v88 offset:4096\n"
"v_mfma_f32_16x16x32_bf16 v[32:35], v[20:23], v[68:71], 0\n"
"v_mfma_f32_16x16x32_bf16 v[28:31], v[72:75], v[72:75], v[28:31]\n"
"ds_read_b128 v[68:71], v88 offset:20480\n"
"v_mfma_f32_16x16x32_bf16 v[32:35], v[20:23], v[72:75], v[32:35]\n"
"v_mfma_f32_16x16x32_bf16 v[28:31], v[76:79], v[76:79], v[28:31]\n"
"ds_read_b128 v[72:75], v88 offset:21504\n"
"v_mfma_f32_16x16x32_bf16 v[32:35], v[20:23], v[76:79], v[32:35]\n"
"v_mfma_f32_16x16x32_bf16 v[28:31], v[80:83], v[80:83], v[28:31]\n"
"ds_read_b128 v[76:79], v88 offset:22528\n"
"v_mfma_f32_16x16x32_bf16 v[32:35], v[20:23], v[80:83], v[32:35]\n"
"s_waitcnt lgkmcnt(3)\n"
"v_mfma_f32_16x16x32_bf16 v[28:31], v[84:87], v[84:87], v[28:31]\n"
"ds_read_b128 v[80:83], v88 offset:23552\n"
"v_mfma_f32_16x16x32_bf16 v[32:35], v[20:23], v[84:87], v[32:35]\n"
"s_waitcnt lgkmcnt(3)\n"
"v_mfma_f32_16x16x32_bf16 v[36:39], v[68:71], v[68:71], 0\n"
"ds_read_b128 v[84:87], v88 offset:24576\n"
"v_mfma_f32_16x16x32_bf16 v[40:43], v[20:23], v[68:71], 0\n"
"s_waitcnt lgkmcnt(3)\n"
"v_mfma_f32_16x16x32_bf16 v[36:39], v[72:75], v[72:75], v[36:39]\n"
"v_add_u32 v88, 0x12400, v88\n"
"ds_read_b128 v[68:71], v88 offset:0\n"
"v_mfma_f32_16x16x32_bf16 v[40:43], v[20:23], v[72:75], v[40:43]\n"
"ds_write_b128 v92, v[28:31] offset:0\n"
"s_waitcnt lgkmcnt(4)\n"
"v_mfma_f32_16x16x32_bf16 v[36:39], v[76:79], v[76:79], v[36:39]\n"
"ds_read_b128 v[72:75], v88 offset:1024\n"
"v_mfma_f32_16x16x32_bf16 v[40:43], v[20:23], v[76:79], v[40:43]\n"
"ds_write_b32 v91, v32 offset:0\n"
"s_waitcnt lgkmcnt(5)\n"
"v_mfma_f32_16x16x32_bf16 v[36:39], v[80:83], v[80:83], v[36:39]\n"
"ds_read_b128 v[76:79], v88 offset:2048\n"
"v_mfma_f32_16x16x32_bf16 v[40:43], v[20:23], v[80:83], v[40:43]\n"
"s_waitcnt lgkmcnt(5)\n"
"v_mfma_f32_16x16x32_bf16 v[36:39], v[84:87], v[84:87], v[36:39]\n"
"ds_read_b128 v[80:83], v88 offset:3072\n"
"v_mfma_f32_16x16x32_bf16 v[40:43], v[20:23], v[84:87], v[40:43]\n"
"s_waitcnt lgkmcnt(5)\n"
"v_mfma_f32_16x16x32_bf16 v[44:47], v[68:71], v[68:71], 0\n"
"ds_read_b128 v[84:87], v88 offset:4096\n"
"v_mfma_f32_16x16x32_bf16 v[48:51], v[20:23], v[68:71], 0\n"
"s_waitcnt lgkmcnt(4)\n"
"v_mfma_f32_16x16x32_bf16 v[44:47], v[72:75], v[72:75], v[44:47]\n"
"ds_read_b128 v[68:71], v88 offset:20480\n"
"v_mfma_f32_16x16x32_bf16 v[48:51], v[20:23], v[72:75], v[48:51]\n"
"ds_write_b128 v92, v[36:39] offset:256\n"
"s_waitcnt lgkmcnt(4)\n"
"v_mfma_f32_16x16x32_bf16 v[44:47], v[76:79], v[76:79], v[44:47]\n"
"ds_read_b128 v[72:75], v88 offset:21504\n"
"v_mfma_f32_16x16x32_bf16 v[48:51], v[20:23], v[76:79], v[48:51]\n"
"ds_write_b32 v91, v40 offset:256\n"
"s_waitcnt lgkmcnt(5)\n"
"v_mfma_f32_16x16x32_bf16 v[44:47], v[80:83], v[80:83], v[44:47]\n"
"ds_read_b128 v[76:79], v88 offset:22528\n"
"v_mfma_f32_16x16x32_bf16 v[48:51], v[20:23], v[80:83], v[48:51]\n"
"s_waitcnt lgkmcnt(5)\n"
"v_mfma_f32_16x16x32_bf16 v[44:47], v[84:87], v[84:87], v[44:47]\n"
"ds_read_b128 v[80:83], v88 offset:23552\n"
"v_mfma_f32_16x16x32_bf16 v[48:51], v[20:23], v[84:87], v[48:51]\n"
"s_waitcnt lgkmcnt(5)\n"
"v_mfma_f32_16x16x32_bf16 v[52:55], v[68:71], v[68:71], 0\n"
"ds_read_b128 v[84:87], v88 offset:24576\n"
"v_mfma_f32_16x16x32_bf16 v[56:59], v[20:23], v[68:71], 0\n"
"s_waitcnt lgkmcnt(4)\n"
"v_mfma_f32_16x16x32_bf16 v[52:55], v[72:75], v[72:75], v[52:55]\n"
"v_add_u32 v88, 0xfffe3c00, v88\n"
"ds_read_b128 v[68:71], v88 offset:0\n"
"v_mfma_f32_16x16x32_bf16 v[56:59], v[20:23], v[72:75], v[56:59]\n"
"ds_write_b128 v92, v[44:47] offset:512\n"
"s_waitcnt lgkmcnt(4)\n"
"v_mfma_f32_16x16x32_bf16 v[52:55], v[76:79], v[76:79], v[52:55]\n"
"ds_read_b128 v[72:75], v88 offset:1024\n"
"v_mfma_f32_16x16x32_bf16 v[56:59], v[20:23], v[76:79], v[56:59]\n"
"ds_write_b32 v91, v48 offset:512\n"
"ds_read_b32 v90, v14 offset:0\n"
"s_waitcnt lgkmcnt(6)\n"
"v_mfma_f32_16x16x32_bf16 v[52:55], v[80:83], v[80:83], v[52:55]\n"
"ds_read_b128 v[76:79], v88 offset:2048\n"
"v_mfma_f32_16x16x32_bf16 v[56:59], v[20:23], v[80:83], v[56:59]\n"
"s_waitcnt lgkmcnt(6)\n"
"v_mfma_f32_16x16x32_bf16 v[52:55], v[84:87], v[84:87], v[52:55]\n"
"ds_read_b128 v[80:83], v88 offset:3072\n"
"v_mfma_f32_16x16x32_bf16 v[56:59], v[20:23], v[84:87], v[56:59]\n"
"s_waitcnt lgkmcnt(6)\n"
"v_mfma_f32_16x16x32_bf16 v[60:63], v[68:71], v[68:71], 0\n"
"ds_read_b128 v[84:87], v88 offset:4096\n"
"v_mfma_f32_16x16x32_bf16 v[64:67], v[20:23], v[68:71], 0\n"
"s_waitcnt lgkmcnt(3)\n"
"v_cmp_eq_u32 vcc, 0, v90\n"
"v_mfma_f32_16x16x32_bf16 v[60:63], v[72:75], v[72:75], v[60:63]\n"
"v_mfma_f32_16x16x32_bf16 v[64:67], v[20:23], v[72:75], v[64:67]\n"
"ds_write_b128 v92, v[52:55] offset:768\n"
"s_waitcnt lgkmcnt(3)\n"
"v_mfma_f32_16x16x32_bf16 v[60:63], v[76:79], v[76:79], v[60:63]\n"
"v_mfma_f32_16x16x32_bf16 v[64:67], v[20:23], v[76:79], v[64:67]\n"
"ds_write_b32 v91, v56 offset:768\n"
"s_waitcnt lgkmcnt(3)\n"
"v_mfma_f32_16x16x32_bf16 v[60:63], v[80:83], v[80:83], v[60:63]\n"
"v_mfma_f32_16x16x32_bf16 v[64:67], v[20:23], v[80:83], v[64:67]\n"
"s_waitcnt lgkmcnt(2)\n"
"v_mfma_f32_16x16x32_bf16 v[60:63], v[84:87], v[84:87], v[60:63]\n"
"v_mfma_f32_16x16x32_bf16 v[64:67], v[20:23], v[84:87], v[64:67]\n"
"buffer_load_dwordx4 v[96:99], v19, s[4:7], s50 offen sc1\n"
"s_mov_b32 s17, 0x100000\n"
".Ltp_13:\n"
"ds_read_b32 v90, v14 offset:0\n"
"s_sub_u32 s17, s17, 1\n"
"s_cbranch_scc1 .Lou_14\n"
"s_waitcnt lgkmcnt(0)\n"
"v_cmp_eq_u32 vcc, 0, v90\n"
"s_cbranch_vccnz .Ltp_13\n"
".Lou_14:\n"
"s_waitcnt lgkmcnt(0)\n"
"v_add_u32 v3, 0x5000, v3\n"
"ds_read_b128 v[68:71], v3 offset:20480\n"
"ds_read_b128 v[72:75], v3 offset:40960\n"
"v_add_u32 v95, 0x17400, v3\n"
"ds_read_b128 v[76:79], v95 offset:0\n"
"ds_read_b128 v[80:83], v95 offset:20480\n"
"ds_write_b128 v92, v[60:63] offset:1024\n"
"ds_write_b32 v91, v64 offset:1024\n"
"s_waitcnt vmcnt(0) lgkmcnt(2)\n"
"s_mov_b32 s17, 32768\n"
"s_branch .Lmd_17\n"
".Ltp_15:\n"
"buffer_load_dwordx4 v[96:99], v19, s[4:7], s50 offen sc1\n"
".Lmd_17:\n"
"s_sub_u32 s17, s17, 1\n"
"s_cbranch_scc1 .Lou_16\n"
"s_waitcnt vmcnt(0)\n"
"v_xor_b32 v96, s16, v96\n"
"v_xor_b32 v97, s16, v97\n"
"v_xor_b32 v98, s16, v98\n"
"v_xor_b32 v99, s16, v99\n"
"v_or_b32 v96, v96, v97\n"
"v_or3_b32 v96, v96, v98, v99\n"
"v_cmp_ne_u32 vcc, 0, v96\n"
"s_cbranch_vccz .Lou_16\n"
"s_sleep 8\n"
"s_branch .Ltp_15\n"
".Lou_16:\n"
"s_waitcnt vmcnt(0)\n"
"buffer_load_dwordx4 v[96:99], v1, s[4:7], s52 offen offset:0 sc1\n"
"buffer_load_dwordx4 v[100:103], v1, s[4:7], s52 offen offset:1024 sc1\n"
"buffer_load_dwordx4 v[104:107], v1, s[4:7], s52 offen offset:2048 sc1\n"
"buffer_load_dwordx4 v[108:111], v1, s[4:7], s52 offen offset:3072 sc1\n"
"s_add_u32 s52, s52, 4096\n"
"buffer_load_dwordx4 v[112:115], v1, s[4:7], s52 offen offset:0 sc1\n"
"buffer_load_dwordx4 v[116:119], v1, s[4:7], s52 offen offset:1024 sc1\n"
"buffer_load_dwordx4 v[120:123], v1, s[4:7], s52 offen offset:2048 sc1\n"
"buffer_load_dwordx4 v[124:127], v1, s[4:7], s52 offen offset:3072 sc1\n"
"s_add_u32 s52, s52, 4096\n"
"buffer_load_dwordx4 v[128:131], v1, s[4:7], s52 offen offset:0 sc1\n"
"buffer_load_dwordx4 v[132:135], v1, s[4:7], s52 offen offset:1024 sc1\n"
"buffer_load_dwordx4 v[136:139], v1, s[4:7], s52 offen offset:2048 sc1\n"
"buffer_load_dwordx4 v[140:143], v1, s[4:7], s52 offen offset:3072 sc1\n"
"s_add_u32 s52, s52, 4096\n"
"buffer_load_dwordx4 v[144:147], v1, s[4:7], s52 offen offset:0 sc1\n"
"buffer_load_dwordx4 v[148:151], v1, s[4:7], s52 offen offset:1024 sc1\n"
"buffer_load_dwordx4 v[152:155], v1, s[4:7], s52 offen offset:2048 sc1\n"
"buffer_load_dwordx4 v[156:159], v1, s[4:7], s52 offen offset:3072 sc1\n"
".Ljn_12:\n"
"s_waitcnt lgkmcnt(2)\n"
"ds_write_b32 v13, v13 offset:16\n"
"s_waitcnt vmcnt(14)\n"
"v_mfma_f32_16x16x32_bf16 v[28:31], v[96:99], v[68:71], 0\n"
"ds_read_b128 v[84:87], v3 offset:21504\n"
"s_add_u32 s52, s52, 4096\n"
"v_mfma_f32_16x16x32_bf16 v[32:35], v[100:103], v[68:71], 0\n"
"v_mfma_f32_16x16x32_bf16 v[36:39], v[96:99], v[72:75], 0\n"
"ds_read_b128 v[68:71], v3 offset:41984\n"
"buffer_load_dwordx4 v[160:163], v1, s[4:7], s52 offen offset:0 sc1\n"
"v_mfma_f32_16x16x32_bf16 v[40:43], v[100:103], v[72:75], 0\n"
"v_mfma_f32_16x16x32_bf16 v[44:47], v[96:99], v[76:79], 0\n"
"ds_read_b128 v[72:75], v95 offset:1024\n"
"v_mfma_f32_16x16x32_bf16 v[48:51], v[100:103], v[76:79], 0\n"
"buffer_load_dwordx4 v[164:167], v1, s[4:7], s52 offen offset:1024 sc1\n"
"v_mfma_f32_16x16x32_bf16 v[52:55], v[96:99], v[80:83], 0\n"
"ds_read_b128 v[76:79], v95 offset:21504\n"
"v_mfma_f32_16x16x32_bf16 v[56:59], v[100:103], v[80:83], 0\n"
"s_waitcnt vmcnt(14) lgkmcnt(2)\n"
"v_mfma_f32_16x16x32_bf16 v[28:31], v[104:107], v[84:87], v[28:31]\n"
"ds_read_b128 v[80:83], v3 offset:22528\n"
"buffer_load_dwordx4 v[168:171], v1, s[4:7], s52 offen offset:2048 sc1\n"
"v_mfma_f32_16x16x32_bf16 v[32:35], v[108:111], v[84:87], v[32:35]\n"
"v_mfma_f32_16x16x32_bf16 v[36:39], v[104:107], v[68:71], v[36:39]\n"
"ds_read_b128 v[84:87], v3 offset:43008\n"
"v_mfma_f32_16x16x32_bf16 v[40:43], v[108:111], v[68:71], v[40:43]\n"
"buffer_load_dwordx4 v[172:175], v1, s[4:7], s52 offen offset:3072 sc1\n"
"v_mov_b32 v20, -1\n"
"s_waitcnt lgkmcnt(2)\n"
"v_mfma_f32_16x16x32_bf16 v[44:47], v[104:107], v[72:75], v[44:47]\n"
"ds_read_b128 v[68:71], v95 offset:2048\n"
"s_add_u32 s52, s52, 4096\n"
"v_mov_b32 v21, -1\n"
"v_mfma_f32_16x16x32_bf16 v[48:51], v[108:111], v[72:75], v[48:51]\n"
"v_mov_b32 v22, -1\n"
"v_mov_b32 v23, -1\n"
"v_cndmask_b32 v91, 2.0, v1, s[40:41]\n"
"v_mfma_f32_16x16x32_bf16 v[52:55], v[104:107], v[76:79], v[52:55]\n"
"ds_read_b128 v[72:75], v95 offset:22528\n"
"buffer_load_dwordx4 v[176:179], v1, s[4:7], s52 offen offset:0 sc1\n"
"s_mov_b32 s33, s30\n"
"v_mfma_f32_16x16x32_bf16 v[56:59], v[108:111], v[76:79], v[56:59]\n"
"buffer_store_dwordx4 v[20:23], v1, s[4:7], s33 offen offset:0 sc1\n"
"s_waitcnt vmcnt(16) lgkmcnt(2)\n"
"v_mfma_f32_16x16x32_bf16 v[28:31], v[112:115], v[80:83], v[28:31]\n"
"ds_read_b128 v[76:79], v3 offset:23552\n"
"v_mfma_f32_16x16x32_bf16 v[32:35], v[116:119], v[80:83], v[32:35]\n"
"buffer_load_dwordx4 v[180:183], v1, s[4:7], s52 offen offset:1024 sc1\n"
"v_mfma_f32_16x16x32_bf16 v[36:39], v[112:115], v[84:87], v[36:39]\n"
"ds_read_b128 v[80:83], v3 offset:44032\n"
"v_mfma_f32_16x16x32_bf16 v[40:43], v[116:119], v[84:87], v[40:43]\n"
"s_waitcnt lgkmcnt(2)\n"
"v_mfma_f32_16x16x32_bf16 v[44:47], v[112:115], v[68:71], v[44:47]\n"
"ds_read_b128 v[84:87], v95 offset:3072\n"
"buffer_load_dwordx4 v[184:187], v1, s[4:7], s52 offen offset:2048 sc1\n"
"buffer_store_dwordx4 v[20:23], v91, s[4:7], s33 offen offset:1024 sc1\n"
"v_mfma_f32_16x16x32_bf16 v[48:51], v[116:119], v[68:71], v[48:51]\n"
"s_add_u32 s33, s30, 0x140000\n"
"ds_read_b32 v90, v14 offset:16\n"
"v_mfma_f32_16x16x32_bf16 v[52:55], v[112:115], v[72:75], v[52:55]\n"
"ds_read_b128 v[68:71], v95 offset:23552\n"
"v_mfma_f32_16x16x32_bf16 v[56:59], v[116:119], v[72:75], v[56:59]\n"
"buffer_load_dwordx4 v[188:191], v1, s[4:7], s52 offen offset:3072 sc1\n"
"s_waitcnt vmcnt(18) lgkmcnt(3)\n"
"v_mfma_f32_16x16x32_bf16 v[28:31], v[120:123], v[76:79], v[28:31]\n"
"ds_read_b128 v[72:75], v3 offset:24576\n"
"s_add_u32 s52, s52, 4096\n"
"v_mfma_f32_16x16x32_bf16 v[32:35], v[124:127], v[76:79], v[32:35]\n"
"buffer_store_dwordx4 v[20:23], v1, s[4:7], s33 offen offset:0 sc1\n"
"v_mfma_f32_16x16x32_bf16 v[36:39], v[120:123], v[80:83], v[36:39]\n"
"ds_read_b128 v[76:79], v3 offset:45056\n"
"buffer_load_dwordx4 v[192:195], v1, s[4:7], s52 offen offset:0 sc1\n"
"v_mfma_f32_16x16x32_bf16 v[40:43], v[124:127], v[80:83], v[40:43]\n"
"s_waitcnt lgkmcnt(2)\n"
"v_mfma_f32_16x16x32_bf16 v[44:47], v[120:123], v[84:87], v[44:47]\n"
"ds_read_b128 v[80:83], v95 offset:4096\n"
"v_mfma_f32_16x16x32_bf16 v[48:51], v[124:127], v[84:87], v[48:51]\n"
"buffer_load_dwordx4 v[196:199], v1, s[4:7], s52 offen offset:1024 sc1\n"
"v_mfma_f32_16x16x32_bf16 v[52:55], v[120:123], v[68:71], v[52:55]\n"
"ds_read_b128 v[84:87], v95 offset:24576\n"
"buffer_store_dwordx4 v[20:23], v91, s[4:7], s33 offen offset:1024 sc1\n"
"v_mfma_f32_16x16x32_bf16 v[56:59], v[124:127], v[68:71], v[56:59]\n"
"s_lshl_b32 s79, s2, 2\n"
"s_add_u32 s79, s79, s3\n"
"v_cmp_eq_u32 vcc, 0, v90\n"
"s_waitcnt vmcnt(20) lgkmcnt(2)\n"
"v_mfma_f32_16x16x32_bf16 v[28:31], v[128:131], v[72:75], v[28:31]\n"
"ds_read_b128 v[68:71], v3 offset:25600\n"
"buffer_load_dwordx4 v[200:203], v1, s[4:7], s52 offen offset:2048 sc1\n"
"s_lshl_b32 s79, s79, 10\n"
"v_mfma_f32_16x16x32_bf16 v[32:35], v[132:135], v[72:75], v[32:35]\n"
"s_add_u32 s33, s79, 0xbc8000\n"
"s_sub_u32 s79, 0xc27fff, s33\n"
"s_and_b32 s79, s79, 0x80000000\n"
"s_cbranch_vccnz .Lsl_26\n"
".Lbk_27:\n"
"v_mfma_f32_16x16x32_bf16 v[36:39], v[128:131], v[76:79], v[36:39]\n"
"ds_read_b128 v[72:75], v3 offset:46080\n"
"s_or_b32 s33, s33, s79\n"
"v_mfma_f32_16x16x32_bf16 v[40:43], v[132:135], v[76:79], v[40:43]\n"
"buffer_load_dwordx4 v[204:207], v1, s[4:7], s52 offen offset:3072 sc1\n"
"buffer_store_dwordx4 v[20:23], v1, s[4:7], s33 offen sc1\n"
"ds_read2st64_b32 v[60:61], v17 offset0:0 offset1:8\n"
"s_waitcnt lgkmcnt(3)\n"
"v_mfma_f32_16x16x32_bf16 v[44:47], v[128:131], v[80:83], v[44:47]\n"
"ds_read_b128 v[76:79], v95 offset:5120\n"
"s_add_u32 s52, s52, 4096\n"
"v_mfma_f32_16x16x32_bf16 v[48:51], v[132:135], v[80:83], v[48:51]\n"
"ds_read2st64_b32 v[62:63], v17 offset0:16 offset1:24\n"
"v_mfma_f32_16x16x32_bf16 v[52:55], v[128:131], v[84:87], v[52:55]\n"
"ds_read_b128 v[80:83], v95 offset:25600\n"
"buffer_load_dwordx4 v[208:211], v1, s[4:7], s52 offen offset:0 sc1\n"
"v_mfma_f32_16x16x32_bf16 v[56:59], v[132:135], v[84:87], v[56:59]\n"
"ds_read2st64_b32 v[64:65], v18 offset0:0 offset1:8\n"
"s_waitcnt vmcnt(22) lgkmcnt(5)\n"
"v_mfma_f32_16x16x32_bf16 v[28:31], v[136:139], v[68:71], v[28:31]\n"
"ds_read_b128 v[84:87], v3 offset:26624\n"
"v_mfma_f32_16x16x32_bf16 v[32:35], v[140:143], v[68:71], v[32:35]\n"
"buffer_load_dwordx4 v[212:215], v1, s[4:7], s52 offen offset:1024 sc1\n"
"ds_read2st64_b32 v[66:67], v18 offset0:16 offset1:24\n"
"v_mfma_f32_16x16x32_bf16 v[36:39], v[136:139], v[72:75], v[36:39]\n"
"ds_read_b128 v[68:71], v3 offset:47104\n"
"v_mfma_f32_16x16x32_bf16 v[40:43], v[140:143], v[72:75], v[40:43]\n"
"s_waitcnt lgkmcnt(4)\n"
"v_mfma_f32_16x16x32_bf16 v[44:47], v[136:139], v[76:79], v[44:47]\n"
"ds_read_b128 v[72:75], v95 offset:6144\n"
"buffer_load_dwordx4 v[216:219], v1, s[4:7], s52 offen offset:2048 sc1\n"
"v_mfma_f32_16x16x32_bf16 v[48:51], v[140:143], v[76:79], v[48:51]\n"
"v_mfma_f32_16x16x32_bf16 v[52:55], v[136:139], v[80:83], v[52:55]\n"
"ds_read_b128 v[76:79], v95 offset:26624\n"
"v_mfma_f32_16x16x32_bf16 v[56:59], v[140:143], v[80:83], v[56:59]\n"
"buffer_load_dwordx4 v[220:223], v1, s[4:7], s52 offen offset:3072 sc1\n"
"s_waitcnt vmcnt(23) lgkmcnt(2)\n"
"v_mfma_f32_16x16x32_bf16 v[28:31], v[144:147], v[84:87], v[28:31]\n"
"ds_read_b128 v[80:83], v3 offset:27648\n"
"s_add_u32 s52, s52, 4096\n"
"v_mfma_f32_16x16x32_bf16 v[32:35], v[148:151], v[84:87], v[32:35]\n"
"v_mfma_f32_16x16x32_bf16 v[36:39], v[144:147], v[68:71], v[36:39]\n"
"ds_read_b128 v[84:87], v3 offset:48128\n"
"buffer_load_dwordx4 v[224:227], v1, s[4:7], s52 offen offset:0 sc1\n"
"v_mfma_f32_16x16x32_bf16 v[40:43], v[148:151], v[68:71], v[40:43]\n"
"v_add_f32 v60, v60, v61\n"
"s_waitcnt lgkmcnt(2)\n"
"v_mfma_f32_16x16x32_bf16 v[44:47], v[144:147], v[72:75], v[44:47]\n"
"ds_read_b128 v[68:71], v95 offset:7168\n"
"v_add_f32 v62, v62, v63\n"
"v_mfma_f32_16x16x32_bf16 v[48:51], v[148:151], v[72:75], v[48:51]\n"
"buffer_load_dwordx4 v[228:231], v1, s[4:7], s52 offen offset:1024 sc1\n"
"v_add_f32 v64, v64, v65\n"
"v_mfma_f32_16x16x32_bf16 v[52:55], v[144:147], v[76:79], v[52:55]\n"
"ds_read_b128 v[72:75], v95 offset:27648\n"
"v_add_f32 v66, v66, v67\n"
"v_mfma_f32_16x16x32_bf16 v[56:59], v[148:151], v[76:79], v[56:59]\n"
"v_add_f32 v64, v64, v66\n"
"s_waitcnt vmcnt(23) lgkmcnt(2)\n"
"v_mfma_f32_16x16x32_bf16 v[28:31], v[152:155], v[80:83], v[28:31]\n"
"ds_read_b128 v[76:79], v3 offset:28672\n"
"buffer_load_dwordx4 v[232:235], v1, s[4:7], s53 offen offset:0 sc1\n"
"v_mfma_f32_16x16x32_bf16 v[32:35], v[156:159], v[80:83], v[32:35]\n"
"v_add_f32 v60, v60, v62\n"
"v_mul_f32 v63, 0x3acccccd, v64\n"
"v_mfma_f32_16x16x32_bf16 v[36:39], v[152:155], v[84:87], v[36:39]\n"
"ds_read_b128 v[80:83], v3 offset:49152\n"
"v_fma_f32 v60, -v64, v63, v60\n"
"v_mfma_f32_16x16x32_bf16 v[40:43], v[156:159], v[84:87], v[40:43]\n"
"buffer_load_dwordx4 v[236:239], v1, s[4:7], s53 offen offset:1024 sc1\n"
"s_waitcnt lgkmcnt(2)\n"
"v_mfma_f32_16x16x32_bf16 v[44:47], v[152:155], v[68:71], v[44:47]\n"
"ds_read_b128 v[84:87], v95 offset:8192\n"
"v_fmamk_f32 v60, v60, 0x3acccccd, v27\n"
"v_mfma_f32_16x16x32_bf16 v[48:51], v[156:159], v[68:71], v[48:51]\n"
"v_max_f32 v60, v60, v27\n"
"v_rsq_f32 v62, v60\n"
"v_mfma_f32_16x16x32_bf16 v[52:55], v[152:155], v[72:75], v[52:55]\n"
"ds_read_b128 v[68:71], v95 offset:28672\n"
"buffer_load_dwordx4 v[240:243], v1, s[4:7], s54 offen offset:0 sc1\n"
"v_mfma_f32_16x16x32_bf16 v[56:59], v[156:159], v[72:75], v[56:59]\n"
"v_mul_f32 v63, -v63, v62\n"
"s_waitcnt vmcnt(24) lgkmcnt(2)\n"
"v_mfma_f32_16x16x32_bf16 v[28:31], v[160:163], v[76:79], v[28:31]\n"
"ds_read_b128 v[72:75], v3 offset:29696\n"
"v_mfma_f32_16x16x32_bf16 v[32:35], v[164:167], v[76:79], v[32:35]\n"
"buffer_load_dwordx4 v[244:247], v1, s[4:7], s54 offen offset:1024 sc1\n"
"v_mfma_f32_16x16x32_bf16 v[36:39], v[160:163], v[80:83], v[36:39]\n"
"ds_read_b128 v[76:79], v3 offset:50176\n"
"v_mfma_f32_16x16x32_bf16 v[40:43], v[164:167], v[80:83], v[40:43]\n"
"ds_write_b64 v16, v[62:63] offset:0\n"
"s_waitcnt lgkmcnt(3)\n"
"v_mfma_f32_16x16x32_bf16 v[44:47], v[160:163], v[84:87], v[44:47]\n"
"ds_read_b128 v[80:83], v95 offset:9216\n"
"buffer_load_dwordx4 v[248:251], v1, s[4:7], s55 offen offset:0 sc1\n"
"v_mfma_f32_16x16x32_bf16 v[48:51], v[164:167], v[84:87], v[48:51]\n"
"v_mfma_f32_16x16x32_bf16 v[52:55], v[160:163], v[68:71], v[52:55]\n"
"ds_read_b128 v[84:87], v95 offset:29696\n"
"v_mfma_f32_16x16x32_bf16 v[56:59], v[164:167], v[68:71], v[56:59]\n"
"buffer_load_dwordx4 v[252:255], v1, s[4:7], s55 offen offset:1024 sc1\n"
"ds_read2st64_b32 v[60:61], v17 offset0:4 offset1:12\n"
"s_waitcnt vmcnt(25) lgkmcnt(4)\n"
"v_mfma_f32_16x16x32_bf16 v[28:31], v[168:171], v[72:75], v[28:31]\n"
"ds_read_b128 v[68:71], v3 offset:30720\n"
"s_mov_b32 m0, 163328\n"
"v_mfma_f32_16x16x32_bf16 v[32:35], v[172:175], v[72:75], v[32:35]\n"
"ds_read2st64_b32 v[62:63], v17 offset0:20 offset1:28\n"
"v_mfma_f32_16x16x32_bf16 v[36:39], v[168:171], v[76:79], v[36:39]\n"
"ds_read_b128 v[72:75], v3 offset:51200\n"
"buffer_load_dword v89, s[8:11], 0 offen lds\n"
"v_mfma_f32_16x16x32_bf16 v[40:43], v[172:175], v[76:79], v[40:43]\n"
"ds_read2st64_b32 v[64:65], v18 offset0:4 offset1:12\n"
"s_waitcnt lgkmcnt(5)\n"
"v_mfma_f32_16x16x32_bf16 v[44:47], v[168:171], v[80:83], v[44:47]\n"
"ds_read_b128 v[76:79], v95 offset:10240\n"
"ds_write_b32 v13, v13 offset:32\n"
"v_mfma_f32_16x16x32_bf16 v[48:51], v[172:175], v[80:83], v[48:51]\n"
"ds_read2st64_b32 v[66:67], v18 offset0:20 offset1:28\n"
"v_mfma_f32_16x16x32_bf16 v[52:55], v[168:171], v[84:87], v[52:55]\n"
"ds_read_b128 v[80:83], v95 offset:30720\n"
"v_mfma_f32_16x16x32_bf16 v[56:59], v[172:175], v[84:87], v[56:59]\n"
"s_waitcnt vmcnt(23) lgkmcnt(5)\n"
"v_mfma_f32_16x16x32_bf16 v[28:31], v[176:179], v[68:71], v[28:31]\n"
"ds_read_b128 v[84:87], v3 offset:31744\n"
"v_mfma_f32_16x16x32_bf16 v[32:35], v[180:183], v[68:71], v[32:35]\n"
"v_mfma_f32_16x16x32_bf16 v[36:39], v[176:179], v[72:75], v[36:39]\n"
"ds_read_b128 v[68:71], v3 offset:52224\n"
"v_mfma_f32_16x16x32_bf16 v[40:43], v[180:183], v[72:75], v[40:43]\n"
"s_waitcnt lgkmcnt(2)\n"
"v_mfma_f32_16x16x32_bf16 v[44:47], v[176:179], v[76:79], v[44:47]\n"
"ds_read_b128 v[72:75], v95 offset:11264\n"
"v_mfma_f32_16x16x32_bf16 v[48:51], v[180:183], v[76:79], v[48:51]\n"
"s_waitcnt vmcnt(13)\n"
"ds_write_b32 v13, v13 offset:48\n"
"v_mfma_f32_16x16x32_bf16 v[52:55], v[176:179], v[80:83], v[52:55]\n"
"ds_read_b128 v[76:79], v95 offset:31744\n"
"v_mfma_f32_16x16x32_bf16 v[56:59], v[180:183], v[80:83], v[56:59]\n"
"v_add_f32 v60, v60, v61\n"
"s_waitcnt lgkmcnt(3)\n"
"v_mfma_f32_16x16x32_bf16 v[28:31], v[184:187], v[84:87], v[28:31]\n"
"ds_read_b128 v[80:83], v3 offset:32768\n"
"v_add_f32 v62, v62, v63\n"
"v_mfma_f32_16x16x32_bf16 v[32:35], v[188:191], v[84:87], v[32:35]\n"
"v_add_f32 v64, v64, v65\n"
"v_add_f32 v66, v66, v67\n"
"v_mfma_f32_16x16x32_bf16 v[36:39], v[184:187], v[68:71], v[36:39]\n"
"ds_read_b128 v[84:87], v3 offset:53248\n"
"v_add_f32 v64, v64, v66\n"
"v_mfma_f32_16x16x32_bf16 v[40:43], v[188:191], v[68:71], v[40:43]\n"
"v_add_f32 v60, v60, v62\n"
"s_waitcnt lgkmcnt(2)\n"
"v_mfma_f32_16x16x32_bf16 v[44:47], v[184:187], v[72:75], v[44:47]\n"
"ds_read_b128 v[68:71], v95 offset:12288\n"
"v_mul_f32 v63, 0x3acccccd, v64\n"
"v_mfma_f32_16x16x32_bf16 v[48:51], v[188:191], v[72:75], v[48:51]\n"
"v_fma_f32 v60, -v64, v63, v60\n"
"v_fmamk_f32 v60, v60, 0x3acccccd, v27\n"
"v_mfma_f32_16x16x32_bf16 v[52:55], v[184:187], v[76:79], v[52:55]\n"
"ds_read_b128 v[72:75], v95 offset:32768\n"
"v_max_f32 v60, v60, v27\n"
"v_mfma_f32_16x16x32_bf16 v[56:59], v[188:191], v[76:79], v[56:59]\n"
"ds_read_b32 v90, v14 offset:48\n"
"s_waitcnt lgkmcnt(3)\n"
"v_mfma_f32_16x16x32_bf16 v[28:31], v[192:195], v[80:83], v[28:31]\n"
"ds_read_b128 v[76:79], v3 offset:33792\n"
"v_rsq_f32 v62, v60\n"
"v_mfma_f32_16x16x32_bf16 v[32:35], v[196:199], v[80:83], v[32:35]\n"
"v_mul_f32 v63, -v63, v62\n"
"v_cndmask_b32 v16, 2.0, v16, s[40:41]\n"
"v_mfma_f32_16x16x32_bf16 v[36:39], v[192:195], v[84:87], v[36:39]\n"
"ds_read_b128 v[80:83], v3 offset:54272\n"
"v_mfma_f32_16x16x32_bf16 v[40:43], v[196:199], v[84:87], v[40:43]\n"
"s_waitcnt lgkmcnt(3)\n"
"v_mfma_f32_16x16x32_bf16 v[44:47], v[192:195], v[68:71], v[44:47]\n"
"ds_read_b128 v[84:87], v95 offset:13312\n"
"v_mfma_f32_16x16x32_bf16 v[48:51], v[196:199], v[68:71], v[48:51]\n"
"v_mfma_f32_16x16x32_bf16 v[52:55], v[192:195], v[72:75], v[52:55]\n"
"ds_read_b128 v[68:71], v95 offset:33792\n"
"v_mfma_f32_16x16x32_bf16 v[56:59], v[196:199], v[72:75], v[56:59]\n"
"s_waitcnt lgkmcnt(4)\n"
"v_cmp_eq_u32 vcc, 0, v90\n"
"ds_write_b64 v16, v[62:63] offset:512\n"
"s_waitcnt lgkmcnt(3)\n"
"v_mfma_f32_16x16x32_bf16 v[28:31], v[200:203], v[76:79], v[28:31]\n"
"ds_read_b128 v[72:75], v3 offset:34816\n"
"v_mfma_f32_16x16x32_bf16 v[32:35], v[204:207], v[76:79], v[32:35]\n"
"s_cbranch_vccnz .Lsl_18\n"
".Lbk_19:\n"
"ds_write_b32 v13, v13 offset:80\n"
"v_mfma_f32_16x16x32_bf16 v[36:39], v[200:203], v[80:83], v[36:39]\n"
"ds_read_b128 v[76:79], v3 offset:55296\n"
"v_lshrrev_b32 v19, 3, v0\n"
"v_mfma_f32_16x16x32_bf16 v[40:43], v[204:207], v[80:83], v[40:43]\n"
"v_add_u32 v19, s3, v19\n"
"s_waitcnt lgkmcnt(4)\n"
"v_mfma_f32_16x16x32_bf16 v[44:47], v[200:203], v[84:87], v[44:47]\n"
"ds_read_b128 v[80:83], v95 offset:14336\n"
"v_lshlrev_b32 v91, 27, v19\n"
"v_mfma_f32_16x16x32_bf16 v[48:51], v[204:207], v[84:87], v[48:51]\n"
"v_lshl_add_u32 v91, v0, 14, v91\n"
"v_mov_b32 v19, s16\n"
"v_mfma_f32_16x16x32_bf16 v[52:55], v[200:203], v[68:71], v[52:55]\n"
"ds_read_b128 v[84:87], v95 offset:34816\n"
"s_lshr_b32 s79, s2, 2\n"
"v_mfma_f32_16x16x32_bf16 v[56:59], v[204:207], v[68:71], v[56:59]\n"
"s_lshl_b32 s79, s79, 5\n"
"s_waitcnt vmcnt(11) lgkmcnt(2)\n"
"v_mfma_f32_16x16x32_bf16 v[28:31], v[208:211], v[72:75], v[28:31]\n"
"ds_read_b128 v[68:71], v3 offset:35840\n"
"s_and_b32 s78, s2, 3\n"
"v_mfma_f32_16x16x32_bf16 v[32:35], v[212:215], v[72:75], v[32:35]\n"
"s_lshl_b32 s78, s78, 2\n"
"s_add_u32 s79, s79, s78\n"
"v_mfma_f32_16x16x32_bf16 v[36:39], v[208:211], v[76:79], v[36:39]\n"
"ds_read_b128 v[72:75], v3 offset:56320\n"
"s_add_u32 s32, s79, 0x4000\n"
"v_mfma_f32_16x16x32_bf16 v[40:43], v[212:215], v[76:79], v[40:43]\n"
"buffer_store_dword v19, v91, s[4:7], s32 offen sc1\n"
"s_waitcnt lgkmcnt(2)\n"
"v_mfma_f32_16x16x32_bf16 v[44:47], v[208:211], v[80:83], v[44:47]\n"
"ds_read_b128 v[76:79], v95 offset:15360\n"
"v_mfma_f32_16x16x32_bf16 v[48:51], v[212:215], v[80:83], v[48:51]\n"
"v_mfma_f32_16x16x32_bf16 v[52:55], v[208:211], v[84:87], v[52:55]\n"
"ds_read_b128 v[80:83], v95 offset:35840\n"
"v_mfma_f32_16x16x32_bf16 v[56:59], v[212:215], v[84:87], v[56:59]\n"
"s_waitcnt vmcnt(10) lgkmcnt(2)\n"
"v_mfma_f32_16x16x32_bf16 v[28:31], v[216:219], v[68:71], v[28:31]\n"
"ds_read_b128 v[84:87], v3 offset:36864\n"
"v_mfma_f32_16x16x32_bf16 v[32:35], v[220:223], v[68:71], v[32:35]\n"
"ds_read_b32 v90, v14 offset:80\n"
"v_mfma_f32_16x16x32_bf16 v[36:39], v[216:219], v[72:75], v[36:39]\n"
"ds_read_b128 v[68:71], v4 offset:40960\n"
"v_mfma_f32_16x16x32_bf16 v[40:43], v[220:223], v[72:75], v[40:43]\n"
"s_waitcnt lgkmcnt(3)\n"
"v_mfma_f32_16x16x32_bf16 v[44:47], v[216:219], v[76:79], v[44:47]\n"
"ds_read_b128 v[72:75], v5 offset:40960\n"
"v_mfma_f32_16x16x32_bf16 v[48:51], v[220:223], v[76:79], v[48:51]\n"
"ds_read_b64 v[88:89], v94 offset:0\n"
"v_mfma_f32_16x16x32_bf16 v[52:55], v[216:219], v[80:83], v[52:55]\n"
"ds_read_b128 v[76:79], v6 offset:40960\n"
"v_mfma_f32_16x16x32_bf16 v[56:59], v[220:223], v[80:83], v[56:59]\n"
"s_waitcnt vmcnt(6) lgkmcnt(3)\n"
"v_mfma_f32_16x16x32_bf16 v[28:31], v[224:227], v[84:87], v[28:31]\n"
"ds_read_b128 v[80:83], v3 offset:57344\n"
"v_mfma_f32_16x16x32_bf16 v[32:35], v[228:231], v[84:87], v[32:35]\n"
"ds_read_b64 v[92:93], v94 offset:128\n"
"v_mfma_f32_16x16x32_bf16 v[28:31], v[232:235], v[68:71], v[28:31]\n"
"ds_read_b128 v[84:87], v4 offset:61440\n"
"v_mfma_f32_16x16x32_bf16 v[32:35], v[236:239], v[68:71], v[32:35]\n"
"v_cmp_eq_u32 vcc, 0, v90\n"
"s_waitcnt vmcnt(2) lgkmcnt(3)\n"
"v_mfma_f32_16x16x32_bf16 v[28:31], v[240:243], v[72:75], v[28:31]\n"
"ds_read_b128 v[68:71], v5 offset:61440\n"
"v_mfma_f32_16x16x32_bf16 v[32:35], v[244:247], v[72:75], v[32:35]\n"
"s_cbranch_vccnz .Lsl_30\n"
".Lbk_31:\n"
"v_mfma_f32_16x16x32_bf16 v[28:31], v[248:251], v[76:79], v[28:31]\n"
"ds_read_b128 v[72:75], v6 offset:61440\n"
"v_mfma_f32_16x16x32_bf16 v[32:35], v[252:255], v[76:79], v[32:35]\n"
"s_waitcnt lgkmcnt(2)\n"
"v_mfma_f32_16x16x32_bf16 v[36:39], v[224:227], v[80:83], v[36:39]\n"
"ds_read_b128 v[76:79], v95 offset:16384\n"
"v_mfma_f32_16x16x32_bf16 v[40:43], v[228:231], v[80:83], v[40:43]\n"
"v_mov_b32_dpp v16, v28 row_newbcast:0 row_mask:0xf bank_mask:0xf\n"
"v_mov_b32_dpp v17, v29 row_newbcast:0 row_mask:0xf bank_mask:0xf\n"
"v_mov_b32_dpp v18, v30 row_newbcast:0 row_mask:0xf bank_mask:0xf\n"
"v_mfma_f32_16x16x32_bf16 v[36:39], v[232:235], v[84:87], v[36:39]\n"
"v_add_u32 v4, 0x1c400, v4\n"
"ds_read_b128 v[80:83], v4 offset:0\n"
"v_mov_b32_dpp v19, v31 row_newbcast:0 row_mask:0xf bank_mask:0xf\n"
"v_mfma_f32_16x16x32_bf16 v[40:43], v[236:239], v[84:87], v[40:43]\n"
"v_mov_b32_dpp v20, v32 row_newbcast:0 row_mask:0xf bank_mask:0xf\n"
"v_mov_b32_dpp v21, v33 row_newbcast:0 row_mask:0xf bank_mask:0xf\n"
"s_waitcnt lgkmcnt(2)\n"
"v_mfma_f32_16x16x32_bf16 v[36:39], v[240:243], v[68:71], v[36:39]\n"
"v_add_u32 v5, 0x1c400, v5\n"
"ds_read_b128 v[84:87], v5 offset:0\n"
"v_mfma_f32_16x16x32_bf16 v[40:43], v[244:247], v[68:71], v[40:43]\n"
"v_mov_b32_dpp v22, v34 row_newbcast:0 row_mask:0xf bank_mask:0xf\n"
"v_mov_b32_dpp v23, v35 row_newbcast:0 row_mask:0xf bank_mask:0xf\n"
"v_mul_f32 v28, v88, v28\n"
"v_mfma_f32_16x16x32_bf16 v[36:39], v[248:251], v[72:75], v[36:39]\n"
"v_add_u32 v6, 0x1c400, v6\n"
"ds_read_b128 v[68:71], v6 offset:0\n"
"v_mul_f32 v29, v88, v29\n"
"v_mfma_f32_16x16x32_bf16 v[40:43], v[252:255], v[72:75], v[40:43]\n"
"v_mul_f32 v30, v88, v30\n"
"v_mul_f32 v31, v88, v31\n"
"s_waitcnt lgkmcnt(2)\n"
"v_mfma_f32_16x16x32_bf16 v[44:47], v[224:227], v[76:79], v[44:47]\n"
"ds_read_b128 v[72:75], v95 offset:36864\n"
"v_fmac_f32 v28, v89, v16\n"
"v_fmac_f32 v29, v89, v17\n"
"v_mfma_f32_16x16x32_bf16 v[48:51], v[228:231], v[76:79], v[48:51]\n"
"ds_write_b32 v13, v13 offset:64\n"
"v_fmac_f32 v30, v89, v18\n"
"v_mfma_f32_16x16x32_bf16 v[44:47], v[232:235], v[80:83], v[44:47]\n"
"ds_read_b128 v[76:79], v4 offset:20480\n"
"v_fmac_f32 v31, v89, v19\n"
"v_cvt_pk_bf16_f32 v28, v28, v29\n"
"v_mfma_f32_16x16x32_bf16 v[48:51], v[236:239], v[80:83], v[48:51]\n"
"v_cvt_pk_bf16_f32 v29, v30, v31\n"
"ds_write_b64 v7, v[28:29] offset:0\n"
"s_waitcnt lgkmcnt(4)\n"
"v_mfma_f32_16x16x32_bf16 v[44:47], v[240:243], v[84:87], v[44:47]\n"
"ds_read_b128 v[80:83], v5 offset:20480\n"
"v_mul_f32 v32, v88, v32\n"
"v_mul_f32 v33, v88, v33\n"
"v_mfma_f32_16x16x32_bf16 v[48:51], v[244:247], v[84:87], v[48:51]\n"
"v_mul_f32 v34, v88, v34\n"
"v_mul_f32 v35, v88, v35\n"
"v_fmac_f32 v32, v89, v20\n"
"v_mfma_f32_16x16x32_bf16 v[44:47], v[248:251], v[68:71], v[44:47]\n"
"ds_read_b128 v[84:87], v6 offset:20480\n"
"v_fmac_f32 v33, v89, v21\n"
"v_fmac_f32 v34, v89, v22\n"
"v_mfma_f32_16x16x32_bf16 v[48:51], v[252:255], v[68:71], v[48:51]\n"
"v_fmac_f32 v35, v89, v23\n"
"v_cvt_pk_bf16_f32 v32, v32, v33\n"
"s_waitcnt lgkmcnt(3)\n"
"v_mfma_f32_16x16x32_bf16 v[52:55], v[224:227], v[72:75], v[52:55]\n"
"v_add_u32 v3, 0xffffb000, v3\n"
"ds_read_b128 v[68:71], v3 offset:0\n"
"v_cvt_pk_bf16_f32 v33, v34, v35\n"
"v_mfma_f32_16x16x32_bf16 v[56:59], v[228:231], v[72:75], v[56:59]\n"
"ds_read_b32 v90, v14 offset:64\n"
"ds_write_b64 v8, v[32:33] offset:0\n"
"v_mfma_f32_16x16x32_bf16 v[52:55], v[232:235], v[76:79], v[52:55]\n"
"ds_read_b128 v[72:75], v3 offset:1024\n"
"ds_read_b64 v[88:89], v94 offset:256\n"
"v_mul_f32 v36, v92, v36\n"
"v_mfma_f32_16x16x32_bf16 v[56:59], v[236:239], v[76:79], v[56:59]\n"
"v_mul_f32 v37, v92, v37\n"
"v_mul_f32 v38, v92, v38\n"
"s_waitcnt lgkmcnt(5)\n"
"v_mfma_f32_16x16x32_bf16 v[52:55], v[240:243], v[80:83], v[52:55]\n"
"ds_read_b128 v[76:79], v3 offset:2048\n"
"v_mul_f32 v39, v92, v39\n"
"v_fmac_f32 v36, v93, v16\n"
"v_mfma_f32_16x16x32_bf16 v[56:59], v[244:247], v[80:83], v[56:59]\n"
"v_fmac_f32 v37, v93, v17\n"
"v_fmac_f32 v38, v93, v18\n"
"v_fmac_f32 v39, v93, v19\n"
"v_mfma_f32_16x16x32_bf16 v[52:55], v[248:251], v[84:87], v[52:55]\n"
"ds_read_b128 v[80:83], v3 offset:3072\n"
"v_cvt_pk_bf16_f32 v36, v36, v37\n"
"v_cvt_pk_bf16_f32 v37, v38, v39\n"
"v_mfma_f32_16x16x32_bf16 v[56:59], v[252:255], v[84:87], v[56:59]\n"
"ds_write_b64 v7, v[36:37] offset:3968\n"
"s_waitcnt lgkmcnt(4)\n"
"v_mfma_f32_16x16x32_bf16 v[60:63], v[96:99], v[68:71], 0\n"
"ds_read_b128 v[84:87], v3 offset:4096\n"
"v_mul_f32 v40, v92, v40\n"
"v_mul_f32 v41, v92, v41\n"
"v_mfma_f32_16x16x32_bf16 v[64:67], v[100:103], v[68:71], 0\n"
"v_cmp_eq_u32 vcc, 0, v90\n"
"v_mul_f32 v42, v92, v42\n"
"v_mul_f32 v43, v92, v43\n"
"v_mfma_f32_16x16x32_bf16 v[60:63], v[104:107], v[72:75], v[60:63]\n"
"ds_read_b128 v[68:71], v3 offset:5120\n"
"v_fmac_f32 v40, v93, v20\n"
"v_fmac_f32 v41, v93, v21\n"
"v_mfma_f32_16x16x32_bf16 v[64:67], v[108:111], v[72:75], v[64:67]\n"
"s_cbranch_vccnz .Lsl_22\n"
".Lbk_23:\n"
"s_waitcnt lgkmcnt(3)\n"
"v_mfma_f32_16x16x32_bf16 v[60:63], v[112:115], v[76:79], v[60:63]\n"
"ds_read_b128 v[72:75], v3 offset:6144\n"
"v_fmac_f32 v42, v93, v22\n"
"v_fmac_f32 v43, v93, v23\n"
"v_mfma_f32_16x16x32_bf16 v[64:67], v[116:119], v[76:79], v[64:67]\n"
"v_cvt_pk_bf16_f32 v40, v40, v41\n"
"v_cvt_pk_bf16_f32 v41, v42, v43\n"
"ds_write_b64 v8, v[40:41] offset:3968\n"
"v_mfma_f32_16x16x32_bf16 v[60:63], v[120:123], v[80:83], v[60:63]\n"
"ds_read_b128 v[76:79], v3 offset:7168\n"
"ds_read_b64 v[92:93], v94 offset:384\n"
"v_mul_f32 v44, v88, v44\n"
"v_mfma_f32_16x16x32_bf16 v[64:67], v[124:127], v[80:83], v[64:67]\n"
"s_add_u32 s31, s28, 0x140000\n"
"v_mul_f32 v45, v88, v45\n"
"s_waitcnt lgkmcnt(4)\n"
"v_mfma_f32_16x16x32_bf16 v[60:63], v[128:131], v[84:87], v[60:63]\n"
"ds_read_b128 v[80:83], v3 offset:8192\n"
"s_add_u32 m0, s29, 0xa000\n"
"v_mul_f32 v46, v88, v46\n"
"v_mfma_f32_16x16x32_bf16 v[64:67], v[132:135], v[84:87], v[64:67]\n"
"buffer_load_dwordx4 v2, s[8:11], s31 offen offset:0 lds\n"
"v_mul_f32 v47, v88, v47\n"
"v_fmac_f32 v44, v89, v16\n"
"v_mfma_f32_16x16x32_bf16 v[60:63], v[136:139], v[68:71], v[60:63]\n"
"ds_read_b128 v[84:87], v3 offset:9216\n"
"s_add_u32 m0, s29, 0xa3c0\n"
"v_fmac_f32 v45, v89, v17\n"
"v_mfma_f32_16x16x32_bf16 v[64:67], v[140:143], v[68:71], v[64:67]\n"
"v_fmac_f32 v46, v89, v18\n"
"v_fmac_f32 v47, v89, v19\n"
"s_waitcnt lgkmcnt(3)\n"
"v_mfma_f32_16x16x32_bf16 v[60:63], v[144:147], v[72:75], v[60:63]\n"
"ds_read_b128 v[68:71], v3 offset:10240\n"
"buffer_load_dwordx4 v2, s[8:11], s31 offen offset:64 lds\n"
"v_cvt_pk_bf16_f32 v44, v44, v45\n"
"v_mfma_f32_16x16x32_bf16 v[64:67], v[148:151], v[72:75], v[64:67]\n"
"s_add_u32 m0, s29, 0xa780\n"
"v_cvt_pk_bf16_f32 v45, v46, v47\n"
"ds_write_b64 v7, v[44:45] offset:7936\n"
"v_mfma_f32_16x16x32_bf16 v[60:63], v[152:155], v[76:79], v[60:63]\n"
"ds_read_b128 v[72:75], v3 offset:11264\n"
"v_mul_f32 v48, v88, v48\n"
"v_mul_f32 v49, v88, v49\n"
"v_mfma_f32_16x16x32_bf16 v[64:67], v[156:159], v[76:79], v[64:67]\n"
"buffer_load_dwordx4 v2, s[8:11], s31 offen offset:128 lds\n"
"v_mul_f32 v50, v88, v50\n"
"s_waitcnt lgkmcnt(3)\n"
"v_mfma_f32_16x16x32_bf16 v[60:63], v[160:163], v[80:83], v[60:63]\n"
"ds_read_b128 v[76:79], v3 offset:12288\n"
"s_add_u32 m0, s29, 0xab40\n"
"v_mul_f32 v51, v88, v51\n"
"v_mfma_f32_16x16x32_bf16 v[64:67], v[164:167], v[80:83], v[64:67]\n"
"v_fmac_f32 v48, v89, v20\n"
"v_fmac_f32 v49, v89, v21\n"
"v_fmac_f32 v50, v89, v22\n"
"v_mfma_f32_16x16x32_bf16 v[60:63], v[168:171], v[84:87], v[60:63]\n"
"ds_read_b128 v[80:83], v3 offset:13312\n"
"buffer_load_dwordx4 v2, s[8:11], s31 offen offset:192 lds\n"
"v_fmac_f32 v51, v89, v23\n"
"v_mfma_f32_16x16x32_bf16 v[64:67], v[172:175], v[84:87], v[64:67]\n"
"s_add_u32 m0, s29, 0xaf00\n"
"v_cvt_pk_bf16_f32 v48, v48, v49\n"
"s_waitcnt lgkmcnt(2)\n"
"v_mfma_f32_16x16x32_bf16 v[60:63], v[176:179], v[68:71], v[60:63]\n"
"ds_read_b128 v[84:87], v3 offset:14336\n"
"v_cvt_pk_bf16_f32 v49, v50, v51\n"
"ds_write_b64 v8, v[48:49] offset:7936\n"
"v_mfma_f32_16x16x32_bf16 v[64:67], v[180:183], v[68:71], v[64:67]\n"
"buffer_load_dwordx4 v2, s[8:11], s31 offen offset:256 lds\n"
"ds_read_b64 v[88:89], v94 offset:512\n"
"v_mul_f32 v52, v92, v52\n"
"v_mfma_f32_16x16x32_bf16 v[60:63], v[184:187], v[72:75], v[60:63]\n"
"ds_read_b128 v[68:71], v3 offset:15360\n"
"s_add_u32 s31, s28, 0x145000\n"
"v_mul_f32 v53, v92, v53\n"
"v_mfma_f32_16x16x32_bf16 v[64:67], v[188:191], v[72:75], v[64:67]\n"
"s_add_u32 m0, s29, 0xf000\n"
"v_mul_f32 v54, v92, v54\n"
"s_waitcnt lgkmcnt(4)\n"
"v_mfma_f32_16x16x32_bf16 v[60:63], v[192:195], v[76:79], v[60:63]\n"
"ds_read_b128 v[72:75], v3 offset:16384\n"
"buffer_load_dwordx4 v2, s[8:11], s31 offen offset:0 lds\n"
"v_mul_f32 v55, v92, v55\n"
"v_mfma_f32_16x16x32_bf16 v[64:67], v[196:199], v[76:79], v[64:67]\n"
"s_add_u32 m0, s29, 0xf3c0\n"
"v_fmac_f32 v52, v93, v16\n"
"v_fmac_f32 v53, v93, v17\n"
"v_mfma_f32_16x16x32_bf16 v[60:63], v[200:203], v[80:83], v[60:63]\n"
"v_add_u32 v4, 0xfffe3c00, v4\n"
"ds_read_b128 v[76:79], v4 offset:0\n"
"v_fmac_f32 v54, v93, v18\n"
"v_mfma_f32_16x16x32_bf16 v[64:67], v[204:207], v[80:83], v[64:67]\n"
"buffer_load_dwordx4 v2, s[8:11], s31 offen offset:64 lds\n"
"v_fmac_f32 v55, v93, v19\n"
"s_waitcnt lgkmcnt(2)\n"
"v_mfma_f32_16x16x32_bf16 v[60:63], v[208:211], v[84:87], v[60:63]\n"
"v_add_u32 v5, 0xfffe3c00, v5\n"
"ds_read_b128 v[80:83], v5 offset:0\n"
"v_cvt_pk_bf16_f32 v52, v52, v53\n"
"v_mfma_f32_16x16x32_bf16 v[64:67], v[212:215], v[84:87], v[64:67]\n"
"s_add_u32 m0, s29, 0xf780\n"
"v_cvt_pk_bf16_f32 v53, v54, v55\n"
"ds_write_b64 v7, v[52:53] offset:11904\n"
"v_mfma_f32_16x16x32_bf16 v[60:63], v[216:219], v[68:71], v[60:63]\n"
"v_add_u32 v6, 0xfffe3c00, v6\n"
"ds_read_b128 v[84:87], v6 offset:0\n"
"buffer_load_dwordx4 v2, s[8:11], s31 offen offset:128 lds\n"
"v_mfma_f32_16x16x32_bf16 v[64:67], v[220:223], v[68:71], v[64:67]\n"
"s_add_u32 m0, s29, 0xfb40\n"
"v_mul_f32 v56, v92, v56\n"
"s_waitcnt lgkmcnt(3)\n"
"v_mfma_f32_16x16x32_bf16 v[60:63], v[224:227], v[72:75], v[60:63]\n"
"v_mul_f32 v57, v92, v57\n"
"v_mul_f32 v58, v92, v58\n"
"v_mul_f32 v59, v92, v59\n"
"v_mfma_f32_16x16x32_bf16 v[64:67], v[228:231], v[72:75], v[64:67]\n"
"buffer_load_dwordx4 v2, s[8:11], s31 offen offset:192 lds\n"
"v_fmac_f32 v56, v93, v20\n"
"v_fmac_f32 v57, v93, v21\n"
"v_mfma_f32_16x16x32_bf16 v[60:63], v[232:235], v[76:79], v[60:63]\n"
"s_add_u32 m0, s29, 0xff00\n"
"v_fmac_f32 v58, v93, v22\n"
"v_mfma_f32_16x16x32_bf16 v[64:67], v[236:239], v[76:79], v[64:67]\n"
"v_fmac_f32 v59, v93, v23\n"
"s_waitcnt lgkmcnt(0)\n"
"v_mfma_f32_16x16x32_bf16 v[60:63], v[240:243], v[80:83], v[60:63]\n"
"buffer_load_dwordx4 v2, s[8:11], s31 offen offset:256 lds\n"
"v_cvt_pk_bf16_f32 v56, v56, v57\n"
"v_mfma_f32_16x16x32_bf16 v[64:67], v[244:247], v[80:83], v[64:67]\n"
"v_cvt_pk_bf16_f32 v57, v58, v59\n"
"ds_write_b64 v8, v[56:57] offset:11904\n"
"v_mfma_f32_16x16x32_bf16 v[60:63], v[248:251], v[84:87], v[60:63]\n"
"v_mfma_f32_16x16x32_bf16 v[64:67], v[252:255], v[84:87], v[64:67]\n"
"s_nop 4\n"
"v_mul_f32 v60, v88, v60\n"
"v_mul_f32 v61, v88, v61\n"
"v_mul_f32 v62, v88, v62\n"
"v_mul_f32 v63, v88, v63\n"
"v_fmac_f32 v60, v89, v16\n"
"v_fmac_f32 v61, v89, v17\n"
"v_fmac_f32 v62, v89, v18\n"
"v_fmac_f32 v63, v89, v19\n"
"v_cvt_pk_bf16_f32 v60, v60, v61\n"
"v_cvt_pk_bf16_f32 v61, v62, v63\n"
"ds_write_b64 v7, v[60:61] offset:15872\n"
"v_mul_f32 v64, v88, v64\n"
"v_mul_f32 v65, v88, v65\n"
"v_mul_f32 v66, v88, v66\n"
"v_mul_f32 v67, v88, v67\n"
"v_fmac_f32 v64, v89, v20\n"
"v_fmac_f32 v65, v89, v21\n"
"v_fmac_f32 v66, v89, v22\n"
"v_fmac_f32 v67, v89, v23\n"
"v_cvt_pk_bf16_f32 v64, v64, v65\n"
"v_cvt_pk_bf16_f32 v65, v66, v67\n"
"ds_write_b64 v8, v[64:65] offset:15872\n"
"ds_write_b32 v13, v13 offset:96\n"
"s_cmp_lt_i32 s3, 2\n"
"s_cselect_b32 s33, 1, 0\n"
"v_mov_b32 v46, 0\n"
"v_mov_b32 v47, 0\n"
"v_mov_b32 v50, 0\n"
"v_mov_b32 v51, 0\n"
"v_mov_b32 v72, s42\n"
"v_mov_b32 v73, s42\n"
"v_mov_b32 v74, s42\n"
"v_mov_b32 v75, s42\n"
"s_mov_b32 s17, 0x100000\n"
".Ltp_34:\n"
"ds_read_b32 v90, v14 offset:96\n"
"s_sub_u32 s17, s17, 1\n"
"s_cbranch_scc1 .Lou_35\n"
"s_waitcnt lgkmcnt(0)\n"
"v_cmp_eq_u32 vcc, 0, v90\n"
"s_cbranch_vccnz .Ltp_34\n"
".Lou_35:\n"
"s_waitcnt lgkmcnt(0)\n"
"s_mov_b64 exec, 0xffff\n"
"ds_read_b64 v[46:47], v24 offset:4000\n"
"ds_read_b64 v[50:51], v24 offset:4040\n"
"s_mov_b64 exec, -1\n"
"ds_read_b64 v[44:45], v25 offset:3968\n"
"ds_read_b64 v[56:57], v25 offset:80\n"
"ds_read_b64 v[58:59], v24 offset:112\n"
"ds_read_b64 v[64:65], v25 offset:4048\n"
"ds_read_b64 v[66:67], v24 offset:4080\n"
"ds_read_b64 v[48:49], v25 offset:4008\n"
"ds_read_b64 v[60:61], v25 offset:120\n"
"ds_read_b64 v[62:63], v24 offset:152\n"
"ds_read_b64 v[68:69], v25 offset:4088\n"
"ds_read_b64 v[70:71], v24 offset:4120\n"
"s_add_u32 s31, s28, 0x14a000\n"
"s_add_u32 m0, s29, 0\n"
"s_nop 0\n"
"buffer_load_dwordx4 v2, s[8:11], s31 offen offset:0 lds\n"
"ds_read_b64_tr_b16 v[28:29], v26 offset:0\n"
"ds_read_b64_tr_b16 v[30:31], v26 offset:3968\n"
"ds_read_b64_tr_b16 v[32:33], v26 offset:40\n"
"v_mov_b32 v52, 0x0c0c0c0c\n"
"v_mov_b32 v53, 0xc0c0100\n"
"v_mov_b32 v54, 0x1000c0c\n"
"v_cndmask_b32 v53, v53, v54, s[34:35]\n"
"v_cndmask_b32 v80, v53, v52, s[36:37]\n"
"v_cndmask_b32 v82, v52, v53, s[36:37]\n"
"v_mov_b32 v53, 0xc0c0302\n"
"v_mov_b32 v54, 0x3020c0c\n"
"v_cndmask_b32 v53, v53, v54, s[34:35]\n"
"v_cndmask_b32 v81, v53, v52, s[38:39]\n"
"v_cndmask_b32 v83, v52, v53, s[38:39]\n"
"s_waitcnt lgkmcnt(10)\n"
"v_mfma_f32_16x16x32_bf16 v[56:59], v[56:59], v[44:47], v[72:75]\n"
"s_waitcnt lgkmcnt(8)\n"
"v_mfma_f32_16x16x32_bf16 v[64:67], v[64:67], v[44:47], 0\n"
"ds_read_b64_tr_b16 v[34:35], v26 offset:4008\n"
"s_add_u32 m0, s29, 0x3c0\n"
"s_nop 0\n"
"buffer_load_dwordx4 v2, s[8:11], s31 offen offset:64 lds\n"
"ds_read_b64_tr_b16 v[36:37], v26 offset:32\n"
"ds_read_b64_tr_b16 v[38:39], v26 offset:4000\n"
"s_waitcnt lgkmcnt(8)\n"
"v_mfma_f32_16x16x32_bf16 v[60:63], v[60:63], v[48:51], v[72:75]\n"
"s_waitcnt lgkmcnt(6)\n"
"v_mfma_f32_16x16x32_bf16 v[68:71], v[68:71], v[48:51], 0\n"
"ds_read_b64_tr_b16 v[40:41], v26 offset:72\n"
"ds_read_b64_tr_b16 v[42:43], v26 offset:4040\n"
"v_mov_b32 v84, 0x3f803f80\n"
"v_mov_b32 v85, 0x3f803f80\n"
"v_mov_b32 v86, 0x3f803f80\n"
"v_mov_b32 v87, 0x3f803f80\n"
"v_cndmask_b32 v76, v56, v57, s[34:35]\n"
"v_cndmask_b32 v72, v64, v65, s[34:35]\n"
"v_cndmask_b32 v76, v76, v72, s[36:37]\n"
"v_cndmask_b32 v77, v58, v59, s[34:35]\n"
"v_cndmask_b32 v72, v66, v67, s[34:35]\n"
"v_cndmask_b32 v77, v77, v72, s[38:39]\n"
"v_max_f32 v52, v76, v77\n"
"s_add_u32 m0, s29, 0x780\n"
"s_nop 0\n"
"buffer_load_dwordx4 v2, s[8:11], s31 offen offset:128 lds\n"
"v_max_f32 v54, v76, v77\n"
"v_cndmask_b32 v78, v60, v61, s[34:35]\n"
"v_cndmask_b32 v72, v68, v69, s[34:35]\n"
"v_cndmask_b32 v78, v78, v72, s[36:37]\n"
"v_cndmask_b32 v79, v62, v63, s[34:35]\n"
"v_cndmask_b32 v72, v70, v71, s[34:35]\n"
"v_cndmask_b32 v79, v79, v72, s[38:39]\n"
"v_max_f32 v53, v78, v79\n"
"v_max_f32 v55, v78, v79\n"
"v_permlane16_swap_b32 v52, v54\n"
"s_nop 0\n"
"v_permlane16_swap_b32 v53, v55\n"
"v_max_f32 v72, v52, v54\n"
"v_max_f32 v52, v52, v54\n"
"v_max_f32 v73, v53, v55\n"
"v_max_f32 v53, v53, v55\n"
"v_permlane32_swap_b32 v52, v72\n"
"s_nop 0\n"
"v_permlane32_swap_b32 v53, v73\n"
"s_add_u32 m0, s29, 0xb40\n"
"s_nop 0\n"
"buffer_load_dwordx4 v2, s[8:11], s31 offen offset:192 lds\n"
"v_max_f32 v52, v52, v72\n"
"v_max_f32 v53, v53, v73\n"
"v_mul_f32 v52, 0xbea52b5b, v52\n"
"v_mul_f32 v53, 0xbea52b5b, v53\n"
"v_fmamk_f32 v76, v76, 0x3ea52b5b, v52\n"
"v_fmamk_f32 v77, v77, 0x3ea52b5b, v52\n"
"v_fmamk_f32 v78, v78, 0x3ea52b5b, v53\n"
"v_fmamk_f32 v79, v79, 0x3ea52b5b, v53\n"
"v_exp_f32 v76, v76\n"
"v_exp_f32 v77, v77\n"
"v_exp_f32 v78, v78\n"
"v_exp_f32 v79, v79\n"
"v_cvt_pk_bf16_f32 v52, v76, v77\n"
"v_cvt_pk_bf16_f32 v53, v78, v79\n"
"v_perm_b32 v44, v52, v52, v80\n"
"v_perm_b32 v45, v52, v52, v81\n"
"v_perm_b32 v46, v52, v52, v82\n"
"s_add_u32 m0, s29, 0xf00\n"
"s_nop 0\n"
"buffer_load_dwordx4 v2, s[8:11], s31 offen offset:256 lds\n"
"v_perm_b32 v47, v52, v52, v83\n"
"v_perm_b32 v48, v53, v53, v80\n"
"v_perm_b32 v49, v53, v53, v81\n"
"v_perm_b32 v50, v53, v53, v82\n"
"v_perm_b32 v51, v53, v53, v83\n"
"s_waitcnt lgkmcnt(0)\n"
"s_waitcnt vmcnt(5)\n"
"ds_write_b32 v13, v13 offset:112\n"
"v_mfma_f32_16x16x32_bf16 v[56:59], v[84:87], v[44:47], 0\n"
"v_mfma_f32_16x16x32_bf16 v[60:63], v[84:87], v[48:51], 0\n"
"v_mfma_f32_16x16x32_bf16 v[28:31], v[28:31], v[44:47], 0\n"
"v_mfma_f32_16x16x32_bf16 v[32:35], v[32:35], v[48:51], 0\n"
"v_mfma_f32_16x16x32_bf16 v[36:39], v[36:39], v[44:47], 0\n"
"v_mfma_f32_16x16x32_bf16 v[40:43], v[40:43], v[48:51], 0\n"
"v_cndmask_b32 v88, 2.0, v1, s[40:41]\n"
"v_rcp_f32 v52, v56\n"
"v_rcp_f32 v53, v60\n"
"s_mov_b32 s17, 0x100000\n"
".Ltp_36:\n"
"ds_read_b32 v90, v14 offset:112\n"
"s_sub_u32 s17, s17, 1\n"
"s_cbranch_scc1 .Lou_37\n"
"s_waitcnt lgkmcnt(0)\n"
"v_cmp_eq_u32 vcc, 0, v90\n"
"s_cbranch_vccnz .Ltp_36\n"
".Lou_37:\n"
"s_waitcnt lgkmcnt(0)\n"
"ds_read_b128 v[56:59], v3 offset:40960\n"
"s_mov_b32 s31, s30\n"
"v_mul_f32 v28, v52, v28\n"
"v_mul_f32 v29, v52, v29\n"
"v_mul_f32 v30, v52, v30\n"
"v_mul_f32 v31, v52, v31\n"
"v_mul_f32 v32, v53, v32\n"
"ds_read_b128 v[60:63], v3 offset:41984\n"
"v_mul_f32 v33, v53, v33\n"
"v_mul_f32 v34, v53, v34\n"
"v_mul_f32 v35, v53, v35\n"
"v_cvt_pk_bf16_f32 v28, v28, v29\n"
"v_cvt_pk_bf16_f32 v29, v30, v31\n"
"v_cvt_pk_bf16_f32 v30, v32, v33\n"
"ds_read_b128 v[64:67], v3 offset:43008\n"
"v_cvt_pk_bf16_f32 v31, v34, v35\n"
"v_min_u32 v28, -2, v28\n"
"v_min_u32 v29, -2, v29\n"
"v_min_u32 v30, -2, v30\n"
"v_min_u32 v31, -2, v31\n"
"buffer_store_dwordx4 v[28:31], v1, s[4:7], s31 offen offset:0 sc1\n"
"ds_read_b128 v[68:71], v3 offset:44032\n"
"v_mul_f32 v36, v52, v36\n"
"v_mul_f32 v37, v52, v37\n"
"v_mul_f32 v38, v52, v38\n"
"v_mul_f32 v39, v52, v39\n"
"v_mul_f32 v40, v53, v40\n"
"v_mul_f32 v41, v53, v41\n"
"ds_read_b128 v[72:75], v3 offset:45056\n"
"v_mul_f32 v42, v53, v42\n"
"v_mul_f32 v43, v53, v43\n"
"v_cvt_pk_bf16_f32 v36, v36, v37\n"
"v_cvt_pk_bf16_f32 v37, v38, v39\n"
"v_cvt_pk_bf16_f32 v38, v40, v41\n"
"v_cvt_pk_bf16_f32 v39, v42, v43\n"
"v_min_u32 v36, -2, v36\n"
"v_min_u32 v37, -2, v37\n"
"v_min_u32 v38, -2, v38\n"
"v_min_u32 v39, -2, v39\n"
"buffer_store_dwordx4 v[36:39], v88, s[4:7], s31 offen offset:1024 sc1\n"
"ds_write_b32 v13, v13 offset:128\n"
"s_mul_i32 s79, s43, 0x1400\n"
"v_lshrrev_b32 v92, 2, v0\n"
"v_and_b32 v91, 3, v0\n"
"v_lshlrev_b32 v91, 4, v91\n"
"v_lshl_add_u32 v92, v92, 10, v91\n"
"v_add_u32 v92, s79, v92\n"
"v_mov_b32 v91, 0x40000000\n"
"v_cmp_gt_i32 vcc, 20, v0\n"
"s_nop 1\n"
"v_cndmask_b32 v93, v91, v92, vcc\n"
"s_waitcnt lgkmcnt(4)\n"
"v_mfma_f32_16x16x32_bf16 v[28:31], v[96:99], v[56:59], 0\n"
"ds_read_b128 v[76:79], v3 offset:46080\n"
"s_add_u32 s31, s28, 0x14f000\n"
"v_mfma_f32_16x16x32_bf16 v[32:35], v[100:103], v[56:59], 0\n"
"s_add_u32 m0, s29, 0x5000\n"
"v_mfma_f32_16x16x32_bf16 v[52:55], v[56:59], v[56:59], 0\n"
"v_mfma_f32_16x16x32_bf16 v[28:31], v[104:107], v[60:63], v[28:31]\n"
"ds_read_b128 v[56:59], v3 offset:47104\n"
"buffer_load_dwordx4 v2, s[8:11], s31 offen offset:0 lds\n"
"v_mfma_f32_16x16x32_bf16 v[32:35], v[108:111], v[60:63], v[32:35]\n"
"s_add_u32 m0, s29, 0x53c0\n"
"s_waitcnt lgkmcnt(4)\n"
"v_mfma_f32_16x16x32_bf16 v[28:31], v[112:115], v[64:67], v[28:31]\n"
"ds_read_b128 v[60:63], v3 offset:48128\n"
"v_mfma_f32_16x16x32_bf16 v[32:35], v[116:119], v[64:67], v[32:35]\n"
"v_mfma_f32_16x16x32_bf16 v[28:31], v[120:123], v[68:71], v[28:31]\n"
"ds_read_b128 v[64:67], v3 offset:49152\n"
"buffer_load_dwordx4 v2, s[8:11], s31 offen offset:64 lds\n"
"v_mfma_f32_16x16x32_bf16 v[32:35], v[124:127], v[68:71], v[32:35]\n"
"s_add_u32 m0, s29, 0x5780\n"
"s_waitcnt lgkmcnt(3)\n"
"v_mfma_f32_16x16x32_bf16 v[28:31], v[128:131], v[72:75], v[28:31]\n"
"ds_read_b128 v[68:71], v3 offset:50176\n"
"v_mfma_f32_16x16x32_bf16 v[32:35], v[132:135], v[72:75], v[32:35]\n"
"v_mfma_f32_16x16x32_bf16 v[52:55], v[72:75], v[72:75], v[52:55]\n"
"buffer_load_dwordx4 v2, s[8:11], s31 offen offset:128 lds\n"
"v_mfma_f32_16x16x32_bf16 v[28:31], v[136:139], v[76:79], v[28:31]\n"
"ds_read_b128 v[72:75], v3 offset:51200\n"
"s_add_u32 m0, s29, 0x5b40\n"
"v_mfma_f32_16x16x32_bf16 v[32:35], v[140:143], v[76:79], v[32:35]\n"
"s_waitcnt lgkmcnt(3)\n"
"v_mfma_f32_16x16x32_bf16 v[28:31], v[144:147], v[56:59], v[28:31]\n"
"ds_read_b128 v[76:79], v3 offset:52224\n"
"v_mfma_f32_16x16x32_bf16 v[32:35], v[148:151], v[56:59], v[32:35]\n"
"buffer_load_dwordx4 v2, s[8:11], s31 offen offset:192 lds\n"
"v_mfma_f32_16x16x32_bf16 v[28:31], v[152:155], v[60:63], v[28:31]\n"
"ds_read_b128 v[56:59], v3 offset:53248\n"
"s_add_u32 m0, s29, 0x5f00\n"
"v_mfma_f32_16x16x32_bf16 v[32:35], v[156:159], v[60:63], v[32:35]\n"
"s_waitcnt lgkmcnt(3)\n"
"v_mfma_f32_16x16x32_bf16 v[28:31], v[160:163], v[64:67], v[28:31]\n"
"ds_read_b128 v[60:63], v3 offset:54272\n"
"v_mfma_f32_16x16x32_bf16 v[32:35], v[164:167], v[64:67], v[32:35]\n"
"buffer_load_dwordx4 v2, s[8:11], s31 offen offset:256 lds\n"
"v_mfma_f32_16x16x32_bf16 v[52:55], v[64:67], v[64:67], v[52:55]\n"
"v_mfma_f32_16x16x32_bf16 v[28:31], v[168:171], v[68:71], v[28:31]\n"
"ds_read_b128 v[64:67], v3 offset:55296\n"
"v_mfma_f32_16x16x32_bf16 v[32:35], v[172:175], v[68:71], v[32:35]\n"
"s_waitcnt lgkmcnt(3)\n"
"v_mfma_f32_16x16x32_bf16 v[28:31], v[176:179], v[72:75], v[28:31]\n"
"ds_read_b128 v[68:71], v3 offset:56320\n"
"v_mfma_f32_16x16x32_bf16 v[32:35], v[180:183], v[72:75], v[32:35]\n"
"v_mfma_f32_16x16x32_bf16 v[28:31], v[184:187], v[76:79], v[28:31]\n"
"ds_read_b128 v[72:75], v3 offset:57344\n"
"v_mfma_f32_16x16x32_bf16 v[32:35], v[188:191], v[76:79], v[32:35]\n"
"s_waitcnt lgkmcnt(3)\n"
"v_mfma_f32_16x16x32_bf16 v[28:31], v[192:195], v[56:59], v[28:31]\n"
"ds_read_b128 v[76:79], v4 offset:40960\n"
"v_mfma_f32_16x16x32_bf16 v[32:35], v[196:199], v[56:59], v[32:35]\n"
"v_mfma_f32_16x16x32_bf16 v[52:55], v[56:59], v[56:59], v[52:55]\n"
"v_mfma_f32_16x16x32_bf16 v[28:31], v[200:203], v[60:63], v[28:31]\n"
"ds_read_b128 v[56:59], v5 offset:40960\n"
"v_mfma_f32_16x16x32_bf16 v[32:35], v[204:207], v[60:63], v[32:35]\n"
"s_waitcnt lgkmcnt(3)\n"
"v_mfma_f32_16x16x32_bf16 v[28:31], v[208:211], v[64:67], v[28:31]\n"
"ds_read_b128 v[60:63], v6 offset:40960\n"
"v_mfma_f32_16x16x32_bf16 v[32:35], v[212:215], v[64:67], v[32:35]\n"
"v_mfma_f32_16x16x32_bf16 v[28:31], v[216:219], v[68:71], v[28:31]\n"
"v_add_u32 v3, 0x5000, v3\n"
"ds_read_b128 v[64:67], v3 offset:40960\n"
"v_mfma_f32_16x16x32_bf16 v[32:35], v[220:223], v[68:71], v[32:35]\n"
"s_waitcnt lgkmcnt(3)\n"
"v_mfma_f32_16x16x32_bf16 v[28:31], v[224:227], v[72:75], v[28:31]\n"
"ds_read_b128 v[68:71], v3 offset:41984\n"
"v_mfma_f32_16x16x32_bf16 v[32:35], v[228:231], v[72:75], v[32:35]\n"
"v_mfma_f32_16x16x32_bf16 v[52:55], v[72:75], v[72:75], v[52:55]\n"
"v_mfma_f32_16x16x32_bf16 v[28:31], v[232:235], v[76:79], v[28:31]\n"
"ds_read_b128 v[72:75], v3 offset:43008\n"
"v_mfma_f32_16x16x32_bf16 v[32:35], v[236:239], v[76:79], v[32:35]\n"
"s_waitcnt lgkmcnt(3)\n"
"v_mfma_f32_16x16x32_bf16 v[28:31], v[240:243], v[56:59], v[28:31]\n"
"ds_read_b128 v[76:79], v3 offset:44032\n"
"v_cndmask_b32 v94, v52, v53, s[34:35]\n"
"v_mfma_f32_16x16x32_bf16 v[32:35], v[244:247], v[56:59], v[32:35]\n"
"v_cndmask_b32 v54, v54, v55, s[34:35]\n"
"v_cndmask_b32 v94, v94, v54, s[20:21]\n"
"v_mfma_f32_16x16x32_bf16 v[28:31], v[248:251], v[60:63], v[28:31]\n"
"ds_read_b128 v[56:59], v3 offset:45056\n"
"v_mfma_f32_16x16x32_bf16 v[32:35], v[252:255], v[60:63], v[32:35]\n"
"s_waitcnt lgkmcnt(3)\n"
"v_mfma_f32_16x16x32_bf16 v[36:39], v[96:99], v[64:67], 0\n"
"ds_read_b128 v[60:63], v3 offset:46080\n"
"v_mfma_f32_16x16x32_bf16 v[40:43], v[100:103], v[64:67], 0\n"
"v_cndmask_b32 v94, v28, v94, s[22:23]\n"
"s_waitcnt vmcnt(7)\n"
"v_mfma_f32_16x16x32_bf16 v[52:55], v[64:67], v[64:67], 0\n"
"ds_write_b32 v9, v94 offset:0\n"
"v_mfma_f32_16x16x32_bf16 v[36:39], v[104:107], v[68:71], v[36:39]\n"
"ds_read_b128 v[64:67], v3 offset:47104\n"
"v_mfma_f32_16x16x32_bf16 v[40:43], v[108:111], v[68:71], v[40:43]\n"
"s_waitcnt lgkmcnt(4)\n"
"v_mfma_f32_16x16x32_bf16 v[36:39], v[112:115], v[72:75], v[36:39]\n"
"ds_read_b128 v[68:71], v3 offset:48128\n"
"v_mfma_f32_16x16x32_bf16 v[40:43], v[116:119], v[72:75], v[40:43]\n"
"v_mfma_f32_16x16x32_bf16 v[36:39], v[120:123], v[76:79], v[36:39]\n"
"ds_read_b128 v[72:75], v3 offset:49152\n"
"v_mfma_f32_16x16x32_bf16 v[40:43], v[124:127], v[76:79], v[40:43]\n"
"s_waitcnt lgkmcnt(4)\n"
"v_mfma_f32_16x16x32_bf16 v[36:39], v[128:131], v[56:59], v[36:39]\n"
"ds_read_b128 v[76:79], v3 offset:50176\n"
"v_mfma_f32_16x16x32_bf16 v[40:43], v[132:135], v[56:59], v[40:43]\n"
"v_mfma_f32_16x16x32_bf16 v[52:55], v[56:59], v[56:59], v[52:55]\n"
"v_mfma_f32_16x16x32_bf16 v[36:39], v[136:139], v[60:63], v[36:39]\n"
"ds_read_b128 v[56:59], v3 offset:51200\n"
"v_mfma_f32_16x16x32_bf16 v[40:43], v[140:143], v[60:63], v[40:43]\n"
"s_waitcnt lgkmcnt(3)\n"
"v_mfma_f32_16x16x32_bf16 v[36:39], v[144:147], v[64:67], v[36:39]\n"
"ds_read_b128 v[60:63], v3 offset:52224\n"
"ds_read_b128 v[84:87], v11 offset:0\n"
"ds_read_b32 v91, v12 offset:0\n"
"v_mfma_f32_16x16x32_bf16 v[40:43], v[148:151], v[64:67], v[40:43]\n"
"v_mfma_f32_16x16x32_bf16 v[36:39], v[152:155], v[68:71], v[36:39]\n"
"ds_read_b128 v[64:67], v3 offset:53248\n"
"v_mfma_f32_16x16x32_bf16 v[40:43], v[156:159], v[68:71], v[40:43]\n"
"s_waitcnt lgkmcnt(5)\n"
"v_mfma_f32_16x16x32_bf16 v[36:39], v[160:163], v[72:75], v[36:39]\n"
"ds_read_b128 v[68:71], v3 offset:54272\n"
"v_mfma_f32_16x16x32_bf16 v[40:43], v[164:167], v[72:75], v[40:43]\n"
"v_mfma_f32_16x16x32_bf16 v[52:55], v[72:75], v[72:75], v[52:55]\n"
"v_mfma_f32_16x16x32_bf16 v[36:39], v[168:171], v[76:79], v[36:39]\n"
"ds_read_b128 v[72:75], v3 offset:55296\n"
"s_waitcnt lgkmcnt(4)\n"
"v_add_f32 v84, v84, v85\n"
"v_mfma_f32_16x16x32_bf16 v[40:43], v[172:175], v[76:79], v[40:43]\n"
"v_add_f32 v86, v86, v87\n"
"v_mfma_f32_16x16x32_bf16 v[36:39], v[176:179], v[56:59], v[36:39]\n"
"ds_read_b128 v[76:79], v3 offset:56320\n"
"v_add_f32 v84, v84, v86\n"
"v_mfma_f32_16x16x32_bf16 v[40:43], v[180:183], v[56:59], v[40:43]\n"
"s_waitcnt lgkmcnt(4)\n"
"v_mul_f32 v89, 0x3acccccd, v91\n"
"v_fma_f32 v84, -v91, v89, v84\n"
"v_mfma_f32_16x16x32_bf16 v[36:39], v[184:187], v[60:63], v[36:39]\n"
"ds_read_b128 v[56:59], v3 offset:57344\n"
"v_fmamk_f32 v84, v84, 0x3acccccd, v27\n"
"v_mfma_f32_16x16x32_bf16 v[40:43], v[188:191], v[60:63], v[40:43]\n"
"v_cmp_u_f32 vcc, v84, v84\n"
"s_waitcnt lgkmcnt(3)\n"
"v_mfma_f32_16x16x32_bf16 v[36:39], v[192:195], v[64:67], v[36:39]\n"
"ds_read_b128 v[60:63], v4 offset:61440\n"
"v_max_f32 v84, v84, v27\n"
"v_mfma_f32_16x16x32_bf16 v[40:43], v[196:199], v[64:67], v[40:43]\n"
"v_rsq_f32 v88, v84\n"
"v_mfma_f32_16x16x32_bf16 v[52:55], v[64:67], v[64:67], v[52:55]\n"
"v_mul_f32 v89, -v89, v88\n"
"v_mfma_f32_16x16x32_bf16 v[36:39], v[200:203], v[68:71], v[36:39]\n"
"ds_read_b128 v[64:67], v5 offset:61440\n"
"s_cbranch_vccnz .Lsl_38\n"
".Lbk_39:\n"
"v_mfma_f32_16x16x32_bf16 v[40:43], v[204:207], v[68:71], v[40:43]\n"
"s_add_u32 s31, s28, 0x13b000\n"
"s_waitcnt lgkmcnt(3)\n"
"v_mfma_f32_16x16x32_bf16 v[36:39], v[208:211], v[72:75], v[36:39]\n"
"ds_read_b128 v[68:71], v6 offset:61440\n"
"s_add_u32 m0, s29, 0xa000\n"
"v_mfma_f32_16x16x32_bf16 v[40:43], v[212:215], v[72:75], v[40:43]\n"
"v_mul_f32 v28, v88, v28\n"
"v_mul_f32 v29, v88, v29\n"
"v_mfma_f32_16x16x32_bf16 v[36:39], v[216:219], v[76:79], v[36:39]\n"
"v_add_u32 v3, 0xffffb000, v3\n"
"ds_read_b128 v[72:75], v3 offset:0\n"
"buffer_load_dwordx4 v2, s[8:11], s31 offen offset:0 lds\n"
"v_mfma_f32_16x16x32_bf16 v[40:43], v[220:223], v[76:79], v[40:43]\n"
"s_add_u32 m0, s29, 0xa3c0\n"
"s_waitcnt lgkmcnt(3)\n"
"v_mfma_f32_16x16x32_bf16 v[36:39], v[224:227], v[56:59], v[36:39]\n"
"ds_read_b128 v[76:79], v3 offset:1024\n"
"v_mul_f32 v30, v88, v30\n"
"v_mfma_f32_16x16x32_bf16 v[40:43], v[228:231], v[56:59], v[40:43]\n"
"v_mul_f32 v31, v88, v31\n"
"v_fmac_f32 v28, v89, v16\n"
"v_mfma_f32_16x16x32_bf16 v[52:55], v[56:59], v[56:59], v[52:55]\n"
"buffer_load_dwordx4 v2, s[8:11], s31 offen offset:64 lds\n"
"v_fmac_f32 v29, v89, v17\n"
"v_mfma_f32_16x16x32_bf16 v[36:39], v[232:235], v[60:63], v[36:39]\n"
"ds_read_b128 v[56:59], v3 offset:2048\n"
"s_add_u32 m0, s29, 0xa780\n"
"v_mfma_f32_16x16x32_bf16 v[40:43], v[236:239], v[60:63], v[40:43]\n"
"v_fmac_f32 v30, v89, v18\n"
"s_waitcnt lgkmcnt(3)\n"
"v_mfma_f32_16x16x32_bf16 v[36:39], v[240:243], v[64:67], v[36:39]\n"
"ds_read_b128 v[60:63], v3 offset:3072\n"
"v_cndmask_b32 v94, v52, v53, s[34:35]\n"
"v_mfma_f32_16x16x32_bf16 v[40:43], v[244:247], v[64:67], v[40:43]\n"
"v_cndmask_b32 v54, v54, v55, s[34:35]\n"
"v_cndmask_b32 v94, v94, v54, s[20:21]\n"
"buffer_load_dwordx4 v2, s[8:11], s31 offen offset:128 lds\n"
"v_mfma_f32_16x16x32_bf16 v[36:39], v[248:251], v[68:71], v[36:39]\n"
"ds_read_b128 v[64:67], v3 offset:4096\n"
"s_add_u32 m0, s29, 0xab40\n"
"v_mfma_f32_16x16x32_bf16 v[40:43], v[252:255], v[68:71], v[40:43]\n"
"v_fmac_f32 v31, v89, v19\n"
"s_waitcnt lgkmcnt(3)\n"
"v_mfma_f32_16x16x32_bf16 v[44:47], v[96:99], v[72:75], 0\n"
"ds_read_b128 v[68:71], v3 offset:5120\n"
"v_cvt_pk_bf16_f32 v28, v28, v29\n"
"v_mfma_f32_16x16x32_bf16 v[48:51], v[100:103], v[72:75], 0\n"
"v_cndmask_b32 v94, v36, v94, s[22:23]\n"
"s_waitcnt vmcnt(3)\n"
"buffer_load_dwordx4 v2, s[8:11], s31 offen offset:192 lds\n"
"v_mfma_f32_16x16x32_bf16 v[52:55], v[72:75], v[72:75], 0\n"
"ds_write_b32 v9, v94 offset:320\n"
"v_mfma_f32_16x16x32_bf16 v[44:47], v[104:107], v[76:79], v[44:47]\n"
"ds_read_b128 v[72:75], v3 offset:6144\n"
"s_add_u32 m0, s29, 0xaf00\n"
"v_mfma_f32_16x16x32_bf16 v[48:51], v[108:111], v[76:79], v[48:51]\n"
"v_cvt_pk_bf16_f32 v29, v30, v31\n"
"s_waitcnt lgkmcnt(4)\n"
"v_mfma_f32_16x16x32_bf16 v[44:47], v[112:115], v[56:59], v[44:47]\n"
"ds_read_b128 v[76:79], v3 offset:7168\n"
"buffer_load_dwordx4 v2, s[8:11], s31 offen offset:256 lds\n"
"v_mfma_f32_16x16x32_bf16 v[48:51], v[116:119], v[56:59], v[48:51]\n"
"v_mul_f32 v32, v88, v32\n"
"v_mul_f32 v33, v88, v33\n"
"v_mfma_f32_16x16x32_bf16 v[44:47], v[120:123], v[60:63], v[44:47]\n"
"ds_read_b128 v[56:59], v3 offset:8192\n"
"v_mul_f32 v34, v88, v34\n"
"v_mfma_f32_16x16x32_bf16 v[48:51], v[124:127], v[60:63], v[48:51]\n"
"v_mul_f32 v35, v88, v35\n"
"s_waitcnt lgkmcnt(4)\n"
"v_mfma_f32_16x16x32_bf16 v[44:47], v[128:131], v[64:67], v[44:47]\n"
"ds_read_b128 v[60:63], v3 offset:9216\n"
"v_fmac_f32 v32, v89, v20\n"
"v_mfma_f32_16x16x32_bf16 v[48:51], v[132:135], v[64:67], v[48:51]\n"
"v_fmac_f32 v33, v89, v21\n"
"v_fmac_f32 v34, v89, v22\n"
"v_mfma_f32_16x16x32_bf16 v[52:55], v[64:67], v[64:67], v[52:55]\n"
"v_fmac_f32 v35, v89, v23\n"
"v_cvt_pk_bf16_f32 v32, v32, v33\n"
"v_mfma_f32_16x16x32_bf16 v[44:47], v[136:139], v[68:71], v[44:47]\n"
"ds_read_b128 v[64:67], v3 offset:10240\n"
"v_cvt_pk_bf16_f32 v33, v34, v35\n"
"v_mfma_f32_16x16x32_bf16 v[48:51], v[140:143], v[68:71], v[48:51]\n"
"s_waitcnt lgkmcnt(3)\n"
"v_mfma_f32_16x16x32_bf16 v[44:47], v[144:147], v[72:75], v[44:47]\n"
"ds_read_b128 v[68:71], v3 offset:11264\n"
"ds_read_b128 v[84:87], v11 offset:320\n"
"ds_read_b32 v91, v12 offset:320\n"
"v_mfma_f32_16x16x32_bf16 v[48:51], v[148:151], v[72:75], v[48:51]\n"
"v_mfma_f32_16x16x32_bf16 v[44:47], v[152:155], v[76:79], v[44:47]\n"
"ds_read_b128 v[72:75], v3 offset:12288\n"
"v_mfma_f32_16x16x32_bf16 v[48:51], v[156:159], v[76:79], v[48:51]\n"
"s_waitcnt lgkmcnt(5)\n"
"v_mfma_f32_16x16x32_bf16 v[44:47], v[160:163], v[56:59], v[44:47]\n"
"ds_read_b128 v[76:79], v3 offset:13312\n"
"v_mfma_f32_16x16x32_bf16 v[48:51], v[164:167], v[56:59], v[48:51]\n"
"v_mfma_f32_16x16x32_bf16 v[52:55], v[56:59], v[56:59], v[52:55]\n"
"v_mfma_f32_16x16x32_bf16 v[44:47], v[168:171], v[60:63], v[44:47]\n"
"ds_read_b128 v[56:59], v3 offset:14336\n"
"s_waitcnt lgkmcnt(4)\n"
"v_add_f32 v84, v84, v85\n"
"v_mfma_f32_16x16x32_bf16 v[48:51], v[172:175], v[60:63], v[48:51]\n"
"v_add_f32 v86, v86, v87\n"
"v_mfma_f32_16x16x32_bf16 v[44:47], v[176:179], v[64:67], v[44:47]\n"
"ds_read_b128 v[60:63], v3 offset:15360\n"
"v_add_f32 v84, v84, v86\n"
"v_mfma_f32_16x16x32_bf16 v[48:51], v[180:183], v[64:67], v[48:51]\n"
"s_waitcnt lgkmcnt(4)\n"
"v_mul_f32 v89, 0x3acccccd, v91\n"
"v_fma_f32 v84, -v91, v89, v84\n"
"v_mfma_f32_16x16x32_bf16 v[44:47], v[184:187], v[68:71], v[44:47]\n"
"ds_read_b128 v[64:67], v3 offset:16384\n"
"v_fmamk_f32 v84, v84, 0x3acccccd, v27\n"
"v_mfma_f32_16x16x32_bf16 v[48:51], v[188:191], v[68:71], v[48:51]\n"
"v_cmp_u_f32 vcc, v84, v84\n"
"s_waitcnt lgkmcnt(3)\n"
"v_mfma_f32_16x16x32_bf16 v[44:47], v[192:195], v[72:75], v[44:47]\n"
"ds_read_b128 v[68:71], v4 offset:0\n"
"v_max_f32 v84, v84, v27\n"
"v_mfma_f32_16x16x32_bf16 v[48:51], v[196:199], v[72:75], v[48:51]\n"
"v_rsq_f32 v88, v84\n"
"v_mfma_f32_16x16x32_bf16 v[52:55], v[72:75], v[72:75], v[52:55]\n"
"v_mul_f32 v89, -v89, v88\n"
"v_mfma_f32_16x16x32_bf16 v[44:47], v[200:203], v[76:79], v[44:47]\n"
"ds_read_b128 v[72:75], v5 offset:0\n"
"s_cbranch_vccnz .Lsl_42\n"
".Lbk_43:\n"
"v_mfma_f32_16x16x32_bf16 v[48:51], v[204:207], v[76:79], v[48:51]\n"
"ds_write_b64 v7, v[28:29] offset:44928\n"
"s_waitcnt lgkmcnt(4)\n"
"v_mfma_f32_16x16x32_bf16 v[44:47], v[208:211], v[56:59], v[44:47]\n"
"ds_read_b128 v[76:79], v6 offset:0\n"
"v_mul_f32 v36, v88, v36\n"
"v_mfma_f32_16x16x32_bf16 v[48:51], v[212:215], v[56:59], v[48:51]\n"
"v_mul_f32 v37, v88, v37\n"
"v_mul_f32 v38, v88, v38\n"
"v_mfma_f32_16x16x32_bf16 v[44:47], v[216:219], v[60:63], v[44:47]\n"
"ds_read_b128 v[56:59], v3 offset:20480\n"
"v_mul_f32 v39, v88, v39\n"
"v_mfma_f32_16x16x32_bf16 v[48:51], v[220:223], v[60:63], v[48:51]\n"
"v_fmac_f32 v36, v89, v16\n"
"s_waitcnt lgkmcnt(4)\n"
"v_mfma_f32_16x16x32_bf16 v[44:47], v[224:227], v[64:67], v[44:47]\n"
"ds_read_b128 v[60:63], v3 offset:21504\n"
"v_fmac_f32 v37, v89, v17\n"
"v_mfma_f32_16x16x32_bf16 v[48:51], v[228:231], v[64:67], v[48:51]\n"
"v_mov_b32 v84, 0x3f803f80\n"
"v_mov_b32 v85, 0x3f803f80\n"
"v_mfma_f32_16x16x32_bf16 v[52:55], v[64:67], v[64:67], v[52:55]\n"
"v_mov_b32 v86, 0x3f803f80\n"
"v_mov_b32 v87, 0x3f803f80\n"
"ds_write_b64 v8, v[32:33] offset:44928\n"
"v_mfma_f32_16x16x32_bf16 v[44:47], v[232:235], v[68:71], v[44:47]\n"
"ds_read_b128 v[64:67], v3 offset:22528\n"
"v_fmac_f32 v38, v89, v18\n"
"v_mfma_f32_16x16x32_bf16 v[48:51], v[236:239], v[68:71], v[48:51]\n"
"v_fmac_f32 v39, v89, v19\n"
"s_waitcnt lgkmcnt(4)\n"
"v_mfma_f32_16x16x32_bf16 v[44:47], v[240:243], v[72:75], v[44:47]\n"
"ds_read_b128 v[68:71], v3 offset:23552\n"
"v_cndmask_b32 v94, v52, v53, s[34:35]\n"
"v_mfma_f32_16x16x32_bf16 v[48:51], v[244:247], v[72:75], v[48:51]\n"
"v_cndmask_b32 v54, v54, v55, s[34:35]\n"
"v_cndmask_b32 v94, v94, v54, s[20:21]\n"
"v_mfma_f32_16x16x32_bf16 v[44:47], v[248:251], v[76:79], v[44:47]\n"
"ds_read_b128 v[72:75], v3 offset:24576\n"
"v_cvt_pk_bf16_f32 v36, v36, v37\n"
"v_mfma_f32_16x16x32_bf16 v[48:51], v[252:255], v[76:79], v[48:51]\n"
"v_cvt_pk_bf16_f32 v37, v38, v39\n"
"s_waitcnt lgkmcnt(4)\n"
"v_mfma_f32_16x16x32_bf16 v[28:31], v[96:99], v[56:59], 0\n"
"ds_read_b128 v[76:79], v3 offset:25600\n"
"v_mul_f32 v40, v88, v40\n"
"v_mfma_f32_16x16x32_bf16 v[32:35], v[100:103], v[56:59], 0\n"
"v_cndmask_b32 v94, v44, v94, s[22:23]\n"
"s_waitcnt vmcnt(0)\n"
"ds_write_b128 v93, v[84:87] offset:40960\n"
"v_mfma_f32_16x16x32_bf16 v[52:55], v[56:59], v[56:59], 0\n"
"ds_write_b32 v9, v94 offset:640\n"
"v_mfma_f32_16x16x32_bf16 v[28:31], v[104:107], v[60:63], v[28:31]\n"
"ds_read_b128 v[56:59], v3 offset:26624\n"
"v_mul_f32 v41, v88, v41\n"
"v_mfma_f32_16x16x32_bf16 v[32:35], v[108:111], v[60:63], v[32:35]\n"
"v_mul_f32 v42, v88, v42\n"
"s_waitcnt lgkmcnt(5)\n"
"v_mfma_f32_16x16x32_bf16 v[28:31], v[112:115], v[64:67], v[28:31]\n"
"ds_read_b128 v[60:63], v3 offset:27648\n"
"v_mul_f32 v43, v88, v43\n"
"v_mfma_f32_16x16x32_bf16 v[32:35], v[116:119], v[64:67], v[32:35]\n"
"v_fmac_f32 v40, v89, v20\n"
"v_fmac_f32 v41, v89, v21\n"
"v_mfma_f32_16x16x32_bf16 v[28:31], v[120:123], v[68:71], v[28:31]\n"
"ds_read_b128 v[64:67], v3 offset:28672\n"
"v_fmac_f32 v42, v89, v22\n"
"v_mfma_f32_16x16x32_bf16 v[32:35], v[124:127], v[68:71], v[32:35]\n"
"v_fmac_f32 v43, v89, v23\n"
"s_waitcnt lgkmcnt(5)\n"
"v_mfma_f32_16x16x32_bf16 v[28:31], v[128:131], v[72:75], v[28:31]\n"
"ds_read_b128 v[68:71], v3 offset:29696\n"
"v_cvt_pk_bf16_f32 v40, v40, v41\n"
"v_mfma_f32_16x16x32_bf16 v[32:35], v[132:135], v[72:75], v[32:35]\n"
"v_cvt_pk_bf16_f32 v41, v42, v43\n"
"v_mfma_f32_16x16x32_bf16 v[52:55], v[72:75], v[72:75], v[52:55]\n"
"v_mfma_f32_16x16x32_bf16 v[28:31], v[136:139], v[76:79], v[28:31]\n"
"ds_read_b128 v[72:75], v3 offset:30720\n"
"v_mfma_f32_16x16x32_bf16 v[32:35], v[140:143], v[76:79], v[32:35]\n"
"s_waitcnt lgkmcnt(3)\n"
"v_mfma_f32_16x16x32_bf16 v[28:31], v[144:147], v[56:59], v[28:31]\n"
"ds_read_b128 v[76:79], v3 offset:31744\n"
"ds_read_b128 v[84:87], v11 offset:640\n"
"ds_read_b32 v91, v12 offset:640\n"
"v_mfma_f32_16x16x32_bf16 v[32:35], v[148:151], v[56:59], v[32:35]\n"
"v_mfma_f32_16x16x32_bf16 v[28:31], v[152:155], v[60:63], v[28:31]\n"
"ds_read_b128 v[56:59], v3 offset:32768\n"
"v_mfma_f32_16x16x32_bf16 v[32:35], v[156:159], v[60:63], v[32:35]\n"
"s_waitcnt lgkmcnt(5)\n"
"v_mfma_f32_16x16x32_bf16 v[28:31], v[160:163], v[64:67], v[28:31]\n"
"ds_read_b128 v[60:63], v3 offset:33792\n"
"v_mfma_f32_16x16x32_bf16 v[32:35], v[164:167], v[64:67], v[32:35]\n"
"v_mfma_f32_16x16x32_bf16 v[52:55], v[64:67], v[64:67], v[52:55]\n"
"v_mfma_f32_16x16x32_bf16 v[28:31], v[168:171], v[68:71], v[28:31]\n"
"ds_read_b128 v[64:67], v3 offset:34816\n"
"s_waitcnt lgkmcnt(4)\n"
"v_add_f32 v84, v84, v85\n"
"v_mfma_f32_16x16x32_bf16 v[32:35], v[172:175], v[68:71], v[32:35]\n"
"v_add_f32 v86, v86, v87\n"
"v_mfma_f32_16x16x32_bf16 v[28:31], v[176:179], v[72:75], v[28:31]\n"
"ds_read_b128 v[68:71], v3 offset:35840\n"
"v_add_f32 v84, v84, v86\n"
"v_mfma_f32_16x16x32_bf16 v[32:35], v[180:183], v[72:75], v[32:35]\n"
"s_waitcnt lgkmcnt(4)\n"
"v_mul_f32 v89, 0x3acccccd, v91\n"
"v_fma_f32 v84, -v91, v89, v84\n"
"v_mfma_f32_16x16x32_bf16 v[28:31], v[184:187], v[76:79], v[28:31]\n"
"ds_read_b128 v[72:75], v3 offset:36864\n"
"v_fmamk_f32 v84, v84, 0x3acccccd, v27\n"
"v_mfma_f32_16x16x32_bf16 v[32:35], v[188:191], v[76:79], v[32:35]\n"
"v_cmp_u_f32 vcc, v84, v84\n"
"s_waitcnt lgkmcnt(3)\n"
"v_mfma_f32_16x16x32_bf16 v[28:31], v[192:195], v[56:59], v[28:31]\n"
"ds_read_b128 v[76:79], v4 offset:20480\n"
"v_max_f32 v84, v84, v27\n"
"v_mfma_f32_16x16x32_bf16 v[32:35], v[196:199], v[56:59], v[32:35]\n"
"v_rsq_f32 v88, v84\n"
"v_mfma_f32_16x16x32_bf16 v[52:55], v[56:59], v[56:59], v[52:55]\n"
"v_mul_f32 v89, -v89, v88\n"
"v_mfma_f32_16x16x32_bf16 v[28:31], v[200:203], v[60:63], v[28:31]\n"
"ds_read_b128 v[56:59], v5 offset:20480\n"
"s_cbranch_vccnz .Lsl_46\n"
".Lbk_47:\n"
"v_mfma_f32_16x16x32_bf16 v[32:35], v[204:207], v[60:63], v[32:35]\n"
"ds_write_b64 v7, v[36:37] offset:48896\n"
"s_waitcnt lgkmcnt(4)\n"
"v_mfma_f32_16x16x32_bf16 v[28:31], v[208:211], v[64:67], v[28:31]\n"
"ds_read_b128 v[60:63], v6 offset:20480\n"
"v_mul_f32 v44, v88, v44\n"
"v_mul_f32 v45, v88, v45\n"
"v_mfma_f32_16x16x32_bf16 v[32:35], v[212:215], v[64:67], v[32:35]\n"
"v_mul_f32 v46, v88, v46\n"
"v_mul_f32 v47, v88, v47\n"
"v_fmac_f32 v44, v89, v16\n"
"v_mfma_f32_16x16x32_bf16 v[28:31], v[216:219], v[68:71], v[28:31]\n"
"ds_read_b128 v[64:67], v3 offset:40960\n"
"v_fmac_f32 v45, v89, v17\n"
"v_fmac_f32 v46, v89, v18\n"
"v_mfma_f32_16x16x32_bf16 v[32:35], v[220:223], v[68:71], v[32:35]\n"
"v_fmac_f32 v47, v89, v19\n"
"s_waitcnt lgkmcnt(4)\n"
"v_mfma_f32_16x16x32_bf16 v[28:31], v[224:227], v[72:75], v[28:31]\n"
"ds_read_b128 v[68:71], v3 offset:45056\n"
"v_cvt_pk_bf16_f32 v44, v44, v45\n"
"v_mfma_f32_16x16x32_bf16 v[32:35], v[228:231], v[72:75], v[32:35]\n"
"v_cvt_pk_bf16_f32 v45, v46, v47\n"
"v_mul_f32 v48, v88, v48\n"
"v_mfma_f32_16x16x32_bf16 v[52:55], v[72:75], v[72:75], v[52:55]\n"
"ds_write_b64 v8, v[40:41] offset:48896\n"
"v_mfma_f32_16x16x32_bf16 v[28:31], v[232:235], v[76:79], v[28:31]\n"
"ds_read_b128 v[72:75], v3 offset:49152\n"
"v_mul_f32 v49, v88, v49\n"
"v_mfma_f32_16x16x32_bf16 v[32:35], v[236:239], v[76:79], v[32:35]\n"
"v_mul_f32 v50, v88, v50\n"
"s_waitcnt lgkmcnt(4)\n"
"v_mfma_f32_16x16x32_bf16 v[28:31], v[240:243], v[56:59], v[28:31]\n"
"ds_read_b128 v[76:79], v3 offset:53248\n"
"v_cndmask_b32 v94, v52, v53, s[34:35]\n"
"v_mfma_f32_16x16x32_bf16 v[32:35], v[244:247], v[56:59], v[32:35]\n"
"v_cndmask_b32 v54, v54, v55, s[34:35]\n"
"v_cndmask_b32 v94, v94, v54, s[20:21]\n"
"v_mfma_f32_16x16x32_bf16 v[28:31], v[248:251], v[60:63], v[28:31]\n"
"ds_read_b128 v[56:59], v3 offset:57344\n"
"v_mul_f32 v51, v88, v51\n"
"v_mfma_f32_16x16x32_bf16 v[32:35], v[252:255], v[60:63], v[32:35]\n"
"v_fmac_f32 v48, v89, v20\n"
"s_waitcnt lgkmcnt(4)\n"
"s_cmp_eq_u32 s3, 3\n"
"s_cbranch_scc1 .Lfu_58\n"
"ds_read_b128 v[60:63], v3 offset:41984\n"
"v_fmac_f32 v49, v89, v21\n"
"v_mfma_f32_16x16x32_bf16 v[40:43], v[100:103], v[64:67], 0\n"
"v_cndmask_b32 v94, v28, v94, s[22:23]\n"
"v_fmac_f32 v50, v89, v22\n"
"v_mfma_f32_16x16x32_bf16 v[52:55], v[64:67], v[64:67], 0\n"
"ds_write_b32 v9, v94 offset:960\n"
"ds_read_b128 v[64:67], v3 offset:43008\n"
"v_fmac_f32 v51, v89, v23\n"
"v_mfma_f32_16x16x32_bf16 v[40:43], v[132:135], v[68:71], v[40:43]\n"
"v_cvt_pk_bf16_f32 v48, v48, v49\n"
"v_cvt_pk_bf16_f32 v49, v50, v51\n"
"v_mfma_f32_16x16x32_bf16 v[52:55], v[68:71], v[68:71], v[52:55]\n"
"ds_write_b64 v7, v[44:45] offset:52864\n"
"s_waitcnt lgkmcnt(5)\n"
"ds_read_b128 v[68:71], v3 offset:44032\n"
"v_mfma_f32_16x16x32_bf16 v[40:43], v[164:167], v[72:75], v[40:43]\n"
"v_mfma_f32_16x16x32_bf16 v[52:55], v[72:75], v[72:75], v[52:55]\n"
"ds_write_b64 v8, v[48:49] offset:52864\n"
"ds_read_b128 v[72:75], v3 offset:46080\n"
"v_mfma_f32_16x16x32_bf16 v[40:43], v[196:199], v[76:79], v[40:43]\n"
"v_mfma_f32_16x16x32_bf16 v[52:55], v[76:79], v[76:79], v[52:55]\n"
"ds_read_b128 v[84:87], v11 offset:960\n"
"ds_read_b32 v91, v12 offset:960\n"
"s_waitcnt lgkmcnt(8)\n"
"ds_read_b128 v[76:79], v3 offset:47104\n"
"v_mfma_f32_16x16x32_bf16 v[40:43], v[228:231], v[56:59], v[40:43]\n"
"v_mfma_f32_16x16x32_bf16 v[52:55], v[56:59], v[56:59], v[52:55]\n"
"ds_read_b128 v[56:59], v3 offset:48128\n"
"v_mfma_f32_16x16x32_bf16 v[40:43], v[108:111], v[60:63], v[40:43]\n"
"s_waitcnt lgkmcnt(6)\n"
"ds_read_b128 v[60:63], v3 offset:50176\n"
"s_nop 2\n"
"ds_write_b32 v10, v52 offset:1600\n"
"v_cndmask_b32 v94, v52, v53, s[34:35]\n"
"v_cndmask_b32 v54, v54, v55, s[34:35]\n"
"v_mfma_f32_16x16x32_bf16 v[40:43], v[116:119], v[64:67], v[40:43]\n"
"s_waitcnt lgkmcnt(5)\n"
"v_add_f32 v84, v84, v85\n"
"v_add_f32 v86, v86, v87\n"
"v_add_f32 v84, v84, v86\n"
"v_cndmask_b32 v94, v94, v54, s[20:21]\n"
"ds_write_b32 v9, v94 offset:1280\n"
"ds_read_b128 v[64:67], v3 offset:51200\n"
"s_waitcnt lgkmcnt(6)\n"
"v_mul_f32 v89, 0x3acccccd, v91\n"
"v_fma_f32 v84, -v91, v89, v84\n"
"v_mfma_f32_16x16x32_bf16 v[40:43], v[124:127], v[68:71], v[40:43]\n"
"v_fmamk_f32 v84, v84, 0x3acccccd, v27\n"
"s_waitcnt lgkmcnt(5)\n"
"ds_read_b128 v[68:71], v3 offset:52224\n"
"v_cmp_u_f32 vcc, v84, v84\n"
"v_mfma_f32_16x16x32_bf16 v[40:43], v[140:143], v[72:75], v[40:43]\n"
"v_max_f32 v84, v84, v27\n"
"v_rsq_f32 v88, v84\n"
"ds_read_b128 v[72:75], v3 offset:54272\n"
"v_mul_f32 v89, -v89, v88\n"
"v_mfma_f32_16x16x32_bf16 v[40:43], v[148:151], v[76:79], v[40:43]\n"
"s_cbranch_vccnz .Lsl_50c\n"
".Lbk_51c:\n"
"s_waitcnt lgkmcnt(5)\n"
"ds_read_b128 v[76:79], v3 offset:55296\n"
"v_mul_f32 v28, v88, v28\n"
"v_mul_f32 v29, v88, v29\n"
"v_mfma_f32_16x16x32_bf16 v[40:43], v[156:159], v[56:59], v[40:43]\n"
"ds_read_b128 v[44:47], v11 offset:1280\n"
"ds_read_b128 v[48:51], v11 offset:1600\n"
"v_mul_f32 v30, v88, v30\n"
"ds_read_b128 v[56:59], v3 offset:56320\n"
"v_mul_f32 v31, v88, v31\n"
"v_fmac_f32 v28, v89, v16\n"
"v_mfma_f32_16x16x32_bf16 v[40:43], v[172:175], v[60:63], v[40:43]\n"
"v_fmac_f32 v29, v89, v17\n"
"v_fmac_f32 v30, v89, v18\n"
"s_waitcnt lgkmcnt(5)\n"
"ds_read_b128 v[60:63], v4 offset:40960\n"
"v_fmac_f32 v31, v89, v19\n"
"v_cvt_pk_bf16_f32 v28, v28, v29\n"
"v_mfma_f32_16x16x32_bf16 v[40:43], v[180:183], v[64:67], v[40:43]\n"
"v_cvt_pk_bf16_f32 v29, v30, v31\n"
"ds_write_b64 v7, v[28:29] offset:56832\n"
"ds_read_b128 v[64:67], v5 offset:40960\n"
"v_mul_f32 v32, v88, v32\n"
"v_mul_f32 v33, v88, v33\n"
"v_mfma_f32_16x16x32_bf16 v[40:43], v[188:191], v[68:71], v[40:43]\n"
"v_mul_f32 v34, v88, v34\n"
"v_mul_f32 v35, v88, v35\n"
"s_waitcnt lgkmcnt(6)\n"
"ds_read_b128 v[68:71], v6 offset:40960\n"
"s_waitcnt lgkmcnt(6)\n"
"v_add_f32 v44, v44, v45\n"
"v_fmac_f32 v32, v89, v20\n"
"v_mfma_f32_16x16x32_bf16 v[40:43], v[204:207], v[72:75], v[40:43]\n"
"v_add_f32 v46, v46, v47\n"
"s_waitcnt lgkmcnt(5)\n"
"v_add_f32 v48, v48, v49\n"
"v_fmac_f32 v33, v89, v21\n"
"v_add_f32 v50, v50, v51\n"
"v_add_f32 v48, v48, v50\n"
"v_fmac_f32 v34, v89, v22\n"
"v_mfma_f32_16x16x32_bf16 v[40:43], v[212:215], v[76:79], v[40:43]\n"
"v_add_f32 v44, v44, v46\n"
"v_fmac_f32 v35, v89, v23\n"
"s_waitcnt lgkmcnt(3)\n"
"v_mul_f32 v53, 0x3acccccd, v48\n"
"v_fma_f32 v44, -v48, v53, v44\n"
"v_cvt_pk_bf16_f32 v32, v32, v33\n"
"v_mfma_f32_16x16x32_bf16 v[40:43], v[220:223], v[56:59], v[40:43]\n"
"v_fmamk_f32 v44, v44, 0x3acccccd, v27\n"
"v_cmp_u_f32 vcc, v44, v44\n"
"v_max_f32 v44, v44, v27\n"
"v_rsq_f32 v52, v44\n"
"v_mfma_f32_16x16x32_bf16 v[40:43], v[236:239], v[60:63], v[40:43]\n"
"v_mul_f32 v53, -v53, v52\n"
"s_waitcnt lgkmcnt(0)\n"
"s_cbranch_vccnz .Lsl_54c\n"
".Lbk_55c:\n"
"v_mfma_f32_16x16x32_bf16 v[40:43], v[244:247], v[64:67], v[40:43]\n"
"v_cvt_pk_bf16_f32 v33, v34, v35\n"
"ds_write_b64 v8, v[32:33] offset:56832\n"
"v_mfma_f32_16x16x32_bf16 v[40:43], v[252:255], v[68:71], v[40:43]\n"
"s_nop 7\n"
"v_mul_f32 v40, v52, v40\n"
"v_mul_f32 v41, v52, v41\n"
"v_mul_f32 v42, v52, v42\n"
"v_mul_f32 v43, v52, v43\n"
"v_fmac_f32 v40, v53, v20\n"
"v_fmac_f32 v41, v53, v21\n"
"v_fmac_f32 v42, v53, v22\n"
"v_fmac_f32 v43, v53, v23\n"
"v_cvt_pk_bf16_f32 v40, v40, v41\n"
"v_cvt_pk_bf16_f32 v41, v42, v43\n"
"ds_write_b64 v8, v[40:41] offset:40960\n"
"ds_write_b32 v13, v13 offset:144\n"
"s_branch .Ljn_59\n"
".Lfu_58:\n"
"s_nop 0\n"
"v_mfma_f32_16x16x32_bf16 v[36:39], v[96:99], v[64:67], 0\n"
"ds_read_b128 v[60:63], v3 offset:41984\n"
"v_fmac_f32 v49, v89, v21\n"
"v_mfma_f32_16x16x32_bf16 v[40:43], v[100:103], v[64:67], 0\n"
"v_cndmask_b32 v94, v28, v94, s[22:23]\n"
"v_fmac_f32 v50, v89, v22\n"
"v_mfma_f32_16x16x32_bf16 v[52:55], v[64:67], v[64:67], 0\n"
"ds_write_b32 v9, v94 offset:960\n"
"v_mfma_f32_16x16x32_bf16 v[36:39], v[128:131], v[68:71], v[36:39]\n"
"ds_read_b128 v[64:67], v3 offset:43008\n"
"v_fmac_f32 v51, v89, v23\n"
"v_mfma_f32_16x16x32_bf16 v[40:43], v[132:135], v[68:71], v[40:43]\n"
"v_cvt_pk_bf16_f32 v48, v48, v49\n"
"v_cvt_pk_bf16_f32 v49, v50, v51\n"
"v_mfma_f32_16x16x32_bf16 v[52:55], v[68:71], v[68:71], v[52:55]\n"
"ds_write_b64 v7, v[44:45] offset:52864\n"
"s_waitcnt lgkmcnt(5)\n"
"v_mfma_f32_16x16x32_bf16 v[36:39], v[160:163], v[72:75], v[36:39]\n"
"ds_read_b128 v[68:71], v3 offset:44032\n"
"v_mfma_f32_16x16x32_bf16 v[40:43], v[164:167], v[72:75], v[40:43]\n"
"v_mfma_f32_16x16x32_bf16 v[52:55], v[72:75], v[72:75], v[52:55]\n"
"ds_write_b64 v8, v[48:49] offset:52864\n"
"v_mfma_f32_16x16x32_bf16 v[36:39], v[192:195], v[76:79], v[36:39]\n"
"ds_read_b128 v[72:75], v3 offset:46080\n"
"v_mfma_f32_16x16x32_bf16 v[40:43], v[196:199], v[76:79], v[40:43]\n"
"v_mfma_f32_16x16x32_bf16 v[52:55], v[76:79], v[76:79], v[52:55]\n"
"ds_read_b128 v[84:87], v11 offset:960\n"
"ds_read_b32 v91, v12 offset:960\n"
"s_waitcnt lgkmcnt(8)\n"
"v_mfma_f32_16x16x32_bf16 v[36:39], v[224:227], v[56:59], v[36:39]\n"
"ds_read_b128 v[76:79], v3 offset:47104\n"
"v_mfma_f32_16x16x32_bf16 v[40:43], v[228:231], v[56:59], v[40:43]\n"
"v_mfma_f32_16x16x32_bf16 v[52:55], v[56:59], v[56:59], v[52:55]\n"
"v_mfma_f32_16x16x32_bf16 v[36:39], v[104:107], v[60:63], v[36:39]\n"
"ds_read_b128 v[56:59], v3 offset:48128\n"
"v_mfma_f32_16x16x32_bf16 v[40:43], v[108:111], v[60:63], v[40:43]\n"
"s_waitcnt lgkmcnt(6)\n"
"v_mfma_f32_16x16x32_bf16 v[36:39], v[112:115], v[64:67], v[36:39]\n"
"ds_read_b128 v[60:63], v3 offset:50176\n"
"ds_write_b32 v10, v52 offset:1600\n"
"v_cndmask_b32 v94, v52, v53, s[34:35]\n"
"v_cndmask_b32 v54, v54, v55, s[34:35]\n"
"v_mfma_f32_16x16x32_bf16 v[40:43], v[116:119], v[64:67], v[40:43]\n"
"s_waitcnt lgkmcnt(5)\n"
"v_add_f32 v84, v84, v85\n"
"v_add_f32 v86, v86, v87\n"
"v_add_f32 v84, v84, v86\n"
"v_cndmask_b32 v94, v94, v54, s[20:21]\n"
"ds_write_b32 v9, v94 offset:1280\n"
"v_mfma_f32_16x16x32_bf16 v[36:39], v[120:123], v[68:71], v[36:39]\n"
"ds_read_b128 v[64:67], v3 offset:51200\n"
"s_waitcnt lgkmcnt(6)\n"
"v_mul_f32 v89, 0x3acccccd, v91\n"
"v_fma_f32 v84, -v91, v89, v84\n"
"v_mfma_f32_16x16x32_bf16 v[40:43], v[124:127], v[68:71], v[40:43]\n"
"v_fmamk_f32 v84, v84, 0x3acccccd, v27\n"
"s_waitcnt lgkmcnt(5)\n"
"v_mfma_f32_16x16x32_bf16 v[36:39], v[136:139], v[72:75], v[36:39]\n"
"ds_read_b128 v[68:71], v3 offset:52224\n"
"v_cmp_u_f32 vcc, v84, v84\n"
"v_mfma_f32_16x16x32_bf16 v[40:43], v[140:143], v[72:75], v[40:43]\n"
"v_max_f32 v84, v84, v27\n"
"v_rsq_f32 v88, v84\n"
"v_mfma_f32_16x16x32_bf16 v[36:39], v[144:147], v[76:79], v[36:39]\n"
"ds_read_b128 v[72:75], v3 offset:54272\n"
"v_mul_f32 v89, -v89, v88\n"
"v_mfma_f32_16x16x32_bf16 v[40:43], v[148:151], v[76:79], v[40:43]\n"
"s_cbranch_vccnz .Lsl_50\n"
".Lbk_51:\n"
"s_waitcnt lgkmcnt(5)\n"
"v_mfma_f32_16x16x32_bf16 v[36:39], v[152:155], v[56:59], v[36:39]\n"
"ds_read_b128 v[76:79], v3 offset:55296\n"
"v_mul_f32 v28, v88, v28\n"
"v_mul_f32 v29, v88, v29\n"
"v_mfma_f32_16x16x32_bf16 v[40:43], v[156:159], v[56:59], v[40:43]\n"
"ds_read_b128 v[44:47], v11 offset:1280\n"
"ds_read_b128 v[48:51], v11 offset:1600\n"
"v_mul_f32 v30, v88, v30\n"
"v_mfma_f32_16x16x32_bf16 v[36:39], v[168:171], v[60:63], v[36:39]\n"
"ds_read_b128 v[56:59], v3 offset:56320\n"
"v_mul_f32 v31, v88, v31\n"
"v_fmac_f32 v28, v89, v16\n"
"v_mfma_f32_16x16x32_bf16 v[40:43], v[172:175], v[60:63], v[40:43]\n"
"v_fmac_f32 v29, v89, v17\n"
"v_fmac_f32 v30, v89, v18\n"
"s_waitcnt lgkmcnt(5)\n"
"v_mfma_f32_16x16x32_bf16 v[36:39], v[176:179], v[64:67], v[36:39]\n"
"ds_read_b128 v[60:63], v4 offset:40960\n"
"v_fmac_f32 v31, v89, v19\n"
"v_cvt_pk_bf16_f32 v28, v28, v29\n"
"v_mfma_f32_16x16x32_bf16 v[40:43], v[180:183], v[64:67], v[40:43]\n"
"v_cvt_pk_bf16_f32 v29, v30, v31\n"
"ds_write_b64 v7, v[28:29] offset:56832\n"
"v_mfma_f32_16x16x32_bf16 v[36:39], v[184:187], v[68:71], v[36:39]\n"
"ds_read_b128 v[64:67], v5 offset:40960\n"
"v_mul_f32 v32, v88, v32\n"
"v_mul_f32 v33, v88, v33\n"
"v_mfma_f32_16x16x32_bf16 v[40:43], v[188:191], v[68:71], v[40:43]\n"
"v_mul_f32 v34, v88, v34\n"
"v_mul_f32 v35, v88, v35\n"
"s_waitcnt lgkmcnt(6)\n"
"v_mfma_f32_16x16x32_bf16 v[36:39], v[200:203], v[72:75], v[36:39]\n"
"ds_read_b128 v[68:71], v6 offset:40960\n"
"s_waitcnt lgkmcnt(6)\n"
"v_add_f32 v44, v44, v45\n"
"v_fmac_f32 v32, v89, v20\n"
"v_mfma_f32_16x16x32_bf16 v[40:43], v[204:207], v[72:75], v[40:43]\n"
"v_add_f32 v46, v46, v47\n"
"s_waitcnt lgkmcnt(5)\n"
"v_add_f32 v48, v48, v49\n"
"v_fmac_f32 v33, v89, v21\n"
"v_mfma_f32_16x16x32_bf16 v[36:39], v[208:211], v[76:79], v[36:39]\n"
"v_add_f32 v50, v50, v51\n"
"v_add_f32 v48, v48, v50\n"
"v_fmac_f32 v34, v89, v22\n"
"v_mfma_f32_16x16x32_bf16 v[40:43], v[212:215], v[76:79], v[40:43]\n"
"v_add_f32 v44, v44, v46\n"
"v_fmac_f32 v35, v89, v23\n"
"s_waitcnt lgkmcnt(3)\n"
"v_mfma_f32_16x16x32_bf16 v[36:39], v[216:219], v[56:59], v[36:39]\n"
"v_mul_f32 v53, 0x3acccccd, v48\n"
"v_fma_f32 v44, -v48, v53, v44\n"
"v_cvt_pk_bf16_f32 v32, v32, v33\n"
"v_mfma_f32_16x16x32_bf16 v[40:43], v[220:223], v[56:59], v[40:43]\n"
"v_fmamk_f32 v44, v44, 0x3acccccd, v27\n"
"v_cmp_u_f32 vcc, v44, v44\n"
"v_mfma_f32_16x16x32_bf16 v[36:39], v[232:235], v[60:63], v[36:39]\n"
"v_max_f32 v44, v44, v27\n"
"v_rsq_f32 v52, v44\n"
"v_mfma_f32_16x16x32_bf16 v[40:43], v[236:239], v[60:63], v[40:43]\n"
"v_mul_f32 v53, -v53, v52\n"
"s_waitcnt lgkmcnt(0)\n"
"v_mfma_f32_16x16x32_bf16 v[36:39], v[240:243], v[64:67], v[36:39]\n"
"s_cbranch_vccnz .Lsl_54\n"
".Lbk_55:\n"
"v_mfma_f32_16x16x32_bf16 v[40:43], v[244:247], v[64:67], v[40:43]\n"
"v_cvt_pk_bf16_f32 v33, v34, v35\n"
"ds_write_b64 v8, v[32:33] offset:56832\n"
"v_mfma_f32_16x16x32_bf16 v[36:39], v[248:251], v[68:71], v[36:39]\n"
"v_mfma_f32_16x16x32_bf16 v[40:43], v[252:255], v[68:71], v[40:43]\n"
"s_nop 4\n"
"v_mul_f32 v36, v52, v36\n"
"v_mul_f32 v37, v52, v37\n"
"ds_write_b32 v13, v13 offset:144\n"
"v_mul_f32 v38, v52, v38\n"
"v_mul_f32 v39, v52, v39\n"
"v_fmac_f32 v36, v53, v16\n"
"v_fmac_f32 v37, v53, v17\n"
"v_fmac_f32 v38, v53, v18\n"
"v_fmac_f32 v39, v53, v19\n"
"v_cvt_pk_bf16_f32 v36, v36, v37\n"
"v_cvt_pk_bf16_f32 v37, v38, v39\n"
"ds_write_b64 v7, v[36:37] offset:40960\n"
"v_mul_f32 v40, v52, v40\n"
"v_mul_f32 v41, v52, v41\n"
"v_mul_f32 v42, v52, v42\n"
"v_mul_f32 v43, v52, v43\n"
"v_fmac_f32 v40, v53, v20\n"
"v_fmac_f32 v41, v53, v21\n"
"v_fmac_f32 v42, v53, v22\n"
"v_fmac_f32 v43, v53, v23\n"
"v_cvt_pk_bf16_f32 v40, v40, v41\n"
"v_cvt_pk_bf16_f32 v41, v42, v43\n"
"ds_write_b64 v8, v[40:41] offset:40960\n"
".Ljn_59:\n"
"v_mov_b32 v46, 0\n"
"v_mov_b32 v47, 0\n"
"v_mov_b32 v50, 0\n"
"v_mov_b32 v51, 0\n"
"s_cmp_lg_u32 s3, 2\n"
"s_cbranch_scc1 .Lnw_60\n"
"s_mov_b32 s17, 0x100000\n"
".Ltp_61:\n"
"ds_read_b32 v90, v14 offset:144\n"
"s_sub_u32 s17, s17, 1\n"
"s_cbranch_scc1 .Lou_62\n"
"s_waitcnt lgkmcnt(0)\n"
"v_cmp_eq_u32 vcc, 0, v90\n"
"s_cbranch_vccnz .Ltp_61\n"
".Lou_62:\n"
"s_waitcnt lgkmcnt(0)\n"
".Lnw_60:\n"
"ds_read_b32 v90, v14 offset:144\n"
"s_mov_b64 exec, 0xffff\n"
"ds_read_b64 v[46:47], v24 offset:44960\n"
"ds_read_b64 v[50:51], v24 offset:45000\n"
"s_mov_b64 exec, -1\n"
"ds_read_b64 v[44:45], v25 offset:44928\n"
"ds_read_b64 v[56:57], v25 offset:41040\n"
"ds_read_b64 v[58:59], v24 offset:41072\n"
"ds_read_b64 v[64:65], v25 offset:45008\n"
"ds_read_b64 v[66:67], v24 offset:45040\n"
"ds_read_b64 v[48:49], v25 offset:44968\n"
"ds_read_b64 v[60:61], v25 offset:41080\n"
"ds_read_b64 v[62:63], v24 offset:41112\n"
"ds_read_b64 v[68:69], v25 offset:45048\n"
"ds_read_b64 v[70:71], v24 offset:45080\n"
"s_waitcnt lgkmcnt(12)\n"
"v_or_b32 v90, s33, v90\n"
"s_mov_b32 s17, 0x100000\n"
"v_cmp_eq_u32 vcc, 0, v90\n"
"s_cbranch_vccz .Lok_64\n"
".Ltp_63:\n"
"ds_read_b32 v90, v14 offset:144\n"
"s_mov_b64 exec, 0xffff\n"
"ds_read_b64 v[46:47], v24 offset:44960\n"
"ds_read_b64 v[50:51], v24 offset:45000\n"
"s_mov_b64 exec, -1\n"
"ds_read_b64 v[44:45], v25 offset:44928\n"
"ds_read_b64 v[56:57], v25 offset:41040\n"
"ds_read_b64 v[58:59], v24 offset:41072\n"
"ds_read_b64 v[64:65], v25 offset:45008\n"
"ds_read_b64 v[66:67], v24 offset:45040\n"
"ds_read_b64 v[48:49], v25 offset:44968\n"
"ds_read_b64 v[60:61], v25 offset:41080\n"
"ds_read_b64 v[62:63], v24 offset:41112\n"
"ds_read_b64 v[68:69], v25 offset:45048\n"
"ds_read_b64 v[70:71], v24 offset:45080\n"
"s_sub_u32 s17, s17, 1\n"
"s_cbranch_scc1 .Lok_64\n"
"s_waitcnt lgkmcnt(12)\n"
"v_cmp_eq_u32 vcc, 0, v90\n"
"s_cbranch_vccnz .Ltp_63\n"
"s_waitcnt lgkmcnt(0)\n"
".Lok_64:\n"
"ds_read_b64_tr_b16 v[28:29], v26 offset:40960\n"
"ds_read_b64_tr_b16 v[30:31], v26 offset:44928\n"
"ds_read_b64_tr_b16 v[32:33], v26 offset:41000\n"
"s_waitcnt lgkmcnt(10)\n"
"v_mfma_f32_16x16x32_bf16 v[56:59], v[56:59], v[44:47], 0\n"
"s_waitcnt lgkmcnt(8)\n"
"v_mfma_f32_16x16x32_bf16 v[64:67], v[64:67], v[44:47], 0\n"
"ds_read_b64_tr_b16 v[34:35], v26 offset:44968\n"
"ds_read_b64_tr_b16 v[36:37], v26 offset:40992\n"
"ds_read_b64_tr_b16 v[38:39], v26 offset:44960\n"
"s_waitcnt lgkmcnt(8)\n"
"v_mfma_f32_16x16x32_bf16 v[60:63], v[60:63], v[48:51], 0\n"
"s_waitcnt lgkmcnt(6)\n"
"v_mfma_f32_16x16x32_bf16 v[68:71], v[68:71], v[48:51], 0\n"
"ds_read_b64_tr_b16 v[40:41], v26 offset:41032\n"
"ds_read_b64_tr_b16 v[42:43], v26 offset:45000\n"
"v_mov_b32 v84, 0x3f803f80\n"
"v_mov_b32 v85, 0x3f803f80\n"
"v_mov_b32 v86, 0x3f803f80\n"
"v_mov_b32 v87, 0x3f803f80\n"
"v_cndmask_b32 v76, v56, v57, s[34:35]\n"
"v_cndmask_b32 v72, v64, v65, s[34:35]\n"
"v_cndmask_b32 v76, v76, v72, s[36:37]\n"
"v_cndmask_b32 v77, v58, v59, s[34:35]\n"
"v_cndmask_b32 v72, v66, v67, s[34:35]\n"
"v_cndmask_b32 v77, v77, v72, s[38:39]\n"
"v_max_f32 v52, v76, v77\n"
"v_max_f32 v54, v76, v77\n"
"v_cndmask_b32 v78, v60, v61, s[34:35]\n"
"v_cndmask_b32 v72, v68, v69, s[34:35]\n"
"v_cndmask_b32 v78, v78, v72, s[36:37]\n"
"v_cndmask_b32 v79, v62, v63, s[34:35]\n"
"v_cndmask_b32 v72, v70, v71, s[34:35]\n"
"v_cndmask_b32 v79, v79, v72, s[38:39]\n"
"v_max_f32 v53, v78, v79\n"
"v_max_f32 v55, v78, v79\n"
"v_permlane16_swap_b32 v52, v54\n"
"s_nop 0\n"
"v_permlane16_swap_b32 v53, v55\n"
"v_max_f32 v72, v52, v54\n"
"v_max_f32 v52, v52, v54\n"
"v_max_f32 v73, v53, v55\n"
"v_max_f32 v53, v53, v55\n"
"v_permlane32_swap_b32 v52, v72\n"
"s_nop 0\n"
"v_permlane32_swap_b32 v53, v73\n"
"v_max_f32 v52, v52, v72\n"
"v_max_f32 v53, v53, v73\n"
"v_mul_f32 v52, 0xbea52b5b, v52\n"
"v_mul_f32 v53, 0xbea52b5b, v53\n"
"v_fmamk_f32 v76, v76, 0x3ea52b5b, v52\n"
"v_fmamk_f32 v77, v77, 0x3ea52b5b, v52\n"
"v_fmamk_f32 v78, v78, 0x3ea52b5b, v53\n"
"v_fmamk_f32 v79, v79, 0x3ea52b5b, v53\n"
"v_exp_f32 v76, v76\n"
"v_exp_f32 v77, v77\n"
"v_exp_f32 v78, v78\n"
"v_exp_f32 v79, v79\n"
"v_cvt_pk_bf16_f32 v52, v76, v77\n"
"v_cvt_pk_bf16_f32 v53, v78, v79\n"
"v_perm_b32 v44, v52, v52, v80\n"
"v_perm_b32 v45, v52, v52, v81\n"
"v_perm_b32 v46, v52, v52, v82\n"
"v_perm_b32 v47, v52, v52, v83\n"
"v_perm_b32 v48, v53, v53, v80\n"
"v_perm_b32 v49, v53, v53, v81\n"
"v_perm_b32 v50, v53, v53, v82\n"
"v_perm_b32 v51, v53, v53, v83\n"
"v_mfma_f32_16x16x32_bf16 v[56:59], v[84:87], v[44:47], 0\n"
"s_nop 0\n"
"v_mfma_f32_16x16x32_bf16 v[60:63], v[84:87], v[48:51], 0\n"
"s_waitcnt lgkmcnt(6)\n"
"v_mfma_f32_16x16x32_bf16 v[28:31], v[28:31], v[44:47], 0\n"
"s_waitcnt lgkmcnt(4)\n"
"v_mfma_f32_16x16x32_bf16 v[32:35], v[32:35], v[48:51], 0\n"
"s_waitcnt lgkmcnt(2)\n"
"v_mfma_f32_16x16x32_bf16 v[36:39], v[36:39], v[44:47], 0\n"
"s_waitcnt lgkmcnt(0)\n"
"v_mfma_f32_16x16x32_bf16 v[40:43], v[40:43], v[48:51], 0\n"
"v_cndmask_b32 v88, 2.0, v1, s[40:41]\n"
"v_rcp_f32 v52, v56\n"
"v_rcp_f32 v53, v60\n"
"s_add_u32 s31, s30, 0x140000\n"
"v_mul_f32 v28, v52, v28\n"
"v_mul_f32 v29, v52, v29\n"
"v_mul_f32 v30, v52, v30\n"
"v_mul_f32 v31, v52, v31\n"
"v_mul_f32 v32, v53, v32\n"
"v_mul_f32 v33, v53, v33\n"
"v_mul_f32 v34, v53, v34\n"
"v_mul_f32 v35, v53, v35\n"
"v_cvt_pk_bf16_f32 v28, v28, v29\n"
"v_cvt_pk_bf16_f32 v29, v30, v31\n"
"v_cvt_pk_bf16_f32 v30, v32, v33\n"
"v_cvt_pk_bf16_f32 v31, v34, v35\n"
"v_min_u32 v28, -2, v28\n"
"v_min_u32 v29, -2, v29\n"
"v_min_u32 v30, -2, v30\n"
"v_min_u32 v31, -2, v31\n"
"buffer_store_dwordx4 v[28:31], v1, s[4:7], s31 offen offset:0 sc1\n"
"v_mul_f32 v36, v52, v36\n"
"v_mul_f32 v37, v52, v37\n"
"v_mul_f32 v38, v52, v38\n"
"v_mul_f32 v39, v52, v39\n"
"v_mul_f32 v40, v53, v40\n"
"v_mul_f32 v41, v53, v41\n"
"v_mul_f32 v42, v53, v42\n"
"v_mul_f32 v43, v53, v43\n"
"v_cvt_pk_bf16_f32 v36, v36, v37\n"
"v_cvt_pk_bf16_f32 v37, v38, v39\n"
"v_cvt_pk_bf16_f32 v38, v40, v41\n"
"v_cvt_pk_bf16_f32 v39, v42, v43\n"
"v_min_u32 v36, -2, v36\n"
"v_min_u32 v37, -2, v37\n"
"v_min_u32 v38, -2, v38\n"
"v_min_u32 v39, -2, v39\n"
"buffer_store_dwordx4 v[36:39], v88, s[4:7], s31 offen offset:1024 sc1\n"
"ds_write_b32 v13, v13 offset:160\n"
"s_nop 2\n"
"s_cmp_lt_i32 s45, 0\n"
"s_cbranch_scc1 .Lnc_65\n"
"s_setprio 1\n"
"s_lshl_b32 s79, s3, 10\n"
"v_lshl_add_u32 v34, v0, 4, s79\n"
"v_lshrrev_b32 v59, 7, v34\n"
"v_and_b32 v58, 0x70, v34\n"
"s_mov_b32 s79, 0x5000\n"
"v_mad_i32_i24 v33, v59, s79, v58\n"
"s_mov_b32 s17, 0x100000\n"
".Ltp_66:\n"
"s_sleep 1\n"
"ds_read_b32 v42, v14 offset:144\n"
"s_sub_u32 s17, s17, 1\n"
"s_cbranch_scc1 .Lou_67\n"
"s_waitcnt lgkmcnt(0)\n"
"v_cmp_eq_u32 vcc, 0, v42\n"
"s_cbranch_vccnz .Ltp_66\n"
".Lou_67:\n"
"s_waitcnt lgkmcnt(0)\n"
"s_mov_b32 s17, 0x100000\n"
".Ltp_68:\n"
"s_sleep 1\n"
"ds_read_b32 v42, v14 offset:176\n"
"s_sub_u32 s17, s17, 1\n"
"s_cbranch_scc1 .Lou_69\n"
"s_waitcnt lgkmcnt(0)\n"
"v_cmp_eq_u32 vcc, 0, v42\n"
"s_cbranch_vccnz .Ltp_68\n"
".Lou_69:\n"
"s_waitcnt lgkmcnt(0)\n"
"s_lshl_b32 s79, s45, 8\n"
"s_add_u32 s46, s79, 0x6c8000\n"
"s_add_u32 s47, s46, 0x140000\n"
"s_and_b32 s79, s2, 7\n"
"s_lshl_b32 s79, s79, 14\n"
"s_add_u32 s48, s79, 0xc08000\n"
"s_add_u32 s49, s48, 0x1000\n"
"v_and_b32 v94, 0xffffff80, v33\n"
"v_add_u32 v94, v94, v33\n"
"buffer_load_dwordx4 v[78:81], v94, s[4:7], s46 offen offset:0 sc1\n"
"buffer_load_dwordx4 v[82:85], v94, s[4:7], s46 offen offset:128 sc1\n"
"buffer_load_dwordx4 v[86:89], v94, s[4:7], s47 offen offset:0 sc1\n"
"buffer_load_dwordx4 v[90:93], v94, s[4:7], s47 offen offset:128 sc1\n"
"s_mul_i32 s79, s3, 0x1200\n"
"s_add_u32 s79, s79, 0x1c400\n"
"v_lshl_add_u32 v44, v0, 3, s79\n"
"v_mov_b32 v46, 0\n"
"v_mov_b32 v47, 0\n"
"ds_write_b64 v44, v[46:47]\n"
"v_lshrrev_b32 v44, 4, v0\n"
"v_mov_b32 v59, 0xffff0000\n"
"v_mov_b32 v58, 0xffff\n"
"s_mov_b32 vcc_lo, 0xaaaaaaaa\n"
"s_mov_b32 vcc_hi, 0xaaaaaaaa\n"
"v_cndmask_b32 v45, v58, v59, vcc\n"
"v_and_b32 v59, 15, v0\n"
"v_lshlrev_b32 v46, 5, v59\n"
"v_and_b32 v59, 15, v0\n"
"v_lshrrev_b32 v47, 2, v59\n"
"s_lshl_b32 s79, s3, 10\n"
"v_lshl_add_u32 v48, v0, 4, s79\n"
"v_lshrrev_b32 v49, 2, v48\n"
"v_lshrrev_b32 v50, 6, v48\n"
"v_lshlrev_b32 v59, 3, v44\n"
"v_and_b32 v58, 15, v0\n"
"v_sub_u32 v59, v59, v58\n"
"v_add_u32 v51, 0x3ef, v59\n"
"v_and_b32 v59, 24, v50\n"
"v_xor_b32 v59, v49, v59\n"
"v_add_u32 v36, 0xa000, v59\n"
"s_lshl_b32 s79, s3, 8\n"
"s_add_u32 s79, s79, 0xa000\n"
"v_and_b32 v59, 15, v0\n"
"v_xor_b32 v59, s3, v59\n"
"v_and_b32 v59, 1, v59\n"
"v_lshrrev_b32 v58, 1, v44\n"
"v_xor_b32 v58, v47, v58\n"
"v_and_b32 v58, 1, v58\n"
"v_lshl_add_u32 v59, v59, 1, v58\n"
"v_and_b32 v58, 15, v0\n"
"v_and_b32 v58, 2, v58\n"
"v_and_b32 v57, 15, v0\n"
"v_lshrrev_b32 v57, 3, v57\n"
"v_lshlrev_b32 v57, 7, v57\n"
"v_lshl_add_u32 v57, v47, 11, v57\n"
"v_lshl_add_u32 v57, v44, 6, v57\n"
"v_lshl_add_u32 v58, v58, 4, v57\n"
"v_lshl_add_u32 v59, v59, 3, v58\n"
"v_add_u32 v31, s79, v59\n"
"s_mul_i32 s79, s3, 0x2100\n"
"s_add_u32 s79, s79, 0x13840\n"
"v_lshrrev_b32 v59, 2, v51\n"
"v_and_b32 v58, 3, v51\n"
"v_mul_i32_i24 v58, 0x840, v58\n"
"v_lshl_add_u32 v59, v59, 3, v58\n"
"v_add_u32 v28, s79, v59\n"
"s_mul_i32 s79, s3, 0x1200\n"
"s_add_u32 s79, s79, 0x1c420\n"
"v_lshl_add_u32 v59, v44, 4, v46\n"
"v_add_u32 v29, s79, v59\n"
"s_lshl_b32 s79, s3, 11\n"
"s_lshr_b32 s78, s3, 1\n"
"s_lshl_b32 s78, s78, 7\n"
"s_add_u32 s79, s79, s78\n"
"s_add_u32 s79, s79, 0xa000\n"
"s_and_b32 s78, s3, 1\n"
"v_xor_b32 v59, v44, v47\n"
"v_xor_b32 v59, s78, v59\n"
"v_and_b32 v59, 3, v59\n"
"v_lshl_add_u32 v59, v59, 3, v46\n"
"v_add_u32 v30, s79, v59\n"
"v_xor_b32 v32, 8, v31\n"
"v_add_u32 v35, 0x1c600, v49\n"
"v_xor_b32 v37, 8, v36\n"
"s_mov_b32 vcc_lo, 0x300003\n"
"s_mov_b32 vcc_hi, 0x30000300\n"
"v_cndmask_b32 v38, 0, v45, vcc\n"
"s_mov_b32 vcc_lo, 0xc0000c\n"
"s_mov_b32 vcc_hi, 0xc0000c00\n"
"v_cndmask_b32 v39, 0, v45, vcc\n"
"s_lshl_b32 s79, s3, 2\n"
"s_add_u32 s79, s79, 0x26800\n"
"v_mov_b32 v40, s79\n"
"v_and_b32 v59, 48, v48\n"
"v_mul_i32_i24 v59, 0x50, v59\n"
"s_mov_b32 s79, 0x2800\n"
"v_mad_i32_i24 v41, v50, s79, v59\n"
"s_mul_i32 s72, s3, 0x2100\n"
"v_lshlrev_b32 v47, 4, v0\n"
"v_lshrrev_b32 v48, 5, v0\n"
"s_add_u32 s73, s72, 0x14000\n"
"s_add_u32 s74, s72, 0x15100\n"
"s_add_u32 s79, s74, 0x7f4\n"
"v_lshlrev_b32 v57, 3, v0\n"
"v_sub_u32 v60, s79, v57\n"
"s_sub_u32 s79, s74, 8\n"
"v_lshlrev_b32 v57, 5, v0\n"
"v_sub_u32 v57, 0x7e0, v57\n"
"v_sub_u32 v56, 0x5f0, v47\n"
"s_mov_b32 vcc_lo, 0\n"
"s_mov_b32 vcc_hi, -1\n"
"v_cndmask_b32 v57, v56, v57, vcc\n"
"v_add_u32 v49, s79, v57\n"
"v_mov_b32 v57, 4\n"
"s_mov_b32 vcc_lo, 0\n"
"s_mov_b32 vcc_hi, 0x80000000\n"
"v_cndmask_b32 v57, 0, v57, vcc\n"
"v_sub_u32 v61, v60, v57\n"
"v_mov_b32 v57, 4\n"
"s_mov_b32 vcc_lo, 0x80000000\n"
"s_mov_b32 vcc_hi, -1\n"
"v_cndmask_b32 v57, 0, v57, vcc\n"
"v_sub_u32 v62, v49, v57\n"
"v_add_u32 v67, s73, v47\n"
"s_add_u32 s79, s73, 0x800\n"
"v_and_b32 v57, 15, v0\n"
"v_lshrrev_b32 v56, 4, v0\n"
"v_mul_i32_i24 v56, 0x840, v56\n"
"v_lshl_add_u32 v57, v57, 2, v56\n"
"v_add_u32 v68, s79, v57\n"
"v_mov_b32 v69, 0\n"
"v_mad_i32_i24 v63, v48, 0, v49\n"
"v_lshl_add_u32 v64, v48, 2, v49\n"
"v_lshl_add_u32 v65, v48, 3, v49\n"
"v_mad_i32_i24 v66, v48, 12, v49\n"
"s_mov_b32 s17, 0x100000\n"
".Ltp_70:\n"
"s_sleep 1\n"
"ds_read_b32 v42, v14 offset:192\n"
"s_sub_u32 s17, s17, 1\n"
"s_cbranch_scc1 .Lou_71\n"
"s_waitcnt lgkmcnt(0)\n"
"v_cmp_eq_u32 vcc, 0, v42\n"
"s_cbranch_vccnz .Ltp_70\n"
".Lou_71:\n"
"s_waitcnt lgkmcnt(0)\n"
"ds_read_b32 v44, v60 offset:12\n"
"ds_read_b32 v45, v60 offset:8\n"
"ds_read_b32 v46, v60 offset:4\n"
"ds_read_b32 v47, v61 offset:4\n"
"ds_read_b32 v48, v61 offset:0\n"
"ds_read_b32 v49, v66 offset:24\n"
"ds_read_b32 v50, v66 offset:20\n"
"ds_read_b32 v51, v66 offset:16\n"
"ds_read_b32 v52, v65 offset:16\n"
"ds_read_b32 v53, v64 offset:16\n"
"ds_read_b32 v54, v64 offset:12\n"
"ds_read_b32 v55, v64 offset:8\n"
"ds_read_b32 v56, v63 offset:8\n"
"ds_read_b32 v57, v62 offset:8\n"
"ds_read_b32 v58, v62 offset:4\n"
"ds_read_b32 v59, v62 offset:0\n"
"v_cmp_ne_u32 vcc, 63, v0\n"
"s_waitcnt lgkmcnt(2)\n"
"s_nop 0\n"
"v_cndmask_b32 v57, 0, v57, vcc\n"
"s_waitcnt lgkmcnt(1)\n"
"v_cndmask_b32 v58, 0, v58, vcc\n"
"s_waitcnt lgkmcnt(0)\n"
"v_cndmask_b32 v59, 0, v59, vcc\n"
"v_cvt_pk_f16_f32 v70, v44, v44\n"
"v_cvt_pk_f16_f32 v71, v45, v45\n"
"v_cvt_pk_f16_f32 v72, v45, v46\n"
"v_cvt_pk_f16_f32 v73, v46, v46\n"
"ds_write_b128 v67, v[70:73] offset:0\n"
"v_cvt_pk_f16_f32 v74, v44, v45\n"
"v_cvt_pk_f16_f32 v75, v45, v45\n"
"v_cvt_pk_f16_f32 v76, v46, v46\n"
"v_cvt_pk_f16_f32 v77, v46, v46\n"
"ds_write_b128 v67, v[74:77] offset:2112\n"
"v_cvt_pk_f16_f32 v70, v45, v45\n"
"v_cvt_pk_f16_f32 v71, v45, v46\n"
"v_cvt_pk_f16_f32 v72, v46, v46\n"
"v_cvt_pk_f16_f32 v73, v46, v47\n"
"ds_write_b128 v67, v[70:73] offset:4224\n"
"v_cvt_pk_f16_f32 v74, v45, v45\n"
"v_cvt_pk_f16_f32 v75, v46, v46\n"
"v_cvt_pk_f16_f32 v76, v46, v46\n"
"v_cvt_pk_f16_f32 v77, v47, v48\n"
"ds_write_b128 v67, v[74:77] offset:6336\n"
"v_cvt_pk_f16_f32 v70, v49, v50\n"
"v_cvt_pk_f16_f32 v71, v51, v52\n"
"v_cvt_pk_f16_f32 v72, v53, v54\n"
"v_cvt_pk_f16_f32 v73, v55, v56\n"
"ds_write_b128 v67, v[70:73] offset:1024\n"
"v_cvt_pk_f16_f32 v74, v50, v51\n"
"v_cvt_pk_f16_f32 v75, v52, v53\n"
"v_cvt_pk_f16_f32 v76, v54, v55\n"
"v_cvt_pk_f16_f32 v77, v56, v57\n"
"ds_write_b128 v67, v[74:77] offset:3136\n"
"v_cvt_pk_f16_f32 v70, v51, v52\n"
"v_cvt_pk_f16_f32 v71, v53, v54\n"
"v_cvt_pk_f16_f32 v72, v55, v56\n"
"v_cvt_pk_f16_f32 v73, v57, v58\n"
"ds_write_b128 v67, v[70:73] offset:5248\n"
"v_cvt_pk_f16_f32 v74, v52, v53\n"
"v_cvt_pk_f16_f32 v75, v54, v55\n"
"v_cvt_pk_f16_f32 v76, v56, v57\n"
"v_cvt_pk_f16_f32 v77, v58, v59\n"
"ds_write_b128 v67, v[74:77] offset:7360\n"
"ds_write_b32 v68, v69\n"
"ds_read_b32 v40, v40\n"
"s_waitcnt lgkmcnt(0)\n"
"v_sub_u32 v44, 0x7f000000, v40\n"
"v_lshrrev_b32 v45, 16, v44\n"
"v_or_b32 v44, v44, v45\n"
"v_and_b32 v38, v44, v38\n"
"v_and_b32 v39, v44, v39\n"
"s_mov_b32 s52, 0x05040100\n"
"s_mov_b32 s53, 0x07060302\n"
"s_mov_b32 s17, 32768\n"
".Ltp_72:\n"
"s_waitcnt vmcnt(0)\n"
"s_sub_u32 s17, s17, 1\n"
"s_cbranch_scc1 .Lou_73\n"
"v_bitop3_b32 v54, v79, v81, s16 bitop3:0x7e\n"
"v_bitop3_b32 v55, v83, v85, s16 bitop3:0x7e\n"
"v_bitop3_b32 v56, v87, v89, s16 bitop3:0x7e\n"
"v_bitop3_b32 v57, v91, v93, s16 bitop3:0x7e\n"
"v_or3_b32 v58, v54, v55, v56\n"
"v_or_b32 v58, v58, v57\n"
"v_cmp_ne_u32 vcc, 0, v58\n"
"s_cbranch_vccz .Lou_73\n"
"s_sleep 2\n"
"v_cmp_ne_u32 vcc, 0, v54\n"
"s_cbranch_vccz .Lsk_74\n"
"s_mov_b64 exec, vcc\n"
"buffer_load_dwordx4 v[78:81], v94, s[4:7], s46 offen offset:0 sc1\n"
"s_mov_b64 exec, -1\n"
".Lsk_74:\n"
"v_cmp_ne_u32 vcc, 0, v55\n"
"s_cbranch_vccz .Lsk_75\n"
"s_mov_b64 exec, vcc\n"
"buffer_load_dwordx4 v[82:85], v94, s[4:7], s46 offen offset:128 sc1\n"
"s_mov_b64 exec, -1\n"
".Lsk_75:\n"
"v_cmp_ne_u32 vcc, 0, v56\n"
"s_cbranch_vccz .Lsk_76\n"
"s_mov_b64 exec, vcc\n"
"buffer_load_dwordx4 v[86:89], v94, s[4:7], s47 offen offset:0 sc1\n"
"s_mov_b64 exec, -1\n"
".Lsk_76:\n"
"v_cmp_ne_u32 vcc, 0, v57\n"
"s_cbranch_vccz .Lsk_77\n"
"s_mov_b64 exec, vcc\n"
"buffer_load_dwordx4 v[90:93], v94, s[4:7], s47 offen offset:128 sc1\n"
"s_mov_b64 exec, -1\n"
".Lsk_77:\n"
"s_branch .Ltp_72\n"
".Lou_73:\n"
"s_waitcnt vmcnt(0)\n"
"buffer_load_dwordx4 v[70:73], v34, s[4:7], s48 offen sc1\n"
"buffer_load_dwordx4 v[74:77], v34, s[4:7], s49 offen sc1\n"
"v_perm_b32 v54, v82, v78, s52\n"
"v_perm_b32 v55, v82, v78, s53\n"
"v_perm_b32 v56, v84, v80, s52\n"
"v_perm_b32 v57, v84, v80, s53\n"
"ds_write_b32 v36, v54 offset:0\n"
"ds_write_b32 v37, v55 offset:2048\n"
"ds_write_b32 v36, v56 offset:4224\n"
"ds_write_b32 v37, v57 offset:6272\n"
"v_perm_b32 v54, v90, v86, s52\n"
"v_perm_b32 v55, v90, v86, s53\n"
"v_perm_b32 v56, v92, v88, s52\n"
"v_perm_b32 v57, v92, v88, s53\n"
"ds_write_b32 v36, v54 offset:1024\n"
"ds_write_b32 v37, v55 offset:3072\n"
"ds_write_b32 v36, v56 offset:5248\n"
"ds_write_b32 v37, v57 offset:7296\n"
"v_lshlrev_b32 v54, 16, v78\n"
"v_lshlrev_b32 v55, 16, v82\n"
"v_and_b32 v56, 0xffff0000, v78\n"
"v_and_b32 v57, 0xffff0000, v82\n"
"v_lshlrev_b32 v58, 16, v80\n"
"v_lshlrev_b32 v59, 16, v84\n"
"v_and_b32 v60, 0xffff0000, v80\n"
"v_and_b32 v61, 0xffff0000, v84\n"
"v_lshlrev_b32 v62, 16, v86\n"
"v_lshlrev_b32 v63, 16, v90\n"
"v_and_b32 v64, 0xffff0000, v86\n"
"v_and_b32 v65, 0xffff0000, v90\n"
"v_lshlrev_b32 v66, 16, v88\n"
"v_lshlrev_b32 v67, 16, v92\n"
"v_and_b32 v68, 0xffff0000, v88\n"
"v_and_b32 v69, 0xffff0000, v92\n"
"s_mov_b32 s17, 32768\n"
".Ltp_78:\n"
"s_waitcnt vmcnt(0)\n"
"s_sub_u32 s17, s17, 1\n"
"s_cbranch_scc1 .Lou_79\n"
"v_max_u32 v78, v70, v71\n"
"v_max3_u32 v78, v72, v73, v78\n"
"v_max3_u32 v78, v74, v75, v78\n"
"v_max3_u32 v78, v76, v77, v78\n"
"v_cmp_eq_u32 vcc, -1, v78\n"
"s_cbranch_vccz .Lou_79\n"
"v_max_u32 v78, v72, v73\n"
"v_max3_u32 v78, v70, v71, v78\n"
"v_cmp_eq_u32 vcc, -1, v78\n"
"s_cbranch_vccz .Lsk_80\n"
"s_mov_b64 exec, vcc\n"
"buffer_load_dwordx4 v[70:73], v34, s[4:7], s48 offen sc1\n"
"s_mov_b64 exec, -1\n"
".Lsk_80:\n"
"v_max_u32 v78, v76, v77\n"
"v_max3_u32 v78, v74, v75, v78\n"
"v_cmp_eq_u32 vcc, -1, v78\n"
"s_cbranch_vccz .Lsk_81\n"
"s_mov_b64 exec, vcc\n"
"buffer_load_dwordx4 v[74:77], v34, s[4:7], s49 offen sc1\n"
"s_mov_b64 exec, -1\n"
".Lsk_81:\n"
"s_branch .Ltp_78\n"
".Lou_79:\n"
"s_waitcnt vmcnt(0)\n"
"v_fma_f32 v54, v54, v71, v70\n"
"v_fma_f32 v55, v55, v73, v72\n"
"v_cvt_pk_f16_f32 v54, v54, v55\n"
"v_fma_f32 v56, v56, v71, v70\n"
"v_fma_f32 v57, v57, v73, v72\n"
"v_cvt_pk_f16_f32 v56, v56, v57\n"
"v_fma_f32 v58, v58, v71, v70\n"
"v_fma_f32 v59, v59, v73, v72\n"
"v_cvt_pk_f16_f32 v58, v58, v59\n"
"v_fma_f32 v60, v60, v71, v70\n"
"v_fma_f32 v61, v61, v73, v72\n"
"v_cvt_pk_f16_f32 v60, v60, v61\n"
"ds_write2st64_b32 v35, v54, v56 offset0:0 offset1:18\n"
"ds_write2st64_b32 v35, v58, v60 offset0:36 offset1:54\n"
"v_fma_f32 v62, v62, v75, v74\n"
"v_fma_f32 v63, v63, v77, v76\n"
"v_cvt_pk_f16_f32 v62, v62, v63\n"
"v_fma_f32 v64, v64, v75, v74\n"
"v_fma_f32 v65, v65, v77, v76\n"
"v_cvt_pk_f16_f32 v64, v64, v65\n"
"v_fma_f32 v66, v66, v75, v74\n"
"v_fma_f32 v67, v67, v77, v76\n"
"v_cvt_pk_f16_f32 v66, v66, v67\n"
"v_fma_f32 v68, v68, v75, v74\n"
"v_fma_f32 v69, v69, v77, v76\n"
"v_cvt_pk_f16_f32 v68, v68, v69\n"
"ds_write2st64_b32 v35, v62, v64 offset0:4 offset1:22\n"
"ds_write2st64_b32 v35, v66, v68 offset0:40 offset1:58\n"
"ds_write_b32 v13, v13 offset:208\n"
"s_mov_b32 s17, 0x100000\n"
".Ltp_82:\n"
"ds_read_b32 v42, v14 offset:208\n"
"s_sub_u32 s17, s17, 1\n"
"s_cbranch_scc1 .Lou_83\n"
"s_waitcnt lgkmcnt(0)\n"
"v_cmp_eq_u32 vcc, 0, v42\n"
"s_cbranch_vccnz .Ltp_82\n"
".Lou_83:\n"
"s_waitcnt lgkmcnt(0)\n"
"s_setprio 1\n"
"s_mov_b32 s50, 0xfffefffe\n"
"ds_read_b64 v[60:61], v28 offset:1984\n"
"ds_read_b64 v[62:63], v28 offset:1992\n"
"ds_read_b128 v[64:67], v29 offset:1984\n"
"ds_read_b128 v[68:71], v29 offset:1472\n"
"ds_read_b64 v[72:73], v28 offset:1472\n"
"ds_read_b64 v[74:75], v28 offset:1480\n"
"ds_read_b128 v[76:79], v29 offset:960\n"
"s_waitcnt lgkmcnt(0)\n"
"v_mfma_f32_16x16x32_f16 v[56:59], v[60:63], v[64:67], 0\n"
"ds_read_b64 v[80:81], v28 offset:960\n"
"ds_read_b64 v[82:83], v28 offset:968\n"
"v_mfma_f32_16x16x32_f16 v[52:55], v[60:63], v[68:71], 0\n"
"ds_read_b128 v[84:87], v29 offset:448\n"
"ds_read_b64 v[88:89], v28 offset:448\n"
"v_mfma_f32_16x16x32_f16 v[56:59], v[72:75], v[68:71], v[56:59]\n"
"ds_read_b64 v[90:91], v28 offset:456\n"
"ds_read_b64 v[92:93], v28 offset:1920\n"
"v_mfma_f32_16x16x32_f16 v[48:51], v[60:63], v[76:79], 0\n"
"ds_read_b64 v[94:95], v28 offset:1928\n"
"s_waitcnt lgkmcnt(4)\n"
"v_mfma_f32_16x16x32_f16 v[52:55], v[72:75], v[76:79], v[52:55]\n"
"ds_read_b128 v[2:5], v29 offset:1920\n"
"ds_read_b128 v[34:37], v29 offset:1408\n"
"v_mfma_f32_16x16x32_f16 v[56:59], v[80:83], v[76:79], v[56:59]\n"
"ds_read_b64 v[64:65], v28 offset:1408\n"
"ds_read_b64 v[66:67], v28 offset:1416\n"
"v_mfma_f32_16x16x32_f16 v[44:47], v[60:63], v[84:87], 0\n"
"ds_read_b128 v[68:71], v29 offset:896\n"
"ds_read_b64 v[76:77], v28 offset:896\n"
"v_mfma_f32_16x16x32_f16 v[48:51], v[72:75], v[84:87], v[48:51]\n"
"ds_read_b64 v[78:79], v28 offset:904\n"
"s_waitcnt lgkmcnt(5)\n"
"v_mfma_f32_16x16x32_f16 v[52:55], v[80:83], v[84:87], v[52:55]\n"
"ds_read_b128 v[60:63], v29 offset:384\n"
"ds_read_b64 v[72:73], v28 offset:384\n"
"v_mfma_f32_16x16x32_f16 v[56:59], v[88:91], v[84:87], v[56:59]\n"
"ds_read_b64 v[74:75], v28 offset:392\n"
"ds_read_b64 v[80:81], v28 offset:1856\n"
"v_mfma_f32_16x16x32_f16 v[56:59], v[92:95], v[2:5], v[56:59]\n"
"ds_read_b64 v[82:83], v28 offset:1864\n"
"ds_read_b128 v[84:87], v29 offset:1856\n"
"v_mfma_f32_16x16x32_f16 v[52:55], v[92:95], v[34:37], v[52:55]\n"
"ds_read_b128 v[88:91], v29 offset:1344\n"
"s_waitcnt lgkmcnt(7)\n"
"v_mfma_f32_16x16x32_f16 v[56:59], v[64:67], v[34:37], v[56:59]\n"
"ds_read_b64 v[2:3], v30 offset:0\n"
"ds_read_b64 v[34:35], v28 offset:1344\n"
"v_mfma_f32_16x16x32_f16 v[48:51], v[92:95], v[68:71], v[48:51]\n"
"ds_read_b64 v[36:37], v28 offset:1352\n"
"v_mfma_f32_16x16x32_f16 v[52:55], v[64:67], v[68:71], v[52:55]\n"
"v_mfma_f32_16x16x32_f16 v[56:59], v[76:79], v[68:71], v[56:59]\n"
"ds_read_b128 v[68:71], v29 offset:832\n"
"s_waitcnt lgkmcnt(8)\n"
"v_mfma_f32_16x16x32_f16 v[44:47], v[92:95], v[60:63], v[44:47]\n"
"ds_read_b64 v[92:93], v28 offset:832\n"
"v_mfma_f32_16x16x32_f16 v[48:51], v[64:67], v[60:63], v[48:51]\n"
"ds_read_b64 v[94:95], v28 offset:840\n"
"v_mfma_f32_16x16x32_f16 v[52:55], v[76:79], v[60:63], v[52:55]\n"
"ds_read_b128 v[64:67], v29 offset:320\n"
"ds_read_b64 v[76:77], v28 offset:320\n"
"v_mfma_f32_16x16x32_f16 v[56:59], v[72:75], v[60:63], v[56:59]\n"
"ds_read_b64 v[78:79], v28 offset:328\n"
"s_waitcnt lgkmcnt(6)\n"
"v_mfma_f32_16x16x32_f16 v[56:59], v[80:83], v[84:87], v[56:59]\n"
"ds_read_b64 v[60:61], v30 offset:512\n"
"ds_read_b64 v[72:73], v28 offset:1792\n"
"v_mfma_f32_16x16x32_f16 v[52:55], v[80:83], v[88:91], v[52:55]\n"
"ds_read_b64 v[74:75], v28 offset:1800\n"
"s_and_b32 s79, s45, 0xfc\n"
"v_mfma_f32_16x16x16_bf16 v[44:47], v[38:39], v[2:3], v[44:47]\n"
"ds_read_b128 v[84:87], v29 offset:1792\n"
"s_and_b32 s78, s45, 1\n"
"v_mfma_f32_16x16x32_f16 v[56:59], v[34:37], v[88:91], v[56:59]\n"
"ds_read_b128 v[2:5], v29 offset:1280\n"
"s_waitcnt lgkmcnt(7)\n"
"v_mfma_f32_16x16x32_f16 v[48:51], v[80:83], v[68:71], v[48:51]\n"
"ds_read_b64 v[88:89], v28 offset:1280\n"
"s_lshl_b32 s78, s78, 1\n"
"v_mfma_f32_16x16x32_f16 v[52:55], v[34:37], v[68:71], v[52:55]\n"
"ds_read_b64 v[90:91], v28 offset:1288\n"
"s_add_u32 s79, s79, s78\n"
"v_mfma_f32_16x16x32_f16 v[56:59], v[92:95], v[68:71], v[56:59]\n"
"ds_read_b128 v[68:71], v29 offset:768\n"
"s_lshr_b32 s78, s45, 1\n"
"v_mfma_f32_16x16x32_f16 v[44:47], v[80:83], v[64:67], v[44:47]\n"
"ds_read_b64 v[80:81], v28 offset:768\n"
"s_waitcnt lgkmcnt(8)\n"
"v_mfma_f32_16x16x32_f16 v[48:51], v[34:37], v[64:67], v[48:51]\n"
"ds_read_b64 v[82:83], v28 offset:776\n"
"s_and_b32 s78, s78, 1\n"
"v_mfma_f32_16x16x32_f16 v[52:55], v[92:95], v[64:67], v[52:55]\n"
"ds_read_b128 v[34:37], v29 offset:256\n"
"ds_read_b64 v[92:93], v28 offset:256\n"
"v_mfma_f32_16x16x32_f16 v[56:59], v[76:79], v[64:67], v[56:59]\n"
"ds_read_b64 v[94:95], v28 offset:264\n"
"ds_read_b64 v[64:65], v28 offset:1728\n"
"v_mfma_f32_16x16x16_bf16 v[48:51], v[38:39], v[60:61], v[48:51]\n"
"ds_read_b64 v[66:67], v28 offset:1736\n"
"s_waitcnt lgkmcnt(7)\n"
"v_mfma_f32_16x16x32_f16 v[56:59], v[72:75], v[84:87], v[56:59]\n"
"ds_read_b128 v[76:79], v29 offset:1728\n"
"ds_read_b128 v[60:63], v29 offset:1216\n"
"v_mfma_f32_16x16x32_f16 v[52:55], v[72:75], v[2:5], v[52:55]\n"
"ds_read_b64 v[84:85], v28 offset:1216\n"
"s_add_u32 s79, s79, s78\n"
"v_mfma_f32_16x16x32_f16 v[56:59], v[88:91], v[2:5], v[56:59]\n"
"ds_read_b64 v[86:87], v28 offset:1224\n"
"s_lshl_b32 s79, s79, 7\n"
"v_mfma_f32_16x16x32_f16 v[48:51], v[72:75], v[68:71], v[48:51]\n"
"ds_read_b128 v[2:5], v29 offset:704\n"
"s_waitcnt lgkmcnt(9)\n"
"v_mfma_f32_16x16x32_f16 v[52:55], v[88:91], v[68:71], v[52:55]\n"
"s_add_u32 s46, s79, 0xc28000\n"
"v_mfma_f32_16x16x32_f16 v[56:59], v[80:83], v[68:71], v[56:59]\n"
"ds_read_b64 v[68:69], v30 offset:1024\n"
"v_mfma_f32_16x16x32_f16 v[44:47], v[72:75], v[34:37], v[44:47]\n"
"ds_read_b64 v[72:73], v28 offset:704\n"
"v_mfma_f32_16x16x32_f16 v[48:51], v[88:91], v[34:37], v[48:51]\n"
"ds_read_b64 v[74:75], v28 offset:712\n"
"s_waitcnt lgkmcnt(6)\n"
"v_mfma_f32_16x16x32_f16 v[52:55], v[80:83], v[34:37], v[52:55]\n"
"ds_read_b128 v[88:91], v29 offset:192\n"
"ds_read_b64 v[80:81], v28 offset:192\n"
"v_mfma_f32_16x16x32_f16 v[56:59], v[92:95], v[34:37], v[56:59]\n"
"ds_read_b64 v[82:83], v28 offset:200\n"
"ds_read_b64 v[34:35], v28 offset:1664\n"
"v_mfma_f32_16x16x32_f16 v[56:59], v[64:67], v[76:79], v[56:59]\n"
"ds_read_b64 v[36:37], v28 offset:1672\n"
"ds_read_b128 v[92:95], v29 offset:1664\n"
"v_mfma_f32_16x16x32_f16 v[52:55], v[64:67], v[60:63], v[52:55]\n"
"ds_read_b128 v[76:79], v29 offset:1152\n"
"s_waitcnt lgkmcnt(9)\n"
"v_mfma_f32_16x16x32_f16 v[56:59], v[84:87], v[60:63], v[56:59]\n"
"ds_read_b64 v[60:61], v28 offset:1152\n"
"v_mfma_f32_16x16x32_f16 v[48:51], v[64:67], v[2:5], v[48:51]\n"
"ds_read_b64 v[62:63], v28 offset:1160\n"
"v_mfma_f32_16x16x16_bf16 v[52:55], v[38:39], v[68:69], v[52:55]\n"
"ds_read_b128 v[68:71], v29 offset:640\n"
"s_nop 4\n"
"v_mfma_f32_16x16x32_f16 v[52:55], v[84:87], v[2:5], v[52:55]\n"
"s_waitcnt lgkmcnt(9)\n"
"v_mfma_f32_16x16x32_f16 v[56:59], v[72:75], v[2:5], v[56:59]\n"
"ds_read_b64 v[2:3], v28 offset:640\n"
"v_mfma_f32_16x16x32_f16 v[44:47], v[64:67], v[88:91], v[44:47]\n"
"ds_read_b64 v[4:5], v28 offset:648\n"
"v_mfma_f32_16x16x32_f16 v[48:51], v[84:87], v[88:91], v[48:51]\n"
"ds_read_b128 v[64:67], v29 offset:128\n"
"v_mfma_f32_16x16x32_f16 v[52:55], v[72:75], v[88:91], v[52:55]\n"
"ds_read_b64 v[84:85], v30 offset:1536\n"
"s_waitcnt lgkmcnt(5)\n"
"v_mfma_f32_16x16x32_f16 v[56:59], v[80:83], v[88:91], v[56:59]\n"
"ds_read_b64 v[72:73], v28 offset:128\n"
"ds_read_b64 v[74:75], v28 offset:136\n"
"v_mfma_f32_16x16x32_f16 v[56:59], v[34:37], v[92:95], v[56:59]\n"
"ds_read_b64 v[88:89], v28 offset:1600\n"
"ds_read_b64 v[90:91], v28 offset:1608\n"
"v_mfma_f32_16x16x32_f16 v[52:55], v[34:37], v[76:79], v[52:55]\n"
"ds_read_b128 v[80:83], v29 offset:1600\n"
"v_mfma_f32_16x16x32_f16 v[56:59], v[60:63], v[76:79], v[56:59]\n"
"ds_read_b128 v[92:95], v29 offset:1088\n"
"s_waitcnt lgkmcnt(7)\n"
"v_mfma_f32_16x16x32_f16 v[48:51], v[34:37], v[68:71], v[48:51]\n"
"ds_read_b64 v[76:77], v28 offset:1088\n"
"v_mfma_f32_16x16x32_f16 v[52:55], v[60:63], v[68:71], v[52:55]\n"
"ds_read_b64 v[78:79], v28 offset:1096\n"
"v_mfma_f32_16x16x32_f16 v[56:59], v[2:5], v[68:71], v[56:59]\n"
"ds_read_b128 v[68:71], v29 offset:576\n"
"v_mfma_f32_16x16x32_f16 v[44:47], v[34:37], v[64:67], v[44:47]\n"
"ds_read_b64 v[34:35], v28 offset:576\n"
"s_waitcnt lgkmcnt(8)\n"
"s_nop 0\n"
"v_mfma_f32_16x16x16_bf16 v[56:59], v[38:39], v[84:85], v[56:59]\n"
"ds_read_b64 v[36:37], v28 offset:584\n"
"v_mfma_f32_16x16x32_f16 v[48:51], v[60:63], v[64:67], v[48:51]\n"
"ds_read_b128 v[84:87], v29 offset:64\n"
"ds_read_b64 v[60:61], v28 offset:64\n"
"v_mfma_f32_16x16x32_f16 v[52:55], v[2:5], v[64:67], v[52:55]\n"
"ds_read_b64 v[62:63], v28 offset:72\n"
"ds_read_b64 v[2:3], v28 offset:1536\n"
"v_mfma_f32_16x16x32_f16 v[56:59], v[72:75], v[64:67], v[56:59]\n"
"ds_read_b64 v[4:5], v28 offset:1544\n"
"s_waitcnt lgkmcnt(7)\n"
"v_mfma_f32_16x16x32_f16 v[56:59], v[88:91], v[80:83], v[56:59]\n"
"ds_read_b128 v[64:67], v29 offset:0\n"
"ds_read_b128 v[72:75], v29 offset:512\n"
"v_mfma_f32_16x16x32_f16 v[52:55], v[88:91], v[92:95], v[52:55]\n"
"ds_read_b64 v[80:81], v28 offset:1024\n"
"v_mfma_f32_16x16x32_f16 v[56:59], v[76:79], v[92:95], v[56:59]\n"
"ds_read_b64 v[82:83], v28 offset:1032\n"
"v_mfma_f32_16x16x32_f16 v[48:51], v[88:91], v[68:71], v[48:51]\n"
"ds_read_b128 v[92:95], v29 offset:1024\n"
"s_waitcnt lgkmcnt(9)\n"
"v_mfma_f32_16x16x32_f16 v[52:55], v[76:79], v[68:71], v[52:55]\n"
"v_mfma_f32_16x16x32_f16 v[56:59], v[34:37], v[68:71], v[56:59]\n"
"ds_read_b64 v[68:69], v28 offset:512\n"
"v_mfma_f32_16x16x32_f16 v[44:47], v[88:91], v[84:87], v[44:47]\n"
"ds_read_b64 v[70:71], v28 offset:520\n"
"v_mfma_f32_16x16x32_f16 v[48:51], v[76:79], v[84:87], v[48:51]\n"
"ds_read_b128 v[88:91], v29 offset:1536\n"
"s_waitcnt lgkmcnt(6)\n"
"v_mfma_f32_16x16x32_f16 v[52:55], v[34:37], v[84:87], v[52:55]\n"
"ds_read_b64 v[76:77], v28 offset:0\n"
"v_mfma_f32_16x16x32_f16 v[56:59], v[60:63], v[84:87], v[56:59]\n"
"ds_read_b64 v[78:79], v28 offset:8\n"
"v_mfma_f32_16x16x32_f16 v[44:47], v[2:5], v[64:67], v[44:47]\n"
"v_mfma_f32_16x16x32_f16 v[48:51], v[2:5], v[72:75], v[48:51]\n"
"s_waitcnt lgkmcnt(3)\n"
"v_mfma_f32_16x16x32_f16 v[48:51], v[80:83], v[64:67], v[48:51]\n"
"v_mfma_f32_16x16x32_f16 v[52:55], v[2:5], v[92:95], v[52:55]\n"
"v_mul_f32 v44, v40, v44\n"
"v_mul_f32 v45, v40, v45\n"
"v_mul_f32 v46, v40, v46\n"
"v_mfma_f32_16x16x32_f16 v[52:55], v[80:83], v[72:75], v[52:55]\n"
"v_mul_f32 v47, v40, v47\n"
"v_cvt_pk_bf16_f32 v44, v44, v45\n"
"v_cvt_pk_bf16_f32 v45, v46, v47\n"
"v_mfma_f32_16x16x32_f16 v[52:55], v[68:71], v[64:67], v[52:55]\n"
"v_pk_min_u16 v44, v44, s50\n"
"v_pk_min_u16 v45, v45, s50\n"
"s_waitcnt lgkmcnt(0)\n"
"v_mfma_f32_16x16x32_f16 v[56:59], v[2:5], v[88:91], v[56:59]\n"
"ds_write_b64 v30, v[44:45] offset:0\n"
"v_mfma_f32_16x16x32_f16 v[56:59], v[80:83], v[92:95], v[56:59]\n"
"v_mul_f32 v48, v40, v48\n"
"v_mul_f32 v49, v40, v49\n"
"v_mfma_f32_16x16x32_f16 v[56:59], v[68:71], v[72:75], v[56:59]\n"
"v_mul_f32 v50, v40, v50\n"
"v_mul_f32 v51, v40, v51\n"
"v_mfma_f32_16x16x32_f16 v[56:59], v[76:79], v[64:67], v[56:59]\n"
"v_cvt_pk_bf16_f32 v48, v48, v49\n"
"v_cvt_pk_bf16_f32 v49, v50, v51\n"
"v_pk_min_u16 v48, v48, s50\n"
"v_pk_min_u16 v49, v49, s50\n"
"ds_write_b64 v30, v[48:49] offset:512\n"
"v_mul_f32 v52, v40, v52\n"
"v_mul_f32 v53, v40, v53\n"
"v_mul_f32 v54, v40, v54\n"
"v_mul_f32 v55, v40, v55\n"
"v_cvt_pk_bf16_f32 v52, v52, v53\n"
"v_cvt_pk_bf16_f32 v53, v54, v55\n"
"v_pk_min_u16 v52, v52, s50\n"
"v_pk_min_u16 v53, v53, s50\n"
"ds_write_b64 v30, v[52:53] offset:1024\n"
"v_mul_f32 v56, v40, v56\n"
"v_mul_f32 v57, v40, v57\n"
"v_mul_f32 v58, v40, v58\n"
"v_mul_f32 v59, v40, v59\n"
"v_cvt_pk_bf16_f32 v56, v56, v57\n"
"v_cvt_pk_bf16_f32 v57, v58, v59\n"
"v_pk_min_u16 v56, v56, s50\n"
"v_pk_min_u16 v57, v57, s50\n"
"ds_write_b64 v30, v[56:57] offset:1536\n"
"ds_write_b32 v13, v13 offset:224\n"
"s_mov_b32 s17, 0x100000\n"
".Ltp_84:\n"
"ds_read_b32 v42, v14 offset:224\n"
"s_sub_u32 s17, s17, 1\n"
"s_cbranch_scc1 .Lou_85\n"
"s_waitcnt lgkmcnt(0)\n"
"v_cmp_eq_u32 vcc, 0, v42\n"
"s_cbranch_vccnz .Ltp_84\n"
".Lou_85:\n"
"s_waitcnt lgkmcnt(0)\n"
"ds_read_b64_tr_b16 v[60:61], v31 offset:0\n"
"ds_read_b64_tr_b16 v[62:63], v32 offset:0\n"
"ds_read_b64_tr_b16 v[64:65], v31 offset:1024\n"
"ds_read_b64_tr_b16 v[66:67], v32 offset:1024\n"
"s_add_u32 s47, s46, 0xa0000\n"
"s_waitcnt lgkmcnt(2)\n"
"buffer_store_dwordx4 v[60:63], v33, s[4:7], s46 offen sc1\n"
"s_waitcnt lgkmcnt(0)\n"
"buffer_store_dwordx4 v[64:67], v33, s[4:7], s47 offen sc1\n"
"ds_write_b32 v13, v13 offset:240\n"
"s_setprio 1\n"
"s_mov_b32 s17, 0x100000\n"
".Ltp_86:\n"
"ds_read_b32 v42, v14 offset:240\n"
"s_sub_u32 s17, s17, 1\n"
"s_cbranch_scc1 .Lou_87\n"
"s_waitcnt lgkmcnt(0)\n"
"v_cmp_eq_u32 vcc, 0, v42\n"
"s_cbranch_vccnz .Ltp_86\n"
".Lou_87:\n"
"s_waitcnt lgkmcnt(0)\n"
"s_nop 2\n"
".Lnc_65:\n"
"v_and_b32 v33, 15, v0\n"
"s_lshl_b32 s74, s43, 10\n"
"v_lshrrev_b32 v34, 4, v0\n"
"v_and_b32 v79, 4, v33\n"
"v_lshrrev_b32 v78, 3, v33\n"
"v_and_b32 v77, 3, v33\n"
"v_lshrrev_b32 v76, 1, v34\n"
"v_and_b32 v75, 1, v34\n"
"v_lshlrev_b32 v75, 7, v75\n"
"v_lshl_add_u32 v76, v76, 9, v75\n"
"v_lshl_add_u32 v77, v77, 4, v76\n"
"v_lshl_add_u32 v78, v78, 6, v77\n"
"v_lshl_add_u32 v35, v79, 1, v78\n"
"v_add_u32 v3, s74, v35\n"
"s_add_u32 s79, s74, 0xfffff400\n"
"s_add_u32 s78, s74, 0x4400\n"
"s_cmp_ge_i32 s43, 3\n"
"s_cselect_b32 s79, s79, s78\n"
"v_add_u32 v4, s79, v35\n"
"s_add_u32 s79, s74, 0xfffff800\n"
"s_add_u32 s78, s74, 0x4800\n"
"s_cmp_ge_i32 s43, 2\n"
"s_cselect_b32 s79, s79, s78\n"
"v_add_u32 v5, s79, v35\n"
"s_add_u32 s79, s74, 0xfffffc00\n"
"s_add_u32 s78, s74, 0x4c00\n"
"s_cmp_ge_i32 s43, 1\n"
"s_cselect_b32 s79, s79, s78\n"
"v_add_u32 v6, s79, v35\n"
"s_mul_i32 s79, s43, 0x1400\n"
"v_lshl_add_u32 v79, v0, 7, s79\n"
"v_mov_b32 v78, 0x40000000\n"
"v_cmp_gt_i32 vcc, 40, v0\n"
"s_nop 1\n"
"v_cndmask_b32 v93, v78, v79, vcc\n"
"v_mov_b32 v28, 0x0c0c0c0c\n"
"v_mov_b32 v29, 0xc0c0100\n"
"v_mov_b32 v30, 0x1000c0c\n"
"v_cndmask_b32 v29, v29, v30, s[34:35]\n"
"v_cndmask_b32 v80, v29, v28, s[36:37]\n"
"v_cndmask_b32 v82, v28, v29, s[36:37]\n"
"v_mov_b32 v29, 0xc0c0302\n"
"v_mov_b32 v30, 0x3020c0c\n"
"v_cndmask_b32 v29, v29, v30, s[34:35]\n"
"v_cndmask_b32 v81, v29, v28, s[38:39]\n"
"v_cndmask_b32 v83, v28, v29, s[38:39]\n"
"s_mov_b32 s17, 0x100000\n"
".Ltp_88:\n"
"ds_read_b32 v90, v14 offset:160\n"
"s_sub_u32 s17, s17, 1\n"
"s_cbranch_scc1 .Lou_89\n"
"s_waitcnt lgkmcnt(0)\n"
"v_cmp_eq_u32 vcc, 0, v90\n"
"s_cbranch_vccnz .Ltp_88\n"
".Lou_89:\n"
"s_waitcnt lgkmcnt(0)\n"
"s_cmp_ge_i32 s45, 0\n"
"s_cbranch_scc1 .Lnw_90\n"
"s_mov_b32 s17, 0x100000\n"
".Ltp_91:\n"
"s_sleep 1\n"
"ds_read_b32 v90, v14 offset:176\n"
"s_sub_u32 s17, s17, 1\n"
"s_cbranch_scc1 .Lou_92\n"
"s_waitcnt lgkmcnt(0)\n"
"v_cmp_eq_u32 vcc, 0, v90\n"
"s_cbranch_vccnz .Ltp_91\n"
".Lou_92:\n"
"s_waitcnt lgkmcnt(0)\n"
"s_mov_b32 s89, 30\n"
".Ldl_93:\n"
"s_sub_u32 s89, s89, 1\n"
"s_cbranch_scc1 .Lde_94\n"
"s_sleep 1\n"
"s_branch .Ldl_93\n"
".Lde_94:\n"
"s_and_b32 s79, s2, 7\n"
"s_lshl_b32 s79, s79, 14\n"
"s_add_u32 s46, s79, 0xc08000\n"
"s_add_u32 s47, s46, 0x1000\n"
"buffer_load_dwordx4 v[28:31], v1, s[4:7], s46 offen offset:0 sc1\n"
"buffer_load_dwordx4 v[32:35], v1, s[4:7], s46 offen offset:1024 sc1\n"
"buffer_load_dwordx4 v[36:39], v1, s[4:7], s46 offen offset:2048 sc1\n"
"buffer_load_dwordx4 v[40:43], v1, s[4:7], s46 offen offset:3072 sc1\n"
"buffer_load_dwordx4 v[44:47], v1, s[4:7], s47 offen offset:0 sc1\n"
"buffer_load_dwordx4 v[48:51], v1, s[4:7], s47 offen offset:1024 sc1\n"
"buffer_load_dwordx4 v[52:55], v1, s[4:7], s47 offen offset:2048 sc1\n"
"buffer_load_dwordx4 v[56:59], v1, s[4:7], s47 offen offset:3072 sc1\n"
"s_mov_b32 s17, 32768\n"
".Ltp_95:\n"
"s_waitcnt vmcnt(0)\n"
"s_sub_u32 s17, s17, 1\n"
"s_cbranch_scc1 .Lou_96\n"
"v_max_u32 v60, v28, v29\n"
"v_max3_u32 v60, v30, v31, v60\n"
"v_max3_u32 v60, v32, v33, v60\n"
"v_max3_u32 v60, v34, v35, v60\n"
"v_max3_u32 v60, v36, v37, v60\n"
"v_max3_u32 v60, v38, v39, v60\n"
"v_max3_u32 v60, v40, v41, v60\n"
"v_max3_u32 v60, v42, v43, v60\n"
"v_max3_u32 v60, v44, v45, v60\n"
"v_max3_u32 v60, v46, v47, v60\n"
"v_max3_u32 v60, v48, v49, v60\n"
"v_max3_u32 v60, v50, v51, v60\n"
"v_max3_u32 v60, v52, v53, v60\n"
"v_max3_u32 v60, v54, v55, v60\n"
"v_max3_u32 v60, v56, v57, v60\n"
"v_max3_u32 v60, v58, v59, v60\n"
"v_cmp_eq_u32 vcc, -1, v60\n"
"s_cbranch_vccz .Lou_96\n"
"s_sleep 4\n"
"v_max_u32 v60, v30, v31\n"
"v_max3_u32 v60, v28, v29, v60\n"
"v_cmp_eq_u32 vcc, -1, v60\n"
"s_cbranch_vccz .Lsk_97\n"
"s_mov_b64 exec, vcc\n"
"buffer_load_dwordx4 v[28:31], v1, s[4:7], s46 offen offset:0 sc1\n"
"s_mov_b64 exec, -1\n"
".Lsk_97:\n"
"v_max_u32 v60, v34, v35\n"
"v_max3_u32 v60, v32, v33, v60\n"
"v_cmp_eq_u32 vcc, -1, v60\n"
"s_cbranch_vccz .Lsk_98\n"
"s_mov_b64 exec, vcc\n"
"buffer_load_dwordx4 v[32:35], v1, s[4:7], s46 offen offset:1024 sc1\n"
"s_mov_b64 exec, -1\n"
".Lsk_98:\n"
"v_max_u32 v60, v38, v39\n"
"v_max3_u32 v60, v36, v37, v60\n"
"v_cmp_eq_u32 vcc, -1, v60\n"
"s_cbranch_vccz .Lsk_99\n"
"s_mov_b64 exec, vcc\n"
"buffer_load_dwordx4 v[36:39], v1, s[4:7], s46 offen offset:2048 sc1\n"
"s_mov_b64 exec, -1\n"
".Lsk_99:\n"
"v_max_u32 v60, v42, v43\n"
"v_max3_u32 v60, v40, v41, v60\n"
"v_cmp_eq_u32 vcc, -1, v60\n"
"s_cbranch_vccz .Lsk_100\n"
"s_mov_b64 exec, vcc\n"
"buffer_load_dwordx4 v[40:43], v1, s[4:7], s46 offen offset:3072 sc1\n"
"s_mov_b64 exec, -1\n"
".Lsk_100:\n"
"v_max_u32 v60, v46, v47\n"
"v_max3_u32 v60, v44, v45, v60\n"
"v_cmp_eq_u32 vcc, -1, v60\n"
"s_cbranch_vccz .Lsk_101\n"
"s_mov_b64 exec, vcc\n"
"buffer_load_dwordx4 v[44:47], v1, s[4:7], s47 offen offset:0 sc1\n"
"s_mov_b64 exec, -1\n"
".Lsk_101:\n"
"v_max_u32 v60, v50, v51\n"
"v_max3_u32 v60, v48, v49, v60\n"
"v_cmp_eq_u32 vcc, -1, v60\n"
"s_cbranch_vccz .Lsk_102\n"
"s_mov_b64 exec, vcc\n"
"buffer_load_dwordx4 v[48:51], v1, s[4:7], s47 offen offset:1024 sc1\n"
"s_mov_b64 exec, -1\n"
".Lsk_102:\n"
"v_max_u32 v60, v54, v55\n"
"v_max3_u32 v60, v52, v53, v60\n"
"v_cmp_eq_u32 vcc, -1, v60\n"
"s_cbranch_vccz .Lsk_103\n"
"s_mov_b64 exec, vcc\n"
"buffer_load_dwordx4 v[52:55], v1, s[4:7], s47 offen offset:2048 sc1\n"
"s_mov_b64 exec, -1\n"
".Lsk_103:\n"
"v_max_u32 v60, v58, v59\n"
"v_max3_u32 v60, v56, v57, v60\n"
"v_cmp_eq_u32 vcc, -1, v60\n"
"s_cbranch_vccz .Lsk_104\n"
"s_mov_b64 exec, vcc\n"
"buffer_load_dwordx4 v[56:59], v1, s[4:7], s47 offen offset:3072 sc1\n"
"s_mov_b64 exec, -1\n"
".Lsk_104:\n"
"s_branch .Ltp_95\n"
".Lou_96:\n"
"s_waitcnt vmcnt(0)\n"
"s_mov_b32 s89, 30\n"
".Ldl_105:\n"
"s_sub_u32 s89, s89, 1\n"
"s_cbranch_scc1 .Lde_106\n"
"s_sleep 1\n"
"s_branch .Ldl_105\n"
".Lde_106:\n"
".Lnw_90:\n"
"s_mov_b32 s89, 8\n"
".Ldl_107:\n"
"s_sub_u32 s89, s89, 1\n"
"s_cbranch_scc1 .Lde_108\n"
"s_sleep 1\n"
"s_branch .Ldl_107\n"
".Lde_108:\n"
"s_cmp_ge_i32 s45, 0\n"
"s_cbranch_scc1 .Lnw_109\n"
"v_min_i32 v79, 3, v0\n"
"v_mul_i32_i24 v28, 0x500, v79\n"
"s_and_b32 s79, s2, 7\n"
"s_lshl_b32 s79, s79, 3\n"
"s_add_u32 s79, s79, 7\n"
"s_mul_i32 s79, s79, 0x5000\n"
"s_and_b32 s78, s2, 7\n"
"s_lshr_b32 s78, s78, 1\n"
"s_lshl_b32 s78, s78, 7\n"
"s_add_u32 s79, s79, s78\n"
"s_add_u32 s79, s79, s29\n"
"s_add_u32 s46, s79, 0xc28000\n"
"s_mov_b32 s17, 32768\n"
".Ltp_110:\n"
"buffer_load_dword v29, v28, s[4:7], s46 offen sc1\n"
"s_sub_u32 s17, s17, 1\n"
"s_cbranch_scc1 .Lou_111\n"
"s_waitcnt vmcnt(0)\n"
"v_cmp_eq_u32 vcc, -1, v29\n"
"s_cbranch_vccz .Lou_111\n"
"s_mov_b32 s89, 2\n"
".Ldl_112:\n"
"s_sub_u32 s89, s89, 1\n"
"s_cbranch_scc1 .Lde_113\n"
"s_sleep 1\n"
"s_branch .Ldl_112\n"
".Lde_113:\n"
"s_branch .Ltp_110\n"
".Lou_111:\n"
"s_waitcnt vmcnt(0)\n"
".Lnw_109:\n"
"s_mov_b32 s17, 0x100000\n"
".Ltp_114:\n"
"ds_read_b32 v90, v14 offset:256\n"
"s_sub_u32 s17, s17, 1\n"
"s_cbranch_scc1 .Lou_115\n"
"s_waitcnt lgkmcnt(0)\n"
"v_cmp_eq_u32 vcc, 0, v90\n"
"s_cbranch_vccnz .Ltp_114\n"
".Lou_115:\n"
"s_waitcnt lgkmcnt(0)\n"
"s_mov_b32 s54, 0\n"
"s_nop 1\n"
".Lag_116:\n"
"v_mov_b32 v92, 0\n"
"s_mov_b32 s46, s44\n"
"s_add_u32 s47, s46, 0x1000\n"
"v_add_u32 v2, s29, v1\n"
"buffer_load_dwordx4 v[28:31], v1, s[4:7], s46 offen offset:0 sc1\n"
"buffer_load_dwordx4 v[32:35], v1, s[4:7], s46 offen offset:1024 sc1\n"
"buffer_load_dwordx4 v[36:39], v1, s[4:7], s46 offen offset:2048 sc1\n"
"buffer_load_dwordx4 v[40:43], v1, s[4:7], s46 offen offset:3072 sc1\n"
"buffer_load_dwordx4 v[44:47], v1, s[4:7], s47 offen offset:0 sc1\n"
"s_mov_b32 s17, 32768\n"
".Ltp_119:\n"
"s_waitcnt vmcnt(0)\n"
"s_sub_u32 s17, s17, 1\n"
"s_cbranch_scc1 .Lou_120\n"
"v_max_u32 v48, v28, v29\n"
"v_max3_u32 v48, v30, v31, v48\n"
"v_max3_u32 v48, v32, v33, v48\n"
"v_max3_u32 v48, v34, v35, v48\n"
"v_max3_u32 v48, v36, v37, v48\n"
"v_max3_u32 v48, v38, v39, v48\n"
"v_max3_u32 v48, v40, v41, v48\n"
"v_max3_u32 v48, v42, v43, v48\n"
"v_max3_u32 v48, v44, v45, v48\n"
"v_max3_u32 v48, v46, v47, v48\n"
"v_cmp_eq_u32 vcc, -1, v48\n"
"s_cbranch_vccz .Lou_120\n"
"s_sleep 1\n"
"v_max_u32 v48, v30, v31\n"
"v_max3_u32 v48, v28, v29, v48\n"
"v_cmp_eq_u32 vcc, -1, v48\n"
"s_cbranch_vccz .Lsk_121\n"
"s_mov_b64 exec, vcc\n"
"buffer_load_dwordx4 v[28:31], v1, s[4:7], s46 offen offset:0 sc1\n"
"s_mov_b64 exec, -1\n"
".Lsk_121:\n"
"v_max_u32 v48, v34, v35\n"
"v_max3_u32 v48, v32, v33, v48\n"
"v_cmp_eq_u32 vcc, -1, v48\n"
"s_cbranch_vccz .Lsk_122\n"
"s_mov_b64 exec, vcc\n"
"buffer_load_dwordx4 v[32:35], v1, s[4:7], s46 offen offset:1024 sc1\n"
"s_mov_b64 exec, -1\n"
".Lsk_122:\n"
"v_max_u32 v48, v38, v39\n"
"v_max3_u32 v48, v36, v37, v48\n"
"v_cmp_eq_u32 vcc, -1, v48\n"
"s_cbranch_vccz .Lsk_123\n"
"s_mov_b64 exec, vcc\n"
"buffer_load_dwordx4 v[36:39], v1, s[4:7], s46 offen offset:2048 sc1\n"
"s_mov_b64 exec, -1\n"
".Lsk_123:\n"
"v_max_u32 v48, v42, v43\n"
"v_max3_u32 v48, v40, v41, v48\n"
"v_cmp_eq_u32 vcc, -1, v48\n"
"s_cbranch_vccz .Lsk_124\n"
"s_mov_b64 exec, vcc\n"
"buffer_load_dwordx4 v[40:43], v1, s[4:7], s46 offen offset:3072 sc1\n"
"s_mov_b64 exec, -1\n"
".Lsk_124:\n"
"v_max_u32 v48, v46, v47\n"
"v_max3_u32 v48, v44, v45, v48\n"
"v_cmp_eq_u32 vcc, -1, v48\n"
"s_cbranch_vccz .Lsk_125\n"
"s_mov_b64 exec, vcc\n"
"buffer_load_dwordx4 v[44:47], v1, s[4:7], s47 offen offset:0 sc1\n"
"s_mov_b64 exec, -1\n"
".Lsk_125:\n"
"s_branch .Ltp_119\n"
".Lou_120:\n"
"s_waitcnt vmcnt(0)\n"
"s_add_u32 m0, s29, 0x5000\n"
"s_add_u32 s31, s44, 0x5000\n"
"buffer_load_dwordx4 v1, s[4:7], s31 offen offset:0 sc1 lds\n"
"ds_write_b128 v2, v[28:31] offset:0\n"
"buffer_load_dwordx4 v1, s[4:7], s31 offen offset:1024 sc1 lds\n"
"ds_write_b128 v2, v[32:35] offset:1024\n"
"buffer_load_dwordx4 v1, s[4:7], s31 offen offset:2048 sc1 lds\n"
"ds_write_b128 v2, v[36:39] offset:2048\n"
"buffer_load_dwordx4 v1, s[4:7], s31 offen offset:3072 sc1 lds\n"
"ds_write_b128 v2, v[40:43] offset:3072\n"
"s_add_u32 m0, m0, 1024\n"
"s_add_u32 s31, s31, 1024\n"
"buffer_load_dwordx4 v1, s[4:7], s31 offen offset:3072 sc1 lds\n"
"ds_write_b128 v2, v[44:47] offset:4096\n"
"ds_write_b32 v13, v13 offset:272\n"
"s_mov_b32 s17, 0x100000\n"
".Ltp_126:\n"
"ds_read_b32 v90, v14 offset:272\n"
"s_sub_u32 s17, s17, 1\n"
"s_cbranch_scc1 .Lou_127\n"
"s_waitcnt lgkmcnt(0)\n"
"v_cmp_eq_u32 vcc, 0, v90\n"
"s_cbranch_vccnz .Ltp_126\n"
".Lou_127:\n"
"s_waitcnt lgkmcnt(0)\n"
"ds_read_b64 v[56:57], v3 offset:0\n"
"ds_read_b64 v[58:59], v3 offset:256\n"
"ds_read_b64 v[60:61], v3 offset:1024\n"
"ds_read_b64 v[62:63], v3 offset:1280\n"
"ds_read_b64 v[64:65], v3 offset:2048\n"
"ds_read_b64 v[66:67], v3 offset:2304\n"
"ds_read_b64 v[68:69], v3 offset:3072\n"
"ds_read_b64 v[70:71], v3 offset:3328\n"
"ds_read_b64 v[72:73], v3 offset:4096\n"
"ds_read_b64 v[74:75], v3 offset:4352\n"
"s_waitcnt lgkmcnt(6)\n"
"v_mfma_f32_16x16x32_bf16 v[28:31], v[96:99], v[56:59], 0\n"
"ds_read_b64 v[76:77], v3 offset:5120\n"
"s_add_u32 m0, s29, 0xa000\n"
"v_mfma_f32_16x16x32_bf16 v[32:35], v[100:103], v[56:59], 0\n"
"ds_read_b64 v[78:79], v3 offset:5376\n"
"s_add_u32 s31, s44, 0xa000\n"
"v_mfma_f32_16x16x32_bf16 v[52:55], v[56:59], v[56:59], 0\n"
"v_mfma_f32_16x16x32_bf16 v[28:31], v[104:107], v[60:63], v[28:31]\n"
"ds_read_b64 v[56:57], v3 offset:6144\n"
"buffer_load_dwordx4 v1, s[4:7], s31 offen offset:0 sc1 lds\n"
"v_mfma_f32_16x16x32_bf16 v[32:35], v[108:111], v[60:63], v[32:35]\n"
"ds_read_b64 v[58:59], v3 offset:6400\n"
"s_waitcnt lgkmcnt(6)\n"
"v_mfma_f32_16x16x32_bf16 v[28:31], v[112:115], v[64:67], v[28:31]\n"
"ds_read_b64 v[60:61], v3 offset:7168\n"
"v_mfma_f32_16x16x32_bf16 v[32:35], v[116:119], v[64:67], v[32:35]\n"
"ds_read_b64 v[62:63], v3 offset:7424\n"
"v_mfma_f32_16x16x32_bf16 v[28:31], v[120:123], v[68:71], v[28:31]\n"
"ds_read_b64 v[64:65], v3 offset:8192\n"
"buffer_load_dwordx4 v1, s[4:7], s31 offen offset:1024 sc1 lds\n"
"v_mfma_f32_16x16x32_bf16 v[32:35], v[124:127], v[68:71], v[32:35]\n"
"ds_read_b64 v[66:67], v3 offset:8448\n"
"s_waitcnt lgkmcnt(6)\n"
"v_mfma_f32_16x16x32_bf16 v[28:31], v[128:131], v[72:75], v[28:31]\n"
"ds_read_b64 v[68:69], v3 offset:9216\n"
"v_mfma_f32_16x16x32_bf16 v[32:35], v[132:135], v[72:75], v[32:35]\n"
"ds_read_b64 v[70:71], v3 offset:9472\n"
"v_mfma_f32_16x16x32_bf16 v[52:55], v[72:75], v[72:75], v[52:55]\n"
"buffer_load_dwordx4 v1, s[4:7], s31 offen offset:2048 sc1 lds\n"
"v_mfma_f32_16x16x32_bf16 v[28:31], v[136:139], v[76:79], v[28:31]\n"
"ds_read_b64 v[72:73], v3 offset:10240\n"
"v_mfma_f32_16x16x32_bf16 v[32:35], v[140:143], v[76:79], v[32:35]\n"
"ds_read_b64 v[74:75], v3 offset:10496\n"
"s_waitcnt lgkmcnt(6)\n"
"v_mfma_f32_16x16x32_bf16 v[28:31], v[144:147], v[56:59], v[28:31]\n"
"ds_read_b64 v[76:77], v3 offset:11264\n"
"v_mfma_f32_16x16x32_bf16 v[32:35], v[148:151], v[56:59], v[32:35]\n"
"ds_read_b64 v[78:79], v3 offset:11520\n"
"buffer_load_dwordx4 v1, s[4:7], s31 offen offset:3072 sc1 lds\n"
"v_mfma_f32_16x16x32_bf16 v[28:31], v[152:155], v[60:63], v[28:31]\n"
"ds_read_b64 v[56:57], v3 offset:12288\n"
"s_add_u32 m0, m0, 1024\n"
"v_mfma_f32_16x16x32_bf16 v[32:35], v[156:159], v[60:63], v[32:35]\n"
"ds_read_b64 v[58:59], v3 offset:12544\n"
"s_waitcnt lgkmcnt(6)\n"
"v_mfma_f32_16x16x32_bf16 v[28:31], v[160:163], v[64:67], v[28:31]\n"
"ds_read_b64 v[60:61], v3 offset:13312\n"
"s_add_u32 s31, s31, 1024\n"
"v_mfma_f32_16x16x32_bf16 v[32:35], v[164:167], v[64:67], v[32:35]\n"
"ds_read_b64 v[62:63], v3 offset:13568\n"
"buffer_load_dwordx4 v1, s[4:7], s31 offen offset:3072 sc1 lds\n"
"v_mfma_f32_16x16x32_bf16 v[52:55], v[64:67], v[64:67], v[52:55]\n"
"s_waitcnt vmcnt(5)\n"
"ds_write_b32 v13, v13 offset:288\n"
"v_mfma_f32_16x16x32_bf16 v[28:31], v[168:171], v[68:71], v[28:31]\n"
"ds_read_b64 v[64:65], v3 offset:14336\n"
"s_add_u32 m0, s29, 0xf000\n"
"s_add_u32 s31, s44, 0xf000\n"
"v_mfma_f32_16x16x32_bf16 v[32:35], v[172:175], v[68:71], v[32:35]\n"
"ds_read_b64 v[66:67], v3 offset:14592\n"
"s_waitcnt lgkmcnt(7)\n"
"v_mfma_f32_16x16x32_bf16 v[28:31], v[176:179], v[72:75], v[28:31]\n"
"ds_read_b64 v[68:69], v3 offset:15360\n"
"buffer_load_dwordx4 v1, s[4:7], s31 offen offset:0 sc1 lds\n"
"v_mfma_f32_16x16x32_bf16 v[32:35], v[180:183], v[72:75], v[32:35]\n"
"ds_read_b64 v[70:71], v3 offset:15616\n"
"v_mfma_f32_16x16x32_bf16 v[28:31], v[184:187], v[76:79], v[28:31]\n"
"ds_read_b64 v[72:73], v3 offset:16384\n"
"v_mfma_f32_16x16x32_bf16 v[32:35], v[188:191], v[76:79], v[32:35]\n"
"ds_read_b64 v[74:75], v3 offset:16640\n"
"s_waitcnt lgkmcnt(7)\n"
"v_mfma_f32_16x16x32_bf16 v[28:31], v[192:195], v[56:59], v[28:31]\n"
"ds_read_b64 v[76:77], v4 offset:0\n"
"buffer_load_dwordx4 v1, s[4:7], s31 offen offset:1024 sc1 lds\n"
"v_mfma_f32_16x16x32_bf16 v[32:35], v[196:199], v[56:59], v[32:35]\n"
"ds_read_b64 v[78:79], v4 offset:256\n"
"ds_read_b32 v90, v14 offset:288\n"
"v_mfma_f32_16x16x32_bf16 v[52:55], v[56:59], v[56:59], v[52:55]\n"
"v_mfma_f32_16x16x32_bf16 v[28:31], v[200:203], v[60:63], v[28:31]\n"
"ds_read_b64 v[56:57], v5 offset:0\n"
"v_mfma_f32_16x16x32_bf16 v[32:35], v[204:207], v[60:63], v[32:35]\n"
"ds_read_b64 v[58:59], v5 offset:256\n"
"buffer_load_dwordx4 v1, s[4:7], s31 offen offset:2048 sc1 lds\n"
"s_waitcnt lgkmcnt(7)\n"
"v_mfma_f32_16x16x32_bf16 v[28:31], v[208:211], v[64:67], v[28:31]\n"
"ds_read_b64 v[60:61], v6 offset:0\n"
"s_waitcnt lgkmcnt(3)\n"
"v_cmp_eq_u32 vcc, 0, v90\n"
"v_mfma_f32_16x16x32_bf16 v[32:35], v[212:215], v[64:67], v[32:35]\n"
"ds_read_b64 v[62:63], v6 offset:256\n"
"v_mfma_f32_16x16x32_bf16 v[28:31], v[216:219], v[68:71], v[28:31]\n"
"s_cbranch_vccnz .Lsl_128\n"
".Lbk_129:\n"
"ds_read_b64 v[64:65], v3 offset:20480\n"
"v_mfma_f32_16x16x32_bf16 v[32:35], v[220:223], v[68:71], v[32:35]\n"
"ds_read_b64 v[66:67], v3 offset:20736\n"
"buffer_load_dwordx4 v1, s[4:7], s31 offen offset:3072 sc1 lds\n"
"v_mfma_f32_16x16x32_bf16 v[28:31], v[224:227], v[72:75], v[28:31]\n"
"ds_read_b64 v[68:69], v3 offset:21504\n"
"s_add_u32 m0, m0, 1024\n"
"v_mfma_f32_16x16x32_bf16 v[32:35], v[228:231], v[72:75], v[32:35]\n"
"ds_read_b64 v[70:71], v3 offset:21760\n"
"s_add_u32 s31, s31, 1024\n"
"v_mfma_f32_16x16x32_bf16 v[52:55], v[72:75], v[72:75], v[52:55]\n"
"v_mfma_f32_16x16x32_bf16 v[28:31], v[232:235], v[76:79], v[28:31]\n"
"ds_read_b64 v[72:73], v3 offset:22528\n"
"buffer_load_dwordx4 v1, s[4:7], s31 offen offset:3072 sc1 lds\n"
"v_mfma_f32_16x16x32_bf16 v[32:35], v[236:239], v[76:79], v[32:35]\n"
"ds_read_b64 v[74:75], v3 offset:22784\n"
"s_waitcnt lgkmcnt(6)\n"
"v_mfma_f32_16x16x32_bf16 v[28:31], v[240:243], v[56:59], v[28:31]\n"
"ds_read_b64 v[76:77], v3 offset:23552\n"
"v_cndmask_b32 v94, v52, v53, s[34:35]\n"
"v_mfma_f32_16x16x32_bf16 v[32:35], v[244:247], v[56:59], v[32:35]\n"
"ds_read_b64 v[78:79], v3 offset:23808\n"
"v_cndmask_b32 v54, v54, v55, s[34:35]\n"
"v_mfma_f32_16x16x32_bf16 v[28:31], v[248:251], v[60:63], v[28:31]\n"
"ds_read_b64 v[56:57], v3 offset:24576\n"
"v_cndmask_b32 v94, v94, v54, s[20:21]\n"
"v_mfma_f32_16x16x32_bf16 v[32:35], v[252:255], v[60:63], v[32:35]\n"
"ds_read_b64 v[58:59], v3 offset:24832\n"
"s_waitcnt lgkmcnt(6)\n"
"v_mfma_f32_16x16x32_bf16 v[36:39], v[96:99], v[64:67], 0\n"
"ds_read_b64 v[60:61], v3 offset:25600\n"
"v_mfma_f32_16x16x32_bf16 v[40:43], v[100:103], v[64:67], 0\n"
"ds_read_b64 v[62:63], v3 offset:25856\n"
"v_cndmask_b32 v94, v28, v94, s[22:23]\n"
"s_waitcnt vmcnt(5)\n"
"v_mfma_f32_16x16x32_bf16 v[52:55], v[64:67], v[64:67], 0\n"
"ds_write_b32 v9, v94 offset:1856\n"
"v_mfma_f32_16x16x32_bf16 v[36:39], v[104:107], v[68:71], v[36:39]\n"
"ds_read_b64 v[64:65], v3 offset:26624\n"
"v_mfma_f32_16x16x32_bf16 v[40:43], v[108:111], v[68:71], v[40:43]\n"
"ds_read_b64 v[66:67], v3 offset:26880\n"
"s_waitcnt lgkmcnt(7)\n"
"v_mfma_f32_16x16x32_bf16 v[36:39], v[112:115], v[72:75], v[36:39]\n"
"ds_read_b64 v[68:69], v3 offset:27648\n"
"v_mfma_f32_16x16x32_bf16 v[40:43], v[116:119], v[72:75], v[40:43]\n"
"ds_read_b64 v[70:71], v3 offset:27904\n"
"v_mfma_f32_16x16x32_bf16 v[36:39], v[120:123], v[76:79], v[36:39]\n"
"ds_read_b64 v[72:73], v3 offset:28672\n"
"v_mfma_f32_16x16x32_bf16 v[40:43], v[124:127], v[76:79], v[40:43]\n"
"ds_read_b64 v[74:75], v3 offset:28928\n"
"s_waitcnt lgkmcnt(7)\n"
"v_mfma_f32_16x16x32_bf16 v[36:39], v[128:131], v[56:59], v[36:39]\n"
"ds_read_b64 v[76:77], v3 offset:29696\n"
"v_mfma_f32_16x16x32_bf16 v[40:43], v[132:135], v[56:59], v[40:43]\n"
"ds_read_b64 v[78:79], v3 offset:29952\n"
"v_mfma_f32_16x16x32_bf16 v[52:55], v[56:59], v[56:59], v[52:55]\n"
"v_mfma_f32_16x16x32_bf16 v[36:39], v[136:139], v[60:63], v[36:39]\n"
"ds_read_b64 v[56:57], v3 offset:30720\n"
"v_mfma_f32_16x16x32_bf16 v[40:43], v[140:143], v[60:63], v[40:43]\n"
"ds_read_b64 v[58:59], v3 offset:30976\n"
"s_waitcnt lgkmcnt(6)\n"
"v_mfma_f32_16x16x32_bf16 v[36:39], v[144:147], v[64:67], v[36:39]\n"
"ds_read_b64 v[60:61], v3 offset:31744\n"
"ds_read_b128 v[84:87], v11 offset:1856\n"
"ds_read_b32 v91, v12 offset:1856\n"
"v_mfma_f32_16x16x32_bf16 v[40:43], v[148:151], v[64:67], v[40:43]\n"
"ds_read_b64 v[62:63], v3 offset:32000\n"
"v_mfma_f32_16x16x32_bf16 v[36:39], v[152:155], v[68:71], v[36:39]\n"
"ds_read_b64 v[64:65], v3 offset:32768\n"
"v_mfma_f32_16x16x32_bf16 v[40:43], v[156:159], v[68:71], v[40:43]\n"
"ds_read_b64 v[66:67], v3 offset:33024\n"
"s_waitcnt lgkmcnt(8)\n"
"v_mfma_f32_16x16x32_bf16 v[36:39], v[160:163], v[72:75], v[36:39]\n"
"ds_read_b64 v[68:69], v3 offset:33792\n"
"v_mfma_f32_16x16x32_bf16 v[40:43], v[164:167], v[72:75], v[40:43]\n"
"ds_read_b64 v[70:71], v3 offset:34048\n"
"v_mfma_f32_16x16x32_bf16 v[52:55], v[72:75], v[72:75], v[52:55]\n"
"v_mfma_f32_16x16x32_bf16 v[36:39], v[168:171], v[76:79], v[36:39]\n"
"ds_read_b64 v[72:73], v3 offset:34816\n"
"s_waitcnt lgkmcnt(7)\n"
"v_add_f32 v84, v84, v85\n"
"v_add_f32 v86, v86, v87\n"
"v_mfma_f32_16x16x32_bf16 v[40:43], v[172:175], v[76:79], v[40:43]\n"
"ds_read_b64 v[74:75], v3 offset:35072\n"
"v_add_f32 v84, v84, v86\n"
"s_waitcnt lgkmcnt(6)\n"
"v_mfma_f32_16x16x32_bf16 v[36:39], v[176:179], v[56:59], v[36:39]\n"
"ds_read_b64 v[76:77], v3 offset:35840\n"
"v_mul_f32 v89, 0x3acccccd, v91\n"
"v_fma_f32 v84, -v91, v89, v84\n"
"v_mfma_f32_16x16x32_bf16 v[40:43], v[180:183], v[56:59], v[40:43]\n"
"ds_read_b64 v[78:79], v3 offset:36096\n"
"v_fmamk_f32 v84, v84, 0x3acccccd, v27\n"
"v_mfma_f32_16x16x32_bf16 v[36:39], v[184:187], v[60:63], v[36:39]\n"
"ds_read_b64 v[56:57], v3 offset:36864\n"
"v_cmp_u_f32 vcc, v84, v84\n"
"v_mfma_f32_16x16x32_bf16 v[40:43], v[188:191], v[60:63], v[40:43]\n"
"ds_read_b64 v[58:59], v3 offset:37120\n"
"s_waitcnt lgkmcnt(6)\n"
"v_mfma_f32_16x16x32_bf16 v[36:39], v[192:195], v[64:67], v[36:39]\n"
"ds_read_b64 v[60:61], v4 offset:20480\n"
"v_max_f32 v84, v84, v27\n"
"v_mfma_f32_16x16x32_bf16 v[40:43], v[196:199], v[64:67], v[40:43]\n"
"ds_read_b64 v[62:63], v4 offset:20736\n"
"v_rsq_f32 v88, v84\n"
"v_mfma_f32_16x16x32_bf16 v[52:55], v[64:67], v[64:67], v[52:55]\n"
"v_mul_f32 v89, -v89, v88\n"
"v_mfma_f32_16x16x32_bf16 v[36:39], v[200:203], v[68:71], v[36:39]\n"
"ds_read_b64 v[64:65], v5 offset:20480\n"
"s_cbranch_vccnz .Lsl_132\n"
".Lbk_133:\n"
"v_mfma_f32_16x16x32_bf16 v[40:43], v[204:207], v[68:71], v[40:43]\n"
"ds_read_b64 v[66:67], v5 offset:20736\n"
"s_add_u32 m0, s29, 0\n"
"s_waitcnt lgkmcnt(6)\n"
"v_mfma_f32_16x16x32_bf16 v[36:39], v[208:211], v[72:75], v[36:39]\n"
"ds_read_b64 v[68:69], v6 offset:20480\n"
"s_sub_u32 s79, s44, 0x5000\n"
"s_add_u32 s78, s29, 0xc28000\n"
"v_mfma_f32_16x16x32_bf16 v[40:43], v[212:215], v[72:75], v[40:43]\n"
"ds_read_b64 v[70:71], v6 offset:20736\n"
"s_max_i32 s31, s79, s78\n"
"v_mul_f32 v28, v88, v28\n"
"v_mfma_f32_16x16x32_bf16 v[36:39], v[216:219], v[76:79], v[36:39]\n"
"ds_read_b64 v[72:73], v3 offset:40960\n"
"buffer_load_dwordx4 v1, s[4:7], s31 offen offset:0 sc1 lds\n"
"v_mul_f32 v29, v88, v29\n"
"v_mfma_f32_16x16x32_bf16 v[40:43], v[220:223], v[76:79], v[40:43]\n"
"ds_read_b64 v[74:75], v3 offset:41216\n"
"s_waitcnt lgkmcnt(6)\n"
"v_mfma_f32_16x16x32_bf16 v[36:39], v[224:227], v[56:59], v[36:39]\n"
"ds_read_b64 v[76:77], v3 offset:41984\n"
"v_mul_f32 v30, v88, v30\n"
"v_mfma_f32_16x16x32_bf16 v[40:43], v[228:231], v[56:59], v[40:43]\n"
"ds_read_b64 v[78:79], v3 offset:42240\n"
"v_mul_f32 v31, v88, v31\n"
"v_mfma_f32_16x16x32_bf16 v[52:55], v[56:59], v[56:59], v[52:55]\n"
"buffer_load_dwordx4 v1, s[4:7], s31 offen offset:1024 sc1 lds\n"
"v_fmac_f32 v28, v89, v16\n"
"v_mfma_f32_16x16x32_bf16 v[36:39], v[232:235], v[60:63], v[36:39]\n"
"ds_read_b64 v[56:57], v3 offset:43008\n"
"v_fmac_f32 v29, v89, v17\n"
"v_mfma_f32_16x16x32_bf16 v[40:43], v[236:239], v[60:63], v[40:43]\n"
"ds_read_b64 v[58:59], v3 offset:43264\n"
"s_waitcnt lgkmcnt(6)\n"
"v_mfma_f32_16x16x32_bf16 v[36:39], v[240:243], v[64:67], v[36:39]\n"
"ds_read_b64 v[60:61], v3 offset:44032\n"
"v_cndmask_b32 v94, v52, v53, s[34:35]\n"
"v_mfma_f32_16x16x32_bf16 v[40:43], v[244:247], v[64:67], v[40:43]\n"
"ds_read_b64 v[62:63], v3 offset:44288\n"
"v_cndmask_b32 v54, v54, v55, s[34:35]\n"
"buffer_load_dwordx4 v1, s[4:7], s31 offen offset:2048 sc1 lds\n"
"v_mfma_f32_16x16x32_bf16 v[36:39], v[248:251], v[68:71], v[36:39]\n"
"ds_read_b64 v[64:65], v3 offset:45056\n"
"v_cndmask_b32 v94, v94, v54, s[20:21]\n"
"v_mfma_f32_16x16x32_bf16 v[40:43], v[252:255], v[68:71], v[40:43]\n"
"ds_read_b64 v[66:67], v3 offset:45312\n"
"s_waitcnt lgkmcnt(6)\n"
"v_mfma_f32_16x16x32_bf16 v[44:47], v[96:99], v[72:75], 0\n"
"ds_read_b64 v[68:69], v3 offset:46080\n"
"v_fmac_f32 v30, v89, v18\n"
"v_mfma_f32_16x16x32_bf16 v[48:51], v[100:103], v[72:75], 0\n"
"ds_read_b64 v[70:71], v3 offset:46336\n"
"v_cndmask_b32 v94, v36, v94, s[22:23]\n"
"s_waitcnt vmcnt(3)\n"
"buffer_load_dwordx4 v1, s[4:7], s31 offen offset:3072 sc1 lds\n"
"v_mfma_f32_16x16x32_bf16 v[52:55], v[72:75], v[72:75], 0\n"
"ds_write_b32 v9, v94 offset:2176\n"
"s_add_u32 m0, m0, 1024\n"
"v_mfma_f32_16x16x32_bf16 v[44:47], v[104:107], v[76:79], v[44:47]\n"
"ds_read_b64 v[72:73], v3 offset:47104\n"
"s_add_u32 s31, s31, 1024\n"
"v_mfma_f32_16x16x32_bf16 v[48:51], v[108:111], v[76:79], v[48:51]\n"
"ds_read_b64 v[74:75], v3 offset:47360\n"
"s_waitcnt lgkmcnt(7)\n"
"v_mfma_f32_16x16x32_bf16 v[44:47], v[112:115], v[56:59], v[44:47]\n"
"ds_read_b64 v[76:77], v3 offset:48128\n"
"buffer_load_dwordx4 v1, s[4:7], s31 offen offset:3072 sc1 lds\n"
"v_mfma_f32_16x16x32_bf16 v[48:51], v[116:119], v[56:59], v[48:51]\n"
"ds_read_b64 v[78:79], v3 offset:48384\n"
"v_fmac_f32 v31, v89, v19\n"
"v_mfma_f32_16x16x32_bf16 v[44:47], v[120:123], v[60:63], v[44:47]\n"
"ds_read_b64 v[56:57], v3 offset:49152\n"
"v_cvt_pk_bf16_f32 v28, v28, v29\n"
"v_mfma_f32_16x16x32_bf16 v[48:51], v[124:127], v[60:63], v[48:51]\n"
"ds_read_b64 v[58:59], v3 offset:49408\n"
"s_waitcnt lgkmcnt(7)\n"
"v_mfma_f32_16x16x32_bf16 v[44:47], v[128:131], v[64:67], v[44:47]\n"
"ds_read_b64 v[60:61], v3 offset:50176\n"
"v_cvt_pk_bf16_f32 v29, v30, v31\n"
"v_mfma_f32_16x16x32_bf16 v[48:51], v[132:135], v[64:67], v[48:51]\n"
"ds_read_b64 v[62:63], v3 offset:50432\n"
"v_mul_f32 v32, v88, v32\n"
"v_mfma_f32_16x16x32_bf16 v[52:55], v[64:67], v[64:67], v[52:55]\n"
"v_mul_f32 v33, v88, v33\n"
"v_mul_f32 v34, v88, v34\n"
"v_mfma_f32_16x16x32_bf16 v[44:47], v[136:139], v[68:71], v[44:47]\n"
"ds_read_b64 v[64:65], v3 offset:51200\n"
"v_mul_f32 v35, v88, v35\n"
"v_mfma_f32_16x16x32_bf16 v[48:51], v[140:143], v[68:71], v[48:51]\n"
"ds_read_b64 v[66:67], v3 offset:51456\n"
"s_waitcnt lgkmcnt(6)\n"
"v_mfma_f32_16x16x32_bf16 v[44:47], v[144:147], v[72:75], v[44:47]\n"
"ds_read_b64 v[68:69], v3 offset:52224\n"
"ds_read_b128 v[84:87], v11 offset:2176\n"
"ds_read_b32 v91, v12 offset:2176\n"
"v_mfma_f32_16x16x32_bf16 v[48:51], v[148:151], v[72:75], v[48:51]\n"
"ds_read_b64 v[70:71], v3 offset:52480\n"
"v_fmac_f32 v32, v89, v20\n"
"v_mfma_f32_16x16x32_bf16 v[44:47], v[152:155], v[76:79], v[44:47]\n"
"ds_read_b64 v[72:73], v3 offset:53248\n"
"v_fmac_f32 v33, v89, v21\n"
"v_mfma_f32_16x16x32_bf16 v[48:51], v[156:159], v[76:79], v[48:51]\n"
"ds_read_b64 v[74:75], v3 offset:53504\n"
"s_waitcnt lgkmcnt(8)\n"
"v_mfma_f32_16x16x32_bf16 v[44:47], v[160:163], v[56:59], v[44:47]\n"
"ds_read_b64 v[76:77], v3 offset:54272\n"
"v_fmac_f32 v34, v89, v22\n"
"v_mfma_f32_16x16x32_bf16 v[48:51], v[164:167], v[56:59], v[48:51]\n"
"ds_read_b64 v[78:79], v3 offset:54528\n"
"v_fmac_f32 v35, v89, v23\n"
"v_mfma_f32_16x16x32_bf16 v[52:55], v[56:59], v[56:59], v[52:55]\n"
"v_cvt_pk_bf16_f32 v32, v32, v33\n"
"v_cvt_pk_bf16_f32 v33, v34, v35\n"
"v_mfma_f32_16x16x32_bf16 v[44:47], v[168:171], v[60:63], v[44:47]\n"
"ds_read_b64 v[56:57], v3 offset:55296\n"
"s_waitcnt lgkmcnt(7)\n"
"v_add_f32 v84, v84, v85\n"
"v_add_f32 v86, v86, v87\n"
"v_mfma_f32_16x16x32_bf16 v[48:51], v[172:175], v[60:63], v[48:51]\n"
"ds_read_b64 v[58:59], v3 offset:55552\n"
"v_add_f32 v84, v84, v86\n"
"s_waitcnt lgkmcnt(6)\n"
"v_mfma_f32_16x16x32_bf16 v[44:47], v[176:179], v[64:67], v[44:47]\n"
"ds_read_b64 v[60:61], v3 offset:56320\n"
"v_mul_f32 v89, 0x3acccccd, v91\n"
"v_fma_f32 v84, -v91, v89, v84\n"
"v_mfma_f32_16x16x32_bf16 v[48:51], v[180:183], v[64:67], v[48:51]\n"
"ds_read_b64 v[62:63], v3 offset:56576\n"
"v_fmamk_f32 v84, v84, 0x3acccccd, v27\n"
"v_mfma_f32_16x16x32_bf16 v[44:47], v[184:187], v[68:71], v[44:47]\n"
"ds_read_b64 v[64:65], v3 offset:57344\n"
"v_cmp_u_f32 vcc, v84, v84\n"
"v_mfma_f32_16x16x32_bf16 v[48:51], v[188:191], v[68:71], v[48:51]\n"
"ds_read_b64 v[66:67], v3 offset:57600\n"
"s_waitcnt lgkmcnt(6)\n"
"v_mfma_f32_16x16x32_bf16 v[44:47], v[192:195], v[72:75], v[44:47]\n"
"ds_read_b64 v[68:69], v4 offset:40960\n"
"v_max_f32 v84, v84, v27\n"
"v_mfma_f32_16x16x32_bf16 v[48:51], v[196:199], v[72:75], v[48:51]\n"
"ds_read_b64 v[70:71], v4 offset:41216\n"
"v_rsq_f32 v88, v84\n"
"v_mfma_f32_16x16x32_bf16 v[52:55], v[72:75], v[72:75], v[52:55]\n"
"v_mul_f32 v89, -v89, v88\n"
"v_mfma_f32_16x16x32_bf16 v[44:47], v[200:203], v[76:79], v[44:47]\n"
"ds_read_b64 v[72:73], v5 offset:40960\n"
"s_cbranch_vccnz .Lsl_136\n"
".Lbk_137:\n"
"v_mfma_f32_16x16x32_bf16 v[48:51], v[204:207], v[76:79], v[48:51]\n"
"ds_read_b64 v[74:75], v5 offset:41216\n"
"ds_write_b64 v7, v[28:29] offset:3968\n"
"s_waitcnt lgkmcnt(7)\n"
"v_mfma_f32_16x16x32_bf16 v[44:47], v[208:211], v[56:59], v[44:47]\n"
"ds_read_b64 v[76:77], v6 offset:40960\n"
"v_mul_f32 v36, v88, v36\n"
"v_mfma_f32_16x16x32_bf16 v[48:51], v[212:215], v[56:59], v[48:51]\n"
"ds_read_b64 v[78:79], v6 offset:41216\n"
"v_mul_f32 v37, v88, v37\n"
"v_mfma_f32_16x16x32_bf16 v[44:47], v[216:219], v[60:63], v[44:47]\n"
"v_add_u32 v3, 0x5000, v3\n"
"ds_read_b64 v[56:57], v3 offset:40960\n"
"v_mfma_f32_16x16x32_bf16 v[48:51], v[220:223], v[60:63], v[48:51]\n"
"ds_read_b64 v[58:59], v3 offset:41216\n"
"s_waitcnt lgkmcnt(7)\n"
"v_mfma_f32_16x16x32_bf16 v[44:47], v[224:227], v[64:67], v[44:47]\n"
"ds_read_b64 v[60:61], v3 offset:41984\n"
"v_mul_f32 v38, v88, v38\n"
"v_mfma_f32_16x16x32_bf16 v[48:51], v[228:231], v[64:67], v[48:51]\n"
"ds_read_b64 v[62:63], v3 offset:42240\n"
"v_mov_b32 v84, 0x3f803f80\n"
"v_mfma_f32_16x16x32_bf16 v[52:55], v[64:67], v[64:67], v[52:55]\n"
"v_mov_b32 v85, 0x3f803f80\n"
"ds_write_b64 v8, v[32:33] offset:3968\n"
"v_mfma_f32_16x16x32_bf16 v[44:47], v[232:235], v[68:71], v[44:47]\n"
"ds_read_b64 v[64:65], v3 offset:43008\n"
"v_mul_f32 v39, v88, v39\n"
"v_mfma_f32_16x16x32_bf16 v[48:51], v[236:239], v[68:71], v[48:51]\n"
"ds_read_b64 v[66:67], v3 offset:43264\n"
"s_waitcnt lgkmcnt(7)\n"
"v_mfma_f32_16x16x32_bf16 v[44:47], v[240:243], v[72:75], v[44:47]\n"
"ds_read_b64 v[68:69], v3 offset:44032\n"
"v_cndmask_b32 v94, v52, v53, s[34:35]\n"
"v_mfma_f32_16x16x32_bf16 v[48:51], v[244:247], v[72:75], v[48:51]\n"
"ds_read_b64 v[70:71], v3 offset:44288\n"
"v_cndmask_b32 v54, v54, v55, s[34:35]\n"
"v_mfma_f32_16x16x32_bf16 v[44:47], v[248:251], v[76:79], v[44:47]\n"
"ds_read_b64 v[72:73], v3 offset:45056\n"
"v_cndmask_b32 v94, v94, v54, s[20:21]\n"
"v_mfma_f32_16x16x32_bf16 v[48:51], v[252:255], v[76:79], v[48:51]\n"
"ds_read_b64 v[74:75], v3 offset:45312\n"
"s_waitcnt lgkmcnt(7)\n"
"v_mfma_f32_16x16x32_bf16 v[28:31], v[96:99], v[56:59], 0\n"
"ds_read_b64 v[76:77], v3 offset:46080\n"
"v_fmac_f32 v36, v89, v16\n"
"v_mfma_f32_16x16x32_bf16 v[32:35], v[100:103], v[56:59], 0\n"
"ds_read_b64 v[78:79], v3 offset:46336\n"
"v_cndmask_b32 v94, v44, v94, s[22:23]\n"
"s_waitcnt vmcnt(0)\n"
"ds_write_b64 v93, v[84:85] offset:0\n"
"v_mfma_f32_16x16x32_bf16 v[52:55], v[56:59], v[56:59], 0\n"
"ds_write_b32 v9, v94 offset:2496\n"
"v_mfma_f32_16x16x32_bf16 v[28:31], v[104:107], v[60:63], v[28:31]\n"
"ds_read_b64 v[56:57], v3 offset:47104\n"
"v_fmac_f32 v37, v89, v17\n"
"v_mfma_f32_16x16x32_bf16 v[32:35], v[108:111], v[60:63], v[32:35]\n"
"ds_read_b64 v[58:59], v3 offset:47360\n"
"s_waitcnt lgkmcnt(8)\n"
"v_mfma_f32_16x16x32_bf16 v[28:31], v[112:115], v[64:67], v[28:31]\n"
"ds_read_b64 v[60:61], v3 offset:48128\n"
"v_fmac_f32 v38, v89, v18\n"
"v_mfma_f32_16x16x32_bf16 v[32:35], v[116:119], v[64:67], v[32:35]\n"
"ds_read_b64 v[62:63], v3 offset:48384\n"
"v_fmac_f32 v39, v89, v19\n"
"v_mfma_f32_16x16x32_bf16 v[28:31], v[120:123], v[68:71], v[28:31]\n"
"ds_read_b64 v[64:65], v3 offset:49152\n"
"v_cvt_pk_bf16_f32 v36, v36, v37\n"
"v_mfma_f32_16x16x32_bf16 v[32:35], v[124:127], v[68:71], v[32:35]\n"
"ds_read_b64 v[66:67], v3 offset:49408\n"
"s_waitcnt lgkmcnt(8)\n"
"v_mfma_f32_16x16x32_bf16 v[28:31], v[128:131], v[72:75], v[28:31]\n"
"ds_read_b64 v[68:69], v3 offset:50176\n"
"v_cvt_pk_bf16_f32 v37, v38, v39\n"
"v_mfma_f32_16x16x32_bf16 v[32:35], v[132:135], v[72:75], v[32:35]\n"
"ds_read_b64 v[70:71], v3 offset:50432\n"
"v_mul_f32 v40, v88, v40\n"
"v_mfma_f32_16x16x32_bf16 v[52:55], v[72:75], v[72:75], v[52:55]\n"
"v_mul_f32 v41, v88, v41\n"
"v_mul_f32 v42, v88, v42\n"
"v_mfma_f32_16x16x32_bf16 v[28:31], v[136:139], v[76:79], v[28:31]\n"
"ds_read_b64 v[72:73], v3 offset:51200\n"
"v_mul_f32 v43, v88, v43\n"
"v_mfma_f32_16x16x32_bf16 v[32:35], v[140:143], v[76:79], v[32:35]\n"
"ds_read_b64 v[74:75], v3 offset:51456\n"
"s_waitcnt lgkmcnt(6)\n"
"v_mfma_f32_16x16x32_bf16 v[28:31], v[144:147], v[56:59], v[28:31]\n"
"ds_read_b64 v[76:77], v3 offset:52224\n"
"ds_read_b128 v[84:87], v11 offset:2496\n"
"ds_read_b32 v91, v12 offset:2496\n"
"v_mfma_f32_16x16x32_bf16 v[32:35], v[148:151], v[56:59], v[32:35]\n"
"ds_read_b64 v[78:79], v3 offset:52480\n"
"v_fmac_f32 v40, v89, v20\n"
"v_mfma_f32_16x16x32_bf16 v[28:31], v[152:155], v[60:63], v[28:31]\n"
"ds_read_b64 v[56:57], v3 offset:53248\n"
"v_fmac_f32 v41, v89, v21\n"
"v_mfma_f32_16x16x32_bf16 v[32:35], v[156:159], v[60:63], v[32:35]\n"
"ds_read_b64 v[58:59], v3 offset:53504\n"
"s_waitcnt lgkmcnt(8)\n"
"v_mfma_f32_16x16x32_bf16 v[28:31], v[160:163], v[64:67], v[28:31]\n"
"ds_read_b64 v[60:61], v3 offset:54272\n"
"v_fmac_f32 v42, v89, v22\n"
"v_mfma_f32_16x16x32_bf16 v[32:35], v[164:167], v[64:67], v[32:35]\n"
"ds_read_b64 v[62:63], v3 offset:54528\n"
"v_fmac_f32 v43, v89, v23\n"
"v_mfma_f32_16x16x32_bf16 v[52:55], v[64:67], v[64:67], v[52:55]\n"
"v_cvt_pk_bf16_f32 v40, v40, v41\n"
"v_cvt_pk_bf16_f32 v41, v42, v43\n"
"v_mfma_f32_16x16x32_bf16 v[28:31], v[168:171], v[68:71], v[28:31]\n"
"ds_read_b64 v[64:65], v3 offset:55296\n"
"s_waitcnt lgkmcnt(7)\n"
"v_add_f32 v84, v84, v85\n"
"v_add_f32 v86, v86, v87\n"
"v_mfma_f32_16x16x32_bf16 v[32:35], v[172:175], v[68:71], v[32:35]\n"
"ds_read_b64 v[66:67], v3 offset:55552\n"
"v_add_f32 v84, v84, v86\n"
"s_waitcnt lgkmcnt(6)\n"
"v_mfma_f32_16x16x32_bf16 v[28:31], v[176:179], v[72:75], v[28:31]\n"
"ds_read_b64 v[68:69], v3 offset:56320\n"
"v_mul_f32 v89, 0x3acccccd, v91\n"
"v_fma_f32 v84, -v91, v89, v84\n"
"v_mfma_f32_16x16x32_bf16 v[32:35], v[180:183], v[72:75], v[32:35]\n"
"ds_read_b64 v[70:71], v3 offset:56576\n"
"v_fmamk_f32 v84, v84, 0x3acccccd, v27\n"
"v_mfma_f32_16x16x32_bf16 v[28:31], v[184:187], v[76:79], v[28:31]\n"
"ds_read_b64 v[72:73], v3 offset:57344\n"
"v_cmp_u_f32 vcc, v84, v84\n"
"v_mfma_f32_16x16x32_bf16 v[32:35], v[188:191], v[76:79], v[32:35]\n"
"ds_read_b64 v[74:75], v3 offset:57600\n"
"s_waitcnt lgkmcnt(6)\n"
"v_mfma_f32_16x16x32_bf16 v[28:31], v[192:195], v[56:59], v[28:31]\n"
"ds_read_b64 v[76:77], v4 offset:61440\n"
"v_max_f32 v84, v84, v27\n"
"v_mfma_f32_16x16x32_bf16 v[32:35], v[196:199], v[56:59], v[32:35]\n"
"ds_read_b64 v[78:79], v4 offset:61696\n"
"v_rsq_f32 v88, v84\n"
"v_mfma_f32_16x16x32_bf16 v[52:55], v[56:59], v[56:59], v[52:55]\n"
"v_mul_f32 v89, -v89, v88\n"
"v_mfma_f32_16x16x32_bf16 v[28:31], v[200:203], v[60:63], v[28:31]\n"
"ds_read_b64 v[56:57], v5 offset:61440\n"
"s_cbranch_vccnz .Lsl_140\n"
".Lbk_141:\n"
"v_mfma_f32_16x16x32_bf16 v[32:35], v[204:207], v[60:63], v[32:35]\n"
"ds_read_b64 v[58:59], v5 offset:61696\n"
"ds_write_b64 v7, v[36:37] offset:7936\n"
"s_waitcnt lgkmcnt(7)\n"
"v_mfma_f32_16x16x32_bf16 v[28:31], v[208:211], v[64:67], v[28:31]\n"
"ds_read_b64 v[60:61], v6 offset:61440\n"
"v_mul_f32 v44, v88, v44\n"
"v_mul_f32 v45, v88, v45\n"
"v_mfma_f32_16x16x32_bf16 v[32:35], v[212:215], v[64:67], v[32:35]\n"
"ds_read_b64 v[62:63], v6 offset:61696\n"
"v_mul_f32 v46, v88, v46\n"
"v_mul_f32 v47, v88, v47\n"
"v_mfma_f32_16x16x32_bf16 v[28:31], v[216:219], v[68:71], v[28:31]\n"
"v_add_u32 v3, 0xffffb000, v3\n"
"ds_read_b64 v[64:65], v3 offset:0\n"
"v_fmac_f32 v44, v89, v16\n"
"v_mfma_f32_16x16x32_bf16 v[32:35], v[220:223], v[68:71], v[32:35]\n"
"ds_read_b64 v[66:67], v3 offset:256\n"
"v_fmac_f32 v45, v89, v17\n"
"s_waitcnt lgkmcnt(7)\n"
"v_mfma_f32_16x16x32_bf16 v[28:31], v[224:227], v[72:75], v[28:31]\n"
"ds_read_b64 v[68:69], v3 offset:4096\n"
"v_fmac_f32 v46, v89, v18\n"
"v_fmac_f32 v47, v89, v19\n"
"v_mfma_f32_16x16x32_bf16 v[32:35], v[228:231], v[72:75], v[32:35]\n"
"ds_read_b64 v[70:71], v3 offset:4352\n"
"v_cvt_pk_bf16_f32 v44, v44, v45\n"
"v_cvt_pk_bf16_f32 v45, v46, v47\n"
"v_mfma_f32_16x16x32_bf16 v[52:55], v[72:75], v[72:75], v[52:55]\n"
"ds_write_b64 v8, v[40:41] offset:7936\n"
"v_mfma_f32_16x16x32_bf16 v[28:31], v[232:235], v[76:79], v[28:31]\n"
"ds_read_b64 v[72:73], v3 offset:8192\n"
"v_mul_f32 v48, v88, v48\n"
"v_mul_f32 v49, v88, v49\n"
"v_mfma_f32_16x16x32_bf16 v[32:35], v[236:239], v[76:79], v[32:35]\n"
"ds_read_b64 v[74:75], v3 offset:8448\n"
"v_mul_f32 v50, v88, v50\n"
"s_waitcnt lgkmcnt(7)\n"
"v_mfma_f32_16x16x32_bf16 v[28:31], v[240:243], v[56:59], v[28:31]\n"
"ds_read_b64 v[76:77], v3 offset:12288\n"
"v_cndmask_b32 v94, v52, v53, s[34:35]\n"
"v_mul_f32 v51, v88, v51\n"
"v_mfma_f32_16x16x32_bf16 v[32:35], v[244:247], v[56:59], v[32:35]\n"
"ds_read_b64 v[78:79], v3 offset:12544\n"
"v_cndmask_b32 v54, v54, v55, s[34:35]\n"
"v_fmac_f32 v48, v89, v20\n"
"v_mfma_f32_16x16x32_bf16 v[28:31], v[248:251], v[60:63], v[28:31]\n"
"ds_read_b64 v[56:57], v3 offset:16384\n"
"v_cndmask_b32 v94, v94, v54, s[20:21]\n"
"v_fmac_f32 v49, v89, v21\n"
"v_mfma_f32_16x16x32_bf16 v[32:35], v[252:255], v[60:63], v[32:35]\n"
"ds_read_b64 v[58:59], v3 offset:16640\n"
"v_fmac_f32 v50, v89, v22\n"
"s_waitcnt lgkmcnt(7)\n"
"s_cmp_eq_u32 s3, 3\n"
"s_cbranch_scc1 .Lfu_152\n"
"ds_read_b64 v[60:61], v3 offset:1024\n"
"v_fmac_f32 v51, v89, v23\n"
"v_mfma_f32_16x16x32_bf16 v[40:43], v[100:103], v[64:67], 0\n"
"ds_read_b64 v[62:63], v3 offset:1280\n"
"v_cndmask_b32 v94, v28, v94, s[22:23]\n"
"v_mfma_f32_16x16x32_bf16 v[52:55], v[64:67], v[64:67], 0\n"
"ds_write_b32 v9, v94 offset:2816\n"
"ds_read_b64 v[64:65], v3 offset:2048\n"
"v_cvt_pk_bf16_f32 v48, v48, v49\n"
"v_mfma_f32_16x16x32_bf16 v[40:43], v[132:135], v[68:71], v[40:43]\n"
"ds_read_b64 v[66:67], v3 offset:2304\n"
"v_cvt_pk_bf16_f32 v49, v50, v51\n"
"v_mfma_f32_16x16x32_bf16 v[52:55], v[68:71], v[68:71], v[52:55]\n"
"ds_write_b64 v7, v[44:45] offset:11904\n"
"s_waitcnt lgkmcnt(8)\n"
"ds_read_b64 v[68:69], v3 offset:3072\n"
"v_mfma_f32_16x16x32_bf16 v[40:43], v[164:167], v[72:75], v[40:43]\n"
"ds_read_b64 v[70:71], v3 offset:3328\n"
"v_mfma_f32_16x16x32_bf16 v[52:55], v[72:75], v[72:75], v[52:55]\n"
"ds_write_b64 v8, v[48:49] offset:11904\n"
"ds_read_b64 v[72:73], v3 offset:5120\n"
"v_mfma_f32_16x16x32_bf16 v[40:43], v[196:199], v[76:79], v[40:43]\n"
"ds_read_b64 v[74:75], v3 offset:5376\n"
"v_mfma_f32_16x16x32_bf16 v[52:55], v[76:79], v[76:79], v[52:55]\n"
"ds_read_b128 v[84:87], v11 offset:2816\n"
"ds_read_b32 v91, v12 offset:2816\n"
"s_waitcnt lgkmcnt(11)\n"
"ds_read_b64 v[76:77], v3 offset:6144\n"
"v_mfma_f32_16x16x32_bf16 v[40:43], v[228:231], v[56:59], v[40:43]\n"
"ds_read_b64 v[78:79], v3 offset:6400\n"
"v_mfma_f32_16x16x32_bf16 v[52:55], v[56:59], v[56:59], v[52:55]\n"
"ds_read_b64 v[56:57], v3 offset:7168\n"
"v_mfma_f32_16x16x32_bf16 v[40:43], v[108:111], v[60:63], v[40:43]\n"
"ds_read_b64 v[58:59], v3 offset:7424\n"
"s_waitcnt lgkmcnt(9)\n"
"ds_read_b64 v[60:61], v3 offset:9216\n"
"s_nop 1\n"
"ds_write_b32 v10, v52 offset:3456\n"
"v_cndmask_b32 v94, v52, v53, s[34:35]\n"
"v_cndmask_b32 v54, v54, v55, s[34:35]\n"
"v_mfma_f32_16x16x32_bf16 v[40:43], v[116:119], v[64:67], v[40:43]\n"
"ds_read_b64 v[62:63], v3 offset:9472\n"
"s_waitcnt lgkmcnt(8)\n"
"v_add_f32 v84, v84, v85\n"
"v_add_f32 v86, v86, v87\n"
"v_cndmask_b32 v94, v94, v54, s[20:21]\n"
"ds_write_b32 v9, v94 offset:3136\n"
"ds_read_b64 v[64:65], v3 offset:10240\n"
"v_add_f32 v84, v84, v86\n"
"s_waitcnt lgkmcnt(9)\n"
"v_mul_f32 v89, 0x3acccccd, v91\n"
"v_mfma_f32_16x16x32_bf16 v[40:43], v[124:127], v[68:71], v[40:43]\n"
"ds_read_b64 v[66:67], v3 offset:10496\n"
"v_fma_f32 v84, -v91, v89, v84\n"
"s_waitcnt lgkmcnt(8)\n"
"ds_read_b64 v[68:69], v3 offset:11264\n"
"v_fmamk_f32 v84, v84, 0x3acccccd, v27\n"
"v_cmp_u_f32 vcc, v84, v84\n"
"v_mfma_f32_16x16x32_bf16 v[40:43], v[140:143], v[72:75], v[40:43]\n"
"ds_read_b64 v[70:71], v3 offset:11520\n"
"v_max_f32 v84, v84, v27\n"
"v_rsq_f32 v88, v84\n"
"ds_read_b64 v[72:73], v3 offset:13312\n"
"v_mul_f32 v89, -v89, v88\n"
"v_mfma_f32_16x16x32_bf16 v[40:43], v[148:151], v[76:79], v[40:43]\n"
"ds_read_b64 v[74:75], v3 offset:13568\n"
"s_cbranch_vccnz .Lsl_144c\n"
".Lbk_145c:\n"
"s_waitcnt lgkmcnt(7)\n"
"ds_read_b64 v[76:77], v3 offset:14336\n"
"v_mul_f32 v28, v88, v28\n"
"v_mul_f32 v29, v88, v29\n"
"v_mfma_f32_16x16x32_bf16 v[40:43], v[156:159], v[56:59], v[40:43]\n"
"ds_read_b64 v[78:79], v3 offset:14592\n"
"ds_read_b128 v[44:47], v11 offset:3136\n"
"ds_read_b128 v[48:51], v11 offset:3456\n"
"ds_read_b64 v[56:57], v3 offset:15360\n"
"v_mul_f32 v30, v88, v30\n"
"v_mul_f32 v31, v88, v31\n"
"v_mfma_f32_16x16x32_bf16 v[40:43], v[172:175], v[60:63], v[40:43]\n"
"ds_read_b64 v[58:59], v3 offset:15616\n"
"v_fmac_f32 v28, v89, v16\n"
"s_waitcnt lgkmcnt(8)\n"
"ds_read_b64 v[60:61], v4 offset:0\n"
"v_fmac_f32 v29, v89, v17\n"
"v_fmac_f32 v30, v89, v18\n"
"v_mfma_f32_16x16x32_bf16 v[40:43], v[180:183], v[64:67], v[40:43]\n"
"ds_read_b64 v[62:63], v4 offset:256\n"
"v_fmac_f32 v31, v89, v19\n"
"v_cvt_pk_bf16_f32 v28, v28, v29\n"
"ds_read_b64 v[64:65], v5 offset:0\n"
"v_cvt_pk_bf16_f32 v29, v30, v31\n"
"ds_write_b64 v7, v[28:29] offset:15872\n"
"v_mfma_f32_16x16x32_bf16 v[40:43], v[188:191], v[68:71], v[40:43]\n"
"ds_read_b64 v[66:67], v5 offset:256\n"
"v_mul_f32 v32, v88, v32\n"
"s_waitcnt lgkmcnt(9)\n"
"ds_read_b64 v[68:69], v6 offset:0\n"
"s_waitcnt lgkmcnt(9)\n"
"v_add_f32 v44, v44, v45\n"
"v_mul_f32 v33, v88, v33\n"
"v_mfma_f32_16x16x32_bf16 v[40:43], v[204:207], v[72:75], v[40:43]\n"
"ds_read_b64 v[70:71], v6 offset:256\n"
"v_add_f32 v46, v46, v47\n"
"v_mul_f32 v34, v88, v34\n"
"s_waitcnt lgkmcnt(9)\n"
"v_add_f32 v48, v48, v49\n"
"v_add_f32 v50, v50, v51\n"
"v_mul_f32 v35, v88, v35\n"
"v_mfma_f32_16x16x32_bf16 v[40:43], v[212:215], v[76:79], v[40:43]\n"
"v_add_f32 v48, v48, v50\n"
"v_fmac_f32 v32, v89, v20\n"
"s_waitcnt lgkmcnt(5)\n"
"v_add_f32 v44, v44, v46\n"
"v_mul_f32 v53, 0x3acccccd, v48\n"
"v_fmac_f32 v33, v89, v21\n"
"v_mfma_f32_16x16x32_bf16 v[40:43], v[220:223], v[56:59], v[40:43]\n"
"v_fma_f32 v44, -v48, v53, v44\n"
"v_fmamk_f32 v44, v44, 0x3acccccd, v27\n"
"v_fmac_f32 v34, v89, v22\n"
"v_cmp_u_f32 vcc, v44, v44\n"
"v_max_f32 v44, v44, v27\n"
"v_fmac_f32 v35, v89, v23\n"
"v_mfma_f32_16x16x32_bf16 v[40:43], v[236:239], v[60:63], v[40:43]\n"
"v_rsq_f32 v52, v44\n"
"s_waitcnt lgkmcnt(0)\n"
"v_mul_f32 v53, -v53, v52\n"
"v_cvt_pk_bf16_f32 v32, v32, v33\n"
"v_cvt_pk_bf16_f32 v33, v34, v35\n"
"v_mfma_f32_16x16x32_bf16 v[40:43], v[244:247], v[64:67], v[40:43]\n"
"s_cbranch_vccnz .Lsl_148c\n"
".Lbk_149c:\n"
"ds_write_b64 v8, v[32:33] offset:15872\n"
"v_mfma_f32_16x16x32_bf16 v[40:43], v[252:255], v[68:71], v[40:43]\n"
"v_cmp_u_f32 vcc, v92, v92\n"
"s_nop 6\n"
"v_mul_f32 v40, v52, v40\n"
"v_mul_f32 v41, v52, v41\n"
"v_mul_f32 v42, v52, v42\n"
"v_mul_f32 v43, v52, v43\n"
"v_fmac_f32 v40, v53, v20\n"
"v_fmac_f32 v41, v53, v21\n"
"v_fmac_f32 v42, v53, v22\n"
"v_fmac_f32 v43, v53, v23\n"
"v_cvt_pk_bf16_f32 v40, v40, v41\n"
"v_cvt_pk_bf16_f32 v41, v42, v43\n"
"ds_write_b64 v8, v[40:41] offset:0\n"
"s_cbranch_vccnz .Lrd_118\n"
".Lgo_117c:\n"
"ds_write_b32 v13, v13 offset:304\n"
"s_branch .Ljn_153\n"
".Lfu_152:\n"
"s_nop 0\n"
"v_mfma_f32_16x16x32_bf16 v[36:39], v[96:99], v[64:67], 0\n"
"ds_read_b64 v[60:61], v3 offset:1024\n"
"v_fmac_f32 v51, v89, v23\n"
"v_mfma_f32_16x16x32_bf16 v[40:43], v[100:103], v[64:67], 0\n"
"ds_read_b64 v[62:63], v3 offset:1280\n"
"v_cndmask_b32 v94, v28, v94, s[22:23]\n"
"v_mfma_f32_16x16x32_bf16 v[52:55], v[64:67], v[64:67], 0\n"
"ds_write_b32 v9, v94 offset:2816\n"
"v_mfma_f32_16x16x32_bf16 v[36:39], v[128:131], v[68:71], v[36:39]\n"
"ds_read_b64 v[64:65], v3 offset:2048\n"
"v_cvt_pk_bf16_f32 v48, v48, v49\n"
"v_mfma_f32_16x16x32_bf16 v[40:43], v[132:135], v[68:71], v[40:43]\n"
"ds_read_b64 v[66:67], v3 offset:2304\n"
"v_cvt_pk_bf16_f32 v49, v50, v51\n"
"v_mfma_f32_16x16x32_bf16 v[52:55], v[68:71], v[68:71], v[52:55]\n"
"ds_write_b64 v7, v[44:45] offset:11904\n"
"s_waitcnt lgkmcnt(8)\n"
"v_mfma_f32_16x16x32_bf16 v[36:39], v[160:163], v[72:75], v[36:39]\n"
"ds_read_b64 v[68:69], v3 offset:3072\n"
"v_mfma_f32_16x16x32_bf16 v[40:43], v[164:167], v[72:75], v[40:43]\n"
"ds_read_b64 v[70:71], v3 offset:3328\n"
"v_mfma_f32_16x16x32_bf16 v[52:55], v[72:75], v[72:75], v[52:55]\n"
"ds_write_b64 v8, v[48:49] offset:11904\n"
"v_mfma_f32_16x16x32_bf16 v[36:39], v[192:195], v[76:79], v[36:39]\n"
"ds_read_b64 v[72:73], v3 offset:5120\n"
"v_mfma_f32_16x16x32_bf16 v[40:43], v[196:199], v[76:79], v[40:43]\n"
"ds_read_b64 v[74:75], v3 offset:5376\n"
"v_mfma_f32_16x16x32_bf16 v[52:55], v[76:79], v[76:79], v[52:55]\n"
"ds_read_b128 v[84:87], v11 offset:2816\n"
"ds_read_b32 v91, v12 offset:2816\n"
"s_waitcnt lgkmcnt(11)\n"
"v_mfma_f32_16x16x32_bf16 v[36:39], v[224:227], v[56:59], v[36:39]\n"
"ds_read_b64 v[76:77], v3 offset:6144\n"
"v_mfma_f32_16x16x32_bf16 v[40:43], v[228:231], v[56:59], v[40:43]\n"
"ds_read_b64 v[78:79], v3 offset:6400\n"
"v_mfma_f32_16x16x32_bf16 v[52:55], v[56:59], v[56:59], v[52:55]\n"
"v_mfma_f32_16x16x32_bf16 v[36:39], v[104:107], v[60:63], v[36:39]\n"
"ds_read_b64 v[56:57], v3 offset:7168\n"
"v_mfma_f32_16x16x32_bf16 v[40:43], v[108:111], v[60:63], v[40:43]\n"
"ds_read_b64 v[58:59], v3 offset:7424\n"
"s_waitcnt lgkmcnt(9)\n"
"v_mfma_f32_16x16x32_bf16 v[36:39], v[112:115], v[64:67], v[36:39]\n"
"ds_read_b64 v[60:61], v3 offset:9216\n"
"ds_write_b32 v10, v52 offset:3456\n"
"v_cndmask_b32 v94, v52, v53, s[34:35]\n"
"v_cndmask_b32 v54, v54, v55, s[34:35]\n"
"v_mfma_f32_16x16x32_bf16 v[40:43], v[116:119], v[64:67], v[40:43]\n"
"ds_read_b64 v[62:63], v3 offset:9472\n"
"s_waitcnt lgkmcnt(8)\n"
"v_add_f32 v84, v84, v85\n"
"v_add_f32 v86, v86, v87\n"
"v_cndmask_b32 v94, v94, v54, s[20:21]\n"
"ds_write_b32 v9, v94 offset:3136\n"
"v_mfma_f32_16x16x32_bf16 v[36:39], v[120:123], v[68:71], v[36:39]\n"
"ds_read_b64 v[64:65], v3 offset:10240\n"
"v_add_f32 v84, v84, v86\n"
"s_waitcnt lgkmcnt(9)\n"
"v_mul_f32 v89, 0x3acccccd, v91\n"
"v_mfma_f32_16x16x32_bf16 v[40:43], v[124:127], v[68:71], v[40:43]\n"
"ds_read_b64 v[66:67], v3 offset:10496\n"
"v_fma_f32 v84, -v91, v89, v84\n"
"s_waitcnt lgkmcnt(8)\n"
"v_mfma_f32_16x16x32_bf16 v[36:39], v[136:139], v[72:75], v[36:39]\n"
"ds_read_b64 v[68:69], v3 offset:11264\n"
"v_fmamk_f32 v84, v84, 0x3acccccd, v27\n"
"v_cmp_u_f32 vcc, v84, v84\n"
"v_mfma_f32_16x16x32_bf16 v[40:43], v[140:143], v[72:75], v[40:43]\n"
"ds_read_b64 v[70:71], v3 offset:11520\n"
"v_max_f32 v84, v84, v27\n"
"v_rsq_f32 v88, v84\n"
"v_mfma_f32_16x16x32_bf16 v[36:39], v[144:147], v[76:79], v[36:39]\n"
"ds_read_b64 v[72:73], v3 offset:13312\n"
"v_mul_f32 v89, -v89, v88\n"
"v_mfma_f32_16x16x32_bf16 v[40:43], v[148:151], v[76:79], v[40:43]\n"
"ds_read_b64 v[74:75], v3 offset:13568\n"
"s_cbranch_vccnz .Lsl_144\n"
".Lbk_145:\n"
"s_waitcnt lgkmcnt(7)\n"
"v_mfma_f32_16x16x32_bf16 v[36:39], v[152:155], v[56:59], v[36:39]\n"
"ds_read_b64 v[76:77], v3 offset:14336\n"
"v_mul_f32 v28, v88, v28\n"
"v_mul_f32 v29, v88, v29\n"
"v_mfma_f32_16x16x32_bf16 v[40:43], v[156:159], v[56:59], v[40:43]\n"
"ds_read_b64 v[78:79], v3 offset:14592\n"
"ds_read_b128 v[44:47], v11 offset:3136\n"
"ds_read_b128 v[48:51], v11 offset:3456\n"
"v_mfma_f32_16x16x32_bf16 v[36:39], v[168:171], v[60:63], v[36:39]\n"
"ds_read_b64 v[56:57], v3 offset:15360\n"
"v_mul_f32 v30, v88, v30\n"
"v_mul_f32 v31, v88, v31\n"
"v_mfma_f32_16x16x32_bf16 v[40:43], v[172:175], v[60:63], v[40:43]\n"
"ds_read_b64 v[58:59], v3 offset:15616\n"
"v_fmac_f32 v28, v89, v16\n"
"s_waitcnt lgkmcnt(8)\n"
"v_mfma_f32_16x16x32_bf16 v[36:39], v[176:179], v[64:67], v[36:39]\n"
"ds_read_b64 v[60:61], v4 offset:0\n"
"v_fmac_f32 v29, v89, v17\n"
"v_fmac_f32 v30, v89, v18\n"
"v_mfma_f32_16x16x32_bf16 v[40:43], v[180:183], v[64:67], v[40:43]\n"
"ds_read_b64 v[62:63], v4 offset:256\n"
"v_fmac_f32 v31, v89, v19\n"
"v_cvt_pk_bf16_f32 v28, v28, v29\n"
"v_mfma_f32_16x16x32_bf16 v[36:39], v[184:187], v[68:71], v[36:39]\n"
"ds_read_b64 v[64:65], v5 offset:0\n"
"v_cvt_pk_bf16_f32 v29, v30, v31\n"
"ds_write_b64 v7, v[28:29] offset:15872\n"
"v_mfma_f32_16x16x32_bf16 v[40:43], v[188:191], v[68:71], v[40:43]\n"
"ds_read_b64 v[66:67], v5 offset:256\n"
"v_mul_f32 v32, v88, v32\n"
"s_waitcnt lgkmcnt(9)\n"
"v_mfma_f32_16x16x32_bf16 v[36:39], v[200:203], v[72:75], v[36:39]\n"
"ds_read_b64 v[68:69], v6 offset:0\n"
"s_waitcnt lgkmcnt(9)\n"
"v_add_f32 v44, v44, v45\n"
"v_mul_f32 v33, v88, v33\n"
"v_mfma_f32_16x16x32_bf16 v[40:43], v[204:207], v[72:75], v[40:43]\n"
"ds_read_b64 v[70:71], v6 offset:256\n"
"v_add_f32 v46, v46, v47\n"
"v_mul_f32 v34, v88, v34\n"
"v_mfma_f32_16x16x32_bf16 v[36:39], v[208:211], v[76:79], v[36:39]\n"
"s_waitcnt lgkmcnt(9)\n"
"v_add_f32 v48, v48, v49\n"
"v_add_f32 v50, v50, v51\n"
"v_mul_f32 v35, v88, v35\n"
"v_mfma_f32_16x16x32_bf16 v[40:43], v[212:215], v[76:79], v[40:43]\n"
"v_add_f32 v48, v48, v50\n"
"v_fmac_f32 v32, v89, v20\n"
"s_waitcnt lgkmcnt(5)\n"
"v_mfma_f32_16x16x32_bf16 v[36:39], v[216:219], v[56:59], v[36:39]\n"
"v_add_f32 v44, v44, v46\n"
"v_mul_f32 v53, 0x3acccccd, v48\n"
"v_fmac_f32 v33, v89, v21\n"
"v_mfma_f32_16x16x32_bf16 v[40:43], v[220:223], v[56:59], v[40:43]\n"
"v_fma_f32 v44, -v48, v53, v44\n"
"v_fmamk_f32 v44, v44, 0x3acccccd, v27\n"
"v_fmac_f32 v34, v89, v22\n"
"v_mfma_f32_16x16x32_bf16 v[36:39], v[232:235], v[60:63], v[36:39]\n"
"v_cmp_u_f32 vcc, v44, v44\n"
"v_max_f32 v44, v44, v27\n"
"v_fmac_f32 v35, v89, v23\n"
"v_mfma_f32_16x16x32_bf16 v[40:43], v[236:239], v[60:63], v[40:43]\n"
"v_rsq_f32 v52, v44\n"
"s_waitcnt lgkmcnt(0)\n"
"v_mfma_f32_16x16x32_bf16 v[36:39], v[240:243], v[64:67], v[36:39]\n"
"v_mul_f32 v53, -v53, v52\n"
"v_cvt_pk_bf16_f32 v32, v32, v33\n"
"v_cvt_pk_bf16_f32 v33, v34, v35\n"
"v_mfma_f32_16x16x32_bf16 v[40:43], v[244:247], v[64:67], v[40:43]\n"
"s_cbranch_vccnz .Lsl_148\n"
".Lbk_149:\n"
"ds_write_b64 v8, v[32:33] offset:15872\n"
"v_mfma_f32_16x16x32_bf16 v[36:39], v[248:251], v[68:71], v[36:39]\n"
"v_mfma_f32_16x16x32_bf16 v[40:43], v[252:255], v[68:71], v[40:43]\n"
"v_cmp_u_f32 vcc, v92, v92\n"
"s_nop 3\n"
"v_mul_f32 v36, v52, v36\n"
"v_mul_f32 v37, v52, v37\n"
"s_cbranch_vccnz .Lrd_118\n"
".Lgo_117:\n"
"ds_write_b32 v13, v13 offset:304\n"
"v_mul_f32 v38, v52, v38\n"
"v_mul_f32 v39, v52, v39\n"
"v_fmac_f32 v36, v53, v16\n"
"v_fmac_f32 v37, v53, v17\n"
"v_fmac_f32 v38, v53, v18\n"
"v_fmac_f32 v39, v53, v19\n"
"v_cvt_pk_bf16_f32 v36, v36, v37\n"
"v_cvt_pk_bf16_f32 v37, v38, v39\n"
"ds_write_b64 v7, v[36:37] offset:0\n"
"v_mul_f32 v40, v52, v40\n"
"v_mul_f32 v41, v52, v41\n"
"v_mul_f32 v42, v52, v42\n"
"v_mul_f32 v43, v52, v43\n"
"v_fmac_f32 v40, v53, v20\n"
"v_fmac_f32 v41, v53, v21\n"
"v_fmac_f32 v42, v53, v22\n"
"v_fmac_f32 v43, v53, v23\n"
"v_cvt_pk_bf16_f32 v40, v40, v41\n"
"v_cvt_pk_bf16_f32 v41, v42, v43\n"
"ds_write_b64 v8, v[40:41] offset:0\n"
".Ljn_153:\n"
"v_mov_b32 v46, 0\n"
"v_mov_b32 v47, 0\n"
"v_mov_b32 v50, 0\n"
"v_mov_b32 v51, 0\n"
"v_mov_b32 v72, s42\n"
"v_mov_b32 v73, s42\n"
"v_mov_b32 v74, s42\n"
"v_mov_b32 v75, s42\n"
"s_cmp_lg_u32 s3, 2\n"
"s_cbranch_scc1 .Lnw_196\n"
"s_mov_b32 s17, 0x100000\n"
".Ltp_197:\n"
"ds_read_b32 v90, v14 offset:304\n"
"s_sub_u32 s17, s17, 1\n"
"s_cbranch_scc1 .Lou_198\n"
"s_waitcnt lgkmcnt(0)\n"
"v_cmp_eq_u32 vcc, 0, v90\n"
"s_cbranch_vccnz .Ltp_197\n"
".Lou_198:\n"
"s_waitcnt lgkmcnt(0)\n"
".Lnw_196:\n"
"ds_read_b32 v90, v14 offset:304\n"
"s_mov_b64 exec, 0xffff\n"
"ds_read_b64 v[46:47], v24 offset:4000\n"
"ds_read_b64 v[50:51], v24 offset:4040\n"
"s_mov_b64 exec, -1\n"
"ds_read_b64 v[44:45], v25 offset:3968\n"
"ds_read_b64 v[56:57], v25 offset:80\n"
"ds_read_b64 v[58:59], v24 offset:112\n"
"ds_read_b64 v[64:65], v25 offset:4048\n"
"ds_read_b64 v[66:67], v24 offset:4080\n"
"ds_read_b64 v[48:49], v25 offset:4008\n"
"ds_read_b64 v[60:61], v25 offset:120\n"
"ds_read_b64 v[62:63], v24 offset:152\n"
"ds_read_b64 v[68:69], v25 offset:4088\n"
"ds_read_b64 v[70:71], v24 offset:4120\n"
"s_waitcnt lgkmcnt(12)\n"
"v_or_b32 v90, s33, v90\n"
"s_mov_b32 s17, 0x100000\n"
"v_cmp_eq_u32 vcc, 0, v90\n"
"s_cbranch_vccz .Lok_200\n"
".Ltp_199:\n"
"ds_read_b32 v90, v14 offset:304\n"
"s_mov_b64 exec, 0xffff\n"
"ds_read_b64 v[46:47], v24 offset:4000\n"
"ds_read_b64 v[50:51], v24 offset:4040\n"
"s_mov_b64 exec, -1\n"
"ds_read_b64 v[44:45], v25 offset:3968\n"
"ds_read_b64 v[56:57], v25 offset:80\n"
"ds_read_b64 v[58:59], v24 offset:112\n"
"ds_read_b64 v[64:65], v25 offset:4048\n"
"ds_read_b64 v[66:67], v24 offset:4080\n"
"ds_read_b64 v[48:49], v25 offset:4008\n"
"ds_read_b64 v[60:61], v25 offset:120\n"
"ds_read_b64 v[62:63], v24 offset:152\n"
"ds_read_b64 v[68:69], v25 offset:4088\n"
"ds_read_b64 v[70:71], v24 offset:4120\n"
"s_sub_u32 s17, s17, 1\n"
"s_cbranch_scc1 .Lok_200\n"
"s_waitcnt lgkmcnt(12)\n"
"v_cmp_eq_u32 vcc, 0, v90\n"
"s_cbranch_vccnz .Ltp_199\n"
"s_waitcnt lgkmcnt(0)\n"
".Lok_200:\n"
"ds_read_b64_tr_b16 v[28:29], v26 offset:0\n"
"ds_read_b64_tr_b16 v[30:31], v26 offset:3968\n"
"ds_read_b64_tr_b16 v[32:33], v26 offset:40\n"
"s_waitcnt lgkmcnt(10)\n"
"v_mfma_f32_16x16x32_bf16 v[56:59], v[56:59], v[44:47], v[72:75]\n"
"s_waitcnt lgkmcnt(8)\n"
"v_mfma_f32_16x16x32_bf16 v[64:67], v[64:67], v[44:47], 0\n"
"ds_read_b64_tr_b16 v[34:35], v26 offset:4008\n"
"ds_read_b64_tr_b16 v[36:37], v26 offset:32\n"
"ds_read_b64_tr_b16 v[38:39], v26 offset:4000\n"
"s_waitcnt lgkmcnt(8)\n"
"v_mfma_f32_16x16x32_bf16 v[60:63], v[60:63], v[48:51], v[72:75]\n"
"s_waitcnt lgkmcnt(6)\n"
"v_mfma_f32_16x16x32_bf16 v[68:71], v[68:71], v[48:51], 0\n"
"ds_read_b64_tr_b16 v[40:41], v26 offset:72\n"
"ds_read_b64_tr_b16 v[42:43], v26 offset:4040\n"
"v_mov_b32 v84, 0x3f803f80\n"
"v_mov_b32 v85, 0x3f803f80\n"
"v_mov_b32 v86, 0x3f803f80\n"
"v_mov_b32 v87, 0x3f803f80\n"
"v_cndmask_b32 v76, v56, v57, s[34:35]\n"
"v_cndmask_b32 v72, v64, v65, s[34:35]\n"
"v_cndmask_b32 v76, v76, v72, s[36:37]\n"
"v_cndmask_b32 v77, v58, v59, s[34:35]\n"
"v_cndmask_b32 v72, v66, v67, s[34:35]\n"
"v_cndmask_b32 v77, v77, v72, s[38:39]\n"
"v_max_f32 v52, v76, v77\n"
"v_max_f32 v54, v76, v77\n"
"v_cndmask_b32 v78, v60, v61, s[34:35]\n"
"v_cndmask_b32 v72, v68, v69, s[34:35]\n"
"v_cndmask_b32 v78, v78, v72, s[36:37]\n"
"v_cndmask_b32 v79, v62, v63, s[34:35]\n"
"v_cndmask_b32 v72, v70, v71, s[34:35]\n"
"v_cndmask_b32 v79, v79, v72, s[38:39]\n"
"v_max_f32 v53, v78, v79\n"
"v_max_f32 v55, v78, v79\n"
"v_permlane16_swap_b32 v52, v54\n"
"s_nop 0\n"
"v_permlane16_swap_b32 v53, v55\n"
"v_max_f32 v72, v52, v54\n"
"v_max_f32 v52, v52, v54\n"
"v_max_f32 v73, v53, v55\n"
"v_max_f32 v53, v53, v55\n"
"v_permlane32_swap_b32 v52, v72\n"
"s_nop 0\n"
"v_permlane32_swap_b32 v53, v73\n"
"v_max_f32 v52, v52, v72\n"
"v_max_f32 v53, v53, v73\n"
"v_mul_f32 v52, 0xbea52b5b, v52\n"
"v_mul_f32 v53, 0xbea52b5b, v53\n"
"v_fmamk_f32 v76, v76, 0x3ea52b5b, v52\n"
"v_fmamk_f32 v77, v77, 0x3ea52b5b, v52\n"
"v_fmamk_f32 v78, v78, 0x3ea52b5b, v53\n"
"v_fmamk_f32 v79, v79, 0x3ea52b5b, v53\n"
"v_exp_f32 v76, v76\n"
"v_exp_f32 v77, v77\n"
"v_exp_f32 v78, v78\n"
"v_exp_f32 v79, v79\n"
"v_cvt_pk_bf16_f32 v52, v76, v77\n"
"v_cvt_pk_bf16_f32 v53, v78, v79\n"
"v_perm_b32 v44, v52, v52, v80\n"
"v_perm_b32 v45, v52, v52, v81\n"
"v_perm_b32 v46, v52, v52, v82\n"
"v_perm_b32 v47, v52, v52, v83\n"
"v_perm_b32 v48, v53, v53, v80\n"
"v_perm_b32 v49, v53, v53, v81\n"
"v_perm_b32 v50, v53, v53, v82\n"
"v_perm_b32 v51, v53, v53, v83\n"
"v_mfma_f32_16x16x32_bf16 v[56:59], v[84:87], v[44:47], 0\n"
"s_nop 0\n"
"v_mfma_f32_16x16x32_bf16 v[60:63], v[84:87], v[48:51], 0\n"
"s_waitcnt lgkmcnt(6)\n"
"v_mfma_f32_16x16x32_bf16 v[28:31], v[28:31], v[44:47], 0\n"
"s_waitcnt lgkmcnt(4)\n"
"v_mfma_f32_16x16x32_bf16 v[32:35], v[32:35], v[48:51], 0\n"
"s_waitcnt lgkmcnt(2)\n"
"v_mfma_f32_16x16x32_bf16 v[36:39], v[36:39], v[44:47], 0\n"
"s_waitcnt lgkmcnt(0)\n"
"v_mfma_f32_16x16x32_bf16 v[40:43], v[40:43], v[48:51], 0\n"
"v_cndmask_b32 v88, 2.0, v1, s[40:41]\n"
"v_rcp_f32 v52, v56\n"
"v_rcp_f32 v53, v60\n"
"ds_read_b32 v90, v14 offset:320\n"
"ds_read_b64 v[56:57], v3 offset:40960\n"
"ds_read_b64 v[58:59], v3 offset:41216\n"
"s_add_u32 s31, s30, 0xa60000\n"
"v_mul_f32 v28, v52, v28\n"
"v_mul_f32 v29, v52, v29\n"
"v_mul_f32 v30, v52, v30\n"
"v_mul_f32 v31, v52, v31\n"
"v_mul_f32 v32, v53, v32\n"
"ds_read_b64 v[60:61], v3 offset:41984\n"
"ds_read_b64 v[62:63], v3 offset:42240\n"
"v_mul_f32 v33, v53, v33\n"
"v_mul_f32 v34, v53, v34\n"
"v_mul_f32 v35, v53, v35\n"
"v_cvt_pk_bf16_f32 v28, v28, v29\n"
"v_cvt_pk_bf16_f32 v29, v30, v31\n"
"v_cvt_pk_bf16_f32 v30, v32, v33\n"
"ds_read_b64 v[64:65], v3 offset:43008\n"
"ds_read_b64 v[66:67], v3 offset:43264\n"
"v_cvt_pk_bf16_f32 v31, v34, v35\n"
"v_min_u32 v28, -2, v28\n"
"v_min_u32 v29, -2, v29\n"
"v_min_u32 v30, -2, v30\n"
"v_min_u32 v31, -2, v31\n"
"buffer_store_dwordx4 v[28:31], v1, s[4:7], s31 offen offset:0 sc1\n"
"ds_read_b64 v[68:69], v3 offset:44032\n"
"ds_read_b64 v[70:71], v3 offset:44288\n"
"v_mul_f32 v36, v52, v36\n"
"v_mul_f32 v37, v52, v37\n"
"v_mul_f32 v38, v52, v38\n"
"v_mul_f32 v39, v52, v39\n"
"v_mul_f32 v40, v53, v40\n"
"v_mul_f32 v41, v53, v41\n"
"ds_read_b64 v[72:73], v3 offset:45056\n"
"ds_read_b64 v[74:75], v3 offset:45312\n"
"v_mul_f32 v42, v53, v42\n"
"v_mul_f32 v43, v53, v43\n"
"v_cvt_pk_bf16_f32 v36, v36, v37\n"
"v_cvt_pk_bf16_f32 v37, v38, v39\n"
"v_cvt_pk_bf16_f32 v38, v40, v41\n"
"v_cvt_pk_bf16_f32 v39, v42, v43\n"
"v_min_u32 v36, -2, v36\n"
"v_min_u32 v37, -2, v37\n"
"v_min_u32 v38, -2, v38\n"
"v_min_u32 v39, -2, v39\n"
"buffer_store_dwordx4 v[36:39], v88, s[4:7], s31 offen offset:1024 sc1\n"
"ds_write_b32 v13, v13 offset:368\n"
"s_cmp_lg_u32 s54, 0\n"
"s_cbranch_scc1 .Lfx_204\n"
".Lbk_205:\n"
"s_mov_b32 s54, 0\n"
".Lag_201:\n"
"v_mov_b32 v92, 0\n"
"s_waitcnt lgkmcnt(11)\n"
"v_cmp_eq_u32 vcc, 0, v90\n"
"s_cbranch_vccnz .Lsl_238\n"
".Lbk_239:\n"
"s_waitcnt lgkmcnt(7)\n"
"v_mfma_f32_16x16x32_bf16 v[28:31], v[96:99], v[56:59], 0\n"
"ds_read_b64 v[76:77], v3 offset:46080\n"
"v_mfma_f32_16x16x32_bf16 v[32:35], v[100:103], v[56:59], 0\n"
"ds_read_b64 v[78:79], v3 offset:46336\n"
"v_mfma_f32_16x16x32_bf16 v[52:55], v[56:59], v[56:59], 0\n"
"v_mfma_f32_16x16x32_bf16 v[28:31], v[104:107], v[60:63], v[28:31]\n"
"ds_read_b64 v[56:57], v3 offset:47104\n"
"v_mfma_f32_16x16x32_bf16 v[32:35], v[108:111], v[60:63], v[32:35]\n"
"ds_read_b64 v[58:59], v3 offset:47360\n"
"s_waitcnt lgkmcnt(7)\n"
"v_mfma_f32_16x16x32_bf16 v[28:31], v[112:115], v[64:67], v[28:31]\n"
"ds_read_b64 v[60:61], v3 offset:48128\n"
"v_mfma_f32_16x16x32_bf16 v[32:35], v[116:119], v[64:67], v[32:35]\n"
"ds_read_b64 v[62:63], v3 offset:48384\n"
"v_mfma_f32_16x16x32_bf16 v[28:31], v[120:123], v[68:71], v[28:31]\n"
"ds_read_b64 v[64:65], v3 offset:49152\n"
"v_mfma_f32_16x16x32_bf16 v[32:35], v[124:127], v[68:71], v[32:35]\n"
"ds_read_b64 v[66:67], v3 offset:49408\n"
"s_waitcnt lgkmcnt(6)\n"
"v_mfma_f32_16x16x32_bf16 v[28:31], v[128:131], v[72:75], v[28:31]\n"
"ds_read_b64 v[68:69], v3 offset:50176\n"
"v_mfma_f32_16x16x32_bf16 v[32:35], v[132:135], v[72:75], v[32:35]\n"
"ds_read_b64 v[70:71], v3 offset:50432\n"
"v_mfma_f32_16x16x32_bf16 v[52:55], v[72:75], v[72:75], v[52:55]\n"
"v_mfma_f32_16x16x32_bf16 v[28:31], v[136:139], v[76:79], v[28:31]\n"
"ds_read_b64 v[72:73], v3 offset:51200\n"
"v_mfma_f32_16x16x32_bf16 v[32:35], v[140:143], v[76:79], v[32:35]\n"
"ds_read_b64 v[74:75], v3 offset:51456\n"
"s_waitcnt lgkmcnt(6)\n"
"v_mfma_f32_16x16x32_bf16 v[28:31], v[144:147], v[56:59], v[28:31]\n"
"ds_read_b64 v[76:77], v3 offset:52224\n"
"v_mfma_f32_16x16x32_bf16 v[32:35], v[148:151], v[56:59], v[32:35]\n"
"ds_read_b64 v[78:79], v3 offset:52480\n"
"v_mfma_f32_16x16x32_bf16 v[28:31], v[152:155], v[60:63], v[28:31]\n"
"ds_read_b64 v[56:57], v3 offset:53248\n"
"v_mfma_f32_16x16x32_bf16 v[32:35], v[156:159], v[60:63], v[32:35]\n"
"ds_read_b64 v[58:59], v3 offset:53504\n"
"s_waitcnt lgkmcnt(6)\n"
"v_mfma_f32_16x16x32_bf16 v[28:31], v[160:163], v[64:67], v[28:31]\n"
"ds_read_b64 v[60:61], v3 offset:54272\n"
"v_mfma_f32_16x16x32_bf16 v[32:35], v[164:167], v[64:67], v[32:35]\n"
"ds_read_b64 v[62:63], v3 offset:54528\n"
"v_mfma_f32_16x16x32_bf16 v[52:55], v[64:67], v[64:67], v[52:55]\n"
"v_mfma_f32_16x16x32_bf16 v[28:31], v[168:171], v[68:71], v[28:31]\n"
"ds_read_b64 v[64:65], v3 offset:55296\n"
"v_mfma_f32_16x16x32_bf16 v[32:35], v[172:175], v[68:71], v[32:35]\n"
"ds_read_b64 v[66:67], v3 offset:55552\n"
"s_waitcnt lgkmcnt(6)\n"
"v_mfma_f32_16x16x32_bf16 v[28:31], v[176:179], v[72:75], v[28:31]\n"
"ds_read_b64 v[68:69], v3 offset:56320\n"
"v_mfma_f32_16x16x32_bf16 v[32:35], v[180:183], v[72:75], v[32:35]\n"
"ds_read_b64 v[70:71], v3 offset:56576\n"
"v_mfma_f32_16x16x32_bf16 v[28:31], v[184:187], v[76:79], v[28:31]\n"
"ds_read_b64 v[72:73], v3 offset:57344\n"
"v_mfma_f32_16x16x32_bf16 v[32:35], v[188:191], v[76:79], v[32:35]\n"
"ds_read_b64 v[74:75], v3 offset:57600\n"
"s_waitcnt lgkmcnt(6)\n"
"v_mfma_f32_16x16x32_bf16 v[28:31], v[192:195], v[56:59], v[28:31]\n"
"ds_read_b64 v[76:77], v4 offset:40960\n"
"v_mfma_f32_16x16x32_bf16 v[32:35], v[196:199], v[56:59], v[32:35]\n"
"ds_read_b64 v[78:79], v4 offset:41216\n"
"v_mfma_f32_16x16x32_bf16 v[52:55], v[56:59], v[56:59], v[52:55]\n"
"v_mfma_f32_16x16x32_bf16 v[28:31], v[200:203], v[60:63], v[28:31]\n"
"ds_read_b64 v[56:57], v5 offset:40960\n"
"v_mfma_f32_16x16x32_bf16 v[32:35], v[204:207], v[60:63], v[32:35]\n"
"ds_read_b64 v[58:59], v5 offset:41216\n"
"s_waitcnt lgkmcnt(6)\n"
"v_mfma_f32_16x16x32_bf16 v[28:31], v[208:211], v[64:67], v[28:31]\n"
"ds_read_b64 v[60:61], v6 offset:40960\n"
"v_mfma_f32_16x16x32_bf16 v[32:35], v[212:215], v[64:67], v[32:35]\n"
"ds_read_b64 v[62:63], v6 offset:41216\n"
"v_mfma_f32_16x16x32_bf16 v[28:31], v[216:219], v[68:71], v[28:31]\n"
"v_add_u32 v3, 0x5000, v3\n"
"ds_read_b64 v[64:65], v3 offset:40960\n"
"v_mfma_f32_16x16x32_bf16 v[32:35], v[220:223], v[68:71], v[32:35]\n"
"ds_read_b64 v[66:67], v3 offset:41216\n"
"s_waitcnt lgkmcnt(6)\n"
"v_mfma_f32_16x16x32_bf16 v[28:31], v[224:227], v[72:75], v[28:31]\n"
"ds_read_b64 v[68:69], v3 offset:41984\n"
"v_mfma_f32_16x16x32_bf16 v[32:35], v[228:231], v[72:75], v[32:35]\n"
"ds_read_b64 v[70:71], v3 offset:42240\n"
"v_mfma_f32_16x16x32_bf16 v[52:55], v[72:75], v[72:75], v[52:55]\n"
"v_mfma_f32_16x16x32_bf16 v[28:31], v[232:235], v[76:79], v[28:31]\n"
"ds_read_b64 v[72:73], v3 offset:43008\n"
"v_mfma_f32_16x16x32_bf16 v[32:35], v[236:239], v[76:79], v[32:35]\n"
"ds_read_b64 v[74:75], v3 offset:43264\n"
"s_waitcnt lgkmcnt(6)\n"
"v_mfma_f32_16x16x32_bf16 v[28:31], v[240:243], v[56:59], v[28:31]\n"
"ds_read_b64 v[76:77], v3 offset:44032\n"
"v_cndmask_b32 v94, v52, v53, s[34:35]\n"
"v_mfma_f32_16x16x32_bf16 v[32:35], v[244:247], v[56:59], v[32:35]\n"
"ds_read_b64 v[78:79], v3 offset:44288\n"
"v_cndmask_b32 v54, v54, v55, s[34:35]\n"
"v_mfma_f32_16x16x32_bf16 v[28:31], v[248:251], v[60:63], v[28:31]\n"
"ds_read_b64 v[56:57], v3 offset:45056\n"
"v_cndmask_b32 v94, v94, v54, s[20:21]\n"
"v_mfma_f32_16x16x32_bf16 v[32:35], v[252:255], v[60:63], v[32:35]\n"
"ds_read_b64 v[58:59], v3 offset:45312\n"
"s_waitcnt lgkmcnt(6)\n"
"v_mfma_f32_16x16x32_bf16 v[36:39], v[96:99], v[64:67], 0\n"
"ds_read_b64 v[60:61], v3 offset:46080\n"
"v_mfma_f32_16x16x32_bf16 v[40:43], v[100:103], v[64:67], 0\n"
"ds_read_b64 v[62:63], v3 offset:46336\n"
"v_cndmask_b32 v94, v28, v94, s[22:23]\n"
"v_mfma_f32_16x16x32_bf16 v[52:55], v[64:67], v[64:67], 0\n"
"ds_write_b32 v9, v94 offset:3712\n"
"v_mfma_f32_16x16x32_bf16 v[36:39], v[104:107], v[68:71], v[36:39]\n"
"ds_read_b64 v[64:65], v3 offset:47104\n"
"v_mfma_f32_16x16x32_bf16 v[40:43], v[108:111], v[68:71], v[40:43]\n"
"ds_read_b64 v[66:67], v3 offset:47360\n"
"s_waitcnt lgkmcnt(7)\n"
"v_mfma_f32_16x16x32_bf16 v[36:39], v[112:115], v[72:75], v[36:39]\n"
"ds_read_b64 v[68:69], v3 offset:48128\n"
"v_mfma_f32_16x16x32_bf16 v[40:43], v[116:119], v[72:75], v[40:43]\n"
"ds_read_b64 v[70:71], v3 offset:48384\n"
"v_mfma_f32_16x16x32_bf16 v[36:39], v[120:123], v[76:79], v[36:39]\n"
"ds_read_b64 v[72:73], v3 offset:49152\n"
"v_mfma_f32_16x16x32_bf16 v[40:43], v[124:127], v[76:79], v[40:43]\n"
"ds_read_b64 v[74:75], v3 offset:49408\n"
"s_waitcnt lgkmcnt(7)\n"
"v_mfma_f32_16x16x32_bf16 v[36:39], v[128:131], v[56:59], v[36:39]\n"
"ds_read_b64 v[76:77], v3 offset:50176\n"
"v_mfma_f32_16x16x32_bf16 v[40:43], v[132:135], v[56:59], v[40:43]\n"
"ds_read_b64 v[78:79], v3 offset:50432\n"
"v_mfma_f32_16x16x32_bf16 v[52:55], v[56:59], v[56:59], v[52:55]\n"
"v_mfma_f32_16x16x32_bf16 v[36:39], v[136:139], v[60:63], v[36:39]\n"
"ds_read_b64 v[56:57], v3 offset:51200\n"
"v_mfma_f32_16x16x32_bf16 v[40:43], v[140:143], v[60:63], v[40:43]\n"
"ds_read_b64 v[58:59], v3 offset:51456\n"
"s_waitcnt lgkmcnt(6)\n"
"v_mfma_f32_16x16x32_bf16 v[36:39], v[144:147], v[64:67], v[36:39]\n"
"ds_read_b64 v[60:61], v3 offset:52224\n"
"ds_read_b128 v[84:87], v11 offset:3712\n"
"ds_read_b32 v91, v12 offset:3712\n"
"v_mfma_f32_16x16x32_bf16 v[40:43], v[148:151], v[64:67], v[40:43]\n"
"ds_read_b64 v[62:63], v3 offset:52480\n"
"v_mfma_f32_16x16x32_bf16 v[36:39], v[152:155], v[68:71], v[36:39]\n"
"ds_read_b64 v[64:65], v3 offset:53248\n"
"v_mfma_f32_16x16x32_bf16 v[40:43], v[156:159], v[68:71], v[40:43]\n"
"ds_read_b64 v[66:67], v3 offset:53504\n"
"s_waitcnt lgkmcnt(8)\n"
"v_mfma_f32_16x16x32_bf16 v[36:39], v[160:163], v[72:75], v[36:39]\n"
"ds_read_b64 v[68:69], v3 offset:54272\n"
"v_mfma_f32_16x16x32_bf16 v[40:43], v[164:167], v[72:75], v[40:43]\n"
"ds_read_b64 v[70:71], v3 offset:54528\n"
"v_mfma_f32_16x16x32_bf16 v[52:55], v[72:75], v[72:75], v[52:55]\n"
"v_mfma_f32_16x16x32_bf16 v[36:39], v[168:171], v[76:79], v[36:39]\n"
"ds_read_b64 v[72:73], v3 offset:55296\n"
"s_waitcnt lgkmcnt(7)\n"
"v_add_f32 v84, v84, v85\n"
"v_add_f32 v86, v86, v87\n"
"v_mfma_f32_16x16x32_bf16 v[40:43], v[172:175], v[76:79], v[40:43]\n"
"ds_read_b64 v[74:75], v3 offset:55552\n"
"v_add_f32 v84, v84, v86\n"
"s_waitcnt lgkmcnt(6)\n"
"v_mfma_f32_16x16x32_bf16 v[36:39], v[176:179], v[56:59], v[36:39]\n"
"ds_read_b64 v[76:77], v3 offset:56320\n"
"v_mul_f32 v89, 0x3acccccd, v91\n"
"v_fma_f32 v84, -v91, v89, v84\n"
"v_mfma_f32_16x16x32_bf16 v[40:43], v[180:183], v[56:59], v[40:43]\n"
"ds_read_b64 v[78:79], v3 offset:56576\n"
"v_fmamk_f32 v84, v84, 0x3acccccd, v27\n"
"v_cmp_u_f32 vcc, v84, v84\n"
"v_mfma_f32_16x16x32_bf16 v[36:39], v[184:187], v[60:63], v[36:39]\n"
"ds_read_b64 v[56:57], v3 offset:57344\n"
"v_max_f32 v84, v84, v27\n"
"v_mfma_f32_16x16x32_bf16 v[40:43], v[188:191], v[60:63], v[40:43]\n"
"ds_read_b64 v[58:59], v3 offset:57600\n"
"s_waitcnt lgkmcnt(6)\n"
"v_mfma_f32_16x16x32_bf16 v[36:39], v[192:195], v[64:67], v[36:39]\n"
"ds_read_b64 v[60:61], v4 offset:61440\n"
"v_rsq_f32 v88, v84\n"
"v_mfma_f32_16x16x32_bf16 v[40:43], v[196:199], v[64:67], v[40:43]\n"
"ds_read_b64 v[62:63], v4 offset:61696\n"
"ds_read_b32 v90, v14 offset:400\n"
"v_mfma_f32_16x16x32_bf16 v[52:55], v[64:67], v[64:67], v[52:55]\n"
"v_mul_f32 v89, -v89, v88\n"
"v_mfma_f32_16x16x32_bf16 v[36:39], v[200:203], v[68:71], v[36:39]\n"
"ds_read_b64 v[64:65], v5 offset:61440\n"
"s_cbranch_vccnz .Lsl_216\n"
".Lbk_217:\n"
"v_mfma_f32_16x16x32_bf16 v[40:43], v[204:207], v[68:71], v[40:43]\n"
"ds_read_b64 v[66:67], v5 offset:61696\n"
"s_add_u32 m0, s29, 0xa000\n"
"s_waitcnt lgkmcnt(7)\n"
"v_mfma_f32_16x16x32_bf16 v[36:39], v[208:211], v[72:75], v[36:39]\n"
"ds_read_b64 v[68:69], v6 offset:61440\n"
"s_waitcnt lgkmcnt(3)\n"
"v_cmp_eq_u32 vcc, 0, v90\n"
"v_mul_f32 v28, v88, v28\n"
"v_mfma_f32_16x16x32_bf16 v[40:43], v[212:215], v[72:75], v[40:43]\n"
"ds_read_b64 v[70:71], v6 offset:61696\n"
"s_add_u32 s31, s44, 0x13b000\n"
"v_mul_f32 v29, v88, v29\n"
"v_mfma_f32_16x16x32_bf16 v[36:39], v[216:219], v[76:79], v[36:39]\n"
"s_cbranch_vccnz .Lsl_208\n"
".Lbk_209:\n"
"v_add_u32 v3, 0xffffb000, v3\n"
"ds_read_b64 v[72:73], v3 offset:0\n"
"buffer_load_dwordx4 v1, s[4:7], s31 offen offset:0 sc1 lds\n"
"v_mfma_f32_16x16x32_bf16 v[40:43], v[220:223], v[76:79], v[40:43]\n"
"ds_read_b64 v[74:75], v3 offset:256\n"
"v_mfma_f32_16x16x32_bf16 v[36:39], v[224:227], v[56:59], v[36:39]\n"
"ds_read_b64 v[76:77], v3 offset:1024\n"
"v_mul_f32 v30, v88, v30\n"
"v_mfma_f32_16x16x32_bf16 v[40:43], v[228:231], v[56:59], v[40:43]\n"
"ds_read_b64 v[78:79], v3 offset:1280\n"
"v_mul_f32 v31, v88, v31\n"
"v_mfma_f32_16x16x32_bf16 v[52:55], v[56:59], v[56:59], v[52:55]\n"
"buffer_load_dwordx4 v1, s[4:7], s31 offen offset:1024 sc1 lds\n"
"v_fmac_f32 v28, v89, v16\n"
"v_mfma_f32_16x16x32_bf16 v[36:39], v[232:235], v[60:63], v[36:39]\n"
"ds_read_b64 v[56:57], v3 offset:2048\n"
"v_fmac_f32 v29, v89, v17\n"
"v_mfma_f32_16x16x32_bf16 v[40:43], v[236:239], v[60:63], v[40:43]\n"
"ds_read_b64 v[58:59], v3 offset:2304\n"
"s_waitcnt lgkmcnt(6)\n"
"v_mfma_f32_16x16x32_bf16 v[36:39], v[240:243], v[64:67], v[36:39]\n"
"ds_read_b64 v[60:61], v3 offset:3072\n"
"v_cndmask_b32 v94, v52, v53, s[34:35]\n"
"v_mfma_f32_16x16x32_bf16 v[40:43], v[244:247], v[64:67], v[40:43]\n"
"ds_read_b64 v[62:63], v3 offset:3328\n"
"v_cndmask_b32 v54, v54, v55, s[34:35]\n"
"buffer_load_dwordx4 v1, s[4:7], s31 offen offset:2048 sc1 lds\n"
"v_mfma_f32_16x16x32_bf16 v[36:39], v[248:251], v[68:71], v[36:39]\n"
"ds_read_b64 v[64:65], v3 offset:4096\n"
"v_cndmask_b32 v94, v94, v54, s[20:21]\n"
"v_mfma_f32_16x16x32_bf16 v[40:43], v[252:255], v[68:71], v[40:43]\n"
"ds_read_b64 v[66:67], v3 offset:4352\n"
"s_waitcnt lgkmcnt(6)\n"
"v_mfma_f32_16x16x32_bf16 v[44:47], v[96:99], v[72:75], 0\n"
"ds_read_b64 v[68:69], v3 offset:5120\n"
"v_fmac_f32 v30, v89, v18\n"
"v_mfma_f32_16x16x32_bf16 v[48:51], v[100:103], v[72:75], 0\n"
"ds_read_b64 v[70:71], v3 offset:5376\n"
"v_cndmask_b32 v94, v36, v94, s[22:23]\n"
"buffer_load_dwordx4 v1, s[4:7], s31 offen offset:3072 sc1 lds\n"
"v_mfma_f32_16x16x32_bf16 v[52:55], v[72:75], v[72:75], 0\n"
"ds_write_b32 v9, v94 offset:4032\n"
"s_add_u32 m0, m0, 1024\n"
"v_mfma_f32_16x16x32_bf16 v[44:47], v[104:107], v[76:79], v[44:47]\n"
"ds_read_b64 v[72:73], v3 offset:6144\n"
"s_add_u32 s31, s31, 1024\n"
"v_mfma_f32_16x16x32_bf16 v[48:51], v[108:111], v[76:79], v[48:51]\n"
"ds_read_b64 v[74:75], v3 offset:6400\n"
"s_waitcnt lgkmcnt(7)\n"
"v_mfma_f32_16x16x32_bf16 v[44:47], v[112:115], v[56:59], v[44:47]\n"
"ds_read_b64 v[76:77], v3 offset:7168\n"
"buffer_load_dwordx4 v1, s[4:7], s31 offen offset:3072 sc1 lds\n"
"v_mfma_f32_16x16x32_bf16 v[48:51], v[116:119], v[56:59], v[48:51]\n"
"ds_read_b64 v[78:79], v3 offset:7424\n"
"v_fmac_f32 v31, v89, v19\n"
"v_mfma_f32_16x16x32_bf16 v[44:47], v[120:123], v[60:63], v[44:47]\n"
"ds_read_b64 v[56:57], v3 offset:8192\n"
"v_cvt_pk_bf16_f32 v28, v28, v29\n"
"v_mfma_f32_16x16x32_bf16 v[48:51], v[124:127], v[60:63], v[48:51]\n"
"ds_read_b64 v[58:59], v3 offset:8448\n"
"s_waitcnt lgkmcnt(7)\n"
"v_mfma_f32_16x16x32_bf16 v[44:47], v[128:131], v[64:67], v[44:47]\n"
"ds_read_b64 v[60:61], v3 offset:9216\n"
"v_cvt_pk_bf16_f32 v29, v30, v31\n"
"v_mfma_f32_16x16x32_bf16 v[48:51], v[132:135], v[64:67], v[48:51]\n"
"ds_read_b64 v[62:63], v3 offset:9472\n"
"v_mul_f32 v32, v88, v32\n"
"v_mfma_f32_16x16x32_bf16 v[52:55], v[64:67], v[64:67], v[52:55]\n"
"v_mul_f32 v33, v88, v33\n"
"v_mul_f32 v34, v88, v34\n"
"v_mfma_f32_16x16x32_bf16 v[44:47], v[136:139], v[68:71], v[44:47]\n"
"ds_read_b64 v[64:65], v3 offset:10240\n"
"v_mul_f32 v35, v88, v35\n"
"v_mfma_f32_16x16x32_bf16 v[48:51], v[140:143], v[68:71], v[48:51]\n"
"ds_read_b64 v[66:67], v3 offset:10496\n"
"s_waitcnt lgkmcnt(6)\n"
"v_mfma_f32_16x16x32_bf16 v[44:47], v[144:147], v[72:75], v[44:47]\n"
"ds_read_b64 v[68:69], v3 offset:11264\n"
"ds_read_b128 v[84:87], v11 offset:4032\n"
"ds_read_b32 v91, v12 offset:4032\n"
"v_mfma_f32_16x16x32_bf16 v[48:51], v[148:151], v[72:75], v[48:51]\n"
"ds_read_b64 v[70:71], v3 offset:11520\n"
"v_fmac_f32 v32, v89, v20\n"
"v_mfma_f32_16x16x32_bf16 v[44:47], v[152:155], v[76:79], v[44:47]\n"
"ds_read_b64 v[72:73], v3 offset:12288\n"
"v_fmac_f32 v33, v89, v21\n"
"v_mfma_f32_16x16x32_bf16 v[48:51], v[156:159], v[76:79], v[48:51]\n"
"ds_read_b64 v[74:75], v3 offset:12544\n"
"s_waitcnt lgkmcnt(8)\n"
"v_mfma_f32_16x16x32_bf16 v[44:47], v[160:163], v[56:59], v[44:47]\n"
"ds_read_b64 v[76:77], v3 offset:13312\n"
"v_fmac_f32 v34, v89, v22\n"
"v_mfma_f32_16x16x32_bf16 v[48:51], v[164:167], v[56:59], v[48:51]\n"
"ds_read_b64 v[78:79], v3 offset:13568\n"
"v_fmac_f32 v35, v89, v23\n"
"v_mfma_f32_16x16x32_bf16 v[52:55], v[56:59], v[56:59], v[52:55]\n"
"v_cvt_pk_bf16_f32 v32, v32, v33\n"
"v_cvt_pk_bf16_f32 v33, v34, v35\n"
"v_mfma_f32_16x16x32_bf16 v[44:47], v[168:171], v[60:63], v[44:47]\n"
"ds_read_b64 v[56:57], v3 offset:14336\n"
"s_waitcnt lgkmcnt(7)\n"
"v_add_f32 v84, v84, v85\n"
"v_add_f32 v86, v86, v87\n"
"v_mfma_f32_16x16x32_bf16 v[48:51], v[172:175], v[60:63], v[48:51]\n"
"ds_read_b64 v[58:59], v3 offset:14592\n"
"v_add_f32 v84, v84, v86\n"
"s_waitcnt lgkmcnt(6)\n"
"v_mfma_f32_16x16x32_bf16 v[44:47], v[176:179], v[64:67], v[44:47]\n"
"ds_read_b64 v[60:61], v3 offset:15360\n"
"v_mul_f32 v89, 0x3acccccd, v91\n"
"v_fma_f32 v84, -v91, v89, v84\n"
"v_mfma_f32_16x16x32_bf16 v[48:51], v[180:183], v[64:67], v[48:51]\n"
"ds_read_b64 v[62:63], v3 offset:15616\n"
"v_fmamk_f32 v84, v84, 0x3acccccd, v27\n"
"v_cmp_u_f32 vcc, v84, v84\n"
"v_mfma_f32_16x16x32_bf16 v[44:47], v[184:187], v[68:71], v[44:47]\n"
"ds_read_b64 v[64:65], v3 offset:16384\n"
"v_max_f32 v84, v84, v27\n"
"v_mfma_f32_16x16x32_bf16 v[48:51], v[188:191], v[68:71], v[48:51]\n"
"ds_read_b64 v[66:67], v3 offset:16640\n"
"s_waitcnt lgkmcnt(6)\n"
"v_mfma_f32_16x16x32_bf16 v[44:47], v[192:195], v[72:75], v[44:47]\n"
"ds_read_b64 v[68:69], v4 offset:0\n"
"v_rsq_f32 v88, v84\n"
"v_mfma_f32_16x16x32_bf16 v[48:51], v[196:199], v[72:75], v[48:51]\n"
"ds_read_b64 v[70:71], v4 offset:256\n"
"ds_read_b32 v90, v14 offset:416\n"
"v_mfma_f32_16x16x32_bf16 v[52:55], v[72:75], v[72:75], v[52:55]\n"
"v_mul_f32 v89, -v89, v88\n"
"v_mfma_f32_16x16x32_bf16 v[44:47], v[200:203], v[76:79], v[44:47]\n"
"ds_read_b64 v[72:73], v5 offset:0\n"
"s_cbranch_vccnz .Lsl_220\n"
".Lbk_221:\n"
"v_mfma_f32_16x16x32_bf16 v[48:51], v[204:207], v[76:79], v[48:51]\n"
"ds_read_b64 v[74:75], v5 offset:256\n"
"ds_write_b64 v7, v[28:29] offset:44928\n"
"s_waitcnt lgkmcnt(8)\n"
"v_mfma_f32_16x16x32_bf16 v[44:47], v[208:211], v[56:59], v[44:47]\n"
"ds_read_b64 v[76:77], v6 offset:0\n"
"s_waitcnt lgkmcnt(4)\n"
"v_cmp_eq_u32 vcc, 0, v90\n"
"v_mul_f32 v36, v88, v36\n"
"v_mfma_f32_16x16x32_bf16 v[48:51], v[212:215], v[56:59], v[48:51]\n"
"ds_read_b64 v[78:79], v6 offset:256\n"
"v_mul_f32 v37, v88, v37\n"
"v_mfma_f32_16x16x32_bf16 v[44:47], v[216:219], v[60:63], v[44:47]\n"
"s_cbranch_vccnz .Lsl_212\n"
".Lbk_213:\n"
"ds_read_b64 v[56:57], v3 offset:20480\n"
"v_mfma_f32_16x16x32_bf16 v[48:51], v[220:223], v[60:63], v[48:51]\n"
"ds_read_b64 v[58:59], v3 offset:20736\n"
"v_mfma_f32_16x16x32_bf16 v[44:47], v[224:227], v[64:67], v[44:47]\n"
"ds_read_b64 v[60:61], v3 offset:21504\n"
"v_mul_f32 v38, v88, v38\n"
"v_mfma_f32_16x16x32_bf16 v[48:51], v[228:231], v[64:67], v[48:51]\n"
"ds_read_b64 v[62:63], v3 offset:21760\n"
"v_mov_b32 v84, 0x3f803f80\n"
"v_mfma_f32_16x16x32_bf16 v[52:55], v[64:67], v[64:67], v[52:55]\n"
"v_mov_b32 v85, 0x3f803f80\n"
"ds_write_b64 v8, v[32:33] offset:44928\n"
"v_mfma_f32_16x16x32_bf16 v[44:47], v[232:235], v[68:71], v[44:47]\n"
"ds_read_b64 v[64:65], v3 offset:22528\n"
"v_mul_f32 v39, v88, v39\n"
"v_mfma_f32_16x16x32_bf16 v[48:51], v[236:239], v[68:71], v[48:51]\n"
"ds_read_b64 v[66:67], v3 offset:22784\n"
"s_waitcnt lgkmcnt(7)\n"
"v_mfma_f32_16x16x32_bf16 v[44:47], v[240:243], v[72:75], v[44:47]\n"
"ds_read_b64 v[68:69], v3 offset:23552\n"
"v_cndmask_b32 v94, v52, v53, s[34:35]\n"
"v_mfma_f32_16x16x32_bf16 v[48:51], v[244:247], v[72:75], v[48:51]\n"
"ds_read_b64 v[70:71], v3 offset:23808\n"
"v_cndmask_b32 v54, v54, v55, s[34:35]\n"
"v_mfma_f32_16x16x32_bf16 v[44:47], v[248:251], v[76:79], v[44:47]\n"
"ds_read_b64 v[72:73], v3 offset:24576\n"
"v_cndmask_b32 v94, v94, v54, s[20:21]\n"
"v_mfma_f32_16x16x32_bf16 v[48:51], v[252:255], v[76:79], v[48:51]\n"
"ds_read_b64 v[74:75], v3 offset:24832\n"
"s_waitcnt lgkmcnt(7)\n"
"v_mfma_f32_16x16x32_bf16 v[28:31], v[96:99], v[56:59], 0\n"
"ds_read_b64 v[76:77], v3 offset:25600\n"
"v_fmac_f32 v36, v89, v16\n"
"v_mfma_f32_16x16x32_bf16 v[32:35], v[100:103], v[56:59], 0\n"
"ds_read_b64 v[78:79], v3 offset:25856\n"
"v_cndmask_b32 v94, v44, v94, s[22:23]\n"
"s_waitcnt vmcnt(0)\n"
"ds_write_b64 v93, v[84:85] offset:40960\n"
"v_mfma_f32_16x16x32_bf16 v[52:55], v[56:59], v[56:59], 0\n"
"ds_write_b32 v9, v94 offset:4352\n"
"v_mfma_f32_16x16x32_bf16 v[28:31], v[104:107], v[60:63], v[28:31]\n"
"ds_read_b64 v[56:57], v3 offset:26624\n"
"v_fmac_f32 v37, v89, v17\n"
"v_mfma_f32_16x16x32_bf16 v[32:35], v[108:111], v[60:63], v[32:35]\n"
"ds_read_b64 v[58:59], v3 offset:26880\n"
"s_waitcnt lgkmcnt(8)\n"
"v_mfma_f32_16x16x32_bf16 v[28:31], v[112:115], v[64:67], v[28:31]\n"
"ds_read_b64 v[60:61], v3 offset:27648\n"
"v_fmac_f32 v38, v89, v18\n"
"v_mfma_f32_16x16x32_bf16 v[32:35], v[116:119], v[64:67], v[32:35]\n"
"ds_read_b64 v[62:63], v3 offset:27904\n"
"v_fmac_f32 v39, v89, v19\n"
"v_mfma_f32_16x16x32_bf16 v[28:31], v[120:123], v[68:71], v[28:31]\n"
"ds_read_b64 v[64:65], v3 offset:28672\n"
"v_cvt_pk_bf16_f32 v36, v36, v37\n"
"v_mfma_f32_16x16x32_bf16 v[32:35], v[124:127], v[68:71], v[32:35]\n"
"ds_read_b64 v[66:67], v3 offset:28928\n"
"s_waitcnt lgkmcnt(8)\n"
"v_mfma_f32_16x16x32_bf16 v[28:31], v[128:131], v[72:75], v[28:31]\n"
"ds_read_b64 v[68:69], v3 offset:29696\n"
"v_cvt_pk_bf16_f32 v37, v38, v39\n"
"v_mfma_f32_16x16x32_bf16 v[32:35], v[132:135], v[72:75], v[32:35]\n"
"ds_read_b64 v[70:71], v3 offset:29952\n"
"v_mul_f32 v40, v88, v40\n"
"v_mfma_f32_16x16x32_bf16 v[52:55], v[72:75], v[72:75], v[52:55]\n"
"v_mul_f32 v41, v88, v41\n"
"v_mul_f32 v42, v88, v42\n"
"v_mfma_f32_16x16x32_bf16 v[28:31], v[136:139], v[76:79], v[28:31]\n"
"ds_read_b64 v[72:73], v3 offset:30720\n"
"v_mul_f32 v43, v88, v43\n"
"v_mfma_f32_16x16x32_bf16 v[32:35], v[140:143], v[76:79], v[32:35]\n"
"ds_read_b64 v[74:75], v3 offset:30976\n"
"s_waitcnt lgkmcnt(6)\n"
"v_mfma_f32_16x16x32_bf16 v[28:31], v[144:147], v[56:59], v[28:31]\n"
"ds_read_b64 v[76:77], v3 offset:31744\n"
"ds_read_b128 v[84:87], v11 offset:4352\n"
"ds_read_b32 v91, v12 offset:4352\n"
"v_mfma_f32_16x16x32_bf16 v[32:35], v[148:151], v[56:59], v[32:35]\n"
"ds_read_b64 v[78:79], v3 offset:32000\n"
"v_fmac_f32 v40, v89, v20\n"
"v_mfma_f32_16x16x32_bf16 v[28:31], v[152:155], v[60:63], v[28:31]\n"
"ds_read_b64 v[56:57], v3 offset:32768\n"
"v_fmac_f32 v41, v89, v21\n"
"v_mfma_f32_16x16x32_bf16 v[32:35], v[156:159], v[60:63], v[32:35]\n"
"ds_read_b64 v[58:59], v3 offset:33024\n"
"s_waitcnt lgkmcnt(8)\n"
"v_mfma_f32_16x16x32_bf16 v[28:31], v[160:163], v[64:67], v[28:31]\n"
"ds_read_b64 v[60:61], v3 offset:33792\n"
"v_fmac_f32 v42, v89, v22\n"
"v_mfma_f32_16x16x32_bf16 v[32:35], v[164:167], v[64:67], v[32:35]\n"
"ds_read_b64 v[62:63], v3 offset:34048\n"
"v_fmac_f32 v43, v89, v23\n"
"v_mfma_f32_16x16x32_bf16 v[52:55], v[64:67], v[64:67], v[52:55]\n"
"v_cvt_pk_bf16_f32 v40, v40, v41\n"
"v_cvt_pk_bf16_f32 v41, v42, v43\n"
"v_mfma_f32_16x16x32_bf16 v[28:31], v[168:171], v[68:71], v[28:31]\n"
"ds_read_b64 v[64:65], v3 offset:34816\n"
"s_waitcnt lgkmcnt(7)\n"
"v_add_f32 v84, v84, v85\n"
"v_add_f32 v86, v86, v87\n"
"v_mfma_f32_16x16x32_bf16 v[32:35], v[172:175], v[68:71], v[32:35]\n"
"ds_read_b64 v[66:67], v3 offset:35072\n"
"v_add_f32 v84, v84, v86\n"
"s_waitcnt lgkmcnt(6)\n"
"v_mfma_f32_16x16x32_bf16 v[28:31], v[176:179], v[72:75], v[28:31]\n"
"ds_read_b64 v[68:69], v3 offset:35840\n"
"v_mul_f32 v89, 0x3acccccd, v91\n"
"v_fma_f32 v84, -v91, v89, v84\n"
"v_mfma_f32_16x16x32_bf16 v[32:35], v[180:183], v[72:75], v[32:35]\n"
"ds_read_b64 v[70:71], v3 offset:36096\n"
"v_fmamk_f32 v84, v84, 0x3acccccd, v27\n"
"v_mfma_f32_16x16x32_bf16 v[28:31], v[184:187], v[76:79], v[28:31]\n"
"ds_read_b64 v[72:73], v3 offset:36864\n"
"v_cmp_u_f32 vcc, v84, v84\n"
"v_mfma_f32_16x16x32_bf16 v[32:35], v[188:191], v[76:79], v[32:35]\n"
"ds_read_b64 v[74:75], v3 offset:37120\n"
"s_waitcnt lgkmcnt(6)\n"
"v_mfma_f32_16x16x32_bf16 v[28:31], v[192:195], v[56:59], v[28:31]\n"
"ds_read_b64 v[76:77], v4 offset:20480\n"
"v_max_f32 v84, v84, v27\n"
"v_mfma_f32_16x16x32_bf16 v[32:35], v[196:199], v[56:59], v[32:35]\n"
"ds_read_b64 v[78:79], v4 offset:20736\n"
"v_rsq_f32 v88, v84\n"
"v_mfma_f32_16x16x32_bf16 v[52:55], v[56:59], v[56:59], v[52:55]\n"
"v_mul_f32 v89, -v89, v88\n"
"v_mfma_f32_16x16x32_bf16 v[28:31], v[200:203], v[60:63], v[28:31]\n"
"ds_read_b64 v[56:57], v5 offset:20480\n"
"s_cbranch_vccnz .Lsl_224\n"
".Lbk_225:\n"
"v_mfma_f32_16x16x32_bf16 v[32:35], v[204:207], v[60:63], v[32:35]\n"
"ds_read_b64 v[58:59], v5 offset:20736\n"
"ds_write_b64 v7, v[36:37] offset:48896\n"
"s_waitcnt lgkmcnt(7)\n"
"v_mfma_f32_16x16x32_bf16 v[28:31], v[208:211], v[64:67], v[28:31]\n"
"ds_read_b64 v[60:61], v6 offset:20480\n"
"v_mul_f32 v44, v88, v44\n"
"v_mul_f32 v45, v88, v45\n"
"v_mfma_f32_16x16x32_bf16 v[32:35], v[212:215], v[64:67], v[32:35]\n"
"ds_read_b64 v[62:63], v6 offset:20736\n"
"v_mul_f32 v46, v88, v46\n"
"v_mul_f32 v47, v88, v47\n"
"v_mfma_f32_16x16x32_bf16 v[28:31], v[216:219], v[68:71], v[28:31]\n"
"ds_read_b64 v[64:65], v3 offset:40960\n"
"v_fmac_f32 v44, v89, v16\n"
"v_fmac_f32 v45, v89, v17\n"
"v_mfma_f32_16x16x32_bf16 v[32:35], v[220:223], v[68:71], v[32:35]\n"
"ds_read_b64 v[66:67], v3 offset:41216\n"
"v_fmac_f32 v46, v89, v18\n"
"s_waitcnt lgkmcnt(7)\n"
"v_mfma_f32_16x16x32_bf16 v[28:31], v[224:227], v[72:75], v[28:31]\n"
"ds_read_b64 v[68:69], v3 offset:45056\n"
"v_fmac_f32 v47, v89, v19\n"
"v_cvt_pk_bf16_f32 v44, v44, v45\n"
"v_mfma_f32_16x16x32_bf16 v[32:35], v[228:231], v[72:75], v[32:35]\n"
"ds_read_b64 v[70:71], v3 offset:45312\n"
"v_cvt_pk_bf16_f32 v45, v46, v47\n"
"v_mul_f32 v48, v88, v48\n"
"v_mfma_f32_16x16x32_bf16 v[52:55], v[72:75], v[72:75], v[52:55]\n"
"ds_write_b64 v8, v[40:41] offset:48896\n"
"v_mfma_f32_16x16x32_bf16 v[28:31], v[232:235], v[76:79], v[28:31]\n"
"ds_read_b64 v[72:73], v3 offset:49152\n"
"v_mul_f32 v49, v88, v49\n"
"v_mul_f32 v50, v88, v50\n"
"v_mfma_f32_16x16x32_bf16 v[32:35], v[236:239], v[76:79], v[32:35]\n"
"ds_read_b64 v[74:75], v3 offset:49408\n"
"v_mul_f32 v51, v88, v51\n"
"s_waitcnt lgkmcnt(7)\n"
"v_mfma_f32_16x16x32_bf16 v[28:31], v[240:243], v[56:59], v[28:31]\n"
"ds_read_b64 v[76:77], v3 offset:53248\n"
"v_cndmask_b32 v94, v52, v53, s[34:35]\n"
"v_fmac_f32 v48, v89, v20\n"
"v_mfma_f32_16x16x32_bf16 v[32:35], v[244:247], v[56:59], v[32:35]\n"
"ds_read_b64 v[78:79], v3 offset:53504\n"
"v_cndmask_b32 v54, v54, v55, s[34:35]\n"
"v_fmac_f32 v49, v89, v21\n"
"v_mfma_f32_16x16x32_bf16 v[28:31], v[248:251], v[60:63], v[28:31]\n"
"ds_read_b64 v[56:57], v3 offset:57344\n"
"v_cndmask_b32 v94, v94, v54, s[20:21]\n"
"v_fmac_f32 v50, v89, v22\n"
"v_mfma_f32_16x16x32_bf16 v[32:35], v[252:255], v[60:63], v[32:35]\n"
"ds_read_b64 v[58:59], v3 offset:57600\n"
"s_waitcnt lgkmcnt(7)\n"
"s_cmp_eq_u32 s3, 3\n"
"s_cbranch_scc1 .Lfu_236\n"
"ds_read_b64 v[60:61], v3 offset:41984\n"
"v_fmac_f32 v51, v89, v23\n"
"v_mfma_f32_16x16x32_bf16 v[40:43], v[100:103], v[64:67], 0\n"
"ds_read_b64 v[62:63], v3 offset:42240\n"
"v_cndmask_b32 v94, v28, v94, s[22:23]\n"
"v_mfma_f32_16x16x32_bf16 v[52:55], v[64:67], v[64:67], 0\n"
"ds_write_b32 v9, v94 offset:4672\n"
"ds_read_b64 v[64:65], v3 offset:43008\n"
"v_cvt_pk_bf16_f32 v48, v48, v49\n"
"v_mfma_f32_16x16x32_bf16 v[40:43], v[132:135], v[68:71], v[40:43]\n"
"ds_read_b64 v[66:67], v3 offset:43264\n"
"v_cvt_pk_bf16_f32 v49, v50, v51\n"
"v_mfma_f32_16x16x32_bf16 v[52:55], v[68:71], v[68:71], v[52:55]\n"
"ds_write_b64 v7, v[44:45] offset:52864\n"
"s_waitcnt lgkmcnt(8)\n"
"ds_read_b64 v[68:69], v3 offset:44032\n"
"v_mfma_f32_16x16x32_bf16 v[40:43], v[164:167], v[72:75], v[40:43]\n"
"ds_read_b64 v[70:71], v3 offset:44288\n"
"v_mfma_f32_16x16x32_bf16 v[52:55], v[72:75], v[72:75], v[52:55]\n"
"ds_write_b64 v8, v[48:49] offset:52864\n"
"ds_read_b64 v[72:73], v3 offset:46080\n"
"v_mfma_f32_16x16x32_bf16 v[40:43], v[196:199], v[76:79], v[40:43]\n"
"ds_read_b64 v[74:75], v3 offset:46336\n"
"v_mfma_f32_16x16x32_bf16 v[52:55], v[76:79], v[76:79], v[52:55]\n"
"ds_read_b128 v[84:87], v11 offset:4672\n"
"ds_read_b32 v91, v12 offset:4672\n"
"s_waitcnt lgkmcnt(11)\n"
"ds_read_b64 v[76:77], v3 offset:47104\n"
"v_mfma_f32_16x16x32_bf16 v[40:43], v[228:231], v[56:59], v[40:43]\n"
"ds_read_b64 v[78:79], v3 offset:47360\n"
"v_mfma_f32_16x16x32_bf16 v[52:55], v[56:59], v[56:59], v[52:55]\n"
"ds_read_b64 v[56:57], v3 offset:48128\n"
"v_mfma_f32_16x16x32_bf16 v[40:43], v[108:111], v[60:63], v[40:43]\n"
"ds_read_b64 v[58:59], v3 offset:48384\n"
"s_waitcnt lgkmcnt(9)\n"
"ds_read_b64 v[60:61], v3 offset:50176\n"
"s_nop 1\n"
"ds_write_b32 v10, v52 offset:5312\n"
"v_cndmask_b32 v94, v52, v53, s[34:35]\n"
"v_cndmask_b32 v54, v54, v55, s[34:35]\n"
"v_mfma_f32_16x16x32_bf16 v[40:43], v[116:119], v[64:67], v[40:43]\n"
"ds_read_b64 v[62:63], v3 offset:50432\n"
"s_waitcnt lgkmcnt(8)\n"
"v_add_f32 v84, v84, v85\n"
"v_add_f32 v86, v86, v87\n"
"v_cndmask_b32 v94, v94, v54, s[20:21]\n"
"ds_write_b32 v9, v94 offset:4992\n"
"ds_read_b64 v[64:65], v3 offset:51200\n"
"v_add_f32 v84, v84, v86\n"
"s_waitcnt lgkmcnt(9)\n"
"v_mul_f32 v89, 0x3acccccd, v91\n"
"v_mfma_f32_16x16x32_bf16 v[40:43], v[124:127], v[68:71], v[40:43]\n"
"ds_read_b64 v[66:67], v3 offset:51456\n"
"v_fma_f32 v84, -v91, v89, v84\n"
"s_waitcnt lgkmcnt(8)\n"
"ds_read_b64 v[68:69], v3 offset:52224\n"
"v_fmamk_f32 v84, v84, 0x3acccccd, v27\n"
"v_cmp_u_f32 vcc, v84, v84\n"
"v_mfma_f32_16x16x32_bf16 v[40:43], v[140:143], v[72:75], v[40:43]\n"
"ds_read_b64 v[70:71], v3 offset:52480\n"
"v_max_f32 v84, v84, v27\n"
"v_rsq_f32 v88, v84\n"
"ds_read_b64 v[72:73], v3 offset:54272\n"
"v_mul_f32 v89, -v89, v88\n"
"v_mfma_f32_16x16x32_bf16 v[40:43], v[148:151], v[76:79], v[40:43]\n"
"ds_read_b64 v[74:75], v3 offset:54528\n"
"s_cbranch_vccnz .Lsl_228c\n"
".Lbk_229c:\n"
"s_waitcnt lgkmcnt(7)\n"
"ds_read_b64 v[76:77], v3 offset:55296\n"
"v_mul_f32 v28, v88, v28\n"
"v_mul_f32 v29, v88, v29\n"
"v_mfma_f32_16x16x32_bf16 v[40:43], v[156:159], v[56:59], v[40:43]\n"
"ds_read_b64 v[78:79], v3 offset:55552\n"
"ds_read_b128 v[44:47], v11 offset:4992\n"
"ds_read_b128 v[48:51], v11 offset:5312\n"
"ds_read_b64 v[56:57], v3 offset:56320\n"
"v_mul_f32 v30, v88, v30\n"
"v_mul_f32 v31, v88, v31\n"
"v_mfma_f32_16x16x32_bf16 v[40:43], v[172:175], v[60:63], v[40:43]\n"
"ds_read_b64 v[58:59], v3 offset:56576\n"
"v_fmac_f32 v28, v89, v16\n"
"s_waitcnt lgkmcnt(8)\n"
"ds_read_b64 v[60:61], v4 offset:40960\n"
"v_fmac_f32 v29, v89, v17\n"
"v_fmac_f32 v30, v89, v18\n"
"v_mfma_f32_16x16x32_bf16 v[40:43], v[180:183], v[64:67], v[40:43]\n"
"ds_read_b64 v[62:63], v4 offset:41216\n"
"v_fmac_f32 v31, v89, v19\n"
"v_cvt_pk_bf16_f32 v28, v28, v29\n"
"ds_read_b64 v[64:65], v5 offset:40960\n"
"v_cvt_pk_bf16_f32 v29, v30, v31\n"
"ds_write_b64 v7, v[28:29] offset:56832\n"
"v_mfma_f32_16x16x32_bf16 v[40:43], v[188:191], v[68:71], v[40:43]\n"
"ds_read_b64 v[66:67], v5 offset:41216\n"
"v_mul_f32 v32, v88, v32\n"
"s_waitcnt lgkmcnt(9)\n"
"ds_read_b64 v[68:69], v6 offset:40960\n"
"s_waitcnt lgkmcnt(9)\n"
"v_add_f32 v44, v44, v45\n"
"v_mul_f32 v33, v88, v33\n"
"v_mfma_f32_16x16x32_bf16 v[40:43], v[204:207], v[72:75], v[40:43]\n"
"ds_read_b64 v[70:71], v6 offset:41216\n"
"v_add_f32 v46, v46, v47\n"
"v_mul_f32 v34, v88, v34\n"
"s_waitcnt lgkmcnt(9)\n"
"v_add_f32 v48, v48, v49\n"
"v_add_f32 v50, v50, v51\n"
"v_mul_f32 v35, v88, v35\n"
"v_mfma_f32_16x16x32_bf16 v[40:43], v[212:215], v[76:79], v[40:43]\n"
"v_add_f32 v48, v48, v50\n"
"v_fmac_f32 v32, v89, v20\n"
"s_waitcnt lgkmcnt(5)\n"
"v_add_f32 v44, v44, v46\n"
"v_mul_f32 v53, 0x3acccccd, v48\n"
"v_fmac_f32 v33, v89, v21\n"
"v_mfma_f32_16x16x32_bf16 v[40:43], v[220:223], v[56:59], v[40:43]\n"
"v_fma_f32 v44, -v48, v53, v44\n"
"v_fmamk_f32 v44, v44, 0x3acccccd, v27\n"
"v_fmac_f32 v34, v89, v22\n"
"v_cmp_u_f32 vcc, v44, v44\n"
"v_max_f32 v44, v44, v27\n"
"v_fmac_f32 v35, v89, v23\n"
"v_mfma_f32_16x16x32_bf16 v[40:43], v[236:239], v[60:63], v[40:43]\n"
"v_rsq_f32 v52, v44\n"
"s_waitcnt lgkmcnt(0)\n"
"v_mul_f32 v53, -v53, v52\n"
"v_cvt_pk_bf16_f32 v32, v32, v33\n"
"v_cvt_pk_bf16_f32 v33, v34, v35\n"
"v_mfma_f32_16x16x32_bf16 v[40:43], v[244:247], v[64:67], v[40:43]\n"
"s_cbranch_vccnz .Lsl_232c\n"
".Lbk_233c:\n"
"ds_write_b64 v8, v[32:33] offset:56832\n"
"v_mfma_f32_16x16x32_bf16 v[40:43], v[252:255], v[68:71], v[40:43]\n"
"v_cmp_u_f32 vcc, v92, v92\n"
"s_nop 6\n"
"v_mul_f32 v40, v52, v40\n"
"v_mul_f32 v41, v52, v41\n"
"v_mul_f32 v42, v52, v42\n"
"v_mul_f32 v43, v52, v43\n"
"v_fmac_f32 v40, v53, v20\n"
"v_fmac_f32 v41, v53, v21\n"
"v_fmac_f32 v42, v53, v22\n"
"v_fmac_f32 v43, v53, v23\n"
"v_cvt_pk_bf16_f32 v40, v40, v41\n"
"v_cvt_pk_bf16_f32 v41, v42, v43\n"
"ds_write_b64 v8, v[40:41] offset:40960\n"
"s_cbranch_vccnz .Lrd_203\n"
".Lgo_202c:\n"
"ds_write_b32 v13, v13 offset:432\n"
"s_branch .Ljn_237\n"
".Lfu_236:\n"
"s_nop 0\n"
"v_mfma_f32_16x16x32_bf16 v[36:39], v[96:99], v[64:67], 0\n"
"ds_read_b64 v[60:61], v3 offset:41984\n"
"v_fmac_f32 v51, v89, v23\n"
"v_mfma_f32_16x16x32_bf16 v[40:43], v[100:103], v[64:67], 0\n"
"ds_read_b64 v[62:63], v3 offset:42240\n"
"v_cndmask_b32 v94, v28, v94, s[22:23]\n"
"v_mfma_f32_16x16x32_bf16 v[52:55], v[64:67], v[64:67], 0\n"
"ds_write_b32 v9, v94 offset:4672\n"
"v_mfma_f32_16x16x32_bf16 v[36:39], v[128:131], v[68:71], v[36:39]\n"
"ds_read_b64 v[64:65], v3 offset:43008\n"
"v_cvt_pk_bf16_f32 v48, v48, v49\n"
"v_mfma_f32_16x16x32_bf16 v[40:43], v[132:135], v[68:71], v[40:43]\n"
"ds_read_b64 v[66:67], v3 offset:43264\n"
"v_cvt_pk_bf16_f32 v49, v50, v51\n"
"v_mfma_f32_16x16x32_bf16 v[52:55], v[68:71], v[68:71], v[52:55]\n"
"ds_write_b64 v7, v[44:45] offset:52864\n"
"s_waitcnt lgkmcnt(8)\n"
"v_mfma_f32_16x16x32_bf16 v[36:39], v[160:163], v[72:75], v[36:39]\n"
"ds_read_b64 v[68:69], v3 offset:44032\n"
"v_mfma_f32_16x16x32_bf16 v[40:43], v[164:167], v[72:75], v[40:43]\n"
"ds_read_b64 v[70:71], v3 offset:44288\n"
"v_mfma_f32_16x16x32_bf16 v[52:55], v[72:75], v[72:75], v[52:55]\n"
"ds_write_b64 v8, v[48:49] offset:52864\n"
"v_mfma_f32_16x16x32_bf16 v[36:39], v[192:195], v[76:79], v[36:39]\n"
"ds_read_b64 v[72:73], v3 offset:46080\n"
"v_mfma_f32_16x16x32_bf16 v[40:43], v[196:199], v[76:79], v[40:43]\n"
"ds_read_b64 v[74:75], v3 offset:46336\n"
"v_mfma_f32_16x16x32_bf16 v[52:55], v[76:79], v[76:79], v[52:55]\n"
"ds_read_b128 v[84:87], v11 offset:4672\n"
"ds_read_b32 v91, v12 offset:4672\n"
"s_waitcnt lgkmcnt(11)\n"
"v_mfma_f32_16x16x32_bf16 v[36:39], v[224:227], v[56:59], v[36:39]\n"
"ds_read_b64 v[76:77], v3 offset:47104\n"
"v_mfma_f32_16x16x32_bf16 v[40:43], v[228:231], v[56:59], v[40:43]\n"
"ds_read_b64 v[78:79], v3 offset:47360\n"
"v_mfma_f32_16x16x32_bf16 v[52:55], v[56:59], v[56:59], v[52:55]\n"
"v_mfma_f32_16x16x32_bf16 v[36:39], v[104:107], v[60:63], v[36:39]\n"
"ds_read_b64 v[56:57], v3 offset:48128\n"
"v_mfma_f32_16x16x32_bf16 v[40:43], v[108:111], v[60:63], v[40:43]\n"
"ds_read_b64 v[58:59], v3 offset:48384\n"
"s_waitcnt lgkmcnt(9)\n"
"v_mfma_f32_16x16x32_bf16 v[36:39], v[112:115], v[64:67], v[36:39]\n"
"ds_read_b64 v[60:61], v3 offset:50176\n"
"ds_write_b32 v10, v52 offset:5312\n"
"v_cndmask_b32 v94, v52, v53, s[34:35]\n"
"v_cndmask_b32 v54, v54, v55, s[34:35]\n"
"v_mfma_f32_16x16x32_bf16 v[40:43], v[116:119], v[64:67], v[40:43]\n"
"ds_read_b64 v[62:63], v3 offset:50432\n"
"s_waitcnt lgkmcnt(8)\n"
"v_add_f32 v84, v84, v85\n"
"v_add_f32 v86, v86, v87\n"
"v_cndmask_b32 v94, v94, v54, s[20:21]\n"
"ds_write_b32 v9, v94 offset:4992\n"
"v_mfma_f32_16x16x32_bf16 v[36:39], v[120:123], v[68:71], v[36:39]\n"
"ds_read_b64 v[64:65], v3 offset:51200\n"
"v_add_f32 v84, v84, v86\n"
"s_waitcnt lgkmcnt(9)\n"
"v_mul_f32 v89, 0x3acccccd, v91\n"
"v_mfma_f32_16x16x32_bf16 v[40:43], v[124:127], v[68:71], v[40:43]\n"
"ds_read_b64 v[66:67], v3 offset:51456\n"
"v_fma_f32 v84, -v91, v89, v84\n"
"s_waitcnt lgkmcnt(8)\n"
"v_mfma_f32_16x16x32_bf16 v[36:39], v[136:139], v[72:75], v[36:39]\n"
"ds_read_b64 v[68:69], v3 offset:52224\n"
"v_fmamk_f32 v84, v84, 0x3acccccd, v27\n"
"v_cmp_u_f32 vcc, v84, v84\n"
"v_mfma_f32_16x16x32_bf16 v[40:43], v[140:143], v[72:75], v[40:43]\n"
"ds_read_b64 v[70:71], v3 offset:52480\n"
"v_max_f32 v84, v84, v27\n"
"v_rsq_f32 v88, v84\n"
"v_mfma_f32_16x16x32_bf16 v[36:39], v[144:147], v[76:79], v[36:39]\n"
"ds_read_b64 v[72:73], v3 offset:54272\n"
"v_mul_f32 v89, -v89, v88\n"
"v_mfma_f32_16x16x32_bf16 v[40:43], v[148:151], v[76:79], v[40:43]\n"
"ds_read_b64 v[74:75], v3 offset:54528\n"
"s_cbranch_vccnz .Lsl_228\n"
".Lbk_229:\n"
"s_waitcnt lgkmcnt(7)\n"
"v_mfma_f32_16x16x32_bf16 v[36:39], v[152:155], v[56:59], v[36:39]\n"
"ds_read_b64 v[76:77], v3 offset:55296\n"
"v_mul_f32 v28, v88, v28\n"
"v_mul_f32 v29, v88, v29\n"
"v_mfma_f32_16x16x32_bf16 v[40:43], v[156:159], v[56:59], v[40:43]\n"
"ds_read_b64 v[78:79], v3 offset:55552\n"
"ds_read_b128 v[44:47], v11 offset:4992\n"
"ds_read_b128 v[48:51], v11 offset:5312\n"
"v_mfma_f32_16x16x32_bf16 v[36:39], v[168:171], v[60:63], v[36:39]\n"
"ds_read_b64 v[56:57], v3 offset:56320\n"
"v_mul_f32 v30, v88, v30\n"
"v_mul_f32 v31, v88, v31\n"
"v_mfma_f32_16x16x32_bf16 v[40:43], v[172:175], v[60:63], v[40:43]\n"
"ds_read_b64 v[58:59], v3 offset:56576\n"
"v_fmac_f32 v28, v89, v16\n"
"s_waitcnt lgkmcnt(8)\n"
"v_mfma_f32_16x16x32_bf16 v[36:39], v[176:179], v[64:67], v[36:39]\n"
"ds_read_b64 v[60:61], v4 offset:40960\n"
"v_fmac_f32 v29, v89, v17\n"
"v_fmac_f32 v30, v89, v18\n"
"v_mfma_f32_16x16x32_bf16 v[40:43], v[180:183], v[64:67], v[40:43]\n"
"ds_read_b64 v[62:63], v4 offset:41216\n"
"v_fmac_f32 v31, v89, v19\n"
"v_cvt_pk_bf16_f32 v28, v28, v29\n"
"v_mfma_f32_16x16x32_bf16 v[36:39], v[184:187], v[68:71], v[36:39]\n"
"ds_read_b64 v[64:65], v5 offset:40960\n"
"v_cvt_pk_bf16_f32 v29, v30, v31\n"
"ds_write_b64 v7, v[28:29] offset:56832\n"
"v_mfma_f32_16x16x32_bf16 v[40:43], v[188:191], v[68:71], v[40:43]\n"
"ds_read_b64 v[66:67], v5 offset:41216\n"
"v_mul_f32 v32, v88, v32\n"
"s_waitcnt lgkmcnt(9)\n"
"v_mfma_f32_16x16x32_bf16 v[36:39], v[200:203], v[72:75], v[36:39]\n"
"ds_read_b64 v[68:69], v6 offset:40960\n"
"s_waitcnt lgkmcnt(9)\n"
"v_add_f32 v44, v44, v45\n"
"v_mul_f32 v33, v88, v33\n"
"v_mfma_f32_16x16x32_bf16 v[40:43], v[204:207], v[72:75], v[40:43]\n"
"ds_read_b64 v[70:71], v6 offset:41216\n"
"v_add_f32 v46, v46, v47\n"
"v_mul_f32 v34, v88, v34\n"
"v_mfma_f32_16x16x32_bf16 v[36:39], v[208:211], v[76:79], v[36:39]\n"
"s_waitcnt lgkmcnt(9)\n"
"v_add_f32 v48, v48, v49\n"
"v_add_f32 v50, v50, v51\n"
"v_mul_f32 v35, v88, v35\n"
"v_mfma_f32_16x16x32_bf16 v[40:43], v[212:215], v[76:79], v[40:43]\n"
"v_add_f32 v48, v48, v50\n"
"v_fmac_f32 v32, v89, v20\n"
"s_waitcnt lgkmcnt(5)\n"
"v_mfma_f32_16x16x32_bf16 v[36:39], v[216:219], v[56:59], v[36:39]\n"
"v_add_f32 v44, v44, v46\n"
"v_mul_f32 v53, 0x3acccccd, v48\n"
"v_fmac_f32 v33, v89, v21\n"
"v_mfma_f32_16x16x32_bf16 v[40:43], v[220:223], v[56:59], v[40:43]\n"
"v_fma_f32 v44, -v48, v53, v44\n"
"v_fmamk_f32 v44, v44, 0x3acccccd, v27\n"
"v_fmac_f32 v34, v89, v22\n"
"v_mfma_f32_16x16x32_bf16 v[36:39], v[232:235], v[60:63], v[36:39]\n"
"v_cmp_u_f32 vcc, v44, v44\n"
"v_max_f32 v44, v44, v27\n"
"v_fmac_f32 v35, v89, v23\n"
"v_mfma_f32_16x16x32_bf16 v[40:43], v[236:239], v[60:63], v[40:43]\n"
"v_rsq_f32 v52, v44\n"
"s_waitcnt lgkmcnt(0)\n"
"v_mfma_f32_16x16x32_bf16 v[36:39], v[240:243], v[64:67], v[36:39]\n"
"v_mul_f32 v53, -v53, v52\n"
"v_cvt_pk_bf16_f32 v32, v32, v33\n"
"v_cvt_pk_bf16_f32 v33, v34, v35\n"
"v_mfma_f32_16x16x32_bf16 v[40:43], v[244:247], v[64:67], v[40:43]\n"
"s_cbranch_vccnz .Lsl_232\n"
".Lbk_233:\n"
"ds_write_b64 v8, v[32:33] offset:56832\n"
"v_mfma_f32_16x16x32_bf16 v[36:39], v[248:251], v[68:71], v[36:39]\n"
"v_mfma_f32_16x16x32_bf16 v[40:43], v[252:255], v[68:71], v[40:43]\n"
"v_cmp_u_f32 vcc, v92, v92\n"
"s_nop 3\n"
"v_mul_f32 v36, v52, v36\n"
"v_mul_f32 v37, v52, v37\n"
"s_cbranch_vccnz .Lrd_203\n"
".Lgo_202:\n"
"ds_write_b32 v13, v13 offset:432\n"
"v_mul_f32 v38, v52, v38\n"
"v_mul_f32 v39, v52, v39\n"
"v_fmac_f32 v36, v53, v16\n"
"v_fmac_f32 v37, v53, v17\n"
"v_fmac_f32 v38, v53, v18\n"
"v_fmac_f32 v39, v53, v19\n"
"v_cvt_pk_bf16_f32 v36, v36, v37\n"
"v_cvt_pk_bf16_f32 v37, v38, v39\n"
"ds_write_b64 v7, v[36:37] offset:40960\n"
"v_mul_f32 v40, v52, v40\n"
"v_mul_f32 v41, v52, v41\n"
"v_mul_f32 v42, v52, v42\n"
"v_mul_f32 v43, v52, v43\n"
"v_fmac_f32 v40, v53, v20\n"
"v_fmac_f32 v41, v53, v21\n"
"v_fmac_f32 v42, v53, v22\n"
"v_fmac_f32 v43, v53, v23\n"
"v_cvt_pk_bf16_f32 v40, v40, v41\n"
"v_cvt_pk_bf16_f32 v41, v42, v43\n"
"ds_write_b64 v8, v[40:41] offset:40960\n"
".Ljn_237:\n"
"v_mov_b32 v46, 0\n"
"v_mov_b32 v47, 0\n"
"v_mov_b32 v50, 0\n"
"v_mov_b32 v51, 0\n"
"s_cmp_lg_u32 s3, 2\n"
"s_cbranch_scc1 .Lnw_282\n"
"s_mov_b32 s17, 0x100000\n"
".Ltp_283:\n"
"ds_read_b32 v90, v14 offset:432\n"
"s_sub_u32 s17, s17, 1\n"
"s_cbranch_scc1 .Lou_284\n"
"s_waitcnt lgkmcnt(0)\n"
"v_cmp_eq_u32 vcc, 0, v90\n"
"s_cbranch_vccnz .Ltp_283\n"
".Lou_284:\n"
"s_waitcnt lgkmcnt(0)\n"
".Lnw_282:\n"
"ds_read_b32 v90, v14 offset:432\n"
"s_mov_b64 exec, 0xffff\n"
"ds_read_b64 v[46:47], v24 offset:44960\n"
"ds_read_b64 v[50:51], v24 offset:45000\n"
"s_mov_b64 exec, -1\n"
"ds_read_b64 v[44:45], v25 offset:44928\n"
"ds_read_b64 v[56:57], v25 offset:41040\n"
"ds_read_b64 v[58:59], v24 offset:41072\n"
"ds_read_b64 v[64:65], v25 offset:45008\n"
"ds_read_b64 v[66:67], v24 offset:45040\n"
"ds_read_b64 v[48:49], v25 offset:44968\n"
"ds_read_b64 v[60:61], v25 offset:41080\n"
"ds_read_b64 v[62:63], v24 offset:41112\n"
"ds_read_b64 v[68:69], v25 offset:45048\n"
"ds_read_b64 v[70:71], v24 offset:45080\n"
"s_waitcnt lgkmcnt(12)\n"
"v_or_b32 v90, s33, v90\n"
"s_mov_b32 s17, 0x100000\n"
"v_cmp_eq_u32 vcc, 0, v90\n"
"s_cbranch_vccz .Lok_286\n"
".Ltp_285:\n"
"ds_read_b32 v90, v14 offset:432\n"
"s_mov_b64 exec, 0xffff\n"
"ds_read_b64 v[46:47], v24 offset:44960\n"
"ds_read_b64 v[50:51], v24 offset:45000\n"
"s_mov_b64 exec, -1\n"
"ds_read_b64 v[44:45], v25 offset:44928\n"
"ds_read_b64 v[56:57], v25 offset:41040\n"
"ds_read_b64 v[58:59], v24 offset:41072\n"
"ds_read_b64 v[64:65], v25 offset:45008\n"
"ds_read_b64 v[66:67], v24 offset:45040\n"
"ds_read_b64 v[48:49], v25 offset:44968\n"
"ds_read_b64 v[60:61], v25 offset:41080\n"
"ds_read_b64 v[62:63], v24 offset:41112\n"
"ds_read_b64 v[68:69], v25 offset:45048\n"
"ds_read_b64 v[70:71], v24 offset:45080\n"
"s_sub_u32 s17, s17, 1\n"
"s_cbranch_scc1 .Lok_286\n"
"s_waitcnt lgkmcnt(12)\n"
"v_cmp_eq_u32 vcc, 0, v90\n"
"s_cbranch_vccnz .Ltp_285\n"
"s_waitcnt lgkmcnt(0)\n"
".Lok_286:\n"
"ds_read_b64_tr_b16 v[28:29], v26 offset:40960\n"
"ds_read_b64_tr_b16 v[30:31], v26 offset:44928\n"
"ds_read_b64_tr_b16 v[32:33], v26 offset:41000\n"
"s_waitcnt lgkmcnt(10)\n"
"v_mfma_f32_16x16x32_bf16 v[56:59], v[56:59], v[44:47], 0\n"
"s_waitcnt lgkmcnt(8)\n"
"v_mfma_f32_16x16x32_bf16 v[64:67], v[64:67], v[44:47], 0\n"
"ds_read_b64_tr_b16 v[34:35], v26 offset:44968\n"
"ds_read_b64_tr_b16 v[36:37], v26 offset:40992\n"
"ds_read_b64_tr_b16 v[38:39], v26 offset:44960\n"
"s_waitcnt lgkmcnt(8)\n"
"v_mfma_f32_16x16x32_bf16 v[60:63], v[60:63], v[48:51], 0\n"
"s_waitcnt lgkmcnt(6)\n"
"v_mfma_f32_16x16x32_bf16 v[68:71], v[68:71], v[48:51], 0\n"
"ds_read_b64_tr_b16 v[40:41], v26 offset:41032\n"
"ds_read_b64_tr_b16 v[42:43], v26 offset:45000\n"
"v_mov_b32 v84, 0x3f803f80\n"
"v_mov_b32 v85, 0x3f803f80\n"
"v_mov_b32 v86, 0x3f803f80\n"
"v_mov_b32 v87, 0x3f803f80\n"
"v_cndmask_b32 v76, v56, v57, s[34:35]\n"
"v_cndmask_b32 v72, v64, v65, s[34:35]\n"
"v_cndmask_b32 v76, v76, v72, s[36:37]\n"
"v_cndmask_b32 v77, v58, v59, s[34:35]\n"
"v_cndmask_b32 v72, v66, v67, s[34:35]\n"
"v_cndmask_b32 v77, v77, v72, s[38:39]\n"
"v_max_f32 v52, v76, v77\n"
"v_max_f32 v54, v76, v77\n"
"v_cndmask_b32 v78, v60, v61, s[34:35]\n"
"v_cndmask_b32 v72, v68, v69, s[34:35]\n"
"v_cndmask_b32 v78, v78, v72, s[36:37]\n"
"v_cndmask_b32 v79, v62, v63, s[34:35]\n"
"v_cndmask_b32 v72, v70, v71, s[34:35]\n"
"v_cndmask_b32 v79, v79, v72, s[38:39]\n"
"v_max_f32 v53, v78, v79\n"
"v_max_f32 v55, v78, v79\n"
"v_permlane16_swap_b32 v52, v54\n"
"s_nop 0\n"
"v_permlane16_swap_b32 v53, v55\n"
"v_max_f32 v72, v52, v54\n"
"v_max_f32 v52, v52, v54\n"
"v_max_f32 v73, v53, v55\n"
"v_max_f32 v53, v53, v55\n"
"v_permlane32_swap_b32 v52, v72\n"
"s_nop 0\n"
"v_permlane32_swap_b32 v53, v73\n"
"v_max_f32 v52, v52, v72\n"
"v_max_f32 v53, v53, v73\n"
"v_mul_f32 v52, 0xbea52b5b, v52\n"
"v_mul_f32 v53, 0xbea52b5b, v53\n"
"v_fmamk_f32 v76, v76, 0x3ea52b5b, v52\n"
"v_fmamk_f32 v77, v77, 0x3ea52b5b, v52\n"
"v_fmamk_f32 v78, v78, 0x3ea52b5b, v53\n"
"v_fmamk_f32 v79, v79, 0x3ea52b5b, v53\n"
"v_exp_f32 v76, v76\n"
"v_exp_f32 v77, v77\n"
"v_exp_f32 v78, v78\n"
"v_exp_f32 v79, v79\n"
"v_cvt_pk_bf16_f32 v52, v76, v77\n"
"v_cvt_pk_bf16_f32 v53, v78, v79\n"
"v_perm_b32 v44, v52, v52, v80\n"
"v_perm_b32 v45, v52, v52, v81\n"
"v_perm_b32 v46, v52, v52, v82\n"
"v_perm_b32 v47, v52, v52, v83\n"
"v_perm_b32 v48, v53, v53, v80\n"
"v_perm_b32 v49, v53, v53, v81\n"
"v_perm_b32 v50, v53, v53, v82\n"
"v_perm_b32 v51, v53, v53, v83\n"
"v_mfma_f32_16x16x32_bf16 v[56:59], v[84:87], v[44:47], 0\n"
"s_nop 0\n"
"v_mfma_f32_16x16x32_bf16 v[60:63], v[84:87], v[48:51], 0\n"
"s_waitcnt lgkmcnt(6)\n"
"v_mfma_f32_16x16x32_bf16 v[28:31], v[28:31], v[44:47], 0\n"
"s_waitcnt lgkmcnt(4)\n"
"v_mfma_f32_16x16x32_bf16 v[32:35], v[32:35], v[48:51], 0\n"
"s_waitcnt lgkmcnt(2)\n"
"v_mfma_f32_16x16x32_bf16 v[36:39], v[36:39], v[44:47], 0\n"
"s_waitcnt lgkmcnt(0)\n"
"v_mfma_f32_16x16x32_bf16 v[40:43], v[40:43], v[48:51], 0\n"
"v_cndmask_b32 v88, 2.0, v1, s[40:41]\n"
"v_rcp_f32 v52, v56\n"
"v_rcp_f32 v53, v60\n"
"s_add_u32 s31, s30, 0xba0000\n"
"v_mul_f32 v28, v52, v28\n"
"v_mul_f32 v29, v52, v29\n"
"v_mul_f32 v30, v52, v30\n"
"v_mul_f32 v31, v52, v31\n"
"v_mul_f32 v32, v53, v32\n"
"v_mul_f32 v33, v53, v33\n"
"v_mul_f32 v34, v53, v34\n"
"v_mul_f32 v35, v53, v35\n"
"v_cvt_pk_bf16_f32 v28, v28, v29\n"
"v_cvt_pk_bf16_f32 v29, v30, v31\n"
"v_cvt_pk_bf16_f32 v30, v32, v33\n"
"v_cvt_pk_bf16_f32 v31, v34, v35\n"
"v_min_u32 v28, -2, v28\n"
"v_min_u32 v29, -2, v29\n"
"v_min_u32 v30, -2, v30\n"
"v_min_u32 v31, -2, v31\n"
"buffer_store_dwordx4 v[28:31], v1, s[4:7], s31 offen offset:0 sc1\n"
"v_mul_f32 v36, v52, v36\n"
"v_mul_f32 v37, v52, v37\n"
"v_mul_f32 v38, v52, v38\n"
"v_mul_f32 v39, v52, v39\n"
"v_mul_f32 v40, v53, v40\n"
"v_mul_f32 v41, v53, v41\n"
"v_mul_f32 v42, v53, v42\n"
"v_mul_f32 v43, v53, v43\n"
"v_cvt_pk_bf16_f32 v36, v36, v37\n"
"v_cvt_pk_bf16_f32 v37, v38, v39\n"
"v_cvt_pk_bf16_f32 v38, v40, v41\n"
"v_cvt_pk_bf16_f32 v39, v42, v43\n"
"v_min_u32 v36, -2, v36\n"
"v_min_u32 v37, -2, v37\n"
"v_min_u32 v38, -2, v38\n"
"v_min_u32 v39, -2, v39\n"
"buffer_store_dwordx4 v[36:39], v88, s[4:7], s31 offen offset:1024 sc1\n"
"ds_write_b32 v13, v13 offset:480\n"
"s_nop 2\n"
"s_cmp_lt_i32 s45, 0\n"
"s_cbranch_scc1 .Lnc_287\n"
"s_setprio 3\n"
"s_lshl_b32 s79, s3, 10\n"
"v_lshl_add_u32 v34, v0, 4, s79\n"
"v_lshrrev_b32 v59, 7, v34\n"
"v_and_b32 v58, 0x70, v34\n"
"s_mov_b32 s79, 0x5000\n"
"v_mad_i32_i24 v33, v59, s79, v58\n"
"s_mov_b32 s17, 0x100000\n"
".Ltp_288:\n"
"s_sleep 1\n"
"ds_read_b32 v42, v14 offset:432\n"
"s_sub_u32 s17, s17, 1\n"
"s_cbranch_scc1 .Lou_289\n"
"s_waitcnt lgkmcnt(0)\n"
"v_cmp_eq_u32 vcc, 0, v42\n"
"s_cbranch_vccnz .Ltp_288\n"
".Lou_289:\n"
"s_waitcnt lgkmcnt(0)\n"
"s_mov_b32 s17, 0x100000\n"
".Ltp_290:\n"
"s_sleep 1\n"
"ds_read_b32 v42, v14 offset:496\n"
"s_sub_u32 s17, s17, 1\n"
"s_cbranch_scc1 .Lou_291\n"
"s_waitcnt lgkmcnt(0)\n"
"v_cmp_eq_u32 vcc, 0, v42\n"
"s_cbranch_vccnz .Ltp_290\n"
".Lou_291:\n"
"s_waitcnt lgkmcnt(0)\n"
"s_lshl_b32 s79, s45, 8\n"
"s_add_u32 s46, s79, 0x1128000\n"
"s_add_u32 s47, s46, 0x140000\n"
"s_and_b32 s79, s2, 7\n"
"s_lshl_b32 s79, s79, 14\n"
"s_add_u32 s48, s79, 0x1668000\n"
"s_add_u32 s49, s48, 0x1000\n"
"v_and_b32 v94, 0xffffff80, v33\n"
"v_add_u32 v94, v94, v33\n"
"buffer_load_dwordx4 v[78:81], v94, s[4:7], s46 offen offset:0 sc1\n"
"buffer_load_dwordx4 v[82:85], v94, s[4:7], s46 offen offset:128 sc1\n"
"buffer_load_dwordx4 v[86:89], v94, s[4:7], s47 offen offset:0 sc1\n"
"buffer_load_dwordx4 v[90:93], v94, s[4:7], s47 offen offset:128 sc1\n"
"buffer_load_dwordx4 v[70:73], v34, s[4:7], s48 offen sc1\n"
"buffer_load_dwordx4 v[74:77], v34, s[4:7], s49 offen sc1\n"
"v_lshrrev_b32 v44, 4, v0\n"
"v_mov_b32 v59, 0xffff0000\n"
"v_mov_b32 v58, 0xffff\n"
"s_mov_b32 vcc_lo, 0xaaaaaaaa\n"
"s_mov_b32 vcc_hi, 0xaaaaaaaa\n"
"v_cndmask_b32 v45, v58, v59, vcc\n"
"v_and_b32 v59, 15, v0\n"
"v_lshlrev_b32 v46, 5, v59\n"
"v_and_b32 v59, 15, v0\n"
"v_lshrrev_b32 v47, 2, v59\n"
"s_lshl_b32 s79, s3, 10\n"
"v_lshl_add_u32 v48, v0, 4, s79\n"
"v_lshrrev_b32 v49, 2, v48\n"
"v_lshrrev_b32 v50, 6, v48\n"
"v_lshlrev_b32 v59, 3, v44\n"
"v_and_b32 v58, 15, v0\n"
"v_sub_u32 v59, v59, v58\n"
"v_add_u32 v51, 0x3ef, v59\n"
"v_and_b32 v59, 24, v50\n"
"v_xor_b32 v59, v49, v59\n"
"v_add_u32 v36, 0xa000, v59\n"
"s_lshl_b32 s79, s3, 8\n"
"s_add_u32 s79, s79, 0xa000\n"
"v_and_b32 v59, 15, v0\n"
"v_xor_b32 v59, s3, v59\n"
"v_and_b32 v59, 1, v59\n"
"v_lshrrev_b32 v58, 1, v44\n"
"v_xor_b32 v58, v47, v58\n"
"v_and_b32 v58, 1, v58\n"
"v_lshl_add_u32 v59, v59, 1, v58\n"
"v_and_b32 v58, 15, v0\n"
"v_and_b32 v58, 2, v58\n"
"v_and_b32 v57, 15, v0\n"
"v_lshrrev_b32 v57, 3, v57\n"
"v_lshlrev_b32 v57, 7, v57\n"
"v_lshl_add_u32 v57, v47, 11, v57\n"
"v_lshl_add_u32 v57, v44, 6, v57\n"
"v_lshl_add_u32 v58, v58, 4, v57\n"
"v_lshl_add_u32 v59, v59, 3, v58\n"
"v_add_u32 v31, s79, v59\n"
"s_mul_i32 s79, s3, 0x2100\n"
"s_add_u32 s79, s79, 0x13840\n"
"v_lshrrev_b32 v59, 2, v51\n"
"v_and_b32 v58, 3, v51\n"
"v_mul_i32_i24 v58, 0x840, v58\n"
"v_lshl_add_u32 v59, v59, 3, v58\n"
"v_add_u32 v28, s79, v59\n"
"s_mul_i32 s79, s3, 0x1200\n"
"s_add_u32 s79, s79, 0x1c420\n"
"v_lshl_add_u32 v59, v44, 4, v46\n"
"v_add_u32 v29, s79, v59\n"
"s_lshl_b32 s79, s3, 11\n"
"s_lshr_b32 s78, s3, 1\n"
"s_lshl_b32 s78, s78, 7\n"
"s_add_u32 s79, s79, s78\n"
"s_add_u32 s79, s79, 0xa000\n"
"s_and_b32 s78, s3, 1\n"
"v_xor_b32 v59, v44, v47\n"
"v_xor_b32 v59, s78, v59\n"
"v_and_b32 v59, 3, v59\n"
"v_lshl_add_u32 v59, v59, 3, v46\n"
"v_add_u32 v30, s79, v59\n"
"v_xor_b32 v32, 8, v31\n"
"v_add_u32 v35, 0x1c600, v49\n"
"v_xor_b32 v37, 8, v36\n"
"s_mov_b32 vcc_lo, 0x300003\n"
"s_mov_b32 vcc_hi, 0x30000300\n"
"v_cndmask_b32 v38, 0, v45, vcc\n"
"s_mov_b32 vcc_lo, 0xc0000c\n"
"s_mov_b32 vcc_hi, 0xc0000c00\n"
"v_cndmask_b32 v39, 0, v45, vcc\n"
"s_lshl_b32 s79, s3, 2\n"
"s_add_u32 s79, s79, 0x26800\n"
"v_mov_b32 v40, s79\n"
"v_and_b32 v59, 48, v48\n"
"v_mul_i32_i24 v59, 0x50, v59\n"
"s_mov_b32 s79, 0x2800\n"
"v_mad_i32_i24 v41, v50, s79, v59\n"
"ds_read_b32 v40, v40\n"
"s_waitcnt lgkmcnt(0)\n"
"v_sub_u32 v44, 0x7f000000, v40\n"
"v_lshrrev_b32 v45, 16, v44\n"
"v_or_b32 v44, v44, v45\n"
"v_and_b32 v38, v44, v38\n"
"v_and_b32 v39, v44, v39\n"
"ds_read_b64 v[96:97], v28 offset:1984\n"
"ds_read_b64 v[98:99], v28 offset:1992\n"
"ds_read_b64 v[100:101], v28 offset:1920\n"
"ds_read_b64 v[102:103], v28 offset:1928\n"
"ds_read_b64 v[104:105], v28 offset:1856\n"
"ds_read_b64 v[106:107], v28 offset:1864\n"
"ds_read_b64 v[108:109], v28 offset:1792\n"
"ds_read_b64 v[110:111], v28 offset:1800\n"
"ds_read_b64 v[112:113], v28 offset:1728\n"
"ds_read_b64 v[114:115], v28 offset:1736\n"
"ds_read_b64 v[116:117], v28 offset:1664\n"
"ds_read_b64 v[118:119], v28 offset:1672\n"
"ds_read_b64 v[120:121], v28 offset:1600\n"
"ds_read_b64 v[122:123], v28 offset:1608\n"
"ds_read_b64 v[124:125], v28 offset:1536\n"
"ds_read_b64 v[126:127], v28 offset:1544\n"
"ds_read_b64 v[128:129], v28 offset:1472\n"
"ds_read_b64 v[130:131], v28 offset:1480\n"
"ds_read_b64 v[132:133], v28 offset:1408\n"
"ds_read_b64 v[134:135], v28 offset:1416\n"
"ds_read_b64 v[136:137], v28 offset:1344\n"
"ds_read_b64 v[138:139], v28 offset:1352\n"
"ds_read_b64 v[140:141], v28 offset:1280\n"
"ds_read_b64 v[142:143], v28 offset:1288\n"
"ds_read_b64 v[144:145], v28 offset:1216\n"
"ds_read_b64 v[146:147], v28 offset:1224\n"
"ds_read_b64 v[148:149], v28 offset:1152\n"
"ds_read_b64 v[150:151], v28 offset:1160\n"
"ds_read_b64 v[152:153], v28 offset:1088\n"
"ds_read_b64 v[154:155], v28 offset:1096\n"
"ds_read_b64 v[156:157], v28 offset:1024\n"
"ds_read_b64 v[158:159], v28 offset:1032\n"
"ds_read_b64 v[160:161], v28 offset:960\n"
"ds_read_b64 v[162:163], v28 offset:968\n"
"ds_read_b64 v[164:165], v28 offset:896\n"
"ds_read_b64 v[166:167], v28 offset:904\n"
"ds_read_b64 v[168:169], v28 offset:832\n"
"ds_read_b64 v[170:171], v28 offset:840\n"
"ds_read_b64 v[172:173], v28 offset:768\n"
"ds_read_b64 v[174:175], v28 offset:776\n"
"ds_read_b64 v[176:177], v28 offset:704\n"
"ds_read_b64 v[178:179], v28 offset:712\n"
"ds_read_b64 v[180:181], v28 offset:640\n"
"ds_read_b64 v[182:183], v28 offset:648\n"
"ds_read_b64 v[184:185], v28 offset:576\n"
"ds_read_b64 v[186:187], v28 offset:584\n"
"ds_read_b64 v[188:189], v28 offset:512\n"
"ds_read_b64 v[190:191], v28 offset:520\n"
"ds_read_b64 v[192:193], v28 offset:448\n"
"ds_read_b64 v[194:195], v28 offset:456\n"
"ds_read_b64 v[196:197], v28 offset:384\n"
"ds_read_b64 v[198:199], v28 offset:392\n"
"ds_read_b64 v[200:201], v28 offset:320\n"
"ds_read_b64 v[202:203], v28 offset:328\n"
"ds_read_b64 v[204:205], v28 offset:256\n"
"ds_read_b64 v[206:207], v28 offset:264\n"
"ds_read_b64 v[208:209], v28 offset:192\n"
"ds_read_b64 v[210:211], v28 offset:200\n"
"ds_read_b64 v[212:213], v28 offset:128\n"
"ds_read_b64 v[214:215], v28 offset:136\n"
"ds_read_b64 v[216:217], v28 offset:64\n"
"ds_read_b64 v[218:219], v28 offset:72\n"
"ds_read_b64 v[220:221], v28 offset:0\n"
"ds_read_b64 v[222:223], v28 offset:8\n"
"s_mov_b32 s52, 0x05040100\n"
"s_mov_b32 s53, 0x07060302\n"
"s_mov_b32 s17, 32768\n"
".Ltp_292:\n"
"s_waitcnt vmcnt(0)\n"
"s_sub_u32 s17, s17, 1\n"
"s_cbranch_scc1 .Lou_293\n"
"v_bitop3_b32 v54, v79, v81, s16 bitop3:0x7e\n"
"v_bitop3_b32 v55, v83, v85, s16 bitop3:0x7e\n"
"v_bitop3_b32 v56, v87, v89, s16 bitop3:0x7e\n"
"v_bitop3_b32 v57, v91, v93, s16 bitop3:0x7e\n"
"v_or3_b32 v58, v54, v55, v56\n"
"v_or_b32 v58, v58, v57\n"
"v_cmp_ne_u32 vcc, 0, v58\n"
"s_cbranch_vccz .Lou_293\n"
"s_sleep 2\n"
"v_cmp_ne_u32 vcc, 0, v54\n"
"s_cbranch_vccz .Lsk_294\n"
"s_mov_b64 exec, vcc\n"
"buffer_load_dwordx4 v[78:81], v94, s[4:7], s46 offen offset:0 sc1\n"
"s_mov_b64 exec, -1\n"
".Lsk_294:\n"
"v_cmp_ne_u32 vcc, 0, v55\n"
"s_cbranch_vccz .Lsk_295\n"
"s_mov_b64 exec, vcc\n"
"buffer_load_dwordx4 v[82:85], v94, s[4:7], s46 offen offset:128 sc1\n"
"s_mov_b64 exec, -1\n"
".Lsk_295:\n"
"v_cmp_ne_u32 vcc, 0, v56\n"
"s_cbranch_vccz .Lsk_296\n"
"s_mov_b64 exec, vcc\n"
"buffer_load_dwordx4 v[86:89], v94, s[4:7], s47 offen offset:0 sc1\n"
"s_mov_b64 exec, -1\n"
".Lsk_296:\n"
"v_cmp_ne_u32 vcc, 0, v57\n"
"s_cbranch_vccz .Lsk_297\n"
"s_mov_b64 exec, vcc\n"
"buffer_load_dwordx4 v[90:93], v94, s[4:7], s47 offen offset:128 sc1\n"
"s_mov_b64 exec, -1\n"
".Lsk_297:\n"
"s_branch .Ltp_292\n"
".Lou_293:\n"
"s_waitcnt vmcnt(0)\n"
"v_perm_b32 v54, v82, v78, s52\n"
"v_perm_b32 v55, v82, v78, s53\n"
"v_perm_b32 v56, v84, v80, s52\n"
"v_perm_b32 v57, v84, v80, s53\n"
"ds_write_b32 v36, v54 offset:0\n"
"ds_write_b32 v37, v55 offset:2048\n"
"ds_write_b32 v36, v56 offset:4224\n"
"ds_write_b32 v37, v57 offset:6272\n"
"v_perm_b32 v54, v90, v86, s52\n"
"v_perm_b32 v55, v90, v86, s53\n"
"v_perm_b32 v56, v92, v88, s52\n"
"v_perm_b32 v57, v92, v88, s53\n"
"ds_write_b32 v36, v54 offset:1024\n"
"ds_write_b32 v37, v55 offset:3072\n"
"ds_write_b32 v36, v56 offset:5248\n"
"ds_write_b32 v37, v57 offset:7296\n"
"v_lshlrev_b32 v54, 16, v78\n"
"v_lshlrev_b32 v55, 16, v82\n"
"v_and_b32 v56, 0xffff0000, v78\n"
"v_and_b32 v57, 0xffff0000, v82\n"
"v_lshlrev_b32 v58, 16, v80\n"
"v_lshlrev_b32 v59, 16, v84\n"
"v_and_b32 v60, 0xffff0000, v80\n"
"v_and_b32 v61, 0xffff0000, v84\n"
"v_lshlrev_b32 v62, 16, v86\n"
"v_lshlrev_b32 v63, 16, v90\n"
"v_and_b32 v64, 0xffff0000, v86\n"
"v_and_b32 v65, 0xffff0000, v90\n"
"v_lshlrev_b32 v66, 16, v88\n"
"v_lshlrev_b32 v67, 16, v92\n"
"v_and_b32 v68, 0xffff0000, v88\n"
"v_and_b32 v69, 0xffff0000, v92\n"
"s_mov_b32 s17, 32768\n"
".Ltp_298:\n"
"s_waitcnt vmcnt(0)\n"
"s_sub_u32 s17, s17, 1\n"
"s_cbranch_scc1 .Lou_299\n"
"v_max_u32 v78, v70, v71\n"
"v_max3_u32 v78, v72, v73, v78\n"
"v_max3_u32 v78, v74, v75, v78\n"
"v_max3_u32 v78, v76, v77, v78\n"
"v_cmp_eq_u32 vcc, -1, v78\n"
"s_cbranch_vccz .Lou_299\n"
"v_max_u32 v78, v72, v73\n"
"v_max3_u32 v78, v70, v71, v78\n"
"v_cmp_eq_u32 vcc, -1, v78\n"
"s_cbranch_vccz .Lsk_300\n"
"s_mov_b64 exec, vcc\n"
"buffer_load_dwordx4 v[70:73], v34, s[4:7], s48 offen sc1\n"
"s_mov_b64 exec, -1\n"
".Lsk_300:\n"
"v_max_u32 v78, v76, v77\n"
"v_max3_u32 v78, v74, v75, v78\n"
"v_cmp_eq_u32 vcc, -1, v78\n"
"s_cbranch_vccz .Lsk_301\n"
"s_mov_b64 exec, vcc\n"
"buffer_load_dwordx4 v[74:77], v34, s[4:7], s49 offen sc1\n"
"s_mov_b64 exec, -1\n"
".Lsk_301:\n"
"s_branch .Ltp_298\n"
".Lou_299:\n"
"s_waitcnt vmcnt(0)\n"
"v_fma_f32 v54, v54, v71, v70\n"
"v_fma_f32 v55, v55, v73, v72\n"
"v_cvt_pk_f16_f32 v54, v54, v55\n"
"v_fma_f32 v56, v56, v71, v70\n"
"v_fma_f32 v57, v57, v73, v72\n"
"v_cvt_pk_f16_f32 v56, v56, v57\n"
"v_fma_f32 v58, v58, v71, v70\n"
"v_fma_f32 v59, v59, v73, v72\n"
"v_cvt_pk_f16_f32 v58, v58, v59\n"
"v_fma_f32 v60, v60, v71, v70\n"
"v_fma_f32 v61, v61, v73, v72\n"
"v_cvt_pk_f16_f32 v60, v60, v61\n"
"ds_write2st64_b32 v35, v54, v56 offset0:0 offset1:18\n"
"ds_write2st64_b32 v35, v58, v60 offset0:36 offset1:54\n"
"v_fma_f32 v62, v62, v75, v74\n"
"v_fma_f32 v63, v63, v77, v76\n"
"v_cvt_pk_f16_f32 v62, v62, v63\n"
"v_fma_f32 v64, v64, v75, v74\n"
"v_fma_f32 v65, v65, v77, v76\n"
"v_cvt_pk_f16_f32 v64, v64, v65\n"
"v_fma_f32 v66, v66, v75, v74\n"
"v_fma_f32 v67, v67, v77, v76\n"
"v_cvt_pk_f16_f32 v66, v66, v67\n"
"v_fma_f32 v68, v68, v75, v74\n"
"v_fma_f32 v69, v69, v77, v76\n"
"v_cvt_pk_f16_f32 v68, v68, v69\n"
"ds_write2st64_b32 v35, v62, v64 offset0:4 offset1:22\n"
"ds_write2st64_b32 v35, v66, v68 offset0:40 offset1:58\n"
"ds_write_b32 v13, v13 offset:512\n"
"s_mov_b32 s17, 0x100000\n"
".Ltp_302:\n"
"ds_read_b32 v42, v14 offset:512\n"
"s_sub_u32 s17, s17, 1\n"
"s_cbranch_scc1 .Lou_303\n"
"s_waitcnt lgkmcnt(0)\n"
"v_cmp_eq_u32 vcc, 0, v42\n"
"s_cbranch_vccnz .Ltp_302\n"
".Lou_303:\n"
"s_waitcnt lgkmcnt(0)\n"
"s_setprio 0\n"
"s_mov_b32 s50, 0xfffefffe\n"
"s_lshl_b32 s46, s45, 3\n"
"s_add_u32 s47, s46, 0x1400\n"
"s_add_u32 s48, s46, 0xa0000\n"
"s_add_u32 s49, s46, 0xa1400\n"
"ds_read_b64 v[60:61], v30 offset:0\n"
"ds_read_b64 v[64:65], v30 offset:512\n"
"ds_read_b64 v[68:69], v30 offset:1024\n"
"ds_read_b64 v[72:73], v30 offset:1536\n"
"ds_read_b128 v[76:79], v29 offset:448\n"
"s_waitcnt lgkmcnt(1)\n"
"v_mfma_f32_16x16x16_bf16 v[44:47], v[38:39], v[60:61], 0\n"
"ds_read_b128 v[80:83], v29 offset:960\n"
"ds_read_b128 v[84:87], v29 offset:384\n"
"v_mfma_f32_16x16x16_bf16 v[48:51], v[38:39], v[64:65], 0\n"
"ds_read_b128 v[88:91], v29 offset:896\n"
"ds_read_b128 v[92:95], v29 offset:320\n"
"v_mfma_f32_16x16x16_bf16 v[52:55], v[38:39], v[68:69], 0\n"
"ds_read_b128 v[2:5], v29 offset:832\n"
"v_mfma_f32_16x16x16_bf16 v[56:59], v[38:39], v[72:73], 0\n"
"ds_read_b128 v[34:37], v29 offset:256\n"
"s_waitcnt lgkmcnt(4)\n"
"v_mfma_f32_16x16x32_f16 v[44:47], v[96:99], v[76:79], v[44:47]\n"
"ds_read_b128 v[60:63], v29 offset:768\n"
"v_mfma_f32_16x16x32_f16 v[48:51], v[128:131], v[76:79], v[48:51]\n"
"ds_read_b128 v[64:67], v29 offset:192\n"
"v_mfma_f32_16x16x32_f16 v[48:51], v[96:99], v[80:83], v[48:51]\n"
"ds_read_b128 v[68:71], v29 offset:704\n"
"v_mfma_f32_16x16x32_f16 v[44:47], v[100:103], v[84:87], v[44:47]\n"
"ds_read_b128 v[72:75], v29 offset:128\n"
"s_waitcnt lgkmcnt(6)\n"
"v_mfma_f32_16x16x32_f16 v[48:51], v[132:135], v[84:87], v[48:51]\n"
"v_mfma_f32_16x16x32_f16 v[48:51], v[100:103], v[88:91], v[48:51]\n"
"ds_read_b128 v[76:79], v29 offset:640\n"
"v_mfma_f32_16x16x32_f16 v[44:47], v[104:107], v[92:95], v[44:47]\n"
"ds_read_b128 v[80:83], v29 offset:64\n"
"v_mfma_f32_16x16x32_f16 v[48:51], v[136:139], v[92:95], v[48:51]\n"
"s_waitcnt lgkmcnt(5)\n"
"v_mfma_f32_16x16x32_f16 v[48:51], v[104:107], v[2:5], v[48:51]\n"
"ds_read_b128 v[84:87], v29 offset:576\n"
"v_mfma_f32_16x16x32_f16 v[44:47], v[108:111], v[34:37], v[44:47]\n"
"ds_read_b128 v[88:91], v29 offset:0\n"
"v_mfma_f32_16x16x32_f16 v[48:51], v[140:143], v[34:37], v[48:51]\n"
"v_mfma_f32_16x16x32_f16 v[48:51], v[108:111], v[60:63], v[48:51]\n"
"ds_read_b128 v[92:95], v29 offset:512\n"
"s_waitcnt lgkmcnt(5)\n"
"v_mfma_f32_16x16x32_f16 v[44:47], v[112:115], v[64:67], v[44:47]\n"
"ds_read_b128 v[2:5], v29 offset:448\n"
"v_mfma_f32_16x16x32_f16 v[48:51], v[144:147], v[64:67], v[48:51]\n"
"v_mfma_f32_16x16x32_f16 v[48:51], v[112:115], v[68:71], v[48:51]\n"
"ds_read_b128 v[34:37], v29 offset:960\n"
"v_mfma_f32_16x16x32_f16 v[44:47], v[116:119], v[72:75], v[44:47]\n"
"s_waitcnt lgkmcnt(5)\n"
"v_mfma_f32_16x16x32_f16 v[48:51], v[148:151], v[72:75], v[48:51]\n"
"ds_read_b128 v[60:63], v29 offset:1472\n"
"v_mfma_f32_16x16x32_f16 v[48:51], v[116:119], v[76:79], v[48:51]\n"
"v_mfma_f32_16x16x32_f16 v[44:47], v[120:123], v[80:83], v[44:47]\n"
"ds_read_b128 v[64:67], v29 offset:1984\n"
"v_mfma_f32_16x16x32_f16 v[48:51], v[152:155], v[80:83], v[48:51]\n"
"ds_read_b128 v[68:71], v29 offset:384\n"
"s_waitcnt lgkmcnt(5)\n"
"v_mfma_f32_16x16x32_f16 v[48:51], v[120:123], v[84:87], v[48:51]\n"
"v_mfma_f32_16x16x32_f16 v[44:47], v[124:127], v[88:91], v[44:47]\n"
"ds_read_b128 v[72:75], v29 offset:896\n"
"v_mfma_f32_16x16x32_f16 v[48:51], v[156:159], v[88:91], v[48:51]\n"
"v_mfma_f32_16x16x32_f16 v[48:51], v[124:127], v[92:95], v[48:51]\n"
"ds_read_b128 v[76:79], v29 offset:1408\n"
"s_waitcnt lgkmcnt(5)\n"
"v_mfma_f32_16x16x32_f16 v[52:55], v[160:163], v[2:5], v[52:55]\n"
"v_mfma_f32_16x16x32_f16 v[56:59], v[192:195], v[2:5], v[56:59]\n"
"ds_read_b128 v[80:83], v29 offset:1920\n"
"v_mfma_f32_16x16x32_f16 v[52:55], v[128:131], v[34:37], v[52:55]\n"
"ds_read_b128 v[84:87], v29 offset:320\n"
"v_mul_f32 v44, v40, v44\n"
"v_mul_f32 v45, v40, v45\n"
"v_mfma_f32_16x16x32_f16 v[56:59], v[160:163], v[34:37], v[56:59]\n"
"v_mul_f32 v46, v40, v46\n"
"v_mul_f32 v47, v40, v47\n"
"s_waitcnt lgkmcnt(4)\n"
"v_mfma_f32_16x16x32_f16 v[52:55], v[96:99], v[60:63], v[52:55]\n"
"ds_read_b128 v[88:91], v29 offset:832\n"
"v_cvt_pk_bf16_f32 v44, v44, v45\n"
"v_cvt_pk_bf16_f32 v45, v46, v47\n"
"v_mfma_f32_16x16x32_f16 v[56:59], v[128:131], v[60:63], v[56:59]\n"
"v_pk_min_u16 v44, v44, s50\n"
"v_pk_min_u16 v45, v45, s50\n"
"ds_write_b64 v30, v[44:45] offset:0\n"
"v_mfma_f32_16x16x32_f16 v[56:59], v[96:99], v[64:67], v[56:59]\n"
"ds_read_b128 v[92:95], v29 offset:1344\n"
"v_mul_f32 v48, v40, v48\n"
"v_mul_f32 v49, v40, v49\n"
"v_mfma_f32_16x16x32_f16 v[52:55], v[164:167], v[68:71], v[52:55]\n"
"v_mul_f32 v50, v40, v50\n"
"v_mul_f32 v51, v40, v51\n"
"s_waitcnt lgkmcnt(5)\n"
"v_mfma_f32_16x16x32_f16 v[56:59], v[196:199], v[68:71], v[56:59]\n"
"ds_read_b128 v[2:5], v29 offset:1856\n"
"v_cvt_pk_bf16_f32 v48, v48, v49\n"
"v_cvt_pk_bf16_f32 v49, v50, v51\n"
"v_mfma_f32_16x16x32_f16 v[52:55], v[132:135], v[72:75], v[52:55]\n"
"ds_read_b128 v[34:37], v29 offset:256\n"
"v_pk_min_u16 v48, v48, s50\n"
"v_pk_min_u16 v49, v49, s50\n"
"v_mfma_f32_16x16x32_f16 v[56:59], v[164:167], v[72:75], v[56:59]\n"
"ds_write_b64 v30, v[48:49] offset:512\n"
"v_mfma_f32_16x16x32_f16 v[52:55], v[100:103], v[76:79], v[52:55]\n"
"ds_read_b128 v[60:63], v29 offset:768\n"
"s_waitcnt lgkmcnt(7)\n"
"v_mfma_f32_16x16x32_f16 v[56:59], v[132:135], v[76:79], v[56:59]\n"
"ds_write_b32 v13, v13 offset:528\n"
"v_mfma_f32_16x16x32_f16 v[56:59], v[100:103], v[80:83], v[56:59]\n"
"ds_read_b128 v[64:67], v29 offset:1280\n"
"v_mfma_f32_16x16x32_f16 v[52:55], v[168:171], v[84:87], v[52:55]\n"
"v_mfma_f32_16x16x32_f16 v[56:59], v[200:203], v[84:87], v[56:59]\n"
"ds_read_b128 v[68:71], v29 offset:1792\n"
"s_waitcnt lgkmcnt(7)\n"
"v_mfma_f32_16x16x32_f16 v[52:55], v[136:139], v[88:91], v[52:55]\n"
"ds_read_b128 v[72:75], v29 offset:192\n"
"v_mfma_f32_16x16x32_f16 v[56:59], v[168:171], v[88:91], v[56:59]\n"
"v_mfma_f32_16x16x32_f16 v[52:55], v[104:107], v[92:95], v[52:55]\n"
"ds_read_b128 v[76:79], v29 offset:704\n"
"v_mfma_f32_16x16x32_f16 v[56:59], v[136:139], v[92:95], v[56:59]\n"
"s_waitcnt lgkmcnt(5)\n"
"v_mfma_f32_16x16x32_f16 v[56:59], v[104:107], v[2:5], v[56:59]\n"
"ds_read_b128 v[80:83], v29 offset:1216\n"
"v_mfma_f32_16x16x32_f16 v[52:55], v[172:175], v[34:37], v[52:55]\n"
"v_mfma_f32_16x16x32_f16 v[56:59], v[204:207], v[34:37], v[56:59]\n"
"ds_read_b128 v[84:87], v29 offset:1728\n"
"v_mfma_f32_16x16x32_f16 v[52:55], v[140:143], v[60:63], v[52:55]\n"
"ds_read_b128 v[88:91], v29 offset:128\n"
"s_waitcnt lgkmcnt(5)\n"
"v_mfma_f32_16x16x32_f16 v[56:59], v[172:175], v[60:63], v[56:59]\n"
"v_mfma_f32_16x16x32_f16 v[52:55], v[108:111], v[64:67], v[52:55]\n"
"ds_read_b128 v[92:95], v29 offset:640\n"
"v_mfma_f32_16x16x32_f16 v[56:59], v[140:143], v[64:67], v[56:59]\n"
"v_mfma_f32_16x16x32_f16 v[56:59], v[108:111], v[68:71], v[56:59]\n"
"ds_read_b128 v[2:5], v29 offset:1152\n"
"s_waitcnt lgkmcnt(5)\n"
"v_mfma_f32_16x16x32_f16 v[52:55], v[176:179], v[72:75], v[52:55]\n"
"v_mfma_f32_16x16x32_f16 v[56:59], v[208:211], v[72:75], v[56:59]\n"
"ds_read_b128 v[34:37], v29 offset:1664\n"
"v_mfma_f32_16x16x32_f16 v[52:55], v[144:147], v[76:79], v[52:55]\n"
"ds_read_b128 v[60:63], v29 offset:64\n"
"v_mfma_f32_16x16x32_f16 v[56:59], v[176:179], v[76:79], v[56:59]\n"
"s_waitcnt lgkmcnt(4)\n"
"v_mfma_f32_16x16x32_f16 v[52:55], v[112:115], v[80:83], v[52:55]\n"
"ds_read_b128 v[64:67], v29 offset:576\n"
"ds_read_b32 v42, v14 offset:528\n"
"v_mfma_f32_16x16x32_f16 v[56:59], v[144:147], v[80:83], v[56:59]\n"
"ds_read_b64_tr_b16 v[224:225], v31 offset:0\n"
"ds_read_b64_tr_b16 v[226:227], v32 offset:0\n"
"v_mfma_f32_16x16x32_f16 v[56:59], v[112:115], v[84:87], v[56:59]\n"
"ds_read_b128 v[68:71], v29 offset:1088\n"
"v_mfma_f32_16x16x32_f16 v[52:55], v[180:183], v[88:91], v[52:55]\n"
"s_waitcnt lgkmcnt(7)\n"
"v_mfma_f32_16x16x32_f16 v[56:59], v[212:215], v[88:91], v[56:59]\n"
"ds_read_b128 v[72:75], v29 offset:1600\n"
"v_mfma_f32_16x16x32_f16 v[52:55], v[148:151], v[92:95], v[52:55]\n"
"ds_read_b128 v[76:79], v29 offset:0\n"
"v_mfma_f32_16x16x32_f16 v[56:59], v[180:183], v[92:95], v[56:59]\n"
"ds_read_b128 v[80:83], v29 offset:512\n"
"v_mfma_f32_16x16x32_f16 v[52:55], v[116:119], v[2:5], v[52:55]\n"
"ds_read_b128 v[84:87], v29 offset:1024\n"
"s_waitcnt lgkmcnt(7)\n"
"v_cmp_eq_u32 vcc, 0, v42\n"
"v_mfma_f32_16x16x32_f16 v[56:59], v[148:151], v[2:5], v[56:59]\n"
"v_mfma_f32_16x16x32_f16 v[56:59], v[116:119], v[34:37], v[56:59]\n"
"s_cbranch_vccnz .Lsl_304\n"
".Lbk_305:\n"
"v_mfma_f32_16x16x32_f16 v[52:55], v[184:187], v[60:63], v[52:55]\n"
"s_waitcnt lgkmcnt(6)\n"
"buffer_store_dwordx2 v[224:225], v41, s[12:15], s46 offen\n"
"v_mfma_f32_16x16x32_f16 v[56:59], v[216:219], v[60:63], v[56:59]\n"
"ds_read_b128 v[88:91], v29 offset:1536\n"
"s_waitcnt lgkmcnt(5)\n"
"v_mfma_f32_16x16x32_f16 v[52:55], v[152:155], v[64:67], v[52:55]\n"
"v_mfma_f32_16x16x32_f16 v[56:59], v[184:187], v[64:67], v[56:59]\n"
"v_mfma_f32_16x16x32_f16 v[52:55], v[120:123], v[68:71], v[52:55]\n"
"v_mfma_f32_16x16x32_f16 v[56:59], v[152:155], v[68:71], v[56:59]\n"
"s_waitcnt lgkmcnt(1)\n"
"v_mfma_f32_16x16x32_f16 v[56:59], v[120:123], v[72:75], v[56:59]\n"
"v_mfma_f32_16x16x32_f16 v[52:55], v[188:191], v[76:79], v[52:55]\n"
"v_mfma_f32_16x16x32_f16 v[52:55], v[156:159], v[80:83], v[52:55]\n"
"v_mfma_f32_16x16x32_f16 v[52:55], v[124:127], v[84:87], v[52:55]\n"
"s_waitcnt lgkmcnt(0)\n"
"v_mfma_f32_16x16x32_f16 v[56:59], v[220:223], v[76:79], v[56:59]\n"
"v_mfma_f32_16x16x32_f16 v[56:59], v[188:191], v[80:83], v[56:59]\n"
"v_mfma_f32_16x16x32_f16 v[56:59], v[156:159], v[84:87], v[56:59]\n"
"buffer_store_dwordx2 v[226:227], v41, s[12:15], s47 offen\n"
"v_mul_f32 v52, v40, v52\n"
"v_mul_f32 v53, v40, v53\n"
"v_mfma_f32_16x16x32_f16 v[56:59], v[124:127], v[88:91], v[56:59]\n"
"v_mul_f32 v54, v40, v54\n"
"v_mul_f32 v55, v40, v55\n"
"v_cvt_pk_bf16_f32 v52, v52, v53\n"
"v_cvt_pk_bf16_f32 v53, v54, v55\n"
"v_pk_min_u16 v52, v52, s50\n"
"v_pk_min_u16 v53, v53, s50\n"
"ds_write_b64 v30, v[52:53] offset:1024\n"
"s_nop 0\n"
"v_mul_f32 v56, v40, v56\n"
"v_mul_f32 v57, v40, v57\n"
"v_mul_f32 v58, v40, v58\n"
"v_mul_f32 v59, v40, v59\n"
"v_cvt_pk_bf16_f32 v56, v56, v57\n"
"v_cvt_pk_bf16_f32 v57, v58, v59\n"
"v_pk_min_u16 v56, v56, s50\n"
"v_pk_min_u16 v57, v57, s50\n"
"ds_write_b64 v30, v[56:57] offset:1536\n"
"ds_write_b32 v13, v13 offset:544\n"
"s_mov_b32 s17, 0x100000\n"
".Ltp_308:\n"
"ds_read_b32 v42, v14 offset:544\n"
"s_sub_u32 s17, s17, 1\n"
"s_cbranch_scc1 .Lou_309\n"
"s_waitcnt lgkmcnt(0)\n"
"v_cmp_eq_u32 vcc, 0, v42\n"
"s_cbranch_vccnz .Ltp_308\n"
".Lou_309:\n"
"s_waitcnt lgkmcnt(0)\n"
"ds_read_b64_tr_b16 v[228:229], v31 offset:1024\n"
"ds_read_b64_tr_b16 v[230:231], v32 offset:1024\n"
"s_waitcnt lgkmcnt(1)\n"
"buffer_store_dwordx2 v[228:229], v41, s[12:15], s48 offen\n"
"s_mov_b32 s89, 18\n"
".Ldl_310:\n"
"s_sub_u32 s89, s89, 1\n"
"s_cbranch_scc1 .Lde_311\n"
"s_sleep 1\n"
"s_branch .Ldl_310\n"
".Lde_311:\n"
"s_waitcnt lgkmcnt(0)\n"
"buffer_store_dwordx2 v[230:231], v41, s[12:15], s49 offen\n"
"v_add_u32 v31, 0x16c00, v31\n"
"v_add_u32 v32, 0x16c00, v32\n"
"s_add_u32 s46, s46, 0x140000\n"
"s_add_u32 s47, s47, 0x140000\n"
"s_add_u32 s48, s48, 0x140000\n"
"s_add_u32 s49, s49, 0x140000\n"
"s_lshr_b32 s79, s3, 1\n"
"s_mul_i32 s79, s79, -16\n"
"v_add_u32 v76, s79, v14\n"
"s_mov_b32 s17, 0x100000\n"
".Ltp_312:\n"
"s_sleep 1\n"
"ds_read_b32 v42, v14 offset:592\n"
"s_sub_u32 s17, s17, 1\n"
"s_cbranch_scc1 .Lou_313\n"
"s_waitcnt lgkmcnt(0)\n"
"v_cmp_eq_u32 vcc, 0, v42\n"
"s_cbranch_vccnz .Ltp_312\n"
".Lou_313:\n"
"s_waitcnt lgkmcnt(0)\n"
"ds_read_b64_tr_b16 v[68:69], v31 offset:0\n"
"ds_read_b64_tr_b16 v[70:71], v32 offset:0\n"
"s_waitcnt lgkmcnt(1)\n"
"buffer_store_dwordx2 v[68:69], v41, s[12:15], s46 offen\n"
"s_waitcnt lgkmcnt(0)\n"
"buffer_store_dwordx2 v[70:71], v41, s[12:15], s47 offen\n"
"s_mov_b32 s17, 0x100000\n"
".Ltp_314:\n"
"s_sleep 1\n"
"ds_read_b32 v42, v76 offset:576\n"
"s_sub_u32 s17, s17, 1\n"
"s_cbranch_scc1 .Lou_315\n"
"s_waitcnt lgkmcnt(0)\n"
"v_cmp_eq_u32 vcc, 0, v42\n"
"s_cbranch_vccnz .Ltp_314\n"
".Lou_315:\n"
"s_waitcnt lgkmcnt(0)\n"
"ds_read_b64_tr_b16 v[72:73], v31 offset:1024\n"
"ds_read_b64_tr_b16 v[74:75], v32 offset:1024\n"
"s_waitcnt lgkmcnt(1)\n"
"buffer_store_dwordx2 v[72:73], v41, s[12:15], s48 offen\n"
"s_waitcnt lgkmcnt(0)\n"
"buffer_store_dwordx2 v[74:75], v41, s[12:15], s49 offen\n"
"s_setprio 1\n"
"s_nop 0\n"
".Lnc_287:\n"
"s_add_u32 s31, s2, s3\n"
"s_cmp_lg_u32 s31, 0\n"
"s_cbranch_scc1 .Lfin_316\n"
"v_mov_b32 v32, s16\n"
"s_mov_b64 exec, 1\n"
"buffer_store_dword v32, off, s[4:7], 0 sc1\n"
"s_mov_b64 exec, -1\n"
".Lfin_316:\n"
"s_endpgm\n"
"s_branch .L_xend\n"
".Lsl_7:\n"
"s_nop 7\n"
"s_mov_b32 s17, 0x100000\n"
".Ltp_9:\n"
"ds_read_b32 v90, v14 offset:0\n"
"s_sub_u32 s17, s17, 1\n"
"s_cbranch_scc1 .Lou_10\n"
"s_waitcnt lgkmcnt(0)\n"
"v_cmp_eq_u32 vcc, 0, v90\n"
"s_cbranch_vccnz .Ltp_9\n"
".Lou_10:\n"
"s_waitcnt lgkmcnt(0)\n"
"s_branch .Lbk_8\n"
".Lsl_18:\n"
"s_nop 7\n"
"s_mov_b32 s17, 0x100000\n"
".Ltp_20:\n"
"ds_read_b32 v90, v14 offset:48\n"
"s_sub_u32 s17, s17, 1\n"
"s_cbranch_scc1 .Lou_21\n"
"s_waitcnt lgkmcnt(0)\n"
"v_cmp_eq_u32 vcc, 0, v90\n"
"s_cbranch_vccnz .Ltp_20\n"
".Lou_21:\n"
"s_waitcnt lgkmcnt(0)\n"
"s_branch .Lbk_19\n"
".Lsl_22:\n"
"s_nop 7\n"
"s_mov_b32 s17, 0x100000\n"
".Ltp_24:\n"
"ds_read_b32 v90, v14 offset:64\n"
"s_sub_u32 s17, s17, 1\n"
"s_cbranch_scc1 .Lou_25\n"
"s_waitcnt lgkmcnt(0)\n"
"v_cmp_eq_u32 vcc, 0, v90\n"
"s_cbranch_vccnz .Ltp_24\n"
".Lou_25:\n"
"s_waitcnt lgkmcnt(0)\n"
"s_branch .Lbk_23\n"
".Lsl_26:\n"
"s_nop 7\n"
"s_mov_b32 s17, 0x100000\n"
".Ltp_28:\n"
"ds_read_b32 v90, v14 offset:16\n"
"s_sub_u32 s17, s17, 1\n"
"s_cbranch_scc1 .Lou_29\n"
"s_waitcnt lgkmcnt(0)\n"
"v_cmp_eq_u32 vcc, 0, v90\n"
"s_cbranch_vccnz .Ltp_28\n"
".Lou_29:\n"
"s_waitcnt lgkmcnt(0)\n"
"s_branch .Lbk_27\n"
".Lsl_30:\n"
"s_nop 7\n"
"s_mov_b32 s17, 0x100000\n"
".Ltp_32:\n"
"ds_read_b32 v90, v14 offset:80\n"
"s_sub_u32 s17, s17, 1\n"
"s_cbranch_scc1 .Lou_33\n"
"s_waitcnt lgkmcnt(0)\n"
"v_cmp_eq_u32 vcc, 0, v90\n"
"s_cbranch_vccnz .Ltp_32\n"
".Lou_33:\n"
"s_waitcnt lgkmcnt(0)\n"
"s_branch .Lbk_31\n"
".Lsl_38:\n"
"s_nop 7\n"
"s_mov_b32 s17, 0x100000\n"
".Ltp_40:\n"
"ds_read_b128 v[84:87], v11 offset:0\n"
"ds_read_b32 v91, v12 offset:0\n"
"s_sub_u32 s17, s17, 1\n"
"s_cbranch_scc1 .Lou_41\n"
"s_waitcnt lgkmcnt(0)\n"
"v_max3_u32 v88, v84, v85, v91\n"
"v_max3_u32 v88, v86, v87, v88\n"
"v_cmp_eq_u32 vcc, -1, v88\n"
"s_cbranch_vccnz .Ltp_40\n"
".Lou_41:\n"
"s_waitcnt lgkmcnt(0)\n"
"v_add_f32 v84, v84, v85\n"
"v_add_f32 v86, v86, v87\n"
"v_add_f32 v84, v84, v86\n"
"v_mul_f32 v89, 0x3acccccd, v91\n"
"v_fma_f32 v84, -v91, v89, v84\n"
"v_fmamk_f32 v84, v84, 0x3acccccd, v27\n"
"v_max_f32 v84, v84, v27\n"
"v_rsq_f32 v88, v84\n"
"s_nop 0\n"
"v_mul_f32 v89, -v89, v88\n"
"s_nop 1\n"
"s_branch .Lbk_39\n"
".Lsl_42:\n"
"s_nop 7\n"
"s_mov_b32 s17, 0x100000\n"
".Ltp_44:\n"
"ds_read_b128 v[84:87], v11 offset:320\n"
"ds_read_b32 v91, v12 offset:320\n"
"s_sub_u32 s17, s17, 1\n"
"s_cbranch_scc1 .Lou_45\n"
"s_waitcnt lgkmcnt(0)\n"
"v_max3_u32 v88, v84, v85, v91\n"
"v_max3_u32 v88, v86, v87, v88\n"
"v_cmp_eq_u32 vcc, -1, v88\n"
"s_cbranch_vccnz .Ltp_44\n"
".Lou_45:\n"
"s_waitcnt lgkmcnt(0)\n"
"v_add_f32 v84, v84, v85\n"
"v_add_f32 v86, v86, v87\n"
"v_add_f32 v84, v84, v86\n"
"v_mul_f32 v89, 0x3acccccd, v91\n"
"v_fma_f32 v84, -v91, v89, v84\n"
"v_fmamk_f32 v84, v84, 0x3acccccd, v27\n"
"v_max_f32 v84, v84, v27\n"
"v_rsq_f32 v88, v84\n"
"s_nop 0\n"
"v_mul_f32 v89, -v89, v88\n"
"s_nop 1\n"
"s_branch .Lbk_43\n"
".Lsl_46:\n"
"s_nop 7\n"
"s_mov_b32 s17, 0x100000\n"
".Ltp_48:\n"
"ds_read_b128 v[84:87], v11 offset:640\n"
"ds_read_b32 v91, v12 offset:640\n"
"s_sub_u32 s17, s17, 1\n"
"s_cbranch_scc1 .Lou_49\n"
"s_waitcnt lgkmcnt(0)\n"
"v_max3_u32 v88, v84, v85, v91\n"
"v_max3_u32 v88, v86, v87, v88\n"
"v_cmp_eq_u32 vcc, -1, v88\n"
"s_cbranch_vccnz .Ltp_48\n"
".Lou_49:\n"
"s_waitcnt lgkmcnt(0)\n"
"v_add_f32 v84, v84, v85\n"
"v_add_f32 v86, v86, v87\n"
"v_add_f32 v84, v84, v86\n"
"v_mul_f32 v89, 0x3acccccd, v91\n"
"v_fma_f32 v84, -v91, v89, v84\n"
"v_fmamk_f32 v84, v84, 0x3acccccd, v27\n"
"v_max_f32 v84, v84, v27\n"
"v_rsq_f32 v88, v84\n"
"s_nop 0\n"
"v_mul_f32 v89, -v89, v88\n"
"s_nop 1\n"
"s_branch .Lbk_47\n"
".Lsl_50:\n"
"s_nop 7\n"
"s_mov_b32 s17, 0x100000\n"
".Ltp_52:\n"
"ds_read_b128 v[84:87], v11 offset:960\n"
"ds_read_b32 v91, v12 offset:960\n"
"s_sub_u32 s17, s17, 1\n"
"s_cbranch_scc1 .Lou_53\n"
"s_waitcnt lgkmcnt(0)\n"
"v_max3_u32 v88, v84, v85, v91\n"
"v_max3_u32 v88, v86, v87, v88\n"
"v_cmp_eq_u32 vcc, -1, v88\n"
"s_cbranch_vccnz .Ltp_52\n"
".Lou_53:\n"
"s_waitcnt lgkmcnt(0)\n"
"v_add_f32 v84, v84, v85\n"
"v_add_f32 v86, v86, v87\n"
"v_add_f32 v84, v84, v86\n"
"v_mul_f32 v89, 0x3acccccd, v91\n"
"v_fma_f32 v84, -v91, v89, v84\n"
"v_fmamk_f32 v84, v84, 0x3acccccd, v27\n"
"v_max_f32 v84, v84, v27\n"
"v_rsq_f32 v88, v84\n"
"s_nop 0\n"
"v_mul_f32 v89, -v89, v88\n"
"s_nop 1\n"
"s_branch .Lbk_51\n"
".Lsl_54:\n"
"s_nop 7\n"
"s_mov_b32 s17, 0x100000\n"
".Ltp_56:\n"
"ds_read_b128 v[44:47], v11 offset:1280\n"
"ds_read_b128 v[48:51], v11 offset:1600\n"
"s_sub_u32 s17, s17, 1\n"
"s_cbranch_scc1 .Lou_57\n"
"s_waitcnt lgkmcnt(0)\n"
"v_max3_u32 v52, v44, v45, v51\n"
"v_max3_u32 v52, v46, v47, v52\n"
"v_max3_u32 v52, v48, v49, v52\n"
"v_max3_u32 v52, v50, v51, v52\n"
"v_cmp_eq_u32 vcc, -1, v52\n"
"s_cbranch_vccnz .Ltp_56\n"
".Lou_57:\n"
"s_waitcnt lgkmcnt(0)\n"
"v_add_f32 v44, v44, v45\n"
"v_add_f32 v46, v46, v47\n"
"v_add_f32 v48, v48, v49\n"
"v_add_f32 v50, v50, v51\n"
"v_add_f32 v48, v48, v50\n"
"v_add_f32 v44, v44, v46\n"
"v_mul_f32 v53, 0x3acccccd, v48\n"
"v_fma_f32 v44, -v48, v53, v44\n"
"v_fmamk_f32 v44, v44, 0x3acccccd, v27\n"
"v_max_f32 v44, v44, v27\n"
"v_rsq_f32 v52, v44\n"
"s_nop 0\n"
"v_mul_f32 v53, -v53, v52\n"
"s_nop 1\n"
"s_branch .Lbk_55\n"
".Lsl_50c:\n"
"s_nop 7\n"
"s_mov_b32 s17, 0x100000\n"
".Ltp_52c:\n"
"ds_read_b128 v[84:87], v11 offset:960\n"
"ds_read_b32 v91, v12 offset:960\n"
"s_sub_u32 s17, s17, 1\n"
"s_cbranch_scc1 .Lou_53c\n"
"s_waitcnt lgkmcnt(0)\n"
"v_max3_u32 v88, v84, v85, v91\n"
"v_max3_u32 v88, v86, v87, v88\n"
"v_cmp_eq_u32 vcc, -1, v88\n"
"s_cbranch_vccnz .Ltp_52c\n"
".Lou_53c:\n"
"s_waitcnt lgkmcnt(0)\n"
"v_add_f32 v84, v84, v85\n"
"v_add_f32 v86, v86, v87\n"
"v_add_f32 v84, v84, v86\n"
"v_mul_f32 v89, 0x3acccccd, v91\n"
"v_fma_f32 v84, -v91, v89, v84\n"
"v_fmamk_f32 v84, v84, 0x3acccccd, v27\n"
"v_max_f32 v84, v84, v27\n"
"v_rsq_f32 v88, v84\n"
"s_nop 0\n"
"v_mul_f32 v89, -v89, v88\n"
"s_nop 1\n"
"s_branch .Lbk_51c\n"
".Lsl_54c:\n"
"s_nop 7\n"
"s_mov_b32 s17, 0x100000\n"
".Ltp_56c:\n"
"ds_read_b128 v[44:47], v11 offset:1280\n"
"ds_read_b128 v[48:51], v11 offset:1600\n"
"s_sub_u32 s17, s17, 1\n"
"s_cbranch_scc1 .Lou_57c\n"
"s_waitcnt lgkmcnt(0)\n"
"v_max3_u32 v52, v44, v45, v51\n"
"v_max3_u32 v52, v46, v47, v52\n"
"v_max3_u32 v52, v48, v49, v52\n"
"v_max3_u32 v52, v50, v51, v52\n"
"v_cmp_eq_u32 vcc, -1, v52\n"
"s_cbranch_vccnz .Ltp_56c\n"
".Lou_57c:\n"
"s_waitcnt lgkmcnt(0)\n"
"v_add_f32 v44, v44, v45\n"
"v_add_f32 v46, v46, v47\n"
"v_add_f32 v48, v48, v49\n"
"v_add_f32 v50, v50, v51\n"
"v_add_f32 v48, v48, v50\n"
"v_add_f32 v44, v44, v46\n"
"v_mul_f32 v53, 0x3acccccd, v48\n"
"v_fma_f32 v44, -v48, v53, v44\n"
"v_fmamk_f32 v44, v44, 0x3acccccd, v27\n"
"v_max_f32 v44, v44, v27\n"
"v_rsq_f32 v52, v44\n"
"s_nop 0\n"
"v_mul_f32 v53, -v53, v52\n"
"s_nop 1\n"
"s_branch .Lbk_55c\n"
".Lsl_128:\n"
"s_nop 7\n"
"s_mov_b32 s17, 0x100000\n"
".Ltp_130:\n"
"ds_read_b32 v90, v14 offset:288\n"
"s_sub_u32 s17, s17, 1\n"
"s_cbranch_scc1 .Lou_131\n"
"s_waitcnt lgkmcnt(0)\n"
"v_cmp_eq_u32 vcc, 0, v90\n"
"s_cbranch_vccnz .Ltp_130\n"
".Lou_131:\n"
"s_waitcnt lgkmcnt(0)\n"
"s_branch .Lbk_129\n"
".Lsl_132:\n"
"s_nop 7\n"
"s_mov_b32 s17, 0x100000\n"
".Ltp_134:\n"
"ds_read_b128 v[84:87], v11 offset:1856\n"
"ds_read_b32 v91, v12 offset:1856\n"
"s_sub_u32 s17, s17, 1\n"
"s_cbranch_scc1 .Lou_135\n"
"s_waitcnt lgkmcnt(0)\n"
"v_max3_u32 v88, v84, v85, v91\n"
"v_max3_u32 v88, v86, v87, v88\n"
"v_cmp_eq_u32 vcc, -1, v88\n"
"s_cbranch_vccnz .Ltp_134\n"
".Lou_135:\n"
"s_waitcnt lgkmcnt(0)\n"
"v_add_f32 v84, v84, v85\n"
"v_add_f32 v86, v86, v87\n"
"v_add_f32 v84, v84, v86\n"
"v_mul_f32 v89, 0x3acccccd, v91\n"
"v_fma_f32 v84, -v91, v89, v84\n"
"v_fmamk_f32 v84, v84, 0x3acccccd, v27\n"
"v_add_f32 v92, v92, v84\n"
"v_max_f32 v84, v84, v27\n"
"v_rsq_f32 v88, v84\n"
"s_nop 0\n"
"v_mul_f32 v89, -v89, v88\n"
"s_nop 1\n"
"s_branch .Lbk_133\n"
".Lsl_136:\n"
"s_nop 7\n"
"s_mov_b32 s17, 0x100000\n"
".Ltp_138:\n"
"ds_read_b128 v[84:87], v11 offset:2176\n"
"ds_read_b32 v91, v12 offset:2176\n"
"s_sub_u32 s17, s17, 1\n"
"s_cbranch_scc1 .Lou_139\n"
"s_waitcnt lgkmcnt(0)\n"
"v_max3_u32 v88, v84, v85, v91\n"
"v_max3_u32 v88, v86, v87, v88\n"
"v_cmp_eq_u32 vcc, -1, v88\n"
"s_cbranch_vccnz .Ltp_138\n"
".Lou_139:\n"
"s_waitcnt lgkmcnt(0)\n"
"v_add_f32 v84, v84, v85\n"
"v_add_f32 v86, v86, v87\n"
"v_add_f32 v84, v84, v86\n"
"v_mul_f32 v89, 0x3acccccd, v91\n"
"v_fma_f32 v84, -v91, v89, v84\n"
"v_fmamk_f32 v84, v84, 0x3acccccd, v27\n"
"v_add_f32 v92, v92, v84\n"
"v_max_f32 v84, v84, v27\n"
"v_rsq_f32 v88, v84\n"
"s_nop 0\n"
"v_mul_f32 v89, -v89, v88\n"
"s_nop 1\n"
"s_branch .Lbk_137\n"
".Lsl_140:\n"
"s_nop 7\n"
"s_mov_b32 s17, 0x100000\n"
".Ltp_142:\n"
"ds_read_b128 v[84:87], v11 offset:2496\n"
"ds_read_b32 v91, v12 offset:2496\n"
"s_sub_u32 s17, s17, 1\n"
"s_cbranch_scc1 .Lou_143\n"
"s_waitcnt lgkmcnt(0)\n"
"v_max3_u32 v88, v84, v85, v91\n"
"v_max3_u32 v88, v86, v87, v88\n"
"v_cmp_eq_u32 vcc, -1, v88\n"
"s_cbranch_vccnz .Ltp_142\n"
".Lou_143:\n"
"s_waitcnt lgkmcnt(0)\n"
"v_add_f32 v84, v84, v85\n"
"v_add_f32 v86, v86, v87\n"
"v_add_f32 v84, v84, v86\n"
"v_mul_f32 v89, 0x3acccccd, v91\n"
"v_fma_f32 v84, -v91, v89, v84\n"
"v_fmamk_f32 v84, v84, 0x3acccccd, v27\n"
"v_add_f32 v92, v92, v84\n"
"v_max_f32 v84, v84, v27\n"
"v_rsq_f32 v88, v84\n"
"s_nop 0\n"
"v_mul_f32 v89, -v89, v88\n"
"s_nop 1\n"
"s_branch .Lbk_141\n"
".Lsl_144:\n"
"s_nop 7\n"
"s_mov_b32 s17, 0x100000\n"
".Ltp_146:\n"
"ds_read_b128 v[84:87], v11 offset:2816\n"
"ds_read_b32 v91, v12 offset:2816\n"
"s_sub_u32 s17, s17, 1\n"
"s_cbranch_scc1 .Lou_147\n"
"s_waitcnt lgkmcnt(0)\n"
"v_max3_u32 v88, v84, v85, v91\n"
"v_max3_u32 v88, v86, v87, v88\n"
"v_cmp_eq_u32 vcc, -1, v88\n"
"s_cbranch_vccnz .Ltp_146\n"
".Lou_147:\n"
"s_waitcnt lgkmcnt(0)\n"
"v_add_f32 v84, v84, v85\n"
"v_add_f32 v86, v86, v87\n"
"v_add_f32 v84, v84, v86\n"
"v_mul_f32 v89, 0x3acccccd, v91\n"
"v_fma_f32 v84, -v91, v89, v84\n"
"v_fmamk_f32 v84, v84, 0x3acccccd, v27\n"
"v_add_f32 v92, v92, v84\n"
"v_max_f32 v84, v84, v27\n"
"v_rsq_f32 v88, v84\n"
"s_nop 0\n"
"v_mul_f32 v89, -v89, v88\n"
"s_nop 1\n"
"s_branch .Lbk_145\n"
".Lsl_148:\n"
"s_nop 7\n"
"s_mov_b32 s17, 0x100000\n"
".Ltp_150:\n"
"ds_read_b128 v[44:47], v11 offset:3136\n"
"ds_read_b128 v[48:51], v11 offset:3456\n"
"s_sub_u32 s17, s17, 1\n"
"s_cbranch_scc1 .Lou_151\n"
"s_waitcnt lgkmcnt(0)\n"
"v_max3_u32 v52, v44, v45, v51\n"
"v_max3_u32 v52, v46, v47, v52\n"
"v_max3_u32 v52, v48, v49, v52\n"
"v_max3_u32 v52, v50, v51, v52\n"
"v_cmp_eq_u32 vcc, -1, v52\n"
"s_cbranch_vccnz .Ltp_150\n"
".Lou_151:\n"
"s_waitcnt lgkmcnt(0)\n"
"v_add_f32 v44, v44, v45\n"
"v_add_f32 v46, v46, v47\n"
"v_add_f32 v48, v48, v49\n"
"v_add_f32 v50, v50, v51\n"
"v_add_f32 v48, v48, v50\n"
"v_add_f32 v44, v44, v46\n"
"v_mul_f32 v53, 0x3acccccd, v48\n"
"v_fma_f32 v44, -v48, v53, v44\n"
"v_fmamk_f32 v44, v44, 0x3acccccd, v27\n"
"v_add_f32 v92, v92, v44\n"
"v_max_f32 v44, v44, v27\n"
"v_rsq_f32 v52, v44\n"
"s_nop 0\n"
"v_mul_f32 v53, -v53, v52\n"
"s_nop 1\n"
"s_branch .Lbk_149\n"
".Lsl_144c:\n"
"s_nop 7\n"
"s_mov_b32 s17, 0x100000\n"
".Ltp_146c:\n"
"ds_read_b128 v[84:87], v11 offset:2816\n"
"ds_read_b32 v91, v12 offset:2816\n"
"s_sub_u32 s17, s17, 1\n"
"s_cbranch_scc1 .Lou_147c\n"
"s_waitcnt lgkmcnt(0)\n"
"v_max3_u32 v88, v84, v85, v91\n"
"v_max3_u32 v88, v86, v87, v88\n"
"v_cmp_eq_u32 vcc, -1, v88\n"
"s_cbranch_vccnz .Ltp_146c\n"
".Lou_147c:\n"
"s_waitcnt lgkmcnt(0)\n"
"v_add_f32 v84, v84, v85\n"
"v_add_f32 v86, v86, v87\n"
"v_add_f32 v84, v84, v86\n"
"v_mul_f32 v89, 0x3acccccd, v91\n"
"v_fma_f32 v84, -v91, v89, v84\n"
"v_fmamk_f32 v84, v84, 0x3acccccd, v27\n"
"v_add_f32 v92, v92, v84\n"
"v_max_f32 v84, v84, v27\n"
"v_rsq_f32 v88, v84\n"
"s_nop 0\n"
"v_mul_f32 v89, -v89, v88\n"
"s_nop 1\n"
"s_branch .Lbk_145c\n"
".Lsl_148c:\n"
"s_nop 7\n"
"s_mov_b32 s17, 0x100000\n"
".Ltp_150c:\n"
"ds_read_b128 v[44:47], v11 offset:3136\n"
"ds_read_b128 v[48:51], v11 offset:3456\n"
"s_sub_u32 s17, s17, 1\n"
"s_cbranch_scc1 .Lou_151c\n"
"s_waitcnt lgkmcnt(0)\n"
"v_max3_u32 v52, v44, v45, v51\n"
"v_max3_u32 v52, v46, v47, v52\n"
"v_max3_u32 v52, v48, v49, v52\n"
"v_max3_u32 v52, v50, v51, v52\n"
"v_cmp_eq_u32 vcc, -1, v52\n"
"s_cbranch_vccnz .Ltp_150c\n"
".Lou_151c:\n"
"s_waitcnt lgkmcnt(0)\n"
"v_add_f32 v44, v44, v45\n"
"v_add_f32 v46, v46, v47\n"
"v_add_f32 v48, v48, v49\n"
"v_add_f32 v50, v50, v51\n"
"v_add_f32 v48, v48, v50\n"
"v_add_f32 v44, v44, v46\n"
"v_mul_f32 v53, 0x3acccccd, v48\n"
"v_fma_f32 v44, -v48, v53, v44\n"
"v_fmamk_f32 v44, v44, 0x3acccccd, v27\n"
"v_add_f32 v92, v92, v44\n"
"v_max_f32 v44, v44, v27\n"
"v_rsq_f32 v52, v44\n"
"s_nop 0\n"
"v_mul_f32 v53, -v53, v52\n"
"s_nop 1\n"
"s_branch .Lbk_149c\n"
".Lrd_118:\n"
"s_nop 7\n"
"s_cmp_eq_u32 s54, 0\n"
"s_cbranch_scc1 .Lfi_154\n"
"s_cmp_lg_u32 s3, 3\n"
"s_cbranch_scc1 .Lgo_117c\n"
"s_branch .Lgo_117\n"
".Lfi_154:\n"
"s_mov_b32 s54, 1\n"
"s_sub_u32 s79, s44, 0x5000\n"
"s_add_u32 s78, s29, 0xc28000\n"
"s_max_i32 s46, s79, s78\n"
"s_add_u32 s47, s46, 0x1000\n"
"buffer_load_dwordx4 v[28:31], v1, s[4:7], s46 offen offset:0 sc1\n"
"buffer_load_dwordx4 v[32:35], v1, s[4:7], s46 offen offset:1024 sc1\n"
"buffer_load_dwordx4 v[36:39], v1, s[4:7], s46 offen offset:2048 sc1\n"
"buffer_load_dwordx4 v[40:43], v1, s[4:7], s46 offen offset:3072 sc1\n"
"buffer_load_dwordx4 v[44:47], v1, s[4:7], s47 offen offset:0 sc1\n"
"s_mov_b32 s17, 32768\n"
".Ltp_155:\n"
"s_waitcnt vmcnt(0)\n"
"s_sub_u32 s17, s17, 1\n"
"s_cbranch_scc1 .Lou_156\n"
"v_max_u32 v48, v28, v29\n"
"v_max3_u32 v48, v30, v31, v48\n"
"v_max3_u32 v48, v32, v33, v48\n"
"v_max3_u32 v48, v34, v35, v48\n"
"v_max3_u32 v48, v36, v37, v48\n"
"v_max3_u32 v48, v38, v39, v48\n"
"v_max3_u32 v48, v40, v41, v48\n"
"v_max3_u32 v48, v42, v43, v48\n"
"v_max3_u32 v48, v44, v45, v48\n"
"v_max3_u32 v48, v46, v47, v48\n"
"v_cmp_eq_u32 vcc, -1, v48\n"
"s_cbranch_vccz .Lou_156\n"
"v_max_u32 v48, v30, v31\n"
"v_max3_u32 v48, v28, v29, v48\n"
"v_cmp_eq_u32 vcc, -1, v48\n"
"s_cbranch_vccz .Lsk_157\n"
"s_mov_b64 exec, vcc\n"
"buffer_load_dwordx4 v[28:31], v1, s[4:7], s46 offen offset:0 sc1\n"
"s_mov_b64 exec, -1\n"
".Lsk_157:\n"
"v_max_u32 v48, v34, v35\n"
"v_max3_u32 v48, v32, v33, v48\n"
"v_cmp_eq_u32 vcc, -1, v48\n"
"s_cbranch_vccz .Lsk_158\n"
"s_mov_b64 exec, vcc\n"
"buffer_load_dwordx4 v[32:35], v1, s[4:7], s46 offen offset:1024 sc1\n"
"s_mov_b64 exec, -1\n"
".Lsk_158:\n"
"v_max_u32 v48, v38, v39\n"
"v_max3_u32 v48, v36, v37, v48\n"
"v_cmp_eq_u32 vcc, -1, v48\n"
"s_cbranch_vccz .Lsk_159\n"
"s_mov_b64 exec, vcc\n"
"buffer_load_dwordx4 v[36:39], v1, s[4:7], s46 offen offset:2048 sc1\n"
"s_mov_b64 exec, -1\n"
".Lsk_159:\n"
"v_max_u32 v48, v42, v43\n"
"v_max3_u32 v48, v40, v41, v48\n"
"v_cmp_eq_u32 vcc, -1, v48\n"
"s_cbranch_vccz .Lsk_160\n"
"s_mov_b64 exec, vcc\n"
"buffer_load_dwordx4 v[40:43], v1, s[4:7], s46 offen offset:3072 sc1\n"
"s_mov_b64 exec, -1\n"
".Lsk_160:\n"
"v_max_u32 v48, v46, v47\n"
"v_max3_u32 v48, v44, v45, v48\n"
"v_cmp_eq_u32 vcc, -1, v48\n"
"s_cbranch_vccz .Lsk_161\n"
"s_mov_b64 exec, vcc\n"
"buffer_load_dwordx4 v[44:47], v1, s[4:7], s47 offen offset:0 sc1\n"
"s_mov_b64 exec, -1\n"
".Lsk_161:\n"
"s_branch .Ltp_155\n"
".Lou_156:\n"
"s_waitcnt vmcnt(0)\n"
"s_mov_b32 s46, s44\n"
"s_add_u32 s47, s46, 0x1000\n"
"buffer_load_dwordx4 v[28:31], v1, s[4:7], s46 offen offset:0 sc1\n"
"buffer_load_dwordx4 v[32:35], v1, s[4:7], s46 offen offset:1024 sc1\n"
"buffer_load_dwordx4 v[36:39], v1, s[4:7], s46 offen offset:2048 sc1\n"
"buffer_load_dwordx4 v[40:43], v1, s[4:7], s46 offen offset:3072 sc1\n"
"buffer_load_dwordx4 v[44:47], v1, s[4:7], s47 offen offset:0 sc1\n"
"s_mov_b32 s17, 32768\n"
".Ltp_162:\n"
"s_waitcnt vmcnt(0)\n"
"s_sub_u32 s17, s17, 1\n"
"s_cbranch_scc1 .Lou_163\n"
"v_max_u32 v48, v28, v29\n"
"v_max3_u32 v48, v30, v31, v48\n"
"v_max3_u32 v48, v32, v33, v48\n"
"v_max3_u32 v48, v34, v35, v48\n"
"v_max3_u32 v48, v36, v37, v48\n"
"v_max3_u32 v48, v38, v39, v48\n"
"v_max3_u32 v48, v40, v41, v48\n"
"v_max3_u32 v48, v42, v43, v48\n"
"v_max3_u32 v48, v44, v45, v48\n"
"v_max3_u32 v48, v46, v47, v48\n"
"v_cmp_eq_u32 vcc, -1, v48\n"
"s_cbranch_vccz .Lou_163\n"
"v_max_u32 v48, v30, v31\n"
"v_max3_u32 v48, v28, v29, v48\n"
"v_cmp_eq_u32 vcc, -1, v48\n"
"s_cbranch_vccz .Lsk_164\n"
"s_mov_b64 exec, vcc\n"
"buffer_load_dwordx4 v[28:31], v1, s[4:7], s46 offen offset:0 sc1\n"
"s_mov_b64 exec, -1\n"
".Lsk_164:\n"
"v_max_u32 v48, v34, v35\n"
"v_max3_u32 v48, v32, v33, v48\n"
"v_cmp_eq_u32 vcc, -1, v48\n"
"s_cbranch_vccz .Lsk_165\n"
"s_mov_b64 exec, vcc\n"
"buffer_load_dwordx4 v[32:35], v1, s[4:7], s46 offen offset:1024 sc1\n"
"s_mov_b64 exec, -1\n"
".Lsk_165:\n"
"v_max_u32 v48, v38, v39\n"
"v_max3_u32 v48, v36, v37, v48\n"
"v_cmp_eq_u32 vcc, -1, v48\n"
"s_cbranch_vccz .Lsk_166\n"
"s_mov_b64 exec, vcc\n"
"buffer_load_dwordx4 v[36:39], v1, s[4:7], s46 offen offset:2048 sc1\n"
"s_mov_b64 exec, -1\n"
".Lsk_166:\n"
"v_max_u32 v48, v42, v43\n"
"v_max3_u32 v48, v40, v41, v48\n"
"v_cmp_eq_u32 vcc, -1, v48\n"
"s_cbranch_vccz .Lsk_167\n"
"s_mov_b64 exec, vcc\n"
"buffer_load_dwordx4 v[40:43], v1, s[4:7], s46 offen offset:3072 sc1\n"
"s_mov_b64 exec, -1\n"
".Lsk_167:\n"
"v_max_u32 v48, v46, v47\n"
"v_max3_u32 v48, v44, v45, v48\n"
"v_cmp_eq_u32 vcc, -1, v48\n"
"s_cbranch_vccz .Lsk_168\n"
"s_mov_b64 exec, vcc\n"
"buffer_load_dwordx4 v[44:47], v1, s[4:7], s47 offen offset:0 sc1\n"
"s_mov_b64 exec, -1\n"
".Lsk_168:\n"
"s_branch .Ltp_162\n"
".Lou_163:\n"
"s_waitcnt vmcnt(0)\n"
"s_add_u32 s46, s44, 0x5000\n"
"s_add_u32 s47, s46, 0x1000\n"
"buffer_load_dwordx4 v[28:31], v1, s[4:7], s46 offen offset:0 sc1\n"
"buffer_load_dwordx4 v[32:35], v1, s[4:7], s46 offen offset:1024 sc1\n"
"buffer_load_dwordx4 v[36:39], v1, s[4:7], s46 offen offset:2048 sc1\n"
"buffer_load_dwordx4 v[40:43], v1, s[4:7], s46 offen offset:3072 sc1\n"
"buffer_load_dwordx4 v[44:47], v1, s[4:7], s47 offen offset:0 sc1\n"
"s_mov_b32 s17, 32768\n"
".Ltp_169:\n"
"s_waitcnt vmcnt(0)\n"
"s_sub_u32 s17, s17, 1\n"
"s_cbranch_scc1 .Lou_170\n"
"v_max_u32 v48, v28, v29\n"
"v_max3_u32 v48, v30, v31, v48\n"
"v_max3_u32 v48, v32, v33, v48\n"
"v_max3_u32 v48, v34, v35, v48\n"
"v_max3_u32 v48, v36, v37, v48\n"
"v_max3_u32 v48, v38, v39, v48\n"
"v_max3_u32 v48, v40, v41, v48\n"
"v_max3_u32 v48, v42, v43, v48\n"
"v_max3_u32 v48, v44, v45, v48\n"
"v_max3_u32 v48, v46, v47, v48\n"
"v_cmp_eq_u32 vcc, -1, v48\n"
"s_cbranch_vccz .Lou_170\n"
"v_max_u32 v48, v30, v31\n"
"v_max3_u32 v48, v28, v29, v48\n"
"v_cmp_eq_u32 vcc, -1, v48\n"
"s_cbranch_vccz .Lsk_171\n"
"s_mov_b64 exec, vcc\n"
"buffer_load_dwordx4 v[28:31], v1, s[4:7], s46 offen offset:0 sc1\n"
"s_mov_b64 exec, -1\n"
".Lsk_171:\n"
"v_max_u32 v48, v34, v35\n"
"v_max3_u32 v48, v32, v33, v48\n"
"v_cmp_eq_u32 vcc, -1, v48\n"
"s_cbranch_vccz .Lsk_172\n"
"s_mov_b64 exec, vcc\n"
"buffer_load_dwordx4 v[32:35], v1, s[4:7], s46 offen offset:1024 sc1\n"
"s_mov_b64 exec, -1\n"
".Lsk_172:\n"
"v_max_u32 v48, v38, v39\n"
"v_max3_u32 v48, v36, v37, v48\n"
"v_cmp_eq_u32 vcc, -1, v48\n"
"s_cbranch_vccz .Lsk_173\n"
"s_mov_b64 exec, vcc\n"
"buffer_load_dwordx4 v[36:39], v1, s[4:7], s46 offen offset:2048 sc1\n"
"s_mov_b64 exec, -1\n"
".Lsk_173:\n"
"v_max_u32 v48, v42, v43\n"
"v_max3_u32 v48, v40, v41, v48\n"
"v_cmp_eq_u32 vcc, -1, v48\n"
"s_cbranch_vccz .Lsk_174\n"
"s_mov_b64 exec, vcc\n"
"buffer_load_dwordx4 v[40:43], v1, s[4:7], s46 offen offset:3072 sc1\n"
"s_mov_b64 exec, -1\n"
".Lsk_174:\n"
"v_max_u32 v48, v46, v47\n"
"v_max3_u32 v48, v44, v45, v48\n"
"v_cmp_eq_u32 vcc, -1, v48\n"
"s_cbranch_vccz .Lsk_175\n"
"s_mov_b64 exec, vcc\n"
"buffer_load_dwordx4 v[44:47], v1, s[4:7], s47 offen offset:0 sc1\n"
"s_mov_b64 exec, -1\n"
".Lsk_175:\n"
"s_branch .Ltp_169\n"
".Lou_170:\n"
"s_waitcnt vmcnt(0)\n"
"s_add_u32 s46, s44, 0xa000\n"
"s_add_u32 s47, s46, 0x1000\n"
"buffer_load_dwordx4 v[28:31], v1, s[4:7], s46 offen offset:0 sc1\n"
"buffer_load_dwordx4 v[32:35], v1, s[4:7], s46 offen offset:1024 sc1\n"
"buffer_load_dwordx4 v[36:39], v1, s[4:7], s46 offen offset:2048 sc1\n"
"buffer_load_dwordx4 v[40:43], v1, s[4:7], s46 offen offset:3072 sc1\n"
"buffer_load_dwordx4 v[44:47], v1, s[4:7], s47 offen offset:0 sc1\n"
"s_mov_b32 s17, 32768\n"
".Ltp_176:\n"
"s_waitcnt vmcnt(0)\n"
"s_sub_u32 s17, s17, 1\n"
"s_cbranch_scc1 .Lou_177\n"
"v_max_u32 v48, v28, v29\n"
"v_max3_u32 v48, v30, v31, v48\n"
"v_max3_u32 v48, v32, v33, v48\n"
"v_max3_u32 v48, v34, v35, v48\n"
"v_max3_u32 v48, v36, v37, v48\n"
"v_max3_u32 v48, v38, v39, v48\n"
"v_max3_u32 v48, v40, v41, v48\n"
"v_max3_u32 v48, v42, v43, v48\n"
"v_max3_u32 v48, v44, v45, v48\n"
"v_max3_u32 v48, v46, v47, v48\n"
"v_cmp_eq_u32 vcc, -1, v48\n"
"s_cbranch_vccz .Lou_177\n"
"v_max_u32 v48, v30, v31\n"
"v_max3_u32 v48, v28, v29, v48\n"
"v_cmp_eq_u32 vcc, -1, v48\n"
"s_cbranch_vccz .Lsk_178\n"
"s_mov_b64 exec, vcc\n"
"buffer_load_dwordx4 v[28:31], v1, s[4:7], s46 offen offset:0 sc1\n"
"s_mov_b64 exec, -1\n"
".Lsk_178:\n"
"v_max_u32 v48, v34, v35\n"
"v_max3_u32 v48, v32, v33, v48\n"
"v_cmp_eq_u32 vcc, -1, v48\n"
"s_cbranch_vccz .Lsk_179\n"
"s_mov_b64 exec, vcc\n"
"buffer_load_dwordx4 v[32:35], v1, s[4:7], s46 offen offset:1024 sc1\n"
"s_mov_b64 exec, -1\n"
".Lsk_179:\n"
"v_max_u32 v48, v38, v39\n"
"v_max3_u32 v48, v36, v37, v48\n"
"v_cmp_eq_u32 vcc, -1, v48\n"
"s_cbranch_vccz .Lsk_180\n"
"s_mov_b64 exec, vcc\n"
"buffer_load_dwordx4 v[36:39], v1, s[4:7], s46 offen offset:2048 sc1\n"
"s_mov_b64 exec, -1\n"
".Lsk_180:\n"
"v_max_u32 v48, v42, v43\n"
"v_max3_u32 v48, v40, v41, v48\n"
"v_cmp_eq_u32 vcc, -1, v48\n"
"s_cbranch_vccz .Lsk_181\n"
"s_mov_b64 exec, vcc\n"
"buffer_load_dwordx4 v[40:43], v1, s[4:7], s46 offen offset:3072 sc1\n"
"s_mov_b64 exec, -1\n"
".Lsk_181:\n"
"v_max_u32 v48, v46, v47\n"
"v_max3_u32 v48, v44, v45, v48\n"
"v_cmp_eq_u32 vcc, -1, v48\n"
"s_cbranch_vccz .Lsk_182\n"
"s_mov_b64 exec, vcc\n"
"buffer_load_dwordx4 v[44:47], v1, s[4:7], s47 offen offset:0 sc1\n"
"s_mov_b64 exec, -1\n"
".Lsk_182:\n"
"s_branch .Ltp_176\n"
".Lou_177:\n"
"s_waitcnt vmcnt(0)\n"
"s_add_u32 s46, s44, 0xf000\n"
"s_add_u32 s47, s46, 0x1000\n"
"buffer_load_dwordx4 v[28:31], v1, s[4:7], s46 offen offset:0 sc1\n"
"buffer_load_dwordx4 v[32:35], v1, s[4:7], s46 offen offset:1024 sc1\n"
"buffer_load_dwordx4 v[36:39], v1, s[4:7], s46 offen offset:2048 sc1\n"
"buffer_load_dwordx4 v[40:43], v1, s[4:7], s46 offen offset:3072 sc1\n"
"buffer_load_dwordx4 v[44:47], v1, s[4:7], s47 offen offset:0 sc1\n"
"s_mov_b32 s17, 32768\n"
".Ltp_183:\n"
"s_waitcnt vmcnt(0)\n"
"s_sub_u32 s17, s17, 1\n"
"s_cbranch_scc1 .Lou_184\n"
"v_max_u32 v48, v28, v29\n"
"v_max3_u32 v48, v30, v31, v48\n"
"v_max3_u32 v48, v32, v33, v48\n"
"v_max3_u32 v48, v34, v35, v48\n"
"v_max3_u32 v48, v36, v37, v48\n"
"v_max3_u32 v48, v38, v39, v48\n"
"v_max3_u32 v48, v40, v41, v48\n"
"v_max3_u32 v48, v42, v43, v48\n"
"v_max3_u32 v48, v44, v45, v48\n"
"v_max3_u32 v48, v46, v47, v48\n"
"v_cmp_eq_u32 vcc, -1, v48\n"
"s_cbranch_vccz .Lou_184\n"
"v_max_u32 v48, v30, v31\n"
"v_max3_u32 v48, v28, v29, v48\n"
"v_cmp_eq_u32 vcc, -1, v48\n"
"s_cbranch_vccz .Lsk_185\n"
"s_mov_b64 exec, vcc\n"
"buffer_load_dwordx4 v[28:31], v1, s[4:7], s46 offen offset:0 sc1\n"
"s_mov_b64 exec, -1\n"
".Lsk_185:\n"
"v_max_u32 v48, v34, v35\n"
"v_max3_u32 v48, v32, v33, v48\n"
"v_cmp_eq_u32 vcc, -1, v48\n"
"s_cbranch_vccz .Lsk_186\n"
"s_mov_b64 exec, vcc\n"
"buffer_load_dwordx4 v[32:35], v1, s[4:7], s46 offen offset:1024 sc1\n"
"s_mov_b64 exec, -1\n"
".Lsk_186:\n"
"v_max_u32 v48, v38, v39\n"
"v_max3_u32 v48, v36, v37, v48\n"
"v_cmp_eq_u32 vcc, -1, v48\n"
"s_cbranch_vccz .Lsk_187\n"
"s_mov_b64 exec, vcc\n"
"buffer_load_dwordx4 v[36:39], v1, s[4:7], s46 offen offset:2048 sc1\n"
"s_mov_b64 exec, -1\n"
".Lsk_187:\n"
"v_max_u32 v48, v42, v43\n"
"v_max3_u32 v48, v40, v41, v48\n"
"v_cmp_eq_u32 vcc, -1, v48\n"
"s_cbranch_vccz .Lsk_188\n"
"s_mov_b64 exec, vcc\n"
"buffer_load_dwordx4 v[40:43], v1, s[4:7], s46 offen offset:3072 sc1\n"
"s_mov_b64 exec, -1\n"
".Lsk_188:\n"
"v_max_u32 v48, v46, v47\n"
"v_max3_u32 v48, v44, v45, v48\n"
"v_cmp_eq_u32 vcc, -1, v48\n"
"s_cbranch_vccz .Lsk_189\n"
"s_mov_b64 exec, vcc\n"
"buffer_load_dwordx4 v[44:47], v1, s[4:7], s47 offen offset:0 sc1\n"
"s_mov_b64 exec, -1\n"
".Lsk_189:\n"
"s_branch .Ltp_183\n"
".Lou_184:\n"
"s_waitcnt vmcnt(0)\n"
"s_mov_b32 s17, 0x100000\n"
".Ltp_190:\n"
"ds_read_b32 v90, v14 offset:320\n"
"s_sub_u32 s17, s17, 1\n"
"s_cbranch_scc1 .Lou_191\n"
"s_waitcnt lgkmcnt(0)\n"
"v_cmp_eq_u32 vcc, 0, v90\n"
"s_cbranch_vccnz .Ltp_190\n"
".Lou_191:\n"
"s_waitcnt lgkmcnt(0)\n"
"ds_write_b32 v13, v13 offset:336\n"
"s_mov_b32 s17, 0x100000\n"
".Ltp_192:\n"
"ds_read_b32 v90, v14 offset:336\n"
"s_sub_u32 s17, s17, 1\n"
"s_cbranch_scc1 .Lou_193\n"
"s_waitcnt lgkmcnt(0)\n"
"v_cmp_eq_u32 vcc, 0, v90\n"
"s_cbranch_vccnz .Ltp_192\n"
".Lou_193:\n"
"s_waitcnt lgkmcnt(0)\n"
"v_mov_b32 v90, 0\n"
"ds_write_b32 v13, v90 offset:272\n"
"ds_write_b32 v13, v90 offset:288\n"
"v_mov_b32 v90, -1\n"
"ds_write_b32 v9, v90 offset:1856\n"
"ds_write_b32 v9, v90 offset:2176\n"
"ds_write_b32 v9, v90 offset:2496\n"
"ds_write_b32 v9, v90 offset:2816\n"
"ds_write_b32 v9, v90 offset:3136\n"
"ds_write_b32 v10, v90 offset:3456\n"
"ds_write_b32 v13, v13 offset:352\n"
"s_mov_b32 s17, 0x100000\n"
".Ltp_194:\n"
"ds_read_b32 v90, v14 offset:352\n"
"s_sub_u32 s17, s17, 1\n"
"s_cbranch_scc1 .Lou_195\n"
"s_waitcnt lgkmcnt(0)\n"
"v_cmp_eq_u32 vcc, 0, v90\n"
"s_cbranch_vccnz .Ltp_194\n"
".Lou_195:\n"
"s_waitcnt lgkmcnt(0)\n"
"s_branch .Lag_116\n"
".Lfx_204:\n"
"s_nop 7\n"
"s_add_u32 m0, s29, 0xa000\n"
"s_add_u32 s31, s44, 0x140000\n"
"buffer_load_dwordx4 v1, s[4:7], s31 offen offset:0 sc1 lds\n"
"buffer_load_dwordx4 v1, s[4:7], s31 offen offset:1024 sc1 lds\n"
"buffer_load_dwordx4 v1, s[4:7], s31 offen offset:2048 sc1 lds\n"
"buffer_load_dwordx4 v1, s[4:7], s31 offen offset:3072 sc1 lds\n"
"s_add_u32 m0, m0, 1024\n"
"s_add_u32 s31, s31, 1024\n"
"buffer_load_dwordx4 v1, s[4:7], s31 offen offset:3072 sc1 lds\n"
"s_add_u32 m0, s29, 0xf000\n"
"s_add_u32 s31, s44, 0x145000\n"
"buffer_load_dwordx4 v1, s[4:7], s31 offen offset:0 sc1 lds\n"
"buffer_load_dwordx4 v1, s[4:7], s31 offen offset:1024 sc1 lds\n"
"buffer_load_dwordx4 v1, s[4:7], s31 offen offset:2048 sc1 lds\n"
"buffer_load_dwordx4 v1, s[4:7], s31 offen offset:3072 sc1 lds\n"
"s_add_u32 m0, m0, 1024\n"
"s_add_u32 s31, s31, 1024\n"
"buffer_load_dwordx4 v1, s[4:7], s31 offen offset:3072 sc1 lds\n"
"s_waitcnt vmcnt(0)\n"
"ds_write_b32 v13, v13 offset:384\n"
"s_mov_b32 s17, 0x100000\n"
".Ltp_206:\n"
"ds_read_b32 v90, v14 offset:384\n"
"s_sub_u32 s17, s17, 1\n"
"s_cbranch_scc1 .Lou_207\n"
"s_waitcnt lgkmcnt(0)\n"
"v_cmp_eq_u32 vcc, 0, v90\n"
"s_cbranch_vccnz .Ltp_206\n"
".Lou_207:\n"
"s_waitcnt lgkmcnt(0)\n"
"v_mov_b32 v90, 0\n"
"s_branch .Lbk_205\n"
".Lsl_208:\n"
"s_nop 7\n"
"s_mov_b32 s17, 0x100000\n"
".Ltp_210:\n"
"ds_read_b32 v90, v14 offset:400\n"
"s_sub_u32 s17, s17, 1\n"
"s_cbranch_scc1 .Lou_211\n"
"s_waitcnt lgkmcnt(0)\n"
"v_cmp_eq_u32 vcc, 0, v90\n"
"s_cbranch_vccnz .Ltp_210\n"
".Lou_211:\n"
"s_waitcnt lgkmcnt(0)\n"
"s_branch .Lbk_209\n"
".Lsl_212:\n"
"s_nop 7\n"
"s_mov_b32 s17, 0x100000\n"
".Ltp_214:\n"
"ds_read_b32 v90, v14 offset:416\n"
"s_sub_u32 s17, s17, 1\n"
"s_cbranch_scc1 .Lou_215\n"
"s_waitcnt lgkmcnt(0)\n"
"v_cmp_eq_u32 vcc, 0, v90\n"
"s_cbranch_vccnz .Ltp_214\n"
".Lou_215:\n"
"s_waitcnt lgkmcnt(0)\n"
"s_branch .Lbk_213\n"
".Lsl_216:\n"
"s_nop 7\n"
"s_mov_b32 s17, 0x100000\n"
".Ltp_218:\n"
"ds_read_b128 v[84:87], v11 offset:3712\n"
"ds_read_b32 v91, v12 offset:3712\n"
"s_sub_u32 s17, s17, 1\n"
"s_cbranch_scc1 .Lou_219\n"
"s_waitcnt lgkmcnt(0)\n"
"v_max3_u32 v88, v84, v85, v91\n"
"v_max3_u32 v88, v86, v87, v88\n"
"v_cmp_eq_u32 vcc, -1, v88\n"
"s_cbranch_vccnz .Ltp_218\n"
".Lou_219:\n"
"s_waitcnt lgkmcnt(0)\n"
"v_add_f32 v84, v84, v85\n"
"v_add_f32 v86, v86, v87\n"
"v_add_f32 v84, v84, v86\n"
"v_mul_f32 v89, 0x3acccccd, v91\n"
"v_fma_f32 v84, -v91, v89, v84\n"
"v_fmamk_f32 v84, v84, 0x3acccccd, v27\n"
"v_add_f32 v92, v92, v84\n"
"v_max_f32 v84, v84, v27\n"
"v_rsq_f32 v88, v84\n"
"s_nop 0\n"
"v_mul_f32 v89, -v89, v88\n"
"s_nop 1\n"
"s_branch .Lbk_217\n"
".Lsl_220:\n"
"s_nop 7\n"
"s_mov_b32 s17, 0x100000\n"
".Ltp_222:\n"
"ds_read_b128 v[84:87], v11 offset:4032\n"
"ds_read_b32 v91, v12 offset:4032\n"
"s_sub_u32 s17, s17, 1\n"
"s_cbranch_scc1 .Lou_223\n"
"s_waitcnt lgkmcnt(0)\n"
"v_max3_u32 v88, v84, v85, v91\n"
"v_max3_u32 v88, v86, v87, v88\n"
"v_cmp_eq_u32 vcc, -1, v88\n"
"s_cbranch_vccnz .Ltp_222\n"
".Lou_223:\n"
"s_waitcnt lgkmcnt(0)\n"
"v_add_f32 v84, v84, v85\n"
"v_add_f32 v86, v86, v87\n"
"v_add_f32 v84, v84, v86\n"
"v_mul_f32 v89, 0x3acccccd, v91\n"
"v_fma_f32 v84, -v91, v89, v84\n"
"v_fmamk_f32 v84, v84, 0x3acccccd, v27\n"
"v_add_f32 v92, v92, v84\n"
"v_max_f32 v84, v84, v27\n"
"v_rsq_f32 v88, v84\n"
"s_nop 0\n"
"v_mul_f32 v89, -v89, v88\n"
"s_nop 1\n"
"s_branch .Lbk_221\n"
".Lsl_224:\n"
"s_nop 7\n"
"s_mov_b32 s17, 0x100000\n"
".Ltp_226:\n"
"ds_read_b128 v[84:87], v11 offset:4352\n"
"ds_read_b32 v91, v12 offset:4352\n"
"s_sub_u32 s17, s17, 1\n"
"s_cbranch_scc1 .Lou_227\n"
"s_waitcnt lgkmcnt(0)\n"
"v_max3_u32 v88, v84, v85, v91\n"
"v_max3_u32 v88, v86, v87, v88\n"
"v_cmp_eq_u32 vcc, -1, v88\n"
"s_cbranch_vccnz .Ltp_226\n"
".Lou_227:\n"
"s_waitcnt lgkmcnt(0)\n"
"v_add_f32 v84, v84, v85\n"
"v_add_f32 v86, v86, v87\n"
"v_add_f32 v84, v84, v86\n"
"v_mul_f32 v89, 0x3acccccd, v91\n"
"v_fma_f32 v84, -v91, v89, v84\n"
"v_fmamk_f32 v84, v84, 0x3acccccd, v27\n"
"v_add_f32 v92, v92, v84\n"
"v_max_f32 v84, v84, v27\n"
"v_rsq_f32 v88, v84\n"
"s_nop 0\n"
"v_mul_f32 v89, -v89, v88\n"
"s_nop 1\n"
"s_branch .Lbk_225\n"
".Lsl_228:\n"
"s_nop 7\n"
"s_mov_b32 s17, 0x100000\n"
".Ltp_230:\n"
"ds_read_b128 v[84:87], v11 offset:4672\n"
"ds_read_b32 v91, v12 offset:4672\n"
"s_sub_u32 s17, s17, 1\n"
"s_cbranch_scc1 .Lou_231\n"
"s_waitcnt lgkmcnt(0)\n"
"v_max3_u32 v88, v84, v85, v91\n"
"v_max3_u32 v88, v86, v87, v88\n"
"v_cmp_eq_u32 vcc, -1, v88\n"
"s_cbranch_vccnz .Ltp_230\n"
".Lou_231:\n"
"s_waitcnt lgkmcnt(0)\n"
"v_add_f32 v84, v84, v85\n"
"v_add_f32 v86, v86, v87\n"
"v_add_f32 v84, v84, v86\n"
"v_mul_f32 v89, 0x3acccccd, v91\n"
"v_fma_f32 v84, -v91, v89, v84\n"
"v_fmamk_f32 v84, v84, 0x3acccccd, v27\n"
"v_add_f32 v92, v92, v84\n"
"v_max_f32 v84, v84, v27\n"
"v_rsq_f32 v88, v84\n"
"s_nop 0\n"
"v_mul_f32 v89, -v89, v88\n"
"s_nop 1\n"
"s_branch .Lbk_229\n"
".Lsl_232:\n"
"s_nop 7\n"
"s_mov_b32 s17, 0x100000\n"
".Ltp_234:\n"
"ds_read_b128 v[44:47], v11 offset:4992\n"
"ds_read_b128 v[48:51], v11 offset:5312\n"
"s_sub_u32 s17, s17, 1\n"
"s_cbranch_scc1 .Lou_235\n"
"s_waitcnt lgkmcnt(0)\n"
"v_max3_u32 v52, v44, v45, v51\n"
"v_max3_u32 v52, v46, v47, v52\n"
"v_max3_u32 v52, v48, v49, v52\n"
"v_max3_u32 v52, v50, v51, v52\n"
"v_cmp_eq_u32 vcc, -1, v52\n"
"s_cbranch_vccnz .Ltp_234\n"
".Lou_235:\n"
"s_waitcnt lgkmcnt(0)\n"
"v_add_f32 v44, v44, v45\n"
"v_add_f32 v46, v46, v47\n"
"v_add_f32 v48, v48, v49\n"
"v_add_f32 v50, v50, v51\n"
"v_add_f32 v48, v48, v50\n"
"v_add_f32 v44, v44, v46\n"
"v_mul_f32 v53, 0x3acccccd, v48\n"
"v_fma_f32 v44, -v48, v53, v44\n"
"v_fmamk_f32 v44, v44, 0x3acccccd, v27\n"
"v_add_f32 v92, v92, v44\n"
"v_max_f32 v44, v44, v27\n"
"v_rsq_f32 v52, v44\n"
"s_nop 0\n"
"v_mul_f32 v53, -v53, v52\n"
"s_nop 1\n"
"s_branch .Lbk_233\n"
".Lsl_228c:\n"
"s_nop 7\n"
"s_mov_b32 s17, 0x100000\n"
".Ltp_230c:\n"
"ds_read_b128 v[84:87], v11 offset:4672\n"
"ds_read_b32 v91, v12 offset:4672\n"
"s_sub_u32 s17, s17, 1\n"
"s_cbranch_scc1 .Lou_231c\n"
"s_waitcnt lgkmcnt(0)\n"
"v_max3_u32 v88, v84, v85, v91\n"
"v_max3_u32 v88, v86, v87, v88\n"
"v_cmp_eq_u32 vcc, -1, v88\n"
"s_cbranch_vccnz .Ltp_230c\n"
".Lou_231c:\n"
"s_waitcnt lgkmcnt(0)\n"
"v_add_f32 v84, v84, v85\n"
"v_add_f32 v86, v86, v87\n"
"v_add_f32 v84, v84, v86\n"
"v_mul_f32 v89, 0x3acccccd, v91\n"
"v_fma_f32 v84, -v91, v89, v84\n"
"v_fmamk_f32 v84, v84, 0x3acccccd, v27\n"
"v_add_f32 v92, v92, v84\n"
"v_max_f32 v84, v84, v27\n"
"v_rsq_f32 v88, v84\n"
"s_nop 0\n"
"v_mul_f32 v89, -v89, v88\n"
"s_nop 1\n"
"s_branch .Lbk_229c\n"
".Lsl_232c:\n"
"s_nop 7\n"
"s_mov_b32 s17, 0x100000\n"
".Ltp_234c:\n"
"ds_read_b128 v[44:47], v11 offset:4992\n"
"ds_read_b128 v[48:51], v11 offset:5312\n"
"s_sub_u32 s17, s17, 1\n"
"s_cbranch_scc1 .Lou_235c\n"
"s_waitcnt lgkmcnt(0)\n"
"v_max3_u32 v52, v44, v45, v51\n"
"v_max3_u32 v52, v46, v47, v52\n"
"v_max3_u32 v52, v48, v49, v52\n"
"v_max3_u32 v52, v50, v51, v52\n"
"v_cmp_eq_u32 vcc, -1, v52\n"
"s_cbranch_vccnz .Ltp_234c\n"
".Lou_235c:\n"
"s_waitcnt lgkmcnt(0)\n"
"v_add_f32 v44, v44, v45\n"
"v_add_f32 v46, v46, v47\n"
"v_add_f32 v48, v48, v49\n"
"v_add_f32 v50, v50, v51\n"
"v_add_f32 v48, v48, v50\n"
"v_add_f32 v44, v44, v46\n"
"v_mul_f32 v53, 0x3acccccd, v48\n"
"v_fma_f32 v44, -v48, v53, v44\n"
"v_fmamk_f32 v44, v44, 0x3acccccd, v27\n"
"v_add_f32 v92, v92, v44\n"
"v_max_f32 v44, v44, v27\n"
"v_rsq_f32 v52, v44\n"
"s_nop 0\n"
"v_mul_f32 v53, -v53, v52\n"
"s_nop 1\n"
"s_branch .Lbk_233c\n"
".Lsl_238:\n"
"s_nop 7\n"
"s_mov_b32 s17, 0x100000\n"
".Ltp_240:\n"
"ds_read_b32 v90, v14 offset:320\n"
"s_sub_u32 s17, s17, 1\n"
"s_cbranch_scc1 .Lou_241\n"
"s_waitcnt lgkmcnt(0)\n"
"v_cmp_eq_u32 vcc, 0, v90\n"
"s_cbranch_vccnz .Ltp_240\n"
".Lou_241:\n"
"ds_read_b64 v[56:57], v3 offset:40960\n"
"ds_read_b64 v[58:59], v3 offset:41216\n"
"ds_read_b64 v[60:61], v3 offset:41984\n"
"ds_read_b64 v[62:63], v3 offset:42240\n"
"ds_read_b64 v[64:65], v3 offset:43008\n"
"ds_read_b64 v[66:67], v3 offset:43264\n"
"ds_read_b64 v[68:69], v3 offset:44032\n"
"ds_read_b64 v[70:71], v3 offset:44288\n"
"ds_read_b64 v[72:73], v3 offset:45056\n"
"ds_read_b64 v[74:75], v3 offset:45312\n"
"s_waitcnt lgkmcnt(0)\n"
"s_branch .Lbk_239\n"
".Lrd_203:\n"
"s_nop 7\n"
"s_cmp_eq_u32 s54, 0\n"
"s_cbranch_scc1 .Lfi_242\n"
"s_cmp_lg_u32 s3, 3\n"
"s_cbranch_scc1 .Lgo_202c\n"
"s_branch .Lgo_202\n"
".Lfi_242:\n"
"s_mov_b32 s54, 1\n"
"s_add_u32 s46, s44, 0x13b000\n"
"s_add_u32 s47, s46, 0x1000\n"
"buffer_load_dwordx4 v[28:31], v1, s[4:7], s46 offen offset:0 sc1\n"
"buffer_load_dwordx4 v[32:35], v1, s[4:7], s46 offen offset:1024 sc1\n"
"buffer_load_dwordx4 v[36:39], v1, s[4:7], s46 offen offset:2048 sc1\n"
"buffer_load_dwordx4 v[40:43], v1, s[4:7], s46 offen offset:3072 sc1\n"
"buffer_load_dwordx4 v[44:47], v1, s[4:7], s47 offen offset:0 sc1\n"
"s_mov_b32 s17, 32768\n"
".Ltp_243:\n"
"s_waitcnt vmcnt(0)\n"
"s_sub_u32 s17, s17, 1\n"
"s_cbranch_scc1 .Lou_244\n"
"v_max_u32 v48, v28, v29\n"
"v_max3_u32 v48, v30, v31, v48\n"
"v_max3_u32 v48, v32, v33, v48\n"
"v_max3_u32 v48, v34, v35, v48\n"
"v_max3_u32 v48, v36, v37, v48\n"
"v_max3_u32 v48, v38, v39, v48\n"
"v_max3_u32 v48, v40, v41, v48\n"
"v_max3_u32 v48, v42, v43, v48\n"
"v_max3_u32 v48, v44, v45, v48\n"
"v_max3_u32 v48, v46, v47, v48\n"
"v_cmp_eq_u32 vcc, -1, v48\n"
"s_cbranch_vccz .Lou_244\n"
"v_max_u32 v48, v30, v31\n"
"v_max3_u32 v48, v28, v29, v48\n"
"v_cmp_eq_u32 vcc, -1, v48\n"
"s_cbranch_vccz .Lsk_245\n"
"s_mov_b64 exec, vcc\n"
"buffer_load_dwordx4 v[28:31], v1, s[4:7], s46 offen offset:0 sc1\n"
"s_mov_b64 exec, -1\n"
".Lsk_245:\n"
"v_max_u32 v48, v34, v35\n"
"v_max3_u32 v48, v32, v33, v48\n"
"v_cmp_eq_u32 vcc, -1, v48\n"
"s_cbranch_vccz .Lsk_246\n"
"s_mov_b64 exec, vcc\n"
"buffer_load_dwordx4 v[32:35], v1, s[4:7], s46 offen offset:1024 sc1\n"
"s_mov_b64 exec, -1\n"
".Lsk_246:\n"
"v_max_u32 v48, v38, v39\n"
"v_max3_u32 v48, v36, v37, v48\n"
"v_cmp_eq_u32 vcc, -1, v48\n"
"s_cbranch_vccz .Lsk_247\n"
"s_mov_b64 exec, vcc\n"
"buffer_load_dwordx4 v[36:39], v1, s[4:7], s46 offen offset:2048 sc1\n"
"s_mov_b64 exec, -1\n"
".Lsk_247:\n"
"v_max_u32 v48, v42, v43\n"
"v_max3_u32 v48, v40, v41, v48\n"
"v_cmp_eq_u32 vcc, -1, v48\n"
"s_cbranch_vccz .Lsk_248\n"
"s_mov_b64 exec, vcc\n"
"buffer_load_dwordx4 v[40:43], v1, s[4:7], s46 offen offset:3072 sc1\n"
"s_mov_b64 exec, -1\n"
".Lsk_248:\n"
"v_max_u32 v48, v46, v47\n"
"v_max3_u32 v48, v44, v45, v48\n"
"v_cmp_eq_u32 vcc, -1, v48\n"
"s_cbranch_vccz .Lsk_249\n"
"s_mov_b64 exec, vcc\n"
"buffer_load_dwordx4 v[44:47], v1, s[4:7], s47 offen offset:0 sc1\n"
"s_mov_b64 exec, -1\n"
".Lsk_249:\n"
"s_branch .Ltp_243\n"
".Lou_244:\n"
"s_waitcnt vmcnt(0)\n"
"s_add_u32 s46, s44, 0x140000\n"
"s_add_u32 s47, s46, 0x1000\n"
"buffer_load_dwordx4 v[28:31], v1, s[4:7], s46 offen offset:0 sc1\n"
"buffer_load_dwordx4 v[32:35], v1, s[4:7], s46 offen offset:1024 sc1\n"
"buffer_load_dwordx4 v[36:39], v1, s[4:7], s46 offen offset:2048 sc1\n"
"buffer_load_dwordx4 v[40:43], v1, s[4:7], s46 offen offset:3072 sc1\n"
"buffer_load_dwordx4 v[44:47], v1, s[4:7], s47 offen offset:0 sc1\n"
"s_mov_b32 s17, 32768\n"
".Ltp_250:\n"
"s_waitcnt vmcnt(0)\n"
"s_sub_u32 s17, s17, 1\n"
"s_cbranch_scc1 .Lou_251\n"
"v_max_u32 v48, v28, v29\n"
"v_max3_u32 v48, v30, v31, v48\n"
"v_max3_u32 v48, v32, v33, v48\n"
"v_max3_u32 v48, v34, v35, v48\n"
"v_max3_u32 v48, v36, v37, v48\n"
"v_max3_u32 v48, v38, v39, v48\n"
"v_max3_u32 v48, v40, v41, v48\n"
"v_max3_u32 v48, v42, v43, v48\n"
"v_max3_u32 v48, v44, v45, v48\n"
"v_max3_u32 v48, v46, v47, v48\n"
"v_cmp_eq_u32 vcc, -1, v48\n"
"s_cbranch_vccz .Lou_251\n"
"v_max_u32 v48, v30, v31\n"
"v_max3_u32 v48, v28, v29, v48\n"
"v_cmp_eq_u32 vcc, -1, v48\n"
"s_cbranch_vccz .Lsk_252\n"
"s_mov_b64 exec, vcc\n"
"buffer_load_dwordx4 v[28:31], v1, s[4:7], s46 offen offset:0 sc1\n"
"s_mov_b64 exec, -1\n"
".Lsk_252:\n"
"v_max_u32 v48, v34, v35\n"
"v_max3_u32 v48, v32, v33, v48\n"
"v_cmp_eq_u32 vcc, -1, v48\n"
"s_cbranch_vccz .Lsk_253\n"
"s_mov_b64 exec, vcc\n"
"buffer_load_dwordx4 v[32:35], v1, s[4:7], s46 offen offset:1024 sc1\n"
"s_mov_b64 exec, -1\n"
".Lsk_253:\n"
"v_max_u32 v48, v38, v39\n"
"v_max3_u32 v48, v36, v37, v48\n"
"v_cmp_eq_u32 vcc, -1, v48\n"
"s_cbranch_vccz .Lsk_254\n"
"s_mov_b64 exec, vcc\n"
"buffer_load_dwordx4 v[36:39], v1, s[4:7], s46 offen offset:2048 sc1\n"
"s_mov_b64 exec, -1\n"
".Lsk_254:\n"
"v_max_u32 v48, v42, v43\n"
"v_max3_u32 v48, v40, v41, v48\n"
"v_cmp_eq_u32 vcc, -1, v48\n"
"s_cbranch_vccz .Lsk_255\n"
"s_mov_b64 exec, vcc\n"
"buffer_load_dwordx4 v[40:43], v1, s[4:7], s46 offen offset:3072 sc1\n"
"s_mov_b64 exec, -1\n"
".Lsk_255:\n"
"v_max_u32 v48, v46, v47\n"
"v_max3_u32 v48, v44, v45, v48\n"
"v_cmp_eq_u32 vcc, -1, v48\n"
"s_cbranch_vccz .Lsk_256\n"
"s_mov_b64 exec, vcc\n"
"buffer_load_dwordx4 v[44:47], v1, s[4:7], s47 offen offset:0 sc1\n"
"s_mov_b64 exec, -1\n"
".Lsk_256:\n"
"s_branch .Ltp_250\n"
".Lou_251:\n"
"s_waitcnt vmcnt(0)\n"
"s_add_u32 s46, s44, 0x145000\n"
"s_add_u32 s47, s46, 0x1000\n"
"buffer_load_dwordx4 v[28:31], v1, s[4:7], s46 offen offset:0 sc1\n"
"buffer_load_dwordx4 v[32:35], v1, s[4:7], s46 offen offset:1024 sc1\n"
"buffer_load_dwordx4 v[36:39], v1, s[4:7], s46 offen offset:2048 sc1\n"
"buffer_load_dwordx4 v[40:43], v1, s[4:7], s46 offen offset:3072 sc1\n"
"buffer_load_dwordx4 v[44:47], v1, s[4:7], s47 offen offset:0 sc1\n"
"s_mov_b32 s17, 32768\n"
".Ltp_257:\n"
"s_waitcnt vmcnt(0)\n"
"s_sub_u32 s17, s17, 1\n"
"s_cbranch_scc1 .Lou_258\n"
"v_max_u32 v48, v28, v29\n"
"v_max3_u32 v48, v30, v31, v48\n"
"v_max3_u32 v48, v32, v33, v48\n"
"v_max3_u32 v48, v34, v35, v48\n"
"v_max3_u32 v48, v36, v37, v48\n"
"v_max3_u32 v48, v38, v39, v48\n"
"v_max3_u32 v48, v40, v41, v48\n"
"v_max3_u32 v48, v42, v43, v48\n"
"v_max3_u32 v48, v44, v45, v48\n"
"v_max3_u32 v48, v46, v47, v48\n"
"v_cmp_eq_u32 vcc, -1, v48\n"
"s_cbranch_vccz .Lou_258\n"
"v_max_u32 v48, v30, v31\n"
"v_max3_u32 v48, v28, v29, v48\n"
"v_cmp_eq_u32 vcc, -1, v48\n"
"s_cbranch_vccz .Lsk_259\n"
"s_mov_b64 exec, vcc\n"
"buffer_load_dwordx4 v[28:31], v1, s[4:7], s46 offen offset:0 sc1\n"
"s_mov_b64 exec, -1\n"
".Lsk_259:\n"
"v_max_u32 v48, v34, v35\n"
"v_max3_u32 v48, v32, v33, v48\n"
"v_cmp_eq_u32 vcc, -1, v48\n"
"s_cbranch_vccz .Lsk_260\n"
"s_mov_b64 exec, vcc\n"
"buffer_load_dwordx4 v[32:35], v1, s[4:7], s46 offen offset:1024 sc1\n"
"s_mov_b64 exec, -1\n"
".Lsk_260:\n"
"v_max_u32 v48, v38, v39\n"
"v_max3_u32 v48, v36, v37, v48\n"
"v_cmp_eq_u32 vcc, -1, v48\n"
"s_cbranch_vccz .Lsk_261\n"
"s_mov_b64 exec, vcc\n"
"buffer_load_dwordx4 v[36:39], v1, s[4:7], s46 offen offset:2048 sc1\n"
"s_mov_b64 exec, -1\n"
".Lsk_261:\n"
"v_max_u32 v48, v42, v43\n"
"v_max3_u32 v48, v40, v41, v48\n"
"v_cmp_eq_u32 vcc, -1, v48\n"
"s_cbranch_vccz .Lsk_262\n"
"s_mov_b64 exec, vcc\n"
"buffer_load_dwordx4 v[40:43], v1, s[4:7], s46 offen offset:3072 sc1\n"
"s_mov_b64 exec, -1\n"
".Lsk_262:\n"
"v_max_u32 v48, v46, v47\n"
"v_max3_u32 v48, v44, v45, v48\n"
"v_cmp_eq_u32 vcc, -1, v48\n"
"s_cbranch_vccz .Lsk_263\n"
"s_mov_b64 exec, vcc\n"
"buffer_load_dwordx4 v[44:47], v1, s[4:7], s47 offen offset:0 sc1\n"
"s_mov_b64 exec, -1\n"
".Lsk_263:\n"
"s_branch .Ltp_257\n"
".Lou_258:\n"
"s_waitcnt vmcnt(0)\n"
"s_add_u32 s46, s44, 0x14a000\n"
"s_add_u32 s47, s46, 0x1000\n"
"buffer_load_dwordx4 v[28:31], v1, s[4:7], s46 offen offset:0 sc1\n"
"buffer_load_dwordx4 v[32:35], v1, s[4:7], s46 offen offset:1024 sc1\n"
"buffer_load_dwordx4 v[36:39], v1, s[4:7], s46 offen offset:2048 sc1\n"
"buffer_load_dwordx4 v[40:43], v1, s[4:7], s46 offen offset:3072 sc1\n"
"buffer_load_dwordx4 v[44:47], v1, s[4:7], s47 offen offset:0 sc1\n"
"s_mov_b32 s17, 32768\n"
".Ltp_264:\n"
"s_waitcnt vmcnt(0)\n"
"s_sub_u32 s17, s17, 1\n"
"s_cbranch_scc1 .Lou_265\n"
"v_max_u32 v48, v28, v29\n"
"v_max3_u32 v48, v30, v31, v48\n"
"v_max3_u32 v48, v32, v33, v48\n"
"v_max3_u32 v48, v34, v35, v48\n"
"v_max3_u32 v48, v36, v37, v48\n"
"v_max3_u32 v48, v38, v39, v48\n"
"v_max3_u32 v48, v40, v41, v48\n"
"v_max3_u32 v48, v42, v43, v48\n"
"v_max3_u32 v48, v44, v45, v48\n"
"v_max3_u32 v48, v46, v47, v48\n"
"v_cmp_eq_u32 vcc, -1, v48\n"
"s_cbranch_vccz .Lou_265\n"
"v_max_u32 v48, v30, v31\n"
"v_max3_u32 v48, v28, v29, v48\n"
"v_cmp_eq_u32 vcc, -1, v48\n"
"s_cbranch_vccz .Lsk_266\n"
"s_mov_b64 exec, vcc\n"
"buffer_load_dwordx4 v[28:31], v1, s[4:7], s46 offen offset:0 sc1\n"
"s_mov_b64 exec, -1\n"
".Lsk_266:\n"
"v_max_u32 v48, v34, v35\n"
"v_max3_u32 v48, v32, v33, v48\n"
"v_cmp_eq_u32 vcc, -1, v48\n"
"s_cbranch_vccz .Lsk_267\n"
"s_mov_b64 exec, vcc\n"
"buffer_load_dwordx4 v[32:35], v1, s[4:7], s46 offen offset:1024 sc1\n"
"s_mov_b64 exec, -1\n"
".Lsk_267:\n"
"v_max_u32 v48, v38, v39\n"
"v_max3_u32 v48, v36, v37, v48\n"
"v_cmp_eq_u32 vcc, -1, v48\n"
"s_cbranch_vccz .Lsk_268\n"
"s_mov_b64 exec, vcc\n"
"buffer_load_dwordx4 v[36:39], v1, s[4:7], s46 offen offset:2048 sc1\n"
"s_mov_b64 exec, -1\n"
".Lsk_268:\n"
"v_max_u32 v48, v42, v43\n"
"v_max3_u32 v48, v40, v41, v48\n"
"v_cmp_eq_u32 vcc, -1, v48\n"
"s_cbranch_vccz .Lsk_269\n"
"s_mov_b64 exec, vcc\n"
"buffer_load_dwordx4 v[40:43], v1, s[4:7], s46 offen offset:3072 sc1\n"
"s_mov_b64 exec, -1\n"
".Lsk_269:\n"
"v_max_u32 v48, v46, v47\n"
"v_max3_u32 v48, v44, v45, v48\n"
"v_cmp_eq_u32 vcc, -1, v48\n"
"s_cbranch_vccz .Lsk_270\n"
"s_mov_b64 exec, vcc\n"
"buffer_load_dwordx4 v[44:47], v1, s[4:7], s47 offen offset:0 sc1\n"
"s_mov_b64 exec, -1\n"
".Lsk_270:\n"
"s_branch .Ltp_264\n"
".Lou_265:\n"
"s_waitcnt vmcnt(0)\n"
"s_add_u32 s46, s44, 0x14f000\n"
"s_add_u32 s47, s46, 0x1000\n"
"buffer_load_dwordx4 v[28:31], v1, s[4:7], s46 offen offset:0 sc1\n"
"buffer_load_dwordx4 v[32:35], v1, s[4:7], s46 offen offset:1024 sc1\n"
"buffer_load_dwordx4 v[36:39], v1, s[4:7], s46 offen offset:2048 sc1\n"
"buffer_load_dwordx4 v[40:43], v1, s[4:7], s46 offen offset:3072 sc1\n"
"buffer_load_dwordx4 v[44:47], v1, s[4:7], s47 offen offset:0 sc1\n"
"s_mov_b32 s17, 32768\n"
".Ltp_271:\n"
"s_waitcnt vmcnt(0)\n"
"s_sub_u32 s17, s17, 1\n"
"s_cbranch_scc1 .Lou_272\n"
"v_max_u32 v48, v28, v29\n"
"v_max3_u32 v48, v30, v31, v48\n"
"v_max3_u32 v48, v32, v33, v48\n"
"v_max3_u32 v48, v34, v35, v48\n"
"v_max3_u32 v48, v36, v37, v48\n"
"v_max3_u32 v48, v38, v39, v48\n"
"v_max3_u32 v48, v40, v41, v48\n"
"v_max3_u32 v48, v42, v43, v48\n"
"v_max3_u32 v48, v44, v45, v48\n"
"v_max3_u32 v48, v46, v47, v48\n"
"v_cmp_eq_u32 vcc, -1, v48\n"
"s_cbranch_vccz .Lou_272\n"
"v_max_u32 v48, v30, v31\n"
"v_max3_u32 v48, v28, v29, v48\n"
"v_cmp_eq_u32 vcc, -1, v48\n"
"s_cbranch_vccz .Lsk_273\n"
"s_mov_b64 exec, vcc\n"
"buffer_load_dwordx4 v[28:31], v1, s[4:7], s46 offen offset:0 sc1\n"
"s_mov_b64 exec, -1\n"
".Lsk_273:\n"
"v_max_u32 v48, v34, v35\n"
"v_max3_u32 v48, v32, v33, v48\n"
"v_cmp_eq_u32 vcc, -1, v48\n"
"s_cbranch_vccz .Lsk_274\n"
"s_mov_b64 exec, vcc\n"
"buffer_load_dwordx4 v[32:35], v1, s[4:7], s46 offen offset:1024 sc1\n"
"s_mov_b64 exec, -1\n"
".Lsk_274:\n"
"v_max_u32 v48, v38, v39\n"
"v_max3_u32 v48, v36, v37, v48\n"
"v_cmp_eq_u32 vcc, -1, v48\n"
"s_cbranch_vccz .Lsk_275\n"
"s_mov_b64 exec, vcc\n"
"buffer_load_dwordx4 v[36:39], v1, s[4:7], s46 offen offset:2048 sc1\n"
"s_mov_b64 exec, -1\n"
".Lsk_275:\n"
"v_max_u32 v48, v42, v43\n"
"v_max3_u32 v48, v40, v41, v48\n"
"v_cmp_eq_u32 vcc, -1, v48\n"
"s_cbranch_vccz .Lsk_276\n"
"s_mov_b64 exec, vcc\n"
"buffer_load_dwordx4 v[40:43], v1, s[4:7], s46 offen offset:3072 sc1\n"
"s_mov_b64 exec, -1\n"
".Lsk_276:\n"
"v_max_u32 v48, v46, v47\n"
"v_max3_u32 v48, v44, v45, v48\n"
"v_cmp_eq_u32 vcc, -1, v48\n"
"s_cbranch_vccz .Lsk_277\n"
"s_mov_b64 exec, vcc\n"
"buffer_load_dwordx4 v[44:47], v1, s[4:7], s47 offen offset:0 sc1\n"
"s_mov_b64 exec, -1\n"
".Lsk_277:\n"
"s_branch .Ltp_271\n"
".Lou_272:\n"
"s_waitcnt vmcnt(0)\n"
"ds_write_b32 v13, v13 offset:448\n"
"s_mov_b32 s17, 0x100000\n"
".Ltp_278:\n"
"ds_read_b32 v90, v14 offset:448\n"
"s_sub_u32 s17, s17, 1\n"
"s_cbranch_scc1 .Lou_279\n"
"s_waitcnt lgkmcnt(0)\n"
"v_cmp_eq_u32 vcc, 0, v90\n"
"s_cbranch_vccnz .Ltp_278\n"
".Lou_279:\n"
"s_waitcnt lgkmcnt(0)\n"
"v_mov_b32 v90, 0\n"
"v_mov_b32 v90, -1\n"
"ds_write_b32 v9, v90 offset:3712\n"
"ds_write_b32 v9, v90 offset:4032\n"
"ds_write_b32 v9, v90 offset:4352\n"
"ds_write_b32 v9, v90 offset:4672\n"
"ds_write_b32 v9, v90 offset:4992\n"
"ds_write_b32 v10, v90 offset:5312\n"
"s_add_u32 m0, s29, 0xa000\n"
"s_add_u32 s31, s44, 0x140000\n"
"buffer_load_dwordx4 v1, s[4:7], s31 offen offset:0 sc1 lds\n"
"buffer_load_dwordx4 v1, s[4:7], s31 offen offset:1024 sc1 lds\n"
"buffer_load_dwordx4 v1, s[4:7], s31 offen offset:2048 sc1 lds\n"
"buffer_load_dwordx4 v1, s[4:7], s31 offen offset:3072 sc1 lds\n"
"s_add_u32 m0, m0, 1024\n"
"s_add_u32 s31, s31, 1024\n"
"buffer_load_dwordx4 v1, s[4:7], s31 offen offset:3072 sc1 lds\n"
"s_add_u32 m0, s29, 0xf000\n"
"s_add_u32 s31, s44, 0x145000\n"
"buffer_load_dwordx4 v1, s[4:7], s31 offen offset:0 sc1 lds\n"
"buffer_load_dwordx4 v1, s[4:7], s31 offen offset:1024 sc1 lds\n"
"buffer_load_dwordx4 v1, s[4:7], s31 offen offset:2048 sc1 lds\n"
"buffer_load_dwordx4 v1, s[4:7], s31 offen offset:3072 sc1 lds\n"
"s_add_u32 m0, m0, 1024\n"
"s_add_u32 s31, s31, 1024\n"
"buffer_load_dwordx4 v1, s[4:7], s31 offen offset:3072 sc1 lds\n"
"s_add_u32 m0, s29, 0\n"
"s_add_u32 s31, s44, 0x14a000\n"
"buffer_load_dwordx4 v1, s[4:7], s31 offen offset:0 sc1 lds\n"
"buffer_load_dwordx4 v1, s[4:7], s31 offen offset:1024 sc1 lds\n"
"buffer_load_dwordx4 v1, s[4:7], s31 offen offset:2048 sc1 lds\n"
"buffer_load_dwordx4 v1, s[4:7], s31 offen offset:3072 sc1 lds\n"
"s_add_u32 m0, m0, 1024\n"
"s_add_u32 s31, s31, 1024\n"
"buffer_load_dwordx4 v1, s[4:7], s31 offen offset:3072 sc1 lds\n"
"s_add_u32 m0, s29, 0x5000\n"
"s_add_u32 s31, s44, 0x14f000\n"
"buffer_load_dwordx4 v1, s[4:7], s31 offen offset:0 sc1 lds\n"
"buffer_load_dwordx4 v1, s[4:7], s31 offen offset:1024 sc1 lds\n"
"buffer_load_dwordx4 v1, s[4:7], s31 offen offset:2048 sc1 lds\n"
"buffer_load_dwordx4 v1, s[4:7], s31 offen offset:3072 sc1 lds\n"
"s_add_u32 m0, m0, 1024\n"
"s_add_u32 s31, s31, 1024\n"
"buffer_load_dwordx4 v1, s[4:7], s31 offen offset:3072 sc1 lds\n"
"s_waitcnt vmcnt(0)\n"
"ds_write_b32 v13, v13 offset:464\n"
"s_mov_b32 s17, 0x100000\n"
".Ltp_280:\n"
"ds_read_b32 v90, v14 offset:464\n"
"s_sub_u32 s17, s17, 1\n"
"s_cbranch_scc1 .Lou_281\n"
"s_waitcnt lgkmcnt(0)\n"
"v_cmp_eq_u32 vcc, 0, v90\n"
"s_cbranch_vccnz .Ltp_280\n"
".Lou_281:\n"
"s_waitcnt lgkmcnt(0)\n"
"v_mov_b32 v90, 0\n"
"s_branch .Lag_201\n"
".Lsl_304:\n"
"s_nop 7\n"
"s_mov_b32 s17, 0x100000\n"
".Ltp_306:\n"
"ds_read_b32 v42, v14 offset:528\n"
"s_sub_u32 s17, s17, 1\n"
"s_cbranch_scc1 .Lou_307\n"
"s_waitcnt lgkmcnt(0)\n"
"v_cmp_eq_u32 vcc, 0, v42\n"
"s_cbranch_vccnz .Ltp_306\n"
".Lou_307:\n"
"ds_read_b64_tr_b16 v[224:225], v31 offset:0\n"
"ds_read_b64_tr_b16 v[226:227], v32 offset:0\n"
"s_waitcnt lgkmcnt(0)\n"
"s_branch .Lbk_305\n"
".L_xend:\n"
"v_and_b32 v0, 63, v0\n"
"s_and_b32 s3, s3, 3\n"
"s_and_b32 s79, s2, 7\n"
"s_lshl_b32 s79, s79, 4\n"
"s_lshr_b32 s78, s2, 3\n"
"s_add_u32 s79, s79, s78\n"
"s_and_b32 s78, s2, 3\n"
"s_lshl_b32 s78, s78, 3\n"
"s_and_b32 s77, s2, 4\n"
"s_cmp_eq_i32 s77, 4\n"
"s_cselect_b32 s78, 32, s78\n"
"s_add_u32 s79, s79, s78\n"
"s_and_b32 s78, s2, 4\n"
"s_lshl_b32 s78, s78, 4\n"
"s_add_u32 s78, s2, s78\n"
"s_cmp_lt_i32 s78, 0xc0\n"
"s_cselect_b32 s18, s79, -1\n"
"s_and_b32 s79, s2, 7\n"
"s_lshl_b32 s79, s79, 4\n"
"s_lshr_b32 s78, s2, 3\n"
"s_add_u32 s79, s79, s78\n"
"s_and_b32 s78, s2, 3\n"
"s_lshl_b32 s78, s78, 3\n"
"s_add_u32 s78, s78, 24\n"
"s_and_b32 s77, s2, 4\n"
"s_cmp_eq_i32 s77, 4\n"
"s_cselect_b32 s78, 48, s78\n"
"s_sub_u32 s79, s79, s78\n"
"s_and_b32 s78, s2, 4\n"
"s_lshl_b32 s78, s78, 4\n"
"s_add_u32 s78, s2, s78\n"
"s_cmp_lt_i32 s78, 0xc0\n"
"s_cselect_b32 s19, 0x3e8, s79\n"
"v_lshlrev_b32 v2, 4, v0\n"
"s_mov_b32 s4, s26\n"
"s_mov_b32 s5, s27\n"
"s_mov_b32 s6, 0x1688000\n"
"s_mov_b32 s7, 0x20000\n"
"s_mul_i32 s79, s3, 0x2100\n"
"s_add_u32 s74, s79, 0x14000\n"
"v_lshl_add_u32 v152, v0, 2, s74\n"
"v_add_u32 v153, 0x440, v152\n"
"v_add_u32 v154, 0x880, v152\n"
"v_add_u32 v155, 0xcc0, v152\n"
"v_and_b32 v99, 15, v0\n"
"v_and_b32 v98, 48, v0\n"
"s_mov_b32 s79, 0x110\n"
"v_mad_i32_i24 v99, v99, s79, v98\n"
"v_add_u32 v141, s74, v99\n"
"s_lshl_b32 s79, s3, 2\n"
"s_add_u32 s79, s79, 0x26400\n"
"v_mov_b32 v99, s79\n"
"s_mov_b32 vcc_lo, 1\n"
"s_mov_b32 vcc_hi, 0\n"
"v_cndmask_b32 v4, 2.0, v99, vcc\n"
"v_and_b32 v99, 3, v0\n"
"v_lshlrev_b32 v99, 2, v99\n"
"v_add_u32 v5, 0x26400, v99\n"
"v_min_i32 v99, 49, v0\n"
"v_lshlrev_b32 v38, 5, v99\n"
"v_mov_b32 v128, 0\n"
"v_mov_b32 v129, 0\n"
"v_mov_b32 v130, 0\n"
"v_mov_b32 v131, 0\n"
"v_mov_b32 v132, 0\n"
"v_mov_b32 v133, 0\n"
"v_mov_b32 v134, 0\n"
"v_mov_b32 v135, 0\n"
"v_mov_b32 v136, 0\n"
"v_mov_b32 v137, 0\n"
"v_mov_b32 v138, 0\n"
"v_mov_b32 v139, 0\n"
"s_waitcnt lgkmcnt(0)\n"
"s_and_b32 s79, s3, 1\n"
"s_lshl_b32 s60, s79, 4\n"
"s_lshr_b32 s40, s40, s60\n"
"s_lshr_b32 s41, s41, s60\n"
"v_mov_b32 v142, s40\n"
"v_mov_b32 v140, s41\n"
"s_mul_i32 s16, s16, 0x2c9277b5\n"
"s_add_u32 s16, s16, 0xac564b05\n"
"s_cmp_lt_i32 s18, 0\n"
"s_cbranch_scc1 .Lnk_317\n"
"s_setprio 0\n"
"s_mov_b32 s55, 2\n"
"s_mov_b32 s52, 0xc2f00000\n"
"s_mov_b32 s53, 0x3fb8aa3b\n"
"s_mov_b32 s54, 0x3f317218\n"
"v_mov_b32 v31, 0x42f00000\n"
"v_lshlrev_b32 v142, 16, v142\n"
"v_med3_f32 v142, v142, s52, v31\n"
"v_mul_f32 v28, s53, v142\n"
"v_exp_f32 v29, v28\n"
"v_fma_f32 v28, v142, s53, -v28\n"
"v_fmac_f32 v28, 0x32a57060, v142\n"
"v_fma_f32 v28, v28, s54, 1.0\n"
"v_mul_f32 v142, v29, v28\n"
"v_cvt_pk_bf16_f32 v142, v142, v142\n"
"v_lshlrev_b32 v142, 16, v142\n"
"s_waitcnt vmcnt(8)\n"
"s_nop 3\n"
".Lkg_324:\n"
"v_lshlrev_b32 v32, 16, v32\n"
"v_lshlrev_b32 v33, 16, v33\n"
"v_lshlrev_b32 v34, 16, v34\n"
"v_lshlrev_b32 v35, 16, v35\n"
"v_mul_f32 v26, v142, v34\n"
"v_mul_f32 v27, v142, v35\n"
"v_max3_f32 v30, |v26|, |v27|, 0\n"
"s_nop 1\n"
"v_max_u32_dpp v30, v30, v30 row_ror:8 row_mask:0xf bank_mask:0xf\n"
"v_med3_f32 v32, v32, s52, v31\n"
"s_nop 0\n"
"v_max_u32_dpp v30, v30, v30 row_ror:4 row_mask:0xf bank_mask:0xf\n"
"v_mul_f32 v28, s53, v32\n"
"s_nop 0\n"
"v_max_u32_dpp v30, v30, v30 row_ror:2 row_mask:0xf bank_mask:0xf\n"
"v_exp_f32 v29, v28\n"
"s_nop 0\n"
"v_max_u32_dpp v30, v30, v30 row_ror:1 row_mask:0xf bank_mask:0xf\n"
"v_fma_f32 v28, v32, s53, -v28\n"
"v_fmac_f32 v28, 0x32a57060, v32\n"
"v_mov_b32 v34, v30\n"
"v_fma_f32 v28, v28, s54, 1.0\n"
"s_nop 0\n"
"v_permlane16_swap_b32 v30, v34\n"
"v_mul_f32 v32, v29, v28\n"
"v_max_u32 v30, v30, v34\n"
"v_cvt_pk_bf16_f32 v32, v32, v32\n"
"v_lshlrev_b32 v32, 16, v32\n"
"v_mov_b32 v34, v30\n"
"v_mul_f32 v25, v142, v33\n"
"s_nop 0\n"
"v_permlane32_swap_b32 v30, v34\n"
"v_mul_f32 v24, v142, -v32\n"
"v_max_u32 v30, v30, v34\n"
"v_mul_f32 v25, 0.15915494, v25\n"
"v_mul_f32 v24, s53, v24\n"
"v_readfirstlane_b32 s51, v30\n"
"s_nop 2\n"
"s_bfe_u32 s50, s51, 0x80017\n"
"s_max_i32 s50, s50, 37\n"
"s_min_i32 s50, s50, 217\n"
"s_cmp_gt_i32 s51, 0\n"
"s_cselect_b32 s50, s50, 127\n"
"s_sub_u32 s51, 267, s50\n"
"s_lshl_b32 s51, s51, 23\n"
"s_sub_u32 s50, s50, 27\n"
"s_lshl_b32 s50, s50, 23\n"
"v_mul_f32 v26, s51, v26\n"
"v_mul_f32 v27, s51, v27\n"
"v_mul_f32 v111, 0x41800000, v25\n"
"v_mul_f32 v100, 0x41800000, v24\n"
"v_fract_f32 v111, v111\n"
"v_exp_f32 v100, v100\n"
"v_cos_f32 v110, v111\n"
"v_sin_f32 v111, -v111\n"
"v_mul_f32 v110, v110, v100\n"
"v_mul_f32 v111, v111, v100\n"
"v_fract_f32 v113, v25\n"
"v_exp_f32 v100, v24\n"
"v_cos_f32 v112, v113\n"
"v_sin_f32 v113, v113\n"
"v_mul_f32 v112, v112, v100\n"
"v_mul_f32 v113, v113, v100\n"
"v_mul_f32 v115, 0x43800000, v25\n"
"v_mul_f32 v100, 0x43800000, v24\n"
"v_fract_f32 v115, v115\n"
"v_exp_f32 v100, v100\n"
"v_cos_f32 v114, v115\n"
"v_sin_f32 v115, v115\n"
"v_mul_f32 v114, v114, v100\n"
"v_mul_f32 v115, v115, v100\n"
"v_mul_f32 v116, v111, v111\n"
"v_add_f32 v117, v110, v110\n"
"v_fma_f32 v116, v110, v110, -v116\n"
"v_mul_f32 v117, v117, v111\n"
"v_mul_f32 v120, v117, v117\n"
"v_add_f32 v121, v116, v116\n"
"v_fma_f32 v120, v116, v116, -v120\n"
"v_mul_f32 v121, v121, v117\n"
"v_mul_f32 v118, v117, v111\n"
"v_mul_f32 v119, v116, v111\n"
"v_fma_f32 v118, v116, v110, -v118\n"
"v_fmac_f32 v119, v117, v110\n"
"v_mul_f32 v122, v121, v121\n"
"v_add_f32 v123, v120, v120\n"
"v_fma_f32 v122, v120, v120, -v122\n"
"v_mul_f32 v123, v123, v121\n"
"v_mov_b32 v84, 0x7400\n"
"v_mul_f32 v124, 0x46800000, v110\n"
"v_mul_f32 v125, 0x46800000, v111\n"
"v_cvt_pk_f16_f32 v100, v124, v125\n"
"v_mul_f32 v124, 0x46800000, v116\n"
"v_mul_f32 v125, 0x46800000, v117\n"
"v_cvt_pk_f16_f32 v85, v124, v125\n"
"v_mul_f32 v124, 0x46800000, v118\n"
"v_mul_f32 v125, 0x46800000, v119\n"
"v_cvt_pk_f16_f32 v101, v124, v125\n"
"v_cvt_pk_f16_f32 v93, v120, v121\n"
"v_cvt_pk_f16_f32 v94, v122, v123\n"
"v_mul_f32 v124, v123, v121\n"
"v_mul_f32 v125, v122, v121\n"
"v_fma_f32 v124, v122, v120, -v124\n"
"v_fmac_f32 v125, v123, v120\n"
"v_cvt_pk_f16_f32 v95, v124, v125\n"
"ds_write2_b32 v152, v84, v100 offset0:0 offset1:68\n"
"v_pk_mul_f16 v86, v84, v93 op_sel:[0,0] op_sel_hi:[1,0]\n"
"v_pk_mul_f16 v102, v100, v93 op_sel:[0,0] op_sel_hi:[1,0]\n"
"v_pk_fma_f16 v86, v84, v93, v86 op_sel:[1,1,0] op_sel_hi:[0,1,1] neg_lo:[1,0,0]\n"
"v_pk_fma_f16 v102, v100, v93, v102 op_sel:[1,1,0] op_sel_hi:[0,1,1] neg_lo:[1,0,0]\n"
"ds_write2_b32 v152, v85, v101 offset0:136 offset1:204\n"
"v_pk_mul_f16 v87, v85, v93 op_sel:[0,0] op_sel_hi:[1,0]\n"
"v_pk_mul_f16 v103, v101, v93 op_sel:[0,0] op_sel_hi:[1,0]\n"
"v_pk_fma_f16 v87, v85, v93, v87 op_sel:[1,1,0] op_sel_hi:[0,1,1] neg_lo:[1,0,0]\n"
"v_pk_fma_f16 v103, v101, v93, v103 op_sel:[1,1,0] op_sel_hi:[0,1,1] neg_lo:[1,0,0]\n"
"ds_write2_b32 v153, v86, v102 offset0:0 offset1:68\n"
"v_pk_mul_f16 v88, v84, v94 op_sel:[0,0] op_sel_hi:[1,0]\n"
"v_pk_mul_f16 v102, v100, v94 op_sel:[0,0] op_sel_hi:[1,0]\n"
"v_pk_fma_f16 v88, v84, v94, v88 op_sel:[1,1,0] op_sel_hi:[0,1,1] neg_lo:[1,0,0]\n"
"v_pk_fma_f16 v102, v100, v94, v102 op_sel:[1,1,0] op_sel_hi:[0,1,1] neg_lo:[1,0,0]\n"
"ds_write2_b32 v153, v87, v103 offset0:136 offset1:204\n"
"v_pk_mul_f16 v89, v85, v94 op_sel:[0,0] op_sel_hi:[1,0]\n"
"v_pk_mul_f16 v103, v101, v94 op_sel:[0,0] op_sel_hi:[1,0]\n"
"v_pk_fma_f16 v89, v85, v94, v89 op_sel:[1,1,0] op_sel_hi:[0,1,1] neg_lo:[1,0,0]\n"
"v_pk_fma_f16 v103, v101, v94, v103 op_sel:[1,1,0] op_sel_hi:[0,1,1] neg_lo:[1,0,0]\n"
"ds_write2_b32 v154, v88, v102 offset0:0 offset1:68\n"
"v_pk_mul_f16 v90, v84, v95 op_sel:[0,0] op_sel_hi:[1,0]\n"
"v_pk_mul_f16 v102, v100, v95 op_sel:[0,0] op_sel_hi:[1,0]\n"
"v_pk_fma_f16 v90, v84, v95, v90 op_sel:[1,1,0] op_sel_hi:[0,1,1] neg_lo:[1,0,0]\n"
"v_pk_fma_f16 v102, v100, v95, v102 op_sel:[1,1,0] op_sel_hi:[0,1,1] neg_lo:[1,0,0]\n"
"ds_write2_b32 v154, v89, v103 offset0:136 offset1:204\n"
"v_pk_mul_f16 v91, v85, v95 op_sel:[0,0] op_sel_hi:[1,0]\n"
"v_pk_mul_f16 v103, v101, v95 op_sel:[0,0] op_sel_hi:[1,0]\n"
"v_pk_fma_f16 v91, v85, v95, v91 op_sel:[1,1,0] op_sel_hi:[0,1,1] neg_lo:[1,0,0]\n"
"v_pk_fma_f16 v103, v101, v95, v103 op_sel:[1,1,0] op_sel_hi:[0,1,1] neg_lo:[1,0,0]\n"
"ds_write2_b32 v155, v90, v102 offset0:0 offset1:68\n"
"ds_write2_b32 v155, v91, v103 offset0:136 offset1:204\n"
"ds_read_b128 v[52:55], v141 offset:0\n"
"ds_read_b128 v[56:59], v141 offset:64\n"
"ds_read_b128 v[60:63], v141 offset:128\n"
"ds_read_b128 v[64:67], v141 offset:192\n"
"v_mul_f32 v116, v113, v113\n"
"v_add_f32 v117, v112, v112\n"
"v_fma_f32 v116, v112, v112, -v116\n"
"v_mul_f32 v117, v117, v113\n"
"v_mul_f32 v120, v117, v117\n"
"v_add_f32 v121, v116, v116\n"
"v_fma_f32 v120, v116, v116, -v120\n"
"v_mul_f32 v121, v121, v117\n"
"v_mul_f32 v118, v117, v113\n"
"v_mul_f32 v119, v116, v113\n"
"v_fma_f32 v118, v116, v112, -v118\n"
"v_fmac_f32 v119, v117, v112\n"
"v_mul_f32 v122, v121, v121\n"
"v_add_f32 v123, v120, v120\n"
"v_fma_f32 v122, v120, v120, -v122\n"
"v_mul_f32 v123, v123, v121\n"
"v_cvt_pk_f16_f32 v84, v26, v27\n"
"v_mul_f32 v124, v27, v113\n"
"v_mul_f32 v125, v26, v113\n"
"v_fma_f32 v124, v26, v112, -v124\n"
"v_fmac_f32 v125, v27, v112\n"
"v_cvt_pk_f16_f32 v100, v124, v125\n"
"v_mul_f32 v124, v27, v117\n"
"v_mul_f32 v125, v26, v117\n"
"v_fma_f32 v124, v26, v116, -v124\n"
"v_fmac_f32 v125, v27, v116\n"
"v_cvt_pk_f16_f32 v85, v124, v125\n"
"v_mul_f32 v124, v27, v119\n"
"v_mul_f32 v125, v26, v119\n"
"v_fma_f32 v124, v26, v118, -v124\n"
"v_fmac_f32 v125, v27, v118\n"
"v_cvt_pk_f16_f32 v101, v124, v125\n"
"v_cvt_pk_f16_f32 v93, v120, v121\n"
"v_cvt_pk_f16_f32 v94, v122, v123\n"
"v_mul_f32 v124, v123, v121\n"
"v_mul_f32 v125, v122, v121\n"
"v_fma_f32 v124, v122, v120, -v124\n"
"v_fmac_f32 v125, v123, v120\n"
"v_cvt_pk_f16_f32 v95, v124, v125\n"
"ds_write2_b32 v152, v84, v100 offset0:0 offset1:68\n"
"v_pk_mul_f16 v86, v84, v93 op_sel:[0,0] op_sel_hi:[1,0]\n"
"v_pk_mul_f16 v102, v100, v93 op_sel:[0,0] op_sel_hi:[1,0]\n"
"v_pk_fma_f16 v86, v84, v93, v86 op_sel:[1,1,0] op_sel_hi:[0,1,1] neg_lo:[1,0,0]\n"
"v_pk_fma_f16 v102, v100, v93, v102 op_sel:[1,1,0] op_sel_hi:[0,1,1] neg_lo:[1,0,0]\n"
"ds_write2_b32 v152, v85, v101 offset0:136 offset1:204\n"
"v_pk_mul_f16 v87, v85, v93 op_sel:[0,0] op_sel_hi:[1,0]\n"
"v_pk_mul_f16 v103, v101, v93 op_sel:[0,0] op_sel_hi:[1,0]\n"
"v_pk_fma_f16 v87, v85, v93, v87 op_sel:[1,1,0] op_sel_hi:[0,1,1] neg_lo:[1,0,0]\n"
"v_pk_fma_f16 v103, v101, v93, v103 op_sel:[1,1,0] op_sel_hi:[0,1,1] neg_lo:[1,0,0]\n"
"ds_write2_b32 v153, v86, v102 offset0:0 offset1:68\n"
"v_pk_mul_f16 v88, v84, v94 op_sel:[0,0] op_sel_hi:[1,0]\n"
"v_pk_mul_f16 v102, v100, v94 op_sel:[0,0] op_sel_hi:[1,0]\n"
"v_pk_fma_f16 v88, v84, v94, v88 op_sel:[1,1,0] op_sel_hi:[0,1,1] neg_lo:[1,0,0]\n"
"v_pk_fma_f16 v102, v100, v94, v102 op_sel:[1,1,0] op_sel_hi:[0,1,1] neg_lo:[1,0,0]\n"
"ds_write2_b32 v153, v87, v103 offset0:136 offset1:204\n"
"v_pk_mul_f16 v89, v85, v94 op_sel:[0,0] op_sel_hi:[1,0]\n"
"v_pk_mul_f16 v103, v101, v94 op_sel:[0,0] op_sel_hi:[1,0]\n"
"v_pk_fma_f16 v89, v85, v94, v89 op_sel:[1,1,0] op_sel_hi:[0,1,1] neg_lo:[1,0,0]\n"
"v_pk_fma_f16 v103, v101, v94, v103 op_sel:[1,1,0] op_sel_hi:[0,1,1] neg_lo:[1,0,0]\n"
"ds_write2_b32 v154, v88, v102 offset0:0 offset1:68\n"
"v_pk_mul_f16 v90, v84, v95 op_sel:[0,0] op_sel_hi:[1,0]\n"
"v_pk_mul_f16 v102, v100, v95 op_sel:[0,0] op_sel_hi:[1,0]\n"
"v_pk_fma_f16 v90, v84, v95, v90 op_sel:[1,1,0] op_sel_hi:[0,1,1] neg_lo:[1,0,0]\n"
"v_pk_fma_f16 v102, v100, v95, v102 op_sel:[1,1,0] op_sel_hi:[0,1,1] neg_lo:[1,0,0]\n"
"ds_write2_b32 v154, v89, v103 offset0:136 offset1:204\n"
"v_pk_mul_f16 v91, v85, v95 op_sel:[0,0] op_sel_hi:[1,0]\n"
"v_pk_mul_f16 v103, v101, v95 op_sel:[0,0] op_sel_hi:[1,0]\n"
"v_pk_fma_f16 v91, v85, v95, v91 op_sel:[1,1,0] op_sel_hi:[0,1,1] neg_lo:[1,0,0]\n"
"v_pk_fma_f16 v103, v101, v95, v103 op_sel:[1,1,0] op_sel_hi:[0,1,1] neg_lo:[1,0,0]\n"
"ds_write2_b32 v155, v90, v102 offset0:0 offset1:68\n"
"ds_write2_b32 v155, v91, v103 offset0:136 offset1:204\n"
"ds_read_b128 v[68:71], v141 offset:0\n"
"ds_read_b128 v[72:75], v141 offset:64\n"
"ds_read_b128 v[76:79], v141 offset:128\n"
"ds_read_b128 v[80:83], v141 offset:192\n"
"v_mul_f32 v116, v115, v115\n"
"v_add_f32 v117, v114, v114\n"
"v_fma_f32 v116, v114, v114, -v116\n"
"v_mul_f32 v117, v117, v115\n"
"v_mul_f32 v120, v117, v117\n"
"v_add_f32 v121, v116, v116\n"
"v_fma_f32 v120, v116, v116, -v120\n"
"v_mul_f32 v121, v121, v117\n"
"v_mul_f32 v118, v117, v115\n"
"v_mul_f32 v119, v116, v115\n"
"v_fma_f32 v118, v116, v114, -v118\n"
"v_fmac_f32 v119, v117, v114\n"
"v_cvt_pk_f16_f32 v92, v114, v115\n"
"v_cvt_pk_f16_f32 v93, v116, v117\n"
"v_cvt_pk_f16_f32 v95, v120, v121\n"
"v_cvt_pk_f16_f32 v94, v118, v119\n"
"v_mul_f32 v124, v121, v115\n"
"v_mul_f32 v125, v120, v115\n"
"v_fma_f32 v124, v120, v114, -v124\n"
"v_fmac_f32 v125, v121, v114\n"
"v_cvt_pk_f16_f32 v96, v124, v125\n"
"v_mul_f32 v126, v121, v117\n"
"v_mul_f32 v127, v120, v117\n"
"v_fma_f32 v126, v120, v116, -v126\n"
"v_fmac_f32 v127, v121, v116\n"
"v_cvt_pk_f16_f32 v97, v126, v127\n"
"v_mul_f32 v124, v121, v119\n"
"v_mul_f32 v125, v120, v119\n"
"v_fma_f32 v124, v120, v118, -v124\n"
"v_fmac_f32 v125, v121, v118\n"
"v_cvt_pk_f16_f32 v98, v124, v125\n"
"v_mul_f32 v126, v121, v121\n"
"v_mul_f32 v127, v120, v121\n"
"v_fma_f32 v126, v120, v120, -v126\n"
"v_fmac_f32 v127, v121, v120\n"
"v_cvt_pk_f16_f32 v99, v126, v127\n"
"s_setprio 3\n"
"s_waitcnt lgkmcnt(3)\n"
"v_mfma_f32_16x16x32_f16 v[144:147], v[68:71], v[52:55], 0\n"
"v_pk_mul_f16 v100, v84, v92 op_sel:[0,0] op_sel_hi:[1,0]\n"
"v_pk_mul_f16 v101, v85, v92 op_sel:[0,0] op_sel_hi:[1,0]\n"
"s_waitcnt lgkmcnt(2)\n"
"v_mfma_f32_16x16x32_f16 v[144:147], v[72:75], v[56:59], v[144:147]\n"
"v_pk_fma_f16 v100, v84, v92, v100 op_sel:[1,1,0] op_sel_hi:[0,1,1] neg_lo:[1,0,0]\n"
"v_pk_fma_f16 v101, v85, v92, v101 op_sel:[1,1,0] op_sel_hi:[0,1,1] neg_lo:[1,0,0]\n"
"s_waitcnt lgkmcnt(1)\n"
"v_mfma_f32_16x16x32_f16 v[144:147], v[76:79], v[60:63], v[144:147]\n"
"v_pk_mul_f16 v102, v86, v92 op_sel:[0,0] op_sel_hi:[1,0]\n"
"v_pk_mul_f16 v103, v87, v92 op_sel:[0,0] op_sel_hi:[1,0]\n"
"s_waitcnt lgkmcnt(0)\n"
"v_mfma_f32_16x16x32_f16 v[144:147], v[80:83], v[64:67], v[144:147]\n"
"s_setprio 0\n"
"v_pk_fma_f16 v102, v86, v92, v102 op_sel:[1,1,0] op_sel_hi:[0,1,1] neg_lo:[1,0,0]\n"
"v_pk_fma_f16 v103, v87, v92, v103 op_sel:[1,1,0] op_sel_hi:[0,1,1] neg_lo:[1,0,0]\n"
"ds_write2_b32 v152, v100, v101 offset0:0 offset1:68\n"
"v_pk_mul_f16 v100, v88, v92 op_sel:[0,0] op_sel_hi:[1,0]\n"
"v_pk_mul_f16 v101, v89, v92 op_sel:[0,0] op_sel_hi:[1,0]\n"
"v_pk_fma_f16 v100, v88, v92, v100 op_sel:[1,1,0] op_sel_hi:[0,1,1] neg_lo:[1,0,0]\n"
"v_pk_fma_f16 v101, v89, v92, v101 op_sel:[1,1,0] op_sel_hi:[0,1,1] neg_lo:[1,0,0]\n"
"ds_write2_b32 v152, v102, v103 offset0:136 offset1:204\n"
"v_pk_mul_f16 v102, v90, v92 op_sel:[0,0] op_sel_hi:[1,0]\n"
"v_pk_mul_f16 v103, v91, v92 op_sel:[0,0] op_sel_hi:[1,0]\n"
"v_pk_fma_f16 v102, v90, v92, v102 op_sel:[1,1,0] op_sel_hi:[0,1,1] neg_lo:[1,0,0]\n"
"v_pk_fma_f16 v103, v91, v92, v103 op_sel:[1,1,0] op_sel_hi:[0,1,1] neg_lo:[1,0,0]\n"
"ds_write2_b32 v153, v100, v101 offset0:0 offset1:68\n"
"v_pk_mul_f16 v100, v84, v93 op_sel:[0,0] op_sel_hi:[1,0]\n"
"v_pk_mul_f16 v101, v86, v93 op_sel:[0,0] op_sel_hi:[1,0]\n"
"v_pk_fma_f16 v100, v84, v93, v100 op_sel:[1,1,0] op_sel_hi:[0,1,1] neg_lo:[1,0,0]\n"
"v_pk_fma_f16 v101, v86, v93, v101 op_sel:[1,1,0] op_sel_hi:[0,1,1] neg_lo:[1,0,0]\n"
"ds_write2_b32 v153, v102, v103 offset0:136 offset1:204\n"
"v_pk_mul_f16 v102, v88, v93 op_sel:[0,0] op_sel_hi:[1,0]\n"
"v_pk_mul_f16 v103, v90, v93 op_sel:[0,0] op_sel_hi:[1,0]\n"
"v_pk_fma_f16 v102, v88, v93, v102 op_sel:[1,1,0] op_sel_hi:[0,1,1] neg_lo:[1,0,0]\n"
"v_pk_fma_f16 v103, v90, v93, v103 op_sel:[1,1,0] op_sel_hi:[0,1,1] neg_lo:[1,0,0]\n"
"ds_write2_b32 v154, v100, v101 offset0:0 offset1:68\n"
"v_pk_mul_f16 v100, v84, v94 op_sel:[0,0] op_sel_hi:[1,0]\n"
"v_pk_mul_f16 v101, v86, v94 op_sel:[0,0] op_sel_hi:[1,0]\n"
"v_pk_fma_f16 v100, v84, v94, v100 op_sel:[1,1,0] op_sel_hi:[0,1,1] neg_lo:[1,0,0]\n"
"v_pk_fma_f16 v101, v86, v94, v101 op_sel:[1,1,0] op_sel_hi:[0,1,1] neg_lo:[1,0,0]\n"
"ds_write2_b32 v154, v102, v103 offset0:136 offset1:204\n"
"v_pk_mul_f16 v102, v88, v94 op_sel:[0,0] op_sel_hi:[1,0]\n"
"v_pk_mul_f16 v103, v90, v94 op_sel:[0,0] op_sel_hi:[1,0]\n"
"v_pk_fma_f16 v102, v88, v94, v102 op_sel:[1,1,0] op_sel_hi:[0,1,1] neg_lo:[1,0,0]\n"
"v_pk_fma_f16 v103, v90, v94, v103 op_sel:[1,1,0] op_sel_hi:[0,1,1] neg_lo:[1,0,0]\n"
"ds_write2_b32 v155, v100, v101 offset0:0 offset1:68\n"
"ds_write2_b32 v155, v102, v103 offset0:136 offset1:204\n"
"ds_read_b128 v[68:71], v141 offset:0\n"
"ds_read_b128 v[72:75], v141 offset:64\n"
"ds_read_b128 v[76:79], v141 offset:128\n"
"ds_read_b128 v[80:83], v141 offset:192\n"
"v_pk_mul_f16 v100, v84, v95 op_sel:[0,0] op_sel_hi:[1,0]\n"
"v_pk_mul_f16 v101, v88, v95 op_sel:[0,0] op_sel_hi:[1,0]\n"
"v_pk_fma_f16 v100, v84, v95, v100 op_sel:[1,1,0] op_sel_hi:[0,1,1] neg_lo:[1,0,0]\n"
"v_pk_fma_f16 v101, v88, v95, v101 op_sel:[1,1,0] op_sel_hi:[0,1,1] neg_lo:[1,0,0]\n"
"v_pk_mul_f16 v102, v84, v96 op_sel:[0,0] op_sel_hi:[1,0]\n"
"v_pk_mul_f16 v103, v88, v96 op_sel:[0,0] op_sel_hi:[1,0]\n"
"v_pk_fma_f16 v102, v84, v96, v102 op_sel:[1,1,0] op_sel_hi:[0,1,1] neg_lo:[1,0,0]\n"
"v_pk_fma_f16 v103, v88, v96, v103 op_sel:[1,1,0] op_sel_hi:[0,1,1] neg_lo:[1,0,0]\n"
"v_pk_mul_f16 v104, v84, v97 op_sel:[0,0] op_sel_hi:[1,0]\n"
"v_pk_mul_f16 v105, v88, v97 op_sel:[0,0] op_sel_hi:[1,0]\n"
"v_pk_fma_f16 v104, v84, v97, v104 op_sel:[1,1,0] op_sel_hi:[0,1,1] neg_lo:[1,0,0]\n"
"v_pk_fma_f16 v105, v88, v97, v105 op_sel:[1,1,0] op_sel_hi:[0,1,1] neg_lo:[1,0,0]\n"
"v_pk_mul_f16 v106, v84, v98 op_sel:[0,0] op_sel_hi:[1,0]\n"
"v_pk_mul_f16 v107, v88, v98 op_sel:[0,0] op_sel_hi:[1,0]\n"
"v_pk_fma_f16 v106, v84, v98, v106 op_sel:[1,1,0] op_sel_hi:[0,1,1] neg_lo:[1,0,0]\n"
"v_pk_fma_f16 v107, v88, v98, v107 op_sel:[1,1,0] op_sel_hi:[0,1,1] neg_lo:[1,0,0]\n"
"v_pk_mul_f16 v108, v84, v99 op_sel:[0,0] op_sel_hi:[1,0]\n"
"v_pk_mul_f16 v109, v88, v99 op_sel:[0,0] op_sel_hi:[1,0]\n"
"v_pk_fma_f16 v108, v84, v99, v108 op_sel:[1,1,0] op_sel_hi:[0,1,1] neg_lo:[1,0,0]\n"
"v_pk_fma_f16 v109, v88, v99, v109 op_sel:[1,1,0] op_sel_hi:[0,1,1] neg_lo:[1,0,0]\n"
"s_setprio 3\n"
"s_waitcnt lgkmcnt(3)\n"
"v_mfma_f32_16x16x32_f16 v[148:151], v[68:71], v[52:55], 0\n"
"ds_write2_b32 v152, v100, v101 offset0:0 offset1:68\n"
"s_waitcnt lgkmcnt(3)\n"
"v_mfma_f32_16x16x32_f16 v[148:151], v[72:75], v[56:59], v[148:151]\n"
"ds_write2_b32 v152, v102, v103 offset0:136 offset1:204\n"
"s_waitcnt lgkmcnt(3)\n"
"v_mfma_f32_16x16x32_f16 v[148:151], v[76:79], v[60:63], v[148:151]\n"
"ds_write2_b32 v153, v104, v105 offset0:0 offset1:68\n"
"s_waitcnt lgkmcnt(3)\n"
"v_mfma_f32_16x16x32_f16 v[148:151], v[80:83], v[64:67], v[148:151]\n"
"s_setprio 0\n"
"ds_write2_b32 v153, v106, v107 offset0:136 offset1:204\n"
"ds_write2_b32 v154, v108, v109 offset0:0 offset1:68\n"
"ds_read_b128 v[68:71], v141 offset:0\n"
"ds_read_b128 v[72:75], v141 offset:64\n"
"ds_read_b128 v[76:79], v141 offset:128\n"
"ds_read_b128 v[80:83], v141 offset:192\n"
"v_fmac_f32 v128, s50, v144\n"
"v_fmac_f32 v129, s50, v145\n"
"v_fmac_f32 v130, s50, v146\n"
"v_fmac_f32 v131, s50, v147\n"
"v_fmac_f32 v132, s50, v148\n"
"v_fmac_f32 v133, s50, v149\n"
"v_fmac_f32 v134, s50, v150\n"
"v_fmac_f32 v135, s50, v151\n"
"s_setprio 3\n"
"s_waitcnt lgkmcnt(3)\n"
"v_mfma_f32_16x16x32_f16 v[48:51], v[68:71], v[52:55], 0\n"
"s_waitcnt lgkmcnt(2)\n"
"v_mfma_f32_16x16x32_f16 v[48:51], v[72:75], v[56:59], v[48:51]\n"
"s_waitcnt lgkmcnt(1)\n"
"v_mfma_f32_16x16x32_f16 v[48:51], v[76:79], v[60:63], v[48:51]\n"
"s_waitcnt lgkmcnt(0)\n"
"v_mfma_f32_16x16x32_f16 v[48:51], v[80:83], v[64:67], v[48:51]\n"
"s_setprio 0\n"
"s_sub_u32 s55, s55, 1\n"
"s_nop 5\n"
"v_fmac_f32 v136, s50, v48\n"
"v_fmac_f32 v137, s50, v49\n"
"v_fmac_f32 v138, s50, v50\n"
"v_fmac_f32 v139, s50, v51\n"
"s_cbranch_scc0 .Lgo_325\n"
"s_setpc_b64 s[60:61]\n"
".Lgo_325:\n"
"s_waitcnt vmcnt(0)\n"
"v_mov_b32 v32, v40\n"
"v_mov_b32 v40, v44\n"
"v_mov_b32 v33, v41\n"
"v_mov_b32 v41, v45\n"
"v_mov_b32 v34, v42\n"
"v_mov_b32 v42, v46\n"
"v_mov_b32 v35, v43\n"
"v_mov_b32 v43, v47\n"
"s_cmp_lg_u32 s55, 0\n"
"s_cbranch_scc1 .Lkg_324\n"
"s_and_b32 s79, s2, 1\n"
"s_lshl_b32 s79, s79, 2\n"
"s_lshr_b32 s78, s2, 3\n"
"s_and_b32 s78, s78, 3\n"
"s_add_u32 s28, s79, s78\n"
"s_lshr_b32 s79, s2, 1\n"
"s_and_b32 s79, s79, 3\n"
"s_lshl_b32 s79, s79, 3\n"
"s_lshr_b32 s78, s2, 3\n"
"s_lshr_b32 s78, s78, 2\n"
"s_add_u32 s29, s79, s78\n"
"s_and_b32 s79, s2, 7\n"
"s_lshl_b32 s79, s79, 14\n"
"s_add_u32 s48, s79, 0x4000\n"
"s_mov_b32 s8, s22\n"
"s_mov_b32 s9, s23\n"
"s_mov_b32 s10, 0x280000\n"
"s_mov_b32 s11, 0x20000\n"
"s_mov_b32 s12, s24\n"
"s_mov_b32 s13, s25\n"
"s_mov_b32 s14, 0x280000\n"
"s_mov_b32 s15, 0x20000\n"
"s_mov_b32 s44, 0xcccccccc\n"
"s_mov_b32 s45, 0xcccccccc\n"
"s_mov_b32 s56, 0\n"
"s_mov_b32 s57, -2\n"
"s_mov_b32 s58, 0\n"
"s_mov_b32 s59, -1\n"
"s_lshl_b32 s79, s29, 1\n"
"s_lshr_b32 s78, s3, 1\n"
"s_add_u32 s79, s79, s78\n"
"s_mul_i32 s30, s79, 0x5000\n"
"s_and_b32 s79, s2, 7\n"
"s_lshl_b32 s79, s79, 14\n"
"s_add_u32 s81, s79, 0x3800\n"
"s_lshl_b32 s79, s28, 2\n"
"s_add_u32 s79, s79, s3\n"
"s_mul_i32 s79, s79, 0x6400\n"
"s_add_u32 s80, s79, 0x380000\n"
"s_lshr_b32 s79, s2, 1\n"
"s_and_b32 s79, s79, 3\n"
"s_lshl_b32 s79, s79, 2\n"
"s_lshr_b32 s78, s2, 3\n"
"s_lshr_b32 s78, s78, 3\n"
"s_add_u32 s79, s79, s78\n"
"s_lshl_b32 s79, s79, 2\n"
"s_add_u32 s79, s79, s3\n"
"s_mul_i32 s79, s79, 0x5000\n"
"s_and_b32 s78, s2, 1\n"
"s_lshl_b32 s78, s78, 3\n"
"s_lshr_b32 s77, s2, 3\n"
"s_and_b32 s77, s77, 7\n"
"s_add_u32 s78, s78, s77\n"
"s_mul_i32 s78, s78, 0x500\n"
"s_add_u32 s79, s79, s78\n"
"s_add_u32 s31, s79, 0x448000\n"
"s_mov_b32 s40, 0\n"
"s_mov_b32 s40, 1\n"
"s_setprio 0\n"
"v_lshlrev_b32 v32, 16, v32\n"
"v_lshlrev_b32 v33, 16, v33\n"
"v_lshlrev_b32 v34, 16, v34\n"
"v_lshlrev_b32 v35, 16, v35\n"
"v_mul_f32 v26, v142, v34\n"
"v_mul_f32 v27, v142, v35\n"
"v_max3_f32 v30, |v26|, |v27|, 0\n"
"s_nop 1\n"
"v_max_u32_dpp v30, v30, v30 row_ror:8 row_mask:0xf bank_mask:0xf\n"
"v_med3_f32 v32, v32, s52, v31\n"
"s_nop 0\n"
"v_max_u32_dpp v30, v30, v30 row_ror:4 row_mask:0xf bank_mask:0xf\n"
"v_mul_f32 v28, s53, v32\n"
"s_nop 0\n"
"v_max_u32_dpp v30, v30, v30 row_ror:2 row_mask:0xf bank_mask:0xf\n"
"v_exp_f32 v29, v28\n"
"s_nop 0\n"
"v_max_u32_dpp v30, v30, v30 row_ror:1 row_mask:0xf bank_mask:0xf\n"
"v_fma_f32 v28, v32, s53, -v28\n"
"v_fmac_f32 v28, 0x32a57060, v32\n"
"v_mov_b32 v34, v30\n"
"v_fma_f32 v28, v28, s54, 1.0\n"
"s_nop 0\n"
"v_permlane16_swap_b32 v30, v34\n"
"v_mul_f32 v32, v29, v28\n"
"v_max_u32 v30, v30, v34\n"
"v_cvt_pk_bf16_f32 v32, v32, v32\n"
"v_lshlrev_b32 v32, 16, v32\n"
"v_mov_b32 v34, v30\n"
"v_mul_f32 v25, v142, v33\n"
"s_nop 0\n"
"v_permlane32_swap_b32 v30, v34\n"
"v_mul_f32 v24, v142, -v32\n"
"v_max_u32 v30, v30, v34\n"
"v_mul_f32 v25, 0.15915494, v25\n"
"v_mul_f32 v24, s53, v24\n"
"v_readfirstlane_b32 s51, v30\n"
"s_nop 2\n"
"s_bfe_u32 s50, s51, 0x80017\n"
"s_max_i32 s50, s50, 37\n"
"s_min_i32 s50, s50, 217\n"
"s_cmp_gt_i32 s51, 0\n"
"s_cselect_b32 s50, s50, 127\n"
"s_sub_u32 s51, 267, s50\n"
"s_lshl_b32 s51, s51, 23\n"
"s_sub_u32 s50, s50, 27\n"
"s_lshl_b32 s50, s50, 23\n"
"v_mul_f32 v26, s51, v26\n"
"v_mul_f32 v27, s51, v27\n"
"v_mul_f32 v111, 0x41800000, v25\n"
"v_mul_f32 v100, 0x41800000, v24\n"
"v_fract_f32 v111, v111\n"
"v_exp_f32 v100, v100\n"
"v_cos_f32 v110, v111\n"
"v_sin_f32 v111, -v111\n"
"v_mul_f32 v110, v110, v100\n"
"v_mul_f32 v111, v111, v100\n"
"v_fract_f32 v113, v25\n"
"v_exp_f32 v100, v24\n"
"v_cos_f32 v112, v113\n"
"v_sin_f32 v113, v113\n"
"v_mul_f32 v112, v112, v100\n"
"v_mul_f32 v113, v113, v100\n"
"v_mul_f32 v115, 0x43800000, v25\n"
"v_mul_f32 v100, 0x43800000, v24\n"
"v_fract_f32 v115, v115\n"
"v_exp_f32 v100, v100\n"
"v_cos_f32 v114, v115\n"
"v_sin_f32 v115, v115\n"
"v_mul_f32 v114, v114, v100\n"
"v_mul_f32 v115, v115, v100\n"
"v_mul_f32 v116, v111, v111\n"
"v_add_f32 v117, v110, v110\n"
"v_fma_f32 v116, v110, v110, -v116\n"
"v_mul_f32 v117, v117, v111\n"
"v_mul_f32 v120, v117, v117\n"
"v_add_f32 v121, v116, v116\n"
"v_fma_f32 v120, v116, v116, -v120\n"
"v_mul_f32 v121, v121, v117\n"
"v_mul_f32 v118, v117, v111\n"
"v_mul_f32 v119, v116, v111\n"
"v_fma_f32 v118, v116, v110, -v118\n"
"v_fmac_f32 v119, v117, v110\n"
"v_mul_f32 v122, v121, v121\n"
"v_add_f32 v123, v120, v120\n"
"v_fma_f32 v122, v120, v120, -v122\n"
"v_mul_f32 v123, v123, v121\n"
"v_mov_b32 v84, 0x7400\n"
"v_mul_f32 v124, 0x46800000, v110\n"
"v_mul_f32 v125, 0x46800000, v111\n"
"v_cvt_pk_f16_f32 v100, v124, v125\n"
"v_mul_f32 v124, 0x46800000, v116\n"
"v_mul_f32 v125, 0x46800000, v117\n"
"v_cvt_pk_f16_f32 v85, v124, v125\n"
"v_mul_f32 v124, 0x46800000, v118\n"
"v_mul_f32 v125, 0x46800000, v119\n"
"v_cvt_pk_f16_f32 v101, v124, v125\n"
"v_cvt_pk_f16_f32 v93, v120, v121\n"
"v_cvt_pk_f16_f32 v94, v122, v123\n"
"v_mul_f32 v124, v123, v121\n"
"v_mul_f32 v125, v122, v121\n"
"v_fma_f32 v124, v122, v120, -v124\n"
"v_fmac_f32 v125, v123, v120\n"
"v_cvt_pk_f16_f32 v95, v124, v125\n"
"ds_write2_b32 v152, v84, v100 offset0:0 offset1:68\n"
"v_pk_mul_f16 v86, v84, v93 op_sel:[0,0] op_sel_hi:[1,0]\n"
"v_pk_mul_f16 v102, v100, v93 op_sel:[0,0] op_sel_hi:[1,0]\n"
"v_pk_fma_f16 v86, v84, v93, v86 op_sel:[1,1,0] op_sel_hi:[0,1,1] neg_lo:[1,0,0]\n"
"v_pk_fma_f16 v102, v100, v93, v102 op_sel:[1,1,0] op_sel_hi:[0,1,1] neg_lo:[1,0,0]\n"
"ds_write2_b32 v152, v85, v101 offset0:136 offset1:204\n"
"v_pk_mul_f16 v87, v85, v93 op_sel:[0,0] op_sel_hi:[1,0]\n"
"v_pk_mul_f16 v103, v101, v93 op_sel:[0,0] op_sel_hi:[1,0]\n"
"v_pk_fma_f16 v87, v85, v93, v87 op_sel:[1,1,0] op_sel_hi:[0,1,1] neg_lo:[1,0,0]\n"
"v_pk_fma_f16 v103, v101, v93, v103 op_sel:[1,1,0] op_sel_hi:[0,1,1] neg_lo:[1,0,0]\n"
"ds_write2_b32 v153, v86, v102 offset0:0 offset1:68\n"
"v_pk_mul_f16 v88, v84, v94 op_sel:[0,0] op_sel_hi:[1,0]\n"
"v_pk_mul_f16 v102, v100, v94 op_sel:[0,0] op_sel_hi:[1,0]\n"
"v_pk_fma_f16 v88, v84, v94, v88 op_sel:[1,1,0] op_sel_hi:[0,1,1] neg_lo:[1,0,0]\n"
"v_pk_fma_f16 v102, v100, v94, v102 op_sel:[1,1,0] op_sel_hi:[0,1,1] neg_lo:[1,0,0]\n"
"ds_write2_b32 v153, v87, v103 offset0:136 offset1:204\n"
"v_pk_mul_f16 v89, v85, v94 op_sel:[0,0] op_sel_hi:[1,0]\n"
"v_pk_mul_f16 v103, v101, v94 op_sel:[0,0] op_sel_hi:[1,0]\n"
"v_pk_fma_f16 v89, v85, v94, v89 op_sel:[1,1,0] op_sel_hi:[0,1,1] neg_lo:[1,0,0]\n"
"v_pk_fma_f16 v103, v101, v94, v103 op_sel:[1,1,0] op_sel_hi:[0,1,1] neg_lo:[1,0,0]\n"
"ds_write2_b32 v154, v88, v102 offset0:0 offset1:68\n"
"v_pk_mul_f16 v90, v84, v95 op_sel:[0,0] op_sel_hi:[1,0]\n"
"v_pk_mul_f16 v102, v100, v95 op_sel:[0,0] op_sel_hi:[1,0]\n"
"v_pk_fma_f16 v90, v84, v95, v90 op_sel:[1,1,0] op_sel_hi:[0,1,1] neg_lo:[1,0,0]\n"
"v_pk_fma_f16 v102, v100, v95, v102 op_sel:[1,1,0] op_sel_hi:[0,1,1] neg_lo:[1,0,0]\n"
"ds_write2_b32 v154, v89, v103 offset0:136 offset1:204\n"
"v_pk_mul_f16 v91, v85, v95 op_sel:[0,0] op_sel_hi:[1,0]\n"
"v_pk_mul_f16 v103, v101, v95 op_sel:[0,0] op_sel_hi:[1,0]\n"
"v_pk_fma_f16 v91, v85, v95, v91 op_sel:[1,1,0] op_sel_hi:[0,1,1] neg_lo:[1,0,0]\n"
"v_pk_fma_f16 v103, v101, v95, v103 op_sel:[1,1,0] op_sel_hi:[0,1,1] neg_lo:[1,0,0]\n"
"ds_write2_b32 v155, v90, v102 offset0:0 offset1:68\n"
"ds_write2_b32 v155, v91, v103 offset0:136 offset1:204\n"
"ds_read_b128 v[52:55], v141 offset:0\n"
"ds_read_b128 v[56:59], v141 offset:64\n"
"ds_read_b128 v[60:63], v141 offset:128\n"
"ds_read_b128 v[64:67], v141 offset:192\n"
"v_mul_f32 v116, v113, v113\n"
"v_add_f32 v117, v112, v112\n"
"v_fma_f32 v116, v112, v112, -v116\n"
"v_mul_f32 v117, v117, v113\n"
"v_mul_f32 v120, v117, v117\n"
"v_add_f32 v121, v116, v116\n"
"v_fma_f32 v120, v116, v116, -v120\n"
"v_mul_f32 v121, v121, v117\n"
"v_mul_f32 v118, v117, v113\n"
"s_setprio 3\n"
"buffer_load_dwordx4 v[44:47], v38, s[4:7], s81 offen sc1\n"
"s_setprio 0\n"
"v_mul_f32 v119, v116, v113\n"
"v_fma_f32 v118, v116, v112, -v118\n"
"v_fmac_f32 v119, v117, v112\n"
"v_mul_f32 v122, v121, v121\n"
"v_add_f32 v123, v120, v120\n"
"v_fma_f32 v122, v120, v120, -v122\n"
"v_mul_f32 v123, v123, v121\n"
"v_cvt_pk_f16_f32 v84, v26, v27\n"
"v_mul_f32 v124, v27, v113\n"
"v_mul_f32 v125, v26, v113\n"
"v_fma_f32 v124, v26, v112, -v124\n"
"v_fmac_f32 v125, v27, v112\n"
"v_cvt_pk_f16_f32 v100, v124, v125\n"
"v_mul_f32 v124, v27, v117\n"
"v_mul_f32 v125, v26, v117\n"
"v_fma_f32 v124, v26, v116, -v124\n"
"v_fmac_f32 v125, v27, v116\n"
"v_cvt_pk_f16_f32 v85, v124, v125\n"
"v_mul_f32 v124, v27, v119\n"
"v_mul_f32 v125, v26, v119\n"
"v_fma_f32 v124, v26, v118, -v124\n"
"v_fmac_f32 v125, v27, v118\n"
"v_cvt_pk_f16_f32 v101, v124, v125\n"
"v_cvt_pk_f16_f32 v93, v120, v121\n"
"v_cvt_pk_f16_f32 v94, v122, v123\n"
"v_mul_f32 v124, v123, v121\n"
"v_mul_f32 v125, v122, v121\n"
"v_fma_f32 v124, v122, v120, -v124\n"
"v_fmac_f32 v125, v123, v120\n"
"v_cvt_pk_f16_f32 v95, v124, v125\n"
"ds_write2_b32 v152, v84, v100 offset0:0 offset1:68\n"
"v_pk_mul_f16 v86, v84, v93 op_sel:[0,0] op_sel_hi:[1,0]\n"
"v_pk_mul_f16 v102, v100, v93 op_sel:[0,0] op_sel_hi:[1,0]\n"
"v_pk_fma_f16 v86, v84, v93, v86 op_sel:[1,1,0] op_sel_hi:[0,1,1] neg_lo:[1,0,0]\n"
"v_pk_fma_f16 v102, v100, v93, v102 op_sel:[1,1,0] op_sel_hi:[0,1,1] neg_lo:[1,0,0]\n"
"ds_write2_b32 v152, v85, v101 offset0:136 offset1:204\n"
"v_pk_mul_f16 v87, v85, v93 op_sel:[0,0] op_sel_hi:[1,0]\n"
"v_pk_mul_f16 v103, v101, v93 op_sel:[0,0] op_sel_hi:[1,0]\n"
"v_pk_fma_f16 v87, v85, v93, v87 op_sel:[1,1,0] op_sel_hi:[0,1,1] neg_lo:[1,0,0]\n"
"v_pk_fma_f16 v103, v101, v93, v103 op_sel:[1,1,0] op_sel_hi:[0,1,1] neg_lo:[1,0,0]\n"
"ds_write2_b32 v153, v86, v102 offset0:0 offset1:68\n"
"v_pk_mul_f16 v88, v84, v94 op_sel:[0,0] op_sel_hi:[1,0]\n"
"v_pk_mul_f16 v102, v100, v94 op_sel:[0,0] op_sel_hi:[1,0]\n"
"v_pk_fma_f16 v88, v84, v94, v88 op_sel:[1,1,0] op_sel_hi:[0,1,1] neg_lo:[1,0,0]\n"
"v_pk_fma_f16 v102, v100, v94, v102 op_sel:[1,1,0] op_sel_hi:[0,1,1] neg_lo:[1,0,0]\n"
"ds_write2_b32 v153, v87, v103 offset0:136 offset1:204\n"
"v_pk_mul_f16 v89, v85, v94 op_sel:[0,0] op_sel_hi:[1,0]\n"
"v_pk_mul_f16 v103, v101, v94 op_sel:[0,0] op_sel_hi:[1,0]\n"
"v_pk_fma_f16 v89, v85, v94, v89 op_sel:[1,1,0] op_sel_hi:[0,1,1] neg_lo:[1,0,0]\n"
"v_pk_fma_f16 v103, v101, v94, v103 op_sel:[1,1,0] op_sel_hi:[0,1,1] neg_lo:[1,0,0]\n"
"ds_write2_b32 v154, v88, v102 offset0:0 offset1:68\n"
"v_pk_mul_f16 v90, v84, v95 op_sel:[0,0] op_sel_hi:[1,0]\n"
"v_pk_mul_f16 v102, v100, v95 op_sel:[0,0] op_sel_hi:[1,0]\n"
"v_pk_fma_f16 v90, v84, v95, v90 op_sel:[1,1,0] op_sel_hi:[0,1,1] neg_lo:[1,0,0]\n"
"s_waitcnt vmcnt(0)\n"
"v_xor_b32 v44, s16, v44\n"
"v_xor_b32 v45, s16, v45\n"
"v_xor_b32 v46, s16, v46\n"
"v_xor_b32 v47, s16, v47\n"
"v_or_b32 v44, v44, v45\n"
"v_or3_b32 v44, v44, v46, v47\n"
"v_cmp_ne_u32 vcc, 0, v44\n"
"s_cbranch_vccnz .Lsl_320\n"
".Lbk_321:\n"
"v_pk_fma_f16 v102, v100, v95, v102 op_sel:[1,1,0] op_sel_hi:[0,1,1] neg_lo:[1,0,0]\n"
"ds_write2_b32 v154, v89, v103 offset0:136 offset1:204\n"
"v_pk_mul_f16 v91, v85, v95 op_sel:[0,0] op_sel_hi:[1,0]\n"
"v_pk_mul_f16 v103, v101, v95 op_sel:[0,0] op_sel_hi:[1,0]\n"
"v_pk_fma_f16 v91, v85, v95, v91 op_sel:[1,1,0] op_sel_hi:[0,1,1] neg_lo:[1,0,0]\n"
"v_pk_fma_f16 v103, v101, v95, v103 op_sel:[1,1,0] op_sel_hi:[0,1,1] neg_lo:[1,0,0]\n"
"buffer_load_dwordx4 v[156:159], v2, s[4:7], s80 offen offset:0 sc1\n"
"ds_write2_b32 v155, v90, v102 offset0:0 offset1:68\n"
"ds_write2_b32 v155, v91, v103 offset0:136 offset1:204\n"
"ds_read_b128 v[68:71], v141 offset:0\n"
"ds_read_b128 v[72:75], v141 offset:64\n"
"ds_read_b128 v[76:79], v141 offset:128\n"
"ds_read_b128 v[80:83], v141 offset:192\n"
"buffer_load_dwordx4 v[160:163], v2, s[4:7], s80 offen offset:1024 sc1\n"
"v_mul_f32 v116, v115, v115\n"
"v_add_f32 v117, v114, v114\n"
"v_fma_f32 v116, v114, v114, -v116\n"
"v_mul_f32 v117, v117, v115\n"
"v_mul_f32 v120, v117, v117\n"
"v_add_f32 v121, v116, v116\n"
"buffer_load_dwordx4 v[164:167], v2, s[4:7], s80 offen offset:2048 sc1\n"
"v_fma_f32 v120, v116, v116, -v120\n"
"v_mul_f32 v121, v121, v117\n"
"v_mul_f32 v118, v117, v115\n"
"v_mul_f32 v119, v116, v115\n"
"v_fma_f32 v118, v116, v114, -v118\n"
"v_fmac_f32 v119, v117, v114\n"
"buffer_load_dwordx4 v[168:171], v2, s[4:7], s80 offen offset:3072 sc1\n"
"v_cvt_pk_f16_f32 v92, v114, v115\n"
"v_cvt_pk_f16_f32 v93, v116, v117\n"
"v_cvt_pk_f16_f32 v95, v120, v121\n"
"v_cvt_pk_f16_f32 v94, v118, v119\n"
"v_mul_f32 v124, v121, v115\n"
"v_mul_f32 v125, v120, v115\n"
"s_add_u32 s80, s80, 4096\n"
"buffer_load_dwordx4 v[172:175], v2, s[4:7], s80 offen offset:0 sc1\n"
"v_fma_f32 v124, v120, v114, -v124\n"
"v_fmac_f32 v125, v121, v114\n"
"v_cvt_pk_f16_f32 v96, v124, v125\n"
"v_mul_f32 v126, v121, v117\n"
"v_mul_f32 v127, v120, v117\n"
"v_fma_f32 v126, v120, v116, -v126\n"
"buffer_load_dwordx4 v[176:179], v2, s[4:7], s80 offen offset:1024 sc1\n"
"v_fmac_f32 v127, v121, v116\n"
"v_cvt_pk_f16_f32 v97, v126, v127\n"
"v_mul_f32 v124, v121, v119\n"
"v_mul_f32 v125, v120, v119\n"
"v_fma_f32 v124, v120, v118, -v124\n"
"v_fmac_f32 v125, v121, v118\n"
"s_waitcnt vmcnt(5)\n"
"buffer_load_dwordx4 v[180:183], v2, s[4:7], s80 offen offset:2048 sc1\n"
"v_cvt_pk_f16_f32 v98, v124, v125\n"
"v_mul_f32 v126, v121, v121\n"
"v_mul_f32 v127, v120, v121\n"
"v_fma_f32 v126, v120, v120, -v126\n"
"v_fmac_f32 v127, v121, v120\n"
"v_cvt_pk_f16_f32 v99, v126, v127\n"
"s_waitcnt vmcnt(5)\n"
"buffer_load_dwordx4 v[184:187], v2, s[4:7], s80 offen offset:3072 sc1\n"
"s_setprio 3\n"
"s_waitcnt lgkmcnt(3)\n"
"v_mfma_f32_16x16x32_f16 v[144:147], v[68:71], v[52:55], 0\n"
"v_pk_mul_f16 v100, v84, v92 op_sel:[0,0] op_sel_hi:[1,0]\n"
"v_pk_mul_f16 v101, v85, v92 op_sel:[0,0] op_sel_hi:[1,0]\n"
"s_waitcnt lgkmcnt(2)\n"
"v_mfma_f32_16x16x32_f16 v[144:147], v[72:75], v[56:59], v[144:147]\n"
"v_pk_fma_f16 v100, v84, v92, v100 op_sel:[1,1,0] op_sel_hi:[0,1,1] neg_lo:[1,0,0]\n"
"s_add_u32 s80, s80, 4096\n"
"s_waitcnt vmcnt(5)\n"
"buffer_load_dwordx4 v[188:191], v2, s[4:7], s80 offen offset:0 sc1\n"
"v_pk_fma_f16 v101, v85, v92, v101 op_sel:[1,1,0] op_sel_hi:[0,1,1] neg_lo:[1,0,0]\n"
"s_waitcnt lgkmcnt(1)\n"
"v_mfma_f32_16x16x32_f16 v[144:147], v[76:79], v[60:63], v[144:147]\n"
"v_pk_mul_f16 v102, v86, v92 op_sel:[0,0] op_sel_hi:[1,0]\n"
"v_pk_mul_f16 v103, v87, v92 op_sel:[0,0] op_sel_hi:[1,0]\n"
"s_waitcnt lgkmcnt(0)\n"
"v_mfma_f32_16x16x32_f16 v[144:147], v[80:83], v[64:67], v[144:147]\n"
"s_setprio 0\n"
"s_waitcnt vmcnt(5)\n"
"buffer_load_dwordx4 v[192:195], v2, s[4:7], s80 offen offset:1024 sc1\n"
"v_pk_fma_f16 v102, v86, v92, v102 op_sel:[1,1,0] op_sel_hi:[0,1,1] neg_lo:[1,0,0]\n"
"v_pk_fma_f16 v103, v87, v92, v103 op_sel:[1,1,0] op_sel_hi:[0,1,1] neg_lo:[1,0,0]\n"
"ds_write2_b32 v152, v100, v101 offset0:0 offset1:68\n"
"v_pk_mul_f16 v100, v88, v92 op_sel:[0,0] op_sel_hi:[1,0]\n"
"v_pk_mul_f16 v101, v89, v92 op_sel:[0,0] op_sel_hi:[1,0]\n"
"v_pk_fma_f16 v100, v88, v92, v100 op_sel:[1,1,0] op_sel_hi:[0,1,1] neg_lo:[1,0,0]\n"
"s_waitcnt vmcnt(5)\n"
"buffer_load_dwordx4 v[196:199], v2, s[4:7], s80 offen offset:2048 sc1\n"
"v_pk_fma_f16 v101, v89, v92, v101 op_sel:[1,1,0] op_sel_hi:[0,1,1] neg_lo:[1,0,0]\n"
"ds_write2_b32 v152, v102, v103 offset0:136 offset1:204\n"
"v_pk_mul_f16 v102, v90, v92 op_sel:[0,0] op_sel_hi:[1,0]\n"
"v_pk_mul_f16 v103, v91, v92 op_sel:[0,0] op_sel_hi:[1,0]\n"
"v_pk_fma_f16 v102, v90, v92, v102 op_sel:[1,1,0] op_sel_hi:[0,1,1] neg_lo:[1,0,0]\n"
"v_pk_fma_f16 v103, v91, v92, v103 op_sel:[1,1,0] op_sel_hi:[0,1,1] neg_lo:[1,0,0]\n"
"s_waitcnt vmcnt(5)\n"
"buffer_load_dwordx4 v[200:203], v2, s[4:7], s80 offen offset:3072 sc1\n"
"ds_write2_b32 v153, v100, v101 offset0:0 offset1:68\n"
"v_pk_mul_f16 v100, v84, v93 op_sel:[0,0] op_sel_hi:[1,0]\n"
"v_pk_mul_f16 v101, v86, v93 op_sel:[0,0] op_sel_hi:[1,0]\n"
"v_pk_fma_f16 v100, v84, v93, v100 op_sel:[1,1,0] op_sel_hi:[0,1,1] neg_lo:[1,0,0]\n"
"v_pk_fma_f16 v101, v86, v93, v101 op_sel:[1,1,0] op_sel_hi:[0,1,1] neg_lo:[1,0,0]\n"
"ds_write2_b32 v153, v102, v103 offset0:136 offset1:204\n"
"s_add_u32 s80, s80, 4096\n"
"s_waitcnt vmcnt(5)\n"
"buffer_load_dwordx4 v[204:207], v2, s[4:7], s80 offen offset:0 sc1\n"
"v_pk_mul_f16 v102, v88, v93 op_sel:[0,0] op_sel_hi:[1,0]\n"
"v_pk_mul_f16 v103, v90, v93 op_sel:[0,0] op_sel_hi:[1,0]\n"
"v_pk_fma_f16 v102, v88, v93, v102 op_sel:[1,1,0] op_sel_hi:[0,1,1] neg_lo:[1,0,0]\n"
"v_pk_fma_f16 v103, v90, v93, v103 op_sel:[1,1,0] op_sel_hi:[0,1,1] neg_lo:[1,0,0]\n"
"ds_write2_b32 v154, v100, v101 offset0:0 offset1:68\n"
"v_pk_mul_f16 v100, v84, v94 op_sel:[0,0] op_sel_hi:[1,0]\n"
"s_waitcnt vmcnt(5)\n"
"buffer_load_dwordx4 v[208:211], v2, s[4:7], s80 offen offset:1024 sc1\n"
"v_pk_mul_f16 v101, v86, v94 op_sel:[0,0] op_sel_hi:[1,0]\n"
"v_pk_fma_f16 v100, v84, v94, v100 op_sel:[1,1,0] op_sel_hi:[0,1,1] neg_lo:[1,0,0]\n"
"v_pk_fma_f16 v101, v86, v94, v101 op_sel:[1,1,0] op_sel_hi:[0,1,1] neg_lo:[1,0,0]\n"
"ds_write2_b32 v154, v102, v103 offset0:136 offset1:204\n"
"v_pk_mul_f16 v102, v88, v94 op_sel:[0,0] op_sel_hi:[1,0]\n"
"v_pk_mul_f16 v103, v90, v94 op_sel:[0,0] op_sel_hi:[1,0]\n"
"s_waitcnt vmcnt(5)\n"
"buffer_load_dwordx4 v[212:215], v2, s[4:7], s80 offen offset:2048 sc1\n"
"v_pk_fma_f16 v102, v88, v94, v102 op_sel:[1,1,0] op_sel_hi:[0,1,1] neg_lo:[1,0,0]\n"
"v_pk_fma_f16 v103, v90, v94, v103 op_sel:[1,1,0] op_sel_hi:[0,1,1] neg_lo:[1,0,0]\n"
"ds_write2_b32 v155, v100, v101 offset0:0 offset1:68\n"
"ds_write2_b32 v155, v102, v103 offset0:136 offset1:204\n"
"ds_read_b128 v[68:71], v141 offset:0\n"
"ds_read_b128 v[72:75], v141 offset:64\n"
"s_waitcnt vmcnt(5)\n"
"buffer_load_dwordx4 v[216:219], v2, s[4:7], s80 offen offset:3072 sc1\n"
"ds_read_b128 v[76:79], v141 offset:128\n"
"ds_read_b128 v[80:83], v141 offset:192\n"
"v_pk_mul_f16 v100, v84, v95 op_sel:[0,0] op_sel_hi:[1,0]\n"
"v_pk_mul_f16 v101, v88, v95 op_sel:[0,0] op_sel_hi:[1,0]\n"
"v_pk_fma_f16 v100, v84, v95, v100 op_sel:[1,1,0] op_sel_hi:[0,1,1] neg_lo:[1,0,0]\n"
"v_pk_fma_f16 v101, v88, v95, v101 op_sel:[1,1,0] op_sel_hi:[0,1,1] neg_lo:[1,0,0]\n"
"s_add_u32 s80, s80, 4096\n"
"s_waitcnt vmcnt(5)\n"
"buffer_load_dwordx4 v[220:223], v2, s[4:7], s80 offen offset:0 sc1\n"
"v_pk_mul_f16 v102, v84, v96 op_sel:[0,0] op_sel_hi:[1,0]\n"
"v_pk_mul_f16 v103, v88, v96 op_sel:[0,0] op_sel_hi:[1,0]\n"
"v_pk_fma_f16 v102, v84, v96, v102 op_sel:[1,1,0] op_sel_hi:[0,1,1] neg_lo:[1,0,0]\n"
"v_pk_fma_f16 v103, v88, v96, v103 op_sel:[1,1,0] op_sel_hi:[0,1,1] neg_lo:[1,0,0]\n"
"v_pk_mul_f16 v104, v84, v97 op_sel:[0,0] op_sel_hi:[1,0]\n"
"v_pk_mul_f16 v105, v88, v97 op_sel:[0,0] op_sel_hi:[1,0]\n"
"s_waitcnt vmcnt(5)\n"
"buffer_load_dwordx4 v[224:227], v2, s[4:7], s80 offen offset:1024 sc1\n"
"v_pk_fma_f16 v104, v84, v97, v104 op_sel:[1,1,0] op_sel_hi:[0,1,1] neg_lo:[1,0,0]\n"
"v_pk_fma_f16 v105, v88, v97, v105 op_sel:[1,1,0] op_sel_hi:[0,1,1] neg_lo:[1,0,0]\n"
"v_pk_mul_f16 v106, v84, v98 op_sel:[0,0] op_sel_hi:[1,0]\n"
"v_pk_mul_f16 v107, v88, v98 op_sel:[0,0] op_sel_hi:[1,0]\n"
"v_pk_fma_f16 v106, v84, v98, v106 op_sel:[1,1,0] op_sel_hi:[0,1,1] neg_lo:[1,0,0]\n"
"v_pk_fma_f16 v107, v88, v98, v107 op_sel:[1,1,0] op_sel_hi:[0,1,1] neg_lo:[1,0,0]\n"
"s_waitcnt vmcnt(5)\n"
"buffer_load_dwordx4 v[228:231], v2, s[4:7], s80 offen offset:2048 sc1\n"
"v_pk_mul_f16 v108, v84, v99 op_sel:[0,0] op_sel_hi:[1,0]\n"
"v_pk_mul_f16 v109, v88, v99 op_sel:[0,0] op_sel_hi:[1,0]\n"
"v_pk_fma_f16 v108, v84, v99, v108 op_sel:[1,1,0] op_sel_hi:[0,1,1] neg_lo:[1,0,0]\n"
"v_pk_fma_f16 v109, v88, v99, v109 op_sel:[1,1,0] op_sel_hi:[0,1,1] neg_lo:[1,0,0]\n"
"s_setprio 3\n"
"s_waitcnt lgkmcnt(3)\n"
"v_mfma_f32_16x16x32_f16 v[148:151], v[68:71], v[52:55], 0\n"
"s_waitcnt vmcnt(5)\n"
"buffer_load_dwordx4 v[232:235], v2, s[4:7], s80 offen offset:3072 sc1\n"
"ds_write2_b32 v152, v100, v101 offset0:0 offset1:68\n"
"s_waitcnt lgkmcnt(3)\n"
"v_mfma_f32_16x16x32_f16 v[148:151], v[72:75], v[56:59], v[148:151]\n"
"ds_write2_b32 v152, v102, v103 offset0:136 offset1:204\n"
"s_waitcnt lgkmcnt(3)\n"
"v_mfma_f32_16x16x32_f16 v[148:151], v[76:79], v[60:63], v[148:151]\n"
"ds_write2_b32 v153, v104, v105 offset0:0 offset1:68\n"
"s_waitcnt lgkmcnt(3)\n"
"v_mfma_f32_16x16x32_f16 v[148:151], v[80:83], v[64:67], v[148:151]\n"
"s_add_u32 s80, s80, 4096\n"
"s_waitcnt vmcnt(5)\n"
"buffer_load_dwordx4 v[236:239], v2, s[4:7], s80 offen offset:0 sc1\n"
"s_setprio 0\n"
"ds_write2_b32 v153, v106, v107 offset0:136 offset1:204\n"
"ds_write2_b32 v154, v108, v109 offset0:0 offset1:68\n"
"ds_read_b128 v[68:71], v141 offset:0\n"
"ds_read_b128 v[72:75], v141 offset:64\n"
"ds_read_b128 v[76:79], v141 offset:128\n"
"s_waitcnt vmcnt(5)\n"
"buffer_load_dwordx4 v[240:243], v2, s[4:7], s80 offen offset:1024 sc1\n"
"ds_read_b128 v[80:83], v141 offset:192\n"
"v_fmac_f32 v128, s50, v144\n"
"v_fmac_f32 v129, s50, v145\n"
"v_fmac_f32 v130, s50, v146\n"
"v_fmac_f32 v131, s50, v147\n"
"v_fmac_f32 v132, s50, v148\n"
"s_waitcnt vmcnt(5)\n"
"buffer_load_dwordx4 v[244:247], v2, s[4:7], s80 offen offset:2048 sc1\n"
"v_fmac_f32 v133, s50, v149\n"
"v_fmac_f32 v134, s50, v150\n"
"v_fmac_f32 v135, s50, v151\n"
"s_setprio 3\n"
"s_waitcnt lgkmcnt(3)\n"
"v_mfma_f32_16x16x32_f16 v[48:51], v[68:71], v[52:55], 0\n"
"s_waitcnt lgkmcnt(2)\n"
"v_mfma_f32_16x16x32_f16 v[48:51], v[72:75], v[56:59], v[48:51]\n"
"s_waitcnt vmcnt(5)\n"
"buffer_load_dwordx4 v[248:251], v2, s[4:7], s80 offen offset:3072 sc1\n"
"s_waitcnt lgkmcnt(1)\n"
"v_mfma_f32_16x16x32_f16 v[48:51], v[76:79], v[60:63], v[48:51]\n"
"s_waitcnt lgkmcnt(0)\n"
"v_mfma_f32_16x16x32_f16 v[48:51], v[80:83], v[64:67], v[48:51]\n"
"s_setprio 0\n"
"s_nop 6\n"
"v_fmac_f32 v136, s50, v48\n"
"v_fmac_f32 v137, s50, v49\n"
"v_fmac_f32 v138, s50, v50\n"
"s_add_u32 s80, s80, 4096\n"
"s_waitcnt vmcnt(5)\n"
"buffer_load_dwordx4 v[252:255], v2, s[4:7], s80 offen offset:0 sc1\n"
"v_fmac_f32 v139, s50, v51\n"
"s_and_b32 s73, s3, 1\n"
"v_and_b32 v78, 15, v0\n"
"v_lshrrev_b32 v79, 4, v0\n"
"s_lshl_b32 s74, s73, 30\n"
"v_mul_i32_i24 v99, 0x500, v78\n"
"v_lshl_add_u32 v14, v79, 3, v99\n"
"v_lshrrev_b32 v99, 1, v78\n"
"v_and_b32 v98, 1, v78\n"
"v_lshlrev_b32 v98, 7, v98\n"
"v_lshl_add_u32 v98, v79, 8, v98\n"
"v_lshl_add_u32 v17, v99, 4, v98\n"
"s_mov_b32 s79, 0x25400\n"
"s_cmp_eq_i32 s3, 0\n"
"s_cselect_b32 s79, s79, 0x21000\n"
"s_mov_b32 s78, 0x25c00\n"
"s_cmp_eq_i32 s3, 2\n"
"s_cselect_b32 s78, s78, 0x23400\n"
"s_cmp_lt_i32 s3, 2\n"
"s_cselect_b32 s79, s79, s78\n"
"v_add_u32 v6, s79, v2\n"
"s_mov_b32 s79, 0x40000000\n"
"s_cmp_eq_i32 s3, 0\n"
"s_cselect_b32 s79, s79, 0x20c00\n"
"s_mov_b32 s78, 0x40000000\n"
"s_cmp_eq_i32 s3, 2\n"
"s_cselect_b32 s78, s78, 0x23000\n"
"s_cmp_lt_i32 s3, 2\n"
"s_cselect_b32 s79, s79, s78\n"
"v_add_u32 v7, s79, v2\n"
"s_mov_b32 s79, 0x23c00\n"
"s_cmp_eq_i32 s3, 0\n"
"s_cselect_b32 s79, s79, 0x1f200\n"
"s_mov_b32 s78, 0x21800\n"
"s_cmp_eq_i32 s3, 2\n"
"s_cselect_b32 s78, s78, 0x1ce00\n"
"s_cmp_lt_i32 s3, 2\n"
"s_cselect_b32 s79, s79, s78\n"
"v_add_u32 v8, s79, v2\n"
"s_mov_b32 s79, 0x20400\n"
"s_cmp_eq_i32 s3, 0\n"
"s_cselect_b32 s79, s79, 0x24c00\n"
"s_mov_b32 s78, 0x1e000\n"
"s_cmp_eq_i32 s3, 2\n"
"s_cselect_b32 s78, s78, 0x22800\n"
"s_cmp_lt_i32 s3, 2\n"
"s_cselect_b32 s79, s79, s78\n"
"v_add_u32 v9, s79, v2\n"
"s_mov_b32 s79, 0x24400\n"
"s_cmp_eq_i32 s3, 0\n"
"s_cselect_b32 s79, s79, 0x24800\n"
"s_mov_b32 s78, 0x22000\n"
"s_cmp_eq_i32 s3, 2\n"
"s_cselect_b32 s78, s78, 0x22400\n"
"s_cmp_lt_i32 s3, 2\n"
"s_cselect_b32 s79, s79, s78\n"
"v_add_u32 v10, s79, v2\n"
"v_add_u32 v15, s74, v14\n"
"v_lshlrev_b32 v99, 3, v78\n"
"s_mov_b32 vcc_lo, 0x500005\n"
"s_mov_b32 vcc_hi, 0x50000500\n"
"v_cndmask_b32 v16, 2.0, v99, vcc\n"
"v_add_u32 v18, s74, v17\n"
"s_sub_u32 s79, s73, 1\n"
"v_mov_b32 v19, s79\n"
"v_lshrrev_b32 v99, 1, v79\n"
"v_and_b32 v98, 1, v79\n"
"v_and_b32 v97, 4, v78\n"
"v_and_b32 v96, 3, v78\n"
"v_lshrrev_b32 v95, 3, v78\n"
"v_lshlrev_b32 v95, 6, v95\n"
"v_lshl_add_u32 v96, v96, 4, v95\n"
"v_lshl_add_u32 v97, v97, 1, v96\n"
"v_lshl_add_u32 v98, v98, 8, v97\n"
"v_lshl_add_u32 v99, v99, 7, v98\n"
"v_sub_u32 v39, v99, v14\n"
"s_mov_b32 s79, 0x20c00\n"
"s_cmp_eq_i32 s3, 0\n"
"s_cselect_b32 s79, s79, 0x25400\n"
"s_mov_b32 s78, 0x23000\n"
"s_cmp_eq_i32 s3, 2\n"
"s_cselect_b32 s78, s78, 0x25c00\n"
"s_cmp_lt_i32 s3, 2\n"
"s_cselect_b32 s79, s79, s78\n"
"v_add_u32 v11, s79, v2\n"
"s_mov_b32 s79, 0x21800\n"
"s_cmp_eq_i32 s3, 0\n"
"s_cselect_b32 s79, s79, 0x1ce00\n"
"s_mov_b32 s78, 0x23c00\n"
"s_cmp_eq_i32 s3, 2\n"
"s_cselect_b32 s78, s78, 0x1f200\n"
"s_cmp_lt_i32 s3, 2\n"
"s_cselect_b32 s79, s79, s78\n"
"v_add_u32 v12, s79, v2\n"
"s_mov_b32 s79, 0x22400\n"
"s_cmp_eq_i32 s3, 0\n"
"s_cselect_b32 s79, s79, 0x1e000\n"
"s_mov_b32 s78, 0x24800\n"
"s_cmp_eq_i32 s3, 2\n"
"s_cselect_b32 s78, s78, 0x20400\n"
"s_cmp_lt_i32 s3, 2\n"
"s_cselect_b32 s79, s79, s78\n"
"v_add_u32 v13, s79, v2\n"
"v_mov_b32 v20, 0x3f803f80\n"
"v_mov_b32 v21, 0x3f803f80\n"
"v_mov_b32 v22, 0x3f803f80\n"
"v_mov_b32 v23, 0x3f803f80\n"
"s_branch .Ljk_318\n"
".Lnk_317:\n"
"s_and_b32 s79, s2, 1\n"
"s_lshl_b32 s79, s79, 2\n"
"s_lshr_b32 s78, s2, 3\n"
"s_and_b32 s78, s78, 3\n"
"s_add_u32 s28, s79, s78\n"
"s_lshr_b32 s79, s2, 1\n"
"s_and_b32 s79, s79, 3\n"
"s_lshl_b32 s79, s79, 3\n"
"s_lshr_b32 s78, s2, 3\n"
"s_lshr_b32 s78, s78, 2\n"
"s_add_u32 s29, s79, s78\n"
"s_and_b32 s79, s2, 7\n"
"s_lshl_b32 s79, s79, 14\n"
"s_add_u32 s48, s79, 0x4000\n"
"s_mov_b32 s8, s22\n"
"s_mov_b32 s9, s23\n"
"s_mov_b32 s10, 0x280000\n"
"s_mov_b32 s11, 0x20000\n"
"s_mov_b32 s12, s24\n"
"s_mov_b32 s13, s25\n"
"s_mov_b32 s14, 0x280000\n"
"s_mov_b32 s15, 0x20000\n"
"s_mov_b32 s44, 0xcccccccc\n"
"s_mov_b32 s45, 0xcccccccc\n"
"s_mov_b32 s56, 0\n"
"s_mov_b32 s57, -2\n"
"s_mov_b32 s58, 0\n"
"s_mov_b32 s59, -1\n"
"s_lshl_b32 s79, s29, 1\n"
"s_lshr_b32 s78, s3, 1\n"
"s_add_u32 s79, s79, s78\n"
"s_mul_i32 s30, s79, 0x5000\n"
"s_and_b32 s79, s2, 7\n"
"s_lshl_b32 s79, s79, 14\n"
"s_add_u32 s81, s79, 0x3800\n"
"s_lshl_b32 s79, s28, 2\n"
"s_add_u32 s79, s79, s3\n"
"s_mul_i32 s79, s79, 0x6400\n"
"s_add_u32 s80, s79, 0x380000\n"
"s_lshr_b32 s79, s2, 1\n"
"s_and_b32 s79, s79, 3\n"
"s_lshl_b32 s79, s79, 2\n"
"s_lshr_b32 s78, s2, 3\n"
"s_lshr_b32 s78, s78, 3\n"
"s_add_u32 s79, s79, s78\n"
"s_lshl_b32 s79, s79, 2\n"
"s_add_u32 s79, s79, s3\n"
"s_mul_i32 s79, s79, 0x5000\n"
"s_and_b32 s78, s2, 1\n"
"s_lshl_b32 s78, s78, 3\n"
"s_lshr_b32 s77, s2, 3\n"
"s_and_b32 s77, s77, 7\n"
"s_add_u32 s78, s78, s77\n"
"s_mul_i32 s78, s78, 0x500\n"
"s_add_u32 s79, s79, s78\n"
"s_add_u32 s31, s79, 0x448000\n"
"s_and_b32 s73, s3, 1\n"
"v_and_b32 v78, 15, v0\n"
"v_lshrrev_b32 v79, 4, v0\n"
"s_lshl_b32 s74, s73, 30\n"
"v_mul_i32_i24 v99, 0x500, v78\n"
"v_lshl_add_u32 v14, v79, 3, v99\n"
"v_lshrrev_b32 v99, 1, v78\n"
"v_and_b32 v98, 1, v78\n"
"v_lshlrev_b32 v98, 7, v98\n"
"v_lshl_add_u32 v98, v79, 8, v98\n"
"v_lshl_add_u32 v17, v99, 4, v98\n"
"s_mov_b32 s79, 0x25400\n"
"s_cmp_eq_i32 s3, 0\n"
"s_cselect_b32 s79, s79, 0x21000\n"
"s_mov_b32 s78, 0x25c00\n"
"s_cmp_eq_i32 s3, 2\n"
"s_cselect_b32 s78, s78, 0x23400\n"
"s_cmp_lt_i32 s3, 2\n"
"s_cselect_b32 s79, s79, s78\n"
"v_add_u32 v6, s79, v2\n"
"s_mov_b32 s79, 0x40000000\n"
"s_cmp_eq_i32 s3, 0\n"
"s_cselect_b32 s79, s79, 0x20c00\n"
"s_mov_b32 s78, 0x40000000\n"
"s_cmp_eq_i32 s3, 2\n"
"s_cselect_b32 s78, s78, 0x23000\n"
"s_cmp_lt_i32 s3, 2\n"
"s_cselect_b32 s79, s79, s78\n"
"v_add_u32 v7, s79, v2\n"
"s_mov_b32 s79, 0x23c00\n"
"s_cmp_eq_i32 s3, 0\n"
"s_cselect_b32 s79, s79, 0x1f200\n"
"s_mov_b32 s78, 0x21800\n"
"s_cmp_eq_i32 s3, 2\n"
"s_cselect_b32 s78, s78, 0x1ce00\n"
"s_cmp_lt_i32 s3, 2\n"
"s_cselect_b32 s79, s79, s78\n"
"v_add_u32 v8, s79, v2\n"
"s_mov_b32 s79, 0x20400\n"
"s_cmp_eq_i32 s3, 0\n"
"s_cselect_b32 s79, s79, 0x24c00\n"
"s_mov_b32 s78, 0x1e000\n"
"s_cmp_eq_i32 s3, 2\n"
"s_cselect_b32 s78, s78, 0x22800\n"
"s_cmp_lt_i32 s3, 2\n"
"s_cselect_b32 s79, s79, s78\n"
"v_add_u32 v9, s79, v2\n"
"s_mov_b32 s79, 0x24400\n"
"s_cmp_eq_i32 s3, 0\n"
"s_cselect_b32 s79, s79, 0x24800\n"
"s_mov_b32 s78, 0x22000\n"
"s_cmp_eq_i32 s3, 2\n"
"s_cselect_b32 s78, s78, 0x22400\n"
"s_cmp_lt_i32 s3, 2\n"
"s_cselect_b32 s79, s79, s78\n"
"v_add_u32 v10, s79, v2\n"
"v_add_u32 v15, s74, v14\n"
"v_lshlrev_b32 v99, 3, v78\n"
"s_mov_b32 vcc_lo, 0x500005\n"
"s_mov_b32 vcc_hi, 0x50000500\n"
"v_cndmask_b32 v16, 2.0, v99, vcc\n"
"v_add_u32 v18, s74, v17\n"
"s_sub_u32 s79, s73, 1\n"
"v_mov_b32 v19, s79\n"
"v_lshrrev_b32 v99, 1, v79\n"
"v_and_b32 v98, 1, v79\n"
"v_and_b32 v97, 4, v78\n"
"v_and_b32 v96, 3, v78\n"
"v_lshrrev_b32 v95, 3, v78\n"
"v_lshlrev_b32 v95, 6, v95\n"
"v_lshl_add_u32 v96, v96, 4, v95\n"
"v_lshl_add_u32 v97, v97, 1, v96\n"
"v_lshl_add_u32 v98, v98, 8, v97\n"
"v_lshl_add_u32 v99, v99, 7, v98\n"
"v_sub_u32 v39, v99, v14\n"
"s_mov_b32 s79, 0x20c00\n"
"s_cmp_eq_i32 s3, 0\n"
"s_cselect_b32 s79, s79, 0x25400\n"
"s_mov_b32 s78, 0x23000\n"
"s_cmp_eq_i32 s3, 2\n"
"s_cselect_b32 s78, s78, 0x25c00\n"
"s_cmp_lt_i32 s3, 2\n"
"s_cselect_b32 s79, s79, s78\n"
"v_add_u32 v11, s79, v2\n"
"s_mov_b32 s79, 0x21800\n"
"s_cmp_eq_i32 s3, 0\n"
"s_cselect_b32 s79, s79, 0x1ce00\n"
"s_mov_b32 s78, 0x23c00\n"
"s_cmp_eq_i32 s3, 2\n"
"s_cselect_b32 s78, s78, 0x1f200\n"
"s_cmp_lt_i32 s3, 2\n"
"s_cselect_b32 s79, s79, s78\n"
"v_add_u32 v12, s79, v2\n"
"s_mov_b32 s79, 0x22400\n"
"s_cmp_eq_i32 s3, 0\n"
"s_cselect_b32 s79, s79, 0x1e000\n"
"s_mov_b32 s78, 0x24800\n"
"s_cmp_eq_i32 s3, 2\n"
"s_cselect_b32 s78, s78, 0x20400\n"
"s_cmp_lt_i32 s3, 2\n"
"s_cselect_b32 s79, s79, s78\n"
"v_add_u32 v13, s79, v2\n"
"v_mov_b32 v20, 0x3f803f80\n"
"v_mov_b32 v21, 0x3f803f80\n"
"v_mov_b32 v22, 0x3f803f80\n"
"v_mov_b32 v23, 0x3f803f80\n"
"s_mov_b32 s17, 0x100000\n"
".Ltp_326:\n"
"s_sleep 2\n"
"ds_read_b32 v3, v5 offset:16\n"
"s_sub_u32 s17, s17, 1\n"
"s_cbranch_scc1 .Lou_327\n"
"s_waitcnt lgkmcnt(0)\n"
"v_cmp_eq_u32 vcc, 0, v3\n"
"s_cbranch_vccnz .Ltp_326\n"
".Lou_327:\n"
"s_waitcnt lgkmcnt(0)\n"
"s_nop 2\n"
"s_mov_b32 s17, 32768\n"
".Ltp_328:\n"
"buffer_load_dwordx4 v[40:43], v38, s[4:7], s81 offen sc1\n"
"s_sub_u32 s17, s17, 1\n"
"s_cbranch_scc1 .Lou_329\n"
"s_waitcnt vmcnt(0)\n"
"v_xor_b32 v40, s16, v40\n"
"v_xor_b32 v41, s16, v41\n"
"v_xor_b32 v42, s16, v42\n"
"v_xor_b32 v43, s16, v43\n"
"v_or_b32 v40, v40, v41\n"
"v_or3_b32 v40, v40, v42, v43\n"
"v_cmp_ne_u32 vcc, 0, v40\n"
"s_cbranch_vccz .Lou_329\n"
"s_sleep 1\n"
"s_branch .Ltp_328\n"
".Lou_329:\n"
"s_waitcnt vmcnt(0)\n"
".Lpl_319:\n"
"s_mov_b32 s17, 0x100000\n"
".Ltp_330:\n"
"s_sleep 2\n"
"ds_read_b32 v3, v5 offset:32\n"
"s_sub_u32 s17, s17, 1\n"
"s_cbranch_scc1 .Lou_331\n"
"s_waitcnt lgkmcnt(0)\n"
"v_cmp_eq_u32 vcc, 0, v3\n"
"s_cbranch_vccnz .Ltp_330\n"
".Lou_331:\n"
"s_waitcnt lgkmcnt(0)\n"
"buffer_load_dwordx4 v[156:159], v2, s[4:7], s80 offen offset:0 sc1\n"
"buffer_load_dwordx4 v[160:163], v2, s[4:7], s80 offen offset:1024 sc1\n"
"buffer_load_dwordx4 v[164:167], v2, s[4:7], s80 offen offset:2048 sc1\n"
"buffer_load_dwordx4 v[168:171], v2, s[4:7], s80 offen offset:3072 sc1\n"
"s_add_u32 s80, s80, 4096\n"
"buffer_load_dwordx4 v[172:175], v2, s[4:7], s80 offen offset:0 sc1\n"
"buffer_load_dwordx4 v[176:179], v2, s[4:7], s80 offen offset:1024 sc1\n"
"s_waitcnt vmcnt(5)\n"
"buffer_load_dwordx4 v[180:183], v2, s[4:7], s80 offen offset:2048 sc1\n"
"s_waitcnt vmcnt(5)\n"
"buffer_load_dwordx4 v[184:187], v2, s[4:7], s80 offen offset:3072 sc1\n"
"s_add_u32 s80, s80, 4096\n"
"s_waitcnt vmcnt(5)\n"
"buffer_load_dwordx4 v[188:191], v2, s[4:7], s80 offen offset:0 sc1\n"
"s_waitcnt vmcnt(5)\n"
"buffer_load_dwordx4 v[192:195], v2, s[4:7], s80 offen offset:1024 sc1\n"
"s_waitcnt vmcnt(5)\n"
"buffer_load_dwordx4 v[196:199], v2, s[4:7], s80 offen offset:2048 sc1\n"
"s_waitcnt vmcnt(5)\n"
"buffer_load_dwordx4 v[200:203], v2, s[4:7], s80 offen offset:3072 sc1\n"
"s_add_u32 s80, s80, 4096\n"
"s_waitcnt vmcnt(5)\n"
"buffer_load_dwordx4 v[204:207], v2, s[4:7], s80 offen offset:0 sc1\n"
"s_waitcnt vmcnt(5)\n"
"buffer_load_dwordx4 v[208:211], v2, s[4:7], s80 offen offset:1024 sc1\n"
"s_waitcnt vmcnt(5)\n"
"buffer_load_dwordx4 v[212:215], v2, s[4:7], s80 offen offset:2048 sc1\n"
"s_waitcnt vmcnt(5)\n"
"buffer_load_dwordx4 v[216:219], v2, s[4:7], s80 offen offset:3072 sc1\n"
"s_add_u32 s80, s80, 4096\n"
"s_waitcnt vmcnt(5)\n"
"buffer_load_dwordx4 v[220:223], v2, s[4:7], s80 offen offset:0 sc1\n"
"s_waitcnt vmcnt(5)\n"
"buffer_load_dwordx4 v[224:227], v2, s[4:7], s80 offen offset:1024 sc1\n"
"s_waitcnt vmcnt(5)\n"
"buffer_load_dwordx4 v[228:231], v2, s[4:7], s80 offen offset:2048 sc1\n"
"s_waitcnt vmcnt(5)\n"
"buffer_load_dwordx4 v[232:235], v2, s[4:7], s80 offen offset:3072 sc1\n"
"s_add_u32 s80, s80, 4096\n"
"s_waitcnt vmcnt(5)\n"
"buffer_load_dwordx4 v[236:239], v2, s[4:7], s80 offen offset:0 sc1\n"
"s_waitcnt vmcnt(5)\n"
"buffer_load_dwordx4 v[240:243], v2, s[4:7], s80 offen offset:1024 sc1\n"
"s_waitcnt vmcnt(5)\n"
"buffer_load_dwordx4 v[244:247], v2, s[4:7], s80 offen offset:2048 sc1\n"
"s_waitcnt vmcnt(5)\n"
"buffer_load_dwordx4 v[248:251], v2, s[4:7], s80 offen offset:3072 sc1\n"
"s_add_u32 s80, s80, 4096\n"
"s_waitcnt vmcnt(5)\n"
"buffer_load_dwordx4 v[252:255], v2, s[4:7], s80 offen offset:0 sc1\n"
".Ljk_318:\n"
"s_cmp_lt_i32 s18, 0\n"
"s_cselect_b32 s43, 3, 0\n"
"s_setprio 2\n"
"v_mov_b32 v40, -1\n"
"v_mov_b32 v41, -1\n"
"v_mov_b32 v42, -1\n"
"v_mov_b32 v43, -1\n"
"s_lshl_b32 s79, s2, 2\n"
"s_add_u32 s79, s79, s3\n"
"s_lshl_b32 s79, s79, 10\n"
"s_add_u32 s49, s79, 0xc28000\n"
"buffer_store_dwordx4 v[40:43], v2, s[4:7], s49 offen sc1\n"
"s_add_u32 s49, s49, 0x100000\n"
"buffer_store_dwordx4 v[40:43], v2, s[4:7], s49 offen sc1\n"
"s_add_u32 s49, s49, 0x100000\n"
"s_sub_u32 s79, 0xea7fff, s49\n"
"s_and_b32 s79, s79, 0x80000000\n"
"s_or_b32 s49, s49, s79\n"
"buffer_store_dwordx4 v[40:43], v2, s[4:7], s49 offen sc1\n"
"s_nop 1\n"
"s_setprio 0\n"
"s_cmp_lg_u32 s43, 0\n"
"s_cbranch_scc1 .Lsk_350\n"
"ds_read_b32 v3, v5 offset:0\n"
"s_waitcnt lgkmcnt(0)\n"
"v_cmp_eq_u32 vcc, 0, v3\n"
"s_cbranch_vccz .Len_349\n"
"s_call_b64 s[46:47], .Lst_332\n"
".Lsk_350:\n"
"s_cmp_lg_u32 s43, 1\n"
"s_cbranch_scc1 .Lsk_351\n"
"ds_read_b32 v3, v5 offset:128\n"
"s_waitcnt lgkmcnt(0)\n"
"v_cmp_eq_u32 vcc, 0, v3\n"
"s_cbranch_vccz .Len_349\n"
"s_call_b64 s[46:47], .Lst_333\n"
".Lsk_351:\n"
"s_cmp_lg_u32 s43, 2\n"
"s_cbranch_scc1 .Lsk_352\n"
"ds_read_b32 v3, v5 offset:128\n"
"s_waitcnt lgkmcnt(0)\n"
"v_cmp_eq_u32 vcc, 0, v3\n"
"s_cbranch_vccz .Len_349\n"
"s_call_b64 s[46:47], .Lst_334\n"
".Lsk_352:\n"
".Len_349:\n"
"s_setprio 2\n"
"s_mul_i32 s79, s28, 0xa0\n"
"s_add_u32 s79, s30, s79\n"
"s_and_b32 s78, s3, 1\n"
"s_mul_i32 s78, s78, 0x60\n"
"s_add_u32 s36, s79, s78\n"
"s_mul_i32 s79, s3, 0x1400\n"
"s_add_u32 s79, s30, s79\n"
"s_add_u32 s32, s79, 0x448000\n"
"s_add_u32 s33, s32, 0x1000\n"
"s_lshl_b32 s79, s29, 1\n"
"s_lshr_b32 s78, s3, 1\n"
"s_xor_b32 s78, s78, 1\n"
"s_add_u32 s79, s79, s78\n"
"s_mul_i32 s79, s79, 0x5000\n"
"s_mul_i32 s78, s3, 0x1400\n"
"s_add_u32 s79, s79, s78\n"
"s_add_u32 s34, s79, 0x448000\n"
"s_add_u32 s35, s34, 0x1000\n"
"s_lshl_b32 s79, s30, 1\n"
"s_mul_i32 s78, s28, 0x1400\n"
"s_add_u32 s79, s79, s78\n"
"s_and_b32 s78, s3, 1\n"
"s_mul_i32 s78, s78, 0xc00\n"
"s_add_u32 s79, s79, s78\n"
"s_add_u32 s37, s79, 0x6c8000\n"
"s_lshl_b32 s79, s29, 1\n"
"s_lshr_b32 s78, s3, 1\n"
"s_add_u32 s79, s79, s78\n"
"s_lshl_b32 s79, s79, 11\n"
"s_lshl_b32 s78, s28, 8\n"
"s_add_u32 s79, s79, s78\n"
"s_and_b32 s78, s3, 1\n"
"s_lshl_b32 s78, s78, 7\n"
"s_add_u32 s79, s79, s78\n"
"s_add_u32 s38, s79, 0xbc8000\n"
"buffer_load_dwordx2 v[40:41], v14, s[8:11], s36 offen offset:0\n"
"buffer_load_dwordx2 v[42:43], v14, s[8:11], s36 offen offset:32\n"
"buffer_load_dwordx2 v[44:45], v15, s[8:11], s36 offen offset:64\n"
"s_mov_b32 s17, 0x100000\n"
".Ltp_353:\n"
"s_sleep 2\n"
"ds_read_b32 v3, v5 offset:96\n"
"s_sub_u32 s17, s17, 1\n"
"s_cbranch_scc1 .Lou_354\n"
"s_waitcnt lgkmcnt(0)\n"
"v_cmp_eq_u32 vcc, 0, v3\n"
"s_cbranch_vccnz .Ltp_353\n"
".Lou_354:\n"
"s_waitcnt lgkmcnt(0)\n"
"s_cmp_eq_u32 s17, 0xfffff\n"
"s_cbranch_scc1 .Lsk_355\n"
".Lsk_355:\n"
"v_lshlrev_b32 v38, 5, v0\n"
"s_waitcnt vmcnt(0)\n"
"s_nop 3\n"
"s_mov_b32 s17, 32768\n"
".Ltp_357:\n"
"buffer_load_dwordx4 v[88:91], v38, s[4:7], s48 offen sc1\n"
"s_sub_u32 s17, s17, 1\n"
"s_cbranch_scc1 .Lou_358\n"
"s_waitcnt vmcnt(0)\n"
"v_xor_b32 v88, s16, v88\n"
"v_xor_b32 v89, s16, v89\n"
"v_xor_b32 v90, s16, v90\n"
"v_xor_b32 v91, s16, v91\n"
"v_or_b32 v88, v88, v89\n"
"v_or3_b32 v88, v88, v90, v91\n"
"v_cmp_ne_u32 vcc, 0, v88\n"
"s_cbranch_vccz .Lou_358\n"
"s_mov_b32 s89, 6\n"
".Ldl_359:\n"
"s_sub_u32 s89, s89, 1\n"
"s_cbranch_scc1 .Lde_360\n"
"s_sleep 1\n"
"s_branch .Ldl_359\n"
".Lde_360:\n"
"s_branch .Ltp_357\n"
".Lou_358:\n"
"s_waitcnt vmcnt(0)\n"
"v_lshlrev_b32 v48, 16, v40\n"
"v_and_b32 v49, 0xffff0000, v40\n"
"v_lshlrev_b32 v50, 16, v41\n"
"v_and_b32 v51, 0xffff0000, v41\n"
"v_lshlrev_b32 v52, 16, v42\n"
"v_and_b32 v53, 0xffff0000, v42\n"
"v_lshlrev_b32 v54, 16, v43\n"
"v_and_b32 v55, 0xffff0000, v43\n"
"v_lshlrev_b32 v56, 16, v44\n"
"v_and_b32 v57, 0xffff0000, v44\n"
"v_lshlrev_b32 v58, 16, v45\n"
"v_and_b32 v59, 0xffff0000, v45\n"
"v_mov_b32 v46, 0\n"
"v_mov_b32 v47, 0\n"
"s_mov_b32 s17, 0x100000\n"
".Ltp_361:\n"
"s_sleep 1\n"
"ds_read_b32 v3, v5 offset:128\n"
"s_sub_u32 s17, s17, 1\n"
"s_cbranch_scc1 .Lou_362\n"
"s_waitcnt lgkmcnt(0)\n"
"v_cmp_eq_u32 vcc, 0, v3\n"
"s_cbranch_vccnz .Ltp_361\n"
".Lou_362:\n"
"s_waitcnt lgkmcnt(0)\n"
"s_cmp_eq_u32 s17, 0xfffff\n"
"s_cbranch_scc1 .Lsk_363\n"
"s_mov_b32 s89, 18\n"
".Ldl_365:\n"
"s_sub_u32 s89, s89, 1\n"
"s_cbranch_scc1 .Lde_366\n"
"s_sleep 1\n"
"s_branch .Ldl_365\n"
".Lde_366:\n"
"s_branch .Ljn_364\n"
".Lsk_363:\n"
".Ljn_364:\n"
"buffer_load_dwordx4 v[108:111], v2, s[4:7], s34 offen offset:0 sc1\n"
"s_nop 9\n"
"buffer_load_dwordx4 v[112:115], v2, s[4:7], s34 offen offset:1024 sc1\n"
"s_nop 9\n"
"buffer_load_dwordx4 v[116:119], v2, s[4:7], s34 offen offset:2048 sc1\n"
"s_nop 9\n"
"buffer_load_dwordx4 v[120:123], v2, s[4:7], s34 offen offset:3072 sc1\n"
"s_nop 9\n"
"buffer_load_dwordx4 v[124:127], v2, s[4:7], s35 offen offset:0 sc1\n"
"s_nop 9\n"
"buffer_load_dwordx4 v[88:91], v2, s[4:7], s32 offen offset:0 sc1\n"
"s_nop 9\n"
"buffer_load_dwordx4 v[92:95], v2, s[4:7], s32 offen offset:1024 sc1\n"
"s_nop 9\n"
"buffer_load_dwordx4 v[96:99], v2, s[4:7], s32 offen offset:2048 sc1\n"
"s_nop 9\n"
"buffer_load_dwordx4 v[100:103], v2, s[4:7], s32 offen offset:3072 sc1\n"
"s_nop 9\n"
"buffer_load_dwordx4 v[104:107], v2, s[4:7], s33 offen offset:0 sc1\n"
"s_waitcnt vmcnt(9)\n"
"v_max_u32 v38, v108, v109\n"
"v_max3_u32 v38, v110, v111, v38\n"
"v_cmp_eq_u32 vcc, -1, v38\n"
"s_cbranch_vccnz .Lsl_367\n"
".Lbk_368:\n"
"v_mfma_f32_16x16x32_bf16 v[68:71], v[156:159], v[108:111], 0\n"
"v_mfma_f32_16x16x32_bf16 v[72:75], v[160:163], v[108:111], 0\n"
"v_mfma_f32_16x16x32_bf16 v[76:79], v[164:167], v[108:111], 0\n"
"s_waitcnt vmcnt(8)\n"
"v_max_u32 v38, v112, v113\n"
"v_max3_u32 v38, v114, v115, v38\n"
"v_mfma_f32_16x16x32_bf16 v[80:83], v[168:171], v[108:111], 0\n"
"v_cmp_eq_u32 vcc, -1, v38\n"
"v_mfma_f32_16x16x32_bf16 v[84:87], v[172:175], v[108:111], 0\n"
"s_cbranch_vccnz .Lsl_369\n"
".Lbk_370:\n"
"v_mfma_f32_16x16x32_bf16 v[68:71], v[176:179], v[112:115], v[68:71]\n"
"v_mfma_f32_16x16x32_bf16 v[72:75], v[180:183], v[112:115], v[72:75]\n"
"v_mfma_f32_16x16x32_bf16 v[76:79], v[184:187], v[112:115], v[76:79]\n"
"s_waitcnt vmcnt(7)\n"
"v_max_u32 v38, v116, v117\n"
"v_max3_u32 v38, v118, v119, v38\n"
"v_mfma_f32_16x16x32_bf16 v[80:83], v[188:191], v[112:115], v[80:83]\n"
"v_cmp_eq_u32 vcc, -1, v38\n"
"v_mfma_f32_16x16x32_bf16 v[84:87], v[192:195], v[112:115], v[84:87]\n"
"s_cbranch_vccnz .Lsl_371\n"
".Lbk_372:\n"
"v_mfma_f32_16x16x32_bf16 v[68:71], v[196:199], v[116:119], v[68:71]\n"
"v_mfma_f32_16x16x32_bf16 v[72:75], v[200:203], v[116:119], v[72:75]\n"
"v_mfma_f32_16x16x32_bf16 v[76:79], v[204:207], v[116:119], v[76:79]\n"
"s_waitcnt vmcnt(6)\n"
"v_max_u32 v38, v120, v121\n"
"v_max3_u32 v38, v122, v123, v38\n"
"v_mfma_f32_16x16x32_bf16 v[80:83], v[208:211], v[116:119], v[80:83]\n"
"v_cmp_eq_u32 vcc, -1, v38\n"
"v_mfma_f32_16x16x32_bf16 v[84:87], v[212:215], v[116:119], v[84:87]\n"
"s_cbranch_vccnz .Lsl_373\n"
".Lbk_374:\n"
"v_mfma_f32_16x16x32_bf16 v[68:71], v[216:219], v[120:123], v[68:71]\n"
"v_mfma_f32_16x16x32_bf16 v[72:75], v[220:223], v[120:123], v[72:75]\n"
"v_mfma_f32_16x16x32_bf16 v[76:79], v[224:227], v[120:123], v[76:79]\n"
"s_waitcnt vmcnt(5)\n"
"v_max_u32 v38, v124, v125\n"
"v_max3_u32 v38, v126, v127, v38\n"
"v_mfma_f32_16x16x32_bf16 v[80:83], v[228:231], v[120:123], v[80:83]\n"
"v_cmp_eq_u32 vcc, -1, v38\n"
"v_mfma_f32_16x16x32_bf16 v[84:87], v[232:235], v[120:123], v[84:87]\n"
"s_cbranch_vccnz .Lsl_375\n"
".Lbk_376:\n"
"v_mfma_f32_16x16x32_bf16 v[68:71], v[236:239], v[124:127], v[68:71]\n"
"v_mfma_f32_16x16x32_bf16 v[72:75], v[240:243], v[124:127], v[72:75]\n"
"v_mfma_f32_16x16x32_bf16 v[76:79], v[244:247], v[124:127], v[76:79]\n"
"s_waitcnt vmcnt(4)\n"
"v_max_u32 v38, v88, v89\n"
"v_max3_u32 v38, v90, v91, v38\n"
"v_mfma_f32_16x16x32_bf16 v[80:83], v[248:251], v[124:127], v[80:83]\n"
"v_cmp_eq_u32 vcc, -1, v38\n"
"v_mfma_f32_16x16x32_bf16 v[84:87], v[252:255], v[124:127], v[84:87]\n"
"ds_write_b128 v8, v[68:71] offset:0\n"
"s_cbranch_vccnz .Lsl_377\n"
".Lbk_378:\n"
"v_mfma_f32_16x16x32_bf16 v[56:59], v[164:167], v[88:91], v[56:59]\n"
"s_waitcnt vmcnt(3)\n"
"v_max_u32 v38, v92, v93\n"
"v_max3_u32 v38, v94, v95, v38\n"
"ds_write_b128 v8, v[72:75] offset:1024\n"
"v_mfma_f32_16x16x32_bf16 v[60:63], v[168:171], v[88:91], 0\n"
"v_cmp_eq_u32 vcc, -1, v38\n"
"ds_write_b128 v10, v[76:79] offset:0\n"
"v_mfma_f32_16x16x32_bf16 v[64:67], v[172:175], v[88:91], 0\n"
"v_mov_b32 v117, s16\n"
"s_cbranch_vccnz .Lsl_379\n"
".Lbk_380:\n"
"v_mfma_f32_16x16x32_bf16 v[56:59], v[184:187], v[92:95], v[56:59]\n"
"s_waitcnt vmcnt(2)\n"
"v_max_u32 v38, v96, v97\n"
"v_max3_u32 v38, v98, v99, v38\n"
"v_mov_b32 v119, s16\n"
"v_mfma_f32_16x16x32_bf16 v[60:63], v[188:191], v[92:95], v[60:63]\n"
"v_cmp_eq_u32 vcc, -1, v38\n"
"ds_write_b128 v9, v[80:83] offset:0\n"
"v_mfma_f32_16x16x32_bf16 v[64:67], v[192:195], v[92:95], v[64:67]\n"
"ds_write_b128 v9, v[84:87] offset:1024\n"
"s_cbranch_vccnz .Lsl_381\n"
".Lbk_382:\n"
"v_mfma_f32_16x16x32_bf16 v[56:59], v[204:207], v[96:99], v[56:59]\n"
"s_waitcnt vmcnt(1)\n"
"v_max_u32 v38, v100, v101\n"
"v_max3_u32 v38, v102, v103, v38\n"
"ds_write_b32 v4, v4 offset:608\n"
"v_mfma_f32_16x16x32_bf16 v[60:63], v[208:211], v[96:99], v[60:63]\n"
"v_cmp_eq_u32 vcc, -1, v38\n"
"v_mov_b32 v121, s16\n"
"v_mov_b32 v123, s16\n"
"v_mfma_f32_16x16x32_bf16 v[64:67], v[212:215], v[96:99], v[64:67]\n"
"v_mov_b32 v125, s16\n"
"s_cbranch_vccnz .Lsl_383\n"
".Lbk_384:\n"
"v_mfma_f32_16x16x32_bf16 v[56:59], v[224:227], v[100:103], v[56:59]\n"
"s_waitcnt vmcnt(0)\n"
"v_max_u32 v38, v104, v105\n"
"v_max3_u32 v38, v106, v107, v38\n"
"v_mov_b32 v127, s16\n"
"v_mfma_f32_16x16x32_bf16 v[60:63], v[228:231], v[100:103], v[60:63]\n"
"v_cmp_eq_u32 vcc, -1, v38\n"
"ds_read_b32 v3, v5 offset:608\n"
"v_mfma_f32_16x16x32_bf16 v[64:67], v[232:235], v[100:103], v[64:67]\n"
"s_cbranch_vccnz .Lsl_385\n"
".Lbk_386:\n"
"v_mfma_f32_16x16x32_bf16 v[56:59], v[244:247], v[104:107], v[56:59]\n"
"ds_read_b128 v[76:79], v12 offset:2048\n"
"ds_read_b128 v[108:111], v13 offset:2048\n"
"v_mfma_f32_16x16x32_bf16 v[60:63], v[248:251], v[104:107], v[60:63]\n"
"ds_read_b128 v[68:71], v12 offset:0\n"
"ds_read_b128 v[80:83], v13 offset:0\n"
"v_mfma_f32_16x16x32_bf16 v[64:67], v[252:255], v[104:107], v[64:67]\n"
"ds_read_b128 v[72:75], v12 offset:1024\n"
"ds_read_b128 v[84:87], v13 offset:1024\n"
"v_mfma_f32_16x16x32_bf16 v[48:51], v[156:159], v[88:91], v[48:51]\n"
"ds_write_b128 v7, v[56:59] offset:0\n"
"v_mfma_f32_16x16x32_bf16 v[52:55], v[160:163], v[88:91], v[52:55]\n"
"ds_write_b128 v6, v[60:63] offset:0\n"
"v_mfma_f32_16x16x32_bf16 v[48:51], v[176:179], v[92:95], v[48:51]\n"
"ds_write_b128 v6, v[64:67] offset:1024\n"
"v_mfma_f32_16x16x32_bf16 v[52:55], v[180:183], v[92:95], v[52:55]\n"
"ds_write_b32 v4, v4 offset:624\n"
"v_mfma_f32_16x16x32_bf16 v[48:51], v[196:199], v[96:99], v[48:51]\n"
"s_mov_b32 s17, 0x100000\n"
"s_waitcnt lgkmcnt(10)\n"
"v_cmp_eq_u32 vcc, 0, v3\n"
"s_cbranch_vccz .Lok_388\n"
".Ltp_387:\n"
"ds_read_b32 v3, v5 offset:608\n"
"ds_read_b128 v[76:79], v12 offset:2048\n"
"ds_read_b128 v[108:111], v13 offset:2048\n"
"ds_read_b128 v[68:71], v12 offset:0\n"
"ds_read_b128 v[80:83], v13 offset:0\n"
"ds_read_b128 v[72:75], v12 offset:1024\n"
"ds_read_b128 v[84:87], v13 offset:1024\n"
"s_sub_u32 s17, s17, 1\n"
"s_cbranch_scc1 .Lok_388\n"
"s_waitcnt lgkmcnt(6)\n"
"v_cmp_eq_u32 vcc, 0, v3\n"
"s_cbranch_vccnz .Ltp_387\n"
"s_waitcnt lgkmcnt(0)\n"
".Lok_388:\n"
"v_mfma_f32_16x16x32_bf16 v[52:55], v[200:203], v[96:99], v[52:55]\n"
"s_waitcnt lgkmcnt(8)\n"
"v_add_f32 v76, v76, v108\n"
"v_add_f32 v77, v77, v109\n"
"v_add_f32 v78, v78, v110\n"
"v_mfma_f32_16x16x32_bf16 v[48:51], v[216:219], v[100:103], v[48:51]\n"
"v_add_f32 v79, v79, v111\n"
"s_waitcnt lgkmcnt(6)\n"
"v_add_f32 v68, v68, v80\n"
"v_add_f32 v69, v69, v81\n"
"v_mfma_f32_16x16x32_bf16 v[52:55], v[220:223], v[100:103], v[52:55]\n"
"v_add_f32 v70, v70, v82\n"
"v_add_f32 v71, v71, v83\n"
"s_waitcnt lgkmcnt(4)\n"
"v_add_f32 v72, v72, v84\n"
"v_mfma_f32_16x16x32_bf16 v[48:51], v[236:239], v[104:107], v[48:51]\n"
"v_add_f32 v73, v73, v85\n"
"v_add_f32 v74, v74, v86\n"
"v_add_f32 v75, v75, v87\n"
"v_mfma_f32_16x16x32_bf16 v[52:55], v[240:243], v[104:107], v[52:55]\n"
"ds_read_b32 v3, v5 offset:624\n"
"ds_read_b128 v[112:115], v11 offset:2048\n"
"ds_read_b128 v[60:63], v11 offset:0\n"
"ds_read_b128 v[64:67], v11 offset:1024\n"
"v_add_f32 v56, v56, v76\n"
"v_add_f32 v57, v57, v77\n"
"v_add_f32 v58, v58, v78\n"
"v_add_f32 v59, v59, v79\n"
"v_add_f32 v48, v48, v68\n"
"v_add_f32 v49, v49, v69\n"
"v_add_f32 v50, v50, v70\n"
"v_add_f32 v51, v51, v71\n"
"v_add_f32 v52, v52, v72\n"
"v_add_f32 v53, v53, v73\n"
"v_add_f32 v54, v54, v74\n"
"v_add_f32 v55, v55, v75\n"
"s_mov_b32 s17, 0x100000\n"
"s_waitcnt lgkmcnt(3)\n"
"v_cmp_eq_u32 vcc, 0, v3\n"
"s_cbranch_vccz .Lok_390\n"
".Ltp_389:\n"
"ds_read_b32 v3, v5 offset:624\n"
"ds_read_b128 v[112:115], v11 offset:2048\n"
"ds_read_b128 v[60:63], v11 offset:0\n"
"ds_read_b128 v[64:67], v11 offset:1024\n"
"s_sub_u32 s17, s17, 1\n"
"s_cbranch_scc1 .Lok_390\n"
"s_waitcnt lgkmcnt(3)\n"
"v_cmp_eq_u32 vcc, 0, v3\n"
"s_cbranch_vccnz .Ltp_389\n"
"s_waitcnt lgkmcnt(0)\n"
".Lok_390:\n"
"s_waitcnt lgkmcnt(2)\n"
"v_add_f32 v56, v56, v112\n"
"v_add_f32 v57, v57, v113\n"
"v_add_f32 v58, v58, v114\n"
"v_add_f32 v59, v59, v115\n"
"v_cvt_pk_bf16_f32 v44, v56, v57\n"
"v_cvt_pk_bf16_f32 v45, v58, v59\n"
"v_and_b32 v44, v19, v44\n"
"v_and_b32 v45, v19, v45\n"
"s_waitcnt lgkmcnt(1)\n"
"v_add_f32 v48, v48, v60\n"
"v_add_f32 v49, v49, v61\n"
"v_add_f32 v50, v50, v62\n"
"v_add_f32 v51, v51, v63\n"
"v_cvt_pk_bf16_f32 v40, v48, v49\n"
"v_cvt_pk_bf16_f32 v41, v50, v51\n"
"v_mfma_f32_16x16x32_bf16 v[88:91], v[44:47], v[44:47], 0\n"
"v_mfma_f32_16x16x32_bf16 v[92:95], v[20:23], v[44:47], 0\n"
"s_waitcnt lgkmcnt(0)\n"
"v_add_f32 v52, v52, v64\n"
"v_add_f32 v53, v53, v65\n"
"v_add_f32 v54, v54, v66\n"
"v_add_f32 v55, v55, v67\n"
"v_cvt_pk_bf16_f32 v42, v52, v53\n"
"v_cvt_pk_bf16_f32 v43, v54, v55\n"
"v_cvt_pk_bf16_f32 v116, v48, v49\n"
"v_cvt_pk_bf16_f32 v118, v50, v51\n"
"v_mfma_f32_16x16x32_bf16 v[88:91], v[40:43], v[40:43], v[88:91]\n"
"v_mfma_f32_16x16x32_bf16 v[92:95], v[20:23], v[40:43], v[92:95]\n"
"v_cvt_pk_bf16_f32 v120, v52, v53\n"
"v_cvt_pk_bf16_f32 v122, v54, v55\n"
"v_cvt_pk_bf16_f32 v124, v56, v57\n"
"v_cvt_pk_bf16_f32 v126, v58, v59\n"
"s_nop 0\n"
"v_cndmask_b32 v101, v88, v90, s[44:45]\n"
"v_cndmask_b32 v88, v89, v91, s[44:45]\n"
"s_nop 0\n"
"v_min_u32 v100, -2, v92\n"
"v_min_u32 v88, -2, v88\n"
"v_min_u32 v101, -2, v101\n"
"v_mov_b32_dpp v102, v100 quad_perm:[1,0,3,2] row_mask:0xf bank_mask:0xf\n"
"v_mov_b32_dpp v103, v88 quad_perm:[1,0,3,2] row_mask:0xf bank_mask:0xf\n"
"buffer_store_dwordx4 v[100:103], v16, s[4:7], s38 offen sc1\n"
"buffer_store_dwordx4 v[116:119], v17, s[4:7], s37 offen offset:0 sc1\n"
"buffer_store_dwordx4 v[120:123], v17, s[4:7], s37 offen offset:1024 sc1\n"
"buffer_store_dwordx4 v[124:127], v18, s[4:7], s37 offen offset:2048 sc1\n"
"ds_write_b32 v4, v4 offset:176\n"
"s_xor_b32 s79, s3, 1\n"
"s_lshl_b32 s79, s79, 8\n"
"s_add_u32 s32, s19, s79\n"
"s_cmp_ge_u32 s32, 64\n"
"s_cbranch_scc1 .Lnr_391\n"
"s_lshl_b32 s79, s19, 11\n"
"s_add_u32 s32, s79, 0xbc8000\n"
"s_lshl_b32 s79, s19, 7\n"
"s_add_u32 s33, s79, 0xc08000\n"
"v_lshrrev_b32 v99, 3, v0\n"
"v_and_b32 v98, 7, v0\n"
"v_lshlrev_b32 v98, 4, v98\n"
"v_lshl_add_u32 v40, v99, 14, v98\n"
"v_mov_b32 v41, 0x3727c5ac\n"
"s_mov_b32 s89, 15\n"
".Ldl_392:\n"
"s_sub_u32 s89, s89, 1\n"
"s_cbranch_scc1 .Lde_393\n"
"s_sleep 1\n"
"s_branch .Ldl_392\n"
".Lde_393:\n"
"buffer_load_dwordx4 v[48:51], v2, s[4:7], s32 offen offset:0 sc1\n"
"buffer_load_dwordx4 v[52:55], v2, s[4:7], s32 offen offset:1024 sc1\n"
"s_mov_b32 s17, 32768\n"
".Ltp_394:\n"
"s_waitcnt vmcnt(0)\n"
"s_sub_u32 s17, s17, 1\n"
"s_cbranch_scc1 .Lou_395\n"
"v_max_u32 v38, v48, v49\n"
"v_max3_u32 v38, v50, v51, v38\n"
"v_max3_u32 v38, v52, v53, v38\n"
"v_max3_u32 v38, v54, v55, v38\n"
"v_cmp_eq_u32 vcc, -1, v38\n"
"s_cbranch_vccz .Lou_395\n"
"v_max_u32 v38, v50, v51\n"
"v_max3_u32 v38, v48, v49, v38\n"
"v_cmp_eq_u32 vcc, -1, v38\n"
"s_cbranch_vccz .Lsk_396\n"
"s_mov_b64 exec, vcc\n"
"buffer_load_dwordx4 v[48:51], v2, s[4:7], s32 offen offset:0 sc1\n"
"s_mov_b64 exec, -1\n"
".Lsk_396:\n"
"v_max_u32 v38, v54, v55\n"
"v_max3_u32 v38, v52, v53, v38\n"
"v_cmp_eq_u32 vcc, -1, v38\n"
"s_cbranch_vccz .Lsk_397\n"
"s_mov_b64 exec, vcc\n"
"buffer_load_dwordx4 v[52:55], v2, s[4:7], s32 offen offset:1024 sc1\n"
"s_mov_b64 exec, -1\n"
".Lsk_397:\n"
"s_branch .Ltp_394\n"
".Lou_395:\n"
"s_waitcnt vmcnt(0)\n"
"v_add_f32 v48, v48, v52\n"
"v_add_f32 v49, v49, v53\n"
"v_add_f32 v50, v50, v54\n"
"v_add_f32 v51, v51, v55\n"
"v_add_f32_dpp v52, v48, v48 row_ror:8 row_mask:0xf bank_mask:0xf\n"
"v_add_f32_dpp v53, v49, v49 row_ror:8 row_mask:0xf bank_mask:0xf\n"
"v_add_f32_dpp v54, v50, v50 row_ror:8 row_mask:0xf bank_mask:0xf\n"
"v_add_f32_dpp v55, v51, v51 row_ror:8 row_mask:0xf bank_mask:0xf\n"
"v_mov_b32 v48, v52\n"
"v_mov_b32 v49, v53\n"
"v_mov_b32 v50, v54\n"
"v_mov_b32 v51, v55\n"
"v_permlane16_swap_b32 v48, v52\n"
"v_permlane16_swap_b32 v49, v53\n"
"v_permlane16_swap_b32 v50, v54\n"
"v_permlane16_swap_b32 v51, v55\n"
"v_add_f32 v52, v48, v52\n"
"v_add_f32 v53, v49, v53\n"
"v_add_f32 v54, v50, v54\n"
"v_add_f32 v55, v51, v55\n"
"v_mov_b32 v48, v52\n"
"v_mov_b32 v49, v53\n"
"v_mov_b32 v50, v54\n"
"v_mov_b32 v51, v55\n"
"v_permlane32_swap_b32 v48, v52\n"
"v_permlane32_swap_b32 v49, v53\n"
"v_permlane32_swap_b32 v50, v54\n"
"v_permlane32_swap_b32 v51, v55\n"
"v_add_f32 v52, v48, v52\n"
"v_add_f32 v53, v49, v53\n"
"v_add_f32 v54, v50, v54\n"
"v_add_f32 v55, v51, v55\n"
"v_mul_f32 v48, 0x3acccccd, v52\n"
"v_mul_f32 v50, 0x3acccccd, v54\n"
"v_fma_f32 v53, -v52, v48, v53\n"
"v_fma_f32 v55, -v54, v50, v55\n"
"v_fmamk_f32 v53, v53, 0x3acccccd, v41\n"
"v_fmamk_f32 v55, v55, 0x3acccccd, v41\n"
"v_max_f32 v53, v53, v41\n"
"v_max_f32 v55, v55, v41\n"
"v_rsq_f32 v49, v53\n"
"v_rsq_f32 v51, v55\n"
"v_mul_f32 v48, -v48, v49\n"
"v_mul_f32 v50, -v50, v51\n"
"v_min_u32 v49, -2, v49\n"
"v_min_u32 v51, -2, v51\n"
"v_min_u32 v48, -2, v48\n"
"v_min_u32 v50, -2, v50\n"
"buffer_store_dwordx4 v[48:51], v40, s[4:7], s33 offen sc1\n"
"s_nop 3\n"
".Lnr_391:\n"
"v_lshlrev_b32 v99, 4, v0\n"
"v_mov_b32 v98, 0x40000000\n"
"v_cmp_gt_i32 vcc, 16, v0\n"
"s_nop 1\n"
"v_cndmask_b32 v43, v98, v99, vcc\n"
"v_mov_b32 v44, -1\n"
"v_mov_b32 v45, -1\n"
"v_mov_b32 v46, -1\n"
"v_mov_b32 v47, -1\n"
"s_add_u32 s49, s31, 0xa60000\n"
"buffer_store_dwordx4 v[44:47], v2, s[4:7], s49 offen offset:0 sc1\n"
"buffer_store_dwordx4 v[44:47], v43, s[4:7], s49 offen offset:1024 sc1\n"
"s_add_u32 s49, s31, 0xba0000\n"
"buffer_store_dwordx4 v[44:47], v2, s[4:7], s49 offen offset:0 sc1\n"
"buffer_store_dwordx4 v[44:47], v43, s[4:7], s49 offen offset:1024 sc1\n"
"s_lshl_b32 s79, s2, 2\n"
"s_add_u32 s79, s79, s3\n"
"s_lshl_b32 s79, s79, 10\n"
"s_add_u32 s49, s79, 0x1628000\n"
"s_sub_u32 s79, 0x1687fff, s49\n"
"s_and_b32 s79, s79, 0x80000000\n"
"s_or_b32 s49, s49, s79\n"
"buffer_store_dwordx4 v[44:47], v2, s[4:7], s49 offen sc1\n"
"s_nop 1\n"
"s_setprio 0\n"
"s_cmp_lg_u32 s43, 0\n"
"s_cbranch_scc1 .Lsk_399\n"
"s_call_b64 s[46:47], .Lst_332\n"
".Lsk_399:\n"
"s_cmp_lg_u32 s43, 1\n"
"s_cbranch_scc1 .Lsk_400\n"
"s_call_b64 s[46:47], .Lst_333\n"
".Lsk_400:\n"
"s_cmp_lg_u32 s43, 2\n"
"s_cbranch_scc1 .Lsk_401\n"
"ds_read_b32 v3, v5 offset:160\n"
"s_waitcnt lgkmcnt(0)\n"
"v_cmp_eq_u32 vcc, 0, v3\n"
"s_cbranch_vccz .Len_398\n"
"s_call_b64 s[46:47], .Lst_334\n"
".Lsk_401:\n"
".Len_398:\n"
"s_setprio 2\n"
"s_mul_i32 s79, s28, 0xa0\n"
"s_add_u32 s79, s30, s79\n"
"s_and_b32 s78, s3, 1\n"
"s_mul_i32 s78, s78, 0x60\n"
"s_add_u32 s79, s79, s78\n"
"s_add_u32 s36, s79, 0x140000\n"
"s_mul_i32 s79, s3, 0x1400\n"
"s_add_u32 s79, s30, s79\n"
"s_add_u32 s32, s79, 0x588000\n"
"s_add_u32 s33, s32, 0x1000\n"
"s_lshl_b32 s79, s29, 1\n"
"s_lshr_b32 s78, s3, 1\n"
"s_xor_b32 s78, s78, 1\n"
"s_add_u32 s79, s79, s78\n"
"s_mul_i32 s79, s79, 0x5000\n"
"s_mul_i32 s78, s3, 0x1400\n"
"s_add_u32 s79, s79, s78\n"
"s_add_u32 s34, s79, 0x588000\n"
"s_add_u32 s35, s34, 0x1000\n"
"s_lshl_b32 s79, s30, 1\n"
"s_mul_i32 s78, s28, 0x1400\n"
"s_add_u32 s79, s79, s78\n"
"s_and_b32 s78, s3, 1\n"
"s_mul_i32 s78, s78, 0xc00\n"
"s_add_u32 s79, s79, s78\n"
"s_add_u32 s37, s79, 0x948000\n"
"s_lshl_b32 s79, s29, 1\n"
"s_lshr_b32 s78, s3, 1\n"
"s_add_u32 s79, s79, s78\n"
"s_lshl_b32 s79, s79, 11\n"
"s_lshl_b32 s78, s28, 8\n"
"s_add_u32 s79, s79, s78\n"
"s_and_b32 s78, s3, 1\n"
"s_lshl_b32 s78, s78, 7\n"
"s_add_u32 s79, s79, s78\n"
"s_add_u32 s38, s79, 0xbe8000\n"
"buffer_load_dwordx2 v[40:41], v14, s[8:11], s36 offen offset:0\n"
"buffer_load_dwordx2 v[42:43], v14, s[8:11], s36 offen offset:32\n"
"buffer_load_dwordx2 v[44:45], v15, s[8:11], s36 offen offset:64\n"
"s_mov_b32 s17, 0x100000\n"
".Ltp_402:\n"
"ds_read_b32 v3, v5 offset:176\n"
"s_sub_u32 s17, s17, 1\n"
"s_cbranch_scc1 .Lou_403\n"
"s_waitcnt lgkmcnt(0)\n"
"v_cmp_eq_u32 vcc, 0, v3\n"
"s_cbranch_vccnz .Ltp_402\n"
".Lou_403:\n"
"s_waitcnt lgkmcnt(0)\n"
"s_waitcnt vmcnt(2)\n"
"v_lshlrev_b32 v48, 16, v40\n"
"v_and_b32 v49, 0xffff0000, v40\n"
"v_lshlrev_b32 v50, 16, v41\n"
"v_and_b32 v51, 0xffff0000, v41\n"
"s_waitcnt vmcnt(1)\n"
"v_lshlrev_b32 v52, 16, v42\n"
"v_and_b32 v53, 0xffff0000, v42\n"
"v_lshlrev_b32 v54, 16, v43\n"
"v_and_b32 v55, 0xffff0000, v43\n"
"s_waitcnt vmcnt(0)\n"
"v_lshlrev_b32 v56, 16, v44\n"
"v_and_b32 v57, 0xffff0000, v44\n"
"v_lshlrev_b32 v58, 16, v45\n"
"v_and_b32 v59, 0xffff0000, v45\n"
"v_mov_b32 v46, 0\n"
"v_mov_b32 v47, 0\n"
"s_mov_b32 s17, 0x100000\n"
".Ltp_404:\n"
"s_sleep 1\n"
"ds_read_b32 v3, v5 offset:160\n"
"s_sub_u32 s17, s17, 1\n"
"s_cbranch_scc1 .Lou_405\n"
"s_waitcnt lgkmcnt(0)\n"
"v_cmp_eq_u32 vcc, 0, v3\n"
"s_cbranch_vccnz .Ltp_404\n"
".Lou_405:\n"
"s_waitcnt lgkmcnt(0)\n"
"s_cmp_eq_u32 s17, 0xfffff\n"
"s_cbranch_scc1 .Lsk_406\n"
"s_mov_b32 s89, 24\n"
".Ldl_408:\n"
"s_sub_u32 s89, s89, 1\n"
"s_cbranch_scc1 .Lde_409\n"
"s_sleep 1\n"
"s_branch .Ldl_408\n"
".Lde_409:\n"
"s_branch .Ljn_407\n"
".Lsk_406:\n"
".Ljn_407:\n"
"buffer_load_dwordx4 v[108:111], v2, s[4:7], s34 offen offset:0 sc1\n"
"s_nop 9\n"
"buffer_load_dwordx4 v[112:115], v2, s[4:7], s34 offen offset:1024 sc1\n"
"s_nop 9\n"
"buffer_load_dwordx4 v[116:119], v2, s[4:7], s34 offen offset:2048 sc1\n"
"s_nop 9\n"
"buffer_load_dwordx4 v[120:123], v2, s[4:7], s34 offen offset:3072 sc1\n"
"s_nop 9\n"
"buffer_load_dwordx4 v[124:127], v2, s[4:7], s35 offen offset:0 sc1\n"
"s_nop 9\n"
"buffer_load_dwordx4 v[88:91], v2, s[4:7], s32 offen offset:0 sc1\n"
"s_nop 9\n"
"buffer_load_dwordx4 v[92:95], v2, s[4:7], s32 offen offset:1024 sc1\n"
"s_nop 9\n"
"buffer_load_dwordx4 v[96:99], v2, s[4:7], s32 offen offset:2048 sc1\n"
"s_nop 9\n"
"buffer_load_dwordx4 v[100:103], v2, s[4:7], s32 offen offset:3072 sc1\n"
"s_nop 9\n"
"buffer_load_dwordx4 v[104:107], v2, s[4:7], s33 offen offset:0 sc1\n"
"s_waitcnt vmcnt(9)\n"
"v_max_u32 v38, v108, v109\n"
"v_max3_u32 v38, v110, v111, v38\n"
"v_cmp_eq_u32 vcc, -1, v38\n"
"s_cbranch_vccnz .Lsl_410\n"
".Lbk_411:\n"
"v_mfma_f32_16x16x32_bf16 v[68:71], v[156:159], v[108:111], 0\n"
"v_mfma_f32_16x16x32_bf16 v[72:75], v[160:163], v[108:111], 0\n"
"v_mfma_f32_16x16x32_bf16 v[76:79], v[164:167], v[108:111], 0\n"
"s_waitcnt vmcnt(8)\n"
"v_max_u32 v38, v112, v113\n"
"v_max3_u32 v38, v114, v115, v38\n"
"v_mfma_f32_16x16x32_bf16 v[80:83], v[168:171], v[108:111], 0\n"
"v_cmp_eq_u32 vcc, -1, v38\n"
"v_mfma_f32_16x16x32_bf16 v[84:87], v[172:175], v[108:111], 0\n"
"s_cbranch_vccnz .Lsl_412\n"
".Lbk_413:\n"
"v_mfma_f32_16x16x32_bf16 v[68:71], v[176:179], v[112:115], v[68:71]\n"
"v_mfma_f32_16x16x32_bf16 v[72:75], v[180:183], v[112:115], v[72:75]\n"
"v_mfma_f32_16x16x32_bf16 v[76:79], v[184:187], v[112:115], v[76:79]\n"
"s_waitcnt vmcnt(7)\n"
"v_max_u32 v38, v116, v117\n"
"v_max3_u32 v38, v118, v119, v38\n"
"v_mfma_f32_16x16x32_bf16 v[80:83], v[188:191], v[112:115], v[80:83]\n"
"v_cmp_eq_u32 vcc, -1, v38\n"
"v_mfma_f32_16x16x32_bf16 v[84:87], v[192:195], v[112:115], v[84:87]\n"
"s_cbranch_vccnz .Lsl_414\n"
".Lbk_415:\n"
"v_mfma_f32_16x16x32_bf16 v[68:71], v[196:199], v[116:119], v[68:71]\n"
"v_mfma_f32_16x16x32_bf16 v[72:75], v[200:203], v[116:119], v[72:75]\n"
"v_mfma_f32_16x16x32_bf16 v[76:79], v[204:207], v[116:119], v[76:79]\n"
"s_waitcnt vmcnt(6)\n"
"v_max_u32 v38, v120, v121\n"
"v_max3_u32 v38, v122, v123, v38\n"
"v_mfma_f32_16x16x32_bf16 v[80:83], v[208:211], v[116:119], v[80:83]\n"
"v_cmp_eq_u32 vcc, -1, v38\n"
"v_mfma_f32_16x16x32_bf16 v[84:87], v[212:215], v[116:119], v[84:87]\n"
"s_cbranch_vccnz .Lsl_416\n"
".Lbk_417:\n"
"v_mfma_f32_16x16x32_bf16 v[68:71], v[216:219], v[120:123], v[68:71]\n"
"v_mfma_f32_16x16x32_bf16 v[72:75], v[220:223], v[120:123], v[72:75]\n"
"v_mfma_f32_16x16x32_bf16 v[76:79], v[224:227], v[120:123], v[76:79]\n"
"s_waitcnt vmcnt(5)\n"
"v_max_u32 v38, v124, v125\n"
"v_max3_u32 v38, v126, v127, v38\n"
"v_mfma_f32_16x16x32_bf16 v[80:83], v[228:231], v[120:123], v[80:83]\n"
"v_cmp_eq_u32 vcc, -1, v38\n"
"v_mfma_f32_16x16x32_bf16 v[84:87], v[232:235], v[120:123], v[84:87]\n"
"s_cbranch_vccnz .Lsl_418\n"
".Lbk_419:\n"
"v_mfma_f32_16x16x32_bf16 v[68:71], v[236:239], v[124:127], v[68:71]\n"
"v_mfma_f32_16x16x32_bf16 v[72:75], v[240:243], v[124:127], v[72:75]\n"
"v_mfma_f32_16x16x32_bf16 v[76:79], v[244:247], v[124:127], v[76:79]\n"
"s_waitcnt vmcnt(4)\n"
"v_max_u32 v38, v88, v89\n"
"v_max3_u32 v38, v90, v91, v38\n"
"v_mfma_f32_16x16x32_bf16 v[80:83], v[248:251], v[124:127], v[80:83]\n"
"v_cmp_eq_u32 vcc, -1, v38\n"
"v_mfma_f32_16x16x32_bf16 v[84:87], v[252:255], v[124:127], v[84:87]\n"
"ds_write_b128 v8, v[68:71] offset:0\n"
"s_cbranch_vccnz .Lsl_420\n"
".Lbk_421:\n"
"v_mfma_f32_16x16x32_bf16 v[56:59], v[164:167], v[88:91], v[56:59]\n"
"s_waitcnt vmcnt(3)\n"
"v_max_u32 v38, v92, v93\n"
"v_max3_u32 v38, v94, v95, v38\n"
"ds_write_b128 v8, v[72:75] offset:1024\n"
"v_mfma_f32_16x16x32_bf16 v[60:63], v[168:171], v[88:91], 0\n"
"v_cmp_eq_u32 vcc, -1, v38\n"
"ds_write_b128 v10, v[76:79] offset:0\n"
"v_mfma_f32_16x16x32_bf16 v[64:67], v[172:175], v[88:91], 0\n"
"v_mov_b32 v117, s16\n"
"s_cbranch_vccnz .Lsl_422\n"
".Lbk_423:\n"
"v_mfma_f32_16x16x32_bf16 v[56:59], v[184:187], v[92:95], v[56:59]\n"
"s_waitcnt vmcnt(2)\n"
"v_max_u32 v38, v96, v97\n"
"v_max3_u32 v38, v98, v99, v38\n"
"v_mov_b32 v119, s16\n"
"v_mfma_f32_16x16x32_bf16 v[60:63], v[188:191], v[92:95], v[60:63]\n"
"v_cmp_eq_u32 vcc, -1, v38\n"
"ds_write_b128 v9, v[80:83] offset:0\n"
"v_mfma_f32_16x16x32_bf16 v[64:67], v[192:195], v[92:95], v[64:67]\n"
"ds_write_b128 v9, v[84:87] offset:1024\n"
"s_cbranch_vccnz .Lsl_424\n"
".Lbk_425:\n"
"v_mfma_f32_16x16x32_bf16 v[56:59], v[204:207], v[96:99], v[56:59]\n"
"s_waitcnt vmcnt(1)\n"
"v_max_u32 v38, v100, v101\n"
"v_max3_u32 v38, v102, v103, v38\n"
"ds_write_b32 v4, v4 offset:640\n"
"v_mfma_f32_16x16x32_bf16 v[60:63], v[208:211], v[96:99], v[60:63]\n"
"v_cmp_eq_u32 vcc, -1, v38\n"
"v_mov_b32 v121, s16\n"
"v_mov_b32 v123, s16\n"
"v_mfma_f32_16x16x32_bf16 v[64:67], v[212:215], v[96:99], v[64:67]\n"
"v_mov_b32 v125, s16\n"
"s_cbranch_vccnz .Lsl_426\n"
".Lbk_427:\n"
"v_mfma_f32_16x16x32_bf16 v[56:59], v[224:227], v[100:103], v[56:59]\n"
"s_waitcnt vmcnt(0)\n"
"v_max_u32 v38, v104, v105\n"
"v_max3_u32 v38, v106, v107, v38\n"
"v_mov_b32 v127, s16\n"
"v_mfma_f32_16x16x32_bf16 v[60:63], v[228:231], v[100:103], v[60:63]\n"
"v_cmp_eq_u32 vcc, -1, v38\n"
"ds_read_b32 v3, v5 offset:640\n"
"v_mfma_f32_16x16x32_bf16 v[64:67], v[232:235], v[100:103], v[64:67]\n"
"s_cbranch_vccnz .Lsl_428\n"
".Lbk_429:\n"
"v_mfma_f32_16x16x32_bf16 v[56:59], v[244:247], v[104:107], v[56:59]\n"
"ds_read_b128 v[76:79], v12 offset:2048\n"
"ds_read_b128 v[108:111], v13 offset:2048\n"
"v_mfma_f32_16x16x32_bf16 v[60:63], v[248:251], v[104:107], v[60:63]\n"
"ds_read_b128 v[68:71], v12 offset:0\n"
"ds_read_b128 v[80:83], v13 offset:0\n"
"v_mfma_f32_16x16x32_bf16 v[64:67], v[252:255], v[104:107], v[64:67]\n"
"ds_read_b128 v[72:75], v12 offset:1024\n"
"ds_read_b128 v[84:87], v13 offset:1024\n"
"v_mfma_f32_16x16x32_bf16 v[48:51], v[156:159], v[88:91], v[48:51]\n"
"ds_write_b128 v7, v[56:59] offset:0\n"
"v_mfma_f32_16x16x32_bf16 v[52:55], v[160:163], v[88:91], v[52:55]\n"
"ds_write_b128 v6, v[60:63] offset:0\n"
"v_mfma_f32_16x16x32_bf16 v[48:51], v[176:179], v[92:95], v[48:51]\n"
"ds_write_b128 v6, v[64:67] offset:1024\n"
"v_mfma_f32_16x16x32_bf16 v[52:55], v[180:183], v[92:95], v[52:55]\n"
"ds_write_b32 v4, v4 offset:656\n"
"v_mfma_f32_16x16x32_bf16 v[48:51], v[196:199], v[96:99], v[48:51]\n"
"s_mov_b32 s17, 0x100000\n"
"s_waitcnt lgkmcnt(10)\n"
"v_cmp_eq_u32 vcc, 0, v3\n"
"s_cbranch_vccz .Lok_431\n"
".Ltp_430:\n"
"ds_read_b32 v3, v5 offset:640\n"
"ds_read_b128 v[76:79], v12 offset:2048\n"
"ds_read_b128 v[108:111], v13 offset:2048\n"
"ds_read_b128 v[68:71], v12 offset:0\n"
"ds_read_b128 v[80:83], v13 offset:0\n"
"ds_read_b128 v[72:75], v12 offset:1024\n"
"ds_read_b128 v[84:87], v13 offset:1024\n"
"s_sub_u32 s17, s17, 1\n"
"s_cbranch_scc1 .Lok_431\n"
"s_waitcnt lgkmcnt(6)\n"
"v_cmp_eq_u32 vcc, 0, v3\n"
"s_cbranch_vccnz .Ltp_430\n"
"s_waitcnt lgkmcnt(0)\n"
".Lok_431:\n"
"v_mfma_f32_16x16x32_bf16 v[52:55], v[200:203], v[96:99], v[52:55]\n"
"s_waitcnt lgkmcnt(8)\n"
"v_add_f32 v76, v76, v108\n"
"v_add_f32 v77, v77, v109\n"
"v_add_f32 v78, v78, v110\n"
"v_mfma_f32_16x16x32_bf16 v[48:51], v[216:219], v[100:103], v[48:51]\n"
"v_add_f32 v79, v79, v111\n"
"s_waitcnt lgkmcnt(6)\n"
"v_add_f32 v68, v68, v80\n"
"v_add_f32 v69, v69, v81\n"
"v_mfma_f32_16x16x32_bf16 v[52:55], v[220:223], v[100:103], v[52:55]\n"
"v_add_f32 v70, v70, v82\n"
"v_add_f32 v71, v71, v83\n"
"s_waitcnt lgkmcnt(4)\n"
"v_add_f32 v72, v72, v84\n"
"v_mfma_f32_16x16x32_bf16 v[48:51], v[236:239], v[104:107], v[48:51]\n"
"v_add_f32 v73, v73, v85\n"
"v_add_f32 v74, v74, v86\n"
"v_add_f32 v75, v75, v87\n"
"v_mfma_f32_16x16x32_bf16 v[52:55], v[240:243], v[104:107], v[52:55]\n"
"ds_read_b32 v3, v5 offset:656\n"
"ds_read_b128 v[112:115], v11 offset:2048\n"
"ds_read_b128 v[60:63], v11 offset:0\n"
"ds_read_b128 v[64:67], v11 offset:1024\n"
"v_add_f32 v56, v56, v76\n"
"v_add_f32 v57, v57, v77\n"
"v_add_f32 v58, v58, v78\n"
"v_add_f32 v59, v59, v79\n"
"v_add_f32 v48, v48, v68\n"
"v_add_f32 v49, v49, v69\n"
"v_add_f32 v50, v50, v70\n"
"v_add_f32 v51, v51, v71\n"
"v_add_f32 v52, v52, v72\n"
"v_add_f32 v53, v53, v73\n"
"v_add_f32 v54, v54, v74\n"
"v_add_f32 v55, v55, v75\n"
"s_mov_b32 s17, 0x100000\n"
"s_waitcnt lgkmcnt(3)\n"
"v_cmp_eq_u32 vcc, 0, v3\n"
"s_cbranch_vccz .Lok_433\n"
".Ltp_432:\n"
"ds_read_b32 v3, v5 offset:656\n"
"ds_read_b128 v[112:115], v11 offset:2048\n"
"ds_read_b128 v[60:63], v11 offset:0\n"
"ds_read_b128 v[64:67], v11 offset:1024\n"
"s_sub_u32 s17, s17, 1\n"
"s_cbranch_scc1 .Lok_433\n"
"s_waitcnt lgkmcnt(3)\n"
"v_cmp_eq_u32 vcc, 0, v3\n"
"s_cbranch_vccnz .Ltp_432\n"
"s_waitcnt lgkmcnt(0)\n"
".Lok_433:\n"
"s_waitcnt lgkmcnt(2)\n"
"v_add_f32 v56, v56, v112\n"
"v_add_f32 v57, v57, v113\n"
"v_add_f32 v58, v58, v114\n"
"v_add_f32 v59, v59, v115\n"
"v_cvt_pk_bf16_f32 v44, v56, v57\n"
"v_cvt_pk_bf16_f32 v45, v58, v59\n"
"v_and_b32 v44, v19, v44\n"
"v_and_b32 v45, v19, v45\n"
"s_waitcnt lgkmcnt(1)\n"
"v_add_f32 v48, v48, v60\n"
"v_add_f32 v49, v49, v61\n"
"v_add_f32 v50, v50, v62\n"
"v_add_f32 v51, v51, v63\n"
"v_cvt_pk_bf16_f32 v40, v48, v49\n"
"v_cvt_pk_bf16_f32 v41, v50, v51\n"
"v_mfma_f32_16x16x32_bf16 v[88:91], v[44:47], v[44:47], 0\n"
"v_mfma_f32_16x16x32_bf16 v[92:95], v[20:23], v[44:47], 0\n"
"s_waitcnt lgkmcnt(0)\n"
"v_add_f32 v52, v52, v64\n"
"v_add_f32 v53, v53, v65\n"
"v_add_f32 v54, v54, v66\n"
"v_add_f32 v55, v55, v67\n"
"v_cvt_pk_bf16_f32 v42, v52, v53\n"
"v_cvt_pk_bf16_f32 v43, v54, v55\n"
"v_cvt_pk_bf16_f32 v116, v48, v49\n"
"v_cvt_pk_bf16_f32 v118, v50, v51\n"
"v_mfma_f32_16x16x32_bf16 v[88:91], v[40:43], v[40:43], v[88:91]\n"
"v_mfma_f32_16x16x32_bf16 v[92:95], v[20:23], v[40:43], v[92:95]\n"
"v_cvt_pk_bf16_f32 v120, v52, v53\n"
"v_cvt_pk_bf16_f32 v122, v54, v55\n"
"v_cvt_pk_bf16_f32 v124, v56, v57\n"
"v_cvt_pk_bf16_f32 v126, v58, v59\n"
"s_nop 0\n"
"v_cndmask_b32 v101, v88, v90, s[44:45]\n"
"v_cndmask_b32 v88, v89, v91, s[44:45]\n"
"s_nop 0\n"
"v_min_u32 v100, -2, v92\n"
"v_min_u32 v88, -2, v88\n"
"v_min_u32 v101, -2, v101\n"
"v_mov_b32_dpp v102, v100 quad_perm:[1,0,3,2] row_mask:0xf bank_mask:0xf\n"
"v_mov_b32_dpp v103, v88 quad_perm:[1,0,3,2] row_mask:0xf bank_mask:0xf\n"
"buffer_store_dwordx4 v[100:103], v16, s[4:7], s38 offen sc1\n"
"buffer_store_dwordx4 v[116:119], v17, s[4:7], s37 offen offset:0 sc1\n"
"buffer_store_dwordx4 v[120:123], v17, s[4:7], s37 offen offset:1024 sc1\n"
"buffer_store_dwordx4 v[124:127], v18, s[4:7], s37 offen offset:2048 sc1\n"
"ds_write_b32 v4, v4 offset:256\n"
"s_xor_b32 s79, s3, 1\n"
"s_lshl_b32 s79, s79, 8\n"
"s_add_u32 s32, s19, s79\n"
"s_cmp_ge_u32 s32, 64\n"
"s_cbranch_scc1 .Lnr_434\n"
"s_lshl_b32 s79, s19, 11\n"
"s_add_u32 s32, s79, 0xbe8000\n"
"s_lshl_b32 s79, s19, 7\n"
"s_add_u32 s33, s79, 0xc0a000\n"
"v_lshrrev_b32 v99, 3, v0\n"
"v_and_b32 v98, 7, v0\n"
"v_lshlrev_b32 v98, 4, v98\n"
"v_lshl_add_u32 v40, v99, 14, v98\n"
"v_mov_b32 v41, 0x3727c5ac\n"
"s_mov_b32 s89, 12\n"
".Ldl_435:\n"
"s_sub_u32 s89, s89, 1\n"
"s_cbranch_scc1 .Lde_436\n"
"s_sleep 1\n"
"s_branch .Ldl_435\n"
".Lde_436:\n"
"buffer_load_dwordx4 v[48:51], v2, s[4:7], s32 offen offset:0 sc1\n"
"buffer_load_dwordx4 v[52:55], v2, s[4:7], s32 offen offset:1024 sc1\n"
"s_mov_b32 s17, 32768\n"
".Ltp_437:\n"
"s_waitcnt vmcnt(0)\n"
"s_sub_u32 s17, s17, 1\n"
"s_cbranch_scc1 .Lou_438\n"
"v_max_u32 v38, v48, v49\n"
"v_max3_u32 v38, v50, v51, v38\n"
"v_max3_u32 v38, v52, v53, v38\n"
"v_max3_u32 v38, v54, v55, v38\n"
"v_cmp_eq_u32 vcc, -1, v38\n"
"s_cbranch_vccz .Lou_438\n"
"v_max_u32 v38, v50, v51\n"
"v_max3_u32 v38, v48, v49, v38\n"
"v_cmp_eq_u32 vcc, -1, v38\n"
"s_cbranch_vccz .Lsk_439\n"
"s_mov_b64 exec, vcc\n"
"buffer_load_dwordx4 v[48:51], v2, s[4:7], s32 offen offset:0 sc1\n"
"s_mov_b64 exec, -1\n"
".Lsk_439:\n"
"v_max_u32 v38, v54, v55\n"
"v_max3_u32 v38, v52, v53, v38\n"
"v_cmp_eq_u32 vcc, -1, v38\n"
"s_cbranch_vccz .Lsk_440\n"
"s_mov_b64 exec, vcc\n"
"buffer_load_dwordx4 v[52:55], v2, s[4:7], s32 offen offset:1024 sc1\n"
"s_mov_b64 exec, -1\n"
".Lsk_440:\n"
"s_branch .Ltp_437\n"
".Lou_438:\n"
"s_waitcnt vmcnt(0)\n"
"v_add_f32 v48, v48, v52\n"
"v_add_f32 v49, v49, v53\n"
"v_add_f32 v50, v50, v54\n"
"v_add_f32 v51, v51, v55\n"
"v_add_f32_dpp v52, v48, v48 row_ror:8 row_mask:0xf bank_mask:0xf\n"
"v_add_f32_dpp v53, v49, v49 row_ror:8 row_mask:0xf bank_mask:0xf\n"
"v_add_f32_dpp v54, v50, v50 row_ror:8 row_mask:0xf bank_mask:0xf\n"
"v_add_f32_dpp v55, v51, v51 row_ror:8 row_mask:0xf bank_mask:0xf\n"
"v_mov_b32 v48, v52\n"
"v_mov_b32 v49, v53\n"
"v_mov_b32 v50, v54\n"
"v_mov_b32 v51, v55\n"
"v_permlane16_swap_b32 v48, v52\n"
"v_permlane16_swap_b32 v49, v53\n"
"v_permlane16_swap_b32 v50, v54\n"
"v_permlane16_swap_b32 v51, v55\n"
"v_add_f32 v52, v48, v52\n"
"v_add_f32 v53, v49, v53\n"
"v_add_f32 v54, v50, v54\n"
"v_add_f32 v55, v51, v55\n"
"v_mov_b32 v48, v52\n"
"v_mov_b32 v49, v53\n"
"v_mov_b32 v50, v54\n"
"v_mov_b32 v51, v55\n"
"v_permlane32_swap_b32 v48, v52\n"
"v_permlane32_swap_b32 v49, v53\n"
"v_permlane32_swap_b32 v50, v54\n"
"v_permlane32_swap_b32 v51, v55\n"
"v_add_f32 v52, v48, v52\n"
"v_add_f32 v53, v49, v53\n"
"v_add_f32 v54, v50, v54\n"
"v_add_f32 v55, v51, v55\n"
"v_mul_f32 v48, 0x3acccccd, v52\n"
"v_mul_f32 v50, 0x3acccccd, v54\n"
"v_fma_f32 v53, -v52, v48, v53\n"
"v_fma_f32 v55, -v54, v50, v55\n"
"v_fmamk_f32 v53, v53, 0x3acccccd, v41\n"
"v_fmamk_f32 v55, v55, 0x3acccccd, v41\n"
"v_max_f32 v53, v53, v41\n"
"v_max_f32 v55, v55, v41\n"
"v_rsq_f32 v49, v53\n"
"v_rsq_f32 v51, v55\n"
"v_mul_f32 v48, -v48, v49\n"
"v_mul_f32 v50, -v50, v51\n"
"v_min_u32 v49, -2, v49\n"
"v_min_u32 v51, -2, v51\n"
"v_min_u32 v48, -2, v48\n"
"v_min_u32 v50, -2, v50\n"
"buffer_store_dwordx4 v[48:51], v40, s[4:7], s33 offen sc1\n"
"s_nop 3\n"
".Lnr_434:\n"
"s_cmp_lt_i32 s18, 0\n"
"s_cbranch_scc1 .Lnc_441\n"
"s_nop 0\n"
"s_setprio 0\n"
"s_cmp_lg_u32 s43, 0\n"
"s_cbranch_scc1 .Lsk_443\n"
"s_call_b64 s[46:47], .Lst_332\n"
".Lsk_443:\n"
"s_cmp_lg_u32 s43, 1\n"
"s_cbranch_scc1 .Lsk_444\n"
"s_call_b64 s[46:47], .Lst_333\n"
".Lsk_444:\n"
"s_cmp_lg_u32 s43, 2\n"
"s_cbranch_scc1 .Lsk_445\n"
"s_call_b64 s[46:47], .Lst_334\n"
".Lsk_445:\n"
".Len_442:\n"
"s_setprio 2\n"
"s_mov_b32 s17, 0x100000\n"
".Ltp_446:\n"
"s_sleep 1\n"
"ds_read_b32 v3, v5 offset:208\n"
"s_sub_u32 s17, s17, 1\n"
"s_cbranch_scc1 .Lou_447\n"
"s_waitcnt lgkmcnt(0)\n"
"v_cmp_eq_u32 vcc, 0, v3\n"
"s_cbranch_vccnz .Ltp_446\n"
".Lou_447:\n"
"s_waitcnt lgkmcnt(0)\n"
"s_setprio 0\n"
"s_lshl_b32 s79, s18, 8\n"
"s_add_u32 s32, s79, 0x948000\n"
"s_add_u32 s33, s32, 0x140000\n"
"s_and_b32 s79, s2, 7\n"
"s_lshl_b32 s79, s79, 14\n"
"s_add_u32 s34, s79, 0xc0a000\n"
"s_add_u32 s35, s34, 0x1000\n"
"v_and_b32 v126, 0xffffff80, v29\n"
"v_add_u32 v126, v126, v29\n"
"ds_read_b128 v[88:91], v25 offset:448\n"
"ds_read_b128 v[92:95], v25 offset:432\n"
"ds_read_b64 v[96:97], v24 offset:448\n"
"ds_read_b64 v[98:99], v24 offset:456\n"
"ds_read_b128 v[100:103], v25 offset:960\n"
"ds_read_b128 v[104:107], v25 offset:1472\n"
"s_waitcnt lgkmcnt(1)\n"
"v_mfma_f32_16x16x32_f16 v[64:67], v[88:91], v[144:147], 0\n"
"ds_read_b128 v[40:43], v25 offset:1984\n"
"ds_read_b64 v[44:45], v24 offset:960\n"
"v_mfma_f32_16x16x32_f16 v[68:71], v[92:95], v[148:151], 0\n"
"ds_read_b64 v[46:47], v24 offset:968\n"
"ds_read_b64 v[72:73], v24 offset:1472\n"
"v_mfma_f32_16x16x32_f16 v[48:51], v[152:155], v[88:91], 0\n"
"ds_read_b64 v[74:75], v24 offset:1480\n"
"ds_read_b64 v[76:77], v24 offset:384\n"
"v_mfma_f32_16x16x32_f16 v[48:51], v[96:99], v[100:103], v[48:51]\n"
"ds_read_b64 v[78:79], v24 offset:392\n"
"s_waitcnt lgkmcnt(4)\n"
"v_mfma_f32_16x16x32_f16 v[52:55], v[96:99], v[104:107], 0\n"
"ds_read_b128 v[84:87], v25 offset:896\n"
"ds_read_b128 v[92:95], v25 offset:1408\n"
"v_mfma_f32_16x16x32_f16 v[56:59], v[96:99], v[40:43], 0\n"
"ds_read_b128 v[88:91], v25 offset:1920\n"
"ds_read_b64 v[100:101], v24 offset:896\n"
"v_mfma_f32_16x16x32_f16 v[52:55], v[44:47], v[40:43], v[52:55]\n"
"ds_read_b64 v[102:103], v24 offset:904\n"
"ds_read_b64 v[96:97], v24 offset:1408\n"
"v_cvt_pk_f16_f32 v80, v64, v65\n"
"v_mfma_f32_16x16x32_f16 v[48:51], v[44:47], v[104:107], v[48:51]\n"
"ds_read_b64 v[98:99], v24 offset:1416\n"
"s_waitcnt lgkmcnt(4)\n"
"v_mfma_f32_16x16x32_f16 v[48:51], v[72:75], v[40:43], v[48:51]\n"
"ds_read_b128 v[104:107], v25 offset:960\n"
"v_cvt_pk_f16_f32 v81, v66, v67\n"
"v_mfma_f32_16x16x32_f16 v[48:51], v[76:79], v[84:87], v[48:51]\n"
"ds_read_b128 v[44:47], v25 offset:944\n"
"v_cvt_pk_f16_f32 v82, v68, v69\n"
"v_mfma_f32_16x16x32_f16 v[52:55], v[76:79], v[92:95], v[52:55]\n"
"ds_read_b64 v[40:41], v24 offset:320\n"
"ds_read_b64 v[42:43], v24 offset:328\n"
"v_mfma_f32_16x16x32_f16 v[56:59], v[76:79], v[88:91], v[56:59]\n"
"ds_read_b128 v[72:75], v25 offset:832\n"
"s_waitcnt lgkmcnt(5)\n"
"v_mfma_f32_16x16x32_f16 v[52:55], v[100:103], v[88:91], v[52:55]\n"
"ds_read_b128 v[84:87], v25 offset:1344\n"
"v_cvt_pk_f16_f32 v83, v70, v71\n"
"v_mfma_f32_16x16x32_f16 v[48:51], v[100:103], v[92:95], v[48:51]\n"
"ds_read_b128 v[76:79], v25 offset:1856\n"
"ds_read_b64 v[92:93], v24 offset:832\n"
"v_mfma_f32_16x16x32_f16 v[48:51], v[96:99], v[88:91], v[48:51]\n"
"ds_read_b64 v[94:95], v24 offset:840\n"
"v_mfma_f32_16x16x32_f16 v[48:51], v[80:83], v[128:131], v[48:51]\n"
"ds_read_b64 v[100:101], v24 offset:1344\n"
"s_waitcnt lgkmcnt(9)\n"
"v_mfma_f32_16x16x32_f16 v[52:55], v[80:83], v[132:135], v[52:55]\n"
"ds_read_b64 v[102:103], v24 offset:1352\n"
"ds_read_b64 v[88:89], v24 offset:256\n"
"v_mfma_f32_16x16x32_f16 v[56:59], v[80:83], v[136:139], v[56:59]\n"
"ds_read_b64 v[90:91], v24 offset:264\n"
"v_mfma_f32_16x16x32_f16 v[60:63], v[80:83], v[140:143], 0\n"
"ds_read_b128 v[96:99], v25 offset:768\n"
"v_mfma_f32_16x16x32_f16 v[64:67], v[104:107], v[144:147], 0\n"
"s_waitcnt lgkmcnt(8)\n"
"v_mfma_f32_16x16x32_f16 v[68:71], v[44:47], v[148:151], 0\n"
"ds_read_b128 v[44:47], v25 offset:1280\n"
"v_mfma_f32_16x16x32_f16 v[52:55], v[152:155], v[104:107], v[52:55]\n"
"ds_read_b128 v[104:107], v25 offset:1792\n"
"v_mfma_f32_16x16x32_f16 v[48:51], v[40:43], v[72:75], v[48:51]\n"
"ds_read_b64 v[72:73], v24 offset:768\n"
"v_mfma_f32_16x16x32_f16 v[52:55], v[40:43], v[84:87], v[52:55]\n"
"ds_read_b64 v[74:75], v24 offset:776\n"
"s_waitcnt lgkmcnt(7)\n"
"v_mfma_f32_16x16x32_f16 v[56:59], v[40:43], v[76:79], v[56:59]\n"
"ds_read_b64 v[40:41], v24 offset:1280\n"
"v_mfma_f32_16x16x32_f16 v[52:55], v[92:95], v[76:79], v[52:55]\n"
"ds_read_b64 v[42:43], v24 offset:1288\n"
"v_cvt_pk_f16_f32 v80, v64, v65\n"
"v_mfma_f32_16x16x32_f16 v[48:51], v[92:95], v[84:87], v[48:51]\n"
"ds_read_b128 v[84:87], v25 offset:1472\n"
"v_cvt_pk_f16_f32 v81, v66, v67\n"
"v_mfma_f32_16x16x32_f16 v[48:51], v[100:103], v[76:79], v[48:51]\n"
"ds_read_b128 v[92:95], v25 offset:1456\n"
"s_waitcnt lgkmcnt(4)\n"
"v_mfma_f32_16x16x32_f16 v[48:51], v[88:91], v[96:99], v[48:51]\n"
"ds_read_b64 v[76:77], v24 offset:192\n"
"ds_read_b64 v[78:79], v24 offset:200\n"
"v_mfma_f32_16x16x32_f16 v[52:55], v[88:91], v[44:47], v[52:55]\n"
"ds_read_b128 v[100:103], v25 offset:704\n"
"v_cvt_pk_f16_f32 v82, v68, v69\n"
"v_mfma_f32_16x16x32_f16 v[56:59], v[88:91], v[104:107], v[56:59]\n"
"ds_read_b128 v[96:99], v25 offset:1216\n"
"v_cvt_pk_f16_f32 v83, v70, v71\n"
"v_mfma_f32_16x16x32_f16 v[52:55], v[72:75], v[104:107], v[52:55]\n"
"ds_read_b128 v[88:91], v25 offset:1728\n"
"s_waitcnt lgkmcnt(7)\n"
"v_mfma_f32_16x16x32_f16 v[48:51], v[72:75], v[44:47], v[48:51]\n"
"ds_read_b64 v[44:45], v24 offset:704\n"
"v_mfma_f32_16x16x32_f16 v[48:51], v[40:43], v[104:107], v[48:51]\n"
"ds_read_b64 v[46:47], v24 offset:712\n"
"ds_read_b64 v[72:73], v24 offset:1216\n"
"v_mfma_f32_16x16x32_f16 v[52:55], v[80:83], v[128:131], v[52:55]\n"
"ds_read_b64 v[74:75], v24 offset:1224\n"
"ds_read_b64 v[104:105], v24 offset:128\n"
"v_mfma_f32_16x16x32_f16 v[56:59], v[80:83], v[132:135], v[56:59]\n"
"ds_read_b64 v[106:107], v24 offset:136\n"
"s_waitcnt lgkmcnt(11)\n"
"v_mfma_f32_16x16x32_f16 v[60:63], v[80:83], v[136:139], v[60:63]\n"
"ds_read_b128 v[40:43], v25 offset:640\n"
"v_mfma_f32_16x16x32_f16 v[64:67], v[84:87], v[144:147], 0\n"
"v_mfma_f32_16x16x32_f16 v[68:71], v[92:95], v[148:151], 0\n"
"ds_read_b128 v[92:95], v25 offset:1152\n"
"v_mfma_f32_16x16x32_f16 v[56:59], v[152:155], v[84:87], v[56:59]\n"
"ds_read_b128 v[84:87], v25 offset:1664\n"
"s_waitcnt lgkmcnt(7)\n"
"v_mfma_f32_16x16x32_f16 v[48:51], v[76:79], v[100:103], v[48:51]\n"
"ds_read_b64 v[100:101], v24 offset:640\n"
"v_mfma_f32_16x16x32_f16 v[52:55], v[76:79], v[96:99], v[52:55]\n"
"ds_read_b64 v[102:103], v24 offset:648\n"
"v_mfma_f32_16x16x32_f16 v[56:59], v[76:79], v[88:91], v[56:59]\n"
"ds_read_b64 v[76:77], v24 offset:1152\n"
"v_mfma_f32_16x16x32_f16 v[52:55], v[44:47], v[88:91], v[52:55]\n"
"ds_read_b64 v[78:79], v24 offset:1160\n"
"s_waitcnt lgkmcnt(5)\n"
"v_mfma_f32_16x16x32_f16 v[48:51], v[44:47], v[96:99], v[48:51]\n"
"ds_read_b128 v[96:99], v25 offset:1984\n"
"v_cvt_pk_f16_f32 v80, v64, v65\n"
"v_mfma_f32_16x16x32_f16 v[48:51], v[72:75], v[88:91], v[48:51]\n"
"ds_read_b128 v[44:47], v25 offset:1968\n"
"ds_read_b64 v[88:89], v24 offset:64\n"
"v_mfma_f32_16x16x32_f16 v[48:51], v[104:107], v[40:43], v[48:51]\n"
"ds_read_b64 v[90:91], v24 offset:72\n"
"ds_read_b128 v[72:75], v25 offset:576\n"
"v_mfma_f32_16x16x32_f16 v[52:55], v[104:107], v[92:95], v[52:55]\n"
"ds_read_b128 v[40:43], v25 offset:1088\n"
"s_waitcnt lgkmcnt(6)\n"
"v_mfma_f32_16x16x32_f16 v[56:59], v[104:107], v[84:87], v[56:59]\n"
"ds_read_b128 v[104:107], v25 offset:1600\n"
"v_cvt_pk_f16_f32 v81, v66, v67\n"
"v_mfma_f32_16x16x32_f16 v[52:55], v[100:103], v[84:87], v[52:55]\n"
"v_cvt_pk_f16_f32 v82, v68, v69\n"
"v_cvt_pk_f16_f32 v83, v70, v71\n"
"v_mfma_f32_16x16x32_f16 v[48:51], v[100:103], v[92:95], v[48:51]\n"
"ds_read_b64 v[92:93], v24 offset:576\n"
"ds_read_b64 v[94:95], v24 offset:584\n"
"v_mfma_f32_16x16x32_f16 v[48:51], v[76:79], v[84:87], v[48:51]\n"
"ds_read_b64 v[100:101], v24 offset:1088\n"
"s_waitcnt lgkmcnt(8)\n"
"v_mfma_f32_16x16x32_f16 v[56:59], v[80:83], v[128:131], v[56:59]\n"
"ds_read_b64 v[102:103], v24 offset:1096\n"
"ds_read_b64 v[84:85], v24 offset:0\n"
"v_mfma_f32_16x16x32_f16 v[60:63], v[80:83], v[132:135], v[60:63]\n"
"ds_read_b64 v[86:87], v24 offset:8\n"
"v_mfma_f32_16x16x32_f16 v[64:67], v[96:99], v[144:147], 0\n"
"ds_read_b128 v[76:79], v25 offset:512\n"
"v_mfma_f32_16x16x32_f16 v[68:71], v[44:47], v[148:151], 0\n"
"ds_read_b128 v[44:47], v25 offset:1024\n"
"s_waitcnt lgkmcnt(8)\n"
"v_mfma_f32_16x16x32_f16 v[60:63], v[152:155], v[96:99], v[60:63]\n"
"ds_read_b128 v[96:99], v25 offset:1536\n"
"v_mfma_f32_16x16x32_f16 v[48:51], v[88:91], v[72:75], v[48:51]\n"
"ds_read_b64 v[72:73], v24 offset:512\n"
"v_mfma_f32_16x16x32_f16 v[52:55], v[88:91], v[40:43], v[52:55]\n"
"ds_read_b64 v[74:75], v24 offset:520\n"
"v_mfma_f32_16x16x32_f16 v[56:59], v[88:91], v[104:107], v[56:59]\n"
"ds_read_b64 v[88:89], v24 offset:1024\n"
"s_waitcnt lgkmcnt(5)\n"
"v_mfma_f32_16x16x32_f16 v[52:55], v[92:95], v[104:107], v[52:55]\n"
"ds_read_b64 v[90:91], v24 offset:1032\n"
"v_cvt_pk_f16_f32 v80, v64, v65\n"
"v_mfma_f32_16x16x32_f16 v[48:51], v[92:95], v[40:43], v[48:51]\n"
"v_cvt_pk_f16_f32 v81, v66, v67\n"
"v_cvt_pk_f16_f32 v82, v68, v69\n"
"v_mfma_f32_16x16x32_f16 v[48:51], v[100:103], v[104:107], v[48:51]\n"
"v_cvt_pk_f16_f32 v83, v70, v71\n"
"v_mfma_f32_16x16x32_f16 v[48:51], v[84:87], v[76:79], v[48:51]\n"
"s_waitcnt lgkmcnt(2)\n"
"v_mfma_f32_16x16x32_f16 v[52:55], v[84:87], v[44:47], v[52:55]\n"
"v_mfma_f32_16x16x32_f16 v[56:59], v[84:87], v[96:99], v[56:59]\n"
"v_mfma_f32_16x16x32_f16 v[52:55], v[72:75], v[96:99], v[52:55]\n"
"v_mfma_f32_16x16x32_f16 v[48:51], v[72:75], v[44:47], v[48:51]\n"
"s_waitcnt lgkmcnt(0)\n"
"v_mfma_f32_16x16x32_f16 v[48:51], v[88:91], v[96:99], v[48:51]\n"
"v_mfma_f32_16x16x32_f16 v[60:63], v[80:83], v[128:131], v[60:63]\n"
"s_setprio 2\n"
"s_mov_b32 s17, 0x100000\n"
".Ltp_448:\n"
"ds_read_b32 v3, v5 offset:256\n"
"s_sub_u32 s17, s17, 1\n"
"s_cbranch_scc1 .Lou_449\n"
"s_waitcnt lgkmcnt(0)\n"
"v_cmp_eq_u32 vcc, 0, v3\n"
"s_cbranch_vccnz .Ltp_448\n"
".Lou_449:\n"
"s_waitcnt lgkmcnt(0)\n"
"buffer_load_dwordx4 v[110:113], v126, s[4:7], s32 offen offset:0 sc1\n"
"buffer_load_dwordx4 v[114:117], v126, s[4:7], s32 offen offset:128 sc1\n"
"buffer_load_dwordx4 v[118:121], v126, s[4:7], s33 offen offset:0 sc1\n"
"buffer_load_dwordx4 v[122:125], v126, s[4:7], s33 offen offset:128 sc1\n"
"s_mov_b32 s38, 0x05040100\n"
"s_mov_b32 s39, 0x07060302\n"
"s_mov_b32 s17, 32768\n"
".Ltp_450:\n"
"s_waitcnt vmcnt(0)\n"
"s_sub_u32 s17, s17, 1\n"
"s_cbranch_scc1 .Lou_451\n"
"v_bitop3_b32 v64, v111, v113, s16 bitop3:0x7e\n"
"v_bitop3_b32 v65, v115, v117, s16 bitop3:0x7e\n"
"v_bitop3_b32 v66, v119, v121, s16 bitop3:0x7e\n"
"v_bitop3_b32 v67, v123, v125, s16 bitop3:0x7e\n"
"v_or3_b32 v68, v64, v65, v66\n"
"v_or_b32 v68, v68, v67\n"
"v_cmp_ne_u32 vcc, 0, v68\n"
"s_cbranch_vccz .Lou_451\n"
"s_sleep 2\n"
"v_cmp_ne_u32 vcc, 0, v64\n"
"s_cbranch_vccz .Lsk_452\n"
"s_mov_b64 exec, vcc\n"
"buffer_load_dwordx4 v[110:113], v126, s[4:7], s32 offen offset:0 sc1\n"
"s_mov_b64 exec, -1\n"
".Lsk_452:\n"
"v_cmp_ne_u32 vcc, 0, v65\n"
"s_cbranch_vccz .Lsk_453\n"
"s_mov_b64 exec, vcc\n"
"buffer_load_dwordx4 v[114:117], v126, s[4:7], s32 offen offset:128 sc1\n"
"s_mov_b64 exec, -1\n"
".Lsk_453:\n"
"v_cmp_ne_u32 vcc, 0, v66\n"
"s_cbranch_vccz .Lsk_454\n"
"s_mov_b64 exec, vcc\n"
"buffer_load_dwordx4 v[118:121], v126, s[4:7], s33 offen offset:0 sc1\n"
"s_mov_b64 exec, -1\n"
".Lsk_454:\n"
"v_cmp_ne_u32 vcc, 0, v67\n"
"s_cbranch_vccz .Lsk_455\n"
"s_mov_b64 exec, vcc\n"
"buffer_load_dwordx4 v[122:125], v126, s[4:7], s33 offen offset:128 sc1\n"
"s_mov_b64 exec, -1\n"
".Lsk_455:\n"
"s_branch .Ltp_450\n"
".Lou_451:\n"
"s_waitcnt vmcnt(0)\n"
"s_mov_b32 s89, 2\n"
".Ldl_456:\n"
"s_sub_u32 s89, s89, 1\n"
"s_cbranch_scc1 .Lde_457\n"
"s_sleep 1\n"
"s_branch .Ldl_456\n"
".Lde_457:\n"
"buffer_load_dwordx4 v[88:91], v30, s[4:7], s34 offen sc1\n"
"buffer_load_dwordx4 v[92:95], v30, s[4:7], s35 offen sc1\n"
"v_perm_b32 v64, v114, v110, s38\n"
"v_perm_b32 v65, v114, v110, s39\n"
"v_perm_b32 v66, v116, v112, s38\n"
"v_perm_b32 v67, v116, v112, s39\n"
"ds_write_b32 v32, v64 offset:0\n"
"ds_write_b32 v33, v65 offset:2048\n"
"ds_write_b32 v32, v66 offset:4224\n"
"ds_write_b32 v33, v67 offset:6272\n"
"v_perm_b32 v64, v122, v118, s38\n"
"v_perm_b32 v65, v122, v118, s39\n"
"v_perm_b32 v66, v124, v120, s38\n"
"v_perm_b32 v67, v124, v120, s39\n"
"ds_write_b32 v32, v64 offset:1024\n"
"ds_write_b32 v33, v65 offset:3072\n"
"ds_write_b32 v32, v66 offset:5248\n"
"ds_write_b32 v33, v67 offset:7296\n"
"v_lshlrev_b32 v64, 16, v110\n"
"v_lshlrev_b32 v65, 16, v114\n"
"v_and_b32 v66, 0xffff0000, v110\n"
"v_and_b32 v67, 0xffff0000, v114\n"
"v_lshlrev_b32 v68, 16, v112\n"
"v_lshlrev_b32 v69, 16, v116\n"
"v_and_b32 v70, 0xffff0000, v112\n"
"v_and_b32 v71, 0xffff0000, v116\n"
"v_lshlrev_b32 v72, 16, v118\n"
"v_lshlrev_b32 v73, 16, v122\n"
"v_and_b32 v74, 0xffff0000, v118\n"
"v_and_b32 v75, 0xffff0000, v122\n"
"v_lshlrev_b32 v76, 16, v120\n"
"v_lshlrev_b32 v77, 16, v124\n"
"v_and_b32 v78, 0xffff0000, v120\n"
"v_and_b32 v79, 0xffff0000, v124\n"
"s_mov_b32 s17, 32768\n"
".Ltp_458:\n"
"s_waitcnt vmcnt(0)\n"
"s_sub_u32 s17, s17, 1\n"
"s_cbranch_scc1 .Lou_459\n"
"v_max_u32 v110, v88, v89\n"
"v_max3_u32 v110, v90, v91, v110\n"
"v_max3_u32 v110, v92, v93, v110\n"
"v_max3_u32 v110, v94, v95, v110\n"
"v_cmp_eq_u32 vcc, -1, v110\n"
"s_cbranch_vccz .Lou_459\n"
"v_max_u32 v110, v90, v91\n"
"v_max3_u32 v110, v88, v89, v110\n"
"v_cmp_eq_u32 vcc, -1, v110\n"
"s_cbranch_vccz .Lsk_460\n"
"s_mov_b64 exec, vcc\n"
"buffer_load_dwordx4 v[88:91], v30, s[4:7], s34 offen sc1\n"
"s_mov_b64 exec, -1\n"
".Lsk_460:\n"
"v_max_u32 v110, v94, v95\n"
"v_max3_u32 v110, v92, v93, v110\n"
"v_cmp_eq_u32 vcc, -1, v110\n"
"s_cbranch_vccz .Lsk_461\n"
"s_mov_b64 exec, vcc\n"
"buffer_load_dwordx4 v[92:95], v30, s[4:7], s35 offen sc1\n"
"s_mov_b64 exec, -1\n"
".Lsk_461:\n"
"s_branch .Ltp_458\n"
".Lou_459:\n"
"s_waitcnt vmcnt(0)\n"
"v_fma_f32 v64, v64, v89, v88\n"
"v_fma_f32 v65, v65, v91, v90\n"
"v_cvt_pk_f16_f32 v64, v64, v65\n"
"v_fma_f32 v66, v66, v89, v88\n"
"v_fma_f32 v67, v67, v91, v90\n"
"v_cvt_pk_f16_f32 v66, v66, v67\n"
"v_fma_f32 v68, v68, v89, v88\n"
"v_fma_f32 v69, v69, v91, v90\n"
"v_cvt_pk_f16_f32 v68, v68, v69\n"
"v_fma_f32 v70, v70, v89, v88\n"
"v_fma_f32 v71, v71, v91, v90\n"
"v_cvt_pk_f16_f32 v70, v70, v71\n"
"ds_write2st64_b32 v31, v64, v66 offset0:0 offset1:18\n"
"ds_write2st64_b32 v31, v68, v70 offset0:36 offset1:54\n"
"v_fma_f32 v72, v72, v93, v92\n"
"v_fma_f32 v73, v73, v95, v94\n"
"v_cvt_pk_f16_f32 v72, v72, v73\n"
"v_fma_f32 v74, v74, v93, v92\n"
"v_fma_f32 v75, v75, v95, v94\n"
"v_cvt_pk_f16_f32 v74, v74, v75\n"
"v_fma_f32 v76, v76, v93, v92\n"
"v_fma_f32 v77, v77, v95, v94\n"
"v_cvt_pk_f16_f32 v76, v76, v77\n"
"v_fma_f32 v78, v78, v93, v92\n"
"v_fma_f32 v79, v79, v95, v94\n"
"v_cvt_pk_f16_f32 v78, v78, v79\n"
"ds_write2st64_b32 v31, v72, v74 offset0:4 offset1:22\n"
"ds_write2st64_b32 v31, v76, v78 offset0:40 offset1:58\n"
"ds_write_b32 v4, v4 offset:672\n"
"s_mov_b32 s17, 0x100000\n"
".Ltp_462:\n"
"ds_read_b32 v3, v5 offset:672\n"
"s_sub_u32 s17, s17, 1\n"
"s_cbranch_scc1 .Lou_463\n"
"s_waitcnt lgkmcnt(0)\n"
"v_cmp_eq_u32 vcc, 0, v3\n"
"s_cbranch_vccnz .Ltp_462\n"
".Lou_463:\n"
"s_waitcnt lgkmcnt(0)\n"
"s_mov_b32 s36, 0xfffefffe\n"
"ds_read_b64 v[88:89], v24 offset:1984\n"
"ds_read_b64 v[90:91], v24 offset:1992\n"
"ds_read_b128 v[92:95], v25 offset:4032\n"
"ds_read_b128 v[96:99], v25 offset:3520\n"
"ds_read_b64 v[100:101], v24 offset:1472\n"
"ds_read_b64 v[102:103], v24 offset:1480\n"
"ds_read_b128 v[104:107], v25 offset:3008\n"
"s_waitcnt lgkmcnt(0)\n"
"v_mfma_f32_16x16x32_f16 v[60:63], v[88:91], v[92:95], v[60:63]\n"
"ds_read_b64 v[108:109], v24 offset:960\n"
"ds_read_b64 v[110:111], v24 offset:968\n"
"v_mfma_f32_16x16x32_f16 v[56:59], v[88:91], v[96:99], v[56:59]\n"
"ds_read_b128 v[112:115], v25 offset:2496\n"
"ds_read_b64 v[116:117], v24 offset:448\n"
"v_mfma_f32_16x16x32_f16 v[60:63], v[100:103], v[96:99], v[60:63]\n"
"ds_read_b64 v[118:119], v24 offset:456\n"
"ds_read_b64 v[120:121], v24 offset:1920\n"
"v_mfma_f32_16x16x32_f16 v[52:55], v[88:91], v[104:107], v[52:55]\n"
"ds_read_b64 v[122:123], v24 offset:1928\n"
"s_waitcnt lgkmcnt(4)\n"
"v_mfma_f32_16x16x32_f16 v[56:59], v[100:103], v[104:107], v[56:59]\n"
"ds_read_b128 v[124:127], v25 offset:3968\n"
"ds_read_b128 v[40:43], v25 offset:3456\n"
"v_mfma_f32_16x16x32_f16 v[60:63], v[108:111], v[104:107], v[60:63]\n"
"ds_read_b64 v[44:45], v24 offset:1408\n"
"ds_read_b64 v[46:47], v24 offset:1416\n"
"v_mfma_f32_16x16x32_f16 v[48:51], v[88:91], v[112:115], v[48:51]\n"
"ds_read_b128 v[92:95], v25 offset:2944\n"
"ds_read_b64 v[96:97], v24 offset:896\n"
"v_mfma_f32_16x16x32_f16 v[52:55], v[100:103], v[112:115], v[52:55]\n"
"ds_read_b64 v[98:99], v24 offset:904\n"
"s_waitcnt lgkmcnt(5)\n"
"v_mfma_f32_16x16x32_f16 v[56:59], v[108:111], v[112:115], v[56:59]\n"
"ds_read_b128 v[104:107], v25 offset:2432\n"
"ds_read_b64 v[88:89], v24 offset:384\n"
"v_mfma_f32_16x16x32_f16 v[60:63], v[116:119], v[112:115], v[60:63]\n"
"ds_read_b64 v[90:91], v24 offset:392\n"
"ds_read_b64 v[100:101], v24 offset:1856\n"
"v_mfma_f32_16x16x32_f16 v[60:63], v[120:123], v[124:127], v[60:63]\n"
"ds_read_b64 v[102:103], v24 offset:1864\n"
"ds_read_b128 v[108:111], v25 offset:3904\n"
"v_mfma_f32_16x16x32_f16 v[56:59], v[120:123], v[40:43], v[56:59]\n"
"ds_read_b128 v[112:115], v25 offset:3392\n"
"s_waitcnt lgkmcnt(7)\n"
"v_mfma_f32_16x16x32_f16 v[60:63], v[44:47], v[40:43], v[60:63]\n"
"ds_read_b64 v[116:117], v26 offset:0\n"
"ds_read_b64 v[124:125], v24 offset:1344\n"
"v_mfma_f32_16x16x32_f16 v[52:55], v[120:123], v[92:95], v[52:55]\n"
"ds_read_b64 v[126:127], v24 offset:1352\n"
"v_mfma_f32_16x16x32_f16 v[56:59], v[44:47], v[92:95], v[56:59]\n"
"ds_read_b128 v[40:43], v25 offset:2880\n"
"v_mfma_f32_16x16x32_f16 v[60:63], v[96:99], v[92:95], v[60:63]\n"
"ds_read_b64 v[92:93], v24 offset:832\n"
"s_waitcnt lgkmcnt(9)\n"
"v_mfma_f32_16x16x32_f16 v[48:51], v[120:123], v[104:107], v[48:51]\n"
"ds_read_b64 v[94:95], v24 offset:840\n"
"v_mfma_f32_16x16x32_f16 v[52:55], v[44:47], v[104:107], v[52:55]\n"
"ds_read_b128 v[120:123], v25 offset:2368\n"
"v_mfma_f32_16x16x32_f16 v[56:59], v[96:99], v[104:107], v[56:59]\n"
"ds_read_b64 v[44:45], v24 offset:320\n"
"v_mfma_f32_16x16x32_f16 v[60:63], v[88:91], v[104:107], v[60:63]\n"
"ds_read_b64 v[46:47], v24 offset:328\n"
"s_waitcnt lgkmcnt(6)\n"
"v_mfma_f32_16x16x32_f16 v[60:63], v[100:103], v[108:111], v[60:63]\n"
"ds_read_b64 v[96:97], v26 offset:512\n"
"ds_read_b64 v[104:105], v24 offset:1792\n"
"v_mfma_f32_16x16x32_f16 v[56:59], v[100:103], v[112:115], v[56:59]\n"
"ds_read_b64 v[106:107], v24 offset:1800\n"
"ds_read_b128 v[88:91], v25 offset:3840\n"
"v_mfma_f32_16x16x16_bf16 v[48:51], v[34:35], v[116:117], v[48:51]\n"
"ds_read_b128 v[108:111], v25 offset:3328\n"
"ds_read_b64 v[116:117], v24 offset:1280\n"
"v_mfma_f32_16x16x32_f16 v[60:63], v[124:127], v[112:115], v[60:63]\n"
"ds_read_b64 v[118:119], v24 offset:1288\n"
"s_waitcnt lgkmcnt(9)\n"
"v_mfma_f32_16x16x32_f16 v[52:55], v[100:103], v[40:43], v[52:55]\n"
"ds_read_b128 v[112:115], v25 offset:2816\n"
"s_and_b32 s79, s18, 0xfc\n"
"v_mfma_f32_16x16x32_f16 v[56:59], v[124:127], v[40:43], v[56:59]\n"
"s_and_b32 s78, s18, 1\n"
"s_lshl_b32 s78, s78, 1\n"
"v_mfma_f32_16x16x32_f16 v[60:63], v[92:95], v[40:43], v[60:63]\n"
"ds_read_b64 v[40:41], v24 offset:768\n"
"s_add_u32 s79, s79, s78\n"
"v_mfma_f32_16x16x32_f16 v[48:51], v[100:103], v[120:123], v[48:51]\n"
"ds_read_b64 v[42:43], v24 offset:776\n"
"s_waitcnt lgkmcnt(9)\n"
"v_mfma_f32_16x16x32_f16 v[52:55], v[124:127], v[120:123], v[52:55]\n"
"ds_read_b128 v[100:103], v25 offset:2304\n"
"s_lshr_b32 s78, s18, 1\n"
"v_mfma_f32_16x16x32_f16 v[56:59], v[92:95], v[120:123], v[56:59]\n"
"ds_read_b64 v[124:125], v24 offset:256\n"
"s_and_b32 s78, s78, 1\n"
"v_mfma_f32_16x16x32_f16 v[60:63], v[44:47], v[120:123], v[60:63]\n"
"ds_read_b64 v[126:127], v24 offset:264\n"
"ds_read_b64 v[92:93], v24 offset:1728\n"
"v_mfma_f32_16x16x16_bf16 v[52:55], v[34:35], v[96:97], v[52:55]\n"
"ds_read_b64 v[94:95], v24 offset:1736\n"
"s_waitcnt lgkmcnt(7)\n"
"v_mfma_f32_16x16x32_f16 v[60:63], v[104:107], v[88:91], v[60:63]\n"
"ds_read_b128 v[120:123], v25 offset:3776\n"
"ds_read_b128 v[44:47], v25 offset:3264\n"
"v_mfma_f32_16x16x32_f16 v[56:59], v[104:107], v[108:111], v[56:59]\n"
"ds_read_b64 v[96:97], v24 offset:1216\n"
"ds_read_b64 v[98:99], v24 offset:1224\n"
"v_mfma_f32_16x16x32_f16 v[60:63], v[116:119], v[108:111], v[60:63]\n"
"ds_read_b128 v[88:91], v25 offset:2752\n"
"s_add_u32 s79, s79, s78\n"
"v_mfma_f32_16x16x32_f16 v[52:55], v[104:107], v[112:115], v[52:55]\n"
"ds_read_b64 v[108:109], v26 offset:1024\n"
"s_waitcnt lgkmcnt(10)\n"
"v_mfma_f32_16x16x32_f16 v[56:59], v[116:119], v[112:115], v[56:59]\n"
"s_lshl_b32 s79, s79, 7\n"
"s_add_u32 s32, s79, 0xd68000\n"
"v_mfma_f32_16x16x32_f16 v[60:63], v[40:43], v[112:115], v[60:63]\n"
"ds_read_b64 v[112:113], v24 offset:704\n"
"v_mfma_f32_16x16x32_f16 v[48:51], v[104:107], v[100:103], v[48:51]\n"
"ds_read_b64 v[114:115], v24 offset:712\n"
"v_mfma_f32_16x16x32_f16 v[52:55], v[116:119], v[100:103], v[52:55]\n"
"ds_read_b128 v[104:107], v25 offset:2240\n"
"s_waitcnt lgkmcnt(7)\n"
"v_mfma_f32_16x16x32_f16 v[56:59], v[40:43], v[100:103], v[56:59]\n"
"ds_read_b64 v[116:117], v24 offset:192\n"
"v_mfma_f32_16x16x32_f16 v[60:63], v[124:127], v[100:103], v[60:63]\n"
"ds_read_b64 v[118:119], v24 offset:200\n"
"ds_read_b64 v[40:41], v24 offset:1664\n"
"v_mfma_f32_16x16x32_f16 v[60:63], v[92:95], v[120:123], v[60:63]\n"
"ds_read_b64 v[42:43], v24 offset:1672\n"
"ds_read_b128 v[100:103], v25 offset:3712\n"
"v_mfma_f32_16x16x32_f16 v[56:59], v[92:95], v[44:47], v[56:59]\n"
"ds_read_b128 v[124:127], v25 offset:3200\n"
"s_waitcnt lgkmcnt(9)\n"
"v_mfma_f32_16x16x32_f16 v[60:63], v[96:99], v[44:47], v[60:63]\n"
"ds_read_b64 v[120:121], v24 offset:1152\n"
"ds_read_b64 v[122:123], v24 offset:1160\n"
"v_mfma_f32_16x16x32_f16 v[52:55], v[92:95], v[88:91], v[52:55]\n"
"ds_read_b128 v[44:47], v25 offset:2688\n"
"v_mfma_f32_16x16x16_bf16 v[56:59], v[34:35], v[108:109], v[56:59]\n"
"ds_read_b64 v[108:109], v24 offset:640\n"
"s_nop 4\n"
"v_mfma_f32_16x16x32_f16 v[56:59], v[96:99], v[88:91], v[56:59]\n"
"ds_read_b64 v[110:111], v24 offset:648\n"
"s_waitcnt lgkmcnt(11)\n"
"v_mfma_f32_16x16x32_f16 v[60:63], v[112:115], v[88:91], v[60:63]\n"
"ds_read_b128 v[88:91], v25 offset:2176\n"
"v_mfma_f32_16x16x32_f16 v[48:51], v[92:95], v[104:107], v[48:51]\n"
"ds_read_b64 v[92:93], v26 offset:1536\n"
"v_mfma_f32_16x16x32_f16 v[52:55], v[96:99], v[104:107], v[52:55]\n"
"v_mfma_f32_16x16x32_f16 v[56:59], v[112:115], v[104:107], v[56:59]\n"
"ds_read_b64 v[96:97], v24 offset:128\n"
"s_waitcnt lgkmcnt(6)\n"
"v_mfma_f32_16x16x32_f16 v[60:63], v[116:119], v[104:107], v[60:63]\n"
"ds_read_b64 v[98:99], v24 offset:136\n"
"ds_read_b64 v[112:113], v24 offset:1600\n"
"v_mfma_f32_16x16x32_f16 v[60:63], v[40:43], v[100:103], v[60:63]\n"
"ds_read_b64 v[114:115], v24 offset:1608\n"
"ds_read_b128 v[104:107], v25 offset:3648\n"
"v_mfma_f32_16x16x32_f16 v[56:59], v[40:43], v[124:127], v[56:59]\n"
"ds_read_b128 v[116:119], v25 offset:3136\n"
"ds_read_b64 v[100:101], v24 offset:1088\n"
"v_mfma_f32_16x16x32_f16 v[60:63], v[120:123], v[124:127], v[60:63]\n"
"ds_read_b64 v[102:103], v24 offset:1096\n"
"s_waitcnt lgkmcnt(9)\n"
"v_mfma_f32_16x16x32_f16 v[52:55], v[40:43], v[44:47], v[52:55]\n"
"ds_read_b128 v[124:127], v25 offset:2624\n"
"v_mfma_f32_16x16x32_f16 v[56:59], v[120:123], v[44:47], v[56:59]\n"
"v_mfma_f32_16x16x32_f16 v[60:63], v[108:111], v[44:47], v[60:63]\n"
"ds_read_b64 v[44:45], v24 offset:576\n"
"v_mfma_f32_16x16x32_f16 v[48:51], v[40:43], v[88:91], v[48:51]\n"
"ds_read_b64 v[46:47], v24 offset:584\n"
"s_waitcnt lgkmcnt(9)\n"
"s_nop 0\n"
"v_mfma_f32_16x16x16_bf16 v[60:63], v[34:35], v[92:93], v[60:63]\n"
"ds_read_b128 v[40:43], v25 offset:2112\n"
"v_mfma_f32_16x16x32_f16 v[52:55], v[120:123], v[88:91], v[52:55]\n"
"ds_read_b64 v[92:93], v24 offset:64\n"
"v_mfma_f32_16x16x32_f16 v[56:59], v[108:111], v[88:91], v[56:59]\n"
"ds_read_b64 v[94:95], v24 offset:72\n"
"ds_read_b64 v[120:121], v24 offset:1536\n"
"v_mfma_f32_16x16x32_f16 v[60:63], v[96:99], v[88:91], v[60:63]\n"
"ds_read_b64 v[122:123], v24 offset:1544\n"
"s_waitcnt lgkmcnt(7)\n"
"v_mfma_f32_16x16x32_f16 v[60:63], v[112:115], v[104:107], v[60:63]\n"
"ds_read_b128 v[108:111], v25 offset:2048\n"
"ds_read_b128 v[88:91], v25 offset:2560\n"
"v_mfma_f32_16x16x32_f16 v[56:59], v[112:115], v[116:119], v[56:59]\n"
"ds_read_b64 v[96:97], v24 offset:1024\n"
"ds_read_b64 v[98:99], v24 offset:1032\n"
"v_mfma_f32_16x16x32_f16 v[60:63], v[100:103], v[116:119], v[60:63]\n"
"ds_read_b128 v[104:107], v25 offset:3072\n"
"v_mfma_f32_16x16x32_f16 v[52:55], v[112:115], v[124:127], v[52:55]\n"
"ds_read_b64 v[116:117], v24 offset:512\n"
"s_waitcnt lgkmcnt(10)\n"
"v_mfma_f32_16x16x32_f16 v[56:59], v[100:103], v[124:127], v[56:59]\n"
"ds_read_b64 v[118:119], v24 offset:520\n"
"v_mfma_f32_16x16x32_f16 v[60:63], v[44:47], v[124:127], v[60:63]\n"
"ds_read_b128 v[124:127], v25 offset:3584\n"
"v_mfma_f32_16x16x32_f16 v[48:51], v[112:115], v[40:43], v[48:51]\n"
"v_mfma_f32_16x16x32_f16 v[52:55], v[100:103], v[40:43], v[52:55]\n"
"ds_read_b64 v[112:113], v24 offset:0\n"
"s_waitcnt lgkmcnt(7)\n"
"v_mfma_f32_16x16x32_f16 v[56:59], v[44:47], v[40:43], v[56:59]\n"
"ds_read_b64 v[114:115], v24 offset:8\n"
"v_mfma_f32_16x16x32_f16 v[60:63], v[92:95], v[40:43], v[60:63]\n"
"v_mfma_f32_16x16x32_f16 v[48:51], v[120:123], v[108:111], v[48:51]\n"
"v_mfma_f32_16x16x32_f16 v[52:55], v[120:123], v[88:91], v[52:55]\n"
"s_waitcnt lgkmcnt(3)\n"
"v_mfma_f32_16x16x32_f16 v[52:55], v[96:99], v[108:111], v[52:55]\n"
"v_mfma_f32_16x16x32_f16 v[56:59], v[120:123], v[104:107], v[56:59]\n"
"v_mul_f32 v48, v36, v48\n"
"v_mul_f32 v49, v36, v49\n"
"v_mul_f32 v50, v36, v50\n"
"v_mfma_f32_16x16x32_f16 v[56:59], v[96:99], v[88:91], v[56:59]\n"
"v_mul_f32 v51, v36, v51\n"
"v_cvt_pk_bf16_f32 v48, v48, v49\n"
"v_cvt_pk_bf16_f32 v49, v50, v51\n"
"v_mfma_f32_16x16x32_f16 v[56:59], v[116:119], v[108:111], v[56:59]\n"
"v_pk_min_u16 v48, v48, s36\n"
"v_pk_min_u16 v49, v49, s36\n"
"s_waitcnt lgkmcnt(0)\n"
"v_mfma_f32_16x16x32_f16 v[60:63], v[120:123], v[124:127], v[60:63]\n"
"ds_write_b64 v26, v[48:49] offset:0\n"
"v_mfma_f32_16x16x32_f16 v[60:63], v[96:99], v[104:107], v[60:63]\n"
"v_mul_f32 v52, v36, v52\n"
"v_mul_f32 v53, v36, v53\n"
"v_mfma_f32_16x16x32_f16 v[60:63], v[116:119], v[88:91], v[60:63]\n"
"v_mul_f32 v54, v36, v54\n"
"v_mul_f32 v55, v36, v55\n"
"v_mfma_f32_16x16x32_f16 v[60:63], v[112:115], v[108:111], v[60:63]\n"
"v_cvt_pk_bf16_f32 v52, v52, v53\n"
"v_cvt_pk_bf16_f32 v53, v54, v55\n"
"v_pk_min_u16 v52, v52, s36\n"
"v_pk_min_u16 v53, v53, s36\n"
"ds_write_b64 v26, v[52:53] offset:512\n"
"v_mul_f32 v56, v36, v56\n"
"v_mul_f32 v57, v36, v57\n"
"v_mul_f32 v58, v36, v58\n"
"v_mul_f32 v59, v36, v59\n"
"v_cvt_pk_bf16_f32 v56, v56, v57\n"
"v_cvt_pk_bf16_f32 v57, v58, v59\n"
"v_pk_min_u16 v56, v56, s36\n"
"v_pk_min_u16 v57, v57, s36\n"
"ds_write_b64 v26, v[56:57] offset:1024\n"
"v_mul_f32 v60, v36, v60\n"
"v_mul_f32 v61, v36, v61\n"
"v_mul_f32 v62, v36, v62\n"
"v_mul_f32 v63, v36, v63\n"
"v_cvt_pk_bf16_f32 v60, v60, v61\n"
"v_cvt_pk_bf16_f32 v61, v62, v63\n"
"v_pk_min_u16 v60, v60, s36\n"
"v_pk_min_u16 v61, v61, s36\n"
"ds_write_b64 v26, v[60:61] offset:1536\n"
"ds_write_b32 v4, v4 offset:688\n"
"s_mov_b32 s17, 0x100000\n"
".Ltp_464:\n"
"ds_read_b32 v3, v5 offset:688\n"
"s_sub_u32 s17, s17, 1\n"
"s_cbranch_scc1 .Lou_465\n"
"s_waitcnt lgkmcnt(0)\n"
"v_cmp_eq_u32 vcc, 0, v3\n"
"s_cbranch_vccnz .Ltp_464\n"
".Lou_465:\n"
"s_waitcnt lgkmcnt(0)\n"
"ds_read_b64_tr_b16 v[64:65], v27 offset:0\n"
"ds_read_b64_tr_b16 v[66:67], v28 offset:0\n"
"ds_read_b64_tr_b16 v[68:69], v27 offset:1024\n"
"ds_read_b64_tr_b16 v[70:71], v28 offset:1024\n"
"s_add_u32 s33, s32, 0xa0000\n"
"s_waitcnt lgkmcnt(2)\n"
"buffer_store_dwordx4 v[64:67], v29, s[4:7], s32 offen sc1\n"
"s_waitcnt lgkmcnt(0)\n"
"buffer_store_dwordx4 v[68:71], v29, s[4:7], s33 offen sc1\n"
"ds_write_b32 v4, v4 offset:704\n"
"s_nop 0\n"
"s_branch .Ljc_466\n"
".Lnc_441:\n"
"s_mov_b32 s17, 0x100000\n"
".Ltp_467:\n"
"s_sleep 2\n"
"ds_read_b32 v3, v5 offset:272\n"
"s_sub_u32 s17, s17, 1\n"
"s_cbranch_scc1 .Lou_468\n"
"s_waitcnt lgkmcnt(0)\n"
"v_cmp_eq_u32 vcc, 0, v3\n"
"s_cbranch_vccnz .Ltp_467\n"
".Lou_468:\n"
"s_waitcnt lgkmcnt(0)\n"
"s_and_b32 s79, s2, 7\n"
"s_lshl_b32 s79, s79, 14\n"
"s_add_u32 s32, s79, 0xc0a000\n"
"s_add_u32 s33, s32, 0x1000\n"
"buffer_load_dwordx4 v[48:51], v2, s[4:7], s32 offen offset:0 sc1\n"
"buffer_load_dwordx4 v[52:55], v2, s[4:7], s32 offen offset:1024 sc1\n"
"buffer_load_dwordx4 v[56:59], v2, s[4:7], s32 offen offset:2048 sc1\n"
"buffer_load_dwordx4 v[60:63], v2, s[4:7], s32 offen offset:3072 sc1\n"
"buffer_load_dwordx4 v[64:67], v2, s[4:7], s33 offen offset:0 sc1\n"
"buffer_load_dwordx4 v[68:71], v2, s[4:7], s33 offen offset:1024 sc1\n"
"buffer_load_dwordx4 v[72:75], v2, s[4:7], s33 offen offset:2048 sc1\n"
"buffer_load_dwordx4 v[76:79], v2, s[4:7], s33 offen offset:3072 sc1\n"
"s_mov_b32 s17, 32768\n"
".Ltp_469:\n"
"s_waitcnt vmcnt(0)\n"
"s_sub_u32 s17, s17, 1\n"
"s_cbranch_scc1 .Lou_470\n"
"v_max_u32 v80, v48, v49\n"
"v_max3_u32 v80, v50, v51, v80\n"
"v_max3_u32 v80, v52, v53, v80\n"
"v_max3_u32 v80, v54, v55, v80\n"
"v_max3_u32 v80, v56, v57, v80\n"
"v_max3_u32 v80, v58, v59, v80\n"
"v_max3_u32 v80, v60, v61, v80\n"
"v_max3_u32 v80, v62, v63, v80\n"
"v_max3_u32 v80, v64, v65, v80\n"
"v_max3_u32 v80, v66, v67, v80\n"
"v_max3_u32 v80, v68, v69, v80\n"
"v_max3_u32 v80, v70, v71, v80\n"
"v_max3_u32 v80, v72, v73, v80\n"
"v_max3_u32 v80, v74, v75, v80\n"
"v_max3_u32 v80, v76, v77, v80\n"
"v_max3_u32 v80, v78, v79, v80\n"
"v_cmp_eq_u32 vcc, -1, v80\n"
"s_cbranch_vccz .Lou_470\n"
"s_sleep 4\n"
"v_max_u32 v80, v50, v51\n"
"v_max3_u32 v80, v48, v49, v80\n"
"v_cmp_eq_u32 vcc, -1, v80\n"
"s_cbranch_vccz .Lsk_471\n"
"s_mov_b64 exec, vcc\n"
"buffer_load_dwordx4 v[48:51], v2, s[4:7], s32 offen offset:0 sc1\n"
"s_mov_b64 exec, -1\n"
".Lsk_471:\n"
"v_max_u32 v80, v54, v55\n"
"v_max3_u32 v80, v52, v53, v80\n"
"v_cmp_eq_u32 vcc, -1, v80\n"
"s_cbranch_vccz .Lsk_472\n"
"s_mov_b64 exec, vcc\n"
"buffer_load_dwordx4 v[52:55], v2, s[4:7], s32 offen offset:1024 sc1\n"
"s_mov_b64 exec, -1\n"
".Lsk_472:\n"
"v_max_u32 v80, v58, v59\n"
"v_max3_u32 v80, v56, v57, v80\n"
"v_cmp_eq_u32 vcc, -1, v80\n"
"s_cbranch_vccz .Lsk_473\n"
"s_mov_b64 exec, vcc\n"
"buffer_load_dwordx4 v[56:59], v2, s[4:7], s32 offen offset:2048 sc1\n"
"s_mov_b64 exec, -1\n"
".Lsk_473:\n"
"v_max_u32 v80, v62, v63\n"
"v_max3_u32 v80, v60, v61, v80\n"
"v_cmp_eq_u32 vcc, -1, v80\n"
"s_cbranch_vccz .Lsk_474\n"
"s_mov_b64 exec, vcc\n"
"buffer_load_dwordx4 v[60:63], v2, s[4:7], s32 offen offset:3072 sc1\n"
"s_mov_b64 exec, -1\n"
".Lsk_474:\n"
"v_max_u32 v80, v66, v67\n"
"v_max3_u32 v80, v64, v65, v80\n"
"v_cmp_eq_u32 vcc, -1, v80\n"
"s_cbranch_vccz .Lsk_475\n"
"s_mov_b64 exec, vcc\n"
"buffer_load_dwordx4 v[64:67], v2, s[4:7], s33 offen offset:0 sc1\n"
"s_mov_b64 exec, -1\n"
".Lsk_475:\n"
"v_max_u32 v80, v70, v71\n"
"v_max3_u32 v80, v68, v69, v80\n"
"v_cmp_eq_u32 vcc, -1, v80\n"
"s_cbranch_vccz .Lsk_476\n"
"s_mov_b64 exec, vcc\n"
"buffer_load_dwordx4 v[68:71], v2, s[4:7], s33 offen offset:1024 sc1\n"
"s_mov_b64 exec, -1\n"
".Lsk_476:\n"
"v_max_u32 v80, v74, v75\n"
"v_max3_u32 v80, v72, v73, v80\n"
"v_cmp_eq_u32 vcc, -1, v80\n"
"s_cbranch_vccz .Lsk_477\n"
"s_mov_b64 exec, vcc\n"
"buffer_load_dwordx4 v[72:75], v2, s[4:7], s33 offen offset:2048 sc1\n"
"s_mov_b64 exec, -1\n"
".Lsk_477:\n"
"v_max_u32 v80, v78, v79\n"
"v_max3_u32 v80, v76, v77, v80\n"
"v_cmp_eq_u32 vcc, -1, v80\n"
"s_cbranch_vccz .Lsk_478\n"
"s_mov_b64 exec, vcc\n"
"buffer_load_dwordx4 v[76:79], v2, s[4:7], s33 offen offset:3072 sc1\n"
"s_mov_b64 exec, -1\n"
".Lsk_478:\n"
"s_branch .Ltp_469\n"
".Lou_470:\n"
"s_waitcnt vmcnt(0)\n"
".Ljc_466:\n"
"s_setprio 0\n"
"s_lshr_b32 s79, s2, 1\n"
"s_and_b32 s79, s79, 3\n"
"s_lshl_b32 s79, s79, 2\n"
"s_lshr_b32 s78, s2, 3\n"
"s_lshr_b32 s78, s78, 3\n"
"s_add_u32 s79, s79, s78\n"
"s_lshl_b32 s79, s79, 2\n"
"s_add_u32 s79, s79, 64\n"
"s_mul_i32 s79, s79, 0x5000\n"
"s_mul_i32 s78, s3, 0x1400\n"
"s_add_u32 s79, s79, s78\n"
"s_add_u32 s50, s79, 0xc28000\n"
"s_add_u32 s51, s50, 0x1000\n"
"v_min_i32 v99, 3, v0\n"
"v_mul_i32_i24 v48, 0x500, v99\n"
"s_mul_i32 s79, s3, 0x1400\n"
"s_add_u32 s79, s79, 0xa000\n"
"v_add_u32 v50, s79, v2\n"
"s_and_b32 s79, s2, 7\n"
"s_lshl_b32 s79, s79, 3\n"
"s_add_u32 s79, s79, 0x47\n"
"s_mul_i32 s79, s79, 0x5000\n"
"s_and_b32 s78, s2, 7\n"
"s_lshr_b32 s78, s78, 1\n"
"s_lshl_b32 s78, s78, 7\n"
"s_add_u32 s79, s79, s78\n"
"s_mul_i32 s78, s3, 0x1400\n"
"s_add_u32 s79, s79, s78\n"
"s_add_u32 s49, s79, 0xc28000\n"
"s_mov_b32 s17, 0x100000\n"
".Ltp_479:\n"
"s_sleep 2\n"
"ds_read_b32 v3, v5 offset:3264\n"
"s_sub_u32 s17, s17, 1\n"
"s_cbranch_scc1 .Lou_480\n"
"s_waitcnt lgkmcnt(0)\n"
"v_cmp_eq_u32 vcc, -1, v3\n"
"s_cbranch_vccnz .Ltp_479\n"
".Lou_480:\n"
"s_waitcnt lgkmcnt(0)\n"
"s_mov_b32 s89, 8\n"
".Ldl_481:\n"
"s_sub_u32 s89, s89, 1\n"
"s_cbranch_scc1 .Lde_482\n"
"s_sleep 1\n"
"s_branch .Ldl_481\n"
".Lde_482:\n"
"s_mov_b32 s17, 32768\n"
".Ltp_483:\n"
"buffer_load_dword v49, v48, s[4:7], s49 offen sc1\n"
"s_sub_u32 s17, s17, 1\n"
"s_cbranch_scc1 .Lou_484\n"
"s_waitcnt vmcnt(0)\n"
"v_cmp_eq_u32 vcc, -1, v49\n"
"s_cbranch_vccz .Lou_484\n"
"s_mov_b32 s89, 2\n"
".Ldl_485:\n"
"s_sub_u32 s89, s89, 1\n"
"s_cbranch_scc1 .Lde_486\n"
"s_sleep 1\n"
"s_branch .Ldl_485\n"
".Lde_486:\n"
"s_branch .Ltp_483\n"
".Lou_484:\n"
"s_waitcnt vmcnt(0)\n"
"buffer_load_dwordx4 v[52:55], v2, s[4:7], s50 offen offset:0 sc1\n"
"buffer_load_dwordx4 v[56:59], v2, s[4:7], s50 offen offset:1024 sc1\n"
"buffer_load_dwordx4 v[60:63], v2, s[4:7], s50 offen offset:2048 sc1\n"
"buffer_load_dwordx4 v[64:67], v2, s[4:7], s50 offen offset:3072 sc1\n"
"buffer_load_dwordx4 v[68:71], v2, s[4:7], s51 offen offset:0 sc1\n"
"s_mov_b32 s17, 32768\n"
".Ltp_487:\n"
"s_waitcnt vmcnt(0)\n"
"s_sub_u32 s17, s17, 1\n"
"s_cbranch_scc1 .Lou_488\n"
"v_max_u32 v38, v52, v53\n"
"v_max3_u32 v38, v54, v55, v38\n"
"v_max3_u32 v38, v56, v57, v38\n"
"v_max3_u32 v38, v58, v59, v38\n"
"v_max3_u32 v38, v60, v61, v38\n"
"v_max3_u32 v38, v62, v63, v38\n"
"v_max3_u32 v38, v64, v65, v38\n"
"v_max3_u32 v38, v66, v67, v38\n"
"v_max3_u32 v38, v68, v69, v38\n"
"v_max3_u32 v38, v70, v71, v38\n"
"v_cmp_eq_u32 vcc, -1, v38\n"
"s_cbranch_vccz .Lou_488\n"
"s_sleep 1\n"
"v_max_u32 v38, v54, v55\n"
"v_max3_u32 v38, v52, v53, v38\n"
"v_cmp_eq_u32 vcc, -1, v38\n"
"s_cbranch_vccz .Lsk_489\n"
"s_mov_b64 exec, vcc\n"
"buffer_load_dwordx4 v[52:55], v2, s[4:7], s50 offen offset:0 sc1\n"
"s_mov_b64 exec, -1\n"
".Lsk_489:\n"
"v_max_u32 v38, v58, v59\n"
"v_max3_u32 v38, v56, v57, v38\n"
"v_cmp_eq_u32 vcc, -1, v38\n"
"s_cbranch_vccz .Lsk_490\n"
"s_mov_b64 exec, vcc\n"
"buffer_load_dwordx4 v[56:59], v2, s[4:7], s50 offen offset:1024 sc1\n"
"s_mov_b64 exec, -1\n"
".Lsk_490:\n"
"v_max_u32 v38, v62, v63\n"
"v_max3_u32 v38, v60, v61, v38\n"
"v_cmp_eq_u32 vcc, -1, v38\n"
"s_cbranch_vccz .Lsk_491\n"
"s_mov_b64 exec, vcc\n"
"buffer_load_dwordx4 v[60:63], v2, s[4:7], s50 offen offset:2048 sc1\n"
"s_mov_b64 exec, -1\n"
".Lsk_491:\n"
"v_max_u32 v38, v66, v67\n"
"v_max3_u32 v38, v64, v65, v38\n"
"v_cmp_eq_u32 vcc, -1, v38\n"
"s_cbranch_vccz .Lsk_492\n"
"s_mov_b64 exec, vcc\n"
"buffer_load_dwordx4 v[64:67], v2, s[4:7], s50 offen offset:3072 sc1\n"
"s_mov_b64 exec, -1\n"
".Lsk_492:\n"
"v_max_u32 v38, v70, v71\n"
"v_max3_u32 v38, v68, v69, v38\n"
"v_cmp_eq_u32 vcc, -1, v38\n"
"s_cbranch_vccz .Lsk_493\n"
"s_mov_b64 exec, vcc\n"
"buffer_load_dwordx4 v[68:71], v2, s[4:7], s51 offen offset:0 sc1\n"
"s_mov_b64 exec, -1\n"
".Lsk_493:\n"
"s_branch .Ltp_487\n"
".Lou_488:\n"
"s_waitcnt vmcnt(0)\n"
"s_mov_b32 s17, 0x100000\n"
".Ltp_494:\n"
"s_sleep 2\n"
"ds_read_b32 v3, v5 offset:3584\n"
"s_sub_u32 s17, s17, 1\n"
"s_cbranch_scc1 .Lou_495\n"
"s_waitcnt lgkmcnt(0)\n"
"v_cmp_eq_u32 vcc, -1, v3\n"
"s_cbranch_vccnz .Ltp_494\n"
".Lou_495:\n"
"s_waitcnt lgkmcnt(0)\n"
"ds_write_b128 v50, v[52:55] offset:0\n"
"ds_write_b128 v50, v[56:59] offset:1024\n"
"ds_write_b128 v50, v[60:63] offset:2048\n"
"ds_write_b128 v50, v[64:67] offset:3072\n"
"ds_write_b128 v50, v[68:71] offset:4096\n"
"ds_write_b32 v4, v4 offset:720\n"
"s_mov_b32 s17, 0x100000\n"
".Ltp_496:\n"
"s_sleep 2\n"
"ds_read_b32 v3, v5 offset:3904\n"
"s_sub_u32 s17, s17, 1\n"
"s_cbranch_scc1 .Lou_497\n"
"s_waitcnt lgkmcnt(0)\n"
"v_cmp_eq_u32 vcc, -1, v3\n"
"s_cbranch_vccnz .Ltp_496\n"
".Lou_497:\n"
"s_waitcnt lgkmcnt(0)\n"
"s_mul_i32 s79, s3, 0x1400\n"
"s_add_u32 m0, s79, 0xf000\n"
"s_add_u32 s49, s50, 0x5000\n"
"buffer_load_dwordx4 v2, s[4:7], s49 offen offset:0 sc1 lds\n"
"buffer_load_dwordx4 v2, s[4:7], s49 offen offset:1024 sc1 lds\n"
"buffer_load_dwordx4 v2, s[4:7], s49 offen offset:2048 sc1 lds\n"
"buffer_load_dwordx4 v2, s[4:7], s49 offen offset:3072 sc1 lds\n"
"s_add_u32 m0, m0, 1024\n"
"s_add_u32 s49, s49, 1024\n"
"buffer_load_dwordx4 v2, s[4:7], s49 offen offset:3072 sc1 lds\n"
"s_setprio 2\n"
"v_add_u32 v14, v39, v14\n"
"v_add_u32 v15, v39, v15\n"
"s_mul_i32 s79, s28, 0xa00\n"
"s_add_u32 s79, s30, s79\n"
"s_and_b32 s78, s3, 1\n"
"s_mul_i32 s78, s78, 0x600\n"
"s_add_u32 s79, s79, s78\n"
"s_add_u32 s36, s79, 0xc28000\n"
"s_mul_i32 s79, s3, 0x1400\n"
"s_add_u32 s79, s30, s79\n"
"s_add_u32 s32, s79, 0xea8000\n"
"s_add_u32 s33, s32, 0x1000\n"
"s_lshl_b32 s79, s29, 1\n"
"s_lshr_b32 s78, s3, 1\n"
"s_xor_b32 s78, s78, 1\n"
"s_add_u32 s79, s79, s78\n"
"s_mul_i32 s79, s79, 0x5000\n"
"s_mul_i32 s78, s3, 0x1400\n"
"s_add_u32 s79, s79, s78\n"
"s_add_u32 s34, s79, 0xea8000\n"
"s_add_u32 s35, s34, 0x1000\n"
"s_lshl_b32 s79, s30, 1\n"
"s_mul_i32 s78, s28, 0x1400\n"
"s_add_u32 s79, s79, s78\n"
"s_and_b32 s78, s3, 1\n"
"s_mul_i32 s78, s78, 0xc00\n"
"s_add_u32 s79, s79, s78\n"
"s_add_u32 s37, s79, 0x1128000\n"
"s_lshl_b32 s79, s29, 1\n"
"s_lshr_b32 s78, s3, 1\n"
"s_add_u32 s79, s79, s78\n"
"s_lshl_b32 s79, s79, 11\n"
"s_lshl_b32 s78, s28, 8\n"
"s_add_u32 s79, s79, s78\n"
"s_and_b32 s78, s3, 1\n"
"s_lshl_b32 s78, s78, 7\n"
"s_add_u32 s79, s79, s78\n"
"s_add_u32 s38, s79, 0x1628000\n"
"s_mov_b32 s17, 0x100000\n"
".Ltp_498:\n"
"ds_read_b32 v3, v5 offset:256\n"
"s_sub_u32 s17, s17, 1\n"
"s_cbranch_scc1 .Lou_499\n"
"s_waitcnt lgkmcnt(0)\n"
"v_cmp_eq_u32 vcc, 0, v3\n"
"s_cbranch_vccnz .Ltp_498\n"
".Lou_499:\n"
"s_waitcnt lgkmcnt(0)\n"
"s_cmp_lt_i32 s18, 0\n"
"s_cbranch_scc1 .Lcw_500\n"
"s_mov_b32 s17, 0x100000\n"
".Ltp_501:\n"
"ds_read_b32 v3, v5 offset:704\n"
"s_sub_u32 s17, s17, 1\n"
"s_cbranch_scc1 .Lou_502\n"
"s_waitcnt lgkmcnt(0)\n"
"v_cmp_eq_u32 vcc, 0, v3\n"
"s_cbranch_vccnz .Ltp_501\n"
".Lou_502:\n"
"s_waitcnt lgkmcnt(0)\n"
".Lcw_500:\n"
"buffer_load_dwordx2 v[40:41], v14, s[4:7], s36 offen offset:0 sc1\n"
"buffer_load_dwordx2 v[42:43], v14, s[4:7], s36 offen offset:512 sc1\n"
"buffer_load_dwordx2 v[44:45], v15, s[4:7], s36 offen offset:1024 sc1\n"
"s_waitcnt vmcnt(3)\n"
"ds_write_b32 v4, v4 offset:320\n"
"s_mov_b32 s17, 0x100000\n"
".Ltp_506:\n"
"s_sleep 1\n"
"ds_read_b32 v3, v5 offset:304\n"
"s_sub_u32 s17, s17, 1\n"
"s_cbranch_scc1 .Lou_507\n"
"s_waitcnt lgkmcnt(0)\n"
"v_cmp_eq_u32 vcc, 0, v3\n"
"s_cbranch_vccnz .Ltp_506\n"
".Lou_507:\n"
"s_waitcnt lgkmcnt(0)\n"
"s_mul_i32 m0, s3, 0x1400\n"
"s_add_u32 s49, s50, 0xa000\n"
"buffer_load_dwordx4 v2, s[4:7], s49 offen offset:0 sc1 lds\n"
"buffer_load_dwordx4 v2, s[4:7], s49 offen offset:1024 sc1 lds\n"
"buffer_load_dwordx4 v2, s[4:7], s49 offen offset:2048 sc1 lds\n"
"buffer_load_dwordx4 v2, s[4:7], s49 offen offset:3072 sc1 lds\n"
"s_add_u32 m0, m0, 1024\n"
"s_add_u32 s49, s49, 1024\n"
"buffer_load_dwordx4 v2, s[4:7], s49 offen offset:3072 sc1 lds\n"
"s_waitcnt vmcnt(5)\n"
"s_mov_b32 s17, 32768\n"
"s_branch .Lmd_505\n"
".Ltp_503:\n"
"buffer_load_dwordx2 v[40:41], v14, s[4:7], s36 offen offset:0 sc1\n"
"buffer_load_dwordx2 v[42:43], v14, s[4:7], s36 offen offset:512 sc1\n"
"buffer_load_dwordx2 v[44:45], v15, s[4:7], s36 offen offset:1024 sc1\n"
"s_waitcnt vmcnt(0)\n"
".Lmd_505:\n"
"s_sub_u32 s17, s17, 1\n"
"s_cbranch_scc1 .Lou_504\n"
"v_max3_u32 v38, v40, v41, v42\n"
"v_max3_u32 v38, v43, v44, v38\n"
"v_max_u32 v38, v45, v38\n"
"v_cmp_eq_u32 vcc, -1, v38\n"
"s_cbranch_vccnz .Ltp_503\n"
".Lou_504:\n"
"v_lshlrev_b32 v48, 16, v40\n"
"v_and_b32 v49, 0xffff0000, v40\n"
"v_lshlrev_b32 v50, 16, v41\n"
"v_and_b32 v51, 0xffff0000, v41\n"
"v_lshlrev_b32 v52, 16, v42\n"
"v_and_b32 v53, 0xffff0000, v42\n"
"v_lshlrev_b32 v54, 16, v43\n"
"v_and_b32 v55, 0xffff0000, v43\n"
"v_lshlrev_b32 v56, 16, v44\n"
"v_and_b32 v57, 0xffff0000, v44\n"
"v_lshlrev_b32 v58, 16, v45\n"
"v_and_b32 v59, 0xffff0000, v45\n"
"v_mov_b32 v46, 0\n"
"v_mov_b32 v47, 0\n"
"s_mov_b32 s17, 0x100000\n"
".Ltp_508:\n"
"s_sleep 1\n"
"ds_read_b32 v3, v5 offset:368\n"
"s_sub_u32 s17, s17, 1\n"
"s_cbranch_scc1 .Lou_509\n"
"s_waitcnt lgkmcnt(0)\n"
"v_cmp_eq_u32 vcc, 0, v3\n"
"s_cbranch_vccnz .Ltp_508\n"
".Lou_509:\n"
"s_waitcnt lgkmcnt(0)\n"
"s_mul_i32 s79, s3, 0x1400\n"
"s_add_u32 m0, s79, 0x5000\n"
"s_add_u32 s49, s50, 0xf000\n"
"buffer_load_dwordx4 v2, s[4:7], s49 offen offset:0 sc1 lds\n"
"buffer_load_dwordx4 v2, s[4:7], s49 offen offset:1024 sc1 lds\n"
"buffer_load_dwordx4 v2, s[4:7], s49 offen offset:2048 sc1 lds\n"
"buffer_load_dwordx4 v2, s[4:7], s49 offen offset:3072 sc1 lds\n"
"s_add_u32 m0, m0, 1024\n"
"s_add_u32 s49, s49, 1024\n"
"buffer_load_dwordx4 v2, s[4:7], s49 offen offset:3072 sc1 lds\n"
"s_waitcnt vmcnt(5)\n"
"ds_write_b32 v4, v4 offset:400\n"
"s_waitcnt vmcnt(0)\n"
"ds_write_b32 v4, v4 offset:416\n"
"buffer_load_dwordx4 v[108:111], v2, s[4:7], s34 offen offset:0 sc1\n"
"s_nop 9\n"
"buffer_load_dwordx4 v[112:115], v2, s[4:7], s34 offen offset:1024 sc1\n"
"s_nop 9\n"
"buffer_load_dwordx4 v[116:119], v2, s[4:7], s34 offen offset:2048 sc1\n"
"s_nop 9\n"
"buffer_load_dwordx4 v[120:123], v2, s[4:7], s34 offen offset:3072 sc1\n"
"s_nop 9\n"
"buffer_load_dwordx4 v[124:127], v2, s[4:7], s35 offen offset:0 sc1\n"
"s_nop 9\n"
"buffer_load_dwordx4 v[88:91], v2, s[4:7], s32 offen offset:0 sc1\n"
"s_nop 9\n"
"buffer_load_dwordx4 v[92:95], v2, s[4:7], s32 offen offset:1024 sc1\n"
"s_nop 9\n"
"buffer_load_dwordx4 v[96:99], v2, s[4:7], s32 offen offset:2048 sc1\n"
"s_nop 9\n"
"buffer_load_dwordx4 v[100:103], v2, s[4:7], s32 offen offset:3072 sc1\n"
"s_nop 9\n"
"buffer_load_dwordx4 v[104:107], v2, s[4:7], s33 offen offset:0 sc1\n"
"s_waitcnt vmcnt(9)\n"
"v_max_u32 v38, v108, v109\n"
"v_max3_u32 v38, v110, v111, v38\n"
"v_cmp_eq_u32 vcc, -1, v38\n"
"s_cbranch_vccnz .Lsl_510\n"
".Lbk_511:\n"
"v_mfma_f32_16x16x32_bf16 v[68:71], v[156:159], v[108:111], 0\n"
"v_mfma_f32_16x16x32_bf16 v[72:75], v[160:163], v[108:111], 0\n"
"v_mfma_f32_16x16x32_bf16 v[76:79], v[164:167], v[108:111], 0\n"
"s_waitcnt vmcnt(8)\n"
"v_max_u32 v38, v112, v113\n"
"v_max3_u32 v38, v114, v115, v38\n"
"v_mfma_f32_16x16x32_bf16 v[80:83], v[168:171], v[108:111], 0\n"
"v_cmp_eq_u32 vcc, -1, v38\n"
"v_mfma_f32_16x16x32_bf16 v[84:87], v[172:175], v[108:111], 0\n"
"s_cbranch_vccnz .Lsl_512\n"
".Lbk_513:\n"
"v_mfma_f32_16x16x32_bf16 v[68:71], v[176:179], v[112:115], v[68:71]\n"
"v_mfma_f32_16x16x32_bf16 v[72:75], v[180:183], v[112:115], v[72:75]\n"
"v_mfma_f32_16x16x32_bf16 v[76:79], v[184:187], v[112:115], v[76:79]\n"
"s_waitcnt vmcnt(7)\n"
"v_max_u32 v38, v116, v117\n"
"v_max3_u32 v38, v118, v119, v38\n"
"v_mfma_f32_16x16x32_bf16 v[80:83], v[188:191], v[112:115], v[80:83]\n"
"v_cmp_eq_u32 vcc, -1, v38\n"
"v_mfma_f32_16x16x32_bf16 v[84:87], v[192:195], v[112:115], v[84:87]\n"
"s_cbranch_vccnz .Lsl_514\n"
".Lbk_515:\n"
"v_mfma_f32_16x16x32_bf16 v[68:71], v[196:199], v[116:119], v[68:71]\n"
"v_mfma_f32_16x16x32_bf16 v[72:75], v[200:203], v[116:119], v[72:75]\n"
"v_mfma_f32_16x16x32_bf16 v[76:79], v[204:207], v[116:119], v[76:79]\n"
"s_waitcnt vmcnt(6)\n"
"v_max_u32 v38, v120, v121\n"
"v_max3_u32 v38, v122, v123, v38\n"
"v_mfma_f32_16x16x32_bf16 v[80:83], v[208:211], v[116:119], v[80:83]\n"
"v_cmp_eq_u32 vcc, -1, v38\n"
"v_mfma_f32_16x16x32_bf16 v[84:87], v[212:215], v[116:119], v[84:87]\n"
"s_cbranch_vccnz .Lsl_516\n"
".Lbk_517:\n"
"v_mfma_f32_16x16x32_bf16 v[68:71], v[216:219], v[120:123], v[68:71]\n"
"v_mfma_f32_16x16x32_bf16 v[72:75], v[220:223], v[120:123], v[72:75]\n"
"v_mfma_f32_16x16x32_bf16 v[76:79], v[224:227], v[120:123], v[76:79]\n"
"s_waitcnt vmcnt(5)\n"
"v_max_u32 v38, v124, v125\n"
"v_max3_u32 v38, v126, v127, v38\n"
"v_mfma_f32_16x16x32_bf16 v[80:83], v[228:231], v[120:123], v[80:83]\n"
"v_cmp_eq_u32 vcc, -1, v38\n"
"v_mfma_f32_16x16x32_bf16 v[84:87], v[232:235], v[120:123], v[84:87]\n"
"s_cbranch_vccnz .Lsl_518\n"
".Lbk_519:\n"
"v_mfma_f32_16x16x32_bf16 v[68:71], v[236:239], v[124:127], v[68:71]\n"
"v_mfma_f32_16x16x32_bf16 v[72:75], v[240:243], v[124:127], v[72:75]\n"
"v_mfma_f32_16x16x32_bf16 v[76:79], v[244:247], v[124:127], v[76:79]\n"
"s_waitcnt vmcnt(4)\n"
"v_max_u32 v38, v88, v89\n"
"v_max3_u32 v38, v90, v91, v38\n"
"v_mfma_f32_16x16x32_bf16 v[80:83], v[248:251], v[124:127], v[80:83]\n"
"v_cmp_eq_u32 vcc, -1, v38\n"
"v_mfma_f32_16x16x32_bf16 v[84:87], v[252:255], v[124:127], v[84:87]\n"
"ds_write_b128 v8, v[68:71] offset:0\n"
"s_cbranch_vccnz .Lsl_520\n"
".Lbk_521:\n"
"v_mfma_f32_16x16x32_bf16 v[56:59], v[164:167], v[88:91], v[56:59]\n"
"s_waitcnt vmcnt(3)\n"
"v_max_u32 v38, v92, v93\n"
"v_max3_u32 v38, v94, v95, v38\n"
"ds_write_b128 v8, v[72:75] offset:1024\n"
"v_mfma_f32_16x16x32_bf16 v[60:63], v[168:171], v[88:91], 0\n"
"v_cmp_eq_u32 vcc, -1, v38\n"
"ds_write_b128 v10, v[76:79] offset:0\n"
"v_mfma_f32_16x16x32_bf16 v[64:67], v[172:175], v[88:91], 0\n"
"v_mov_b32 v117, s16\n"
"s_cbranch_vccnz .Lsl_522\n"
".Lbk_523:\n"
"v_mfma_f32_16x16x32_bf16 v[56:59], v[184:187], v[92:95], v[56:59]\n"
"s_waitcnt vmcnt(2)\n"
"v_max_u32 v38, v96, v97\n"
"v_max3_u32 v38, v98, v99, v38\n"
"v_mov_b32 v119, s16\n"
"v_mfma_f32_16x16x32_bf16 v[60:63], v[188:191], v[92:95], v[60:63]\n"
"v_cmp_eq_u32 vcc, -1, v38\n"
"ds_write_b128 v9, v[80:83] offset:0\n"
"v_mfma_f32_16x16x32_bf16 v[64:67], v[192:195], v[92:95], v[64:67]\n"
"ds_write_b128 v9, v[84:87] offset:1024\n"
"s_cbranch_vccnz .Lsl_524\n"
".Lbk_525:\n"
"v_mfma_f32_16x16x32_bf16 v[56:59], v[204:207], v[96:99], v[56:59]\n"
"s_waitcnt vmcnt(1)\n"
"v_max_u32 v38, v100, v101\n"
"v_max3_u32 v38, v102, v103, v38\n"
"ds_write_b32 v4, v4 offset:736\n"
"v_mfma_f32_16x16x32_bf16 v[60:63], v[208:211], v[96:99], v[60:63]\n"
"v_cmp_eq_u32 vcc, -1, v38\n"
"v_mov_b32 v121, s16\n"
"v_mov_b32 v123, s16\n"
"v_mfma_f32_16x16x32_bf16 v[64:67], v[212:215], v[96:99], v[64:67]\n"
"v_mov_b32 v125, s16\n"
"s_cbranch_vccnz .Lsl_526\n"
".Lbk_527:\n"
"v_mfma_f32_16x16x32_bf16 v[56:59], v[224:227], v[100:103], v[56:59]\n"
"s_waitcnt vmcnt(0)\n"
"v_max_u32 v38, v104, v105\n"
"v_max3_u32 v38, v106, v107, v38\n"
"v_mov_b32 v127, s16\n"
"v_mfma_f32_16x16x32_bf16 v[60:63], v[228:231], v[100:103], v[60:63]\n"
"v_cmp_eq_u32 vcc, -1, v38\n"
"ds_read_b32 v3, v5 offset:736\n"
"v_mfma_f32_16x16x32_bf16 v[64:67], v[232:235], v[100:103], v[64:67]\n"
"s_cbranch_vccnz .Lsl_528\n"
".Lbk_529:\n"
"v_mfma_f32_16x16x32_bf16 v[56:59], v[244:247], v[104:107], v[56:59]\n"
"ds_read_b128 v[76:79], v12 offset:2048\n"
"ds_read_b128 v[108:111], v13 offset:2048\n"
"v_mfma_f32_16x16x32_bf16 v[60:63], v[248:251], v[104:107], v[60:63]\n"
"ds_read_b128 v[68:71], v12 offset:0\n"
"ds_read_b128 v[80:83], v13 offset:0\n"
"v_mfma_f32_16x16x32_bf16 v[64:67], v[252:255], v[104:107], v[64:67]\n"
"ds_read_b128 v[72:75], v12 offset:1024\n"
"ds_read_b128 v[84:87], v13 offset:1024\n"
"v_mfma_f32_16x16x32_bf16 v[48:51], v[156:159], v[88:91], v[48:51]\n"
"ds_write_b128 v7, v[56:59] offset:0\n"
"v_mfma_f32_16x16x32_bf16 v[52:55], v[160:163], v[88:91], v[52:55]\n"
"ds_write_b128 v6, v[60:63] offset:0\n"
"v_mfma_f32_16x16x32_bf16 v[48:51], v[176:179], v[92:95], v[48:51]\n"
"ds_write_b128 v6, v[64:67] offset:1024\n"
"v_mfma_f32_16x16x32_bf16 v[52:55], v[180:183], v[92:95], v[52:55]\n"
"ds_write_b32 v4, v4 offset:752\n"
"v_mfma_f32_16x16x32_bf16 v[48:51], v[196:199], v[96:99], v[48:51]\n"
"s_mov_b32 s17, 0x100000\n"
"s_waitcnt lgkmcnt(10)\n"
"v_cmp_eq_u32 vcc, 0, v3\n"
"s_cbranch_vccz .Lok_531\n"
".Ltp_530:\n"
"ds_read_b32 v3, v5 offset:736\n"
"ds_read_b128 v[76:79], v12 offset:2048\n"
"ds_read_b128 v[108:111], v13 offset:2048\n"
"ds_read_b128 v[68:71], v12 offset:0\n"
"ds_read_b128 v[80:83], v13 offset:0\n"
"ds_read_b128 v[72:75], v12 offset:1024\n"
"ds_read_b128 v[84:87], v13 offset:1024\n"
"s_sub_u32 s17, s17, 1\n"
"s_cbranch_scc1 .Lok_531\n"
"s_waitcnt lgkmcnt(6)\n"
"v_cmp_eq_u32 vcc, 0, v3\n"
"s_cbranch_vccnz .Ltp_530\n"
"s_waitcnt lgkmcnt(0)\n"
".Lok_531:\n"
"v_mfma_f32_16x16x32_bf16 v[52:55], v[200:203], v[96:99], v[52:55]\n"
"s_waitcnt lgkmcnt(8)\n"
"v_add_f32 v76, v76, v108\n"
"v_add_f32 v77, v77, v109\n"
"v_add_f32 v78, v78, v110\n"
"v_mfma_f32_16x16x32_bf16 v[48:51], v[216:219], v[100:103], v[48:51]\n"
"v_add_f32 v79, v79, v111\n"
"s_waitcnt lgkmcnt(6)\n"
"v_add_f32 v68, v68, v80\n"
"v_add_f32 v69, v69, v81\n"
"v_mfma_f32_16x16x32_bf16 v[52:55], v[220:223], v[100:103], v[52:55]\n"
"v_add_f32 v70, v70, v82\n"
"v_add_f32 v71, v71, v83\n"
"s_waitcnt lgkmcnt(4)\n"
"v_add_f32 v72, v72, v84\n"
"v_mfma_f32_16x16x32_bf16 v[48:51], v[236:239], v[104:107], v[48:51]\n"
"v_add_f32 v73, v73, v85\n"
"v_add_f32 v74, v74, v86\n"
"v_add_f32 v75, v75, v87\n"
"v_mfma_f32_16x16x32_bf16 v[52:55], v[240:243], v[104:107], v[52:55]\n"
"ds_read_b32 v3, v5 offset:752\n"
"ds_read_b128 v[112:115], v11 offset:2048\n"
"ds_read_b128 v[60:63], v11 offset:0\n"
"ds_read_b128 v[64:67], v11 offset:1024\n"
"v_add_f32 v56, v56, v76\n"
"v_add_f32 v57, v57, v77\n"
"v_add_f32 v58, v58, v78\n"
"v_add_f32 v59, v59, v79\n"
"v_add_f32 v48, v48, v68\n"
"v_add_f32 v49, v49, v69\n"
"v_add_f32 v50, v50, v70\n"
"v_add_f32 v51, v51, v71\n"
"v_add_f32 v52, v52, v72\n"
"v_add_f32 v53, v53, v73\n"
"v_add_f32 v54, v54, v74\n"
"v_add_f32 v55, v55, v75\n"
"s_mov_b32 s17, 0x100000\n"
"s_waitcnt lgkmcnt(3)\n"
"v_cmp_eq_u32 vcc, 0, v3\n"
"s_cbranch_vccz .Lok_533\n"
".Ltp_532:\n"
"ds_read_b32 v3, v5 offset:752\n"
"ds_read_b128 v[112:115], v11 offset:2048\n"
"ds_read_b128 v[60:63], v11 offset:0\n"
"ds_read_b128 v[64:67], v11 offset:1024\n"
"s_sub_u32 s17, s17, 1\n"
"s_cbranch_scc1 .Lok_533\n"
"s_waitcnt lgkmcnt(3)\n"
"v_cmp_eq_u32 vcc, 0, v3\n"
"s_cbranch_vccnz .Ltp_532\n"
"s_waitcnt lgkmcnt(0)\n"
".Lok_533:\n"
"s_waitcnt lgkmcnt(2)\n"
"v_add_f32 v56, v56, v112\n"
"v_add_f32 v57, v57, v113\n"
"v_add_f32 v58, v58, v114\n"
"v_add_f32 v59, v59, v115\n"
"v_cvt_pk_bf16_f32 v44, v56, v57\n"
"v_cvt_pk_bf16_f32 v45, v58, v59\n"
"v_and_b32 v44, v19, v44\n"
"v_and_b32 v45, v19, v45\n"
"s_waitcnt lgkmcnt(1)\n"
"v_add_f32 v48, v48, v60\n"
"v_add_f32 v49, v49, v61\n"
"v_add_f32 v50, v50, v62\n"
"v_add_f32 v51, v51, v63\n"
"v_cvt_pk_bf16_f32 v40, v48, v49\n"
"v_cvt_pk_bf16_f32 v41, v50, v51\n"
"v_mfma_f32_16x16x32_bf16 v[88:91], v[44:47], v[44:47], 0\n"
"v_mfma_f32_16x16x32_bf16 v[92:95], v[20:23], v[44:47], 0\n"
"s_waitcnt lgkmcnt(0)\n"
"v_add_f32 v52, v52, v64\n"
"v_add_f32 v53, v53, v65\n"
"v_add_f32 v54, v54, v66\n"
"v_add_f32 v55, v55, v67\n"
"v_cvt_pk_bf16_f32 v42, v52, v53\n"
"v_cvt_pk_bf16_f32 v43, v54, v55\n"
"v_cvt_pk_bf16_f32 v116, v48, v49\n"
"v_cvt_pk_bf16_f32 v118, v50, v51\n"
"v_mfma_f32_16x16x32_bf16 v[88:91], v[40:43], v[40:43], v[88:91]\n"
"v_mfma_f32_16x16x32_bf16 v[92:95], v[20:23], v[40:43], v[92:95]\n"
"v_cvt_pk_bf16_f32 v120, v52, v53\n"
"v_cvt_pk_bf16_f32 v122, v54, v55\n"
"v_cvt_pk_bf16_f32 v124, v56, v57\n"
"v_cvt_pk_bf16_f32 v126, v58, v59\n"
"s_nop 0\n"
"v_cndmask_b32 v101, v88, v90, s[44:45]\n"
"v_cndmask_b32 v88, v89, v91, s[44:45]\n"
"s_nop 0\n"
"v_min_u32 v100, -2, v92\n"
"v_min_u32 v88, -2, v88\n"
"v_min_u32 v101, -2, v101\n"
"v_mov_b32_dpp v102, v100 quad_perm:[1,0,3,2] row_mask:0xf bank_mask:0xf\n"
"v_mov_b32_dpp v103, v88 quad_perm:[1,0,3,2] row_mask:0xf bank_mask:0xf\n"
"buffer_store_dwordx4 v[100:103], v16, s[4:7], s38 offen sc1\n"
"buffer_store_dwordx4 v[116:119], v17, s[4:7], s37 offen offset:0 sc1\n"
"buffer_store_dwordx4 v[120:123], v17, s[4:7], s37 offen offset:1024 sc1\n"
"buffer_store_dwordx4 v[124:127], v18, s[4:7], s37 offen offset:2048 sc1\n"
"ds_write_b32 v4, v4 offset:496\n"
"s_xor_b32 s79, s3, 1\n"
"s_lshl_b32 s79, s79, 8\n"
"s_add_u32 s32, s19, s79\n"
"s_cmp_ge_u32 s32, 64\n"
"s_cbranch_scc1 .Lnr_534\n"
"s_lshl_b32 s79, s19, 11\n"
"s_add_u32 s32, s79, 0x1628000\n"
"s_lshl_b32 s79, s19, 7\n"
"s_add_u32 s33, s79, 0x1668000\n"
"v_lshrrev_b32 v99, 3, v0\n"
"v_and_b32 v98, 7, v0\n"
"v_lshlrev_b32 v98, 4, v98\n"
"v_lshl_add_u32 v40, v99, 14, v98\n"
"v_mov_b32 v41, 0x3727c5ac\n"
"s_mov_b32 s89, 8\n"
".Ldl_535:\n"
"s_sub_u32 s89, s89, 1\n"
"s_cbranch_scc1 .Lde_536\n"
"s_sleep 1\n"
"s_branch .Ldl_535\n"
".Lde_536:\n"
"buffer_load_dwordx4 v[48:51], v2, s[4:7], s32 offen offset:0 sc1\n"
"buffer_load_dwordx4 v[52:55], v2, s[4:7], s32 offen offset:1024 sc1\n"
"s_mov_b32 s17, 32768\n"
".Ltp_537:\n"
"s_waitcnt vmcnt(0)\n"
"s_sub_u32 s17, s17, 1\n"
"s_cbranch_scc1 .Lou_538\n"
"v_max_u32 v38, v48, v49\n"
"v_max3_u32 v38, v50, v51, v38\n"
"v_max3_u32 v38, v52, v53, v38\n"
"v_max3_u32 v38, v54, v55, v38\n"
"v_cmp_eq_u32 vcc, -1, v38\n"
"s_cbranch_vccz .Lou_538\n"
"v_max_u32 v38, v50, v51\n"
"v_max3_u32 v38, v48, v49, v38\n"
"v_cmp_eq_u32 vcc, -1, v38\n"
"s_cbranch_vccz .Lsk_539\n"
"s_mov_b64 exec, vcc\n"
"buffer_load_dwordx4 v[48:51], v2, s[4:7], s32 offen offset:0 sc1\n"
"s_mov_b64 exec, -1\n"
".Lsk_539:\n"
"v_max_u32 v38, v54, v55\n"
"v_max3_u32 v38, v52, v53, v38\n"
"v_cmp_eq_u32 vcc, -1, v38\n"
"s_cbranch_vccz .Lsk_540\n"
"s_mov_b64 exec, vcc\n"
"buffer_load_dwordx4 v[52:55], v2, s[4:7], s32 offen offset:1024 sc1\n"
"s_mov_b64 exec, -1\n"
".Lsk_540:\n"
"s_branch .Ltp_537\n"
".Lou_538:\n"
"s_waitcnt vmcnt(0)\n"
"v_add_f32 v48, v48, v52\n"
"v_add_f32 v49, v49, v53\n"
"v_add_f32 v50, v50, v54\n"
"v_add_f32 v51, v51, v55\n"
"v_add_f32_dpp v52, v48, v48 row_ror:8 row_mask:0xf bank_mask:0xf\n"
"v_add_f32_dpp v53, v49, v49 row_ror:8 row_mask:0xf bank_mask:0xf\n"
"v_add_f32_dpp v54, v50, v50 row_ror:8 row_mask:0xf bank_mask:0xf\n"
"v_add_f32_dpp v55, v51, v51 row_ror:8 row_mask:0xf bank_mask:0xf\n"
"v_mov_b32 v48, v52\n"
"v_mov_b32 v49, v53\n"
"v_mov_b32 v50, v54\n"
"v_mov_b32 v51, v55\n"
"v_permlane16_swap_b32 v48, v52\n"
"v_permlane16_swap_b32 v49, v53\n"
"v_permlane16_swap_b32 v50, v54\n"
"v_permlane16_swap_b32 v51, v55\n"
"v_add_f32 v52, v48, v52\n"
"v_add_f32 v53, v49, v53\n"
"v_add_f32 v54, v50, v54\n"
"v_add_f32 v55, v51, v55\n"
"v_mov_b32 v48, v52\n"
"v_mov_b32 v49, v53\n"
"v_mov_b32 v50, v54\n"
"v_mov_b32 v51, v55\n"
"v_permlane32_swap_b32 v48, v52\n"
"v_permlane32_swap_b32 v49, v53\n"
"v_permlane32_swap_b32 v50, v54\n"
"v_permlane32_swap_b32 v51, v55\n"
"v_add_f32 v52, v48, v52\n"
"v_add_f32 v53, v49, v53\n"
"v_add_f32 v54, v50, v54\n"
"v_add_f32 v55, v51, v55\n"
"v_mul_f32 v48, 0x3acccccd, v52\n"
"v_mul_f32 v50, 0x3acccccd, v54\n"
"v_fma_f32 v53, -v52, v48, v53\n"
"v_fma_f32 v55, -v54, v50, v55\n"
"v_fmamk_f32 v53, v53, 0x3acccccd, v41\n"
"v_fmamk_f32 v55, v55, 0x3acccccd, v41\n"
"v_max_f32 v53, v53, v41\n"
"v_max_f32 v55, v55, v41\n"
"v_rsq_f32 v49, v53\n"
"v_rsq_f32 v51, v55\n"
"v_mul_f32 v48, -v48, v49\n"
"v_mul_f32 v50, -v50, v51\n"
"v_min_u32 v49, -2, v49\n"
"v_min_u32 v51, -2, v51\n"
"v_min_u32 v48, -2, v48\n"
"v_min_u32 v50, -2, v50\n"
"buffer_store_dwordx4 v[48:51], v40, s[4:7], s33 offen sc1\n"
"s_nop 3\n"
".Lnr_534:\n"
"s_mul_i32 s79, s28, 0xa00\n"
"s_add_u32 s79, s30, s79\n"
"s_and_b32 s78, s3, 1\n"
"s_mul_i32 s78, s78, 0x600\n"
"s_add_u32 s79, s79, s78\n"
"s_add_u32 s36, s79, 0xd68000\n"
"s_mul_i32 s79, s3, 0x1400\n"
"s_add_u32 s79, s30, s79\n"
"s_add_u32 s32, s79, 0xfe8000\n"
"s_add_u32 s33, s32, 0x1000\n"
"s_lshl_b32 s79, s29, 1\n"
"s_lshr_b32 s78, s3, 1\n"
"s_xor_b32 s78, s78, 1\n"
"s_add_u32 s79, s79, s78\n"
"s_mul_i32 s79, s79, 0x5000\n"
"s_mul_i32 s78, s3, 0x1400\n"
"s_add_u32 s79, s79, s78\n"
"s_add_u32 s34, s79, 0xfe8000\n"
"s_add_u32 s35, s34, 0x1000\n"
"s_lshl_b32 s79, s30, 1\n"
"s_mul_i32 s78, s28, 0x1400\n"
"s_add_u32 s79, s79, s78\n"
"s_and_b32 s78, s3, 1\n"
"s_mul_i32 s78, s78, 0xc00\n"
"s_add_u32 s79, s79, s78\n"
"s_add_u32 s37, s79, 0x13a8000\n"
"s_lshl_b32 s79, s29, 1\n"
"s_lshr_b32 s78, s3, 1\n"
"s_add_u32 s79, s79, s78\n"
"s_lshl_b32 s79, s79, 11\n"
"s_lshl_b32 s78, s28, 8\n"
"s_add_u32 s79, s79, s78\n"
"s_and_b32 s78, s3, 1\n"
"s_lshl_b32 s78, s78, 7\n"
"s_add_u32 s79, s79, s78\n"
"s_add_u32 s38, s79, 0x1648000\n"
"s_mov_b32 s17, 0x100000\n"
".Ltp_541:\n"
"ds_read_b32 v3, v5 offset:496\n"
"s_sub_u32 s17, s17, 1\n"
"s_cbranch_scc1 .Lou_542\n"
"s_waitcnt lgkmcnt(0)\n"
"v_cmp_eq_u32 vcc, 0, v3\n"
"s_cbranch_vccnz .Ltp_541\n"
".Lou_542:\n"
"s_waitcnt lgkmcnt(0)\n"
"s_mov_b32 s17, 0x100000\n"
".Ltp_546:\n"
"s_sleep 2\n"
"ds_read_b32 v3, v5 offset:4800\n"
"s_sub_u32 s17, s17, 1\n"
"s_cbranch_scc1 .Lou_547\n"
"s_waitcnt lgkmcnt(0)\n"
"v_cmp_eq_u32 vcc, -1, v3\n"
"s_cbranch_vccnz .Ltp_546\n"
".Lou_547:\n"
"s_waitcnt lgkmcnt(0)\n"
"s_mov_b32 s17, 32768\n"
".Ltp_543:\n"
"buffer_load_dwordx2 v[40:41], v14, s[4:7], s36 offen offset:0 sc1\n"
"buffer_load_dwordx2 v[42:43], v14, s[4:7], s36 offen offset:512 sc1\n"
"buffer_load_dwordx2 v[44:45], v15, s[4:7], s36 offen offset:1024 sc1\n"
"s_waitcnt vmcnt(0)\n"
".Lmd_545:\n"
"s_sub_u32 s17, s17, 1\n"
"s_cbranch_scc1 .Lou_544\n"
"v_max3_u32 v38, v40, v41, v42\n"
"v_max3_u32 v38, v43, v44, v38\n"
"v_max_u32 v38, v45, v38\n"
"v_cmp_eq_u32 vcc, -1, v38\n"
"s_cbranch_vccnz .Ltp_543\n"
".Lou_544:\n"
"v_lshlrev_b32 v48, 16, v40\n"
"v_and_b32 v49, 0xffff0000, v40\n"
"v_lshlrev_b32 v50, 16, v41\n"
"v_and_b32 v51, 0xffff0000, v41\n"
"v_lshlrev_b32 v52, 16, v42\n"
"v_and_b32 v53, 0xffff0000, v42\n"
"v_lshlrev_b32 v54, 16, v43\n"
"v_and_b32 v55, 0xffff0000, v43\n"
"v_lshlrev_b32 v56, 16, v44\n"
"v_and_b32 v57, 0xffff0000, v44\n"
"v_lshlrev_b32 v58, 16, v45\n"
"v_and_b32 v59, 0xffff0000, v45\n"
"v_mov_b32 v46, 0\n"
"v_mov_b32 v47, 0\n"
"s_mov_b32 s17, 0x100000\n"
".Ltp_548:\n"
"s_sleep 1\n"
"ds_read_b32 v3, v5 offset:480\n"
"s_sub_u32 s17, s17, 1\n"
"s_cbranch_scc1 .Lou_549\n"
"s_waitcnt lgkmcnt(0)\n"
"v_cmp_eq_u32 vcc, 0, v3\n"
"s_cbranch_vccnz .Ltp_548\n"
".Lou_549:\n"
"s_waitcnt lgkmcnt(0)\n"
"s_cmp_eq_u32 s17, 0xfffff\n"
"s_cbranch_scc1 .Lsk_550\n"
"s_mov_b32 s89, 19\n"
".Ldl_552:\n"
"s_sub_u32 s89, s89, 1\n"
"s_cbranch_scc1 .Lde_553\n"
"s_sleep 1\n"
"s_branch .Ldl_552\n"
".Lde_553:\n"
"s_branch .Ljn_551\n"
".Lsk_550:\n"
"s_mov_b32 s89, 12\n"
".Ldl_554:\n"
"s_sub_u32 s89, s89, 1\n"
"s_cbranch_scc1 .Lde_555\n"
"s_sleep 1\n"
"s_branch .Ldl_554\n"
".Lde_555:\n"
".Ljn_551:\n"
"buffer_load_dwordx4 v[108:111], v2, s[4:7], s34 offen offset:0 sc1\n"
"s_nop 9\n"
"buffer_load_dwordx4 v[112:115], v2, s[4:7], s34 offen offset:1024 sc1\n"
"s_nop 9\n"
"buffer_load_dwordx4 v[116:119], v2, s[4:7], s34 offen offset:2048 sc1\n"
"s_nop 9\n"
"buffer_load_dwordx4 v[120:123], v2, s[4:7], s34 offen offset:3072 sc1\n"
"s_nop 9\n"
"buffer_load_dwordx4 v[124:127], v2, s[4:7], s35 offen offset:0 sc1\n"
"s_nop 9\n"
"buffer_load_dwordx4 v[88:91], v2, s[4:7], s32 offen offset:0 sc1\n"
"s_nop 9\n"
"buffer_load_dwordx4 v[92:95], v2, s[4:7], s32 offen offset:1024 sc1\n"
"s_nop 9\n"
"buffer_load_dwordx4 v[96:99], v2, s[4:7], s32 offen offset:2048 sc1\n"
"s_nop 9\n"
"buffer_load_dwordx4 v[100:103], v2, s[4:7], s32 offen offset:3072 sc1\n"
"s_nop 9\n"
"buffer_load_dwordx4 v[104:107], v2, s[4:7], s33 offen offset:0 sc1\n"
"s_waitcnt vmcnt(9)\n"
"v_max_u32 v38, v108, v109\n"
"v_max3_u32 v38, v110, v111, v38\n"
"v_cmp_eq_u32 vcc, -1, v38\n"
"s_cbranch_vccnz .Lsl_556\n"
".Lbk_557:\n"
"v_mfma_f32_16x16x32_bf16 v[68:71], v[156:159], v[108:111], 0\n"
"v_mfma_f32_16x16x32_bf16 v[72:75], v[160:163], v[108:111], 0\n"
"v_mfma_f32_16x16x32_bf16 v[76:79], v[164:167], v[108:111], 0\n"
"s_waitcnt vmcnt(8)\n"
"v_max_u32 v38, v112, v113\n"
"v_max3_u32 v38, v114, v115, v38\n"
"v_mfma_f32_16x16x32_bf16 v[80:83], v[168:171], v[108:111], 0\n"
"v_cmp_eq_u32 vcc, -1, v38\n"
"v_mfma_f32_16x16x32_bf16 v[84:87], v[172:175], v[108:111], 0\n"
"s_cbranch_vccnz .Lsl_558\n"
".Lbk_559:\n"
"v_mfma_f32_16x16x32_bf16 v[68:71], v[176:179], v[112:115], v[68:71]\n"
"v_mfma_f32_16x16x32_bf16 v[72:75], v[180:183], v[112:115], v[72:75]\n"
"v_mfma_f32_16x16x32_bf16 v[76:79], v[184:187], v[112:115], v[76:79]\n"
"s_waitcnt vmcnt(7)\n"
"v_max_u32 v38, v116, v117\n"
"v_max3_u32 v38, v118, v119, v38\n"
"v_mfma_f32_16x16x32_bf16 v[80:83], v[188:191], v[112:115], v[80:83]\n"
"v_cmp_eq_u32 vcc, -1, v38\n"
"v_mfma_f32_16x16x32_bf16 v[84:87], v[192:195], v[112:115], v[84:87]\n"
"s_cbranch_vccnz .Lsl_560\n"
".Lbk_561:\n"
"v_mfma_f32_16x16x32_bf16 v[68:71], v[196:199], v[116:119], v[68:71]\n"
"v_mfma_f32_16x16x32_bf16 v[72:75], v[200:203], v[116:119], v[72:75]\n"
"v_mfma_f32_16x16x32_bf16 v[76:79], v[204:207], v[116:119], v[76:79]\n"
"s_waitcnt vmcnt(6)\n"
"v_max_u32 v38, v120, v121\n"
"v_max3_u32 v38, v122, v123, v38\n"
"v_mfma_f32_16x16x32_bf16 v[80:83], v[208:211], v[116:119], v[80:83]\n"
"v_cmp_eq_u32 vcc, -1, v38\n"
"v_mfma_f32_16x16x32_bf16 v[84:87], v[212:215], v[116:119], v[84:87]\n"
"s_cbranch_vccnz .Lsl_562\n"
".Lbk_563:\n"
"v_mfma_f32_16x16x32_bf16 v[68:71], v[216:219], v[120:123], v[68:71]\n"
"v_mfma_f32_16x16x32_bf16 v[72:75], v[220:223], v[120:123], v[72:75]\n"
"v_mfma_f32_16x16x32_bf16 v[76:79], v[224:227], v[120:123], v[76:79]\n"
"s_waitcnt vmcnt(5)\n"
"v_max_u32 v38, v124, v125\n"
"v_max3_u32 v38, v126, v127, v38\n"
"v_mfma_f32_16x16x32_bf16 v[80:83], v[228:231], v[120:123], v[80:83]\n"
"v_cmp_eq_u32 vcc, -1, v38\n"
"v_mfma_f32_16x16x32_bf16 v[84:87], v[232:235], v[120:123], v[84:87]\n"
"s_cbranch_vccnz .Lsl_564\n"
".Lbk_565:\n"
"v_mfma_f32_16x16x32_bf16 v[68:71], v[236:239], v[124:127], v[68:71]\n"
"v_mfma_f32_16x16x32_bf16 v[72:75], v[240:243], v[124:127], v[72:75]\n"
"v_mfma_f32_16x16x32_bf16 v[76:79], v[244:247], v[124:127], v[76:79]\n"
"s_waitcnt vmcnt(4)\n"
"v_max_u32 v38, v88, v89\n"
"v_max3_u32 v38, v90, v91, v38\n"
"v_mfma_f32_16x16x32_bf16 v[80:83], v[248:251], v[124:127], v[80:83]\n"
"v_cmp_eq_u32 vcc, -1, v38\n"
"v_mfma_f32_16x16x32_bf16 v[84:87], v[252:255], v[124:127], v[84:87]\n"
"ds_write_b128 v8, v[68:71] offset:0\n"
"s_cbranch_vccnz .Lsl_566\n"
".Lbk_567:\n"
"v_mfma_f32_16x16x32_bf16 v[56:59], v[164:167], v[88:91], v[56:59]\n"
"s_waitcnt vmcnt(3)\n"
"v_max_u32 v38, v92, v93\n"
"v_max3_u32 v38, v94, v95, v38\n"
"ds_write_b128 v8, v[72:75] offset:1024\n"
"v_mfma_f32_16x16x32_bf16 v[60:63], v[168:171], v[88:91], 0\n"
"v_cmp_eq_u32 vcc, -1, v38\n"
"ds_write_b128 v10, v[76:79] offset:0\n"
"v_mfma_f32_16x16x32_bf16 v[64:67], v[172:175], v[88:91], 0\n"
"v_mov_b32 v117, s16\n"
"s_cbranch_vccnz .Lsl_568\n"
".Lbk_569:\n"
"v_mfma_f32_16x16x32_bf16 v[56:59], v[184:187], v[92:95], v[56:59]\n"
"s_waitcnt vmcnt(2)\n"
"v_max_u32 v38, v96, v97\n"
"v_max3_u32 v38, v98, v99, v38\n"
"v_mov_b32 v119, s16\n"
"v_mfma_f32_16x16x32_bf16 v[60:63], v[188:191], v[92:95], v[60:63]\n"
"v_cmp_eq_u32 vcc, -1, v38\n"
"ds_write_b128 v9, v[80:83] offset:0\n"
"v_mfma_f32_16x16x32_bf16 v[64:67], v[192:195], v[92:95], v[64:67]\n"
"ds_write_b128 v9, v[84:87] offset:1024\n"
"s_cbranch_vccnz .Lsl_570\n"
".Lbk_571:\n"
"v_mfma_f32_16x16x32_bf16 v[56:59], v[204:207], v[96:99], v[56:59]\n"
"s_waitcnt vmcnt(1)\n"
"v_max_u32 v38, v100, v101\n"
"v_max3_u32 v38, v102, v103, v38\n"
"ds_write_b32 v4, v4 offset:768\n"
"v_mfma_f32_16x16x32_bf16 v[60:63], v[208:211], v[96:99], v[60:63]\n"
"v_cmp_eq_u32 vcc, -1, v38\n"
"v_mov_b32 v121, s16\n"
"v_mov_b32 v123, s16\n"
"v_mfma_f32_16x16x32_bf16 v[64:67], v[212:215], v[96:99], v[64:67]\n"
"v_mov_b32 v125, s16\n"
"s_cbranch_vccnz .Lsl_572\n"
".Lbk_573:\n"
"v_mfma_f32_16x16x32_bf16 v[56:59], v[224:227], v[100:103], v[56:59]\n"
"s_waitcnt vmcnt(0)\n"
"v_max_u32 v38, v104, v105\n"
"v_max3_u32 v38, v106, v107, v38\n"
"v_mov_b32 v127, s16\n"
"v_mfma_f32_16x16x32_bf16 v[60:63], v[228:231], v[100:103], v[60:63]\n"
"v_cmp_eq_u32 vcc, -1, v38\n"
"ds_read_b32 v3, v5 offset:768\n"
"v_mfma_f32_16x16x32_bf16 v[64:67], v[232:235], v[100:103], v[64:67]\n"
"s_cbranch_vccnz .Lsl_574\n"
".Lbk_575:\n"
"v_mfma_f32_16x16x32_bf16 v[56:59], v[244:247], v[104:107], v[56:59]\n"
"ds_read_b128 v[76:79], v12 offset:2048\n"
"ds_read_b128 v[108:111], v13 offset:2048\n"
"v_mfma_f32_16x16x32_bf16 v[60:63], v[248:251], v[104:107], v[60:63]\n"
"ds_read_b128 v[68:71], v12 offset:0\n"
"ds_read_b128 v[80:83], v13 offset:0\n"
"v_mfma_f32_16x16x32_bf16 v[64:67], v[252:255], v[104:107], v[64:67]\n"
"ds_read_b128 v[72:75], v12 offset:1024\n"
"ds_read_b128 v[84:87], v13 offset:1024\n"
"v_mfma_f32_16x16x32_bf16 v[48:51], v[156:159], v[88:91], v[48:51]\n"
"ds_write_b128 v7, v[56:59] offset:0\n"
"v_mfma_f32_16x16x32_bf16 v[52:55], v[160:163], v[88:91], v[52:55]\n"
"ds_write_b128 v6, v[60:63] offset:0\n"
"v_mfma_f32_16x16x32_bf16 v[48:51], v[176:179], v[92:95], v[48:51]\n"
"ds_write_b128 v6, v[64:67] offset:1024\n"
"v_mfma_f32_16x16x32_bf16 v[52:55], v[180:183], v[92:95], v[52:55]\n"
"ds_write_b32 v4, v4 offset:784\n"
"v_mfma_f32_16x16x32_bf16 v[48:51], v[196:199], v[96:99], v[48:51]\n"
"s_mov_b32 s17, 0x100000\n"
"s_waitcnt lgkmcnt(10)\n"
"v_cmp_eq_u32 vcc, 0, v3\n"
"s_cbranch_vccz .Lok_577\n"
".Ltp_576:\n"
"ds_read_b32 v3, v5 offset:768\n"
"ds_read_b128 v[76:79], v12 offset:2048\n"
"ds_read_b128 v[108:111], v13 offset:2048\n"
"ds_read_b128 v[68:71], v12 offset:0\n"
"ds_read_b128 v[80:83], v13 offset:0\n"
"ds_read_b128 v[72:75], v12 offset:1024\n"
"ds_read_b128 v[84:87], v13 offset:1024\n"
"s_sub_u32 s17, s17, 1\n"
"s_cbranch_scc1 .Lok_577\n"
"s_waitcnt lgkmcnt(6)\n"
"v_cmp_eq_u32 vcc, 0, v3\n"
"s_cbranch_vccnz .Ltp_576\n"
"s_waitcnt lgkmcnt(0)\n"
".Lok_577:\n"
"v_mfma_f32_16x16x32_bf16 v[52:55], v[200:203], v[96:99], v[52:55]\n"
"s_waitcnt lgkmcnt(8)\n"
"v_add_f32 v76, v76, v108\n"
"v_add_f32 v77, v77, v109\n"
"v_add_f32 v78, v78, v110\n"
"v_mfma_f32_16x16x32_bf16 v[48:51], v[216:219], v[100:103], v[48:51]\n"
"v_add_f32 v79, v79, v111\n"
"s_waitcnt lgkmcnt(6)\n"
"v_add_f32 v68, v68, v80\n"
"v_add_f32 v69, v69, v81\n"
"v_mfma_f32_16x16x32_bf16 v[52:55], v[220:223], v[100:103], v[52:55]\n"
"v_add_f32 v70, v70, v82\n"
"v_add_f32 v71, v71, v83\n"
"s_waitcnt lgkmcnt(4)\n"
"v_add_f32 v72, v72, v84\n"
"v_mfma_f32_16x16x32_bf16 v[48:51], v[236:239], v[104:107], v[48:51]\n"
"v_add_f32 v73, v73, v85\n"
"v_add_f32 v74, v74, v86\n"
"v_add_f32 v75, v75, v87\n"
"v_mfma_f32_16x16x32_bf16 v[52:55], v[240:243], v[104:107], v[52:55]\n"
"ds_read_b32 v3, v5 offset:784\n"
"ds_read_b128 v[112:115], v11 offset:2048\n"
"ds_read_b128 v[60:63], v11 offset:0\n"
"ds_read_b128 v[64:67], v11 offset:1024\n"
"v_add_f32 v56, v56, v76\n"
"v_add_f32 v57, v57, v77\n"
"v_add_f32 v58, v58, v78\n"
"v_add_f32 v59, v59, v79\n"
"v_add_f32 v48, v48, v68\n"
"v_add_f32 v49, v49, v69\n"
"v_add_f32 v50, v50, v70\n"
"v_add_f32 v51, v51, v71\n"
"v_add_f32 v52, v52, v72\n"
"v_add_f32 v53, v53, v73\n"
"v_add_f32 v54, v54, v74\n"
"v_add_f32 v55, v55, v75\n"
"s_mov_b32 s17, 0x100000\n"
"s_waitcnt lgkmcnt(3)\n"
"v_cmp_eq_u32 vcc, 0, v3\n"
"s_cbranch_vccz .Lok_579\n"
".Ltp_578:\n"
"ds_read_b32 v3, v5 offset:784\n"
"ds_read_b128 v[112:115], v11 offset:2048\n"
"ds_read_b128 v[60:63], v11 offset:0\n"
"ds_read_b128 v[64:67], v11 offset:1024\n"
"s_sub_u32 s17, s17, 1\n"
"s_cbranch_scc1 .Lok_579\n"
"s_waitcnt lgkmcnt(3)\n"
"v_cmp_eq_u32 vcc, 0, v3\n"
"s_cbranch_vccnz .Ltp_578\n"
"s_waitcnt lgkmcnt(0)\n"
".Lok_579:\n"
"s_waitcnt lgkmcnt(2)\n"
"v_add_f32 v56, v56, v112\n"
"v_add_f32 v57, v57, v113\n"
"v_add_f32 v58, v58, v114\n"
"v_add_f32 v59, v59, v115\n"
"v_cvt_pk_bf16_f32 v44, v56, v57\n"
"v_cvt_pk_bf16_f32 v45, v58, v59\n"
"v_and_b32 v44, v19, v44\n"
"v_and_b32 v45, v19, v45\n"
"s_waitcnt lgkmcnt(1)\n"
"v_add_f32 v48, v48, v60\n"
"v_add_f32 v49, v49, v61\n"
"v_add_f32 v50, v50, v62\n"
"v_add_f32 v51, v51, v63\n"
"v_cvt_pk_bf16_f32 v40, v48, v49\n"
"v_cvt_pk_bf16_f32 v41, v50, v51\n"
"v_mfma_f32_16x16x32_bf16 v[88:91], v[44:47], v[44:47], 0\n"
"v_mfma_f32_16x16x32_bf16 v[92:95], v[20:23], v[44:47], 0\n"
"s_waitcnt lgkmcnt(0)\n"
"v_add_f32 v52, v52, v64\n"
"v_add_f32 v53, v53, v65\n"
"v_add_f32 v54, v54, v66\n"
"v_add_f32 v55, v55, v67\n"
"v_cvt_pk_bf16_f32 v42, v52, v53\n"
"v_cvt_pk_bf16_f32 v43, v54, v55\n"
"v_cvt_pk_bf16_f32 v116, v48, v49\n"
"v_cvt_pk_bf16_f32 v118, v50, v51\n"
"v_mfma_f32_16x16x32_bf16 v[88:91], v[40:43], v[40:43], v[88:91]\n"
"v_mfma_f32_16x16x32_bf16 v[92:95], v[20:23], v[40:43], v[92:95]\n"
"v_cvt_pk_bf16_f32 v120, v52, v53\n"
"v_cvt_pk_bf16_f32 v122, v54, v55\n"
"v_cvt_pk_bf16_f32 v124, v56, v57\n"
"v_cvt_pk_bf16_f32 v126, v58, v59\n"
"s_nop 0\n"
"v_cndmask_b32 v101, v88, v90, s[44:45]\n"
"v_cndmask_b32 v88, v89, v91, s[44:45]\n"
"s_nop 0\n"
"v_min_u32 v100, -2, v92\n"
"v_min_u32 v88, -2, v88\n"
"v_min_u32 v101, -2, v101\n"
"v_mov_b32_dpp v102, v100 quad_perm:[1,0,3,2] row_mask:0xf bank_mask:0xf\n"
"v_mov_b32_dpp v103, v88 quad_perm:[1,0,3,2] row_mask:0xf bank_mask:0xf\n"
"buffer_store_dwordx4 v[100:103], v16, s[4:7], s38 offen sc1\n"
"buffer_store_dwordx4 v[116:119], v17, s[4:7], s37 offen offset:0 sc1\n"
"buffer_store_dwordx4 v[120:123], v17, s[4:7], s37 offen offset:1024 sc1\n"
"buffer_store_dwordx4 v[124:127], v18, s[4:7], s37 offen offset:2048 sc1\n"
"ds_write_b32 v4, v4 offset:800\n"
"s_xor_b32 s79, s3, 1\n"
"s_lshl_b32 s79, s79, 8\n"
"s_add_u32 s32, s19, s79\n"
"s_cmp_ge_u32 s32, 64\n"
"s_cbranch_scc1 .Lnr_580\n"
"s_lshl_b32 s79, s19, 11\n"
"s_add_u32 s32, s79, 0x1648000\n"
"s_lshl_b32 s79, s19, 7\n"
"s_add_u32 s33, s79, 0x166a000\n"
"v_lshrrev_b32 v99, 3, v0\n"
"v_and_b32 v98, 7, v0\n"
"v_lshlrev_b32 v98, 4, v98\n"
"v_lshl_add_u32 v40, v99, 14, v98\n"
"v_mov_b32 v41, 0x3727c5ac\n"
"s_mov_b32 s89, 16\n"
".Ldl_581:\n"
"s_sub_u32 s89, s89, 1\n"
"s_cbranch_scc1 .Lde_582\n"
"s_sleep 1\n"
"s_branch .Ldl_581\n"
".Lde_582:\n"
"buffer_load_dwordx4 v[48:51], v2, s[4:7], s32 offen offset:0 sc1\n"
"buffer_load_dwordx4 v[52:55], v2, s[4:7], s32 offen offset:1024 sc1\n"
"s_mov_b32 s17, 32768\n"
".Ltp_583:\n"
"s_waitcnt vmcnt(0)\n"
"s_sub_u32 s17, s17, 1\n"
"s_cbranch_scc1 .Lou_584\n"
"v_max_u32 v38, v48, v49\n"
"v_max3_u32 v38, v50, v51, v38\n"
"v_max3_u32 v38, v52, v53, v38\n"
"v_max3_u32 v38, v54, v55, v38\n"
"v_cmp_eq_u32 vcc, -1, v38\n"
"s_cbranch_vccz .Lou_584\n"
"v_max_u32 v38, v50, v51\n"
"v_max3_u32 v38, v48, v49, v38\n"
"v_cmp_eq_u32 vcc, -1, v38\n"
"s_cbranch_vccz .Lsk_585\n"
"s_mov_b64 exec, vcc\n"
"buffer_load_dwordx4 v[48:51], v2, s[4:7], s32 offen offset:0 sc1\n"
"s_mov_b64 exec, -1\n"
".Lsk_585:\n"
"v_max_u32 v38, v54, v55\n"
"v_max3_u32 v38, v52, v53, v38\n"
"v_cmp_eq_u32 vcc, -1, v38\n"
"s_cbranch_vccz .Lsk_586\n"
"s_mov_b64 exec, vcc\n"
"buffer_load_dwordx4 v[52:55], v2, s[4:7], s32 offen offset:1024 sc1\n"
"s_mov_b64 exec, -1\n"
".Lsk_586:\n"
"s_branch .Ltp_583\n"
".Lou_584:\n"
"s_waitcnt vmcnt(0)\n"
"v_add_f32 v48, v48, v52\n"
"v_add_f32 v49, v49, v53\n"
"v_add_f32 v50, v50, v54\n"
"v_add_f32 v51, v51, v55\n"
"v_add_f32_dpp v52, v48, v48 row_ror:8 row_mask:0xf bank_mask:0xf\n"
"v_add_f32_dpp v53, v49, v49 row_ror:8 row_mask:0xf bank_mask:0xf\n"
"v_add_f32_dpp v54, v50, v50 row_ror:8 row_mask:0xf bank_mask:0xf\n"
"v_add_f32_dpp v55, v51, v51 row_ror:8 row_mask:0xf bank_mask:0xf\n"
"v_mov_b32 v48, v52\n"
"v_mov_b32 v49, v53\n"
"v_mov_b32 v50, v54\n"
"v_mov_b32 v51, v55\n"
"v_permlane16_swap_b32 v48, v52\n"
"v_permlane16_swap_b32 v49, v53\n"
"v_permlane16_swap_b32 v50, v54\n"
"v_permlane16_swap_b32 v51, v55\n"
"v_add_f32 v52, v48, v52\n"
"v_add_f32 v53, v49, v53\n"
"v_add_f32 v54, v50, v54\n"
"v_add_f32 v55, v51, v55\n"
"v_mov_b32 v48, v52\n"
"v_mov_b32 v49, v53\n"
"v_mov_b32 v50, v54\n"
"v_mov_b32 v51, v55\n"
"v_permlane32_swap_b32 v48, v52\n"
"v_permlane32_swap_b32 v49, v53\n"
"v_permlane32_swap_b32 v50, v54\n"
"v_permlane32_swap_b32 v51, v55\n"
"v_add_f32 v52, v48, v52\n"
"v_add_f32 v53, v49, v53\n"
"v_add_f32 v54, v50, v54\n"
"v_add_f32 v55, v51, v55\n"
"v_mul_f32 v48, 0x3acccccd, v52\n"
"v_mul_f32 v50, 0x3acccccd, v54\n"
"v_fma_f32 v53, -v52, v48, v53\n"
"v_fma_f32 v55, -v54, v50, v55\n"
"v_fmamk_f32 v53, v53, 0x3acccccd, v41\n"
"v_fmamk_f32 v55, v55, 0x3acccccd, v41\n"
"v_max_f32 v53, v53, v41\n"
"v_max_f32 v55, v55, v41\n"
"v_rsq_f32 v49, v53\n"
"v_rsq_f32 v51, v55\n"
"v_mul_f32 v48, -v48, v49\n"
"v_mul_f32 v50, -v50, v51\n"
"v_min_u32 v49, -2, v49\n"
"v_min_u32 v51, -2, v51\n"
"v_min_u32 v48, -2, v48\n"
"v_min_u32 v50, -2, v50\n"
"buffer_store_dwordx4 v[48:51], v40, s[4:7], s33 offen sc1\n"
"s_nop 3\n"
".Lnr_580:\n"
"s_cmp_lt_i32 s18, 0\n"
"s_cbranch_scc1 .Lnc_587\n"
"s_mov_b32 s17, 0x100000\n"
".Ltp_588:\n"
"s_sleep 1\n"
"ds_read_b32 v3, v5 offset:512\n"
"s_sub_u32 s17, s17, 1\n"
"s_cbranch_scc1 .Lou_589\n"
"s_waitcnt lgkmcnt(0)\n"
"v_cmp_eq_u32 vcc, 0, v3\n"
"s_cbranch_vccnz .Ltp_588\n"
".Lou_589:\n"
"s_waitcnt lgkmcnt(0)\n"
"s_setprio 0\n"
"s_lshl_b32 s79, s18, 8\n"
"s_add_u32 s32, s79, 0x13a8000\n"
"s_add_u32 s33, s32, 0x140000\n"
"s_and_b32 s79, s2, 7\n"
"s_lshl_b32 s79, s79, 14\n"
"s_add_u32 s34, s79, 0x166a000\n"
"s_add_u32 s35, s34, 0x1000\n"
"v_and_b32 v126, 0xffffff80, v29\n"
"v_add_u32 v126, v126, v29\n"
"ds_read_b128 v[88:91], v25 offset:448\n"
"ds_read_b128 v[92:95], v25 offset:432\n"
"ds_read_b64 v[96:97], v24 offset:448\n"
"ds_read_b64 v[98:99], v24 offset:456\n"
"ds_read_b128 v[100:103], v25 offset:960\n"
"ds_read_b128 v[104:107], v25 offset:1472\n"
"s_waitcnt lgkmcnt(1)\n"
"v_mfma_f32_16x16x32_f16 v[64:67], v[88:91], v[144:147], 0\n"
"ds_read_b128 v[40:43], v25 offset:1984\n"
"ds_read_b64 v[44:45], v24 offset:960\n"
"v_mfma_f32_16x16x32_f16 v[68:71], v[92:95], v[148:151], 0\n"
"ds_read_b64 v[46:47], v24 offset:968\n"
"ds_read_b64 v[72:73], v24 offset:1472\n"
"v_mfma_f32_16x16x32_f16 v[48:51], v[152:155], v[88:91], 0\n"
"ds_read_b64 v[74:75], v24 offset:1480\n"
"ds_read_b64 v[76:77], v24 offset:384\n"
"v_mfma_f32_16x16x32_f16 v[48:51], v[96:99], v[100:103], v[48:51]\n"
"ds_read_b64 v[78:79], v24 offset:392\n"
"s_waitcnt lgkmcnt(4)\n"
"v_mfma_f32_16x16x32_f16 v[52:55], v[96:99], v[104:107], 0\n"
"ds_read_b128 v[84:87], v25 offset:896\n"
"ds_read_b128 v[92:95], v25 offset:1408\n"
"v_mfma_f32_16x16x32_f16 v[56:59], v[96:99], v[40:43], 0\n"
"ds_read_b128 v[88:91], v25 offset:1920\n"
"ds_read_b64 v[100:101], v24 offset:896\n"
"v_mfma_f32_16x16x32_f16 v[52:55], v[44:47], v[40:43], v[52:55]\n"
"ds_read_b64 v[102:103], v24 offset:904\n"
"ds_read_b64 v[96:97], v24 offset:1408\n"
"v_cvt_pk_f16_f32 v80, v64, v65\n"
"v_mfma_f32_16x16x32_f16 v[48:51], v[44:47], v[104:107], v[48:51]\n"
"ds_read_b64 v[98:99], v24 offset:1416\n"
"s_waitcnt lgkmcnt(4)\n"
"v_mfma_f32_16x16x32_f16 v[48:51], v[72:75], v[40:43], v[48:51]\n"
"ds_read_b128 v[104:107], v25 offset:960\n"
"v_cvt_pk_f16_f32 v81, v66, v67\n"
"v_mfma_f32_16x16x32_f16 v[48:51], v[76:79], v[84:87], v[48:51]\n"
"ds_read_b128 v[44:47], v25 offset:944\n"
"v_cvt_pk_f16_f32 v82, v68, v69\n"
"v_mfma_f32_16x16x32_f16 v[52:55], v[76:79], v[92:95], v[52:55]\n"
"ds_read_b64 v[40:41], v24 offset:320\n"
"ds_read_b64 v[42:43], v24 offset:328\n"
"v_mfma_f32_16x16x32_f16 v[56:59], v[76:79], v[88:91], v[56:59]\n"
"ds_read_b128 v[72:75], v25 offset:832\n"
"s_waitcnt lgkmcnt(5)\n"
"v_mfma_f32_16x16x32_f16 v[52:55], v[100:103], v[88:91], v[52:55]\n"
"ds_read_b128 v[84:87], v25 offset:1344\n"
"v_cvt_pk_f16_f32 v83, v70, v71\n"
"v_mfma_f32_16x16x32_f16 v[48:51], v[100:103], v[92:95], v[48:51]\n"
"ds_read_b128 v[76:79], v25 offset:1856\n"
"ds_read_b64 v[92:93], v24 offset:832\n"
"v_mfma_f32_16x16x32_f16 v[48:51], v[96:99], v[88:91], v[48:51]\n"
"ds_read_b64 v[94:95], v24 offset:840\n"
"v_mfma_f32_16x16x32_f16 v[48:51], v[80:83], v[128:131], v[48:51]\n"
"ds_read_b64 v[100:101], v24 offset:1344\n"
"s_waitcnt lgkmcnt(9)\n"
"v_mfma_f32_16x16x32_f16 v[52:55], v[80:83], v[132:135], v[52:55]\n"
"ds_read_b64 v[102:103], v24 offset:1352\n"
"ds_read_b64 v[88:89], v24 offset:256\n"
"v_mfma_f32_16x16x32_f16 v[56:59], v[80:83], v[136:139], v[56:59]\n"
"ds_read_b64 v[90:91], v24 offset:264\n"
"v_mfma_f32_16x16x32_f16 v[60:63], v[80:83], v[140:143], 0\n"
"ds_read_b128 v[96:99], v25 offset:768\n"
"v_mfma_f32_16x16x32_f16 v[64:67], v[104:107], v[144:147], 0\n"
"s_waitcnt lgkmcnt(8)\n"
"v_mfma_f32_16x16x32_f16 v[68:71], v[44:47], v[148:151], 0\n"
"ds_read_b128 v[44:47], v25 offset:1280\n"
"v_mfma_f32_16x16x32_f16 v[52:55], v[152:155], v[104:107], v[52:55]\n"
"ds_read_b128 v[104:107], v25 offset:1792\n"
"v_mfma_f32_16x16x32_f16 v[48:51], v[40:43], v[72:75], v[48:51]\n"
"ds_read_b64 v[72:73], v24 offset:768\n"
"v_mfma_f32_16x16x32_f16 v[52:55], v[40:43], v[84:87], v[52:55]\n"
"ds_read_b64 v[74:75], v24 offset:776\n"
"s_waitcnt lgkmcnt(7)\n"
"v_mfma_f32_16x16x32_f16 v[56:59], v[40:43], v[76:79], v[56:59]\n"
"ds_read_b64 v[40:41], v24 offset:1280\n"
"v_mfma_f32_16x16x32_f16 v[52:55], v[92:95], v[76:79], v[52:55]\n"
"ds_read_b64 v[42:43], v24 offset:1288\n"
"v_cvt_pk_f16_f32 v80, v64, v65\n"
"v_mfma_f32_16x16x32_f16 v[48:51], v[92:95], v[84:87], v[48:51]\n"
"ds_read_b128 v[84:87], v25 offset:1472\n"
"v_cvt_pk_f16_f32 v81, v66, v67\n"
"v_mfma_f32_16x16x32_f16 v[48:51], v[100:103], v[76:79], v[48:51]\n"
"ds_read_b128 v[92:95], v25 offset:1456\n"
"s_waitcnt lgkmcnt(4)\n"
"v_mfma_f32_16x16x32_f16 v[48:51], v[88:91], v[96:99], v[48:51]\n"
"ds_read_b64 v[76:77], v24 offset:192\n"
"ds_read_b64 v[78:79], v24 offset:200\n"
"v_mfma_f32_16x16x32_f16 v[52:55], v[88:91], v[44:47], v[52:55]\n"
"ds_read_b128 v[100:103], v25 offset:704\n"
"v_cvt_pk_f16_f32 v82, v68, v69\n"
"v_mfma_f32_16x16x32_f16 v[56:59], v[88:91], v[104:107], v[56:59]\n"
"ds_read_b128 v[96:99], v25 offset:1216\n"
"v_cvt_pk_f16_f32 v83, v70, v71\n"
"v_mfma_f32_16x16x32_f16 v[52:55], v[72:75], v[104:107], v[52:55]\n"
"ds_read_b128 v[88:91], v25 offset:1728\n"
"s_waitcnt lgkmcnt(7)\n"
"v_mfma_f32_16x16x32_f16 v[48:51], v[72:75], v[44:47], v[48:51]\n"
"ds_read_b64 v[44:45], v24 offset:704\n"
"v_mfma_f32_16x16x32_f16 v[48:51], v[40:43], v[104:107], v[48:51]\n"
"ds_read_b64 v[46:47], v24 offset:712\n"
"ds_read_b64 v[72:73], v24 offset:1216\n"
"v_mfma_f32_16x16x32_f16 v[52:55], v[80:83], v[128:131], v[52:55]\n"
"ds_read_b64 v[74:75], v24 offset:1224\n"
"ds_read_b64 v[104:105], v24 offset:128\n"
"v_mfma_f32_16x16x32_f16 v[56:59], v[80:83], v[132:135], v[56:59]\n"
"ds_read_b64 v[106:107], v24 offset:136\n"
"s_waitcnt lgkmcnt(11)\n"
"v_mfma_f32_16x16x32_f16 v[60:63], v[80:83], v[136:139], v[60:63]\n"
"ds_read_b128 v[40:43], v25 offset:640\n"
"v_mfma_f32_16x16x32_f16 v[64:67], v[84:87], v[144:147], 0\n"
"v_mfma_f32_16x16x32_f16 v[68:71], v[92:95], v[148:151], 0\n"
"ds_read_b128 v[92:95], v25 offset:1152\n"
"v_mfma_f32_16x16x32_f16 v[56:59], v[152:155], v[84:87], v[56:59]\n"
"ds_read_b128 v[84:87], v25 offset:1664\n"
"s_waitcnt lgkmcnt(7)\n"
"v_mfma_f32_16x16x32_f16 v[48:51], v[76:79], v[100:103], v[48:51]\n"
"ds_read_b64 v[100:101], v24 offset:640\n"
"v_mfma_f32_16x16x32_f16 v[52:55], v[76:79], v[96:99], v[52:55]\n"
"ds_read_b64 v[102:103], v24 offset:648\n"
"v_mfma_f32_16x16x32_f16 v[56:59], v[76:79], v[88:91], v[56:59]\n"
"ds_read_b64 v[76:77], v24 offset:1152\n"
"v_mfma_f32_16x16x32_f16 v[52:55], v[44:47], v[88:91], v[52:55]\n"
"ds_read_b64 v[78:79], v24 offset:1160\n"
"s_waitcnt lgkmcnt(5)\n"
"v_mfma_f32_16x16x32_f16 v[48:51], v[44:47], v[96:99], v[48:51]\n"
"ds_read_b128 v[96:99], v25 offset:1984\n"
"v_cvt_pk_f16_f32 v80, v64, v65\n"
"v_mfma_f32_16x16x32_f16 v[48:51], v[72:75], v[88:91], v[48:51]\n"
"ds_read_b128 v[44:47], v25 offset:1968\n"
"ds_read_b64 v[88:89], v24 offset:64\n"
"v_mfma_f32_16x16x32_f16 v[48:51], v[104:107], v[40:43], v[48:51]\n"
"ds_read_b64 v[90:91], v24 offset:72\n"
"ds_read_b128 v[72:75], v25 offset:576\n"
"v_mfma_f32_16x16x32_f16 v[52:55], v[104:107], v[92:95], v[52:55]\n"
"ds_read_b128 v[40:43], v25 offset:1088\n"
"s_waitcnt lgkmcnt(6)\n"
"v_mfma_f32_16x16x32_f16 v[56:59], v[104:107], v[84:87], v[56:59]\n"
"ds_read_b128 v[104:107], v25 offset:1600\n"
"v_cvt_pk_f16_f32 v81, v66, v67\n"
"v_mfma_f32_16x16x32_f16 v[52:55], v[100:103], v[84:87], v[52:55]\n"
"v_cvt_pk_f16_f32 v82, v68, v69\n"
"v_cvt_pk_f16_f32 v83, v70, v71\n"
"v_mfma_f32_16x16x32_f16 v[48:51], v[100:103], v[92:95], v[48:51]\n"
"ds_read_b64 v[92:93], v24 offset:576\n"
"ds_read_b64 v[94:95], v24 offset:584\n"
"v_mfma_f32_16x16x32_f16 v[48:51], v[76:79], v[84:87], v[48:51]\n"
"ds_read_b64 v[100:101], v24 offset:1088\n"
"s_waitcnt lgkmcnt(8)\n"
"v_mfma_f32_16x16x32_f16 v[56:59], v[80:83], v[128:131], v[56:59]\n"
"ds_read_b64 v[102:103], v24 offset:1096\n"
"ds_read_b64 v[84:85], v24 offset:0\n"
"v_mfma_f32_16x16x32_f16 v[60:63], v[80:83], v[132:135], v[60:63]\n"
"ds_read_b64 v[86:87], v24 offset:8\n"
"v_mfma_f32_16x16x32_f16 v[64:67], v[96:99], v[144:147], 0\n"
"ds_read_b128 v[76:79], v25 offset:512\n"
"v_mfma_f32_16x16x32_f16 v[68:71], v[44:47], v[148:151], 0\n"
"ds_read_b128 v[44:47], v25 offset:1024\n"
"s_waitcnt lgkmcnt(8)\n"
"v_mfma_f32_16x16x32_f16 v[60:63], v[152:155], v[96:99], v[60:63]\n"
"ds_read_b128 v[96:99], v25 offset:1536\n"
"v_mfma_f32_16x16x32_f16 v[48:51], v[88:91], v[72:75], v[48:51]\n"
"ds_read_b64 v[72:73], v24 offset:512\n"
"v_mfma_f32_16x16x32_f16 v[52:55], v[88:91], v[40:43], v[52:55]\n"
"ds_read_b64 v[74:75], v24 offset:520\n"
"v_mfma_f32_16x16x32_f16 v[56:59], v[88:91], v[104:107], v[56:59]\n"
"ds_read_b64 v[88:89], v24 offset:1024\n"
"s_waitcnt lgkmcnt(5)\n"
"v_mfma_f32_16x16x32_f16 v[52:55], v[92:95], v[104:107], v[52:55]\n"
"ds_read_b64 v[90:91], v24 offset:1032\n"
"v_cvt_pk_f16_f32 v80, v64, v65\n"
"v_mfma_f32_16x16x32_f16 v[48:51], v[92:95], v[40:43], v[48:51]\n"
"v_cvt_pk_f16_f32 v81, v66, v67\n"
"v_cvt_pk_f16_f32 v82, v68, v69\n"
"v_mfma_f32_16x16x32_f16 v[48:51], v[100:103], v[104:107], v[48:51]\n"
"v_cvt_pk_f16_f32 v83, v70, v71\n"
"v_mfma_f32_16x16x32_f16 v[48:51], v[84:87], v[76:79], v[48:51]\n"
"s_waitcnt lgkmcnt(2)\n"
"v_mfma_f32_16x16x32_f16 v[52:55], v[84:87], v[44:47], v[52:55]\n"
"v_mfma_f32_16x16x32_f16 v[56:59], v[84:87], v[96:99], v[56:59]\n"
"v_mfma_f32_16x16x32_f16 v[52:55], v[72:75], v[96:99], v[52:55]\n"
"v_mfma_f32_16x16x32_f16 v[48:51], v[72:75], v[44:47], v[48:51]\n"
"s_waitcnt lgkmcnt(0)\n"
"v_mfma_f32_16x16x32_f16 v[48:51], v[88:91], v[96:99], v[48:51]\n"
"v_mfma_f32_16x16x32_f16 v[60:63], v[80:83], v[128:131], v[60:63]\n"
"s_setprio 2\n"
"s_mov_b32 s17, 0x100000\n"
".Ltp_590:\n"
"ds_read_b32 v3, v5 offset:800\n"
"s_sub_u32 s17, s17, 1\n"
"s_cbranch_scc1 .Lou_591\n"
"s_waitcnt lgkmcnt(0)\n"
"v_cmp_eq_u32 vcc, 0, v3\n"
"s_cbranch_vccnz .Ltp_590\n"
".Lou_591:\n"
"s_waitcnt lgkmcnt(0)\n"
"buffer_load_dwordx4 v[110:113], v126, s[4:7], s32 offen offset:0 sc1\n"
"buffer_load_dwordx4 v[114:117], v126, s[4:7], s32 offen offset:128 sc1\n"
"buffer_load_dwordx4 v[118:121], v126, s[4:7], s33 offen offset:0 sc1\n"
"buffer_load_dwordx4 v[122:125], v126, s[4:7], s33 offen offset:128 sc1\n"
"ds_read_b64 v[128:129], v24 offset:1984\n"
"ds_read_b64 v[130:131], v24 offset:1992\n"
"ds_read_b64 v[132:133], v24 offset:1920\n"
"ds_read_b64 v[134:135], v24 offset:1928\n"
"ds_read_b64 v[136:137], v24 offset:1856\n"
"ds_read_b64 v[138:139], v24 offset:1864\n"
"ds_read_b64 v[140:141], v24 offset:1792\n"
"ds_read_b64 v[142:143], v24 offset:1800\n"
"ds_read_b64 v[144:145], v24 offset:1728\n"
"ds_read_b64 v[146:147], v24 offset:1736\n"
"ds_read_b64 v[148:149], v24 offset:1664\n"
"ds_read_b64 v[150:151], v24 offset:1672\n"
"ds_read_b64 v[152:153], v24 offset:1600\n"
"ds_read_b64 v[154:155], v24 offset:1608\n"
"ds_read_b64 v[156:157], v24 offset:1536\n"
"ds_read_b64 v[158:159], v24 offset:1544\n"
"ds_read_b64 v[160:161], v24 offset:1472\n"
"ds_read_b64 v[162:163], v24 offset:1480\n"
"ds_read_b64 v[164:165], v24 offset:1408\n"
"ds_read_b64 v[166:167], v24 offset:1416\n"
"ds_read_b64 v[168:169], v24 offset:1344\n"
"ds_read_b64 v[170:171], v24 offset:1352\n"
"ds_read_b64 v[172:173], v24 offset:1280\n"
"ds_read_b64 v[174:175], v24 offset:1288\n"
"ds_read_b64 v[176:177], v24 offset:1216\n"
"ds_read_b64 v[178:179], v24 offset:1224\n"
"ds_read_b64 v[180:181], v24 offset:1152\n"
"ds_read_b64 v[182:183], v24 offset:1160\n"
"ds_read_b64 v[184:185], v24 offset:1088\n"
"ds_read_b64 v[186:187], v24 offset:1096\n"
"ds_read_b64 v[188:189], v24 offset:1024\n"
"ds_read_b64 v[190:191], v24 offset:1032\n"
"ds_read_b64 v[192:193], v24 offset:960\n"
"ds_read_b64 v[194:195], v24 offset:968\n"
"ds_read_b64 v[196:197], v24 offset:896\n"
"ds_read_b64 v[198:199], v24 offset:904\n"
"ds_read_b64 v[200:201], v24 offset:832\n"
"ds_read_b64 v[202:203], v24 offset:840\n"
"ds_read_b64 v[204:205], v24 offset:768\n"
"ds_read_b64 v[206:207], v24 offset:776\n"
"ds_read_b64 v[208:209], v24 offset:704\n"
"ds_read_b64 v[210:211], v24 offset:712\n"
"ds_read_b64 v[212:213], v24 offset:640\n"
"ds_read_b64 v[214:215], v24 offset:648\n"
"ds_read_b64 v[216:217], v24 offset:576\n"
"ds_read_b64 v[218:219], v24 offset:584\n"
"ds_read_b64 v[220:221], v24 offset:512\n"
"ds_read_b64 v[222:223], v24 offset:520\n"
"ds_read_b64 v[224:225], v24 offset:448\n"
"ds_read_b64 v[226:227], v24 offset:456\n"
"ds_read_b64 v[228:229], v24 offset:384\n"
"ds_read_b64 v[230:231], v24 offset:392\n"
"ds_read_b64 v[232:233], v24 offset:320\n"
"ds_read_b64 v[234:235], v24 offset:328\n"
"ds_read_b64 v[236:237], v24 offset:256\n"
"ds_read_b64 v[238:239], v24 offset:264\n"
"ds_read_b64 v[240:241], v24 offset:192\n"
"ds_read_b64 v[242:243], v24 offset:200\n"
"ds_read_b64 v[244:245], v24 offset:128\n"
"ds_read_b64 v[246:247], v24 offset:136\n"
"ds_read_b64 v[248:249], v24 offset:64\n"
"ds_read_b64 v[250:251], v24 offset:72\n"
"ds_read_b64 v[252:253], v24 offset:0\n"
"ds_read_b64 v[254:255], v24 offset:8\n"
"s_mov_b32 s38, 0x05040100\n"
"s_mov_b32 s39, 0x07060302\n"
"s_mov_b32 s17, 32768\n"
".Ltp_592:\n"
"s_waitcnt vmcnt(0)\n"
"s_sub_u32 s17, s17, 1\n"
"s_cbranch_scc1 .Lou_593\n"
"v_bitop3_b32 v64, v111, v113, s16 bitop3:0x7e\n"
"v_bitop3_b32 v65, v115, v117, s16 bitop3:0x7e\n"
"v_bitop3_b32 v66, v119, v121, s16 bitop3:0x7e\n"
"v_bitop3_b32 v67, v123, v125, s16 bitop3:0x7e\n"
"v_or3_b32 v68, v64, v65, v66\n"
"v_or_b32 v68, v68, v67\n"
"v_cmp_ne_u32 vcc, 0, v68\n"
"s_cbranch_vccz .Lou_593\n"
"s_sleep 2\n"
"v_cmp_ne_u32 vcc, 0, v64\n"
"s_cbranch_vccz .Lsk_594\n"
"s_mov_b64 exec, vcc\n"
"buffer_load_dwordx4 v[110:113], v126, s[4:7], s32 offen offset:0 sc1\n"
"s_mov_b64 exec, -1\n"
".Lsk_594:\n"
"v_cmp_ne_u32 vcc, 0, v65\n"
"s_cbranch_vccz .Lsk_595\n"
"s_mov_b64 exec, vcc\n"
"buffer_load_dwordx4 v[114:117], v126, s[4:7], s32 offen offset:128 sc1\n"
"s_mov_b64 exec, -1\n"
".Lsk_595:\n"
"v_cmp_ne_u32 vcc, 0, v66\n"
"s_cbranch_vccz .Lsk_596\n"
"s_mov_b64 exec, vcc\n"
"buffer_load_dwordx4 v[118:121], v126, s[4:7], s33 offen offset:0 sc1\n"
"s_mov_b64 exec, -1\n"
".Lsk_596:\n"
"v_cmp_ne_u32 vcc, 0, v67\n"
"s_cbranch_vccz .Lsk_597\n"
"s_mov_b64 exec, vcc\n"
"buffer_load_dwordx4 v[122:125], v126, s[4:7], s33 offen offset:128 sc1\n"
"s_mov_b64 exec, -1\n"
".Lsk_597:\n"
"s_branch .Ltp_592\n"
".Lou_593:\n"
"s_waitcnt vmcnt(0)\n"
"buffer_load_dwordx4 v[88:91], v30, s[4:7], s34 offen sc1\n"
"buffer_load_dwordx4 v[92:95], v30, s[4:7], s35 offen sc1\n"
"v_perm_b32 v64, v114, v110, s38\n"
"v_perm_b32 v65, v114, v110, s39\n"
"v_perm_b32 v66, v116, v112, s38\n"
"v_perm_b32 v67, v116, v112, s39\n"
"ds_write_b32 v32, v64 offset:0\n"
"ds_write_b32 v33, v65 offset:2048\n"
"ds_write_b32 v32, v66 offset:4224\n"
"ds_write_b32 v33, v67 offset:6272\n"
"v_perm_b32 v64, v122, v118, s38\n"
"v_perm_b32 v65, v122, v118, s39\n"
"v_perm_b32 v66, v124, v120, s38\n"
"v_perm_b32 v67, v124, v120, s39\n"
"ds_write_b32 v32, v64 offset:1024\n"
"ds_write_b32 v33, v65 offset:3072\n"
"ds_write_b32 v32, v66 offset:5248\n"
"ds_write_b32 v33, v67 offset:7296\n"
"v_lshlrev_b32 v64, 16, v110\n"
"v_lshlrev_b32 v65, 16, v114\n"
"v_and_b32 v66, 0xffff0000, v110\n"
"v_and_b32 v67, 0xffff0000, v114\n"
"v_lshlrev_b32 v68, 16, v112\n"
"v_lshlrev_b32 v69, 16, v116\n"
"v_and_b32 v70, 0xffff0000, v112\n"
"v_and_b32 v71, 0xffff0000, v116\n"
"v_lshlrev_b32 v72, 16, v118\n"
"v_lshlrev_b32 v73, 16, v122\n"
"v_and_b32 v74, 0xffff0000, v118\n"
"v_and_b32 v75, 0xffff0000, v122\n"
"v_lshlrev_b32 v76, 16, v120\n"
"v_lshlrev_b32 v77, 16, v124\n"
"v_and_b32 v78, 0xffff0000, v120\n"
"v_and_b32 v79, 0xffff0000, v124\n"
"s_mov_b32 s17, 32768\n"
".Ltp_598:\n"
"s_waitcnt vmcnt(0)\n"
"s_sub_u32 s17, s17, 1\n"
"s_cbranch_scc1 .Lou_599\n"
"v_max_u32 v110, v88, v89\n"
"v_max3_u32 v110, v90, v91, v110\n"
"v_max3_u32 v110, v92, v93, v110\n"
"v_max3_u32 v110, v94, v95, v110\n"
"v_cmp_eq_u32 vcc, -1, v110\n"
"s_cbranch_vccz .Lou_599\n"
"v_max_u32 v110, v90, v91\n"
"v_max3_u32 v110, v88, v89, v110\n"
"v_cmp_eq_u32 vcc, -1, v110\n"
"s_cbranch_vccz .Lsk_600\n"
"s_mov_b64 exec, vcc\n"
"buffer_load_dwordx4 v[88:91], v30, s[4:7], s34 offen sc1\n"
"s_mov_b64 exec, -1\n"
".Lsk_600:\n"
"v_max_u32 v110, v94, v95\n"
"v_max3_u32 v110, v92, v93, v110\n"
"v_cmp_eq_u32 vcc, -1, v110\n"
"s_cbranch_vccz .Lsk_601\n"
"s_mov_b64 exec, vcc\n"
"buffer_load_dwordx4 v[92:95], v30, s[4:7], s35 offen sc1\n"
"s_mov_b64 exec, -1\n"
".Lsk_601:\n"
"s_branch .Ltp_598\n"
".Lou_599:\n"
"s_waitcnt vmcnt(0)\n"
"v_fma_f32 v64, v64, v89, v88\n"
"v_fma_f32 v65, v65, v91, v90\n"
"v_cvt_pk_f16_f32 v64, v64, v65\n"
"v_fma_f32 v66, v66, v89, v88\n"
"v_fma_f32 v67, v67, v91, v90\n"
"v_cvt_pk_f16_f32 v66, v66, v67\n"
"v_fma_f32 v68, v68, v89, v88\n"
"v_fma_f32 v69, v69, v91, v90\n"
"v_cvt_pk_f16_f32 v68, v68, v69\n"
"v_fma_f32 v70, v70, v89, v88\n"
"v_fma_f32 v71, v71, v91, v90\n"
"v_cvt_pk_f16_f32 v70, v70, v71\n"
"ds_write2st64_b32 v31, v64, v66 offset0:0 offset1:18\n"
"ds_write2st64_b32 v31, v68, v70 offset0:36 offset1:54\n"
"v_fma_f32 v72, v72, v93, v92\n"
"v_fma_f32 v73, v73, v95, v94\n"
"v_cvt_pk_f16_f32 v72, v72, v73\n"
"v_fma_f32 v74, v74, v93, v92\n"
"v_fma_f32 v75, v75, v95, v94\n"
"v_cvt_pk_f16_f32 v74, v74, v75\n"
"v_fma_f32 v76, v76, v93, v92\n"
"v_fma_f32 v77, v77, v95, v94\n"
"v_cvt_pk_f16_f32 v76, v76, v77\n"
"v_fma_f32 v78, v78, v93, v92\n"
"v_fma_f32 v79, v79, v95, v94\n"
"v_cvt_pk_f16_f32 v78, v78, v79\n"
"ds_write2st64_b32 v31, v72, v74 offset0:4 offset1:22\n"
"ds_write2st64_b32 v31, v76, v78 offset0:40 offset1:58\n"
"ds_write_b32 v4, v4 offset:816\n"
"s_mov_b32 s17, 0x100000\n"
".Ltp_602:\n"
"ds_read_b32 v3, v5 offset:816\n"
"s_sub_u32 s17, s17, 1\n"
"s_cbranch_scc1 .Lou_603\n"
"s_waitcnt lgkmcnt(0)\n"
"v_cmp_eq_u32 vcc, 0, v3\n"
"s_cbranch_vccnz .Ltp_602\n"
".Lou_603:\n"
"s_waitcnt lgkmcnt(0)\n"
"s_mov_b32 s36, 0xfffefffe\n"
"ds_read_b64 v[88:89], v26 offset:0\n"
"ds_read_b64 v[92:93], v26 offset:512\n"
"ds_read_b64 v[96:97], v26 offset:1024\n"
"ds_read_b64 v[100:101], v26 offset:1536\n"
"ds_read_b128 v[104:107], v25 offset:2496\n"
"s_waitcnt lgkmcnt(1)\n"
"v_mfma_f32_16x16x16_bf16 v[48:51], v[34:35], v[88:89], v[48:51]\n"
"ds_read_b128 v[108:111], v25 offset:3008\n"
"ds_read_b128 v[112:115], v25 offset:2432\n"
"v_mfma_f32_16x16x16_bf16 v[52:55], v[34:35], v[92:93], v[52:55]\n"
"ds_read_b128 v[116:119], v25 offset:2944\n"
"ds_read_b128 v[120:123], v25 offset:2368\n"
"v_mfma_f32_16x16x16_bf16 v[56:59], v[34:35], v[96:97], v[56:59]\n"
"ds_read_b128 v[124:127], v25 offset:2880\n"
"v_mfma_f32_16x16x16_bf16 v[60:63], v[34:35], v[100:101], v[60:63]\n"
"ds_read_b128 v[40:43], v25 offset:2304\n"
"s_waitcnt lgkmcnt(4)\n"
"v_mfma_f32_16x16x32_f16 v[48:51], v[128:131], v[104:107], v[48:51]\n"
"ds_read_b128 v[44:47], v25 offset:2816\n"
"v_mfma_f32_16x16x32_f16 v[52:55], v[160:163], v[104:107], v[52:55]\n"
"ds_read_b128 v[88:91], v25 offset:2240\n"
"v_mfma_f32_16x16x32_f16 v[52:55], v[128:131], v[108:111], v[52:55]\n"
"ds_read_b128 v[92:95], v25 offset:2752\n"
"v_mfma_f32_16x16x32_f16 v[48:51], v[132:135], v[112:115], v[48:51]\n"
"ds_read_b128 v[96:99], v25 offset:2176\n"
"s_waitcnt lgkmcnt(6)\n"
"v_mfma_f32_16x16x32_f16 v[52:55], v[164:167], v[112:115], v[52:55]\n"
"v_mfma_f32_16x16x32_f16 v[52:55], v[132:135], v[116:119], v[52:55]\n"
"ds_read_b128 v[100:103], v25 offset:2688\n"
"v_mfma_f32_16x16x32_f16 v[48:51], v[136:139], v[120:123], v[48:51]\n"
"ds_read_b128 v[104:107], v25 offset:2112\n"
"v_mfma_f32_16x16x32_f16 v[52:55], v[168:171], v[120:123], v[52:55]\n"
"s_waitcnt lgkmcnt(5)\n"
"v_mfma_f32_16x16x32_f16 v[52:55], v[136:139], v[124:127], v[52:55]\n"
"ds_read_b128 v[108:111], v25 offset:2624\n"
"v_mfma_f32_16x16x32_f16 v[48:51], v[140:143], v[40:43], v[48:51]\n"
"ds_read_b128 v[112:115], v25 offset:2048\n"
"v_mfma_f32_16x16x32_f16 v[52:55], v[172:175], v[40:43], v[52:55]\n"
"v_mfma_f32_16x16x32_f16 v[52:55], v[140:143], v[44:47], v[52:55]\n"
"ds_read_b128 v[116:119], v25 offset:2560\n"
"s_waitcnt lgkmcnt(5)\n"
"v_mfma_f32_16x16x32_f16 v[48:51], v[144:147], v[88:91], v[48:51]\n"
"ds_read_b128 v[120:123], v25 offset:2496\n"
"v_mfma_f32_16x16x32_f16 v[52:55], v[176:179], v[88:91], v[52:55]\n"
"ds_read_b128 v[124:127], v25 offset:3008\n"
"v_mfma_f32_16x16x32_f16 v[52:55], v[144:147], v[92:95], v[52:55]\n"
"ds_read_b128 v[40:43], v25 offset:3520\n"
"v_mfma_f32_16x16x32_f16 v[48:51], v[148:151], v[96:99], v[48:51]\n"
"ds_read_b128 v[44:47], v25 offset:2432\n"
"s_waitcnt lgkmcnt(7)\n"
"v_mfma_f32_16x16x32_f16 v[52:55], v[180:183], v[96:99], v[52:55]\n"
"ds_read_b128 v[88:91], v25 offset:2944\n"
"v_mfma_f32_16x16x32_f16 v[52:55], v[148:151], v[100:103], v[52:55]\n"
"ds_read_b128 v[92:95], v25 offset:3456\n"
"v_mfma_f32_16x16x32_f16 v[48:51], v[152:155], v[104:107], v[48:51]\n"
"ds_read_b128 v[96:99], v25 offset:2368\n"
"v_mfma_f32_16x16x32_f16 v[52:55], v[184:187], v[104:107], v[52:55]\n"
"ds_read_b128 v[100:103], v25 offset:2880\n"
"s_waitcnt lgkmcnt(8)\n"
"v_mfma_f32_16x16x32_f16 v[52:55], v[152:155], v[108:111], v[52:55]\n"
"ds_read_b128 v[104:107], v25 offset:3392\n"
"v_mfma_f32_16x16x32_f16 v[48:51], v[156:159], v[112:115], v[48:51]\n"
"ds_read_b128 v[108:111], v25 offset:2304\n"
"v_mfma_f32_16x16x32_f16 v[52:55], v[188:191], v[112:115], v[52:55]\n"
"ds_read_b128 v[112:115], v25 offset:2816\n"
"v_mfma_f32_16x16x32_f16 v[52:55], v[156:159], v[116:119], v[52:55]\n"
"ds_read_b128 v[116:119], v25 offset:3328\n"
"s_waitcnt lgkmcnt(8)\n"
"v_mfma_f32_16x16x32_f16 v[56:59], v[192:195], v[120:123], v[56:59]\n"
"ds_read_b128 v[120:123], v25 offset:2240\n"
"v_mfma_f32_16x16x32_f16 v[56:59], v[160:163], v[124:127], v[56:59]\n"
"ds_read_b128 v[124:127], v25 offset:2752\n"
"v_mfma_f32_16x16x32_f16 v[56:59], v[128:131], v[40:43], v[56:59]\n"
"ds_read_b128 v[40:43], v25 offset:3264\n"
"v_mul_f32 v48, v36, v48\n"
"v_mul_f32 v49, v36, v49\n"
"v_mul_f32 v50, v36, v50\n"
"v_mfma_f32_16x16x32_f16 v[56:59], v[196:199], v[44:47], v[56:59]\n"
"ds_read_b128 v[44:47], v25 offset:2176\n"
"v_mul_f32 v51, v36, v51\n"
"v_cvt_pk_bf16_f32 v48, v48, v49\n"
"s_waitcnt lgkmcnt(8)\n"
"v_mfma_f32_16x16x32_f16 v[56:59], v[164:167], v[88:91], v[56:59]\n"
"ds_read_b128 v[88:91], v25 offset:2688\n"
"v_cvt_pk_bf16_f32 v49, v50, v51\n"
"v_pk_min_u16 v48, v48, s36\n"
"v_pk_min_u16 v49, v49, s36\n"
"v_mfma_f32_16x16x32_f16 v[56:59], v[132:135], v[92:95], v[56:59]\n"
"ds_read_b128 v[92:95], v25 offset:3200\n"
"ds_write_b64 v26, v[48:49] offset:0\n"
"v_mfma_f32_16x16x32_f16 v[56:59], v[200:203], v[96:99], v[56:59]\n"
"ds_read_b128 v[96:99], v25 offset:2112\n"
"v_mul_f32 v52, v36, v52\n"
"v_mul_f32 v53, v36, v53\n"
"v_mul_f32 v54, v36, v54\n"
"v_mfma_f32_16x16x32_f16 v[56:59], v[168:171], v[100:103], v[56:59]\n"
"ds_read_b128 v[100:103], v25 offset:2624\n"
"v_mul_f32 v55, v36, v55\n"
"v_cvt_pk_bf16_f32 v52, v52, v53\n"
"s_waitcnt lgkmcnt(9)\n"
"v_mfma_f32_16x16x32_f16 v[56:59], v[136:139], v[104:107], v[56:59]\n"
"ds_read_b128 v[104:107], v25 offset:3136\n"
"v_cvt_pk_bf16_f32 v53, v54, v55\n"
"v_pk_min_u16 v52, v52, s36\n"
"v_pk_min_u16 v53, v53, s36\n"
"v_mfma_f32_16x16x32_f16 v[56:59], v[204:207], v[108:111], v[56:59]\n"
"ds_read_b128 v[108:111], v25 offset:2048\n"
"ds_write_b64 v26, v[52:53] offset:512\n"
"v_mfma_f32_16x16x32_f16 v[56:59], v[172:175], v[112:115], v[56:59]\n"
"ds_read_b128 v[112:115], v25 offset:2560\n"
"ds_write_b32 v4, v4 offset:592\n"
"v_mfma_f32_16x16x32_f16 v[56:59], v[140:143], v[116:119], v[56:59]\n"
"ds_read_b128 v[116:119], v25 offset:3072\n"
"s_waitcnt lgkmcnt(11)\n"
"v_mfma_f32_16x16x32_f16 v[56:59], v[208:211], v[120:123], v[56:59]\n"
"ds_read_b128 v[120:123], v25 offset:2496\n"
"v_mfma_f32_16x16x32_f16 v[56:59], v[176:179], v[124:127], v[56:59]\n"
"ds_read_b128 v[124:127], v25 offset:3008\n"
"v_mfma_f32_16x16x32_f16 v[56:59], v[144:147], v[40:43], v[56:59]\n"
"ds_read_b128 v[40:43], v25 offset:3520\n"
"v_mfma_f32_16x16x32_f16 v[56:59], v[212:215], v[44:47], v[56:59]\n"
"ds_read_b128 v[44:47], v25 offset:4032\n"
"s_waitcnt lgkmcnt(10)\n"
"v_mfma_f32_16x16x32_f16 v[56:59], v[180:183], v[88:91], v[56:59]\n"
"ds_read_b128 v[88:91], v25 offset:2432\n"
"v_mfma_f32_16x16x32_f16 v[56:59], v[148:151], v[92:95], v[56:59]\n"
"ds_read_b128 v[92:95], v25 offset:2944\n"
"v_mfma_f32_16x16x32_f16 v[56:59], v[216:219], v[96:99], v[56:59]\n"
"ds_read_b128 v[96:99], v25 offset:3456\n"
"v_mfma_f32_16x16x32_f16 v[56:59], v[184:187], v[100:103], v[56:59]\n"
"ds_read_b128 v[100:103], v25 offset:3968\n"
"s_waitcnt lgkmcnt(8)\n"
"v_mfma_f32_16x16x32_f16 v[56:59], v[152:155], v[104:107], v[56:59]\n"
"ds_read_b128 v[104:107], v25 offset:2368\n"
"v_mfma_f32_16x16x32_f16 v[56:59], v[220:223], v[108:111], v[56:59]\n"
"ds_read_b128 v[108:111], v25 offset:2880\n"
"v_mfma_f32_16x16x32_f16 v[56:59], v[188:191], v[112:115], v[56:59]\n"
"ds_read_b128 v[112:115], v25 offset:3392\n"
"v_mfma_f32_16x16x32_f16 v[56:59], v[156:159], v[116:119], v[56:59]\n"
"ds_read_b128 v[116:119], v25 offset:3904\n"
"s_waitcnt lgkmcnt(8)\n"
"v_mfma_f32_16x16x32_f16 v[60:63], v[224:227], v[120:123], v[60:63]\n"
"ds_read_b128 v[120:123], v25 offset:2304\n"
"v_mfma_f32_16x16x32_f16 v[60:63], v[192:195], v[124:127], v[60:63]\n"
"ds_read_b128 v[124:127], v25 offset:2816\n"
"v_mfma_f32_16x16x32_f16 v[60:63], v[160:163], v[40:43], v[60:63]\n"
"ds_read_b128 v[40:43], v25 offset:3328\n"
"v_mul_f32 v56, v36, v56\n"
"v_mul_f32 v57, v36, v57\n"
"v_mul_f32 v58, v36, v58\n"
"v_mul_f32 v59, v36, v59\n"
"v_mfma_f32_16x16x32_f16 v[60:63], v[128:131], v[44:47], v[60:63]\n"
"ds_read_b128 v[44:47], v25 offset:3840\n"
"v_cvt_pk_bf16_f32 v56, v56, v57\n"
"v_cvt_pk_bf16_f32 v57, v58, v59\n"
"v_pk_min_u16 v56, v56, s36\n"
"s_waitcnt lgkmcnt(8)\n"
"v_mfma_f32_16x16x32_f16 v[60:63], v[228:231], v[88:91], v[60:63]\n"
"ds_read_b128 v[88:91], v25 offset:2240\n"
"v_pk_min_u16 v57, v57, s36\n"
"ds_write_b64 v26, v[56:57] offset:1024\n"
"v_mfma_f32_16x16x32_f16 v[60:63], v[196:199], v[92:95], v[60:63]\n"
"ds_read_b128 v[92:95], v25 offset:2752\n"
"ds_write_b32 v4, v4 offset:576\n"
"v_mfma_f32_16x16x32_f16 v[60:63], v[164:167], v[96:99], v[60:63]\n"
"ds_read_b128 v[96:99], v25 offset:3264\n"
"v_mfma_f32_16x16x32_f16 v[60:63], v[132:135], v[100:103], v[60:63]\n"
"ds_read_b128 v[100:103], v25 offset:3776\n"
"s_waitcnt lgkmcnt(10)\n"
"v_mfma_f32_16x16x32_f16 v[60:63], v[232:235], v[104:107], v[60:63]\n"
"ds_read_b128 v[104:107], v25 offset:2176\n"
"v_mfma_f32_16x16x32_f16 v[60:63], v[200:203], v[108:111], v[60:63]\n"
"ds_read_b128 v[108:111], v25 offset:2688\n"
"v_mfma_f32_16x16x32_f16 v[60:63], v[168:171], v[112:115], v[60:63]\n"
"ds_read_b128 v[112:115], v25 offset:3200\n"
"v_mfma_f32_16x16x32_f16 v[60:63], v[136:139], v[116:119], v[60:63]\n"
"ds_read_b128 v[116:119], v25 offset:3712\n"
"s_waitcnt lgkmcnt(10)\n"
"v_mfma_f32_16x16x32_f16 v[60:63], v[236:239], v[120:123], v[60:63]\n"
"ds_read_b128 v[120:123], v25 offset:2112\n"
"v_mfma_f32_16x16x32_f16 v[60:63], v[204:207], v[124:127], v[60:63]\n"
"ds_read_b128 v[124:127], v25 offset:2624\n"
"v_mfma_f32_16x16x32_f16 v[60:63], v[172:175], v[40:43], v[60:63]\n"
"ds_read_b128 v[40:43], v25 offset:3136\n"
"v_mfma_f32_16x16x32_f16 v[60:63], v[140:143], v[44:47], v[60:63]\n"
"ds_read_b128 v[44:47], v25 offset:3648\n"
"s_waitcnt lgkmcnt(8)\n"
"v_mfma_f32_16x16x32_f16 v[60:63], v[240:243], v[88:91], v[60:63]\n"
"ds_read_b128 v[88:91], v25 offset:2048\n"
"v_mfma_f32_16x16x32_f16 v[60:63], v[208:211], v[92:95], v[60:63]\n"
"ds_read_b128 v[92:95], v25 offset:2560\n"
"v_mfma_f32_16x16x32_f16 v[60:63], v[176:179], v[96:99], v[60:63]\n"
"ds_read_b128 v[96:99], v25 offset:3072\n"
"v_mfma_f32_16x16x32_f16 v[60:63], v[144:147], v[100:103], v[60:63]\n"
"ds_read_b128 v[100:103], v25 offset:3584\n"
"s_waitcnt lgkmcnt(8)\n"
"v_mfma_f32_16x16x32_f16 v[60:63], v[244:247], v[104:107], v[60:63]\n"
"v_mfma_f32_16x16x32_f16 v[60:63], v[212:215], v[108:111], v[60:63]\n"
"v_mfma_f32_16x16x32_f16 v[60:63], v[180:183], v[112:115], v[60:63]\n"
"v_mfma_f32_16x16x32_f16 v[60:63], v[148:151], v[116:119], v[60:63]\n"
"s_waitcnt lgkmcnt(4)\n"
"v_mfma_f32_16x16x32_f16 v[60:63], v[248:251], v[120:123], v[60:63]\n"
"v_mfma_f32_16x16x32_f16 v[60:63], v[216:219], v[124:127], v[60:63]\n"
"v_mfma_f32_16x16x32_f16 v[60:63], v[184:187], v[40:43], v[60:63]\n"
"v_mfma_f32_16x16x32_f16 v[60:63], v[152:155], v[44:47], v[60:63]\n"
"s_waitcnt lgkmcnt(0)\n"
"v_mfma_f32_16x16x32_f16 v[60:63], v[252:255], v[88:91], v[60:63]\n"
"v_mfma_f32_16x16x32_f16 v[60:63], v[220:223], v[92:95], v[60:63]\n"
"v_mfma_f32_16x16x32_f16 v[60:63], v[188:191], v[96:99], v[60:63]\n"
"v_mfma_f32_16x16x32_f16 v[60:63], v[156:159], v[100:103], v[60:63]\n"
"s_nop 7\n"
"v_mul_f32 v60, v36, v60\n"
"v_mul_f32 v61, v36, v61\n"
"v_mul_f32 v62, v36, v62\n"
"v_mul_f32 v63, v36, v63\n"
"v_cvt_pk_bf16_f32 v60, v60, v61\n"
"v_cvt_pk_bf16_f32 v61, v62, v63\n"
"v_pk_min_u16 v60, v60, s36\n"
"v_pk_min_u16 v61, v61, s36\n"
"ds_write_b64 v26, v[60:61] offset:1536\n"
"ds_write_b32 v4, v4 offset:560\n"
"s_nop 2\n"
".Lnc_587:\n"
"s_branch .L_yend\n"
".Lsl_320:\n"
"s_nop 7\n"
"s_mov_b32 s17, 32768\n"
".Ltp_322:\n"
"buffer_load_dwordx4 v[44:47], v38, s[4:7], s81 offen sc1\n"
"s_sub_u32 s17, s17, 1\n"
"s_cbranch_scc1 .Lou_323\n"
"s_waitcnt vmcnt(0)\n"
"v_xor_b32 v44, s16, v44\n"
"v_xor_b32 v45, s16, v45\n"
"v_xor_b32 v46, s16, v46\n"
"v_xor_b32 v47, s16, v47\n"
"v_or_b32 v44, v44, v45\n"
"v_or3_b32 v44, v44, v46, v47\n"
"v_cmp_ne_u32 vcc, 0, v44\n"
"s_cbranch_vccz .Lou_323\n"
"s_sleep 16\n"
"s_branch .Ltp_322\n"
".Lou_323:\n"
"s_waitcnt vmcnt(0)\n"
"s_branch .Lbk_321\n"
".Lst_332:\n"
"s_nop 7\n"
"s_lshl_b32 s79, s3, 2\n"
"s_add_u32 s79, s79, 0x26800\n"
"v_mov_b32 v36, s79\n"
"s_cmp_lg_u32 s40, 0\n"
"s_cbranch_scc1 .Lhv_335\n"
"s_call_b64 s[60:61], .Lkg_324\n"
"s_setprio 0\n"
".Lhv_335:\n"
"v_and_b32 v73, 15, v0\n"
"v_lshlrev_b32 v74, 2, v0\n"
"v_lshrrev_b32 v75, 4, v0\n"
"s_mul_i32 s79, s3, 0x2100\n"
"s_add_u32 s72, s79, 0x15100\n"
"v_lshlrev_b32 v76, 3, v73\n"
"v_lshlrev_b32 v77, 4, v75\n"
"v_lshlrev_b32 v78, 8, v75\n"
"s_add_u32 s73, s72, 0x400\n"
"s_sub_u32 s74, s72, 0x1100\n"
"s_add_u32 s79, s72, 0x800\n"
"v_add3_u32 v79, v78, v76, s79\n"
"v_lshl_add_u32 v99, v73, 6, v77\n"
"v_add_u32 v80, s72, v99\n"
"v_lshl_add_u32 v99, v73, 5, v77\n"
"v_lshl_add_u32 v98, v73, 4, v78\n"
"s_mov_b32 vcc_lo, -1\n"
"s_mov_b32 vcc_hi, 0\n"
"v_cndmask_b32 v99, v98, v99, vcc\n"
"v_add_u32 v81, s73, v99\n"
"s_mov_b32 vcc_lo, -1\n"
"s_mov_b32 vcc_hi, 1\n"
"v_cndmask_b32 v82, 2.0, v79, vcc\n"
"s_mov_b32 vcc_lo, -1\n"
"s_mov_b32 vcc_hi, 0\n"
"v_cndmask_b32 v83, 2.0, v79, vcc\n"
"s_add_u32 s79, s72, 0x7ec\n"
"s_add_u32 s79, s79, 0x2c0\n"
"v_lshlrev_b32 v99, 5, v75\n"
"v_sub_u32 v99, v76, v99\n"
"v_add_u32 v84, s79, v99\n"
"s_add_u32 s79, s72, 0xa40\n"
"v_add_u32 v99, s79, v74\n"
"s_mov_b32 vcc_lo, -1\n"
"s_mov_b32 vcc_hi, 1\n"
"v_cndmask_b32 v100, 2.0, v99, vcc\n"
"v_sub_u32 v99, 0x420, v0\n"
"v_sub_u32 v98, 0x41f, v0\n"
"v_bfe_u32 v98, v98, 2, 1\n"
"v_add_u32 v99, v99, v98\n"
"v_and_b32 v99, 0xffc, v99\n"
"v_min_i32 v99, 0x400, v99\n"
"v_mov_b32 v98, 0x640\n"
"s_mov_b32 vcc_lo, 0xf8000000\n"
"s_mov_b32 vcc_hi, -1\n"
"v_cndmask_b32 v99, v98, v99, vcc\n"
"v_add_u32 v101, s73, v99\n"
"v_add_u32 v102, s74, v74\n"
"v_lshlrev_b32 v99, 3, v75\n"
"v_sub_u32 v99, v99, v73\n"
"s_add_u32 s79, s74, 60\n"
"v_lshl_add_u32 v103, v99, 2, s79\n"
"v_mov_b32 v99, 0x3f800000\n"
"s_mov_b32 vcc_lo, 0x7f00000\n"
"s_mov_b32 vcc_hi, 0\n"
"v_cndmask_b32 v104, 0, v99, vcc\n"
"v_mov_b32 v99, 0x3f800000\n"
"s_mov_b32 vcc_lo, 0xff80000\n"
"s_mov_b32 vcc_hi, 0\n"
"v_cndmask_b32 v105, 0, v99, vcc\n"
"v_mov_b32 v106, 0\n"
"v_cmp_eq_u32 vcc, 0, v0\n"
"v_lshlrev_b32 v140, 16, v140\n"
"v_mul_f32 v140, 0.5, v140\n"
"v_cndmask_b32 v140, 0, v140, vcc\n"
"v_add_f32 v128, v128, v140\n"
"v_cndmask_b32 v136, v136, 0, s[56:57]\n"
"v_cndmask_b32 v137, v137, 0, s[58:59]\n"
"v_cndmask_b32 v138, v138, 0, s[58:59]\n"
"v_cndmask_b32 v139, v139, 0, s[58:59]\n"
"v_max3_f32 v85, |v128|, |v129|, 0\n"
"v_max3_f32 v85, |v130|, |v131|, v85\n"
"v_max3_f32 v85, |v132|, |v133|, v85\n"
"v_max3_f32 v85, |v134|, |v135|, v85\n"
"v_max3_f32 v85, |v136|, |v137|, v85\n"
"v_max3_f32 v85, |v138|, |v139|, v85\n"
"s_nop 1\n"
"v_max_u32_dpp v85, v85, v85 row_ror:8 row_mask:0xf bank_mask:0xf\n"
"s_nop 1\n"
"v_max_u32_dpp v85, v85, v85 row_ror:4 row_mask:0xf bank_mask:0xf\n"
"s_nop 1\n"
"v_max_u32_dpp v85, v85, v85 row_ror:2 row_mask:0xf bank_mask:0xf\n"
"s_nop 1\n"
"v_max_u32_dpp v85, v85, v85 row_ror:1 row_mask:0xf bank_mask:0xf\n"
"v_mov_b32 v86, v85\n"
"s_nop 1\n"
"v_permlane16_swap_b32 v85, v86\n"
"v_max_u32 v85, v85, v86\n"
"v_mov_b32 v86, v85\n"
"s_nop 1\n"
"v_permlane32_swap_b32 v85, v86\n"
"v_max_u32 v85, v85, v86\n"
"s_nop 0\n"
"v_readfirstlane_b32 s51, v85\n"
"s_nop 2\n"
"s_bfe_u32 s50, s51, 0x80017\n"
"s_max_i32 s50, s50, 77\n"
"s_min_i32 s50, s50, 227\n"
"s_cmp_gt_i32 s51, 0\n"
"s_cselect_b32 s50, s50, 127\n"
"s_sub_u32 s51, 268, s50\n"
"s_lshl_b32 s51, s51, 23\n"
"s_sub_u32 s50, s50, 13\n"
"s_lshl_b32 s50, s50, 23\n"
"v_mul_f32 v128, s51, v128\n"
"v_mul_f32 v129, s51, v129\n"
"v_mul_f32 v130, s51, v130\n"
"v_mul_f32 v131, s51, v131\n"
"v_mul_f32 v132, s51, v132\n"
"v_mul_f32 v133, s51, v133\n"
"v_mul_f32 v134, s51, v134\n"
"v_mul_f32 v135, s51, v135\n"
"v_mul_f32 v136, s51, v136\n"
"v_mul_f32 v137, s51, v137\n"
"v_mul_f32 v138, s51, v138\n"
"v_mul_f32 v139, s51, v139\n"
"ds_write_b128 v80, v[128:131]\n"
"ds_write_b128 v81, v[132:135]\n"
"ds_write_b64 v82, v[136:137]\n"
"ds_write_b64 v83, v[138:139] offset:128\n"
"v_mov_b32 v87, s50\n"
"ds_write_b32 v36, v87\n"
"ds_write_b64 v82, v[136:137] offset:704\n"
"ds_write_b64 v83, v[138:139] offset:832\n"
"ds_write_b32 v100, v106\n"
"ds_read_b32 v101, v101\n"
"ds_write2st64_b32 v102, v104, v105 offset0:1 offset1:2\n"
"ds_read2_b32 v[48:49], v84 offset0:6 offset1:4\n"
"ds_read2_b32 v[50:51], v84 offset0:2 offset1:0\n"
"ds_read2_b32 v[52:53], v84 offset0:7 offset1:5\n"
"ds_read2_b32 v[54:55], v84 offset0:3 offset1:1\n"
"ds_read2_b32 v[56:57], v84 offset0:38 offset1:36\n"
"ds_read2_b32 v[58:59], v84 offset0:34 offset1:32\n"
"ds_read2_b32 v[60:61], v84 offset0:39 offset1:37\n"
"ds_read2_b32 v[62:63], v84 offset0:35 offset1:33\n"
"s_waitcnt lgkmcnt(9)\n"
"ds_write_b32 v102, v101\n"
"ds_read2_b32 v[64:65], v84 offset0:70 offset1:68\n"
"ds_read2_b32 v[66:67], v84 offset0:66 offset1:64\n"
"ds_read2_b32 v[68:69], v84 offset0:71 offset1:69\n"
"ds_read2_b32 v[70:71], v84 offset0:67 offset1:65\n"
"ds_read2_b32 v[72:73], v84 offset0:102 offset1:100\n"
"ds_read2_b32 v[74:75], v84 offset0:98 offset1:96\n"
"ds_read2_b32 v[76:77], v84 offset0:103 offset1:101\n"
"ds_read2_b32 v[78:79], v84 offset0:99 offset1:97\n"
"ds_read2_b32 v[88:89], v103 offset0:0 offset1:1\n"
"s_waitcnt lgkmcnt(15)\n"
"v_cvt_pk_f16_f32 v128, v48, v49\n"
"ds_read2_b32 v[90:91], v103 offset0:2 offset1:3\n"
"v_cvt_pk_f16_f32 v129, v50, v51\n"
"ds_read2_b32 v[92:93], v103 offset0:4 offset1:5\n"
"v_cvt_pk_f16_f32 v130, v52, v53\n"
"ds_read2_b32 v[94:95], v103 offset0:6 offset1:7\n"
"s_waitcnt lgkmcnt(15)\n"
"v_cvt_pk_f16_f32 v131, v54, v55\n"
"ds_read2_b32 v[96:97], v103 offset0:64 offset1:65\n"
"v_cvt_pk_f16_f32 v132, v56, v57\n"
"ds_read2_b32 v[98:99], v103 offset0:66 offset1:67\n"
"v_cvt_pk_f16_f32 v133, v58, v59\n"
"ds_read2_b32 v[108:109], v103 offset0:68 offset1:69\n"
"s_waitcnt lgkmcnt(15)\n"
"v_cvt_pk_f16_f32 v134, v60, v61\n"
"ds_read2_b32 v[110:111], v103 offset0:70 offset1:71\n"
"v_cvt_pk_f16_f32 v135, v62, v63\n"
"ds_read2_b32 v[112:113], v103 offset0:128 offset1:129\n"
"s_waitcnt lgkmcnt(15)\n"
"v_cvt_pk_f16_f32 v136, v64, v65\n"
"ds_read2_b32 v[114:115], v103 offset0:130 offset1:131\n"
"v_cvt_pk_f16_f32 v137, v66, v67\n"
"ds_read2_b32 v[116:117], v103 offset0:132 offset1:133\n"
"s_waitcnt lgkmcnt(15)\n"
"v_cvt_pk_f16_f32 v138, v68, v69\n"
"ds_read2_b32 v[118:119], v103 offset0:134 offset1:135\n"
"v_cvt_pk_f16_f32 v139, v70, v71\n"
"s_waitcnt lgkmcnt(15)\n"
"v_cvt_pk_f16_f32 v140, v72, v73\n"
"s_waitcnt lgkmcnt(14)\n"
"v_cvt_pk_f16_f32 v141, v74, v75\n"
"s_waitcnt lgkmcnt(13)\n"
"v_cvt_pk_f16_f32 v142, v76, v77\n"
"s_waitcnt lgkmcnt(12)\n"
"v_cvt_pk_f16_f32 v143, v78, v79\n"
"s_waitcnt lgkmcnt(11)\n"
"v_cvt_pk_f16_f32 v152, v88, v89\n"
"s_waitcnt lgkmcnt(10)\n"
"v_cvt_pk_f16_f32 v153, v90, v91\n"
"s_waitcnt lgkmcnt(9)\n"
"v_cvt_pk_f16_f32 v154, v92, v93\n"
"s_waitcnt lgkmcnt(8)\n"
"v_cvt_pk_f16_f32 v155, v94, v95\n"
"s_waitcnt lgkmcnt(7)\n"
"v_cvt_pk_f16_f32 v144, v96, v97\n"
"s_waitcnt lgkmcnt(6)\n"
"v_cvt_pk_f16_f32 v145, v98, v99\n"
"s_waitcnt lgkmcnt(5)\n"
"v_cvt_pk_f16_f32 v146, v108, v109\n"
"s_waitcnt lgkmcnt(4)\n"
"v_cvt_pk_f16_f32 v147, v110, v111\n"
"s_waitcnt lgkmcnt(3)\n"
"v_cvt_pk_f16_f32 v148, v112, v113\n"
"s_waitcnt lgkmcnt(2)\n"
"v_cvt_pk_f16_f32 v149, v114, v115\n"
"s_waitcnt lgkmcnt(1)\n"
"v_cvt_pk_f16_f32 v150, v116, v117\n"
"s_waitcnt lgkmcnt(0)\n"
"v_cvt_pk_f16_f32 v151, v118, v119\n"
"ds_write_b32 v4, v4 offset:192\n"
"s_mov_b32 s43, 1\n"
"s_nop 2\n"
"s_setpc_b64 s[46:47]\n"
".Lst_333:\n"
"s_nop 7\n"
"s_mov_b32 s43, 2\n"
"s_nop 0\n"
"s_setpc_b64 s[46:47]\n"
".Lst_334:\n"
"s_nop 7\n"
"v_and_b32 v72, 15, v0\n"
"v_lshrrev_b32 v73, 4, v0\n"
"v_mov_b32 v99, 0xffff0000\n"
"v_mov_b32 v98, 0xffff\n"
"s_mov_b32 vcc_lo, 0xaaaaaaaa\n"
"s_mov_b32 vcc_hi, 0xaaaaaaaa\n"
"v_cndmask_b32 v74, v98, v99, vcc\n"
"v_lshlrev_b32 v75, 5, v72\n"
"v_lshrrev_b32 v76, 2, v72\n"
"s_lshl_b32 s79, s3, 10\n"
"v_lshl_add_u32 v30, v0, 4, s79\n"
"v_lshrrev_b32 v77, 2, v30\n"
"v_lshrrev_b32 v78, 6, v30\n"
"v_lshlrev_b32 v99, 3, v73\n"
"v_sub_u32 v99, v99, v72\n"
"v_add_u32 v79, 0x3ef, v99\n"
"v_and_b32 v99, 24, v78\n"
"v_xor_b32 v99, v77, v99\n"
"v_add_u32 v32, 0x20c00, v99\n"
"s_lshl_b32 s79, s3, 8\n"
"s_add_u32 s79, s79, 0x20c00\n"
"v_xor_b32 v99, s3, v72\n"
"v_and_b32 v99, 1, v99\n"
"v_lshrrev_b32 v98, 1, v73\n"
"v_xor_b32 v98, v76, v98\n"
"v_and_b32 v98, 1, v98\n"
"v_lshl_add_u32 v99, v99, 1, v98\n"
"v_and_b32 v98, 2, v72\n"
"v_lshrrev_b32 v97, 3, v72\n"
"v_lshlrev_b32 v97, 7, v97\n"
"v_lshl_add_u32 v97, v76, 11, v97\n"
"v_lshl_add_u32 v97, v73, 6, v97\n"
"v_lshl_add_u32 v98, v98, 4, v97\n"
"v_lshl_add_u32 v99, v99, 3, v98\n"
"v_add_u32 v27, s79, v99\n"
"s_mul_i32 s79, s3, 0x2100\n"
"s_add_u32 s79, s79, 0x13840\n"
"v_lshrrev_b32 v99, 2, v79\n"
"v_and_b32 v98, 3, v79\n"
"v_mul_i32_i24 v98, 0x840, v98\n"
"v_lshl_add_u32 v99, v99, 3, v98\n"
"v_add_u32 v24, s79, v99\n"
"s_mul_i32 s79, s3, 0x1200\n"
"s_add_u32 s79, s79, 0x1c420\n"
"v_lshl_add_u32 v99, v73, 4, v75\n"
"v_add_u32 v25, s79, v99\n"
"s_lshl_b32 s79, s3, 11\n"
"s_lshr_b32 s78, s3, 1\n"
"s_lshl_b32 s78, s78, 7\n"
"s_add_u32 s79, s79, s78\n"
"s_add_u32 s79, s79, 0x20c00\n"
"s_and_b32 s78, s3, 1\n"
"v_xor_b32 v99, v73, v76\n"
"v_xor_b32 v99, s78, v99\n"
"v_and_b32 v99, 3, v99\n"
"v_lshl_add_u32 v99, v99, 3, v75\n"
"v_add_u32 v26, s79, v99\n"
"v_xor_b32 v28, 8, v27\n"
"v_lshrrev_b32 v99, 7, v30\n"
"v_and_b32 v98, 0x70, v30\n"
"s_mov_b32 s79, 0x5000\n"
"v_mad_i32_i24 v29, v99, s79, v98\n"
"v_add_u32 v31, 0x1ce00, v77\n"
"v_xor_b32 v33, 8, v32\n"
"s_mov_b32 vcc_lo, 0x300003\n"
"s_mov_b32 vcc_hi, 0x30000300\n"
"v_cndmask_b32 v34, 0, v74, vcc\n"
"s_mov_b32 vcc_lo, 0xc0000c\n"
"s_mov_b32 vcc_hi, 0xc0000c00\n"
"v_cndmask_b32 v35, 0, v74, vcc\n"
"s_lshl_b32 s79, s3, 2\n"
"s_add_u32 s79, s79, 0x26800\n"
"v_mov_b32 v36, s79\n"
"v_and_b32 v99, 48, v30\n"
"v_mul_i32_i24 v99, 0x50, v99\n"
"s_mov_b32 s79, 0x2800\n"
"v_mad_i32_i24 v37, v78, s79, v99\n"
"ds_read_b32 v36, v36\n"
"s_waitcnt lgkmcnt(0)\n"
"v_sub_u32 v76, 0x7f000000, v36\n"
"v_lshrrev_b32 v77, 16, v76\n"
"v_or_b32 v76, v76, v77\n"
"v_and_b32 v34, v76, v34\n"
"v_and_b32 v35, v76, v35\n"
"s_mov_b32 s43, 3\n"
"s_nop 3\n"
"s_setpc_b64 s[46:47]\n"
".Loa_336:\n"
"s_nop 7\n"
"s_mov_b32 s17, 32768\n"
".Ltp_337:\n"
"s_waitcnt vmcnt(0)\n"
"s_sub_u32 s17, s17, 1\n"
"s_cbranch_scc1 .Lou_338\n"
"v_max_u32 v38, v88, v89\n"
"v_max3_u32 v38, v90, v91, v38\n"
"v_max3_u32 v38, v92, v93, v38\n"
"v_max3_u32 v38, v94, v95, v38\n"
"v_max3_u32 v38, v96, v97, v38\n"
"v_max3_u32 v38, v98, v99, v38\n"
"v_max3_u32 v38, v100, v101, v38\n"
"v_max3_u32 v38, v102, v103, v38\n"
"v_max3_u32 v38, v104, v105, v38\n"
"v_max3_u32 v38, v106, v107, v38\n"
"v_max3_u32 v38, v108, v109, v38\n"
"v_max3_u32 v38, v110, v111, v38\n"
"v_max3_u32 v38, v112, v113, v38\n"
"v_max3_u32 v38, v114, v115, v38\n"
"v_max3_u32 v38, v116, v117, v38\n"
"v_max3_u32 v38, v118, v119, v38\n"
"v_max3_u32 v38, v120, v121, v38\n"
"v_max3_u32 v38, v122, v123, v38\n"
"v_max3_u32 v38, v124, v125, v38\n"
"v_max3_u32 v38, v126, v127, v38\n"
"v_cmp_eq_u32 vcc, -1, v38\n"
"s_cbranch_vccz .Lou_338\n"
"s_sleep 1\n"
"v_max_u32 v38, v90, v91\n"
"v_max3_u32 v38, v88, v89, v38\n"
"v_cmp_eq_u32 vcc, -1, v38\n"
"s_cbranch_vccz .Lsk_339\n"
"s_mov_b64 exec, vcc\n"
"buffer_load_dwordx4 v[88:91], v2, s[4:7], s32 offen offset:0 sc1\n"
"s_mov_b64 exec, -1\n"
".Lsk_339:\n"
"v_max_u32 v38, v94, v95\n"
"v_max3_u32 v38, v92, v93, v38\n"
"v_cmp_eq_u32 vcc, -1, v38\n"
"s_cbranch_vccz .Lsk_340\n"
"s_mov_b64 exec, vcc\n"
"buffer_load_dwordx4 v[92:95], v2, s[4:7], s32 offen offset:1024 sc1\n"
"s_mov_b64 exec, -1\n"
".Lsk_340:\n"
"v_max_u32 v38, v98, v99\n"
"v_max3_u32 v38, v96, v97, v38\n"
"v_cmp_eq_u32 vcc, -1, v38\n"
"s_cbranch_vccz .Lsk_341\n"
"s_mov_b64 exec, vcc\n"
"buffer_load_dwordx4 v[96:99], v2, s[4:7], s32 offen offset:2048 sc1\n"
"s_mov_b64 exec, -1\n"
".Lsk_341:\n"
"v_max_u32 v38, v102, v103\n"
"v_max3_u32 v38, v100, v101, v38\n"
"v_cmp_eq_u32 vcc, -1, v38\n"
"s_cbranch_vccz .Lsk_342\n"
"s_mov_b64 exec, vcc\n"
"buffer_load_dwordx4 v[100:103], v2, s[4:7], s32 offen offset:3072 sc1\n"
"s_mov_b64 exec, -1\n"
".Lsk_342:\n"
"v_max_u32 v38, v106, v107\n"
"v_max3_u32 v38, v104, v105, v38\n"
"v_cmp_eq_u32 vcc, -1, v38\n"
"s_cbranch_vccz .Lsk_343\n"
"s_mov_b64 exec, vcc\n"
"buffer_load_dwordx4 v[104:107], v2, s[4:7], s33 offen offset:0 sc1\n"
"s_mov_b64 exec, -1\n"
".Lsk_343:\n"
"v_max_u32 v38, v110, v111\n"
"v_max3_u32 v38, v108, v109, v38\n"
"v_cmp_eq_u32 vcc, -1, v38\n"
"s_cbranch_vccz .Lsk_344\n"
"s_mov_b64 exec, vcc\n"
"buffer_load_dwordx4 v[108:111], v2, s[4:7], s34 offen offset:0 sc1\n"
"s_mov_b64 exec, -1\n"
".Lsk_344:\n"
"v_max_u32 v38, v114, v115\n"
"v_max3_u32 v38, v112, v113, v38\n"
"v_cmp_eq_u32 vcc, -1, v38\n"
"s_cbranch_vccz .Lsk_345\n"
"s_mov_b64 exec, vcc\n"
"buffer_load_dwordx4 v[112:115], v2, s[4:7], s34 offen offset:1024 sc1\n"
"s_mov_b64 exec, -1\n"
".Lsk_345:\n"
"v_max_u32 v38, v118, v119\n"
"v_max3_u32 v38, v116, v117, v38\n"
"v_cmp_eq_u32 vcc, -1, v38\n"
"s_cbranch_vccz .Lsk_346\n"
"s_mov_b64 exec, vcc\n"
"buffer_load_dwordx4 v[116:119], v2, s[4:7], s34 offen offset:2048 sc1\n"
"s_mov_b64 exec, -1\n"
".Lsk_346:\n"
"v_max_u32 v38, v122, v123\n"
"v_max3_u32 v38, v120, v121, v38\n"
"v_cmp_eq_u32 vcc, -1, v38\n"
"s_cbranch_vccz .Lsk_347\n"
"s_mov_b64 exec, vcc\n"
"buffer_load_dwordx4 v[120:123], v2, s[4:7], s34 offen offset:3072 sc1\n"
"s_mov_b64 exec, -1\n"
".Lsk_347:\n"
"v_max_u32 v38, v126, v127\n"
"v_max3_u32 v38, v124, v125, v38\n"
"v_cmp_eq_u32 vcc, -1, v38\n"
"s_cbranch_vccz .Lsk_348\n"
"s_mov_b64 exec, vcc\n"
"buffer_load_dwordx4 v[124:127], v2, s[4:7], s35 offen offset:0 sc1\n"
"s_mov_b64 exec, -1\n"
".Lsk_348:\n"
"s_branch .Ltp_337\n"
".Lou_338:\n"
"s_waitcnt vmcnt(0)\n"
"s_setpc_b64 s[46:47]\n"
".Lsl_367:\n"
"s_nop 7\n"
"s_call_b64 s[46:47], .Loa_336\n"
"s_branch .Lbk_368\n"
".Lsl_369:\n"
"s_nop 7\n"
"s_call_b64 s[46:47], .Loa_336\n"
"s_branch .Lbk_370\n"
".Lsl_371:\n"
"s_nop 7\n"
"s_call_b64 s[46:47], .Loa_336\n"
"s_branch .Lbk_372\n"
".Lsl_373:\n"
"s_nop 7\n"
"s_call_b64 s[46:47], .Loa_336\n"
"s_branch .Lbk_374\n"
".Lsl_375:\n"
"s_nop 7\n"
"s_call_b64 s[46:47], .Loa_336\n"
"s_branch .Lbk_376\n"
".Lsl_377:\n"
"s_nop 7\n"
"s_call_b64 s[46:47], .Loa_336\n"
"s_branch .Lbk_378\n"
".Lsl_379:\n"
"s_nop 7\n"
"s_call_b64 s[46:47], .Loa_336\n"
"s_branch .Lbk_380\n"
".Lsl_381:\n"
"s_nop 7\n"
"s_call_b64 s[46:47], .Loa_336\n"
"s_branch .Lbk_382\n"
".Lsl_383:\n"
"s_nop 7\n"
"s_call_b64 s[46:47], .Loa_336\n"
"s_branch .Lbk_384\n"
".Lsl_385:\n"
"s_nop 7\n"
"s_call_b64 s[46:47], .Loa_336\n"
"s_branch .Lbk_386\n"
".Lsl_410:\n"
"s_nop 7\n"
"s_call_b64 s[46:47], .Loa_336\n"
"s_branch .Lbk_411\n"
".Lsl_412:\n"
"s_nop 7\n"
"s_call_b64 s[46:47], .Loa_336\n"
"s_branch .Lbk_413\n"
".Lsl_414:\n"
"s_nop 7\n"
"s_call_b64 s[46:47], .Loa_336\n"
"s_branch .Lbk_415\n"
".Lsl_416:\n"
"s_nop 7\n"
"s_call_b64 s[46:47], .Loa_336\n"
"s_branch .Lbk_417\n"
".Lsl_418:\n"
"s_nop 7\n"
"s_call_b64 s[46:47], .Loa_336\n"
"s_branch .Lbk_419\n"
".Lsl_420:\n"
"s_nop 7\n"
"s_call_b64 s[46:47], .Loa_336\n"
"s_branch .Lbk_421\n"
".Lsl_422:\n"
"s_nop 7\n"
"s_call_b64 s[46:47], .Loa_336\n"
"s_branch .Lbk_423\n"
".Lsl_424:\n"
"s_nop 7\n"
"s_call_b64 s[46:47], .Loa_336\n"
"s_branch .Lbk_425\n"
".Lsl_426:\n"
"s_nop 7\n"
"s_call_b64 s[46:47], .Loa_336\n"
"s_branch .Lbk_427\n"
".Lsl_428:\n"
"s_nop 7\n"
"s_call_b64 s[46:47], .Loa_336\n"
"s_branch .Lbk_429\n"
".Lsl_510:\n"
"s_nop 7\n"
"s_call_b64 s[46:47], .Loa_336\n"
"s_branch .Lbk_511\n"
".Lsl_512:\n"
"s_nop 7\n"
"s_call_b64 s[46:47], .Loa_336\n"
"s_branch .Lbk_513\n"
".Lsl_514:\n"
"s_nop 7\n"
"s_call_b64 s[46:47], .Loa_336\n"
"s_branch .Lbk_515\n"
".Lsl_516:\n"
"s_nop 7\n"
"s_call_b64 s[46:47], .Loa_336\n"
"s_branch .Lbk_517\n"
".Lsl_518:\n"
"s_nop 7\n"
"s_call_b64 s[46:47], .Loa_336\n"
"s_branch .Lbk_519\n"
".Lsl_520:\n"
"s_nop 7\n"
"s_call_b64 s[46:47], .Loa_336\n"
"s_branch .Lbk_521\n"
".Lsl_522:\n"
"s_nop 7\n"
"s_call_b64 s[46:47], .Loa_336\n"
"s_branch .Lbk_523\n"
".Lsl_524:\n"
"s_nop 7\n"
"s_call_b64 s[46:47], .Loa_336\n"
"s_branch .Lbk_525\n"
".Lsl_526:\n"
"s_nop 7\n"
"s_call_b64 s[46:47], .Loa_336\n"
"s_branch .Lbk_527\n"
".Lsl_528:\n"
"s_nop 7\n"
"s_call_b64 s[46:47], .Loa_336\n"
"s_branch .Lbk_529\n"
".Lsl_556:\n"
"s_nop 7\n"
"s_call_b64 s[46:47], .Loa_336\n"
"s_branch .Lbk_557\n"
".Lsl_558:\n"
"s_nop 7\n"
"s_call_b64 s[46:47], .Loa_336\n"
"s_branch .Lbk_559\n"
".Lsl_560:\n"
"s_nop 7\n"
"s_call_b64 s[46:47], .Loa_336\n"
"s_branch .Lbk_561\n"
".Lsl_562:\n"
"s_nop 7\n"
"s_call_b64 s[46:47], .Loa_336\n"
"s_branch .Lbk_563\n"
".Lsl_564:\n"
"s_nop 7\n"
"s_call_b64 s[46:47], .Loa_336\n"
"s_branch .Lbk_565\n"
".Lsl_566:\n"
"s_nop 7\n"
"s_call_b64 s[46:47], .Loa_336\n"
"s_branch .Lbk_567\n"
".Lsl_568:\n"
"s_nop 7\n"
"s_call_b64 s[46:47], .Loa_336\n"
"s_branch .Lbk_569\n"
".Lsl_570:\n"
"s_nop 7\n"
"s_call_b64 s[46:47], .Loa_336\n"
"s_branch .Lbk_571\n"
".Lsl_572:\n"
"s_nop 7\n"
"s_call_b64 s[46:47], .Loa_336\n"
"s_branch .Lbk_573\n"
".Lsl_574:\n"
"s_nop 7\n"
"s_call_b64 s[46:47], .Loa_336\n"
"s_branch .Lbk_575\n"
".L_yend:\n"
      :: "{s[0:1]}"(__builtin_amdgcn_kernarg_segment_ptr()), "{s2}"((int)__builtin_amdgcn_workgroup_id_x()),
         "{s3}"(__builtin_amdgcn_readfirstlane(tid >> 6)), "{v0}"(tid),
         "{v1}"((unsigned)(size_t)(__attribute__((address_space(3))) unsigned char*)smem)
      : "memory", "vcc", "scc", "m0", "v255", "s95");
}

extern "C" void kernel_launch(void* const* d_in, const int* in_sizes, int n_in,
                              void* d_out, int out_size, void* d_ws, size_t ws_size,
                              hipStream_t stream) {
  mega_kernel<<<256, 512, 0, stream>>>((const u16*)d_in[0], (const u16*)d_in[1], (const u16*)d_in[2],
                                       (const u16*)d_in[4], (const u16*)d_in[5], (const u16*)d_in[6],
                                       (const u16*)d_in[7], (const u16*)d_in[8], (const u16*)d_in[9],
                                       (u16*)d_out, (unsigned char*)d_ws);
}
```

```cpp
#include <hip/hip_runtime.h>
typedef unsigned short u16;

__global__ __launch_bounds__(512, 2) void mega_kernel(
    const u16* __restrict__ x_, const u16* __restrict__ wqkv_, const u16* __restrict__ wo_,
    const u16* __restrict__ log_A_real_, const u16* __restrict__ A_imag_,
    const u16* __restrict__ C_real_, const u16* __restrict__ C_imag_, const u16* __restrict__ log_dt_,
    const u16* __restrict__ D_skip, u16* __restrict__ out, unsigned char* __restrict__ ws_) {
  __shared__ __attribute__((aligned(16))) unsigned char smem[163840];
  const int tid = (int)__builtin_amdgcn_workitem_id_x();
  asm volatile(
"s_cmp_lt_u32 s3, 4\n"
"s_cbranch_scc1 .Lxs_1\n"
"s_load_dwordx8 s[8:15], s[0:1], 0x18\n"
"s_load_dwordx4 s[16:19], s[0:1], 0x38\n"
"s_load_dwordx2 s[22:23], s[0:1], 0x0\n"
"s_load_dwordx4 s[24:27], s[0:1], 0x48\n"
"s_getpc_b64 s[4:5]\n"
".Lmk_a:\n"
"v_lshrrev_b32 v79, 6, v0\n"
"v_and_b32 v78, 7, v0\n"
"v_lshl_add_u32 v79, v79, 3, v78\n"
"s_lshr_b32 s79, s2, 3\n"
"s_lshl_b32 s79, s79, 7\n"
"s_sub_u32 s79, s79, 0x20000\n"
"v_lshl_add_u32 v2, v79, 12, s79\n"
"v_min_u32 v2, .L_yend-.Lmk_a-4, v2\n"
"s_mov_b32 m0, 163328\n"
"v_mov_b32 v4, 0\n"
"global_load_lds_dword v2, s[4:5]\n"
"v_lshlrev_b32 v79, 2, v0\n"
"v_add_u32 v2, 0x26000, v79\n"
"ds_write_b32 v2, v4\n"
"v_lshlrev_b32 v79, 4, v0\n"
"v_add_u32 v2, 0x25840, v79\n"
"v_mov_b32 v8, -1\n"
"v_mov_b32 v9, -1\n"
"v_mov_b32 v10, -1\n"
"v_mov_b32 v11, -1\n"
"ds_write_b128 v2, v[8:11]\n"
"ds_write_b128 v2, v[8:11] offset:4096\n"
"s_and_b32 s79, s2, 7\n"
"s_lshl_b32 s79, s79, 4\n"
"s_lshr_b32 s78, s2, 3\n"
"s_add_u32 s79, s79, s78\n"
"s_and_b32 s78, s2, 3\n"
"s_lshl_b32 s78, s78, 3\n"
"s_and_b32 s77, s2, 4\n"
"s_cmp_eq_i32 s77, 4\n"
"s_cselect_b32 s78, 32, s78\n"
"s_add_u32 s79, s79, s78\n"
"s_lshl_b32 s79, s79, 2\n"
"s_and_b32 s78, s3, 3\n"
"s_add_u32 s6, s79, s78\n"
"v_and_b32 v79, 63, v0\n"
"s_mul_i32 s79, s6, 0x180\n"
"v_lshl_add_u32 v2, v79, 1, s79\n"
"s_lshl_b32 s79, s6, 1\n"
"s_and_b32 s20, s79, 0xffc\n"
"s_and_b32 s79, s2, 4\n"
"s_lshl_b32 s79, s79, 4\n"
"s_add_u32 s7, s2, s79\n"
"s_waitcnt lgkmcnt(0)\n"
"s_cmp_ge_u32 s7, 192\n"
"s_cbranch_scc1 .Lnl_2\n"
"s_load_dword s40, s[16:17], s20\n"
"global_load_ushort v32, v2, s[8:9] offset:0\n"
"global_load_ushort v33, v2, s[10:11] offset:0\n"
"global_load_ushort v34, v2, s[12:13] offset:0\n"
"global_load_ushort v35, v2, s[14:15] offset:0\n"
"global_load_ushort v40, v2, s[8:9] offset:128\n"
"global_load_ushort v41, v2, s[10:11] offset:128\n"
"global_load_ushort v42, v2, s[12:13] offset:128\n"
"global_load_ushort v43, v2, s[14:15] offset:128\n"
"global_load_ushort v44, v2, s[8:9] offset:256\n"
"global_load_ushort v45, v2, s[10:11] offset:256\n"
"global_load_ushort v46, v2, s[12:13] offset:256\n"
"global_load_ushort v47, v2, s[14:15] offset:256\n"
"s_load_dword s41, s[18:19], s20\n"
".Lnl_2:\n"
"s_load_dword s16, s[26:27], 0x0\n"
"s_barrier\n"
"s_branch .L_xend\n"
".Lxs_1:\n"
"s_load_dwordx4 s[20:23], s[0:1], 0x0\n"
"s_load_dwordx2 s[56:57], s[0:1], 0x10\n"
"s_load_dwordx4 s[24:27], s[0:1], 0x48\n"
"v_and_b32 v0, 63, v0\n"
"s_lshl_b32 s79, s2, 2\n"
"s_add_u32 s31, s79, s3\n"
"s_mul_i32 s79, s31, 0xcd\n"
"s_lshr_b32 s46, s79, 11\n"
"s_mul_i32 s79, s46, 10\n"
"s_sub_u32 s47, s31, s79\n"
"s_lshl_b32 s79, s47, 6\n"
"v_add_u32 v68, s79, v0\n"
"v_lshlrev_b32 v80, 1, v68\n"
"s_mul_i32 s48, s46, 0x7800\n"
"s_mov_b32 s14, 0x258000\n"
"s_mov_b32 s15, 0x20000\n"
"s_waitcnt lgkmcnt(0)\n"
"s_mov_b64 s[12:13], s[22:23]\n"
"s_cmp_ge_u32 s2, 200\n"
"s_cbranch_scc1 .Lnorep_3\n"
"buffer_load_ushort v28, v80, s[12:15], s48 offen offset:0\n"
"buffer_load_ushort v36, v80, s[12:15], s48 offen offset:1280\n"
"buffer_load_ushort v44, v80, s[12:15], s48 offen offset:2560\n"
"s_add_u32 s48, s48, 3840\n"
"buffer_load_ushort v29, v80, s[12:15], s48 offen offset:0\n"
"buffer_load_ushort v37, v80, s[12:15], s48 offen offset:1280\n"
"buffer_load_ushort v45, v80, s[12:15], s48 offen offset:2560\n"
"s_add_u32 s48, s48, 3840\n"
"buffer_load_ushort v30, v80, s[12:15], s48 offen offset:0\n"
"buffer_load_ushort v38, v80, s[12:15], s48 offen offset:1280\n"
"buffer_load_ushort v46, v80, s[12:15], s48 offen offset:2560\n"
"s_add_u32 s48, s48, 3840\n"
"buffer_load_ushort v31, v80, s[12:15], s48 offen offset:0\n"
"buffer_load_ushort v39, v80, s[12:15], s48 offen offset:1280\n"
"buffer_load_ushort v47, v80, s[12:15], s48 offen offset:2560\n"
"s_add_u32 s48, s48, 3840\n"
"buffer_load_ushort v32, v80, s[12:15], s48 offen offset:0\n"
"buffer_load_ushort v40, v80, s[12:15], s48 offen offset:1280\n"
"buffer_load_ushort v48, v80, s[12:15], s48 offen offset:2560\n"
"s_add_u32 s48, s48, 3840\n"
"buffer_load_ushort v33, v80, s[12:15], s48 offen offset:0\n"
"buffer_load_ushort v41, v80, s[12:15], s48 offen offset:1280\n"
"buffer_load_ushort v49, v80, s[12:15], s48 offen offset:2560\n"
"s_add_u32 s48, s48, 3840\n"
"buffer_load_ushort v34, v80, s[12:15], s48 offen offset:0\n"
"buffer_load_ushort v42, v80, s[12:15], s48 offen offset:1280\n"
"buffer_load_ushort v50, v80, s[12:15], s48 offen offset:2560\n"
"s_add_u32 s48, s48, 3840\n"
"buffer_load_ushort v35, v80, s[12:15], s48 offen offset:0\n"
"buffer_load_ushort v43, v80, s[12:15], s48 offen offset:1280\n"
"buffer_load_ushort v51, v80, s[12:15], s48 offen offset:2560\n"
"s_barrier\n"
"s_branch .Lrep_4\n"
".Lnorep_3:\n"
"s_barrier\n"
"s_sleep 12\n"
"s_mov_b64 s[12:13], s[56:57]\n"
"s_cmp_lt_i32 s31, 0x3e8\n"
"s_cselect_b32 s14, 0xc8000, 0\n"
"v_lshlrev_b32 v80, 1, v0\n"
"s_lshl_b32 s79, s31, 2\n"
"s_add_u32 s79, s79, 0xfffff380\n"
"s_mul_i32 s79, s79, 0xcd\n"
"s_lshr_b32 s80, s79, 11\n"
"s_lshl_b32 s79, s31, 2\n"
"s_add_u32 s79, s79, 0xfffff380\n"
"s_mul_i32 s78, s80, 10\n"
"s_sub_u32 s84, s79, s78\n"
"s_mul_i32 s79, s80, 0x67\n"
"s_lshr_b32 s79, s79, 9\n"
"s_mul_i32 s79, s79, 20\n"
"s_lshl_b32 s78, s80, 2\n"
"s_add_u32 s79, s79, s78\n"
"s_mul_i32 s79, s79, 0x500\n"
"s_lshl_b32 s78, s84, 7\n"
"s_add_u32 s48, s79, s78\n"
"s_add_u32 s49, s48, 0x6400\n"
"buffer_load_ushort v100, v80, s[12:15], s48 offen offset:0\n"
"buffer_load_ushort v101, v80, s[12:15], s48 offen offset:1280\n"
"buffer_load_ushort v102, v80, s[12:15], s48 offen offset:2560\n"
"buffer_load_ushort v103, v80, s[12:15], s48 offen offset:3840\n"
"buffer_load_ushort v104, v80, s[12:15], s49 offen offset:0\n"
"buffer_load_ushort v105, v80, s[12:15], s49 offen offset:1280\n"
"buffer_load_ushort v106, v80, s[12:15], s49 offen offset:2560\n"
"buffer_load_ushort v107, v80, s[12:15], s49 offen offset:3840\n"
"s_lshl_b32 s79, s31, 2\n"
"s_add_u32 s79, s79, 0xfffff381\n"
"s_mul_i32 s79, s79, 0xcd\n"
"s_lshr_b32 s81, s79, 11\n"
"s_lshl_b32 s79, s31, 2\n"
"s_add_u32 s79, s79, 0xfffff381\n"
"s_mul_i32 s78, s81, 10\n"
"s_sub_u32 s85, s79, s78\n"
"s_mul_i32 s79, s81, 0x67\n"
"s_lshr_b32 s79, s79, 9\n"
"s_mul_i32 s79, s79, 20\n"
"s_lshl_b32 s78, s81, 2\n"
"s_add_u32 s79, s79, s78\n"
"s_mul_i32 s79, s79, 0x500\n"
"s_lshl_b32 s78, s85, 7\n"
"s_add_u32 s48, s79, s78\n"
"s_add_u32 s49, s48, 0x6400\n"
"buffer_load_ushort v108, v80, s[12:15], s48 offen offset:0\n"
"buffer_load_ushort v109, v80, s[12:15], s48 offen offset:1280\n"
"buffer_load_ushort v110, v80, s[12:15], s48 offen offset:2560\n"
"buffer_load_ushort v111, v80, s[12:15], s48 offen offset:3840\n"
"buffer_load_ushort v112, v80, s[12:15], s49 offen offset:0\n"
"buffer_load_ushort v113, v80, s[12:15], s49 offen offset:1280\n"
"buffer_load_ushort v114, v80, s[12:15], s49 offen offset:2560\n"
"buffer_load_ushort v115, v80, s[12:15], s49 offen offset:3840\n"
"s_lshl_b32 s79, s31, 2\n"
"s_add_u32 s79, s79, 0xfffff382\n"
"s_mul_i32 s79, s79, 0xcd\n"
"s_lshr_b32 s82, s79, 11\n"
"s_lshl_b32 s79, s31, 2\n"
"s_add_u32 s79, s79, 0xfffff382\n"
"s_mul_i32 s78, s82, 10\n"
"s_sub_u32 s86, s79, s78\n"
"s_mul_i32 s79, s82, 0x67\n"
"s_lshr_b32 s79, s79, 9\n"
"s_mul_i32 s79, s79, 20\n"
"s_lshl_b32 s78, s82, 2\n"
"s_add_u32 s79, s79, s78\n"
"s_mul_i32 s79, s79, 0x500\n"
"s_lshl_b32 s78, s86, 7\n"
"s_add_u32 s48, s79, s78\n"
"s_add_u32 s49, s48, 0x6400\n"
"buffer_load_ushort v116, v80, s[12:15], s48 offen offset:0\n"
"buffer_load_ushort v117, v80, s[12:15], s48 offen offset:1280\n"
"buffer_load_ushort v118, v80, s[12:15], s48 offen offset:2560\n"
"buffer_load_ushort v119, v80, s[12:15], s48 offen offset:3840\n"
"buffer_load_ushort v120, v80, s[12:15], s49 offen offset:0\n"
"buffer_load_ushort v121, v80, s[12:15], s49 offen offset:1280\n"
"buffer_load_ushort v122, v80, s[12:15], s49 offen offset:2560\n"
"buffer_load_ushort v123, v80, s[12:15], s49 offen offset:3840\n"
"s_lshl_b32 s79, s31, 2\n"
"s_add_u32 s79, s79, 0xfffff383\n"
"s_mul_i32 s79, s79, 0xcd\n"
"s_lshr_b32 s83, s79, 11\n"
"s_lshl_b32 s79, s31, 2\n"
"s_add_u32 s79, s79, 0xfffff383\n"
"s_mul_i32 s78, s83, 10\n"
"s_sub_u32 s87, s79, s78\n"
"s_mul_i32 s79, s83, 0x67\n"
"s_lshr_b32 s79, s79, 9\n"
"s_mul_i32 s79, s79, 20\n"
"s_lshl_b32 s78, s83, 2\n"
"s_add_u32 s79, s79, s78\n"
"s_mul_i32 s79, s79, 0x500\n"
"s_lshl_b32 s78, s87, 7\n"
"s_add_u32 s48, s79, s78\n"
"s_add_u32 s49, s48, 0x6400\n"
"buffer_load_ushort v124, v80, s[12:15], s48 offen offset:0\n"
"buffer_load_ushort v125, v80, s[12:15], s48 offen offset:1280\n"
"buffer_load_ushort v126, v80, s[12:15], s48 offen offset:2560\n"
"buffer_load_ushort v127, v80, s[12:15], s48 offen offset:3840\n"
"buffer_load_ushort v128, v80, s[12:15], s49 offen offset:0\n"
"buffer_load_ushort v129, v80, s[12:15], s49 offen offset:1280\n"
"buffer_load_ushort v130, v80, s[12:15], s49 offen offset:2560\n"
"buffer_load_ushort v131, v80, s[12:15], s49 offen offset:3840\n"
".Lrep_4:\n"
"s_mov_b32 s4, s26\n"
"s_mov_b32 s5, s27\n"
"s_mov_b32 s6, 0x1ec8000\n"
"s_mov_b32 s7, 0x20000\n"
"s_load_dword s16, s[26:27], 0x0\n"
"v_lshlrev_b32 v1, 4, v0\n"
"s_and_b32 s79, s2, 7\n"
"s_lshl_b32 s79, s79, 4\n"
"s_lshr_b32 s78, s2, 3\n"
"s_add_u32 s79, s79, s78\n"
"s_and_b32 s78, s2, 3\n"
"s_lshl_b32 s78, s78, 3\n"
"s_and_b32 s77, s2, 4\n"
"s_cmp_eq_i32 s77, 4\n"
"s_cselect_b32 s78, 32, s78\n"
"s_add_u32 s79, s79, s78\n"
"s_and_b32 s78, s2, 4\n"
"s_lshl_b32 s78, s78, 4\n"
"s_add_u32 s78, s2, s78\n"
"s_cmp_lt_i32 s78, 0xc0\n"
"s_cselect_b32 s45, s79, -1\n"
"s_mov_b32 s8, s20\n"
"s_mov_b32 s9, s21\n"
"s_mov_b32 s10, 0x280000\n"
"s_mov_b32 s11, 0x20000\n"
"s_mov_b32 s12, s24\n"
"s_mov_b32 s13, s25\n"
"s_mov_b32 s14, 0x280000\n"
"s_mov_b32 s15, 0x20000\n"
"s_and_b32 s79, s2, 1\n"
"s_lshl_b32 s79, s79, 3\n"
"s_lshr_b32 s78, s2, 3\n"
"s_and_b32 s78, s78, 7\n"
"s_add_u32 s18, s79, s78\n"
"s_lshr_b32 s79, s2, 1\n"
"s_and_b32 s79, s79, 3\n"
"s_lshl_b32 s79, s79, 2\n"
"s_lshr_b32 s78, s2, 3\n"
"s_lshr_b32 s78, s78, 3\n"
"s_add_u32 s19, s79, s78\n"
"s_mul_i32 s79, s19, 0\n"
"s_add_u32 s79, s3, s79\n"
"s_and_b32 s43, s79, 3\n"
"v_mul_i32_i24 v79, 0xcd, v68\n"
"v_lshrrev_b32 v84, 13, v79\n"
"v_mul_i32_i24 v79, 40, v84\n"
"v_sub_u32 v85, v68, v79\n"
"v_mul_i32_i24 v79, 0xa0, v84\n"
"v_lshlrev_b32 v84, 10, v79\n"
"v_subrev_u32 v79, 24, v85\n"
"v_and_b32 v78, 15, v85\n"
"v_cmp_le_i32 vcc, 32, v85\n"
"s_nop 1\n"
"v_cndmask_b32 v79, v78, v79, vcc\n"
"v_lshrrev_b32 v78, 4, v85\n"
"s_mov_b32 s79, 0xa000\n"
"v_mad_i32_i24 v78, v78, s79, v84\n"
"v_lshl_add_u32 v64, v79, 4, v78\n"
"v_mov_b32 v86, v85\n"
"v_and_b32 v79, 15, v86\n"
"v_lshrrev_b32 v78, 4, v86\n"
"v_min_i32 v78, 3, v78\n"
"s_mov_b32 s79, 0xa000\n"
"v_mad_i32_i24 v78, v78, s79, v84\n"
"v_mov_b32 v77, 0\n"
"v_mov_b32 v76, 0x400\n"
"v_lshrrev_b32 v75, 4, v86\n"
"v_cmp_eq_i32 vcc, 3, v75\n"
"s_nop 1\n"
"v_cndmask_b32 v77, v76, v77, vcc\n"
"v_add_u32 v78, v78, v77\n"
"v_lshl_add_u32 v65, v79, 4, v78\n"
"v_add_u32 v86, 40, v85\n"
"v_and_b32 v79, 15, v86\n"
"v_lshrrev_b32 v78, 4, v86\n"
"v_min_i32 v78, 3, v78\n"
"s_mov_b32 s79, 0xa000\n"
"v_mad_i32_i24 v78, v78, s79, v84\n"
"v_mov_b32 v77, 0\n"
"v_mov_b32 v76, 0x400\n"
"v_lshrrev_b32 v75, 4, v86\n"
"v_cmp_eq_i32 vcc, 3, v75\n"
"s_nop 1\n"
"v_cndmask_b32 v77, v76, v77, vcc\n"
"v_add_u32 v78, v78, v77\n"
"v_lshl_add_u32 v66, v79, 4, v78\n"
"v_subrev_u32 v79, 0x80, v64\n"
"v_mov_b32 v78, 0x40000000\n"
"v_cmp_le_i32 vcc, 32, v85\n"
"s_nop 1\n"
"v_cndmask_b32 v67, v78, v79, vcc\n"
"s_lshr_b32 s79, s46, 2\n"
"s_lshl_b32 s79, s79, 11\n"
"s_and_b32 s78, s46, 3\n"
"s_lshl_b32 s78, s78, 8\n"
"s_add_u32 s79, s79, s78\n"
"s_add_u32 s48, s79, 0x100000\n"
"s_lshl_b32 s79, s47, 10\n"
"s_lshr_b32 s78, s46, 2\n"
"s_lshl_b32 s78, s78, 5\n"
"s_add_u32 s79, s79, s78\n"
"s_and_b32 s78, s46, 3\n"
"s_lshl_b32 s78, s78, 2\n"
"s_add_u32 s79, s79, s78\n"
"s_lshl_b32 s78, s2, 5\n"
"s_lshl_b32 s77, s3, 2\n"
"s_add_u32 s78, s78, s77\n"
"s_add_u32 s78, s78, 0xf00\n"
"s_cmp_lt_i32 s31, 0x320\n"
"s_cselect_b32 s79, s79, s78\n"
"s_add_u32 s32, s79, 0x1000\n"
"v_mov_b32 v20, 0x3f803f80\n"
"v_mov_b32 v21, 0x3f803f80\n"
"v_mov_b32 v22, 0x3f803f80\n"
"v_mov_b32 v23, 0x3f803f80\n"
"s_mov_b32 s79, 0x55\n"
"s_cmp_lt_i32 s47, 5\n"
"s_cselect_b32 s79, s79, 0xaa\n"
"s_cmp_lt_i32 s31, 0x320\n"
"s_cselect_b32 s58, s79, 0xff\n"
"s_mov_b32 s59, 0\n"
"s_mul_i32 s79, s19, 0x14000\n"
"s_mul_i32 s78, s43, 0x140\n"
"s_add_u32 s28, s79, s78\n"
"s_mul_i32 s29, s43, 0x1400\n"
"s_lshr_b32 s79, s2, 3\n"
"s_and_b32 s31, s79, 7\n"
"s_mul_i32 s33, s43, 0x140\n"
"s_sub_u32 s79, s31, 5\n"
"s_cmp_ge_i32 s31, 5\n"
"s_cselect_b32 s31, s79, s31\n"
"s_mul_i32 s79, s31, 0x5000\n"
"s_add_u32 s79, s79, s28\n"
"s_sub_u32 s79, s79, 0x5000\n"
"s_max_i32 s60, s79, s33\n"
"s_mul_i32 s79, s31, 0x5000\n"
"s_mov_b32 s78, 0x12400\n"
"s_cmp_ge_i32 s31, 2\n"
"s_cselect_b32 s78, s78, 0xa000\n"
"s_add_u32 s79, s79, s78\n"
"s_cmp_eq_i32 s31, 4\n"
"s_cselect_b32 s79, 0, s79\n"
"s_add_u32 s61, s29, s79\n"
"s_sub_u32 s79, s31, 4\n"
"s_add_u32 s78, s31, 1\n"
"s_cmp_ge_i32 s31, 4\n"
"s_cselect_b32 s31, s79, s78\n"
"s_mul_i32 s79, s31, 0x5000\n"
"s_add_u32 s79, s79, s28\n"
"s_sub_u32 s79, s79, 0x5000\n"
"s_max_i32 s62, s79, s33\n"
"s_mul_i32 s79, s31, 0x5000\n"
"s_mov_b32 s78, 0x12400\n"
"s_cmp_ge_i32 s31, 2\n"
"s_cselect_b32 s78, s78, 0xa000\n"
"s_add_u32 s79, s79, s78\n"
"s_cmp_eq_i32 s31, 4\n"
"s_cselect_b32 s79, 0, s79\n"
"s_add_u32 s63, s29, s79\n"
"s_sub_u32 s79, s31, 4\n"
"s_add_u32 s78, s31, 1\n"
"s_cmp_ge_i32 s31, 4\n"
"s_cselect_b32 s31, s79, s78\n"
"s_mul_i32 s79, s31, 0x5000\n"
"s_add_u32 s79, s79, s28\n"
"s_sub_u32 s79, s79, 0x5000\n"
"s_max_i32 s64, s79, s33\n"
"s_mul_i32 s79, s31, 0x5000\n"
"s_mov_b32 s78, 0x12400\n"
"s_cmp_ge_i32 s31, 2\n"
"s_cselect_b32 s78, s78, 0xa000\n"
"s_add_u32 s79, s79, s78\n"
"s_cmp_eq_i32 s31, 4\n"
"s_cselect_b32 s79, 0, s79\n"
"s_add_u32 s65, s29, s79\n"
"s_sub_u32 s79, s31, 4\n"
"s_add_u32 s78, s31, 1\n"
"s_cmp_ge_i32 s31, 4\n"
"s_cselect_b32 s31, s79, s78\n"
"s_mul_i32 s79, s31, 0x5000\n"
"s_add_u32 s79, s79, s28\n"
"s_sub_u32 s79, s79, 0x5000\n"
"s_max_i32 s66, s79, s33\n"
"s_mul_i32 s79, s31, 0x5000\n"
"s_mov_b32 s78, 0x12400\n"
"s_cmp_ge_i32 s31, 2\n"
"s_cselect_b32 s78, s78, 0xa000\n"
"s_add_u32 s79, s79, s78\n"
"s_cmp_eq_i32 s31, 4\n"
"s_cselect_b32 s79, 0, s79\n"
"s_add_u32 s67, s29, s79\n"
"s_sub_u32 s79, s31, 4\n"
"s_add_u32 s78, s31, 1\n"
"s_cmp_ge_i32 s31, 4\n"
"s_cselect_b32 s31, s79, s78\n"
"s_mul_i32 s79, s31, 0x5000\n"
"s_add_u32 s79, s79, s28\n"
"s_sub_u32 s79, s79, 0x5000\n"
"s_max_i32 s68, s79, s33\n"
"s_mul_i32 s79, s31, 0x5000\n"
"s_mov_b32 s78, 0x12400\n"
"s_cmp_ge_i32 s31, 2\n"
"s_cselect_b32 s78, s78, 0xa000\n"
"s_add_u32 s79, s79, s78\n"
"s_cmp_eq_i32 s31, 4\n"
"s_cselect_b32 s79, 0, s79\n"
"s_add_u32 s69, s29, s79\n"
"v_and_b32 v79, 3, v0\n"
"v_lshrrev_b32 v78, 5, v0\n"
"v_lshlrev_b32 v78, 1, v78\n"
"v_xor_b32 v79, v79, v78\n"
"v_lshrrev_b32 v78, 2, v0\n"
"v_mul_i32_i24 v78, 0x500, v78\n"
"v_lshl_add_u32 v2, v79, 4, v78\n"
"v_min_i32 v79, 24, v0\n"
"s_lshr_b32 s79, s2, 3\n"
"s_and_b32 s79, s79, 7\n"
"s_lshl_b32 s79, s79, 2\n"
"s_add_u32 s79, s79, s3\n"
"s_mul_i32 s79, s79, 0xc80\n"
"s_lshl_b32 s78, s19, 6\n"
"s_add_u32 s78, s78, 0x3f0\n"
"s_mul_i32 s78, s78, 0x500\n"
"s_add_u32 s79, s79, s78\n"
"v_lshl_add_u32 v89, v79, 7, s79\n"
"s_mov_b32 s31, 0x05040100\n"
"s_cmp_ge_u32 s2, 200\n"
"s_cbranch_scc1 .Lnorep_5\n"
"buffer_store_dwordx4 v[20:23], v67, s[4:7], s48 offen sc1\n"
"s_waitcnt vmcnt(1)\n"
"v_perm_b32 v52, v29, v28, s31\n"
"v_perm_b32 v53, v31, v30, s31\n"
"v_perm_b32 v54, v33, v32, s31\n"
"v_perm_b32 v55, v35, v34, s31\n"
"buffer_store_dwordx4 v[52:55], v64, s[4:7], s48 offen sc1\n"
"v_perm_b32 v56, v37, v36, s31\n"
"v_perm_b32 v57, v39, v38, s31\n"
"v_perm_b32 v58, v41, v40, s31\n"
"v_perm_b32 v59, v43, v42, s31\n"
"buffer_store_dwordx4 v[56:59], v65, s[4:7], s48 offen sc1\n"
"v_perm_b32 v60, v45, v44, s31\n"
"v_perm_b32 v61, v47, v46, s31\n"
"v_perm_b32 v62, v49, v48, s31\n"
"v_perm_b32 v63, v51, v50, s31\n"
"buffer_store_dwordx4 v[60:63], v66, s[4:7], s48 offen sc1\n"
"s_branch .Lrep_6\n"
".Lnorep_5:\n"
"s_lshl_b32 s79, s84, 6\n"
"v_add_u32 v68, s79, v0\n"
"v_mul_i32_i24 v79, 0xcd, v68\n"
"v_lshrrev_b32 v81, 14, v79\n"
"s_lshr_b32 s79, s80, 2\n"
"s_mul_i32 s79, s79, 13\n"
"s_lshr_b32 s79, s79, 6\n"
"s_and_b32 s79, s79, 1\n"
"s_lshl_b32 s79, s79, 1\n"
"v_mul_i32_i24 v79, 0x50, v81\n"
"v_sub_u32 v79, v68, v79\n"
"v_lshrrev_b32 v79, 4, v79\n"
"v_add_u32 v82, s79, v79\n"
"v_subrev_u32 v79, 5, v82\n"
"v_cmp_le_i32 vcc, 5, v82\n"
"s_nop 1\n"
"v_cndmask_b32 v79, v82, v79, vcc\n"
"s_mov_b32 s79, 0x64\n"
"v_mad_i32_i24 v79, v81, s79, v79\n"
"v_and_b32 v78, 15, v68\n"
"v_lshlrev_b32 v78, 4, v78\n"
"v_lshl_add_u32 v64, v79, 10, v78\n"
"s_lshr_b32 s79, s80, 2\n"
"s_mul_i32 s79, s79, 0x1400\n"
"s_and_b32 s78, s80, 3\n"
"s_lshl_b32 s78, s78, 8\n"
"s_add_u32 s79, s79, s78\n"
"s_add_u32 s79, s79, 0x380000\n"
"s_cmp_lt_i32 s80, 0x50\n"
"s_cselect_b32 s80, s79, 0x40000000\n"
"s_lshl_b32 s79, s85, 6\n"
"v_add_u32 v68, s79, v0\n"
"v_mul_i32_i24 v79, 0xcd, v68\n"
"v_lshrrev_b32 v81, 14, v79\n"
"s_lshr_b32 s79, s81, 2\n"
"s_mul_i32 s79, s79, 13\n"
"s_lshr_b32 s79, s79, 6\n"
"s_and_b32 s79, s79, 1\n"
"s_lshl_b32 s79, s79, 1\n"
"v_mul_i32_i24 v79, 0x50, v81\n"
"v_sub_u32 v79, v68, v79\n"
"v_lshrrev_b32 v79, 4, v79\n"
"v_add_u32 v82, s79, v79\n"
"v_subrev_u32 v79, 5, v82\n"
"v_cmp_le_i32 vcc, 5, v82\n"
"s_nop 1\n"
"v_cndmask_b32 v79, v82, v79, vcc\n"
"s_mov_b32 s79, 0x64\n"
"v_mad_i32_i24 v79, v81, s79, v79\n"
"v_and_b32 v78, 15, v68\n"
"v_lshlrev_b32 v78, 4, v78\n"
"v_lshl_add_u32 v65, v79, 10, v78\n"
"s_lshr_b32 s79, s81, 2\n"
"s_mul_i32 s79, s79, 0x1400\n"
"s_and_b32 s78, s81, 3\n"
"s_lshl_b32 s78, s78, 8\n"
"s_add_u32 s79, s79, s78\n"
"s_add_u32 s79, s79, 0x380000\n"
"s_cmp_lt_i32 s81, 0x50\n"
"s_cselect_b32 s81, s79, 0x40000000\n"
"s_lshl_b32 s79, s86, 6\n"
"v_add_u32 v68, s79, v0\n"
"v_mul_i32_i24 v79, 0xcd, v68\n"
"v_lshrrev_b32 v81, 14, v79\n"
"s_lshr_b32 s79, s82, 2\n"
"s_mul_i32 s79, s79, 13\n"
"s_lshr_b32 s79, s79, 6\n"
"s_and_b32 s79, s79, 1\n"
"s_lshl_b32 s79, s79, 1\n"
"v_mul_i32_i24 v79, 0x50, v81\n"
"v_sub_u32 v79, v68, v79\n"
"v_lshrrev_b32 v79, 4, v79\n"
"v_add_u32 v82, s79, v79\n"
"v_subrev_u32 v79, 5, v82\n"
"v_cmp_le_i32 vcc, 5, v82\n"
"s_nop 1\n"
"v_cndmask_b32 v79, v82, v79, vcc\n"
"s_mov_b32 s79, 0x64\n"
"v_mad_i32_i24 v79, v81, s79, v79\n"
"v_and_b32 v78, 15, v68\n"
"v_lshlrev_b32 v78, 4, v78\n"
"v_lshl_add_u32 v66, v79, 10, v78\n"
"s_lshr_b32 s79, s82, 2\n"
"s_mul_i32 s79, s79, 0x1400\n"
"s_and_b32 s78, s82, 3\n"
"s_lshl_b32 s78, s78, 8\n"
"s_add_u32 s79, s79, s78\n"
"s_add_u32 s79, s79, 0x380000\n"
"s_cmp_lt_i32 s82, 0x50\n"
"s_cselect_b32 s82, s79, 0x40000000\n"
"s_lshl_b32 s79, s87, 6\n"
"v_add_u32 v68, s79, v0\n"
"v_mul_i32_i24 v79, 0xcd, v68\n"
"v_lshrrev_b32 v81, 14, v79\n"
"s_lshr_b32 s79, s83, 2\n"
"s_mul_i32 s79, s79, 13\n"
"s_lshr_b32 s79, s79, 6\n"
"s_and_b32 s79, s79, 1\n"
"s_lshl_b32 s79, s79, 1\n"
"v_mul_i32_i24 v79, 0x50, v81\n"
"v_sub_u32 v79, v68, v79\n"
"v_lshrrev_b32 v79, 4, v79\n"
"v_add_u32 v82, s79, v79\n"
"v_subrev_u32 v79, 5, v82\n"
"v_cmp_le_i32 vcc, 5, v82\n"
"s_nop 1\n"
"v_cndmask_b32 v79, v82, v79, vcc\n"
"s_mov_b32 s79, 0x64\n"
"v_mad_i32_i24 v79, v81, s79, v79\n"
"v_and_b32 v78, 15, v68\n"
"v_lshlrev_b32 v78, 4, v78\n"
"v_lshl_add_u32 v67, v79, 10, v78\n"
"s_lshr_b32 s79, s83, 2\n"
"s_mul_i32 s79, s79, 0x1400\n"
"s_and_b32 s78, s83, 3\n"
"s_lshl_b32 s78, s78, 8\n"
"s_add_u32 s79, s79, s78\n"
"s_add_u32 s79, s79, 0x380000\n"
"s_cmp_lt_i32 s83, 0x50\n"
"s_cselect_b32 s83, s79, 0x40000000\n"
"s_waitcnt vmcnt(0)\n"
"v_perm_b32 v28, v101, v100, s31\n"
"v_perm_b32 v29, v103, v102, s31\n"
"v_perm_b32 v30, v105, v104, s31\n"
"v_perm_b32 v31, v107, v106, s31\n"
"buffer_store_dwordx4 v[28:31], v64, s[4:7], s80 offen sc1\n"
"v_perm_b32 v32, v109, v108, s31\n"
"v_perm_b32 v33, v111, v110, s31\n"
"v_perm_b32 v34, v113, v112, s31\n"
"v_perm_b32 v35, v115, v114, s31\n"
"buffer_store_dwordx4 v[32:35], v65, s[4:7], s81 offen sc1\n"
"v_perm_b32 v36, v117, v116, s31\n"
"v_perm_b32 v37, v119, v118, s31\n"
"v_perm_b32 v38, v121, v120, s31\n"
"v_perm_b32 v39, v123, v122, s31\n"
"buffer_store_dwordx4 v[36:39], v66, s[4:7], s82 offen sc1\n"
"v_perm_b32 v40, v125, v124, s31\n"
"v_perm_b32 v41, v127, v126, s31\n"
"v_perm_b32 v42, v129, v128, s31\n"
"v_perm_b32 v43, v131, v130, s31\n"
"buffer_store_dwordx4 v[40:43], v67, s[4:7], s83 offen sc1\n"
".Lrep_6:\n"
"v_lshlrev_b32 v80, 14, v0\n"
"s_mov_b32 s34, 0xaaaaaaaa\n"
"s_mov_b32 s35, 0xaaaaaaaa\n"
"s_mov_b32 s40, 0xffff\n"
"s_mov_b32 s41, 0\n"
"s_mov_b32 s36, 0xfff0ffff\n"
"s_mov_b32 s37, 0xf000ff00\n"
"s_mov_b32 s38, 0xffc0fffc\n"
"s_mov_b32 s39, 0xc000fc00\n"
"s_lshl_b32 s79, s19, 2\n"
"s_add_u32 s78, s3, 1\n"
"s_and_b32 s78, s78, 3\n"
"s_add_u32 s79, s79, s78\n"
"s_mul_i32 s79, s79, 0x5000\n"
"s_mul_i32 s78, s18, 0x500\n"
"s_add_u32 s79, s79, s78\n"
"s_add_u32 s30, s79, 0x448000\n"
"s_mul_i32 s79, s18, 40\n"
"s_lshr_b32 s79, s79, 6\n"
"s_lshl_b32 s79, s79, 10\n"
"v_lshl_add_u32 v79, v0, 5, s79\n"
"v_min_i32 v78, 39, v0\n"
"v_subrev_u32 v78, 20, v78\n"
"s_mul_i32 s79, s18, 40\n"
"s_add_u32 s79, s79, 39\n"
"s_lshr_b32 s79, s79, 6\n"
"s_lshl_b32 s79, s79, 10\n"
"v_lshl_add_u32 v78, v78, 5, s79\n"
"v_cmp_gt_i32 vcc, 20, v0\n"
"s_nop 1\n"
"v_cndmask_b32 v19, v78, v79, vcc\n"
"s_and_b32 s79, s2, 7\n"
"s_lshl_b32 s79, s79, 14\n"
"s_add_u32 s50, s79, 0x1000\n"
"s_mul_i32 s79, s19, 0x14000\n"
"s_mul_i32 s78, s43, 0x1400\n"
"s_add_u32 s79, s79, s78\n"
"s_add_u32 s44, s79, 0xc28000\n"
"s_add_u32 s79, s3, 1\n"
"s_and_b32 s79, s79, 3\n"
"s_add_u32 s79, s19, s79\n"
"s_cmp_eq_i32 s79, 0\n"
"s_cselect_b32 s42, 0xfe967699, 0\n"
"s_lshl_b32 s79, s18, 2\n"
"s_add_u32 s79, s79, s3\n"
"s_mul_i32 s79, s79, 0xa000\n"
"s_add_u32 s31, s79, 0x100000\n"
"s_lshl_b32 s79, s43, 11\n"
"s_add_u32 s52, s31, s79\n"
"s_mov_b32 s79, 0xffffe800\n"
"s_cmp_ge_i32 s43, 3\n"
"s_cselect_b32 s79, s79, 0x8800\n"
"s_add_u32 s53, s52, s79\n"
"s_mov_b32 s79, 0xfffff000\n"
"s_cmp_ge_i32 s43, 2\n"
"s_cselect_b32 s79, s79, 0x9000\n"
"s_add_u32 s54, s52, s79\n"
"s_mov_b32 s79, 0xfffff800\n"
"s_cmp_ge_i32 s43, 1\n"
"s_cselect_b32 s79, s79, 0x9800\n"
"s_add_u32 s55, s52, s79\n"
"s_mov_b32 s20, 0xcccccccc\n"
"s_mov_b32 s21, 0xcccccccc\n"
"s_mov_b32 s22, 0xf0000f\n"
"s_mov_b32 s23, 0xf0000f00\n"
"s_lshl_b32 s79, s19, 4\n"
"s_add_u32 s79, s79, s18\n"
"s_lshl_b32 s79, s79, 13\n"
"s_lshl_b32 s78, s3, 11\n"
"s_add_u32 s79, s79, s78\n"
"s_add_u32 s84, s79, 0x1688000\n"
"s_cmp_eq_i32 s3, 3\n"
"s_cselect_b32 s85, -1, 0\n"
"s_cmp_eq_i32 s19, 0\n"
"s_cselect_b32 s86, 0, -1\n"
"v_and_b32 v161, 15, v0\n"
"v_and_b32 v162, 3, v0\n"
"s_mul_i32 s70, s43, 0x1400\n"
"s_lshl_b32 s71, s3, 2\n"
"s_lshl_b32 s72, s3, 5\n"
"s_lshl_b32 s73, s43, 10\n"
"v_lshrrev_b32 v163, 4, v0\n"
"v_and_b32 v164, 3, v161\n"
"v_mul_i32_i24 v165, 0xf8, v161\n"
"v_lshlrev_b32 v166, 4, v161\n"
"v_lshlrev_b32 v167, 3, v163\n"
"v_lshlrev_b32 v79, 2, v161\n"
"v_add_u32 v12, 0x26940, v79\n"
"s_add_u32 s79, s3, 1\n"
"s_and_b32 s79, s79, 3\n"
"s_lshl_b32 s74, s79, 4\n"
"s_add_u32 s79, s71, 0x26840\n"
"v_add_u32 v168, s79, v166\n"
"s_lshl_b32 s79, s3, 11\n"
"s_add_u32 s79, s79, 0x5000\n"
"v_add_u32 v79, s79, v166\n"
"s_mov_b32 vcc_lo, 0xf0000f\n"
"s_mov_b32 vcc_hi, 0xf0000f00\n"
"v_cndmask_b32 v92, 2.0, v79, vcc\n"
"v_add_u32 v79, s74, v161\n"
"v_mul_i32_i24 v79, 0xf8, v79\n"
"v_add_u32 v24, 0x5000, v79\n"
"v_lshrrev_b32 v79, 3, v161\n"
"v_lshlrev_b32 v79, 1, v79\n"
"v_xor_b32 v79, v163, v79\n"
"v_lshlrev_b32 v79, 4, v79\n"
"v_lshl_add_u32 v169, v161, 6, v79\n"
"v_lshl_add_u32 v79, v164, 2, v166\n"
"v_lshl_add_u32 v79, v163, 8, v79\n"
"v_add_u32 v17, 0x5000, v79\n"
"v_add_u32 v79, 48, v167\n"
"v_mov_b32 v78, 0xf0\n"
"s_mov_b32 vcc_lo, 0\n"
"s_mov_b32 vcc_hi, -1\n"
"v_cndmask_b32 v79, v78, v79, vcc\n"
"s_cmp_eq_i32 s3, 3\n"
"s_cselect_b32 s79, 0xb0, s72\n"
"v_add_u32 v78, s79, v167\n"
"v_mov_b32 v77, s3\n"
"v_cmp_eq_i32 vcc, 2, v77\n"
"s_nop 1\n"
"v_cndmask_b32 v79, v78, v79, vcc\n"
"s_mov_b32 s79, 0x5000\n"
"v_add3_u32 v7, v165, v79, s79\n"
"s_add_u32 s79, s72, 0x50\n"
"s_cmp_eq_i32 s3, 3\n"
"s_cselect_b32 s79, 0xd0, s79\n"
"s_add_u32 s79, s79, 0x5000\n"
"v_add3_u32 v8, v165, v167, s79\n"
"v_mov_b32 v79, 0x40000000\n"
"s_xor_b32 s79, s3, 2\n"
"s_lshl_b32 s79, s79, 2\n"
"v_add_u32 v78, s79, v163\n"
"v_mov_b32 v77, 1\n"
"s_mov_b32 vcc_lo, 0xf000f\n"
"s_mov_b32 vcc_hi, 0xf000f\n"
"v_cndmask_b32 v77, 0, v77, vcc\n"
"v_cmp_eq_i32 vcc, v78, v77\n"
"s_nop 1\n"
"v_cndmask_b32 v79, v79, v12, vcc\n"
"s_mov_b32 vcc_lo, 0xf0000f\n"
"s_mov_b32 vcc_hi, 0xf0000f00\n"
"v_cndmask_b32 v9, v79, v168, vcc\n"
"v_add_u32 v11, 0x26840, v166\n"
"s_mov_b32 vcc_lo, 0xffff\n"
"s_mov_b32 vcc_hi, 0\n"
"v_cndmask_b32 v10, 2.0, v168, vcc\n"
"s_add_u32 s79, s71, 0x26400\n"
"v_mov_b32 v79, s79\n"
"s_mov_b32 vcc_lo, 1\n"
"s_mov_b32 vcc_hi, 0\n"
"v_cndmask_b32 v13, 2.0, v79, vcc\n"
"v_lshlrev_b32 v79, 2, v162\n"
"v_add_u32 v14, 0x26400, v79\n"
"v_add_u32 v25, v24, v167\n"
"v_lshl_add_u32 v79, v163, 2, s74\n"
"v_lshrrev_b32 v78, 2, v161\n"
"v_add_u32 v79, v79, v78\n"
"v_mul_i32_i24 v79, 0xf8, v79\n"
"v_lshl_add_u32 v79, v164, 3, v79\n"
"v_add_u32 v26, 0x50a0, v79\n"
"v_mov_b32 v27, 0x3727c5ac\n"
"v_xor_b32 v18, 4, v17\n"
"v_xor_b32 v79, 1, v164\n"
"v_lshl_add_u32 v91, v79, 2, v92\n"
"v_lshrrev_b32 v79, 2, v0\n"
"v_lshlrev_b32 v78, 4, v162\n"
"v_lshl_add_u32 v79, v79, 10, v78\n"
"v_add_u32 v79, s70, v79\n"
"s_mov_b32 vcc_lo, 0xfffff\n"
"s_mov_b32 vcc_hi, 0\n"
"v_cndmask_b32 v93, 2.0, v79, vcc\n"
"v_lshlrev_b32 v79, 3, v161\n"
"v_add_u32 v94, 0x9d80, v79\n"
"v_lshlrev_b32 v79, 3, v0\n"
"v_add_u32 v16, 0x9d80, v79\n"
"v_add_u32 v88, s70, v169\n"
"v_add_u32 v3, s73, v169\n"
"s_add_u32 s79, s73, 0xfffff400\n"
"s_add_u32 s78, s73, 0x4400\n"
"s_cmp_ge_i32 s43, 3\n"
"s_cselect_b32 s79, s79, s78\n"
"v_add_u32 v4, s79, v169\n"
"s_add_u32 s79, s73, 0xfffff800\n"
"s_add_u32 s78, s73, 0x4800\n"
"s_cmp_ge_i32 s43, 2\n"
"s_cselect_b32 s79, s79, s78\n"
"v_add_u32 v5, s79, v169\n"
"s_add_u32 s79, s73, 0xfffffc00\n"
"s_add_u32 s78, s73, 0x4c00\n"
"s_cmp_ge_i32 s43, 1\n"
"s_cselect_b32 s79, s79, s78\n"
"v_add_u32 v6, s79, v169\n"
"s_waitcnt lgkmcnt(0)\n"
"s_mul_i32 s16, s16, 0x2c9277b5\n"
"s_add_u32 s16, s16, 0xac564b05\n"
"v_mov_b32 v82, s16\n"
"s_waitcnt vmcnt(0)\n"
"s_mov_b64 exec, s[58:59]\n"
"buffer_store_dword v82, v80, s[4:7], s32 offen sc1\n"
"s_mov_b64 exec, -1\n"
"s_setprio 1\n"
"s_add_u32 m0, s61, 0\n"
"s_nop 0\n"
"buffer_load_dwordx4 v2, s[8:11], s60 offen offset:0 lds\n"
"s_add_u32 m0, s61, 0x3c0\n"
"s_nop 0\n"
"buffer_load_dwordx4 v2, s[8:11], s60 offen offset:64 lds\n"
"s_add_u32 m0, s61, 0x780\n"
"s_nop 0\n"
"buffer_load_dwordx4 v2, s[8:11], s60 offen offset:128 lds\n"
"s_add_u32 m0, s61, 0xb40\n"
"s_nop 0\n"
"buffer_load_dwordx4 v2, s[8:11], s60 offen offset:192 lds\n"
"s_add_u32 m0, s61, 0xf00\n"
"s_nop 0\n"
"buffer_load_dwordx4 v2, s[8:11], s60 offen offset:256 lds\n"
"s_add_u32 m0, s63, 0\n"
"s_nop 0\n"
"buffer_load_dwordx4 v2, s[8:11], s62 offen offset:0 lds\n"
"s_add_u32 m0, s63, 0x3c0\n"
"s_nop 0\n"
"buffer_load_dwordx4 v2, s[8:11], s62 offen offset:64 lds\n"
"s_add_u32 m0, s63, 0x780\n"
"s_nop 0\n"
"buffer_load_dwordx4 v2, s[8:11], s62 offen offset:128 lds\n"
"s_add_u32 m0, s63, 0xb40\n"
"s_nop 0\n"
"buffer_load_dwordx4 v2, s[8:11], s62 offen offset:192 lds\n"
"s_add_u32 m0, s63, 0xf00\n"
"s_nop 0\n"
"buffer_load_dwordx4 v2, s[8:11], s62 offen offset:256 lds\n"
"s_add_u32 m0, s65, 0\n"
"s_nop 0\n"
"buffer_load_dwordx4 v2, s[8:11], s64 offen offset:0 lds\n"
"s_add_u32 m0, s65, 0x3c0\n"
"s_nop 0\n"
"buffer_load_dwordx4 v2, s[8:11], s64 offen offset:64 lds\n"
"s_add_u32 m0, s65, 0x780\n"
"s_nop 0\n"
"buffer_load_dwordx4 v2, s[8:11], s64 offen offset:128 lds\n"
"s_add_u32 m0, s65, 0xb40\n"
"s_nop 0\n"
"buffer_load_dwordx4 v2, s[8:11], s64 offen offset:192 lds\n"
"s_add_u32 m0, s65, 0xf00\n"
"s_nop 0\n"
"buffer_load_dwordx4 v2, s[8:11], s64 offen offset:256 lds\n"
"s_add_u32 m0, s67, 0\n"
"s_nop 0\n"
"buffer_load_dwordx4 v2, s[8:11], s66 offen offset:0 lds\n"
"s_add_u32 m0, s67, 0x3c0\n"
"s_nop 0\n"
"buffer_load_dwordx4 v2, s[8:11], s66 offen offset:64 lds\n"
"s_add_u32 m0, s67, 0x780\n"
"s_nop 0\n"
"buffer_load_dwordx4 v2, s[8:11], s66 offen offset:128 lds\n"
"s_add_u32 m0, s67, 0xb40\n"
"s_nop 0\n"
"buffer_load_dwordx4 v2, s[8:11], s66 offen offset:192 lds\n"
"s_add_u32 m0, s67, 0xf00\n"
"s_nop 0\n"
"buffer_load_dwordx4 v2, s[8:11], s66 offen offset:256 lds\n"
"s_add_u32 m0, s69, 0\n"
"s_nop 0\n"
"buffer_load_dwordx4 v2, s[8:11], s68 offen offset:0 lds\n"
"s_add_u32 m0, s69, 0x3c0\n"
"s_nop 0\n"
"buffer_load_dwordx4 v2, s[8:11], s68 offen offset:64 lds\n"
"s_add_u32 m0, s69, 0x780\n"
"s_nop 0\n"
"buffer_load_dwordx4 v2, s[8:11], s68 offen offset:128 lds\n"
"s_add_u32 m0, s69, 0xb40\n"
"s_nop 0\n"
"buffer_load_dwordx4 v2, s[8:11], s68 offen offset:192 lds\n"
"s_add_u32 m0, s69, 0xf00\n"
"s_nop 0\n"
"buffer_load_dwordx4 v2, s[8:11], s68 offen offset:256 lds\n"
"buffer_load_dwordx4 v[96:99], v19, s[4:7], s50 offen sc1\n"
"s_waitcnt vmcnt(1)\n"
"ds_write_b128 v93, v[20:23] offset:40960\n"
"ds_write_b32 v13, v13 offset:0\n"
"v_add_u32 v88, 0xa000, v88\n"
"ds_read_b128 v[68:71], v88 offset:0\n"
"ds_read_b128 v[72:75], v88 offset:1024\n"
"ds_read_b128 v[76:79], v88 offset:2048\n"
"ds_read_b128 v[80:83], v88 offset:3072\n"
"s_waitcnt vmcnt(0)\n"
"v_xor_b32 v96, s16, v96\n"
"v_xor_b32 v97, s16, v97\n"
"v_xor_b32 v98, s16, v98\n"
"v_xor_b32 v99, s16, v99\n"
"v_or_b32 v96, v96, v97\n"
"v_or3_b32 v96, v96, v98, v99\n"
"v_cmp_ne_u32 vcc, 0, v96\n"
"s_cbranch_vccnz .Lwt_11\n"
"buffer_load_dwordx4 v[96:99], v1, s[4:7], s52 offen offset:0 sc1\n"
"s_waitcnt lgkmcnt(3)\n"
"v_mfma_f32_16x16x32_bf16 v[28:31], v[68:71], v[68:71], 0\n"
"ds_read_b128 v[84:87], v88 offset:4096\n"
"v_mfma_f32_16x16x32_bf16 v[32:35], v[20:23], v[68:71], 0\n"
"s_waitcnt lgkmcnt(3)\n"
"v_mfma_f32_16x16x32_bf16 v[28:31], v[72:75], v[72:75], v[28:31]\n"
"buffer_load_dwordx4 v[100:103], v1, s[4:7], s52 offen offset:1024 sc1\n"
"ds_read_b128 v[68:71], v88 offset:20480\n"
"v_mfma_f32_16x16x32_bf16 v[32:35], v[20:23], v[72:75], v[32:35]\n"
"s_waitcnt lgkmcnt(3)\n"
"v_mfma_f32_16x16x32_bf16 v[28:31], v[76:79], v[76:79], v[28:31]\n"
"ds_read_b128 v[72:75], v88 offset:21504\n"
"v_mfma_f32_16x16x32_bf16 v[32:35], v[20:23], v[76:79], v[32:35]\n"
"buffer_load_dwordx4 v[104:107], v1, s[4:7], s52 offen offset:2048 sc1\n"
"s_waitcnt lgkmcnt(3)\n"
"v_mfma_f32_16x16x32_bf16 v[28:31], v[80:83], v[80:83], v[28:31]\n"
"ds_read_b128 v[76:79], v88 offset:22528\n"
"v_mfma_f32_16x16x32_bf16 v[32:35], v[20:23], v[80:83], v[32:35]\n"
"s_waitcnt lgkmcnt(3)\n"
"v_mfma_f32_16x16x32_bf16 v[28:31], v[84:87], v[84:87], v[28:31]\n"
"ds_read_b128 v[80:83], v88 offset:23552\n"
"buffer_load_dwordx4 v[108:111], v1, s[4:7], s52 offen offset:3072 sc1\n"
"v_mfma_f32_16x16x32_bf16 v[32:35], v[20:23], v[84:87], v[32:35]\n"
"s_waitcnt lgkmcnt(3)\n"
"v_mfma_f32_16x16x32_bf16 v[36:39], v[68:71], v[68:71], 0\n"
"ds_read_b128 v[84:87], v88 offset:24576\n"
"v_mfma_f32_16x16x32_bf16 v[40:43], v[20:23], v[68:71], 0\n"
"s_waitcnt lgkmcnt(3)\n"
"v_mfma_f32_16x16x32_bf16 v[36:39], v[72:75], v[72:75], v[36:39]\n"
"s_add_u32 s52, s52, 4096\n"
"buffer_load_dwordx4 v[112:115], v1, s[4:7], s52 offen offset:0 sc1\n"
"v_add_u32 v88, 0x12400, v88\n"
"ds_read_b128 v[68:71], v88 offset:0\n"
"v_mfma_f32_16x16x32_bf16 v[40:43], v[20:23], v[72:75], v[40:43]\n"
"ds_write_b128 v92, v[28:31] offset:0\n"
"buffer_load_dwordx4 v[116:119], v1, s[4:7], s52 offen offset:1024 sc1\n"
"s_waitcnt lgkmcnt(4)\n"
"v_mfma_f32_16x16x32_bf16 v[36:39], v[76:79], v[76:79], v[36:39]\n"
"ds_read_b128 v[72:75], v88 offset:1024\n"
"v_mfma_f32_16x16x32_bf16 v[40:43], v[20:23], v[76:79], v[40:43]\n"
"ds_write_b32 v91, v32 offset:0\n"
"s_waitcnt lgkmcnt(5)\n"
"v_mfma_f32_16x16x32_bf16 v[36:39], v[80:83], v[80:83], v[36:39]\n"
"buffer_load_dwordx4 v[120:123], v1, s[4:7], s52 offen offset:2048 sc1\n"
"ds_read_b128 v[76:79], v88 offset:2048\n"
"v_mfma_f32_16x16x32_bf16 v[40:43], v[20:23], v[80:83], v[40:43]\n"
"s_waitcnt lgkmcnt(5)\n"
"v_mfma_f32_16x16x32_bf16 v[36:39], v[84:87], v[84:87], v[36:39]\n"
"ds_read_b128 v[80:83], v88 offset:3072\n"
"v_mfma_f32_16x16x32_bf16 v[40:43], v[20:23], v[84:87], v[40:43]\n"
"buffer_load_dwordx4 v[124:127], v1, s[4:7], s52 offen offset:3072 sc1\n"
"s_waitcnt lgkmcnt(5)\n"
"v_mfma_f32_16x16x32_bf16 v[44:47], v[68:71], v[68:71], 0\n"
"ds_read_b128 v[84:87], v88 offset:4096\n"
"v_mfma_f32_16x16x32_bf16 v[48:51], v[20:23], v[68:71], 0\n"
"s_waitcnt lgkmcnt(4)\n"
"v_mfma_f32_16x16x32_bf16 v[44:47], v[72:75], v[72:75], v[44:47]\n"
"ds_read_b128 v[68:71], v88 offset:20480\n"
"s_add_u32 s52, s52, 4096\n"
"buffer_load_dwordx4 v[128:131], v1, s[4:7], s52 offen offset:0 sc1\n"
"v_mfma_f32_16x16x32_bf16 v[48:51], v[20:23], v[72:75], v[48:51]\n"
"ds_write_b128 v92, v[36:39] offset:256\n"
"s_waitcnt lgkmcnt(4)\n"
"v_mfma_f32_16x16x32_bf16 v[44:47], v[76:79], v[76:79], v[44:47]\n"
"ds_read_b128 v[72:75], v88 offset:21504\n"
"buffer_load_dwordx4 v[132:135], v1, s[4:7], s52 offen offset:1024 sc1\n"
"v_mfma_f32_16x16x32_bf16 v[48:51], v[20:23], v[76:79], v[48:51]\n"
"ds_write_b32 v91, v40 offset:256\n"
"s_waitcnt lgkmcnt(5)\n"
"v_mfma_f32_16x16x32_bf16 v[44:47], v[80:83], v[80:83], v[44:47]\n"
"ds_read_b128 v[76:79], v88 offset:22528\n"
"v_mfma_f32_16x16x32_bf16 v[48:51], v[20:23], v[80:83], v[48:51]\n"
"buffer_load_dwordx4 v[136:139], v1, s[4:7], s52 offen offset:2048 sc1\n"
"s_waitcnt lgkmcnt(5)\n"
"v_mfma_f32_16x16x32_bf16 v[44:47], v[84:87], v[84:87], v[44:47]\n"
"ds_read_b128 v[80:83], v88 offset:23552\n"
"v_mfma_f32_16x16x32_bf16 v[48:51], v[20:23], v[84:87], v[48:51]\n"
"s_waitcnt lgkmcnt(5)\n"
"v_mfma_f32_16x16x32_bf16 v[52:55], v[68:71], v[68:71], 0\n"
"ds_read_b128 v[84:87], v88 offset:24576\n"
"buffer_load_dwordx4 v[140:143], v1, s[4:7], s52 offen offset:3072 sc1\n"
"v_mfma_f32_16x16x32_bf16 v[56:59], v[20:23], v[68:71], 0\n"
"s_waitcnt lgkmcnt(4)\n"
"v_mfma_f32_16x16x32_bf16 v[52:55], v[72:75], v[72:75], v[52:55]\n"
"v_add_u32 v88, 0xfffe3c00, v88\n"
"ds_read_b128 v[68:71], v88 offset:0\n"
"v_mfma_f32_16x16x32_bf16 v[56:59], v[20:23], v[72:75], v[56:59]\n"
"s_add_u32 s52, s52, 4096\n"
"buffer_load_dwordx4 v[144:147], v1, s[4:7], s52 offen offset:0 sc1\n"
"ds_write_b128 v92, v[44:47] offset:512\n"
"s_waitcnt lgkmcnt(4)\n"
"v_mfma_f32_16x16x32_bf16 v[52:55], v[76:79], v[76:79], v[52:55]\n"
"ds_read_b128 v[72:75], v88 offset:1024\n"
"v_mfma_f32_16x16x32_bf16 v[56:59], v[20:23], v[76:79], v[56:59]\n"
"buffer_load_dwordx4 v[148:151], v1, s[4:7], s52 offen offset:1024 sc1\n"
"ds_write_b32 v91, v48 offset:512\n"
"ds_read_b32 v90, v14 offset:0\n"
"s_waitcnt lgkmcnt(6)\n"
"v_mfma_f32_16x16x32_bf16 v[52:55], v[80:83], v[80:83], v[52:55]\n"
"ds_read_b128 v[76:79], v88 offset:2048\n"
"v_mfma_f32_16x16x32_bf16 v[56:59], v[20:23], v[80:83], v[56:59]\n"
"buffer_load_dwordx4 v[152:155], v1, s[4:7], s52 offen offset:2048 sc1\n"
"s_waitcnt lgkmcnt(6)\n"
"v_mfma_f32_16x16x32_bf16 v[52:55], v[84:87], v[84:87], v[52:55]\n"
"ds_read_b128 v[80:83], v88 offset:3072\n"
"v_mfma_f32_16x16x32_bf16 v[56:59], v[20:23], v[84:87], v[56:59]\n"
"s_waitcnt lgkmcnt(6)\n"
"v_mfma_f32_16x16x32_bf16 v[60:63], v[68:71], v[68:71], 0\n"
"ds_read_b128 v[84:87], v88 offset:4096\n"
"buffer_load_dwordx4 v[156:159], v1, s[4:7], s52 offen offset:3072 sc1\n"
"v_mfma_f32_16x16x32_bf16 v[64:67], v[20:23], v[68:71], 0\n"
"s_waitcnt lgkmcnt(3)\n"
"v_cmp_eq_u32 vcc, 0, v90\n"
"v_mfma_f32_16x16x32_bf16 v[60:63], v[72:75], v[72:75], v[60:63]\n"
"v_mfma_f32_16x16x32_bf16 v[64:67], v[20:23], v[72:75], v[64:67]\n"
"ds_write_b128 v92, v[52:55] offset:768\n"
"s_cbranch_vccnz .Lsl_7\n"
".Lbk_8:\n"
"s_waitcnt lgkmcnt(3)\n"
"v_mfma_f32_16x16x32_bf16 v[60:63], v[76:79], v[76:79], v[60:63]\n"
"v_add_u32 v3, 0x5000, v3\n"
"v_mfma_f32_16x16x32_bf16 v[64:67], v[20:23], v[76:79], v[64:67]\n"
"ds_read_b128 v[68:71], v3 offset:20480\n"
"ds_write_b32 v91, v56 offset:768\n"
"ds_read_b128 v[72:75], v3 offset:40960\n"
"s_waitcnt lgkmcnt(5)\n"
"v_mfma_f32_16x16x32_bf16 v[60:63], v[80:83], v[80:83], v[60:63]\n"
"v_add_u32 v95, 0x17400, v3\n"
"v_mfma_f32_16x16x32_bf16 v[64:67], v[20:23], v[80:83], v[64:67]\n"
"ds_read_b128 v[76:79], v95 offset:0\n"
"s_waitcnt lgkmcnt(5)\n"
"v_mfma_f32_16x16x32_bf16 v[60:63], v[84:87], v[84:87], v[60:63]\n"
"ds_read_b128 v[80:83], v95 offset:20480\n"
"v_mfma_f32_16x16x32_bf16 v[64:67], v[20:23], v[84:87], v[64:67]\n"
"s_nop 4\n"
"ds_write_b128 v92, v[60:63] offset:1024\n"
"s_nop 1\n"
"ds_write_b32 v91, v64 offset:1024\n"
"s_branch .Ljn_12\n"
".Lwt_11:\n"
"s_waitcnt lgkmcnt(0)\n"
"v_mfma_f32_16x16x32_bf16 v[28:31], v[68:71], v[68:71], 0\n"
"ds_read_b128 v[84:87], v88 offset:4096\n"
"v_mfma_f32_16x16x32_bf16 v[32:35], v[20:23], v[68:71], 0\n"
"v_mfma_f32_16x16x32_bf16 v[28:31], v[72:75], v[72:75], v[28:31]\n"
"ds_read_b128 v[68:71], v88 offset:20480\n"
"v_mfma_f32_16x16x32_bf16 v[32:35], v[20:23], v[72:75], v[32:35]\n"
"v_mfma_f32_16x16x32_bf16 v[28:31], v[76:79], v[76:79], v[28:31]\n"
"ds_read_b128 v[72:75], v88 offset:21504\n"
"v_mfma_f32_16x16x32_bf16 v[32:35], v[20:23], v[76:79], v[32:35]\n"
"v_mfma_f32_16x16x32_bf16 v[28:31], v[80:83], v[80:83], v[28:31]\n"
"ds_read_b128 v[76:79], v88 offset:22528\n"
"v_mfma_f32_16x16x32_bf16 v[32:35], v[20:23], v[80:83], v[32:35]\n"
"s_waitcnt lgkmcnt(3)\n"
"v_mfma_f32_16x16x32_bf16 v[28:31], v[84:87], v[84:87], v[28:31]\n"
"ds_read_b128 v[80:83], v88 offset:23552\n"
"v_mfma_f32_16x16x32_bf16 v[32:35], v[20:23], v[84:87], v[32:35]\n"
"s_waitcnt lgkmcnt(3)\n"
"v_mfma_f32_16x16x32_bf16 v[36:39], v[68:71], v[68:71], 0\n"
"ds_read_b128 v[84:87], v88 offset:24576\n"
"v_mfma_f32_16x16x32_bf16 v[40:43], v[20:23], v[68:71], 0\n"
"s_waitcnt lgkmcnt(3)\n"
"v_mfma_f32_16x16x32_bf16 v[36:39], v[72:75], v[72:75], v[36:39]\n"
"v_add_u32 v88, 0x12400, v88\n"
"ds_read_b128 v[68:71], v88 offset:0\n"
"v_mfma_f32_16x16x32_bf16 v[40:43], v[20:23], v[72:75], v[40:43]\n"
"ds_write_b128 v92, v[28:31] offset:0\n"
"s_waitcnt lgkmcnt(4)\n"
"v_mfma_f32_16x16x32_bf16 v[36:39], v[76:79], v[76:79], v[36:39]\n"
"ds_read_b128 v[72:75], v88 offset:1024\n"
"v_mfma_f32_16x16x32_bf16 v[40:43], v[20:23], v[76:79], v[40:43]\n"
"ds_write_b32 v91, v32 offset:0\n"
"s_waitcnt lgkmcnt(5)\n"
"v_mfma_f32_16x16x32_bf16 v[36:39], v[80:83], v[80:83], v[36:39]\n"
"ds_read_b128 v[76:79], v88 offset:2048\n"
"v_mfma_f32_16x16x32_bf16 v[40:43], v[20:23], v[80:83], v[40:43]\n"
"s_waitcnt lgkmcnt(5)\n"
"v_mfma_f32_16x16x32_bf16 v[36:39], v[84:87], v[84:87], v[36:39]\n"
"ds_read_b128 v[80:83], v88 offset:3072\n"
"v_mfma_f32_16x16x32_bf16 v[40:43], v[20:23], v[84:87], v[40:43]\n"
"s_waitcnt lgkmcnt(5)\n"
"v_mfma_f32_16x16x32_bf16 v[44:47], v[68:71], v[68:71], 0\n"
"ds_read_b128 v[84:87], v88 offset:4096\n"
"v_mfma_f32_16x16x32_bf16 v[48:51], v[20:23], v[68:71], 0\n"
"s_waitcnt lgkmcnt(4)\n"
"v_mfma_f32_16x16x32_bf16 v[44:47], v[72:75], v[72:75], v[44:47]\n"
"ds_read_b128 v[68:71], v88 offset:20480\n"
"v_mfma_f32_16x16x32_bf16 v[48:51], v[20:23], v[72:75], v[48:51]\n"
"ds_write_b128 v92, v[36:39] offset:256\n"
"s_waitcnt lgkmcnt(4)\n"
"v_mfma_f32_16x16x32_bf16 v[44:47], v[76:79], v[76:79], v[44:47]\n"
"ds_read_b128 v[72:75], v88 offset:21504\n"
"v_mfma_f32_16x16x32_bf16 v[48:51], v[20:23], v[76:79], v[48:51]\n"
"ds_write_b32 v91, v40 offset:256\n"
"s_waitcnt lgkmcnt(5)\n"
"v_mfma_f32_16x16x32_bf16 v[44:47], v[80:83], v[80:83], v[44:47]\n"
"ds_read_b128 v[76:79], v88 offset:22528\n"
"v_mfma_f32_16x16x32_bf16 v[48:51], v[20:23], v[80:83], v[48:51]\n"
"s_waitcnt lgkmcnt(5)\n"
"v_mfma_f32_16x16x32_bf16 v[44:47], v[84:87], v[84:87], v[44:47]\n"
"ds_read_b128 v[80:83], v88 offset:23552\n"
"v_mfma_f32_16x16x32_bf16 v[48:51], v[20:23], v[84:87], v[48:51]\n"
"s_waitcnt lgkmcnt(5)\n"
"v_mfma_f32_16x16x32_bf16 v[52:55], v[68:71], v[68:71], 0\n"
"ds_read_b128 v[84:87], v88 offset:24576\n"
"v_mfma_f32_16x16x32_bf16 v[56:59], v[20:23], v[68:71], 0\n"
"s_waitcnt lgkmcnt(4)\n"
"v_mfma_f32_16x16x32_bf16 v[52:55], v[72:75], v[72:75], v[52:55]\n"
"v_add_u32 v88, 0xfffe3c00, v88\n"
"ds_read_b128 v[68:71], v88 offset:0\n"
"v_mfma_f32_16x16x32_bf16 v[56:59], v[20:23], v[72:75], v[56:59]\n"
"ds_write_b128 v92, v[44:47] offset:512\n"
"s_waitcnt lgkmcnt(4)\n"
"v_mfma_f32_16x16x32_bf16 v[52:55], v[76:79], v[76:79], v[52:55]\n"
"ds_read_b128 v[72:75], v88 offset:1024\n"
"v_mfma_f32_16x16x32_bf16 v[56:59], v[20:23], v[76:79], v[56:59]\n"
"ds_write_b32 v91, v48 offset:512\n"
"ds_read_b32 v90, v14 offset:0\n"
"s_waitcnt lgkmcnt(6)\n"
"v_mfma_f32_16x16x32_bf16 v[52:55], v[80:83], v[80:83], v[52:55]\n"
"ds_read_b128 v[76:79], v88 offset:2048\n"
"v_mfma_f32_16x16x32_bf16 v[56:59], v[20:23], v[80:83], v[56:59]\n"
"s_waitcnt lgkmcnt(6)\n"
"v_mfma_f32_16x16x32_bf16 v[52:55], v[84:87], v[84:87], v[52:55]\n"
"ds_read_b128 v[80:83], v88 offset:3072\n"
"v_mfma_f32_16x16x32_bf16 v[56:59], v[20:23], v[84:87], v[56:59]\n"
"s_waitcnt lgkmcnt(6)\n"
"v_mfma_f32_16x16x32_bf16 v[60:63], v[68:71], v[68:71], 0\n"
"ds_read_b128 v[84:87], v88 offset:4096\n"
"v_mfma_f32_16x16x32_bf16 v[64:67], v[20:23], v[68:71], 0\n"
"s_waitcnt lgkmcnt(3)\n"
"v_cmp_eq_u32 vcc, 0, v90\n"
"v_mfma_f32_16x16x32_bf16 v[60:63], v[72:75], v[72:75], v[60:63]\n"
"v_mfma_f32_16x16x32_bf16 v[64:67], v[20:23], v[72:75], v[64:67]\n"
"ds_write_b128 v92, v[52:55] offset:768\n"
"s_waitcnt lgkmcnt(3)\n"
"v_mfma_f32_16x16x32_bf16 v[60:63], v[76:79], v[76:79], v[60:63]\n"
"v_mfma_f32_16x16x32_bf16 v[64:67], v[20:23], v[76:79], v[64:67]\n"
"ds_write_b32 v91, v56 offset:768\n"
"s_waitcnt lgkmcnt(3)\n"
"v_mfma_f32_16x16x32_bf16 v[60:63], v[80:83], v[80:83], v[60:63]\n"
"v_mfma_f32_16x16x32_bf16 v[64:67], v[20:23], v[80:83], v[64:67]\n"
"s_waitcnt lgkmcnt(2)\n"
"v_mfma_f32_16x16x32_bf16 v[60:63], v[84:87], v[84:87], v[60:63]\n"
"v_mfma_f32_16x16x32_bf16 v[64:67], v[20:23], v[84:87], v[64:67]\n"
"buffer_load_dwordx4 v[96:99], v19, s[4:7], s50 offen sc1\n"
"s_mov_b32 s17, 0x100000\n"
".Ltp_13:\n"
"ds_read_b32 v90, v14 offset:0\n"
"s_sub_u32 s17, s17, 1\n"
"s_cbranch_scc1 .Lou_14\n"
"s_waitcnt lgkmcnt(0)\n"
"v_cmp_eq_u32 vcc, 0, v90\n"
"s_cbranch_vccnz .Ltp_13\n"
".Lou_14:\n"
"s_waitcnt lgkmcnt(0)\n"
"v_add_u32 v3, 0x5000, v3\n"
"ds_read_b128 v[68:71], v3 offset:20480\n"
"ds_read_b128 v[72:75], v3 offset:40960\n"
"v_add_u32 v95, 0x17400, v3\n"
"ds_read_b128 v[76:79], v95 offset:0\n"
"ds_read_b128 v[80:83], v95 offset:20480\n"
"ds_write_b128 v92, v[60:63] offset:1024\n"
"ds_write_b32 v91, v64 offset:1024\n"
"s_waitcnt vmcnt(0) lgkmcnt(2)\n"
"s_mov_b32 s17, 32768\n"
"s_branch .Lmd_17\n"
".Ltp_15:\n"
"buffer_load_dwordx4 v[96:99], v19, s[4:7], s50 offen sc1\n"
".Lmd_17:\n"
"s_sub_u32 s17, s17, 1\n"
"s_cbranch_scc1 .Lou_16\n"
"s_waitcnt vmcnt(0)\n"
"v_xor_b32 v96, s16, v96\n"
"v_xor_b32 v97, s16, v97\n"
"v_xor_b32 v98, s16, v98\n"
"v_xor_b32 v99, s16, v99\n"
"v_or_b32 v96, v96, v97\n"
"v_or3_b32 v96, v96, v98, v99\n"
"v_cmp_ne_u32 vcc, 0, v96\n"
"s_cbranch_vccz .Lou_16\n"
"s_sleep 8\n"
"s_branch .Ltp_15\n"
".Lou_16:\n"
"s_waitcnt vmcnt(0)\n"
"buffer_load_dwordx4 v[96:99], v1, s[4:7], s52 offen offset:0 sc1\n"
"buffer_load_dwordx4 v[100:103], v1, s[4:7], s52 offen offset:1024 sc1\n"
"buffer_load_dwordx4 v[104:107], v1, s[4:7], s52 offen offset:2048 sc1\n"
"buffer_load_dwordx4 v[108:111], v1, s[4:7], s52 offen offset:3072 sc1\n"
"s_add_u32 s52, s52, 4096\n"
"buffer_load_dwordx4 v[112:115], v1, s[4:7], s52 offen offset:0 sc1\n"
"buffer_load_dwordx4 v[116:119], v1, s[4:7], s52 offen offset:1024 sc1\n"
"buffer_load_dwordx4 v[120:123], v1, s[4:7], s52 offen offset:2048 sc1\n"
"buffer_load_dwordx4 v[124:127], v1, s[4:7], s52 offen offset:3072 sc1\n"
"s_add_u32 s52, s52, 4096\n"
"buffer_load_dwordx4 v[128:131], v1, s[4:7], s52 offen offset:0 sc1\n"
"buffer_load_dwordx4 v[132:135], v1, s[4:7], s52 offen offset:1024 sc1\n"
"buffer_load_dwordx4 v[136:139], v1, s[4:7], s52 offen offset:2048 sc1\n"
"buffer_load_dwordx4 v[140:143], v1, s[4:7], s52 offen offset:3072 sc1\n"
"s_add_u32 s52, s52, 4096\n"
"buffer_load_dwordx4 v[144:147], v1, s[4:7], s52 offen offset:0 sc1\n"
"buffer_load_dwordx4 v[148:151], v1, s[4:7], s52 offen offset:1024 sc1\n"
"buffer_load_dwordx4 v[152:155], v1, s[4:7], s52 offen offset:2048 sc1\n"
"buffer_load_dwordx4 v[156:159], v1, s[4:7], s52 offen offset:3072 sc1\n"
".Ljn_12:\n"
"s_waitcnt lgkmcnt(2)\n"
"ds_write_b32 v13, v13 offset:16\n"
"s_waitcnt vmcnt(14)\n"
"v_mfma_f32_16x16x32_bf16 v[28:31], v[96:99], v[68:71], 0\n"
"ds_read_b128 v[84:87], v3 offset:21504\n"
"s_add_u32 s52, s52, 4096\n"
"v_mfma_f32_16x16x32_bf16 v[32:35], v[100:103], v[68:71], 0\n"
"v_mfma_f32_16x16x32_bf16 v[36:39], v[96:99], v[72:75], 0\n"
"ds_read_b128 v[68:71], v3 offset:41984\n"
"buffer_load_dwordx4 v[160:163], v1, s[4:7], s52 offen offset:0 sc1\n"
"v_mfma_f32_16x16x32_bf16 v[40:43], v[100:103], v[72:75], 0\n"
"v_mfma_f32_16x16x32_bf16 v[44:47], v[96:99], v[76:79], 0\n"
"ds_read_b128 v[72:75], v95 offset:1024\n"
"v_mfma_f32_16x16x32_bf16 v[48:51], v[100:103], v[76:79], 0\n"
"buffer_load_dwordx4 v[164:167], v1, s[4:7], s52 offen offset:1024 sc1\n"
"v_mfma_f32_16x16x32_bf16 v[52:55], v[96:99], v[80:83], 0\n"
"ds_read_b128 v[76:79], v95 offset:21504\n"
"v_mfma_f32_16x16x32_bf16 v[56:59], v[100:103], v[80:83], 0\n"
"s_waitcnt vmcnt(14) lgkmcnt(2)\n"
"v_mfma_f32_16x16x32_bf16 v[28:31], v[104:107], v[84:87], v[28:31]\n"
"ds_read_b128 v[80:83], v3 offset:22528\n"
"buffer_load_dwordx4 v[168:171], v1, s[4:7], s52 offen offset:2048 sc1\n"
"v_mfma_f32_16x16x32_bf16 v[32:35], v[108:111], v[84:87], v[32:35]\n"
"v_mfma_f32_16x16x32_bf16 v[36:39], v[104:107], v[68:71], v[36:39]\n"
"ds_read_b128 v[84:87], v3 offset:43008\n"
"v_mfma_f32_16x16x32_bf16 v[40:43], v[108:111], v[68:71], v[40:43]\n"
"buffer_load_dwordx4 v[172:175], v1, s[4:7], s52 offen offset:3072 sc1\n"
"v_mov_b32 v20, -1\n"
"s_waitcnt lgkmcnt(2)\n"
"v_mfma_f32_16x16x32_bf16 v[44:47], v[104:107], v[72:75], v[44:47]\n"
"ds_read_b128 v[68:71], v95 offset:2048\n"
"s_add_u32 s52, s52, 4096\n"
"v_mov_b32 v21, -1\n"
"v_mfma_f32_16x16x32_bf16 v[48:51], v[108:111], v[72:75], v[48:51]\n"
"v_mov_b32 v22, -1\n"
"v_mov_b32 v23, -1\n"
"v_cndmask_b32 v91, 2.0, v1, s[40:41]\n"
"v_mfma_f32_16x16x32_bf16 v[52:55], v[104:107], v[76:79], v[52:55]\n"
"ds_read_b128 v[72:75], v95 offset:22528\n"
"buffer_load_dwordx4 v[176:179], v1, s[4:7], s52 offen offset:0 sc1\n"
"s_mov_b32 s33, s30\n"
"v_mfma_f32_16x16x32_bf16 v[56:59], v[108:111], v[76:79], v[56:59]\n"
"buffer_store_dwordx4 v[20:23], v1, s[4:7], s33 offen offset:0 sc1\n"
"s_waitcnt vmcnt(16) lgkmcnt(2)\n"
"v_mfma_f32_16x16x32_bf16 v[28:31], v[112:115], v[80:83], v[28:31]\n"
"ds_read_b128 v[76:79], v3 offset:23552\n"
"v_mfma_f32_16x16x32_bf16 v[32:35], v[116:119], v[80:83], v[32:35]\n"
"buffer_load_dwordx4 v[180:183], v1, s[4:7], s52 offen offset:1024 sc1\n"
"v_mfma_f32_16x16x32_bf16 v[36:39], v[112:115], v[84:87], v[36:39]\n"
"ds_read_b128 v[80:83], v3 offset:44032\n"
"v_mfma_f32_16x16x32_bf16 v[40:43], v[116:119], v[84:87], v[40:43]\n"
"s_waitcnt lgkmcnt(2)\n"
"v_mfma_f32_16x16x32_bf16 v[44:47], v[112:115], v[68:71], v[44:47]\n"
"ds_read_b128 v[84:87], v95 offset:3072\n"
"buffer_load_dwordx4 v[184:187], v1, s[4:7], s52 offen offset:2048 sc1\n"
"buffer_store_dwordx4 v[20:23], v91, s[4:7], s33 offen offset:1024 sc1\n"
"v_mfma_f32_16x16x32_bf16 v[48:51], v[116:119], v[68:71], v[48:51]\n"
"s_add_u32 s33, s30, 0x140000\n"
"ds_read_b32 v90, v14 offset:16\n"
"v_mfma_f32_16x16x32_bf16 v[52:55], v[112:115], v[72:75], v[52:55]\n"
"ds_read_b128 v[68:71], v95 offset:23552\n"
"v_mfma_f32_16x16x32_bf16 v[56:59], v[116:119], v[72:75], v[56:59]\n"
"buffer_load_dwordx4 v[188:191], v1, s[4:7], s52 offen offset:3072 sc1\n"
"s_waitcnt vmcnt(18) lgkmcnt(3)\n"
"v_mfma_f32_16x16x32_bf16 v[28:31], v[120:123], v[76:79], v[28:31]\n"
"ds_read_b128 v[72:75], v3 offset:24576\n"
"s_add_u32 s52, s52, 4096\n"
"v_mfma_f32_16x16x32_bf16 v[32:35], v[124:127], v[76:79], v[32:35]\n"
"buffer_store_dwordx4 v[20:23], v1, s[4:7], s33 offen offset:0 sc1\n"
"v_mfma_f32_16x16x32_bf16 v[36:39], v[120:123], v[80:83], v[36:39]\n"
"ds_read_b128 v[76:79], v3 offset:45056\n"
"buffer_load_dwordx4 v[192:195], v1, s[4:7], s52 offen offset:0 sc1\n"
"v_mfma_f32_16x16x32_bf16 v[40:43], v[124:127], v[80:83], v[40:43]\n"
"s_waitcnt lgkmcnt(2)\n"
"v_mfma_f32_16x16x32_bf16 v[44:47], v[120:123], v[84:87], v[44:47]\n"
"ds_read_b128 v[80:83], v95 offset:4096\n"
"v_mfma_f32_16x16x32_bf16 v[48:51], v[124:127], v[84:87], v[48:51]\n"
"buffer_load_dwordx4 v[196:199], v1, s[4:7], s52 offen offset:1024 sc1\n"
"v_mfma_f32_16x16x32_bf16 v[52:55], v[120:123], v[68:71], v[52:55]\n"
"ds_read_b128 v[84:87], v95 offset:24576\n"
"buffer_store_dwordx4 v[20:23], v91, s[4:7], s33 offen offset:1024 sc1\n"
"v_mfma_f32_16x16x32_bf16 v[56:59], v[124:127], v[68:71], v[56:59]\n"
"s_lshl_b32 s79, s2, 2\n"
"s_add_u32 s79, s79, s3\n"
"v_cmp_eq_u32 vcc, 0, v90\n"
"s_waitcnt vmcnt(20) lgkmcnt(2)\n"
"v_mfma_f32_16x16x32_bf16 v[28:31], v[128:131], v[72:75], v[28:31]\n"
"ds_read_b128 v[68:71], v3 offset:25600\n"
"buffer_load_dwordx4 v[200:203], v1, s[4:7], s52 offen offset:2048 sc1\n"
"s_lshl_b32 s79, s79, 10\n"
"v_mfma_f32_16x16x32_bf16 v[32:35], v[132:135], v[72:75], v[32:35]\n"
"s_add_u32 s33, s79, 0xbc8000\n"
"s_sub_u32 s79, 0xc27fff, s33\n"
"s_and_b32 s79, s79, 0x80000000\n"
"s_cbranch_vccnz .Lsl_26\n"
".Lbk_27:\n"
"v_mfma_f32_16x16x32_bf16 v[36:39], v[128:131], v[76:79], v[36:39]\n"
"ds_read_b128 v[72:75], v3 offset:46080\n"
"s_or_b32 s33, s33, s79\n"
"v_mfma_f32_16x16x32_bf16 v[40:43], v[132:135], v[76:79], v[40:43]\n"
"buffer_load_dwordx4 v[204:207], v1, s[4:7], s52 offen offset:3072 sc1\n"
"buffer_store_dwordx4 v[20:23], v1, s[4:7], s33 offen sc1\n"
"ds_read2st64_b32 v[60:61], v17 offset0:0 offset1:8\n"
"s_waitcnt lgkmcnt(3)\n"
"v_mfma_f32_16x16x32_bf16 v[44:47], v[128:131], v[80:83], v[44:47]\n"
"ds_read_b128 v[76:79], v95 offset:5120\n"
"s_add_u32 s52, s52, 4096\n"
"v_mfma_f32_16x16x32_bf16 v[48:51], v[132:135], v[80:83], v[48:51]\n"
"ds_read2st64_b32 v[62:63], v17 offset0:16 offset1:24\n"
"v_mfma_f32_16x16x32_bf16 v[52:55], v[128:131], v[84:87], v[52:55]\n"
"ds_read_b128 v[80:83], v95 offset:25600\n"
"buffer_load_dwordx4 v[208:211], v1, s[4:7], s52 offen offset:0 sc1\n"
"v_mfma_f32_16x16x32_bf16 v[56:59], v[132:135], v[84:87], v[56:59]\n"
"ds_read2st64_b32 v[64:65], v18 offset0:0 offset1:8\n"
"s_waitcnt vmcnt(22) lgkmcnt(5)\n"
"v_mfma_f32_16x16x32_bf16 v[28:31], v[136:139], v[68:71], v[28:31]\n"
"ds_read_b128 v[84:87], v3 offset:26624\n"
"v_mfma_f32_16x16x32_bf16 v[32:35], v[140:143], v[68:71], v[32:35]\n"
"buffer_load_dwordx4 v[212:215], v1, s[4:7], s52 offen offset:1024 sc1\n"
"ds_read2st64_b32 v[66:67], v18 offset0:16 offset1:24\n"
"v_mfma_f32_16x16x32_bf16 v[36:39], v[136:139], v[72:75], v[36:39]\n"
"ds_read_b128 v[68:71], v3 offset:47104\n"
"v_mfma_f32_16x16x32_bf16 v[40:43], v[140:143], v[72:75], v[40:43]\n"
"s_waitcnt lgkmcnt(4)\n"
"v_mfma_f32_16x16x32_bf16 v[44:47], v[136:139], v[76:79], v[44:47]\n"
"ds_read_b128 v[72:75], v95 offset:6144\n"
"buffer_load_dwordx4 v[216:219], v1, s[4:7], s52 offen offset:2048 sc1\n"
"v_mfma_f32_16x16x32_bf16 v[48:51], v[140:143], v[76:79], v[48:51]\n"
"v_mfma_f32_16x16x32_bf16 v[52:55], v[136:139], v[80:83], v[52:55]\n"
"ds_read_b128 v[76:79], v95 offset:26624\n"
"v_mfma_f32_16x16x32_bf16 v[56:59], v[140:143], v[80:83], v[56:59]\n"
"buffer_load_dwordx4 v[220:223], v1, s[4:7], s52 offen offset:3072 sc1\n"
"s_waitcnt vmcnt(23) lgkmcnt(2)\n"
"v_mfma_f32_16x16x32_bf16 v[28:31], v[144:147], v[84:87], v[28:31]\n"
"ds_read_b128 v[80:83], v3 offset:27648\n"
"s_add_u32 s52, s52, 4096\n"
"v_mfma_f32_16x16x32_bf16 v[32:35], v[148:151], v[84:87], v[32:35]\n"
"v_mfma_f32_16x16x32_bf16 v[36:39], v[144:147], v[68:71], v[36:39]\n"
"ds_read_b128 v[84:87], v3 offset:48128\n"
"buffer_load_dwordx4 v[224:227], v1, s[4:7], s52 offen offset:0 sc1\n"
"v_mfma_f32_16x16x32_bf16 v[40:43], v[148:151], v[68:71], v[40:43]\n"
"v_add_f32 v60, v60, v61\n"
"s_waitcnt lgkmcnt(2)\n"
"v_mfma_f32_16x16x32_bf16 v[44:47], v[144:147], v[72:75], v[44:47]\n"
"ds_read_b128 v[68:71], v95 offset:7168\n"
"v_add_f32 v62, v62, v63\n"
"v_mfma_f32_16x16x32_bf16 v[48:51], v[148:151], v[72:75], v[48:51]\n"
"buffer_load_dwordx4 v[228:231], v1, s[4:7], s52 offen offset:1024 sc1\n"
"v_add_f32 v64, v64, v65\n"
"v_mfma_f32_16x16x32_bf16 v[52:55], v[144:147], v[76:79], v[52:55]\n"
"ds_read_b128 v[72:75], v95 offset:27648\n"
"v_add_f32 v66, v66, v67\n"
"v_mfma_f32_16x16x32_bf16 v[56:59], v[148:151], v[76:79], v[56:59]\n"
"v_add_f32 v64, v64, v66\n"
"s_waitcnt vmcnt(23) lgkmcnt(2)\n"
"v_mfma_f32_16x16x32_bf16 v[28:31], v[152:155], v[80:83], v[28:31]\n"
"ds_read_b128 v[76:79], v3 offset:28672\n"
"buffer_load_dwordx4 v[232:235], v1, s[4:7], s53 offen offset:0 sc1\n"
"v_mfma_f32_16x16x32_bf16 v[32:35], v[156:159], v[80:83], v[32:35]\n"
"v_add_f32 v60, v60, v62\n"
"v_mul_f32 v63, 0x3acccccd, v64\n"
"v_mfma_f32_16x16x32_bf16 v[36:39], v[152:155], v[84:87], v[36:39]\n"
"ds_read_b128 v[80:83], v3 offset:49152\n"
"v_fma_f32 v60, -v64, v63, v60\n"
"v_mfma_f32_16x16x32_bf16 v[40:43], v[156:159], v[84:87], v[40:43]\n"
"buffer_load_dwordx4 v[236:239], v1, s[4:7], s53 offen offset:1024 sc1\n"
"s_waitcnt lgkmcnt(2)\n"
"v_mfma_f32_16x16x32_bf16 v[44:47], v[152:155], v[68:71], v[44:47]\n"
"ds_read_b128 v[84:87], v95 offset:8192\n"
"v_fmamk_f32 v60, v60, 0x3acccccd, v27\n"
"v_mfma_f32_16x16x32_bf16 v[48:51], v[156:159], v[68:71], v[48:51]\n"
"v_max_f32 v60, v60, v27\n"
"v_rsq_f32 v62, v60\n"
"v_mfma_f32_16x16x32_bf16 v[52:55], v[152:155], v[72:75], v[52:55]\n"
"ds_read_b128 v[68:71], v95 offset:28672\n"
"buffer_load_dwordx4 v[240:243], v1, s[4:7], s54 offen offset:0 sc1\n"
"v_mfma_f32_16x16x32_bf16 v[56:59], v[156:159], v[72:75], v[56:59]\n"
"v_mul_f32 v63, -v63, v62\n"
"s_waitcnt vmcnt(24) lgkmcnt(2)\n"
"v_mfma_f32_16x16x32_bf16 v[28:31], v[160:163], v[76:79], v[28:31]\n"
"ds_read_b128 v[72:75], v3 offset:29696\n"
"v_mfma_f32_16x16x32_bf16 v[32:35], v[164:167], v[76:79], v[32:35]\n"
"buffer_load_dwordx4 v[244:247], v1, s[4:7], s54 offen offset:1024 sc1\n"
"v_mfma_f32_16x16x32_bf16 v[36:39], v[160:163], v[80:83], v[36:39]\n"
"ds_read_b128 v[76:79], v3 offset:50176\n"
"v_mfma_f32_16x16x32_bf16 v[40:43], v[164:167], v[80:83], v[40:43]\n"
"ds_write_b64 v16, v[62:63] offset:0\n"
"s_waitcnt lgkmcnt(3)\n"
"v_mfma_f32_16x16x32_bf16 v[44:47], v[160:163], v[84:87], v[44:47]\n"
"ds_read_b128 v[80:83], v95 offset:9216\n"
"buffer_load_dwordx4 v[248:251], v1, s[4:7], s55 offen offset:0 sc1\n"
"v_mfma_f32_16x16x32_bf16 v[48:51], v[164:167], v[84:87], v[48:51]\n"
"v_mfma_f32_16x16x32_bf16 v[52:55], v[160:163], v[68:71], v[52:55]\n"
"ds_read_b128 v[84:87], v95 offset:29696\n"
"v_mfma_f32_16x16x32_bf16 v[56:59], v[164:167], v[68:71], v[56:59]\n"
"buffer_load_dwordx4 v[252:255], v1, s[4:7], s55 offen offset:1024 sc1\n"
"ds_read2st64_b32 v[60:61], v17 offset0:4 offset1:12\n"
"s_waitcnt vmcnt(25) lgkmcnt(4)\n"
"v_mfma_f32_16x16x32_bf16 v[28:31], v[168:171], v[72:75], v[28:31]\n"
"ds_read_b128 v[68:71], v3 offset:30720\n"
"s_mov_b32 m0, 163328\n"
"v_mfma_f32_16x16x32_bf16 v[32:35], v[172:175], v[72:75], v[32:35]\n"
"ds_read2st64_b32 v[62:63], v17 offset0:20 offset1:28\n"
"v_mfma_f32_16x16x32_bf16 v[36:39], v[168:171], v[76:79], v[36:39]\n"
"ds_read_b128 v[72:75], v3 offset:51200\n"
"buffer_load_dword v89, s[8:11], 0 offen lds\n"
"v_mfma_f32_16x16x32_bf16 v[40:43], v[172:175], v[76:79], v[40:43]\n"
"ds_read2st64_b32 v[64:65], v18 offset0:4 offset1:12\n"
"s_waitcnt lgkmcnt(5)\n"
"v_mfma_f32_16x16x32_bf16 v[44:47], v[168:171], v[80:83], v[44:47]\n"
"ds_read_b128 v[76:79], v95 offset:10240\n"
"ds_write_b32 v13, v13 offset:32\n"
"v_mfma_f32_16x16x32_bf16 v[48:51], v[172:175], v[80:83], v[48:51]\n"
"ds_read2st64_b32 v[66:67], v18 offset0:20 offset1:28\n"
"v_mfma_f32_16x16x32_bf16 v[52:55], v[168:171], v[84:87], v[52:55]\n"
"ds_read_b128 v[80:83], v95 offset:30720\n"
"v_mfma_f32_16x16x32_bf16 v[56:59], v[172:175], v[84:87], v[56:59]\n"
"s_waitcnt vmcnt(23) lgkmcnt(5)\n"
"v_mfma_f32_16x16x32_bf16 v[28:31], v[176:179], v[68:71], v[28:31]\n"
"ds_read_b128 v[84:87], v3 offset:31744\n"
"v_mfma_f32_16x16x32_bf16 v[32:35], v[180:183], v[68:71], v[32:35]\n"
"v_mfma_f32_16x16x32_bf16 v[36:39], v[176:179], v[72:75], v[36:39]\n"
"ds_read_b128 v[68:71], v3 offset:52224\n"
"v_mfma_f32_16x16x32_bf16 v[40:43], v[180:183], v[72:75], v[40:43]\n"
"s_waitcnt lgkmcnt(2)\n"
"v_mfma_f32_16x16x32_bf16 v[44:47], v[176:179], v[76:79], v[44:47]\n"
"ds_read_b128 v[72:75], v95 offset:11264\n"
"v_mfma_f32_16x16x32_bf16 v[48:51], v[180:183], v[76:79], v[48:51]\n"
"s_waitcnt vmcnt(13)\n"
"ds_write_b32 v13, v13 offset:48\n"
"v_mfma_f32_16x16x32_bf16 v[52:55], v[176:179], v[80:83], v[52:55]\n"
"ds_read_b128 v[76:79], v95 offset:31744\n"
"v_mfma_f32_16x16x32_bf16 v[56:59], v[180:183], v[80:83], v[56:59]\n"
"v_add_f32 v60, v60, v61\n"
"s_waitcnt lgkmcnt(3)\n"
"v_mfma_f32_16x16x32_bf16 v[28:31], v[184:187], v[84:87], v[28:31]\n"
"ds_read_b128 v[80:83], v3 offset:32768\n"
"v_add_f32 v62, v62, v63\n"
"v_mfma_f32_16x16x32_bf16 v[32:35], v[188:191], v[84:87], v[32:35]\n"
"v_add_f32 v64, v64, v65\n"
"v_add_f32 v66, v66, v67\n"
"v_mfma_f32_16x16x32_bf16 v[36:39], v[184:187], v[68:71], v[36:39]\n"
"ds_read_b128 v[84:87], v3 offset:53248\n"
"v_add_f32 v64, v64, v66\n"
"v_mfma_f32_16x16x32_bf16 v[40:43], v[188:191], v[68:71], v[40:43]\n"
"v_add_f32 v60, v60, v62\n"
"s_waitcnt lgkmcnt(2)\n"
"v_mfma_f32_16x16x32_bf16 v[44:47], v[184:187], v[72:75], v[44:47]\n"
"ds_read_b128 v[68:71], v95 offset:12288\n"
"v_mul_f32 v63, 0x3acccccd, v64\n"
"v_mfma_f32_16x16x32_bf16 v[48:51], v[188:191], v[72:75], v[48:51]\n"
"v_fma_f32 v60, -v64, v63, v60\n"
"v_fmamk_f32 v60, v60, 0x3acccccd, v27\n"
"v_mfma_f32_16x16x32_bf16 v[52:55], v[184:187], v[76:79], v[52:55]\n"
"ds_read_b128 v[72:75], v95 offset:32768\n"
"v_max_f32 v60, v60, v27\n"
"v_mfma_f32_16x16x32_bf16 v[56:59], v[188:191], v[76:79], v[56:59]\n"
"ds_read_b32 v90, v14 offset:48\n"
"s_waitcnt lgkmcnt(3)\n"
"v_mfma_f32_16x16x32_bf16 v[28:31], v[192:195], v[80:83], v[28:31]\n"
"ds_read_b128 v[76:79], v3 offset:33792\n"
"v_rsq_f32 v62, v60\n"
"v_mfma_f32_16x16x32_bf16 v[32:35], v[196:199], v[80:83], v[32:35]\n"
"v_mul_f32 v63, -v63, v62\n"
"v_cndmask_b32 v16, 2.0, v16, s[40:41]\n"
"v_mfma_f32_16x16x32_bf16 v[36:39], v[192:195], v[84:87], v[36:39]\n"
"ds_read_b128 v[80:83], v3 offset:54272\n"
"v_mfma_f32_16x16x32_bf16 v[40:43], v[196:199], v[84:87], v[40:43]\n"
"s_waitcnt lgkmcnt(3)\n"
"v_mfma_f32_16x16x32_bf16 v[44:47], v[192:195], v[68:71], v[44:47]\n"
"ds_read_b128 v[84:87], v95 offset:13312\n"
"v_mfma_f32_16x16x32_bf16 v[48:51], v[196:199], v[68:71], v[48:51]\n"
"v_mfma_f32_16x16x32_bf16 v[52:55], v[192:195], v[72:75], v[52:55]\n"
"ds_read_b128 v[68:71], v95 offset:33792\n"
"v_mfma_f32_16x16x32_bf16 v[56:59], v[196:199], v[72:75], v[56:59]\n"
"s_waitcnt lgkmcnt(4)\n"
"v_cmp_eq_u32 vcc, 0, v90\n"
"ds_write_b64 v16, v[62:63] offset:512\n"
"s_waitcnt lgkmcnt(3)\n"
"v_mfma_f32_16x16x32_bf16 v[28:31], v[200:203], v[76:79], v[28:31]\n"
"ds_read_b128 v[72:75], v3 offset:34816\n"
"v_mfma_f32_16x16x32_bf16 v[32:35], v[204:207], v[76:79], v[32:35]\n"
"s_cbranch_vccnz .Lsl_18\n"
".Lbk_19:\n"
"ds_write_b32 v13, v13 offset:80\n"
"v_mfma_f32_16x16x32_bf16 v[36:39], v[200:203], v[80:83], v[36:39]\n"
"ds_read_b128 v[76:79], v3 offset:55296\n"
"v_lshrrev_b32 v19, 3, v0\n"
"v_mfma_f32_16x16x32_bf16 v[40:43], v[204:207], v[80:83], v[40:43]\n"
"v_add_u32 v19, s3, v19\n"
"s_waitcnt lgkmcnt(4)\n"
"v_mfma_f32_16x16x32_bf16 v[44:47], v[200:203], v[84:87], v[44:47]\n"
"ds_read_b128 v[80:83], v95 offset:14336\n"
"v_lshlrev_b32 v91, 27, v19\n"
"v_mfma_f32_16x16x32_bf16 v[48:51], v[204:207], v[84:87], v[48:51]\n"
"v_lshl_add_u32 v91, v0, 14, v91\n"
"v_mov_b32 v19, s16\n"
"v_mfma_f32_16x16x32_bf16 v[52:55], v[200:203], v[68:71], v[52:55]\n"
"ds_read_b128 v[84:87], v95 offset:34816\n"
"s_lshr_b32 s79, s2, 2\n"
"v_mfma_f32_16x16x32_bf16 v[56:59], v[204:207], v[68:71], v[56:59]\n"
"s_lshl_b32 s79, s79, 5\n"
"s_waitcnt vmcnt(11) lgkmcnt(2)\n"
"v_mfma_f32_16x16x32_bf16 v[28:31], v[208:211], v[72:75], v[28:31]\n"
"ds_read_b128 v[68:71], v3 offset:35840\n"
"s_and_b32 s78, s2, 3\n"
"v_mfma_f32_16x16x32_bf16 v[32:35], v[212:215], v[72:75], v[32:35]\n"
"s_lshl_b32 s78, s78, 2\n"
"s_add_u32 s79, s79, s78\n"
"v_mfma_f32_16x16x32_bf16 v[36:39], v[208:211], v[76:79], v[36:39]\n"
"ds_read_b128 v[72:75], v3 offset:56320\n"
"s_add_u32 s32, s79, 0x4000\n"
"v_mfma_f32_16x16x32_bf16 v[40:43], v[212:215], v[76:79], v[40:43]\n"
"buffer_store_dword v19, v91, s[4:7], s32 offen sc1\n"
"s_waitcnt lgkmcnt(2)\n"
"v_mfma_f32_16x16x32_bf16 v[44:47], v[208:211], v[80:83], v[44:47]\n"
"ds_read_b128 v[76:79], v95 offset:15360\n"
"v_mfma_f32_16x16x32_bf16 v[48:51], v[212:215], v[80:83], v[48:51]\n"
"v_mfma_f32_16x16x32_bf16 v[52:55], v[208:211], v[84:87], v[52:55]\n"
"ds_read_b128 v[80:83], v95 offset:35840\n"
"v_mfma_f32_16x16x32_bf16 v[56:59], v[212:215], v[84:87], v[56:59]\n"
"s_waitcnt vmcnt(10) lgkmcnt(2)\n"
"v_mfma_f32_16x16x32_bf16 v[28:31], v[216:219], v[68:71], v[28:31]\n"
"ds_read_b128 v[84:87], v3 offset:36864\n"
"v_mfma_f32_16x16x32_bf16 v[32:35], v[220:223], v[68:71], v[32:35]\n"
"ds_read_b32 v90, v14 offset:80\n"
"v_mfma_f32_16x16x32_bf16 v[36:39], v[216:219], v[72:75], v[36:39]\n"
"ds_read_b128 v[68:71], v4 offset:40960\n"
"v_mfma_f32_16x16x32_bf16 v[40:43], v[220:223], v[72:75], v[40:43]\n"
"s_waitcnt lgkmcnt(3)\n"
"v_mfma_f32_16x16x32_bf16 v[44:47], v[216:219], v[76:79], v[44:47]\n"
"ds_read_b128 v[72:75], v5 offset:40960\n"
"v_mfma_f32_16x16x32_bf16 v[48:51], v[220:223], v[76:79], v[48:51]\n"
"ds_read_b64 v[88:89], v94 offset:0\n"
"v_mfma_f32_16x16x32_bf16 v[52:55], v[216:219], v[80:83], v[52:55]\n"
"ds_read_b128 v[76:79], v6 offset:40960\n"
"v_mfma_f32_16x16x32_bf16 v[56:59], v[220:223], v[80:83], v[56:59]\n"
"s_waitcnt vmcnt(6) lgkmcnt(3)\n"
"v_mfma_f32_16x16x32_bf16 v[28:31], v[224:227], v[84:87], v[28:31]\n"
"ds_read_b128 v[80:83], v3 offset:57344\n"
"v_mfma_f32_16x16x32_bf16 v[32:35], v[228:231], v[84:87], v[32:35]\n"
"ds_read_b64 v[92:93], v94 offset:128\n"
"v_mfma_f32_16x16x32_bf16 v[28:31], v[232:235], v[68:71], v[28:31]\n"
"ds_read_b128 v[84:87], v4 offset:61440\n"
"v_mfma_f32_16x16x32_bf16 v[32:35], v[236:239], v[68:71], v[32:35]\n"
"v_cmp_eq_u32 vcc, 0, v90\n"
"s_waitcnt vmcnt(2) lgkmcnt(3)\n"
"v_mfma_f32_16x16x32_bf16 v[28:31], v[240:243], v[72:75], v[28:31]\n"
"ds_read_b128 v[68:71], v5 offset:61440\n"
"v_mfma_f32_16x16x32_bf16 v[32:35], v[244:247], v[72:75], v[32:35]\n"
"s_cbranch_vccnz .Lsl_30\n"
".Lbk_31:\n"
"v_mfma_f32_16x16x32_bf16 v[28:31], v[248:251], v[76:79], v[28:31]\n"
"ds_read_b128 v[72:75], v6 offset:61440\n"
"v_mfma_f32_16x16x32_bf16 v[32:35], v[252:255], v[76:79], v[32:35]\n"
"s_waitcnt lgkmcnt(2)\n"
"v_mfma_f32_16x16x32_bf16 v[36:39], v[224:227], v[80:83], v[36:39]\n"
"ds_read_b128 v[76:79], v95 offset:16384\n"
"v_mfma_f32_16x16x32_bf16 v[40:43], v[228:231], v[80:83], v[40:43]\n"
"v_mov_b32_dpp v16, v28 row_newbcast:0 row_mask:0xf bank_mask:0xf\n"
"v_mov_b32_dpp v17, v29 row_newbcast:0 row_mask:0xf bank_mask:0xf\n"
"v_mov_b32_dpp v18, v30 row_newbcast:0 row_mask:0xf bank_mask:0xf\n"
"v_mfma_f32_16x16x32_bf16 v[36:39], v[232:235], v[84:87], v[36:39]\n"
"v_add_u32 v4, 0x1c400, v4\n"
"ds_read_b128 v[80:83], v4 offset:0\n"
"v_mov_b32_dpp v19, v31 row_newbcast:0 row_mask:0xf bank_mask:0xf\n"
"v_mfma_f32_16x16x32_bf16 v[40:43], v[236:239], v[84:87], v[40:43]\n"
"v_mov_b32_dpp v20, v32 row_newbcast:0 row_mask:0xf bank_mask:0xf\n"
"v_mov_b32_dpp v21, v33 row_newbcast:0 row_mask:0xf bank_mask:0xf\n"
"s_waitcnt lgkmcnt(2)\n"
"v_mfma_f32_16x16x32_bf16 v[36:39], v[240:243], v[68:71], v[36:39]\n"
"v_add_u32 v5, 0x1c400, v5\n"
"ds_read_b128 v[84:87], v5 offset:0\n"
"v_mfma_f32_16x16x32_bf16 v[40:43], v[244:247], v[68:71], v[40:43]\n"
"v_mov_b32_dpp v22, v34 row_newbcast:0 row_mask:0xf bank_mask:0xf\n"
"v_mov_b32_dpp v23, v35 row_newbcast:0 row_mask:0xf bank_mask:0xf\n"
"v_mul_f32 v28, v88, v28\n"
"v_mfma_f32_16x16x32_bf16 v[36:39], v[248:251], v[72:75], v[36:39]\n"
"v_add_u32 v6, 0x1c400, v6\n"
"ds_read_b128 v[68:71], v6 offset:0\n"
"v_mul_f32 v29, v88, v29\n"
"v_mfma_f32_16x16x32_bf16 v[40:43], v[252:255], v[72:75], v[40:43]\n"
"v_mul_f32 v30, v88, v30\n"
"v_mul_f32 v31, v88, v31\n"
"s_waitcnt lgkmcnt(2)\n"
"v_mfma_f32_16x16x32_bf16 v[44:47], v[224:227], v[76:79], v[44:47]\n"
"ds_read_b128 v[72:75], v95 offset:36864\n"
"v_fmac_f32 v28, v89, v16\n"
"v_fmac_f32 v29, v89, v17\n"
"v_mfma_f32_16x16x32_bf16 v[48:51], v[228:231], v[76:79], v[48:51]\n"
"ds_write_b32 v13, v13 offset:64\n"
"v_fmac_f32 v30, v89, v18\n"
"v_mfma_f32_16x16x32_bf16 v[44:47], v[232:235], v[80:83], v[44:47]\n"
"ds_read_b128 v[76:79], v4 offset:20480\n"
"v_fmac_f32 v31, v89, v19\n"
"v_cvt_pk_bf16_f32 v28, v28, v29\n"
"v_mfma_f32_16x16x32_bf16 v[48:51], v[236:239], v[80:83], v[48:51]\n"
"v_cvt_pk_bf16_f32 v29, v30, v31\n"
"ds_write_b64 v7, v[28:29] offset:0\n"
"s_waitcnt lgkmcnt(4)\n"
"v_mfma_f32_16x16x32_bf16 v[44:47], v[240:243], v[84:87], v[44:47]\n"
"ds_read_b128 v[80:83], v5 offset:20480\n"
"v_mul_f32 v32, v88, v32\n"
"v_mul_f32 v33, v88, v33\n"
"v_mfma_f32_16x16x32_bf16 v[48:51], v[244:247], v[84:87], v[48:51]\n"
"v_mul_f32 v34, v88, v34\n"
"v_mul_f32 v35, v88, v35\n"
"v_fmac_f32 v32, v89, v20\n"
"v_mfma_f32_16x16x32_bf16 v[44:47], v[248:251], v[68:71], v[44:47]\n"
"ds_read_b128 v[84:87], v6 offset:20480\n"
"v_fmac_f32 v33, v89, v21\n"
"v_fmac_f32 v34, v89, v22\n"
"v_mfma_f32_16x16x32_bf16 v[48:51], v[252:255], v[68:71], v[48:51]\n"
"v_fmac_f32 v35, v89, v23\n"
"v_cvt_pk_bf16_f32 v32, v32, v33\n"
"s_waitcnt lgkmcnt(3)\n"
"v_mfma_f32_16x16x32_bf16 v[52:55], v[224:227], v[72:75], v[52:55]\n"
"v_add_u32 v3, 0xffffb000, v3\n"
"ds_read_b128 v[68:71], v3 offset:0\n"
"v_cvt_pk_bf16_f32 v33, v34, v35\n"
"v_mfma_f32_16x16x32_bf16 v[56:59], v[228:231], v[72:75], v[56:59]\n"
"ds_read_b32 v90, v14 offset:64\n"
"ds_write_b64 v8, v[32:33] offset:0\n"
"v_mfma_f32_16x16x32_bf16 v[52:55], v[232:235], v[76:79], v[52:55]\n"
"ds_read_b128 v[72:75], v3 offset:1024\n"
"ds_read_b64 v[88:89], v94 offset:256\n"
"v_mul_f32 v36, v92, v36\n"
"v_mfma_f32_16x16x32_bf16 v[56:59], v[236:239], v[76:79], v[56:59]\n"
"v_mul_f32 v37, v92, v37\n"
"v_mul_f32 v38, v92, v38\n"
"s_waitcnt lgkmcnt(5)\n"
"v_mfma_f32_16x16x32_bf16 v[52:55], v[240:243], v[80:83], v[52:55]\n"
"ds_read_b128 v[76:79], v3 offset:2048\n"
"v_mul_f32 v39, v92, v39\n"
"v_fmac_f32 v36, v93, v16\n"
"v_mfma_f32_16x16x32_bf16 v[56:59], v[244:247], v[80:83], v[56:59]\n"
"v_fmac_f32 v37, v93, v17\n"
"v_fmac_f32 v38, v93, v18\n"
"v_fmac_f32 v39, v93, v19\n"
"v_mfma_f32_16x16x32_bf16 v[52:55], v[248:251], v[84:87], v[52:55]\n"
"ds_read_b128 v[80:83], v3 offset:3072\n"
"v_cvt_pk_bf16_f32 v36, v36, v37\n"
"v_cvt_pk_bf16_f32 v37, v38, v39\n"
"v_mfma_f32_16x16x32_bf16 v[56:59], v[252:255], v[84:87], v[56:59]\n"
"ds_write_b64 v7, v[36:37] offset:3968\n"
"s_waitcnt lgkmcnt(4)\n"
"v_mfma_f32_16x16x32_bf16 v[60:63], v[96:99], v[68:71], 0\n"
"ds_read_b128 v[84:87], v3 offset:4096\n"
"v_mul_f32 v40, v92, v40\n"
"v_mul_f32 v41, v92, v41\n"
"v_mfma_f32_16x16x32_bf16 v[64:67], v[100:103], v[68:71], 0\n"
"v_cmp_eq_u32 vcc, 0, v90\n"
"v_mul_f32 v42, v92, v42\n"
"v_mul_f32 v43, v92, v43\n"
"v_mfma_f32_16x16x32_bf16 v[60:63], v[104:107], v[72:75], v[60:63]\n"
"ds_read_b128 v[68:71], v3 offset:5120\n"
"v_fmac_f32 v40, v93, v20\n"
"v_fmac_f32 v41, v93, v21\n"
"v_mfma_f32_16x16x32_bf16 v[64:67], v[108:111], v[72:75], v[64:67]\n"
"s_cbranch_vccnz .Lsl_22\n"
".Lbk_23:\n"
"s_waitcnt lgkmcnt(3)\n"
"v_mfma_f32_16x16x32_bf16 v[60:63], v[112:115], v[76:79], v[60:63]\n"
"ds_read_b128 v[72:75], v3 offset:6144\n"
"v_fmac_f32 v42, v93, v22\n"
"v_fmac_f32 v43, v93, v23\n"
"v_mfma_f32_16x16x32_bf16 v[64:67], v[116:119], v[76:79], v[64:67]\n"
"v_cvt_pk_bf16_f32 v40, v40, v41\n"
"v_cvt_pk_bf16_f32 v41, v42, v43\n"
"ds_write_b64 v8, v[40:41] offset:3968\n"
"v_mfma_f32_16x16x32_bf16 v[60:63], v[120:123], v[80:83], v[60:63]\n"
"ds_read_b128 v[76:79], v3 offset:7168\n"
"ds_read_b64 v[92:93], v94 offset:384\n"
"v_mul_f32 v44, v88, v44\n"
"v_mfma_f32_16x16x32_bf16 v[64:67], v[124:127], v[80:83], v[64:67]\n"
"s_add_u32 s31, s28, 0x140000\n"
"v_mul_f32 v45, v88, v45\n"
"s_waitcnt lgkmcnt(4)\n"
"v_mfma_f32_16x16x32_bf16 v[60:63], v[128:131], v[84:87], v[60:63]\n"
"ds_read_b128 v[80:83], v3 offset:8192\n"
"s_add_u32 m0, s29, 0xa000\n"
"v_mul_f32 v46, v88, v46\n"
"v_mfma_f32_16x16x32_bf16 v[64:67], v[132:135], v[84:87], v[64:67]\n"
"buffer_load_dwordx4 v2, s[8:11], s31 offen offset:0 lds\n"
"v_mul_f32 v47, v88, v47\n"
"v_fmac_f32 v44, v89, v16\n"
"v_mfma_f32_16x16x32_bf16 v[60:63], v[136:139], v[68:71], v[60:63]\n"
"ds_read_b128 v[84:87], v3 offset:9216\n"
"s_add_u32 m0, s29, 0xa3c0\n"
"v_fmac_f32 v45, v89, v17\n"
"v_mfma_f32_16x16x32_bf16 v[64:67], v[140:143], v[68:71], v[64:67]\n"
"v_fmac_f32 v46, v89, v18\n"
"v_fmac_f32 v47, v89, v19\n"
"s_waitcnt lgkmcnt(3)\n"
"v_mfma_f32_16x16x32_bf16 v[60:63], v[144:147], v[72:75], v[60:63]\n"
"ds_read_b128 v[68:71], v3 offset:10240\n"
"buffer_load_dwordx4 v2, s[8:11], s31 offen offset:64 lds\n"
"v_cvt_pk_bf16_f32 v44, v44, v45\n"
"v_mfma_f32_16x16x32_bf16 v[64:67], v[148:151], v[72:75], v[64:67]\n"
"s_add_u32 m0, s29, 0xa780\n"
"v_cvt_pk_bf16_f32 v45, v46, v47\n"
"ds_write_b64 v7, v[44:45] offset:7936\n"
"v_mfma_f32_16x16x32_bf16 v[60:63], v[152:155], v[76:79], v[60:63]\n"
"ds_read_b128 v[72:75], v3 offset:11264\n"
"v_mul_f32 v48, v88, v48\n"
"v_mul_f32 v49, v88, v49\n"
"v_mfma_f32_16x16x32_bf16 v[64:67], v[156:159], v[76:79], v[64:67]\n"
"buffer_load_dwordx4 v2, s[8:11], s31 offen offset:128 lds\n"
"v_mul_f32 v50, v88, v50\n"
"s_waitcnt lgkmcnt(3)\n"
"v_mfma_f32_16x16x32_bf16 v[60:63], v[160:163], v[80:83], v[60:63]\n"
"ds_read_b128 v[76:79], v3 offset:12288\n"
"s_add_u32 m0, s29, 0xab40\n"
"v_mul_f32 v51, v88, v51\n"
"v_mfma_f32_16x16x32_bf16 v[64:67], v[164:167], v[80:83], v[64:67]\n"
"v_fmac_f32 v48, v89, v20\n"
"v_fmac_f32 v49, v89, v21\n"
"v_fmac_f32 v50, v89, v22\n"
"v_mfma_f32_16x16x32_bf16 v[60:63], v[168:171], v[84:87], v[60:63]\n"
"ds_read_b128 v[80:83], v3 offset:13312\n"
"buffer_load_dwordx4 v2, s[8:11], s31 offen offset:192 lds\n"
"v_fmac_f32 v51, v89, v23\n"
"v_mfma_f32_16x16x32_bf16 v[64:67], v[172:175], v[84:87], v[64:67]\n"
"s_add_u32 m0, s29, 0xaf00\n"
"v_cvt_pk_bf16_f32 v48, v48, v49\n"
"s_waitcnt lgkmcnt(2)\n"
"v_mfma_f32_16x16x32_bf16 v[60:63], v[176:179], v[68:71], v[60:63]\n"
"ds_read_b128 v[84:87], v3 offset:14336\n"
"v_cvt_pk_bf16_f32 v49, v50, v51\n"
"ds_write_b64 v8, v[48:49] offset:7936\n"
"v_mfma_f32_16x16x32_bf16 v[64:67], v[180:183], v[68:71], v[64:67]\n"
"buffer_load_dwordx4 v2, s[8:11], s31 offen offset:256 lds\n"
"ds_read_b64 v[88:89], v94 offset:512\n"
"v_mul_f32 v52, v92, v52\n"
"v_mfma_f32_16x16x32_bf16 v[60:63], v[184:187], v[72:75], v[60:63]\n"
"ds_read_b128 v[68:71], v3 offset:15360\n"
"s_add_u32 s31, s28, 0x145000\n"
"v_mul_f32 v53, v92, v53\n"
"v_mfma_f32_16x16x32_bf16 v[64:67], v[188:191], v[72:75], v[64:67]\n"
"s_add_u32 m0, s29, 0xf000\n"
"v_mul_f32 v54, v92, v54\n"
"s_waitcnt lgkmcnt(4)\n"
"v_mfma_f32_16x16x32_bf16 v[60:63], v[192:195], v[76:79], v[60:63]\n"
"ds_read_b128 v[72:75], v3 offset:16384\n"
"buffer_load_dwordx4 v2, s[8:11], s31 offen offset:0 lds\n"
"v_mul_f32 v55, v92, v55\n"
"v_mfma_f32_16x16x32_bf16 v[64:67], v[196:199], v[76:79], v[64:67]\n"
"s_add_u32 m0, s29, 0xf3c0\n"
"v_fmac_f32 v52, v93, v16\n"
"v_fmac_f32 v53, v93, v17\n"
"v_mfma_f32_16x16x32_bf16 v[60:63], v[200:203], v[80:83], v[60:63]\n"
"v_add_u32 v4, 0xfffe3c00, v4\n"
"ds_read_b128 v[76:79], v4 offset:0\n"
"v_fmac_f32 v54, v93, v18\n"
"v_mfma_f32_16x16x32_bf16 v[64:67], v[204:207], v[80:83], v[64:67]\n"
"buffer_load_dwordx4 v2, s[8:11], s31 offen offset:64 lds\n"
"v_fmac_f32 v55, v93, v19\n"
"s_waitcnt lgkmcnt(2)\n"
"v_mfma_f32_16x16x32_bf16 v[60:63], v[208:211], v[84:87], v[60:63]\n"
"v_add_u32 v5, 0xfffe3c00, v5\n"
"ds_read_b128 v[80:83], v5 offset:0\n"
"v_cvt_pk_bf16_f32 v52, v52, v53\n"
"v_mfma_f32_16x16x32_bf16 v[64:67], v[212:215], v[84:87], v[64:67]\n"
"s_add_u32 m0, s29, 0xf780\n"
"v_cvt_pk_bf16_f32 v53, v54, v55\n"
"ds_write_b64 v7, v[52:53] offset:11904\n"
"v_mfma_f32_16x16x32_bf16 v[60:63], v[216:219], v[68:71], v[60:63]\n"
"v_add_u32 v6, 0xfffe3c00, v6\n"
"ds_read_b128 v[84:87], v6 offset:0\n"
"buffer_load_dwordx4 v2, s[8:11], s31 offen offset:128 lds\n"
"v_mfma_f32_16x16x32_bf16 v[64:67], v[220:223], v[68:71], v[64:67]\n"
"s_add_u32 m0, s29, 0xfb40\n"
"v_mul_f32 v56, v92, v56\n"
"s_waitcnt lgkmcnt(3)\n"
"v_mfma_f32_16x16x32_bf16 v[60:63], v[224:227], v[72:75], v[60:63]\n"
"v_mul_f32 v57, v92, v57\n"
"v_mul_f32 v58, v92, v58\n"
"v_mul_f32 v59, v92, v59\n"
"v_mfma_f32_16x16x32_bf16 v[64:67], v[228:231], v[72:75], v[64:67]\n"
"buffer_load_dwordx4 v2, s[8:11], s31 offen offset:192 lds\n"
"v_fmac_f32 v56, v93, v20\n"
"v_fmac_f32 v57, v93, v21\n"
"v_mfma_f32_16x16x32_bf16 v[60:63], v[232:235], v[76:79], v[60:63]\n"
"s_add_u32 m0, s29, 0xff00\n"
"v_fmac_f32 v58, v93, v22\n"
"v_mfma_f32_16x16x32_bf16 v[64:67], v[236:239], v[76:79], v[64:67]\n"
"v_fmac_f32 v59, v93, v23\n"
"s_waitcnt lgkmcnt(0)\n"
"v_mfma_f32_16x16x32_bf16 v[60:63], v[240:243], v[80:83], v[60:63]\n"
"buffer_load_dwordx4 v2, s[8:11], s31 offen offset:256 lds\n"
"v_cvt_pk_bf16_f32 v56, v56, v57\n"
"v_mfma_f32_16x16x32_bf16 v[64:67], v[244:247], v[80:83], v[64:67]\n"
"v_cvt_pk_bf16_f32 v57, v58, v59\n"
"ds_write_b64 v8, v[56:57] offset:11904\n"
"v_mfma_f32_16x16x32_bf16 v[60:63], v[248:251], v[84:87], v[60:63]\n"
"v_mfma_f32_16x16x32_bf16 v[64:67], v[252:255], v[84:87], v[64:67]\n"
"s_nop 4\n"
"v_mul_f32 v60, v88, v60\n"
"v_mul_f32 v61, v88, v61\n"
"v_mul_f32 v62, v88, v62\n"
"v_mul_f32 v63, v88, v63\n"
"v_fmac_f32 v60, v89, v16\n"
"v_fmac_f32 v61, v89, v17\n"
"v_fmac_f32 v62, v89, v18\n"
"v_fmac_f32 v63, v89, v19\n"
"v_cvt_pk_bf16_f32 v60, v60, v61\n"
"v_cvt_pk_bf16_f32 v61, v62, v63\n"
"ds_write_b64 v7, v[60:61] offset:15872\n"
"v_mul_f32 v64, v88, v64\n"
"v_mul_f32 v65, v88, v65\n"
"v_mul_f32 v66, v88, v66\n"
"v_mul_f32 v67, v88, v67\n"
"v_fmac_f32 v64, v89, v20\n"
"v_fmac_f32 v65, v89, v21\n"
"v_fmac_f32 v66, v89, v22\n"
"v_fmac_f32 v67, v89, v23\n"
"v_cvt_pk_bf16_f32 v64, v64, v65\n"
"v_cvt_pk_bf16_f32 v65, v66, v67\n"
"ds_write_b64 v8, v[64:65] offset:15872\n"
"ds_write_b32 v13, v13 offset:96\n"
"s_cmp_lt_i32 s3, 2\n"
"s_cselect_b32 s33, 1, 0\n"
"v_mov_b32 v46, 0\n"
"v_mov_b32 v47, 0\n"
"v_mov_b32 v50, 0\n"
"v_mov_b32 v51, 0\n"
"v_mov_b32 v72, s42\n"
"v_mov_b32 v73, s42\n"
"v_mov_b32 v74, s42\n"
"v_mov_b32 v75, s42\n"
"s_mov_b32 s17, 0x100000\n"
".Ltp_34:\n"
"ds_read_b32 v90, v14 offset:96\n"
"s_sub_u32 s17, s17, 1\n"
"s_cbranch_scc1 .Lou_35\n"
"s_waitcnt lgkmcnt(0)\n"
"v_cmp_eq_u32 vcc, 0, v90\n"
"s_cbranch_vccnz .Ltp_34\n"
".Lou_35:\n"
"s_waitcnt lgkmcnt(0)\n"
"s_mov_b64 exec, 0xffff\n"
"ds_read_b64 v[46:47], v24 offset:4000\n"
"ds_read_b64 v[50:51], v24 offset:4040\n"
"s_mov_b64 exec, -1\n"
"ds_read_b64 v[44:45], v25 offset:3968\n"
"ds_read_b64 v[56:57], v25 offset:80\n"
"ds_read_b64 v[58:59], v24 offset:112\n"
"ds_read_b64 v[64:65], v25 offset:4048\n"
"ds_read_b64 v[66:67], v24 offset:4080\n"
"ds_read_b64 v[48:49], v25 offset:4008\n"
"ds_read_b64 v[60:61], v25 offset:120\n"
"ds_read_b64 v[62:63], v24 offset:152\n"
"ds_read_b64 v[68:69], v25 offset:4088\n"
"ds_read_b64 v[70:71], v24 offset:4120\n"
"s_add_u32 s31, s28, 0x14a000\n"
"s_add_u32 m0, s29, 0\n"
"s_nop 0\n"
"buffer_load_dwordx4 v2, s[8:11], s31 offen offset:0 lds\n"
"ds_read_b64_tr_b16 v[28:29], v26 offset:0\n"
"ds_read_b64_tr_b16 v[30:31], v26 offset:3968\n"
"ds_read_b64_tr_b16 v[32:33], v26 offset:40\n"
"v_mov_b32 v52, 0x0c0c0c0c\n"
"v_mov_b32 v53, 0xc0c0100\n"
"v_mov_b32 v54, 0x1000c0c\n"
"v_cndmask_b32 v53, v53, v54, s[34:35]\n"
"v_cndmask_b32 v80, v53, v52, s[36:37]\n"
"v_cndmask_b32 v82, v52, v53, s[36:37]\n"
"v_mov_b32 v53, 0xc0c0302\n"
"v_mov_b32 v54, 0x3020c0c\n"
"v_cndmask_b32 v53, v53, v54, s[34:35]\n"
"v_cndmask_b32 v81, v53, v52, s[38:39]\n"
"v_cndmask_b32 v83, v52, v53, s[38:39]\n"
"s_waitcnt lgkmcnt(10)\n"
"v_mfma_f32_16x16x32_bf16 v[56:59], v[56:59], v[44:47], v[72:75]\n"
"s_waitcnt lgkmcnt(8)\n"
"v_mfma_f32_16x16x32_bf16 v[64:67], v[64:67], v[44:47], 0\n"
"ds_read_b64_tr_b16 v[34:35], v26 offset:4008\n"
"s_add_u32 m0, s29, 0x3c0\n"
"s_nop 0\n"
"buffer_load_dwordx4 v2, s[8:11], s31 offen offset:64 lds\n"
"ds_read_b64_tr_b16 v[36:37], v26 offset:32\n"
"ds_read_b64_tr_b16 v[38:39], v26 offset:4000\n"
"s_waitcnt lgkmcnt(8)\n"
"v_mfma_f32_16x16x32_bf16 v[60:63], v[60:63], v[48:51], v[72:75]\n"
"s_waitcnt lgkmcnt(6)\n"
"v_mfma_f32_16x16x32_bf16 v[68:71], v[68:71], v[48:51], 0\n"
"ds_read_b64_tr_b16 v[40:41], v26 offset:72\n"
"ds_read_b64_tr_b16 v[42:43], v26 offset:4040\n"
"v_mov_b32 v84, 0x3f803f80\n"
"v_mov_b32 v85, 0x3f803f80\n"
"v_mov_b32 v86, 0x3f803f80\n"
"v_mov_b32 v87, 0x3f803f80\n"
"v_cndmask_b32 v76, v56, v57, s[34:35]\n"
"v_cndmask_b32 v72, v64, v65, s[34:35]\n"
"v_cndmask_b32 v76, v76, v72, s[36:37]\n"
"v_cndmask_b32 v77, v58, v59, s[34:35]\n"
"v_cndmask_b32 v72, v66, v67, s[34:35]\n"
"v_cndmask_b32 v77, v77, v72, s[38:39]\n"
"v_max_f32 v52, v76, v77\n"
"s_add_u32 m0, s29, 0x780\n"
"s_nop 0\n"
"buffer_load_dwordx4 v2, s[8:11], s31 offen offset:128 lds\n"
"v_max_f32 v54, v76, v77\n"
"v_cndmask_b32 v78, v60, v61, s[34:35]\n"
"v_cndmask_b32 v72, v68, v69, s[34:35]\n"
"v_cndmask_b32 v78, v78, v72, s[36:37]\n"
"v_cndmask_b32 v79, v62, v63, s[34:35]\n"
"v_cndmask_b32 v72, v70, v71, s[34:35]\n"
"v_cndmask_b32 v79, v79, v72, s[38:39]\n"
"v_max_f32 v53, v78, v79\n"
"v_max_f32 v55, v78, v79\n"
"v_permlane16_swap_b32 v52, v54\n"
"s_nop 0\n"
"v_permlane16_swap_b32 v53, v55\n"
"v_max_f32 v72, v52, v54\n"
"v_max_f32 v52, v52, v54\n"
"v_max_f32 v73, v53, v55\n"
"v_max_f32 v53, v53, v55\n"
"v_permlane32_swap_b32 v52, v72\n"
"s_nop 0\n"
"v_permlane32_swap_b32 v53, v73\n"
"s_add_u32 m0, s29, 0xb40\n"
"s_nop 0\n"
"buffer_load_dwordx4 v2, s[8:11], s31 offen offset:192 lds\n"
"v_max_f32 v52, v52, v72\n"
"v_max_f32 v53, v53, v73\n"
"v_mul_f32 v52, 0xbea52b5b, v52\n"
"v_mul_f32 v53, 0xbea52b5b, v53\n"
"v_fmamk_f32 v76, v76, 0x3ea52b5b, v52\n"
"v_fmamk_f32 v77, v77, 0x3ea52b5b, v52\n"
"v_fmamk_f32 v78, v78, 0x3ea52b5b, v53\n"
"v_fmamk_f32 v79, v79, 0x3ea52b5b, v53\n"
"v_exp_f32 v76, v76\n"
"v_exp_f32 v77, v77\n"
"v_exp_f32 v78, v78\n"
"v_exp_f32 v79, v79\n"
"v_cvt_pk_bf16_f32 v52, v76, v77\n"
"v_cvt_pk_bf16_f32 v53, v78, v79\n"
"v_perm_b32 v44, v52, v52, v80\n"
"v_perm_b32 v45, v52, v52, v81\n"
"v_perm_b32 v46, v52, v52, v82\n"
"s_add_u32 m0, s29, 0xf00\n"
"s_nop 0\n"
"buffer_load_dwordx4 v2, s[8:11], s31 offen offset:256 lds\n"
"v_perm_b32 v47, v52, v52, v83\n"
"v_perm_b32 v48, v53, v53, v80\n"
"v_perm_b32 v49, v53, v53, v81\n"
"v_perm_b32 v50, v53, v53, v82\n"
"v_perm_b32 v51, v53, v53, v83\n"
"s_waitcnt lgkmcnt(0)\n"
"s_waitcnt vmcnt(5)\n"
"ds_write_b32 v13, v13 offset:112\n"
"v_mfma_f32_16x16x32_bf16 v[56:59], v[84:87], v[44:47], 0\n"
"v_mfma_f32_16x16x32_bf16 v[60:63], v[84:87], v[48:51], 0\n"
"v_mfma_f32_16x16x32_bf16 v[28:31], v[28:31], v[44:47], 0\n"
"v_mfma_f32_16x16x32_bf16 v[32:35], v[32:35], v[48:51], 0\n"
"v_mfma_f32_16x16x32_bf16 v[36:39], v[36:39], v[44:47], 0\n"
"v_mfma_f32_16x16x32_bf16 v[40:43], v[40:43], v[48:51], 0\n"
"v_cndmask_b32 v88, 2.0, v1, s[40:41]\n"
"v_rcp_f32 v52, v56\n"
"v_rcp_f32 v53, v60\n"
"s_mov_b32 s17, 0x100000\n"
".Ltp_36:\n"
"ds_read_b32 v90, v14 offset:112\n"
"s_sub_u32 s17, s17, 1\n"
"s_cbranch_scc1 .Lou_37\n"
"s_waitcnt lgkmcnt(0)\n"
"v_cmp_eq_u32 vcc, 0, v90\n"
"s_cbranch_vccnz .Ltp_36\n"
".Lou_37:\n"
"s_waitcnt lgkmcnt(0)\n"
"ds_read_b128 v[56:59], v3 offset:40960\n"
"s_mov_b32 s31, s30\n"
"v_mul_f32 v28, v52, v28\n"
"v_mul_f32 v29, v52, v29\n"
"v_mul_f32 v30, v52, v30\n"
"v_mul_f32 v31, v52, v31\n"
"v_mul_f32 v32, v53, v32\n"
"ds_read_b128 v[60:63], v3 offset:41984\n"
"v_mul_f32 v33, v53, v33\n"
"v_mul_f32 v34, v53, v34\n"
"v_mul_f32 v35, v53, v35\n"
"v_cvt_pk_bf16_f32 v28, v28, v29\n"
"v_cvt_pk_bf16_f32 v29, v30, v31\n"
"v_cvt_pk_bf16_f32 v30, v32, v33\n"
"ds_read_b128 v[64:67], v3 offset:43008\n"
"v_cvt_pk_bf16_f32 v31, v34, v35\n"
"v_min_u32 v28, -2, v28\n"
"v_min_u32 v29, -2, v29\n"
"v_min_u32 v30, -2, v30\n"
"v_min_u32 v31, -2, v31\n"
"buffer_store_dwordx4 v[28:31], v1, s[4:7], s31 offen offset:0 sc1\n"
"ds_read_b128 v[68:71], v3 offset:44032\n"
"v_mul_f32 v36, v52, v36\n"
"v_mul_f32 v37, v52, v37\n"
"v_mul_f32 v38, v52, v38\n"
"v_mul_f32 v39, v52, v39\n"
"v_mul_f32 v40, v53, v40\n"
"v_mul_f32 v41, v53, v41\n"
"ds_read_b128 v[72:75], v3 offset:45056\n"
"v_mul_f32 v42, v53, v42\n"
"v_mul_f32 v43, v53, v43\n"
"v_cvt_pk_bf16_f32 v36, v36, v37\n"
"v_cvt_pk_bf16_f32 v37, v38, v39\n"
"v_cvt_pk_bf16_f32 v38, v40, v41\n"
"v_cvt_pk_bf16_f32 v39, v42, v43\n"
"v_min_u32 v36, -2, v36\n"
"v_min_u32 v37, -2, v37\n"
"v_min_u32 v38, -2, v38\n"
"v_min_u32 v39, -2, v39\n"
"buffer_store_dwordx4 v[36:39], v88, s[4:7], s31 offen offset:1024 sc1\n"
"ds_write_b32 v13, v13 offset:128\n"
"s_mul_i32 s79, s43, 0x1400\n"
"v_lshrrev_b32 v92, 2, v0\n"
"v_and_b32 v91, 3, v0\n"
"v_lshlrev_b32 v91, 4, v91\n"
"v_lshl_add_u32 v92, v92, 10, v91\n"
"v_add_u32 v92, s79, v92\n"
"v_mov_b32 v91, 0x40000000\n"
"v_cmp_gt_i32 vcc, 20, v0\n"
"s_nop 1\n"
"v_cndmask_b32 v93, v91, v92, vcc\n"
"s_waitcnt lgkmcnt(4)\n"
"v_mfma_f32_16x16x32_bf16 v[28:31], v[96:99], v[56:59], 0\n"
"ds_read_b128 v[76:79], v3 offset:46080\n"
"s_add_u32 s31, s28, 0x14f000\n"
"v_mfma_f32_16x16x32_bf16 v[32:35], v[100:103], v[56:59], 0\n"
"s_add_u32 m0, s29, 0x5000\n"
"v_mfma_f32_16x16x32_bf16 v[52:55], v[56:59], v[56:59], 0\n"
"v_mfma_f32_16x16x32_bf16 v[28:31], v[104:107], v[60:63], v[28:31]\n"
"ds_read_b128 v[56:59], v3 offset:47104\n"
"buffer_load_dwordx4 v2, s[8:11], s31 offen offset:0 lds\n"
"v_mfma_f32_16x16x32_bf16 v[32:35], v[108:111], v[60:63], v[32:35]\n"
"s_add_u32 m0, s29, 0x53c0\n"
"s_waitcnt lgkmcnt(4)\n"
"v_mfma_f32_16x16x32_bf16 v[28:31], v[112:115], v[64:67], v[28:31]\n"
"ds_read_b128 v[60:63], v3 offset:48128\n"
"v_mfma_f32_16x16x32_bf16 v[32:35], v[116:119], v[64:67], v[32:35]\n"
"v_mfma_f32_16x16x32_bf16 v[28:31], v[120:123], v[68:71], v[28:31]\n"
"ds_read_b128 v[64:67], v3 offset:49152\n"
"buffer_load_dwordx4 v2, s[8:11], s31 offen offset:64 lds\n"
"v_mfma_f32_16x16x32_bf16 v[32:35], v[124:127], v[68:71], v[32:35]\n"
"s_add_u32 m0, s29, 0x5780\n"
"s_waitcnt lgkmcnt(3)\n"
"v_mfma_f32_16x16x32_bf16 v[28:31], v[128:131], v[72:75], v[28:31]\n"
"ds_read_b128 v[68:71], v3 offset:50176\n"
"v_mfma_f32_16x16x32_bf16 v[32:35], v[132:135], v[72:75], v[32:35]\n"
"v_mfma_f32_16x16x32_bf16 v[52:55], v[72:75], v[72:75], v[52:55]\n"
"buffer_load_dwordx4 v2, s[8:11], s31 offen offset:128 lds\n"
"v_mfma_f32_16x16x32_bf16 v[28:31], v[136:139], v[76:79], v[28:31]\n"
"ds_read_b128 v[72:75], v3 offset:51200\n"
"s_add_u32 m0, s29, 0x5b40\n"
"v_mfma_f32_16x16x32_bf16 v[32:35], v[140:143], v[76:79], v[32:35]\n"
"s_waitcnt lgkmcnt(3)\n"
"v_mfma_f32_16x16x32_bf16 v[28:31], v[144:147], v[56:59], v[28:31]\n"
"ds_read_b128 v[76:79], v3 offset:52224\n"
"v_mfma_f32_16x16x32_bf16 v[32:35], v[148:151], v[56:59], v[32:35]\n"
"buffer_load_dwordx4 v2, s[8:11], s31 offen offset:192 lds\n"
"v_mfma_f32_16x16x32_bf16 v[28:31], v[152:155], v[60:63], v[28:31]\n"
"ds_read_b128 v[56:59], v3 offset:53248\n"
"s_add_u32 m0, s29, 0x5f00\n"
"v_mfma_f32_16x16x32_bf16 v[32:35], v[156:159], v[60:63], v[32:35]\n"
"s_waitcnt lgkmcnt(3)\n"
"v_mfma_f32_16x16x32_bf16 v[28:31], v[160:163], v[64:67], v[28:31]\n"
"ds_read_b128 v[60:63], v3 offset:54272\n"
"v_mfma_f32_16x16x32_bf16 v[32:35], v[164:167], v[64:67], v[32:35]\n"
"buffer_load_dwordx4 v2, s[8:11], s31 offen offset:256 lds\n"
"v_mfma_f32_16x16x32_bf16 v[52:55], v[64:67], v[64:67], v[52:55]\n"
"v_mfma_f32_16x16x32_bf16 v[28:31], v[168:171], v[68:71], v[28:31]\n"
"ds_read_b128 v[64:67], v3 offset:55296\n"
"v_mfma_f32_16x16x32_bf16 v[32:35], v[172:175], v[68:71], v[32:35]\n"
"s_waitcnt lgkmcnt(3)\n"
"v_mfma_f32_16x16x32_bf16 v[28:31], v[176:179], v[72:75], v[28:31]\n"
"ds_read_b128 v[68:71], v3 offset:56320\n"
"v_mfma_f32_16x16x32_bf16 v[32:35], v[180:183], v[72:75], v[32:35]\n"
"v_mfma_f32_16x16x32_bf16 v[28:31], v[184:187], v[76:79], v[28:31]\n"
"ds_read_b128 v[72:75], v3 offset:57344\n"
"v_mfma_f32_16x16x32_bf16 v[32:35], v[188:191], v[76:79], v[32:35]\n"
"s_waitcnt lgkmcnt(3)\n"
"v_mfma_f32_16x16x32_bf16 v[28:31], v[192:195], v[56:59], v[28:31]\n"
"ds_read_b128 v[76:79], v4 offset:40960\n"
"v_mfma_f32_16x16x32_bf16 v[32:35], v[196:199], v[56:59], v[32:35]\n"
"v_mfma_f32_16x16x32_bf16 v[52:55], v[56:59], v[56:59], v[52:55]\n"
"v_mfma_f32_16x16x32_bf16 v[28:31], v[200:203], v[60:63], v[28:31]\n"
"ds_read_b128 v[56:59], v5 offset:40960\n"
"v_mfma_f32_16x16x32_bf16 v[32:35], v[204:207], v[60:63], v[32:35]\n"
"s_waitcnt lgkmcnt(3)\n"
"v_mfma_f32_16x16x32_bf16 v[28:31], v[208:211], v[64:67], v[28:31]\n"
"ds_read_b128 v[60:63], v6 offset:40960\n"
"v_mfma_f32_16x16x32_bf16 v[32:35], v[212:215], v[64:67], v[32:35]\n"
"v_mfma_f32_16x16x32_bf16 v[28:31], v[216:219], v[68:71], v[28:31]\n"
"v_add_u32 v3, 0x5000, v3\n"
"ds_read_b128 v[64:67], v3 offset:40960\n"
"v_mfma_f32_16x16x32_bf16 v[32:35], v[220:223], v[68:71], v[32:35]\n"
"s_waitcnt lgkmcnt(3)\n"
"v_mfma_f32_16x16x32_bf16 v[28:31], v[224:227], v[72:75], v[28:31]\n"
"ds_read_b128 v[68:71], v3 offset:41984\n"
"v_mfma_f32_16x16x32_bf16 v[32:35], v[228:231], v[72:75], v[32:35]\n"
"v_mfma_f32_16x16x32_bf16 v[52:55], v[72:75], v[72:75], v[52:55]\n"
"v_mfma_f32_16x16x32_bf16 v[28:31], v[232:235], v[76:79], v[28:31]\n"
"ds_read_b128 v[72:75], v3 offset:43008\n"
"v_mfma_f32_16x16x32_bf16 v[32:35], v[236:239], v[76:79], v[32:35]\n"
"s_waitcnt lgkmcnt(3)\n"
"v_mfma_f32_16x16x32_bf16 v[28:31], v[240:243], v[56:59], v[28:31]\n"
"ds_read_b128 v[76:79], v3 offset:44032\n"
"v_cndmask_b32 v94, v52, v53, s[34:35]\n"
"v_mfma_f32_16x16x32_bf16 v[32:35], v[244:247], v[56:59], v[32:35]\n"
"v_cndmask_b32 v54, v54, v55, s[34:35]\n"
"v_cndmask_b32 v94, v94, v54, s[20:21]\n"
"v_mfma_f32_16x16x32_bf16 v[28:31], v[248:251], v[60:63], v[28:31]\n"
"ds_read_b128 v[56:59], v3 offset:45056\n"
"v_mfma_f32_16x16x32_bf16 v[32:35], v[252:255], v[60:63], v[32:35]\n"
"s_waitcnt lgkmcnt(3)\n"
"v_mfma_f32_16x16x32_bf16 v[36:39], v[96:99], v[64:67], 0\n"
"ds_read_b128 v[60:63], v3 offset:46080\n"
"v_mfma_f32_16x16x32_bf16 v[40:43], v[100:103], v[64:67], 0\n"
"v_cndmask_b32 v94, v28, v94, s[22:23]\n"
"s_waitcnt vmcnt(7)\n"
"v_mfma_f32_16x16x32_bf16 v[52:55], v[64:67], v[64:67], 0\n"
"ds_write_b32 v9, v94 offset:0\n"
"v_mfma_f32_16x16x32_bf16 v[36:39], v[104:107], v[68:71], v[36:39]\n"
"ds_read_b128 v[64:67], v3 offset:47104\n"
"v_mfma_f32_16x16x32_bf16 v[40:43], v[108:111], v[68:71], v[40:43]\n"
"s_waitcnt lgkmcnt(4)\n"
"v_mfma_f32_16x16x32_bf16 v[36:39], v[112:115], v[72:75], v[36:39]\n"
"ds_read_b128 v[68:71], v3 offset:48128\n"
"v_mfma_f32_16x16x32_bf16 v[40:43], v[116:119], v[72:75], v[40:43]\n"
"v_mfma_f32_16x16x32_bf16 v[36:39], v[120:123], v[76:79], v[36:39]\n"
"ds_read_b128 v[72:75], v3 offset:49152\n"
"v_mfma_f32_16x16x32_bf16 v[40:43], v[124:127], v[76:79], v[40:43]\n"
"s_waitcnt lgkmcnt(4)\n"
"v_mfma_f32_16x16x32_bf16 v[36:39], v[128:131], v[56:59], v[36:39]\n"
"ds_read_b128 v[76:79], v3 offset:50176\n"
"v_mfma_f32_16x16x32_bf16 v[40:43], v[132:135], v[56:59], v[40:43]\n"
"v_mfma_f32_16x16x32_bf16 v[52:55], v[56:59], v[56:59], v[52:55]\n"
"v_mfma_f32_16x16x32_bf16 v[36:39], v[136:139], v[60:63], v[36:39]\n"
"ds_read_b128 v[56:59], v3 offset:51200\n"
"v_mfma_f32_16x16x32_bf16 v[40:43], v[140:143], v[60:63], v[40:43]\n"
"s_waitcnt lgkmcnt(3)\n"
"v_mfma_f32_16x16x32_bf16 v[36:39], v[144:147], v[64:67], v[36:39]\n"
"ds_read_b128 v[60:63], v3 offset:52224\n"
"ds_read_b128 v[84:87], v11 offset:0\n"
"ds_read_b32 v91, v12 offset:0\n"
"v_mfma_f32_16x16x32_bf16 v[40:43], v[148:151], v[64:67], v[40:43]\n"
"v_mfma_f32_16x16x32_bf16 v[36:39], v[152:155], v[68:71], v[36:39]\n"
"ds_read_b128 v[64:67], v3 offset:53248\n"
"v_mfma_f32_16x16x32_bf16 v[40:43], v[156:159], v[68:71], v[40:43]\n"
"s_waitcnt lgkmcnt(5)\n"
"v_mfma_f32_16x16x32_bf16 v[36:39], v[160:163], v[72:75], v[36:39]\n"
"ds_read_b128 v[68:71], v3 offset:54272\n"
"v_mfma_f32_16x16x32_bf16 v[40:43], v[164:167], v[72:75], v[40:43]\n"
"v_mfma_f32_16x16x32_bf16 v[52:55], v[72:75], v[72:75], v[52:55]\n"
"v_mfma_f32_16x16x32_bf16 v[36:39], v[168:171], v[76:79], v[36:39]\n"
"ds_read_b128 v[72:75], v3 offset:55296\n"
"s_waitcnt lgkmcnt(4)\n"
"v_add_f32 v84, v84, v85\n"
"v_mfma_f32_16x16x32_bf16 v[40:43], v[172:175], v[76:79], v[40:43]\n"
"v_add_f32 v86, v86, v87\n"
"v_mfma_f32_16x16x32_bf16 v[36:39], v[176:179], v[56:59], v[36:39]\n"
"ds_read_b128 v[76:79], v3 offset:56320\n"
"v_add_f32 v84, v84, v86\n"
"v_mfma_f32_16x16x32_bf16 v[40:43], v[180:183], v[56:59], v[40:43]\n"
"s_waitcnt lgkmcnt(4)\n"
"v_mul_f32 v89, 0x3acccccd, v91\n"
"v_fma_f32 v84, -v91, v89, v84\n"
"v_mfma_f32_16x16x32_bf16 v[36:39], v[184:187], v[60:63], v[36:39]\n"
"ds_read_b128 v[56:59], v3 offset:57344\n"
"v_fmamk_f32 v84, v84, 0x3acccccd, v27\n"
"v_mfma_f32_16x16x32_bf16 v[40:43], v[188:191], v[60:63], v[40:43]\n"
"v_cmp_u_f32 vcc, v84, v84\n"
"s_waitcnt lgkmcnt(3)\n"
"v_mfma_f32_16x16x32_bf16 v[36:39], v[192:195], v[64:67], v[36:39]\n"
"ds_read_b128 v[60:63], v4 offset:61440\n"
"v_max_f32 v84, v84, v27\n"
"v_mfma_f32_16x16x32_bf16 v[40:43], v[196:199], v[64:67], v[40:43]\n"
"v_rsq_f32 v88, v84\n"
"v_mfma_f32_16x16x32_bf16 v[52:55], v[64:67], v[64:67], v[52:55]\n"
"v_mul_f32 v89, -v89, v88\n"
"v_mfma_f32_16x16x32_bf16 v[36:39], v[200:203], v[68:71], v[36:39]\n"
"ds_read_b128 v[64:67], v5 offset:61440\n"
"s_cbranch_vccnz .Lsl_38\n"
".Lbk_39:\n"
"v_mfma_f32_16x16x32_bf16 v[40:43], v[204:207], v[68:71], v[40:43]\n"
"s_add_u32 s31, s28, 0x13b000\n"
"s_waitcnt lgkmcnt(3)\n"
"v_mfma_f32_16x16x32_bf16 v[36:39], v[208:211], v[72:75], v[36:39]\n"
"ds_read_b128 v[68:71], v6 offset:61440\n"
"s_add_u32 m0, s29, 0xa000\n"
"v_mfma_f32_16x16x32_bf16 v[40:43], v[212:215], v[72:75], v[40:43]\n"
"v_mul_f32 v28, v88, v28\n"
"v_mul_f32 v29, v88, v29\n"
"v_mfma_f32_16x16x32_bf16 v[36:39], v[216:219], v[76:79], v[36:39]\n"
"v_add_u32 v3, 0xffffb000, v3\n"
"ds_read_b128 v[72:75], v3 offset:0\n"
"buffer_load_dwordx4 v2, s[8:11], s31 offen offset:0 lds\n"
"v_mfma_f32_16x16x32_bf16 v[40:43], v[220:223], v[76:79], v[40:43]\n"
"s_add_u32 m0, s29, 0xa3c0\n"
"s_waitcnt lgkmcnt(3)\n"
"v_mfma_f32_16x16x32_bf16 v[36:39], v[224:227], v[56:59], v[36:39]\n"
"ds_read_b128 v[76:79], v3 offset:1024\n"
"v_mul_f32 v30, v88, v30\n"
"v_mfma_f32_16x16x32_bf16 v[40:43], v[228:231], v[56:59], v[40:43]\n"
"v_mul_f32 v31, v88, v31\n"
"v_fmac_f32 v28, v89, v16\n"
"v_mfma_f32_16x16x32_bf16 v[52:55], v[56:59], v[56:59], v[52:55]\n"
"buffer_load_dwordx4 v2, s[8:11], s31 offen offset:64 lds\n"
"v_fmac_f32 v29, v89, v17\n"
"v_mfma_f32_16x16x32_bf16 v[36:39], v[232:235], v[60:63], v[36:39]\n"
"ds_read_b128 v[56:59], v3 offset:2048\n"
"s_add_u32 m0, s29, 0xa780\n"
"v_mfma_f32_16x16x32_bf16 v[40:43], v[236:239], v[60:63], v[40:43]\n"
"v_fmac_f32 v30, v89, v18\n"
"s_waitcnt lgkmcnt(3)\n"
"v_mfma_f32_16x16x32_bf16 v[36:39], v[240:243], v[64:67], v[36:39]\n"
"ds_read_b128 v[60:63], v3 offset:3072\n"
"v_cndmask_b32 v94, v52, v53, s[34:35]\n"
"v_mfma_f32_16x16x32_bf16 v[40:43], v[244:247], v[64:67], v[40:43]\n"
"v_cndmask_b32 v54, v54, v55, s[34:35]\n"
"v_cndmask_b32 v94, v94, v54, s[20:21]\n"
"buffer_load_dwordx4 v2, s[8:11], s31 offen offset:128 lds\n"
"v_mfma_f32_16x16x32_bf16 v[36:39], v[248:251], v[68:71], v[36:39]\n"
"ds_read_b128 v[64:67], v3 offset:4096\n"
"s_add_u32 m0, s29, 0xab40\n"
"v_mfma_f32_16x16x32_bf16 v[40:43], v[252:255], v[68:71], v[40:43]\n"
"v_fmac_f32 v31, v89, v19\n"
"s_waitcnt lgkmcnt(3)\n"
"v_mfma_f32_16x16x32_bf16 v[44:47], v[96:99], v[72:75], 0\n"
"ds_read_b128 v[68:71], v3 offset:5120\n"
"v_cvt_pk_bf16_f32 v28, v28, v29\n"
"v_mfma_f32_16x16x32_bf16 v[48:51], v[100:103], v[72:75], 0\n"
"v_cndmask_b32 v94, v36, v94, s[22:23]\n"
"s_waitcnt vmcnt(3)\n"
"buffer_load_dwordx4 v2, s[8:11], s31 offen offset:192 lds\n"
"v_mfma_f32_16x16x32_bf16 v[52:55], v[72:75], v[72:75], 0\n"
"ds_write_b32 v9, v94 offset:320\n"
"v_mfma_f32_16x16x32_bf16 v[44:47], v[104:107], v[76:79], v[44:47]\n"
"ds_read_b128 v[72:75], v3 offset:6144\n"
"s_add_u32 m0, s29, 0xaf00\n"
"v_mfma_f32_16x16x32_bf16 v[48:51], v[108:111], v[76:79], v[48:51]\n"
"v_cvt_pk_bf16_f32 v29, v30, v31\n"
"s_waitcnt lgkmcnt(4)\n"
"v_mfma_f32_16x16x32_bf16 v[44:47], v[112:115], v[56:59], v[44:47]\n"
"ds_read_b128 v[76:79], v3 offset:7168\n"
"buffer_load_dwordx4 v2, s[8:11], s31 offen offset:256 lds\n"
"v_mfma_f32_16x16x32_bf16 v[48:51], v[116:119], v[56:59], v[48:51]\n"
"v_mul_f32 v32, v88, v32\n"
"v_mul_f32 v33, v88, v33\n"
"v_mfma_f32_16x16x32_bf16 v[44:47], v[120:123], v[60:63], v[44:47]\n"
"ds_read_b128 v[56:59], v3 offset:8192\n"
"v_mul_f32 v34, v88, v34\n"
"v_mfma_f32_16x16x32_bf16 v[48:51], v[124:127], v[60:63], v[48:51]\n"
"v_mul_f32 v35, v88, v35\n"
"s_waitcnt lgkmcnt(4)\n"
"v_mfma_f32_16x16x32_bf16 v[44:47], v[128:131], v[64:67], v[44:47]\n"
"ds_read_b128 v[60:63], v3 offset:9216\n"
"v_fmac_f32 v32, v89, v20\n"
"v_mfma_f32_16x16x32_bf16 v[48:51], v[132:135], v[64:67], v[48:51]\n"
"v_fmac_f32 v33, v89, v21\n"
"v_fmac_f32 v34, v89, v22\n"
"v_mfma_f32_16x16x32_bf16 v[52:55], v[64:67], v[64:67], v[52:55]\n"
"v_fmac_f32 v35, v89, v23\n"
"v_cvt_pk_bf16_f32 v32, v32, v33\n"
"v_mfma_f32_16x16x32_bf16 v[44:47], v[136:139], v[68:71], v[44:47]\n"
"ds_read_b128 v[64:67], v3 offset:10240\n"
"v_cvt_pk_bf16_f32 v33, v34, v35\n"
"v_mfma_f32_16x16x32_bf16 v[48:51], v[140:143], v[68:71], v[48:51]\n"
"s_waitcnt lgkmcnt(3)\n"
"v_mfma_f32_16x16x32_bf16 v[44:47], v[144:147], v[72:75], v[44:47]\n"
"ds_read_b128 v[68:71], v3 offset:11264\n"
"ds_read_b128 v[84:87], v11 offset:320\n"
"ds_read_b32 v91, v12 offset:320\n"
"v_mfma_f32_16x16x32_bf16 v[48:51], v[148:151], v[72:75], v[48:51]\n"
"v_mfma_f32_16x16x32_bf16 v[44:47], v[152:155], v[76:79], v[44:47]\n"
"ds_read_b128 v[72:75], v3 offset:12288\n"
"v_mfma_f32_16x16x32_bf16 v[48:51], v[156:159], v[76:79], v[48:51]\n"
"s_waitcnt lgkmcnt(5)\n"
"v_mfma_f32_16x16x32_bf16 v[44:47], v[160:163], v[56:59], v[44:47]\n"
"ds_read_b128 v[76:79], v3 offset:13312\n"
"v_mfma_f32_16x16x32_bf16 v[48:51], v[164:167], v[56:59], v[48:51]\n"
"v_mfma_f32_16x16x32_bf16 v[52:55], v[56:59], v[56:59], v[52:55]\n"
"v_mfma_f32_16x16x32_bf16 v[44:47], v[168:171], v[60:63], v[44:47]\n"
"ds_read_b128 v[56:59], v3 offset:14336\n"
"s_waitcnt lgkmcnt(4)\n"
"v_add_f32 v84, v84, v85\n"
"v_mfma_f32_16x16x32_bf16 v[48:51], v[172:175], v[60:63], v[48:51]\n"
"v_add_f32 v86, v86, v87\n"
"v_mfma_f32_16x16x32_bf16 v[44:47], v[176:179], v[64:67], v[44:47]\n"
"ds_read_b128 v[60:63], v3 offset:15360\n"
"v_add_f32 v84, v84, v86\n"
"v_mfma_f32_16x16x32_bf16 v[48:51], v[180:183], v[64:67], v[48:51]\n"
"s_waitcnt lgkmcnt(4)\n"
"v_mul_f32 v89, 0x3acccccd, v91\n"
"v_fma_f32 v84, -v91, v89, v84\n"
"v_mfma_f32_16x16x32_bf16 v[44:47], v[184:187], v[68:71], v[44:47]\n"
"ds_read_b128 v[64:67], v3 offset:16384\n"
"v_fmamk_f32 v84, v84, 0x3acccccd, v27\n"
"v_mfma_f32_16x16x32_bf16 v[48:51], v[188:191], v[68:71], v[48:51]\n"
"v_cmp_u_f32 vcc, v84, v84\n"
"s_waitcnt lgkmcnt(3)\n"
"v_mfma_f32_16x16x32_bf16 v[44:47], v[192:195], v[72:75], v[44:47]\n"
"ds_read_b128 v[68:71], v4 offset:0\n"
"v_max_f32 v84, v84, v27\n"
"v_mfma_f32_16x16x32_bf16 v[48:51], v[196:199], v[72:75], v[48:51]\n"
"v_rsq_f32 v88, v84\n"
"v_mfma_f32_16x16x32_bf16 v[52:55], v[72:75], v[72:75], v[52:55]\n"
"v_mul_f32 v89, -v89, v88\n"
"v_mfma_f32_16x16x32_bf16 v[44:47], v[200:203], v[76:79], v[44:47]\n"
"ds_read_b128 v[72:75], v5 offset:0\n"
"s_cbranch_vccnz .Lsl_42\n"
".Lbk_43:\n"
"v_mfma_f32_16x16x32_bf16 v[48:51], v[204:207], v[76:79], v[48:51]\n"
"ds_write_b64 v7, v[28:29] offset:44928\n"
"s_waitcnt lgkmcnt(4)\n"
"v_mfma_f32_16x16x32_bf16 v[44:47], v[208:211], v[56:59], v[44:47]\n"
"ds_read_b128 v[76:79], v6 offset:0\n"
"v_mul_f32 v36, v88, v36\n"
"v_mfma_f32_16x16x32_bf16 v[48:51], v[212:215], v[56:59], v[48:51]\n"
"v_mul_f32 v37, v88, v37\n"
"v_mul_f32 v38, v88, v38\n"
"v_mfma_f32_16x16x32_bf16 v[44:47], v[216:219], v[60:63], v[44:47]\n"
"ds_read_b128 v[56:59], v3 offset:20480\n"
"v_mul_f32 v39, v88, v39\n"
"v_mfma_f32_16x16x32_bf16 v[48:51], v[220:223], v[60:63], v[48:51]\n"
"v_fmac_f32 v36, v89, v16\n"
"s_waitcnt lgkmcnt(4)\n"
"v_mfma_f32_16x16x32_bf16 v[44:47], v[224:227], v[64:67], v[44:47]\n"
"ds_read_b128 v[60:63], v3 offset:21504\n"
"v_fmac_f32 v37, v89, v17\n"
"v_mfma_f32_16x16x32_bf16 v[48:51], v[228:231], v[64:67], v[48:51]\n"
"v_mov_b32 v84, 0x3f803f80\n"
"v_mov_b32 v85, 0x3f803f80\n"
"v_mfma_f32_16x16x32_bf16 v[52:55], v[64:67], v[64:67], v[52:55]\n"
"v_mov_b32 v86, 0x3f803f80\n"
"v_mov_b32 v87, 0x3f803f80\n"
"ds_write_b64 v8, v[32:33] offset:44928\n"
"v_mfma_f32_16x16x32_bf16 v[44:47], v[232:235], v[68:71], v[44:47]\n"
"ds_read_b128 v[64:67], v3 offset:22528\n"
"v_fmac_f32 v38, v89, v18\n"
"v_mfma_f32_16x16x32_bf16 v[48:51], v[236:239], v[68:71], v[48:51]\n"
"v_fmac_f32 v39, v89, v19\n"
"s_waitcnt lgkmcnt(4)\n"
"v_mfma_f32_16x16x32_bf16 v[44:47], v[240:243], v[72:75], v[44:47]\n"
"ds_read_b128 v[68:71], v3 offset:23552\n"
"v_cndmask_b32 v94, v52, v53, s[34:35]\n"
"v_mfma_f32_16x16x32_bf16 v[48:51], v[244:247], v[72:75], v[48:51]\n"
"v_cndmask_b32 v54, v54, v55, s[34:35]\n"
"v_cndmask_b32 v94, v94, v54, s[20:21]\n"
"v_mfma_f32_16x16x32_bf16 v[44:47], v[248:251], v[76:79], v[44:47]\n"
"ds_read_b128 v[72:75], v3 offset:24576\n"
"v_cvt_pk_bf16_f32 v36, v36, v37\n"
"v_mfma_f32_16x16x32_bf16 v[48:51], v[252:255], v[76:79], v[48:51]\n"
"v_cvt_pk_bf16_f32 v37, v38, v39\n"
"s_waitcnt lgkmcnt(4)\n"
"v_mfma_f32_16x16x32_bf16 v[28:31], v[96:99], v[56:59], 0\n"
"ds_read_b128 v[76:79], v3 offset:25600\n"
"v_mul_f32 v40, v88, v40\n"
"v_mfma_f32_16x16x32_bf16 v[32:35], v[100:103], v[56:59], 0\n"
"v_cndmask_b32 v94, v44, v94, s[22:23]\n"
"s_waitcnt vmcnt(0)\n"
"ds_write_b128 v93, v[84:87] offset:40960\n"
"v_mfma_f32_16x16x32_bf16 v[52:55], v[56:59], v[56:59], 0\n"
"ds_write_b32 v9, v94 offset:640\n"
"v_mfma_f32_16x16x32_bf16 v[28:31], v[104:107], v[60:63], v[28:31]\n"
"ds_read_b128 v[56:59], v3 offset:26624\n"
"v_mul_f32 v41, v88, v41\n"
"v_mfma_f32_16x16x32_bf16 v[32:35], v[108:111], v[60:63], v[32:35]\n"
"v_mul_f32 v42, v88, v42\n"
"s_waitcnt lgkmcnt(5)\n"
"v_mfma_f32_16x16x32_bf16 v[28:31], v[112:115], v[64:67], v[28:31]\n"
"ds_read_b128 v[60:63], v3 offset:27648\n"
"v_mul_f32 v43, v88, v43\n"
"v_mfma_f32_16x16x32_bf16 v[32:35], v[116:119], v[64:67], v[32:35]\n"
"v_fmac_f32 v40, v89, v20\n"
"v_fmac_f32 v41, v89, v21\n"
"v_mfma_f32_16x16x32_bf16 v[28:31], v[120:123], v[68:71], v[28:31]\n"
"ds_read_b128 v[64:67], v3 offset:28672\n"
"v_fmac_f32 v42, v89, v22\n"
"v_mfma_f32_16x16x32_bf16 v[32:35], v[124:127], v[68:71], v[32:35]\n"
"v_fmac_f32 v43, v89, v23\n"
"s_waitcnt lgkmcnt(5)\n"
"v_mfma_f32_16x16x32_bf16 v[28:31], v[128:131], v[72:75], v[28:31]\n"
"ds_read_b128 v[68:71], v3 offset:29696\n"
"v_cvt_pk_bf16_f32 v40, v40, v41\n"
"v_mfma_f32_16x16x32_bf16 v[32:35], v[132:135], v[72:75], v[32:35]\n"
"v_cvt_pk_bf16_f32 v41, v42, v43\n"
"v_mfma_f32_16x16x32_bf16 v[52:55], v[72:75], v[72:75], v[52:55]\n"
"v_mfma_f32_16x16x32_bf16 v[28:31], v[136:139], v[76:79], v[28:31]\n"
"ds_read_b128 v[72:75], v3 offset:30720\n"
"v_mfma_f32_16x16x32_bf16 v[32:35], v[140:143], v[76:79], v[32:35]\n"
"s_waitcnt lgkmcnt(3)\n"
"v_mfma_f32_16x16x32_bf16 v[28:31], v[144:147], v[56:59], v[28:31]\n"
"ds_read_b128 v[76:79], v3 offset:31744\n"
"ds_read_b128 v[84:87], v11 offset:640\n"
"ds_read_b32 v91, v12 offset:640\n"
"v_mfma_f32_16x16x32_bf16 v[32:35], v[148:151], v[56:59], v[32:35]\n"
"v_mfma_f32_16x16x32_bf16 v[28:31], v[152:155], v[60:63], v[28:31]\n"
"ds_read_b128 v[56:59], v3 offset:32768\n"
"v_mfma_f32_16x16x32_bf16 v[32:35], v[156:159], v[60:63], v[32:35]\n"
"s_waitcnt lgkmcnt(5)\n"
"v_mfma_f32_16x16x32_bf16 v[28:31], v[160:163], v[64:67], v[28:31]\n"
"ds_read_b128 v[60:63], v3 offset:33792\n"
"v_mfma_f32_16x16x32_bf16 v[32:35], v[164:167], v[64:67], v[32:35]\n"
"v_mfma_f32_16x16x32_bf16 v[52:55], v[64:67], v[64:67], v[52:55]\n"
"v_mfma_f32_16x16x32_bf16 v[28:31], v[168:171], v[68:71], v[28:31]\n"
"ds_read_b128 v[64:67], v3 offset:34816\n"
"s_waitcnt lgkmcnt(4)\n"
"v_add_f32 v84, v84, v85\n"
"v_mfma_f32_16x16x32_bf16 v[32:35], v[172:175], v[68:71], v[32:35]\n"
"v_add_f32 v86, v86, v87\n"
"v_mfma_f32_16x16x32_bf16 v[28:31], v[176:179], v[72:75], v[28:31]\n"
"ds_read_b128 v[68:71], v3 offset:35840\n"
"v_add_f32 v84, v84, v86\n"
"v_mfma_f32_16x16x32_bf16 v[32:35], v[180:183], v[72:75], v[32:35]\n"
"s_waitcnt lgkmcnt(4)\n"
"v_mul_f32 v89, 0x3acccccd, v91\n"
"v_fma_f32 v84, -v91, v89, v84\n"
"v_mfma_f32_16x16x32_bf16 v[28:31], v[184:187], v[76:79], v[28:31]\n"
"ds_read_b128 v[72:75], v3 offset:36864\n"
"v_fmamk_f32 v84, v84, 0x3acccccd, v27\n"
"v_mfma_f32_16x16x32_bf16 v[32:35], v[188:191], v[76:79], v[32:35]\n"
"v_cmp_u_f32 vcc, v84, v84\n"
"s_waitcnt lgkmcnt(3)\n"
"v_mfma_f32_16x16x32_bf16 v[28:31], v[192:195], v[56:59], v[28:31]\n"
"ds_read_b128 v[76:79], v4 offset:20480\n"
"v_max_f32 v84, v84, v27\n"
"v_mfma_f32_16x16x32_bf16 v[32:35], v[196:199], v[56:59], v[32:35]\n"
"v_rsq_f32 v88, v84\n"
"v_mfma_f32_16x16x32_bf16 v[52:55], v[56:59], v[56:59], v[52:55]\n"
"v_mul_f32 v89, -v89, v88\n"
"v_mfma_f32_16x16x32_bf16 v[28:31], v[200:203], v[60:63], v[28:31]\n"
"ds_read_b128 v[56:59], v5 offset:20480\n"
"s_cbranch_vccnz .Lsl_46\n"
".Lbk_47:\n"
"v_mfma_f32_16x16x32_bf16 v[32:35], v[204:207], v[60:63], v[32:35]\n"
"ds_write_b64 v7, v[36:37] offset:48896\n"
"s_waitcnt lgkmcnt(4)\n"
"v_mfma_f32_16x16x32_bf16 v[28:31], v[208:211], v[64:67], v[28:31]\n"
"ds_read_b128 v[60:63], v6 offset:20480\n"
"v_mul_f32 v44, v88, v44\n"
"v_mul_f32 v45, v88, v45\n"
"v_mfma_f32_16x16x32_bf16 v[32:35], v[212:215], v[64:67], v[32:35]\n"
"v_mul_f32 v46, v88, v46\n"
"v_mul_f32 v47, v88, v47\n"
"v_fmac_f32 v44, v89, v16\n"
"v_mfma_f32_16x16x32_bf16 v[28:31], v[216:219], v[68:71], v[28:31]\n"
"ds_read_b128 v[64:67], v3 offset:40960\n"
"v_fmac_f32 v45, v89, v17\n"
"v_fmac_f32 v46, v89, v18\n"
"v_mfma_f32_16x16x32_bf16 v[32:35], v[220:223], v[68:71], v[32:35]\n"
"v_fmac_f32 v47, v89, v19\n"
"s_waitcnt lgkmcnt(4)\n"
"v_mfma_f32_16x16x32_bf16 v[28:31], v[224:227], v[72:75], v[28:31]\n"
"ds_read_b128 v[68:71], v3 offset:45056\n"
"v_cvt_pk_bf16_f32 v44, v44, v45\n"
"v_mfma_f32_16x16x32_bf16 v[32:35], v[228:231], v[72:75], v[32:35]\n"
"v_cvt_pk_bf16_f32 v45, v46, v47\n"
"v_mul_f32 v48, v88, v48\n"
"v_mfma_f32_16x16x32_bf16 v[52:55], v[72:75], v[72:75], v[52:55]\n"
"ds_write_b64 v8, v[40:41] offset:48896\n"
"v_mfma_f32_16x16x32_bf16 v[28:31], v[232:235], v[76:79], v[28:31]\n"
"ds_read_b128 v[72:75], v3 offset:49152\n"
"v_mul_f32 v49, v88, v49\n"
"v_mfma_f32_16x16x32_bf16 v[32:35], v[236:239], v[76:79], v[32:35]\n"
"v_mul_f32 v50, v88, v50\n"
"s_waitcnt lgkmcnt(4)\n"
"v_mfma_f32_16x16x32_bf16 v[28:31], v[240:243], v[56:59], v[28:31]\n"
"ds_read_b128 v[76:79], v3 offset:53248\n"
"v_cndmask_b32 v94, v52, v53, s[34:35]\n"
"v_mfma_f32_16x16x32_bf16 v[32:35], v[244:247], v[56:59], v[32:35]\n"
"v_cndmask_b32 v54, v54, v55, s[34:35]\n"
"v_cndmask_b32 v94, v94, v54, s[20:21]\n"
"v_mfma_f32_16x16x32_bf16 v[28:31], v[248:251], v[60:63], v[28:31]\n"
"ds_read_b128 v[56:59], v3 offset:57344\n"
"v_mul_f32 v51, v88, v51\n"
"v_mfma_f32_16x16x32_bf16 v[32:35], v[252:255], v[60:63], v[32:35]\n"
"v_fmac_f32 v48, v89, v20\n"
"s_waitcnt lgkmcnt(4)\n"
"s_cmp_eq_u32 s3, 3\n"
"s_cbranch_scc1 .Lfu_58\n"
"ds_read_b128 v[60:63], v3 offset:41984\n"
"v_fmac_f32 v49, v89, v21\n"
"v_mfma_f32_16x16x32_bf16 v[40:43], v[100:103], v[64:67], 0\n"
"v_cndmask_b32 v94, v28, v94, s[22:23]\n"
"v_fmac_f32 v50, v89, v22\n"
"v_mfma_f32_16x16x32_bf16 v[52:55], v[64:67], v[64:67], 0\n"
"ds_write_b32 v9, v94 offset:960\n"
"ds_read_b128 v[64:67], v3 offset:43008\n"
"v_fmac_f32 v51, v89, v23\n"
"v_mfma_f32_16x16x32_bf16 v[40:43], v[132:135], v[68:71], v[40:43]\n"
"v_cvt_pk_bf16_f32 v48, v48, v49\n"
"v_cvt_pk_bf16_f32 v49, v50, v51\n"
"v_mfma_f32_16x16x32_bf16 v[52:55], v[68:71], v[68:71], v[52:55]\n"
"ds_write_b64 v7, v[44:45] offset:52864\n"
"s_waitcnt lgkmcnt(5)\n"
"ds_read_b128 v[68:71], v3 offset:44032\n"
"v_mfma_f32_16x16x32_bf16 v[40:43], v[164:167], v[72:75], v[40:43]\n"
"v_mfma_f32_16x16x32_bf16 v[52:55], v[72:75], v[72:75], v[52:55]\n"
"ds_write_b64 v8, v[48:49] offset:52864\n"
"ds_read_b128 v[72:75], v3 offset:46080\n"
"v_mfma_f32_16x16x32_bf16 v[40:43], v[196:199], v[76:79], v[40:43]\n"
"v_mfma_f32_16x16x32_bf16 v[52:55], v[76:79], v[76:79], v[52:55]\n"
"ds_read_b128 v[84:87], v11 offset:960\n"
"ds_read_b32 v91, v12 offset:960\n"
"s_waitcnt lgkmcnt(8)\n"
"ds_read_b128 v[76:79], v3 offset:47104\n"
"v_mfma_f32_16x16x32_bf16 v[40:43], v[228:231], v[56:59], v[40:43]\n"
"v_mfma_f32_16x16x32_bf16 v[52:55], v[56:59], v[56:59], v[52:55]\n"
"ds_read_b128 v[56:59], v3 offset:48128\n"
"v_mfma_f32_16x16x32_bf16 v[40:43], v[108:111], v[60:63], v[40:43]\n"
"s_waitcnt lgkmcnt(6)\n"
"ds_read_b128 v[60:63], v3 offset:50176\n"
"s_nop 2\n"
"ds_write_b32 v10, v52 offset:1600\n"
"v_cndmask_b32 v94, v52, v53, s[34:35]\n"
"v_cndmask_b32 v54, v54, v55, s[34:35]\n"
"v_mfma_f32_16x16x32_bf16 v[40:43], v[116:119], v[64:67], v[40:43]\n"
"s_waitcnt lgkmcnt(5)\n"
"v_add_f32 v84, v84, v85\n"
"v_add_f32 v86, v86, v87\n"
"v_add_f32 v84, v84, v86\n"
"v_cndmask_b32 v94, v94, v54, s[20:21]\n"
"ds_write_b32 v9, v94 offset:1280\n"
"ds_read_b128 v[64:67], v3 offset:51200\n"
"s_waitcnt lgkmcnt(6)\n"
"v_mul_f32 v89, 0x3acccccd, v91\n"
"v_fma_f32 v84, -v91, v89, v84\n"
"v_mfma_f32_16x16x32_bf16 v[40:43], v[124:127], v[68:71], v[40:43]\n"
"v_fmamk_f32 v84, v84, 0x3acccccd, v27\n"
"s_waitcnt lgkmcnt(5)\n"
"ds_read_b128 v[68:71], v3 offset:52224\n"
"v_cmp_u_f32 vcc, v84, v84\n"
"v_mfma_f32_16x16x32_bf16 v[40:43], v[140:143], v[72:75], v[40:43]\n"
"v_max_f32 v84, v84, v27\n"
"v_rsq_f32 v88, v84\n"
"ds_read_b128 v[72:75], v3 offset:54272\n"
"v_mul_f32 v89, -v89, v88\n"
"v_mfma_f32_16x16x32_bf16 v[40:43], v[148:151], v[76:79], v[40:43]\n"
"s_cbranch_vccnz .Lsl_50c\n"
".Lbk_51c:\n"
"s_waitcnt lgkmcnt(5)\n"
"ds_read_b128 v[76:79], v3 offset:55296\n"
"v_mul_f32 v28, v88, v28\n"
"v_mul_f32 v29, v88, v29\n"
"v_mfma_f32_16x16x32_bf16 v[40:43], v[156:159], v[56:59], v[40:43]\n"
"ds_read_b128 v[44:47], v11 offset:1280\n"
"ds_read_b128 v[48:51], v11 offset:1600\n"
"v_mul_f32 v30, v88, v30\n"
"ds_read_b128 v[56:59], v3 offset:56320\n"
"v_mul_f32 v31, v88, v31\n"
"v_fmac_f32 v28, v89, v16\n"
"v_mfma_f32_16x16x32_bf16 v[40:43], v[172:175], v[60:63], v[40:43]\n"
"v_fmac_f32 v29, v89, v17\n"
"v_fmac_f32 v30, v89, v18\n"
"s_waitcnt lgkmcnt(5)\n"
"ds_read_b128 v[60:63], v4 offset:40960\n"
"v_fmac_f32 v31, v89, v19\n"
"v_cvt_pk_bf16_f32 v28, v28, v29\n"
"v_mfma_f32_16x16x32_bf16 v[40:43], v[180:183], v[64:67], v[40:43]\n"
"v_cvt_pk_bf16_f32 v29, v30, v31\n"
"ds_write_b64 v7, v[28:29] offset:56832\n"
"ds_read_b128 v[64:67], v5 offset:40960\n"
"v_mul_f32 v32, v88, v32\n"
"v_mul_f32 v33, v88, v33\n"
"v_mfma_f32_16x16x32_bf16 v[40:43], v[188:191], v[68:71], v[40:43]\n"
"v_mul_f32 v34, v88, v34\n"
"v_mul_f32 v35, v88, v35\n"
"s_waitcnt lgkmcnt(6)\n"
"ds_read_b128 v[68:71], v6 offset:40960\n"
"s_waitcnt lgkmcnt(6)\n"
"v_add_f32 v44, v44, v45\n"
"v_fmac_f32 v32, v89, v20\n"
"v_mfma_f32_16x16x32_bf16 v[40:43], v[204:207], v[72:75], v[40:43]\n"
"v_add_f32 v46, v46, v47\n"
"s_waitcnt lgkmcnt(5)\n"
"v_add_f32 v48, v48, v49\n"
"v_fmac_f32 v33, v89, v21\n"
"v_add_f32 v50, v50, v51\n"
"v_add_f32 v48, v48, v50\n"
"v_fmac_f32 v34, v89, v22\n"
"v_mfma_f32_16x16x32_bf16 v[40:43], v[212:215], v[76:79], v[40:43]\n"
"v_add_f32 v44, v44, v46\n"
"v_fmac_f32 v35, v89, v23\n"
"s_waitcnt lgkmcnt(3)\n"
"v_mul_f32 v53, 0x3acccccd, v48\n"
"v_fma_f32 v44, -v48, v53, v44\n"
"v_cvt_pk_bf16_f32 v32, v32, v33\n"
"v_mfma_f32_16x16x32_bf16 v[40:43], v[220:223], v[56:59], v[40:43]\n"
"v_fmamk_f32 v44, v44, 0x3acccccd, v27\n"
"v_cmp_u_f32 vcc, v44, v44\n"
"v_max_f32 v44, v44, v27\n"
"v_rsq_f32 v52, v44\n"
"v_mfma_f32_16x16x32_bf16 v[40:43], v[236:239], v[60:63], v[40:43]\n"
"v_mul_f32 v53, -v53, v52\n"
"s_waitcnt lgkmcnt(0)\n"
"s_cbranch_vccnz .Lsl_54c\n"
".Lbk_55c:\n"
"v_mfma_f32_16x16x32_bf16 v[40:43], v[244:247], v[64:67], v[40:43]\n"
"v_cvt_pk_bf16_f32 v33, v34, v35\n"
"ds_write_b64 v8, v[32:33] offset:56832\n"
"v_mfma_f32_16x16x32_bf16 v[40:43], v[252:255], v[68:71], v[40:43]\n"
"s_nop 7\n"
"v_mul_f32 v40, v52, v40\n"
"v_mul_f32 v41, v52, v41\n"
"v_mul_f32 v42, v52, v42\n"
"v_mul_f32 v43, v52, v43\n"
"v_fmac_f32 v40, v53, v20\n"
"v_fmac_f32 v41, v53, v21\n"
"v_fmac_f32 v42, v53, v22\n"
"v_fmac_f32 v43, v53, v23\n"
"v_cvt_pk_bf16_f32 v40, v40, v41\n"
"v_cvt_pk_bf16_f32 v41, v42, v43\n"
"ds_write_b64 v8, v[40:41] offset:40960\n"
"ds_write_b32 v13, v13 offset:144\n"
"s_branch .Ljn_59\n"
".Lfu_58:\n"
"s_nop 0\n"
"v_mfma_f32_16x16x32_bf16 v[36:39], v[96:99], v[64:67], 0\n"
"ds_read_b128 v[60:63], v3 offset:41984\n"
"v_fmac_f32 v49, v89, v21\n"
"v_mfma_f32_16x16x32_bf16 v[40:43], v[100:103], v[64:67], 0\n"
"v_cndmask_b32 v94, v28, v94, s[22:23]\n"
"v_fmac_f32 v50, v89, v22\n"
"v_mfma_f32_16x16x32_bf16 v[52:55], v[64:67], v[64:67], 0\n"
"ds_write_b32 v9, v94 offset:960\n"
"v_mfma_f32_16x16x32_bf16 v[36:39], v[128:131], v[68:71], v[36:39]\n"
"ds_read_b128 v[64:67], v3 offset:43008\n"
"v_fmac_f32 v51, v89, v23\n"
"v_mfma_f32_16x16x32_bf16 v[40:43], v[132:135], v[68:71], v[40:43]\n"
"v_cvt_pk_bf16_f32 v48, v48, v49\n"
"v_cvt_pk_bf16_f32 v49, v50, v51\n"
"v_mfma_f32_16x16x32_bf16 v[52:55], v[68:71], v[68:71], v[52:55]\n"
"ds_write_b64 v7, v[44:45] offset:52864\n"
"s_waitcnt lgkmcnt(5)\n"
"v_mfma_f32_16x16x32_bf16 v[36:39], v[160:163], v[72:75], v[36:39]\n"
"ds_read_b128 v[68:71], v3 offset:44032\n"
"v_mfma_f32_16x16x32_bf16 v[40:43], v[164:167], v[72:75], v[40:43]\n"
"v_mfma_f32_16x16x32_bf16 v[52:55], v[72:75], v[72:75], v[52:55]\n"
"ds_write_b64 v8, v[48:49] offset:52864\n"
"v_mfma_f32_16x16x32_bf16 v[36:39], v[192:195], v[76:79], v[36:39]\n"
"ds_read_b128 v[72:75], v3 offset:46080\n"
"v_mfma_f32_16x16x32_bf16 v[40:43], v[196:199], v[76:79], v[40:43]\n"
"v_mfma_f32_16x16x32_bf16 v[52:55], v[76:79], v[76:79], v[52:55]\n"
"ds_read_b128 v[84:87], v11 offset:960\n"
"ds_read_b32 v91, v12 offset:960\n"
"s_waitcnt lgkmcnt(8)\n"
"v_mfma_f32_16x16x32_bf16 v[36:39], v[224:227], v[56:59], v[36:39]\n"
"ds_read_b128 v[76:79], v3 offset:47104\n"
"v_mfma_f32_16x16x32_bf16 v[40:43], v[228:231], v[56:59], v[40:43]\n"
"v_mfma_f32_16x16x32_bf16 v[52:55], v[56:59], v[56:59], v[52:55]\n"
"v_mfma_f32_16x16x32_bf16 v[36:39], v[104:107], v[60:63], v[36:39]\n"
"ds_read_b128 v[56:59], v3 offset:48128\n"
"v_mfma_f32_16x16x32_bf16 v[40:43], v[108:111], v[60:63], v[40:43]\n"
"s_waitcnt lgkmcnt(6)\n"
"v_mfma_f32_16x16x32_bf16 v[36:39], v[112:115], v[64:67], v[36:39]\n"
"ds_read_b128 v[60:63], v3 offset:50176\n"
"ds_write_b32 v10, v52 offset:1600\n"
"v_cndmask_b32 v94, v52, v53, s[34:35]\n"
"v_cndmask_b32 v54, v54, v55, s[34:35]\n"
"v_mfma_f32_16x16x32_bf16 v[40:43], v[116:119], v[64:67], v[40:43]\n"
"s_waitcnt lgkmcnt(5)\n"
"v_add_f32 v84, v84, v85\n"
"v_add_f32 v86, v86, v87\n"
"v_add_f32 v84, v84, v86\n"
"v_cndmask_b32 v94, v94, v54, s[20:21]\n"
"ds_write_b32 v9, v94 offset:1280\n"
"v_mfma_f32_16x16x32_bf16 v[36:39], v[120:123], v[68:71], v[36:39]\n"
"ds_read_b128 v[64:67], v3 offset:51200\n"
"s_waitcnt lgkmcnt(6)\n"
"v_mul_f32 v89, 0x3acccccd, v91\n"
"v_fma_f32 v84, -v91, v89, v84\n"
"v_mfma_f32_16x16x32_bf16 v[40:43], v[124:127], v[68:71], v[40:43]\n"
"v_fmamk_f32 v84, v84, 0x3acccccd, v27\n"
"s_waitcnt lgkmcnt(5)\n"
"v_mfma_f32_16x16x32_bf16 v[36:39], v[136:139], v[72:75], v[36:39]\n"
"ds_read_b128 v[68:71], v3 offset:52224\n"
"v_cmp_u_f32 vcc, v84, v84\n"
"v_mfma_f32_16x16x32_bf16 v[40:43], v[140:143], v[72:75], v[40:43]\n"
"v_max_f32 v84, v84, v27\n"
"v_rsq_f32 v88, v84\n"
"v_mfma_f32_16x16x32_bf16 v[36:39], v[144:147], v[76:79], v[36:39]\n"
"ds_read_b128 v[72:75], v3 offset:54272\n"
"v_mul_f32 v89, -v89, v88\n"
"v_mfma_f32_16x16x32_bf16 v[40:43], v[148:151], v[76:79], v[40:43]\n"
"s_cbranch_vccnz .Lsl_50\n"
".Lbk_51:\n"
"s_waitcnt lgkmcnt(5)\n"
"v_mfma_f32_16x16x32_bf16 v[36:39], v[152:155], v[56:59], v[36:39]\n"
"ds_read_b128 v[76:79], v3 offset:55296\n"
"v_mul_f32 v28, v88, v28\n"
"v_mul_f32 v29, v88, v29\n"
"v_mfma_f32_16x16x32_bf16 v[40:43], v[156:159], v[56:59], v[40:43]\n"
"ds_read_b128 v[44:47], v11 offset:1280\n"
"ds_read_b128 v[48:51], v11 offset:1600\n"
"v_mul_f32 v30, v88, v30\n"
"v_mfma_f32_16x16x32_bf16 v[36:39], v[168:171], v[60:63], v[36:39]\n"
"ds_read_b128 v[56:59], v3 offset:56320\n"
"v_mul_f32 v31, v88, v31\n"
"v_fmac_f32 v28, v89, v16\n"
"v_mfma_f32_16x16x32_bf16 v[40:43], v[172:175], v[60:63], v[40:43]\n"
"v_fmac_f32 v29, v89, v17\n"
"v_fmac_f32 v30, v89, v18\n"
"s_waitcnt lgkmcnt(5)\n"
"v_mfma_f32_16x16x32_bf16 v[36:39], v[176:179], v[64:67], v[36:39]\n"
"ds_read_b128 v[60:63], v4 offset:40960\n"
"v_fmac_f32 v31, v89, v19\n"
"v_cvt_pk_bf16_f32 v28, v28, v29\n"
"v_mfma_f32_16x16x32_bf16 v[40:43], v[180:183], v[64:67], v[40:43]\n"
"v_cvt_pk_bf16_f32 v29, v30, v31\n"
"ds_write_b64 v7, v[28:29] offset:56832\n"
"v_mfma_f32_16x16x32_bf16 v[36:39], v[184:187], v[68:71], v[36:39]\n"
"ds_read_b128 v[64:67], v5 offset:40960\n"
"v_mul_f32 v32, v88, v32\n"
"v_mul_f32 v33, v88, v33\n"
"v_mfma_f32_16x16x32_bf16 v[40:43], v[188:191], v[68:71], v[40:43]\n"
"v_mul_f32 v34, v88, v34\n"
"v_mul_f32 v35, v88, v35\n"
"s_waitcnt lgkmcnt(6)\n"
"v_mfma_f32_16x16x32_bf16 v[36:39], v[200:203], v[72:75], v[36:39]\n"
"ds_read_b128 v[68:71], v6 offset:40960\n"
"s_waitcnt lgkmcnt(6)\n"
"v_add_f32 v44, v44, v45\n"
"v_fmac_f32 v32, v89, v20\n"
"v_mfma_f32_16x16x32_bf16 v[40:43], v[204:207], v[72:75], v[40:43]\n"
"v_add_f32 v46, v46, v47\n"
"s_waitcnt lgkmcnt(5)\n"
"v_add_f32 v48, v48, v49\n"
"v_fmac_f32 v33, v89, v21\n"
"v_mfma_f32_16x16x32_bf16 v[36:39], v[208:211], v[76:79], v[36:39]\n"
"v_add_f32 v50, v50, v51\n"
"v_add_f32 v48, v48, v50\n"
"v_fmac_f32 v34, v89, v22\n"
"v_mfma_f32_16x16x32_bf16 v[40:43], v[212:215], v[76:79], v[40:43]\n"
"v_add_f32 v44, v44, v46\n"
"v_fmac_f32 v35, v89, v23\n"
"s_waitcnt lgkmcnt(3)\n"
"v_mfma_f32_16x16x32_bf16 v[36:39], v[216:219], v[56:59], v[36:39]\n"
"v_mul_f32 v53, 0x3acccccd, v48\n"
"v_fma_f32 v44, -v48, v53, v44\n"
"v_cvt_pk_bf16_f32 v32, v32, v33\n"
"v_mfma_f32_16x16x32_bf16 v[40:43], v[220:223], v[56:59], v[40:43]\n"
"v_fmamk_f32 v44, v44, 0x3acccccd, v27\n"
"v_cmp_u_f32 vcc, v44, v44\n"
"v_mfma_f32_16x16x32_bf16 v[36:39], v[232:235], v[60:63], v[36:39]\n"
"v_max_f32 v44, v44, v27\n"
"v_rsq_f32 v52, v44\n"
"v_mfma_f32_16x16x32_bf16 v[40:43], v[236:239], v[60:63], v[40:43]\n"
"v_mul_f32 v53, -v53, v52\n"
"s_waitcnt lgkmcnt(0)\n"
"v_mfma_f32_16x16x32_bf16 v[36:39], v[240:243], v[64:67], v[36:39]\n"
"s_cbranch_vccnz .Lsl_54\n"
".Lbk_55:\n"
"v_mfma_f32_16x16x32_bf16 v[40:43], v[244:247], v[64:67], v[40:43]\n"
"v_cvt_pk_bf16_f32 v33, v34, v35\n"
"ds_write_b64 v8, v[32:33] offset:56832\n"
"v_mfma_f32_16x16x32_bf16 v[36:39], v[248:251], v[68:71], v[36:39]\n"
"v_mfma_f32_16x16x32_bf16 v[40:43], v[252:255], v[68:71], v[40:43]\n"
"s_nop 4\n"
"v_mul_f32 v36, v52, v36\n"
"v_mul_f32 v37, v52, v37\n"
"ds_write_b32 v13, v13 offset:144\n"
"v_mul_f32 v38, v52, v38\n"
"v_mul_f32 v39, v52, v39\n"
"v_fmac_f32 v36, v53, v16\n"
"v_fmac_f32 v37, v53, v17\n"
"v_fmac_f32 v38, v53, v18\n"
"v_fmac_f32 v39, v53, v19\n"
"v_cvt_pk_bf16_f32 v36, v36, v37\n"
"v_cvt_pk_bf16_f32 v37, v38, v39\n"
"ds_write_b64 v7, v[36:37] offset:40960\n"
"v_mul_f32 v40, v52, v40\n"
"v_mul_f32 v41, v52, v41\n"
"v_mul_f32 v42, v52, v42\n"
"v_mul_f32 v43, v52, v43\n"
"v_fmac_f32 v40, v53, v20\n"
"v_fmac_f32 v41, v53, v21\n"
"v_fmac_f32 v42, v53, v22\n"
"v_fmac_f32 v43, v53, v23\n"
"v_cvt_pk_bf16_f32 v40, v40, v41\n"
"v_cvt_pk_bf16_f32 v41, v42, v43\n"
"ds_write_b64 v8, v[40:41] offset:40960\n"
".Ljn_59:\n"
"v_mov_b32 v46, 0\n"
"v_mov_b32 v47, 0\n"
"v_mov_b32 v50, 0\n"
"v_mov_b32 v51, 0\n"
"s_cmp_lg_u32 s3, 2\n"
"s_cbranch_scc1 .Lnw_60\n"
"s_mov_b32 s17, 0x100000\n"
".Ltp_61:\n"
"ds_read_b32 v90, v14 offset:144\n"
"s_sub_u32 s17, s17, 1\n"
"s_cbranch_scc1 .Lou_62\n"
"s_waitcnt lgkmcnt(0)\n"
"v_cmp_eq_u32 vcc, 0, v90\n"
"s_cbranch_vccnz .Ltp_61\n"
".Lou_62:\n"
"s_waitcnt lgkmcnt(0)\n"
".Lnw_60:\n"
"ds_read_b32 v90, v14 offset:144\n"
"s_mov_b64 exec, 0xffff\n"
"ds_read_b64 v[46:47], v24 offset:44960\n"
"ds_read_b64 v[50:51], v24 offset:45000\n"
"s_mov_b64 exec, -1\n"
"ds_read_b64 v[44:45], v25 offset:44928\n"
"ds_read_b64 v[56:57], v25 offset:41040\n"
"ds_read_b64 v[58:59], v24 offset:41072\n"
"ds_read_b64 v[64:65], v25 offset:45008\n"
"ds_read_b64 v[66:67], v24 offset:45040\n"
"ds_read_b64 v[48:49], v25 offset:44968\n"
"ds_read_b64 v[60:61], v25 offset:41080\n"
"ds_read_b64 v[62:63], v24 offset:41112\n"
"ds_read_b64 v[68:69], v25 offset:45048\n"
"ds_read_b64 v[70:71], v24 offset:45080\n"
"s_waitcnt lgkmcnt(12)\n"
"v_or_b32 v90, s33, v90\n"
"s_mov_b32 s17, 0x100000\n"
"v_cmp_eq_u32 vcc, 0, v90\n"
"s_cbranch_vccz .Lok_64\n"
".Ltp_63:\n"
"ds_read_b32 v90, v14 offset:144\n"
"s_mov_b64 exec, 0xffff\n"
"ds_read_b64 v[46:47], v24 offset:44960\n"
"ds_read_b64 v[50:51], v24 offset:45000\n"
"s_mov_b64 exec, -1\n"
"ds_read_b64 v[44:45], v25 offset:44928\n"
"ds_read_b64 v[56:57], v25 offset:41040\n"
"ds_read_b64 v[58:59], v24 offset:41072\n"
"ds_read_b64 v[64:65], v25 offset:45008\n"
"ds_read_b64 v[66:67], v24 offset:45040\n"
"ds_read_b64 v[48:49], v25 offset:44968\n"
"ds_read_b64 v[60:61], v25 offset:41080\n"
"ds_read_b64 v[62:63], v24 offset:41112\n"
"ds_read_b64 v[68:69], v25 offset:45048\n"
"ds_read_b64 v[70:71], v24 offset:45080\n"
"s_sub_u32 s17, s17, 1\n"
"s_cbranch_scc1 .Lok_64\n"
"s_waitcnt lgkmcnt(12)\n"
"v_cmp_eq_u32 vcc, 0, v90\n"
"s_cbranch_vccnz .Ltp_63\n"
"s_waitcnt lgkmcnt(0)\n"
".Lok_64:\n"
"ds_read_b64_tr_b16 v[28:29], v26 offset:40960\n"
"ds_read_b64_tr_b16 v[30:31], v26 offset:44928\n"
"ds_read_b64_tr_b16 v[32:33], v26 offset:41000\n"
"s_waitcnt lgkmcnt(10)\n"
"v_mfma_f32_16x16x32_bf16 v[56:59], v[56:59], v[44:47], 0\n"
"s_waitcnt lgkmcnt(8)\n"
"v_mfma_f32_16x16x32_bf16 v[64:67], v[64:67], v[44:47], 0\n"
"ds_read_b64_tr_b16 v[34:35], v26 offset:44968\n"
"ds_read_b64_tr_b16 v[36:37], v26 offset:40992\n"
"ds_read_b64_tr_b16 v[38:39], v26 offset:44960\n"
"s_waitcnt lgkmcnt(8)\n"
"v_mfma_f32_16x16x32_bf16 v[60:63], v[60:63], v[48:51], 0\n"
"s_waitcnt lgkmcnt(6)\n"
"v_mfma_f32_16x16x32_bf16 v[68:71], v[68:71], v[48:51], 0\n"
"ds_read_b64_tr_b16 v[40:41], v26 offset:41032\n"
"ds_read_b64_tr_b16 v[42:43], v26 offset:45000\n"
"v_mov_b32 v84, 0x3f803f80\n"
"v_mov_b32 v85, 0x3f803f80\n"
"v_mov_b32 v86, 0x3f803f80\n"
"v_mov_b32 v87, 0x3f803f80\n"
"v_cndmask_b32 v76, v56, v57, s[34:35]\n"
"v_cndmask_b32 v72, v64, v65, s[34:35]\n"
"v_cndmask_b32 v76, v76, v72, s[36:37]\n"
"v_cndmask_b32 v77, v58, v59, s[34:35]\n"
"v_cndmask_b32 v72, v66, v67, s[34:35]\n"
"v_cndmask_b32 v77, v77, v72, s[38:39]\n"
"v_max_f32 v52, v76, v77\n"
"v_max_f32 v54, v76, v77\n"
"v_cndmask_b32 v78, v60, v61, s[34:35]\n"
"v_cndmask_b32 v72, v68, v69, s[34:35]\n"
"v_cndmask_b32 v78, v78, v72, s[36:37]\n"
"v_cndmask_b32 v79, v62, v63, s[34:35]\n"
"v_cndmask_b32 v72, v70, v71, s[34:35]\n"
"v_cndmask_b32 v79, v79, v72, s[38:39]\n"
"v_max_f32 v53, v78, v79\n"
"v_max_f32 v55, v78, v79\n"
"v_permlane16_swap_b32 v52, v54\n"
"s_nop 0\n"
"v_permlane16_swap_b32 v53, v55\n"
"v_max_f32 v72, v52, v54\n"
"v_max_f32 v52, v52, v54\n"
"v_max_f32 v73, v53, v55\n"
"v_max_f32 v53, v53, v55\n"
"v_permlane32_swap_b32 v52, v72\n"
"s_nop 0\n"
"v_permlane32_swap_b32 v53, v73\n"
"v_max_f32 v52, v52, v72\n"
"v_max_f32 v53, v53, v73\n"
"v_mul_f32 v52, 0xbea52b5b, v52\n"
"v_mul_f32 v53, 0xbea52b5b, v53\n"
"v_fmamk_f32 v76, v76, 0x3ea52b5b, v52\n"
"v_fmamk_f32 v77, v77, 0x3ea52b5b, v52\n"
"v_fmamk_f32 v78, v78, 0x3ea52b5b, v53\n"
"v_fmamk_f32 v79, v79, 0x3ea52b5b, v53\n"
"v_exp_f32 v76, v76\n"
"v_exp_f32 v77, v77\n"
"v_exp_f32 v78, v78\n"
"v_exp_f32 v79, v79\n"
"v_cvt_pk_bf16_f32 v52, v76, v77\n"
"v_cvt_pk_bf16_f32 v53, v78, v79\n"
"v_perm_b32 v44, v52, v52, v80\n"
"v_perm_b32 v45, v52, v52, v81\n"
"v_perm_b32 v46, v52, v52, v82\n"
"v_perm_b32 v47, v52, v52, v83\n"
"v_perm_b32 v48, v53, v53, v80\n"
"v_perm_b32 v49, v53, v53, v81\n"
"v_perm_b32 v50, v53, v53, v82\n"
"v_perm_b32 v51, v53, v53, v83\n"
"v_mfma_f32_16x16x32_bf16 v[56:59], v[84:87], v[44:47], 0\n"
"s_nop 0\n"
"v_mfma_f32_16x16x32_bf16 v[60:63], v[84:87], v[48:51], 0\n"
"s_waitcnt lgkmcnt(6)\n"
"v_mfma_f32_16x16x32_bf16 v[28:31], v[28:31], v[44:47], 0\n"
"s_waitcnt lgkmcnt(4)\n"
"v_mfma_f32_16x16x32_bf16 v[32:35], v[32:35], v[48:51], 0\n"
"s_waitcnt lgkmcnt(2)\n"
"v_mfma_f32_16x16x32_bf16 v[36:39], v[36:39], v[44:47], 0\n"
"s_waitcnt lgkmcnt(0)\n"
"v_mfma_f32_16x16x32_bf16 v[40:43], v[40:43], v[48:51], 0\n"
"v_cndmask_b32 v88, 2.0, v1, s[40:41]\n"
"v_rcp_f32 v52, v56\n"
"v_rcp_f32 v53, v60\n"
"s_add_u32 s31, s30, 0x140000\n"
"v_mul_f32 v28, v52, v28\n"
"v_mul_f32 v29, v52, v29\n"
"v_mul_f32 v30, v52, v30\n"
"v_mul_f32 v31, v52, v31\n"
"v_mul_f32 v32, v53, v32\n"
"v_mul_f32 v33, v53, v33\n"
"v_mul_f32 v34, v53, v34\n"
"v_mul_f32 v35, v53, v35\n"
"v_cvt_pk_bf16_f32 v28, v28, v29\n"
"v_cvt_pk_bf16_f32 v29, v30, v31\n"
"v_cvt_pk_bf16_f32 v30, v32, v33\n"
"v_cvt_pk_bf16_f32 v31, v34, v35\n"
"v_min_u32 v28, -2, v28\n"
"v_min_u32 v29, -2, v29\n"
"v_min_u32 v30, -2, v30\n"
"v_min_u32 v31, -2, v31\n"
"buffer_store_dwordx4 v[28:31], v1, s[4:7], s31 offen offset:0 sc1\n"
"v_mul_f32 v36, v52, v36\n"
"v_mul_f32 v37, v52, v37\n"
"v_mul_f32 v38, v52, v38\n"
"v_mul_f32 v39, v52, v39\n"
"v_mul_f32 v40, v53, v40\n"
"v_mul_f32 v41, v53, v41\n"
"v_mul_f32 v42, v53, v42\n"
"v_mul_f32 v43, v53, v43\n"
"v_cvt_pk_bf16_f32 v36, v36, v37\n"
"v_cvt_pk_bf16_f32 v37, v38, v39\n"
"v_cvt_pk_bf16_f32 v38, v40, v41\n"
"v_cvt_pk_bf16_f32 v39, v42, v43\n"
"v_min_u32 v36, -2, v36\n"
"v_min_u32 v37, -2, v37\n"
"v_min_u32 v38, -2, v38\n"
"v_min_u32 v39, -2, v39\n"
"buffer_store_dwordx4 v[36:39], v88, s[4:7], s31 offen offset:1024 sc1\n"
"ds_write_b32 v13, v13 offset:160\n"
"s_nop 2\n"
"s_cmp_lt_i32 s45, 0\n"
"s_cbranch_scc1 .Lnc_65\n"
"s_setprio 1\n"
"s_lshl_b32 s79, s3, 10\n"
"v_lshl_add_u32 v34, v0, 4, s79\n"
"v_lshrrev_b32 v59, 7, v34\n"
"v_and_b32 v58, 0x70, v34\n"
"s_mov_b32 s79, 0x5000\n"
"v_mad_i32_i24 v33, v59, s79, v58\n"
"s_mov_b32 s17, 0x100000\n"
".Ltp_66:\n"
"s_sleep 1\n"
"ds_read_b32 v42, v14 offset:144\n"
"s_sub_u32 s17, s17, 1\n"
"s_cbranch_scc1 .Lou_67\n"
"s_waitcnt lgkmcnt(0)\n"
"v_cmp_eq_u32 vcc, 0, v42\n"
"s_cbranch_vccnz .Ltp_66\n"
".Lou_67:\n"
"s_waitcnt lgkmcnt(0)\n"
"s_mov_b32 s17, 0x100000\n"
".Ltp_68:\n"
"s_sleep 1\n"
"ds_read_b32 v42, v14 offset:176\n"
"s_sub_u32 s17, s17, 1\n"
"s_cbranch_scc1 .Lou_69\n"
"s_waitcnt lgkmcnt(0)\n"
"v_cmp_eq_u32 vcc, 0, v42\n"
"s_cbranch_vccnz .Ltp_68\n"
".Lou_69:\n"
"s_waitcnt lgkmcnt(0)\n"
"s_lshl_b32 s79, s45, 8\n"
"s_add_u32 s46, s79, 0x6c8000\n"
"s_add_u32 s47, s46, 0x140000\n"
"s_and_b32 s79, s2, 7\n"
"s_lshl_b32 s79, s79, 14\n"
"s_add_u32 s48, s79, 0xc08000\n"
"s_add_u32 s49, s48, 0x1000\n"
"v_and_b32 v94, 0xffffff80, v33\n"
"v_add_u32 v94, v94, v33\n"
"buffer_load_dwordx4 v[78:81], v94, s[4:7], s46 offen offset:0 sc1\n"
"buffer_load_dwordx4 v[82:85], v94, s[4:7], s46 offen offset:128 sc1\n"
"buffer_load_dwordx4 v[86:89], v94, s[4:7], s47 offen offset:0 sc1\n"
"buffer_load_dwordx4 v[90:93], v94, s[4:7], s47 offen offset:128 sc1\n"
"s_mul_i32 s79, s3, 0x1200\n"
"s_add_u32 s79, s79, 0x1c400\n"
"v_lshl_add_u32 v44, v0, 3, s79\n"
"v_mov_b32 v46, 0\n"
"v_mov_b32 v47, 0\n"
"ds_write_b64 v44, v[46:47]\n"
"v_lshrrev_b32 v44, 4, v0\n"
"v_mov_b32 v59, 0xffff0000\n"
"v_mov_b32 v58, 0xffff\n"
"s_mov_b32 vcc_lo, 0xaaaaaaaa\n"
"s_mov_b32 vcc_hi, 0xaaaaaaaa\n"
"v_cndmask_b32 v45, v58, v59, vcc\n"
"v_and_b32 v59, 15, v0\n"
"v_lshlrev_b32 v46, 5, v59\n"
"v_and_b32 v59, 15, v0\n"
"v_lshrrev_b32 v47, 2, v59\n"
"s_lshl_b32 s79, s3, 10\n"
"v_lshl_add_u32 v48, v0, 4, s79\n"
"v_lshrrev_b32 v49, 2, v48\n"
"v_lshrrev_b32 v50, 6, v48\n"
"v_lshlrev_b32 v59, 3, v44\n"
"v_and_b32 v58, 15, v0\n"
"v_sub_u32 v59, v59, v58\n"
"v_add_u32 v51, 0x3ef, v59\n"
"v_and_b32 v59, 24, v50\n"
"v_xor_b32 v36, v49, v59\n"
"s_lshl_b32 s79, s3, 8\n"
"v_and_b32 v59, 15, v0\n"
"v_xor_b32 v59, s3, v59\n"
"v_and_b32 v59, 1, v59\n"
"v_lshrrev_b32 v58, 1, v44\n"
"v_xor_b32 v58, v47, v58\n"
"v_and_b32 v58, 1, v58\n"
"v_lshl_add_u32 v59, v59, 1, v58\n"
"v_and_b32 v58, 15, v0\n"
"v_and_b32 v58, 2, v58\n"
"v_and_b32 v57, 15, v0\n"
"v_lshrrev_b32 v57, 3, v57\n"
"v_lshlrev_b32 v57, 7, v57\n"
"v_lshl_add_u32 v57, v47, 11, v57\n"
"v_lshl_add_u32 v57, v44, 6, v57\n"
"v_lshl_add_u32 v58, v58, 4, v57\n"
"v_lshl_add_u32 v59, v59, 3, v58\n"
"v_add_u32 v31, s79, v59\n"
"s_mul_i32 s79, s3, 0x2100\n"
"s_add_u32 s79, s79, 0x13840\n"
"v_lshrrev_b32 v59, 2, v51\n"
"v_and_b32 v58, 3, v51\n"
"v_mul_i32_i24 v58, 0x840, v58\n"
"v_lshl_add_u32 v59, v59, 3, v58\n"
"v_add_u32 v28, s79, v59\n"
"s_mul_i32 s79, s3, 0x1200\n"
"s_add_u32 s79, s79, 0x1c420\n"
"v_lshl_add_u32 v59, v44, 4, v46\n"
"v_add_u32 v29, s79, v59\n"
"s_lshl_b32 s79, s3, 11\n"
"s_lshr_b32 s78, s3, 1\n"
"s_lshl_b32 s78, s78, 7\n"
"s_add_u32 s79, s79, s78\n"
"s_and_b32 s78, s3, 1\n"
"v_xor_b32 v59, v44, v47\n"
"v_xor_b32 v59, s78, v59\n"
"v_and_b32 v59, 3, v59\n"
"v_lshl_add_u32 v59, v59, 3, v46\n"
"v_add_u32 v30, s79, v59\n"
"v_xor_b32 v32, 8, v31\n"
"v_add_u32 v35, 0x1c600, v49\n"
"v_xor_b32 v37, 8, v36\n"
"s_mov_b32 vcc_lo, 0x300003\n"
"s_mov_b32 vcc_hi, 0x30000300\n"
"v_cndmask_b32 v38, 0, v45, vcc\n"
"s_mov_b32 vcc_lo, 0xc0000c\n"
"s_mov_b32 vcc_hi, 0xc0000c00\n"
"v_cndmask_b32 v39, 0, v45, vcc\n"
"s_lshl_b32 s79, s3, 2\n"
"s_add_u32 s79, s79, 0x26800\n"
"v_mov_b32 v40, s79\n"
"v_and_b32 v59, 48, v48\n"
"v_mul_i32_i24 v59, 0x50, v59\n"
"s_mov_b32 s79, 0x2800\n"
"v_mad_i32_i24 v41, v50, s79, v59\n"
"s_mul_i32 s72, s3, 0x2100\n"
"v_lshlrev_b32 v47, 4, v0\n"
"v_lshrrev_b32 v48, 5, v0\n"
"s_add_u32 s73, s72, 0x14000\n"
"s_add_u32 s74, s72, 0x15100\n"
"s_add_u32 s79, s74, 0x7f4\n"
"v_lshlrev_b32 v57, 3, v0\n"
"v_sub_u32 v60, s79, v57\n"
"s_sub_u32 s79, s74, 8\n"
"v_lshlrev_b32 v57, 5, v0\n"
"v_sub_u32 v57, 0x7e0, v57\n"
"v_sub_u32 v56, 0x5f0, v47\n"
"s_mov_b32 vcc_lo, 0\n"
"s_mov_b32 vcc_hi, -1\n"
"v_cndmask_b32 v57, v56, v57, vcc\n"
"v_add_u32 v49, s79, v57\n"
"v_mov_b32 v57, 4\n"
"s_mov_b32 vcc_lo, 0\n"
"s_mov_b32 vcc_hi, 0x80000000\n"
"v_cndmask_b32 v57, 0, v57, vcc\n"
"v_sub_u32 v61, v60, v57\n"
"v_mov_b32 v57, 4\n"
"s_mov_b32 vcc_lo, 0x80000000\n"
"s_mov_b32 vcc_hi, -1\n"
"v_cndmask_b32 v57, 0, v57, vcc\n"
"v_sub_u32 v62, v49, v57\n"
"v_add_u32 v67, s73, v47\n"
"s_add_u32 s79, s73, 0x800\n"
"v_and_b32 v57, 15, v0\n"
"v_lshrrev_b32 v56, 4, v0\n"
"v_mul_i32_i24 v56, 0x840, v56\n"
"v_lshl_add_u32 v57, v57, 2, v56\n"
"v_add_u32 v68, s79, v57\n"
"v_mov_b32 v69, 0\n"
"v_mad_i32_i24 v63, v48, 0, v49\n"
"v_lshl_add_u32 v64, v48, 2, v49\n"
"v_lshl_add_u32 v65, v48, 3, v49\n"
"v_mad_i32_i24 v66, v48, 12, v49\n"
"s_mov_b32 s17, 0x100000\n"
".Ltp_70:\n"
"s_sleep 1\n"
"ds_read_b32 v42, v14 offset:192\n"
"s_sub_u32 s17, s17, 1\n"
"s_cbranch_scc1 .Lou_71\n"
"s_waitcnt lgkmcnt(0)\n"
"v_cmp_eq_u32 vcc, 0, v42\n"
"s_cbranch_vccnz .Ltp_70\n"
".Lou_71:\n"
"s_waitcnt lgkmcnt(0)\n"
"ds_read_b32 v44, v60 offset:12\n"
"ds_read_b32 v45, v60 offset:8\n"
"ds_read_b32 v46, v60 offset:4\n"
"ds_read_b32 v47, v61 offset:4\n"
"ds_read_b32 v48, v61 offset:0\n"
"ds_read_b32 v49, v66 offset:24\n"
"ds_read_b32 v50, v66 offset:20\n"
"ds_read_b32 v51, v66 offset:16\n"
"ds_read_b32 v52, v65 offset:16\n"
"ds_read_b32 v53, v64 offset:16\n"
"ds_read_b32 v54, v64 offset:12\n"
"ds_read_b32 v55, v64 offset:8\n"
"ds_read_b32 v56, v63 offset:8\n"
"ds_read_b32 v57, v62 offset:8\n"
"ds_read_b32 v58, v62 offset:4\n"
"ds_read_b32 v59, v62 offset:0\n"
"v_cmp_ne_u32 vcc, 63, v0\n"
"s_waitcnt lgkmcnt(2)\n"
"s_nop 0\n"
"v_cndmask_b32 v57, 0, v57, vcc\n"
"s_waitcnt lgkmcnt(1)\n"
"v_cndmask_b32 v58, 0, v58, vcc\n"
"s_waitcnt lgkmcnt(0)\n"
"v_cndmask_b32 v59, 0, v59, vcc\n"
"v_cvt_pk_f16_f32 v70, v44, v44\n"
"v_cvt_pk_f16_f32 v71, v45, v45\n"
"v_cvt_pk_f16_f32 v72, v45, v46\n"
"v_cvt_pk_f16_f32 v73, v46, v46\n"
"ds_write_b128 v67, v[70:73] offset:0\n"
"v_cvt_pk_f16_f32 v74, v44, v45\n"
"v_cvt_pk_f16_f32 v75, v45, v45\n"
"v_cvt_pk_f16_f32 v76, v46, v46\n"
"v_cvt_pk_f16_f32 v77, v46, v46\n"
"ds_write_b128 v67, v[74:77] offset:2112\n"
"v_cvt_pk_f16_f32 v70, v45, v45\n"
"v_cvt_pk_f16_f32 v71, v45, v46\n"
"v_cvt_pk_f16_f32 v72, v46, v46\n"
"v_cvt_pk_f16_f32 v73, v46, v47\n"
"ds_write_b128 v67, v[70:73] offset:4224\n"
"v_cvt_pk_f16_f32 v74, v45, v45\n"
"v_cvt_pk_f16_f32 v75, v46, v46\n"
"v_cvt_pk_f16_f32 v76, v46, v46\n"
"v_cvt_pk_f16_f32 v77, v47, v48\n"
"ds_write_b128 v67, v[74:77] offset:6336\n"
"v_cvt_pk_f16_f32 v70, v49, v50\n"
"v_cvt_pk_f16_f32 v71, v51, v52\n"
"v_cvt_pk_f16_f32 v72, v53, v54\n"
"v_cvt_pk_f16_f32 v73, v55, v56\n"
"ds_write_b128 v67, v[70:73] offset:1024\n"
"v_cvt_pk_f16_f32 v74, v50, v51\n"
"v_cvt_pk_f16_f32 v75, v52, v53\n"
"v_cvt_pk_f16_f32 v76, v54, v55\n"
"v_cvt_pk_f16_f32 v77, v56, v57\n"
"ds_write_b128 v67, v[74:77] offset:3136\n"
"v_cvt_pk_f16_f32 v70, v51, v52\n"
"v_cvt_pk_f16_f32 v71, v53, v54\n"
"v_cvt_pk_f16_f32 v72, v55, v56\n"
"v_cvt_pk_f16_f32 v73, v57, v58\n"
"ds_write_b128 v67, v[70:73] offset:5248\n"
"v_cvt_pk_f16_f32 v74, v52, v53\n"
"v_cvt_pk_f16_f32 v75, v54, v55\n"
"v_cvt_pk_f16_f32 v76, v56, v57\n"
"v_cvt_pk_f16_f32 v77, v58, v59\n"
"ds_write_b128 v67, v[74:77] offset:7360\n"
"ds_write_b32 v68, v69\n"
"ds_read_b32 v40, v40\n"
"s_waitcnt lgkmcnt(0)\n"
"v_sub_u32 v44, 0x7f000000, v40\n"
"v_lshrrev_b32 v45, 16, v44\n"
"v_or_b32 v44, v44, v45\n"
"v_and_b32 v38, v44, v38\n"
"v_and_b32 v39, v44, v39\n"
"s_mov_b32 s52, 0x05040100\n"
"s_mov_b32 s53, 0x07060302\n"
"s_mov_b32 s17, 32768\n"
".Ltp_72:\n"
"s_waitcnt vmcnt(0)\n"
"s_sub_u32 s17, s17, 1\n"
"s_cbranch_scc1 .Lou_73\n"
"v_bitop3_b32 v54, v79, v81, s16 bitop3:0x7e\n"
"v_bitop3_b32 v55, v83, v85, s16 bitop3:0x7e\n"
"v_bitop3_b32 v56, v87, v89, s16 bitop3:0x7e\n"
"v_bitop3_b32 v57, v91, v93, s16 bitop3:0x7e\n"
"v_or3_b32 v58, v54, v55, v56\n"
"v_or_b32 v58, v58, v57\n"
"v_cmp_ne_u32 vcc, 0, v58\n"
"s_cbranch_vccz .Lou_73\n"
"s_sleep 2\n"
"v_cmp_ne_u32 vcc, 0, v54\n"
"s_cbranch_vccz .Lsk_74\n"
"s_mov_b64 exec, vcc\n"
"buffer_load_dwordx4 v[78:81], v94, s[4:7], s46 offen offset:0 sc1\n"
"s_mov_b64 exec, -1\n"
".Lsk_74:\n"
"v_cmp_ne_u32 vcc, 0, v55\n"
"s_cbranch_vccz .Lsk_75\n"
"s_mov_b64 exec, vcc\n"
"buffer_load_dwordx4 v[82:85], v94, s[4:7], s46 offen offset:128 sc1\n"
"s_mov_b64 exec, -1\n"
".Lsk_75:\n"
"v_cmp_ne_u32 vcc, 0, v56\n"
"s_cbranch_vccz .Lsk_76\n"
"s_mov_b64 exec, vcc\n"
"buffer_load_dwordx4 v[86:89], v94, s[4:7], s47 offen offset:0 sc1\n"
"s_mov_b64 exec, -1\n"
".Lsk_76:\n"
"v_cmp_ne_u32 vcc, 0, v57\n"
"s_cbranch_vccz .Lsk_77\n"
"s_mov_b64 exec, vcc\n"
"buffer_load_dwordx4 v[90:93], v94, s[4:7], s47 offen offset:128 sc1\n"
"s_mov_b64 exec, -1\n"
".Lsk_77:\n"
"s_branch .Ltp_72\n"
".Lou_73:\n"
"s_waitcnt vmcnt(0)\n"
"buffer_load_dwordx4 v[70:73], v34, s[4:7], s48 offen sc1\n"
"buffer_load_dwordx4 v[74:77], v34, s[4:7], s49 offen sc1\n"
"v_perm_b32 v54, v82, v78, s52\n"
"v_perm_b32 v55, v82, v78, s53\n"
"v_perm_b32 v56, v84, v80, s52\n"
"v_perm_b32 v57, v84, v80, s53\n"
"ds_write_b32 v36, v54 offset:0\n"
"ds_write_b32 v37, v55 offset:2048\n"
"ds_write_b32 v36, v56 offset:4224\n"
"ds_write_b32 v37, v57 offset:6272\n"
"v_perm_b32 v54, v90, v86, s52\n"
"v_perm_b32 v55, v90, v86, s53\n"
"v_perm_b32 v56, v92, v88, s52\n"
"v_perm_b32 v57, v92, v88, s53\n"
"ds_write_b32 v36, v54 offset:1024\n"
"ds_write_b32 v37, v55 offset:3072\n"
"ds_write_b32 v36, v56 offset:5248\n"
"ds_write_b32 v37, v57 offset:7296\n"
"v_lshlrev_b32 v54, 16, v78\n"
"v_lshlrev_b32 v55, 16, v82\n"
"v_and_b32 v56, 0xffff0000, v78\n"
"v_and_b32 v57, 0xffff0000, v82\n"
"v_lshlrev_b32 v58, 16, v80\n"
"v_lshlrev_b32 v59, 16, v84\n"
"v_and_b32 v60, 0xffff0000, v80\n"
"v_and_b32 v61, 0xffff0000, v84\n"
"v_lshlrev_b32 v62, 16, v86\n"
"v_lshlrev_b32 v63, 16, v90\n"
"v_and_b32 v64, 0xffff0000, v86\n"
"v_and_b32 v65, 0xffff0000, v90\n"
"v_lshlrev_b32 v66, 16, v88\n"
"v_lshlrev_b32 v67, 16, v92\n"
"v_and_b32 v68, 0xffff0000, v88\n"
"v_and_b32 v69, 0xffff0000, v92\n"
"s_mov_b32 s17, 32768\n"
".Ltp_78:\n"
"s_waitcnt vmcnt(0)\n"
"s_sub_u32 s17, s17, 1\n"
"s_cbranch_scc1 .Lou_79\n"
"v_max_u32 v78, v70, v71\n"
"v_max3_u32 v78, v72, v73, v78\n"
"v_max3_u32 v78, v74, v75, v78\n"
"v_max3_u32 v78, v76, v77, v78\n"
"v_cmp_eq_u32 vcc, -1, v78\n"
"s_cbranch_vccz .Lou_79\n"
"v_max_u32 v78, v72, v73\n"
"v_max3_u32 v78, v70, v71, v78\n"
"v_cmp_eq_u32 vcc, -1, v78\n"
"s_cbranch_vccz .Lsk_80\n"
"s_mov_b64 exec, vcc\n"
"buffer_load_dwordx4 v[70:73], v34, s[4:7], s48 offen sc1\n"
"s_mov_b64 exec, -1\n"
".Lsk_80:\n"
"v_max_u32 v78, v76, v77\n"
"v_max3_u32 v78, v74, v75, v78\n"
"v_cmp_eq_u32 vcc, -1, v78\n"
"s_cbranch_vccz .Lsk_81\n"
"s_mov_b64 exec, vcc\n"
"buffer_load_dwordx4 v[74:77], v34, s[4:7], s49 offen sc1\n"
"s_mov_b64 exec, -1\n"
".Lsk_81:\n"
"s_branch .Ltp_78\n"
".Lou_79:\n"
"s_waitcnt vmcnt(0)\n"
"v_fma_f32 v54, v54, v71, v70\n"
"v_fma_f32 v55, v55, v73, v72\n"
"v_cvt_pk_f16_f32 v54, v54, v55\n"
"v_fma_f32 v56, v56, v71, v70\n"
"v_fma_f32 v57, v57, v73, v72\n"
"v_cvt_pk_f16_f32 v56, v56, v57\n"
"v_fma_f32 v58, v58, v71, v70\n"
"v_fma_f32 v59, v59, v73, v72\n"
"v_cvt_pk_f16_f32 v58, v58, v59\n"
"v_fma_f32 v60, v60, v71, v70\n"
"v_fma_f32 v61, v61, v73, v72\n"
"v_cvt_pk_f16_f32 v60, v60, v61\n"
"ds_write2st64_b32 v35, v54, v56 offset0:0 offset1:18\n"
"ds_write2st64_b32 v35, v58, v60 offset0:36 offset1:54\n"
"v_fma_f32 v62, v62, v75, v74\n"
"v_fma_f32 v63, v63, v77, v76\n"
"v_cvt_pk_f16_f32 v62, v62, v63\n"
"v_fma_f32 v64, v64, v75, v74\n"
"v_fma_f32 v65, v65, v77, v76\n"
"v_cvt_pk_f16_f32 v64, v64, v65\n"
"v_fma_f32 v66, v66, v75, v74\n"
"v_fma_f32 v67, v67, v77, v76\n"
"v_cvt_pk_f16_f32 v66, v66, v67\n"
"v_fma_f32 v68, v68, v75, v74\n"
"v_fma_f32 v69, v69, v77, v76\n"
"v_cvt_pk_f16_f32 v68, v68, v69\n"
"ds_write2st64_b32 v35, v62, v64 offset0:4 offset1:22\n"
"ds_write2st64_b32 v35, v66, v68 offset0:40 offset1:58\n"
"ds_write_b32 v13, v13 offset:208\n"
"s_mov_b32 s17, 0x100000\n"
".Ltp_82:\n"
"ds_read_b32 v42, v14 offset:208\n"
"s_sub_u32 s17, s17, 1\n"
"s_cbranch_scc1 .Lou_83\n"
"s_waitcnt lgkmcnt(0)\n"
"v_cmp_eq_u32 vcc, 0, v42\n"
"s_cbranch_vccnz .Ltp_82\n"
".Lou_83:\n"
"s_waitcnt lgkmcnt(0)\n"
"s_setprio 1\n"
"s_mov_b32 s50, 0xfffefffe\n"
"ds_read_b64 v[60:61], v28 offset:1984\n"
"ds_read_b64 v[62:63], v28 offset:1992\n"
"ds_read_b128 v[64:67], v29 offset:1984\n"
"ds_read_b128 v[68:71], v29 offset:1472\n"
"ds_read_b64 v[72:73], v28 offset:1472\n"
"ds_read_b64 v[74:75], v28 offset:1480\n"
"ds_read_b128 v[76:79], v29 offset:960\n"
"s_waitcnt lgkmcnt(0)\n"
"v_mfma_f32_16x16x32_f16 v[56:59], v[60:63], v[64:67], 0\n"
"ds_read_b64 v[80:81], v28 offset:960\n"
"ds_read_b64 v[82:83], v28 offset:968\n"
"v_mfma_f32_16x16x32_f16 v[52:55], v[60:63], v[68:71], 0\n"
"ds_read_b128 v[84:87], v29 offset:448\n"
"ds_read_b64 v[88:89], v28 offset:448\n"
"v_mfma_f32_16x16x32_f16 v[56:59], v[72:75], v[68:71], v[56:59]\n"
"ds_read_b64 v[90:91], v28 offset:456\n"
"ds_read_b64 v[92:93], v28 offset:1920\n"
"v_mfma_f32_16x16x32_f16 v[48:51], v[60:63], v[76:79], 0\n"
"ds_read_b64 v[94:95], v28 offset:1928\n"
"s_waitcnt lgkmcnt(4)\n"
"v_mfma_f32_16x16x32_f16 v[52:55], v[72:75], v[76:79], v[52:55]\n"
"ds_read_b128 v[2:5], v29 offset:1920\n"
"ds_read_b128 v[34:37], v29 offset:1408\n"
"v_mfma_f32_16x16x32_f16 v[56:59], v[80:83], v[76:79], v[56:59]\n"
"ds_read_b64 v[64:65], v28 offset:1408\n"
"ds_read_b64 v[66:67], v28 offset:1416\n"
"v_mfma_f32_16x16x32_f16 v[44:47], v[60:63], v[84:87], 0\n"
"ds_read_b128 v[68:71], v29 offset:896\n"
"ds_read_b64 v[76:77], v28 offset:896\n"
"v_mfma_f32_16x16x32_f16 v[48:51], v[72:75], v[84:87], v[48:51]\n"
"ds_read_b64 v[78:79], v28 offset:904\n"
"s_waitcnt lgkmcnt(5)\n"
"v_mfma_f32_16x16x32_f16 v[52:55], v[80:83], v[84:87], v[52:55]\n"
"ds_read_b128 v[60:63], v29 offset:384\n"
"ds_read_b64 v[72:73], v28 offset:384\n"
"v_mfma_f32_16x16x32_f16 v[56:59], v[88:91], v[84:87], v[56:59]\n"
"ds_read_b64 v[74:75], v28 offset:392\n"
"ds_read_b64 v[80:81], v28 offset:1856\n"
"v_mfma_f32_16x16x32_f16 v[56:59], v[92:95], v[2:5], v[56:59]\n"
"ds_read_b64 v[82:83], v28 offset:1864\n"
"ds_read_b128 v[84:87], v29 offset:1856\n"
"v_mfma_f32_16x16x32_f16 v[52:55], v[92:95], v[34:37], v[52:55]\n"
"ds_read_b128 v[88:91], v29 offset:1344\n"
"s_waitcnt lgkmcnt(7)\n"
"v_mfma_f32_16x16x32_f16 v[56:59], v[64:67], v[34:37], v[56:59]\n"
"ds_read_b64 v[2:3], v30 offset:0\n"
"ds_read_b64 v[34:35], v28 offset:1344\n"
"v_mfma_f32_16x16x32_f16 v[48:51], v[92:95], v[68:71], v[48:51]\n"
"ds_read_b64 v[36:37], v28 offset:1352\n"
"v_mfma_f32_16x16x32_f16 v[52:55], v[64:67], v[68:71], v[52:55]\n"
"v_mfma_f32_16x16x32_f16 v[56:59], v[76:79], v[68:71], v[56:59]\n"
"ds_read_b128 v[68:71], v29 offset:832\n"
"s_waitcnt lgkmcnt(8)\n"
"v_mfma_f32_16x16x32_f16 v[44:47], v[92:95], v[60:63], v[44:47]\n"
"ds_read_b64 v[92:93], v28 offset:832\n"
"v_mfma_f32_16x16x32_f16 v[48:51], v[64:67], v[60:63], v[48:51]\n"
"ds_read_b64 v[94:95], v28 offset:840\n"
"v_mfma_f32_16x16x32_f16 v[52:55], v[76:79], v[60:63], v[52:55]\n"
"ds_read_b128 v[64:67], v29 offset:320\n"
"ds_read_b64 v[76:77], v28 offset:320\n"
"v_mfma_f32_16x16x32_f16 v[56:59], v[72:75], v[60:63], v[56:59]\n"
"ds_read_b64 v[78:79], v28 offset:328\n"
"s_waitcnt lgkmcnt(6)\n"
"v_mfma_f32_16x16x32_f16 v[56:59], v[80:83], v[84:87], v[56:59]\n"
"ds_read_b64 v[60:61], v30 offset:512\n"
"ds_read_b64 v[72:73], v28 offset:1792\n"
"v_mfma_f32_16x16x32_f16 v[52:55], v[80:83], v[88:91], v[52:55]\n"
"ds_read_b64 v[74:75], v28 offset:1800\n"
"s_and_b32 s79, s45, 0xfc\n"
"v_mfma_f32_16x16x16_bf16 v[44:47], v[38:39], v[2:3], v[44:47]\n"
"ds_read_b128 v[84:87], v29 offset:1792\n"
"s_and_b32 s78, s45, 1\n"
"v_mfma_f32_16x16x32_f16 v[56:59], v[34:37], v[88:91], v[56:59]\n"
"ds_read_b128 v[2:5], v29 offset:1280\n"
"s_waitcnt lgkmcnt(7)\n"
"v_mfma_f32_16x16x32_f16 v[48:51], v[80:83], v[68:71], v[48:51]\n"
"ds_read_b64 v[88:89], v28 offset:1280\n"
"s_lshl_b32 s78, s78, 1\n"
"v_mfma_f32_16x16x32_f16 v[52:55], v[34:37], v[68:71], v[52:55]\n"
"ds_read_b64 v[90:91], v28 offset:1288\n"
"s_add_u32 s79, s79, s78\n"
"v_mfma_f32_16x16x32_f16 v[56:59], v[92:95], v[68:71], v[56:59]\n"
"ds_read_b128 v[68:71], v29 offset:768\n"
"s_lshr_b32 s78, s45, 1\n"
"v_mfma_f32_16x16x32_f16 v[44:47], v[80:83], v[64:67], v[44:47]\n"
"ds_read_b64 v[80:81], v28 offset:768\n"
"s_waitcnt lgkmcnt(8)\n"
"v_mfma_f32_16x16x32_f16 v[48:51], v[34:37], v[64:67], v[48:51]\n"
"ds_read_b64 v[82:83], v28 offset:776\n"
"s_and_b32 s78, s78, 1\n"
"v_mfma_f32_16x16x32_f16 v[52:55], v[92:95], v[64:67], v[52:55]\n"
"ds_read_b128 v[34:37], v29 offset:256\n"
"ds_read_b64 v[92:93], v28 offset:256\n"
"v_mfma_f32_16x16x32_f16 v[56:59], v[76:79], v[64:67], v[56:59]\n"
"ds_read_b64 v[94:95], v28 offset:264\n"
"ds_read_b64 v[64:65], v28 offset:1728\n"
"v_mfma_f32_16x16x16_bf16 v[48:51], v[38:39], v[60:61], v[48:51]\n"
"ds_read_b64 v[66:67], v28 offset:1736\n"
"s_waitcnt lgkmcnt(7)\n"
"v_mfma_f32_16x16x32_f16 v[56:59], v[72:75], v[84:87], v[56:59]\n"
"ds_read_b128 v[76:79], v29 offset:1728\n"
"ds_read_b128 v[60:63], v29 offset:1216\n"
"v_mfma_f32_16x16x32_f16 v[52:55], v[72:75], v[2:5], v[52:55]\n"
"ds_read_b64 v[84:85], v28 offset:1216\n"
"s_add_u32 s79, s79, s78\n"
"v_mfma_f32_16x16x32_f16 v[56:59], v[88:91], v[2:5], v[56:59]\n"
"ds_read_b64 v[86:87], v28 offset:1224\n"
"s_lshl_b32 s79, s79, 7\n"
"v_mfma_f32_16x16x32_f16 v[48:51], v[72:75], v[68:71], v[48:51]\n"
"ds_read_b128 v[2:5], v29 offset:704\n"
"s_waitcnt lgkmcnt(9)\n"
"v_mfma_f32_16x16x32_f16 v[52:55], v[88:91], v[68:71], v[52:55]\n"
"s_add_u32 s46, s79, 0xc28000\n"
"v_mfma_f32_16x16x32_f16 v[56:59], v[80:83], v[68:71], v[56:59]\n"
"ds_read_b64 v[68:69], v30 offset:1024\n"
"v_mfma_f32_16x16x32_f16 v[44:47], v[72:75], v[34:37], v[44:47]\n"
"ds_read_b64 v[72:73], v28 offset:704\n"
"v_mfma_f32_16x16x32_f16 v[48:51], v[88:91], v[34:37], v[48:51]\n"
"ds_read_b64 v[74:75], v28 offset:712\n"
"s_waitcnt lgkmcnt(6)\n"
"v_mfma_f32_16x16x32_f16 v[52:55], v[80:83], v[34:37], v[52:55]\n"
"ds_read_b128 v[88:91], v29 offset:192\n"
"ds_read_b64 v[80:81], v28 offset:192\n"
"v_mfma_f32_16x16x32_f16 v[56:59], v[92:95], v[34:37], v[56:59]\n"
"ds_read_b64 v[82:83], v28 offset:200\n"
"ds_read_b64 v[34:35], v28 offset:1664\n"
"v_mfma_f32_16x16x32_f16 v[56:59], v[64:67], v[76:79], v[56:59]\n"
"ds_read_b64 v[36:37], v28 offset:1672\n"
"ds_read_b128 v[92:95], v29 offset:1664\n"
"v_mfma_f32_16x16x32_f16 v[52:55], v[64:67], v[60:63], v[52:55]\n"
"ds_read_b128 v[76:79], v29 offset:1152\n"
"s_waitcnt lgkmcnt(9)\n"
"v_mfma_f32_16x16x32_f16 v[56:59], v[84:87], v[60:63], v[56:59]\n"
"ds_read_b64 v[60:61], v28 offset:1152\n"
"v_mfma_f32_16x16x32_f16 v[48:51], v[64:67], v[2:5], v[48:51]\n"
"ds_read_b64 v[62:63], v28 offset:1160\n"
"v_mfma_f32_16x16x16_bf16 v[52:55], v[38:39], v[68:69], v[52:55]\n"
"ds_read_b128 v[68:71], v29 offset:640\n"
"s_nop 4\n"
"v_mfma_f32_16x16x32_f16 v[52:55], v[84:87], v[2:5], v[52:55]\n"
"s_waitcnt lgkmcnt(9)\n"
"v_mfma_f32_16x16x32_f16 v[56:59], v[72:75], v[2:5], v[56:59]\n"
"ds_read_b64 v[2:3], v28 offset:640\n"
"v_mfma_f32_16x16x32_f16 v[44:47], v[64:67], v[88:91], v[44:47]\n"
"ds_read_b64 v[4:5], v28 offset:648\n"
"v_mfma_f32_16x16x32_f16 v[48:51], v[84:87], v[88:91], v[48:51]\n"
"ds_read_b128 v[64:67], v29 offset:128\n"
"v_mfma_f32_16x16x32_f16 v[52:55], v[72:75], v[88:91], v[52:55]\n"
"ds_read_b64 v[84:85], v30 offset:1536\n"
"s_waitcnt lgkmcnt(5)\n"
"v_mfma_f32_16x16x32_f16 v[56:59], v[80:83], v[88:91], v[56:59]\n"
"ds_read_b64 v[72:73], v28 offset:128\n"
"ds_read_b64 v[74:75], v28 offset:136\n"
"v_mfma_f32_16x16x32_f16 v[56:59], v[34:37], v[92:95], v[56:59]\n"
"ds_read_b64 v[88:89], v28 offset:1600\n"
"ds_read_b64 v[90:91], v28 offset:1608\n"
"v_mfma_f32_16x16x32_f16 v[52:55], v[34:37], v[76:79], v[52:55]\n"
"ds_read_b128 v[80:83], v29 offset:1600\n"
"v_mfma_f32_16x16x32_f16 v[56:59], v[60:63], v[76:79], v[56:59]\n"
"ds_read_b128 v[92:95], v29 offset:1088\n"
"s_waitcnt lgkmcnt(7)\n"
"v_mfma_f32_16x16x32_f16 v[48:51], v[34:37], v[68:71], v[48:51]\n"
"ds_read_b64 v[76:77], v28 offset:1088\n"
"v_mfma_f32_16x16x32_f16 v[52:55], v[60:63], v[68:71], v[52:55]\n"
"ds_read_b64 v[78:79], v28 offset:1096\n"
"v_mfma_f32_16x16x32_f16 v[56:59], v[2:5], v[68:71], v[56:59]\n"
"ds_read_b128 v[68:71], v29 offset:576\n"
"v_mfma_f32_16x16x32_f16 v[44:47], v[34:37], v[64:67], v[44:47]\n"
"ds_read_b64 v[34:35], v28 offset:576\n"
"s_waitcnt lgkmcnt(8)\n"
"s_nop 0\n"
"v_mfma_f32_16x16x16_bf16 v[56:59], v[38:39], v[84:85], v[56:59]\n"
"ds_read_b64 v[36:37], v28 offset:584\n"
"v_mfma_f32_16x16x32_f16 v[48:51], v[60:63], v[64:67], v[48:51]\n"
"ds_read_b128 v[84:87], v29 offset:64\n"
"ds_read_b64 v[60:61], v28 offset:64\n"
"v_mfma_f32_16x16x32_f16 v[52:55], v[2:5], v[64:67], v[52:55]\n"
"ds_read_b64 v[62:63], v28 offset:72\n"
"ds_read_b64 v[2:3], v28 offset:1536\n"
"v_mfma_f32_16x16x32_f16 v[56:59], v[72:75], v[64:67], v[56:59]\n"
"ds_read_b64 v[4:5], v28 offset:1544\n"
"s_waitcnt lgkmcnt(7)\n"
"v_mfma_f32_16x16x32_f16 v[56:59], v[88:91], v[80:83], v[56:59]\n"
"ds_read_b128 v[64:67], v29 offset:0\n"
"ds_read_b128 v[72:75], v29 offset:512\n"
"v_mfma_f32_16x16x32_f16 v[52:55], v[88:91], v[92:95], v[52:55]\n"
"ds_read_b64 v[80:81], v28 offset:1024\n"
"v_mfma_f32_16x16x32_f16 v[56:59], v[76:79], v[92:95], v[56:59]\n"
"ds_read_b64 v[82:83], v28 offset:1032\n"
"v_mfma_f32_16x16x32_f16 v[48:51], v[88:91], v[68:71], v[48:51]\n"
"ds_read_b128 v[92:95], v29 offset:1024\n"
"s_waitcnt lgkmcnt(9)\n"
"v_mfma_f32_16x16x32_f16 v[52:55], v[76:79], v[68:71], v[52:55]\n"
"v_mfma_f32_16x16x32_f16 v[56:59], v[34:37], v[68:71], v[56:59]\n"
"ds_read_b64 v[68:69], v28 offset:512\n"
"v_mfma_f32_16x16x32_f16 v[44:47], v[88:91], v[84:87], v[44:47]\n"
"ds_read_b64 v[70:71], v28 offset:520\n"
"v_mfma_f32_16x16x32_f16 v[48:51], v[76:79], v[84:87], v[48:51]\n"
"ds_read_b128 v[88:91], v29 offset:1536\n"
"s_waitcnt lgkmcnt(6)\n"
"v_mfma_f32_16x16x32_f16 v[52:55], v[34:37], v[84:87], v[52:55]\n"
"ds_read_b64 v[76:77], v28 offset:0\n"
"v_mfma_f32_16x16x32_f16 v[56:59], v[60:63], v[84:87], v[56:59]\n"
"ds_read_b64 v[78:79], v28 offset:8\n"
"v_mfma_f32_16x16x32_f16 v[44:47], v[2:5], v[64:67], v[44:47]\n"
"v_mfma_f32_16x16x32_f16 v[48:51], v[2:5], v[72:75], v[48:51]\n"
"s_waitcnt lgkmcnt(3)\n"
"v_mfma_f32_16x16x32_f16 v[48:51], v[80:83], v[64:67], v[48:51]\n"
"v_mfma_f32_16x16x32_f16 v[52:55], v[2:5], v[92:95], v[52:55]\n"
"v_mul_f32 v44, v40, v44\n"
"v_mul_f32 v45, v40, v45\n"
"v_mul_f32 v46, v40, v46\n"
"v_mfma_f32_16x16x32_f16 v[52:55], v[80:83], v[72:75], v[52:55]\n"
"v_mul_f32 v47, v40, v47\n"
"v_cvt_pk_bf16_f32 v44, v44, v45\n"
"v_cvt_pk_bf16_f32 v45, v46, v47\n"
"v_mfma_f32_16x16x32_f16 v[52:55], v[68:71], v[64:67], v[52:55]\n"
"v_pk_min_u16 v44, v44, s50\n"
"v_pk_min_u16 v45, v45, s50\n"
"s_waitcnt lgkmcnt(0)\n"
"v_mfma_f32_16x16x32_f16 v[56:59], v[2:5], v[88:91], v[56:59]\n"
"ds_write_b64 v30, v[44:45] offset:0\n"
"v_mfma_f32_16x16x32_f16 v[56:59], v[80:83], v[92:95], v[56:59]\n"
"v_mul_f32 v48, v40, v48\n"
"v_mul_f32 v49, v40, v49\n"
"v_mfma_f32_16x16x32_f16 v[56:59], v[68:71], v[72:75], v[56:59]\n"
"v_mul_f32 v50, v40, v50\n"
"v_mul_f32 v51, v40, v51\n"
"v_mfma_f32_16x16x32_f16 v[56:59], v[76:79], v[64:67], v[56:59]\n"
"v_cvt_pk_bf16_f32 v48, v48, v49\n"
"v_cvt_pk_bf16_f32 v49, v50, v51\n"
"v_pk_min_u16 v48, v48, s50\n"
"v_pk_min_u16 v49, v49, s50\n"
"ds_write_b64 v30, v[48:49] offset:512\n"
"v_mul_f32 v52, v40, v52\n"
"v_mul_f32 v53, v40, v53\n"
"v_mul_f32 v54, v40, v54\n"
"v_mul_f32 v55, v40, v55\n"
"v_cvt_pk_bf16_f32 v52, v52, v53\n"
"v_cvt_pk_bf16_f32 v53, v54, v55\n"
"v_pk_min_u16 v52, v52, s50\n"
"v_pk_min_u16 v53, v53, s50\n"
"ds_write_b64 v30, v[52:53] offset:1024\n"
"v_mul_f32 v56, v40, v56\n"
"v_mul_f32 v57, v40, v57\n"
"v_mul_f32 v58, v40, v58\n"
"v_mul_f32 v59, v40, v59\n"
"v_cvt_pk_bf16_f32 v56, v56, v57\n"
"v_cvt_pk_bf16_f32 v57, v58, v59\n"
"v_pk_min_u16 v56, v56, s50\n"
"v_pk_min_u16 v57, v57, s50\n"
"ds_write_b64 v30, v[56:57] offset:1536\n"
"ds_write_b32 v13, v13 offset:224\n"
"s_mov_b32 s17, 0x100000\n"
".Ltp_84:\n"
"ds_read_b32 v42, v14 offset:224\n"
"s_sub_u32 s17, s17, 1\n"
"s_cbranch_scc1 .Lou_85\n"
"s_waitcnt lgkmcnt(0)\n"
"v_cmp_eq_u32 vcc, 0, v42\n"
"s_cbranch_vccnz .Ltp_84\n"
".Lou_85:\n"
"s_waitcnt lgkmcnt(0)\n"
"ds_read_b64_tr_b16 v[60:61], v31 offset:0\n"
"ds_read_b64_tr_b16 v[62:63], v32 offset:0\n"
"ds_read_b64_tr_b16 v[64:65], v31 offset:1024\n"
"ds_read_b64_tr_b16 v[66:67], v32 offset:1024\n"
"s_add_u32 s47, s46, 0xa0000\n"
"s_waitcnt lgkmcnt(2)\n"
"buffer_store_dwordx4 v[60:63], v33, s[4:7], s46 offen sc1\n"
"s_waitcnt lgkmcnt(0)\n"
"buffer_store_dwordx4 v[64:67], v33, s[4:7], s47 offen sc1\n"
"ds_write_b32 v13, v13 offset:240\n"
"s_setprio 1\n"
"s_mov_b32 s17, 0x100000\n"
".Ltp_86:\n"
"ds_read_b32 v42, v14 offset:240\n"
"s_sub_u32 s17, s17, 1\n"
"s_cbranch_scc1 .Lou_87\n"
"s_waitcnt lgkmcnt(0)\n"
"v_cmp_eq_u32 vcc, 0, v42\n"
"s_cbranch_vccnz .Ltp_86\n"
".Lou_87:\n"
"s_waitcnt lgkmcnt(0)\n"
"s_nop 2\n"
".Lnc_65:\n"
"v_and_b32 v33, 15, v0\n"
"s_lshl_b32 s74, s43, 10\n"
"v_lshrrev_b32 v34, 4, v0\n"
"v_and_b32 v79, 4, v33\n"
"v_lshrrev_b32 v78, 3, v33\n"
"v_and_b32 v77, 3, v33\n"
"v_lshrrev_b32 v76, 1, v34\n"
"v_and_b32 v75, 1, v34\n"
"v_lshlrev_b32 v75, 7, v75\n"
"v_lshl_add_u32 v76, v76, 9, v75\n"
"v_lshl_add_u32 v77, v77, 4, v76\n"
"v_lshl_add_u32 v78, v78, 6, v77\n"
"v_lshl_add_u32 v35, v79, 1, v78\n"
"v_add_u32 v3, s74, v35\n"
"s_add_u32 s79, s74, 0xfffff400\n"
"s_add_u32 s78, s74, 0x4400\n"
"s_cmp_ge_i32 s43, 3\n"
"s_cselect_b32 s79, s79, s78\n"
"v_add_u32 v4, s79, v35\n"
"s_add_u32 s79, s74, 0xfffff800\n"
"s_add_u32 s78, s74, 0x4800\n"
"s_cmp_ge_i32 s43, 2\n"
"s_cselect_b32 s79, s79, s78\n"
"v_add_u32 v5, s79, v35\n"
"s_add_u32 s79, s74, 0xfffffc00\n"
"s_add_u32 s78, s74, 0x4c00\n"
"s_cmp_ge_i32 s43, 1\n"
"s_cselect_b32 s79, s79, s78\n"
"v_add_u32 v6, s79, v35\n"
"s_andn2_b32 s32, 0x40000000, s85\n"
"v_add_u32 v93, s32, v1\n"
"s_mov_b32 s17, 0x100000\n"
".Ltp_88:\n"
"ds_read_b32 v90, v14 offset:160\n"
"s_sub_u32 s17, s17, 1\n"
"s_cbranch_scc1 .Lou_89\n"
"s_waitcnt lgkmcnt(0)\n"
"v_cmp_eq_u32 vcc, 0, v90\n"
"s_cbranch_vccnz .Ltp_88\n"
".Lou_89:\n"
"s_waitcnt lgkmcnt(0)\n"
"s_cmp_ge_i32 s45, 0\n"
"s_cbranch_scc1 .Lnw_90\n"
"s_mov_b32 s17, 0x100000\n"
".Ltp_91:\n"
"s_sleep 1\n"
"ds_read_b32 v90, v14 offset:176\n"
"s_sub_u32 s17, s17, 1\n"
"s_cbranch_scc1 .Lou_92\n"
"s_waitcnt lgkmcnt(0)\n"
"v_cmp_eq_u32 vcc, 0, v90\n"
"s_cbranch_vccnz .Ltp_91\n"
".Lou_92:\n"
"s_waitcnt lgkmcnt(0)\n"
"s_mov_b32 s89, 30\n"
".Ldl_93:\n"
"s_sub_u32 s89, s89, 1\n"
"s_cbranch_scc1 .Lde_94\n"
"s_sleep 1\n"
"s_branch .Ldl_93\n"
".Lde_94:\n"
"s_and_b32 s79, s2, 7\n"
"s_lshl_b32 s79, s79, 14\n"
"s_add_u32 s46, s79, 0xc08000\n"
"s_add_u32 s47, s46, 0x1000\n"
"buffer_load_dwordx4 v[28:31], v1, s[4:7], s46 offen offset:0 sc1\n"
"buffer_load_dwordx4 v[32:35], v1, s[4:7], s46 offen offset:1024 sc1\n"
"buffer_load_dwordx4 v[36:39], v1, s[4:7], s46 offen offset:2048 sc1\n"
"buffer_load_dwordx4 v[40:43], v1, s[4:7], s46 offen offset:3072 sc1\n"
"buffer_load_dwordx4 v[44:47], v1, s[4:7], s47 offen offset:0 sc1\n"
"buffer_load_dwordx4 v[48:51], v1, s[4:7], s47 offen offset:1024 sc1\n"
"buffer_load_dwordx4 v[52:55], v1, s[4:7], s47 offen offset:2048 sc1\n"
"buffer_load_dwordx4 v[56:59], v1, s[4:7], s47 offen offset:3072 sc1\n"
"s_mov_b32 s17, 32768\n"
".Ltp_95:\n"
"s_waitcnt vmcnt(0)\n"
"s_sub_u32 s17, s17, 1\n"
"s_cbranch_scc1 .Lou_96\n"
"v_max_u32 v60, v28, v29\n"
"v_max3_u32 v60, v30, v31, v60\n"
"v_max3_u32 v60, v32, v33, v60\n"
"v_max3_u32 v60, v34, v35, v60\n"
"v_max3_u32 v60, v36, v37, v60\n"
"v_max3_u32 v60, v38, v39, v60\n"
"v_max3_u32 v60, v40, v41, v60\n"
"v_max3_u32 v60, v42, v43, v60\n"
"v_max3_u32 v60, v44, v45, v60\n"
"v_max3_u32 v60, v46, v47, v60\n"
"v_max3_u32 v60, v48, v49, v60\n"
"v_max3_u32 v60, v50, v51, v60\n"
"v_max3_u32 v60, v52, v53, v60\n"
"v_max3_u32 v60, v54, v55, v60\n"
"v_max3_u32 v60, v56, v57, v60\n"
"v_max3_u32 v60, v58, v59, v60\n"
"v_cmp_eq_u32 vcc, -1, v60\n"
"s_cbranch_vccz .Lou_96\n"
"s_sleep 4\n"
"v_max_u32 v60, v30, v31\n"
"v_max3_u32 v60, v28, v29, v60\n"
"v_cmp_eq_u32 vcc, -1, v60\n"
"s_cbranch_vccz .Lsk_97\n"
"s_mov_b64 exec, vcc\n"
"buffer_load_dwordx4 v[28:31], v1, s[4:7], s46 offen offset:0 sc1\n"
"s_mov_b64 exec, -1\n"
".Lsk_97:\n"
"v_max_u32 v60, v34, v35\n"
"v_max3_u32 v60, v32, v33, v60\n"
"v_cmp_eq_u32 vcc, -1, v60\n"
"s_cbranch_vccz .Lsk_98\n"
"s_mov_b64 exec, vcc\n"
"buffer_load_dwordx4 v[32:35], v1, s[4:7], s46 offen offset:1024 sc1\n"
"s_mov_b64 exec, -1\n"
".Lsk_98:\n"
"v_max_u32 v60, v38, v39\n"
"v_max3_u32 v60, v36, v37, v60\n"
"v_cmp_eq_u32 vcc, -1, v60\n"
"s_cbranch_vccz .Lsk_99\n"
"s_mov_b64 exec, vcc\n"
"buffer_load_dwordx4 v[36:39], v1, s[4:7], s46 offen offset:2048 sc1\n"
"s_mov_b64 exec, -1\n"
".Lsk_99:\n"
"v_max_u32 v60, v42, v43\n"
"v_max3_u32 v60, v40, v41, v60\n"
"v_cmp_eq_u32 vcc, -1, v60\n"
"s_cbranch_vccz .Lsk_100\n"
"s_mov_b64 exec, vcc\n"
"buffer_load_dwordx4 v[40:43], v1, s[4:7], s46 offen offset:3072 sc1\n"
"s_mov_b64 exec, -1\n"
".Lsk_100:\n"
"v_max_u32 v60, v46, v47\n"
"v_max3_u32 v60, v44, v45, v60\n"
"v_cmp_eq_u32 vcc, -1, v60\n"
"s_cbranch_vccz .Lsk_101\n"
"s_mov_b64 exec, vcc\n"
"buffer_load_dwordx4 v[44:47], v1, s[4:7], s47 offen offset:0 sc1\n"
"s_mov_b64 exec, -1\n"
".Lsk_101:\n"
"v_max_u32 v60, v50, v51\n"
"v_max3_u32 v60, v48, v49, v60\n"
"v_cmp_eq_u32 vcc, -1, v60\n"
"s_cbranch_vccz .Lsk_102\n"
"s_mov_b64 exec, vcc\n"
"buffer_load_dwordx4 v[48:51], v1, s[4:7], s47 offen offset:1024 sc1\n"
"s_mov_b64 exec, -1\n"
".Lsk_102:\n"
"v_max_u32 v60, v54, v55\n"
"v_max3_u32 v60, v52, v53, v60\n"
"v_cmp_eq_u32 vcc, -1, v60\n"
"s_cbranch_vccz .Lsk_103\n"
"s_mov_b64 exec, vcc\n"
"buffer_load_dwordx4 v[52:55], v1, s[4:7], s47 offen offset:2048 sc1\n"
"s_mov_b64 exec, -1\n"
".Lsk_103:\n"
"v_max_u32 v60, v58, v59\n"
"v_max3_u32 v60, v56, v57, v60\n"
"v_cmp_eq_u32 vcc, -1, v60\n"
"s_cbranch_vccz .Lsk_104\n"
"s_mov_b64 exec, vcc\n"
"buffer_load_dwordx4 v[56:59], v1, s[4:7], s47 offen offset:3072 sc1\n"
"s_mov_b64 exec, -1\n"
".Lsk_104:\n"
"s_branch .Ltp_95\n"
".Lou_96:\n"
"s_waitcnt vmcnt(0)\n"
"s_mov_b32 s89, 30\n"
".Ldl_105:\n"
"s_sub_u32 s89, s89, 1\n"
"s_cbranch_scc1 .Lde_106\n"
"s_sleep 1\n"
"s_branch .Ldl_105\n"
".Lde_106:\n"
".Lnw_90:\n"
"s_mov_b32 s89, 8\n"
".Ldl_107:\n"
"s_sub_u32 s89, s89, 1\n"
"s_cbranch_scc1 .Lde_108\n"
"s_sleep 1\n"
"s_branch .Ldl_107\n"
".Lde_108:\n"
"s_cmp_ge_i32 s45, 0\n"
"s_cbranch_scc1 .Lnw_109\n"
"v_min_i32 v79, 3, v0\n"
"v_mul_i32_i24 v28, 0x500, v79\n"
"s_and_b32 s79, s2, 7\n"
"s_lshl_b32 s79, s79, 3\n"
"s_add_u32 s79, s79, 7\n"
"s_mul_i32 s79, s79, 0x5000\n"
"s_and_b32 s78, s2, 7\n"
"s_lshr_b32 s78, s78, 1\n"
"s_lshl_b32 s78, s78, 7\n"
"s_add_u32 s79, s79, s78\n"
"s_add_u32 s79, s79, s29\n"
"s_add_u32 s46, s79, 0xc28000\n"
"s_mov_b32 s17, 32768\n"
".Ltp_110:\n"
"buffer_load_dword v29, v28, s[4:7], s46 offen sc1\n"
"s_sub_u32 s17, s17, 1\n"
"s_cbranch_scc1 .Lou_111\n"
"s_waitcnt vmcnt(0)\n"
"v_cmp_eq_u32 vcc, -1, v29\n"
"s_cbranch_vccz .Lou_111\n"
"s_mov_b32 s89, 5\n"
".Ldl_112:\n"
"s_sub_u32 s89, s89, 1\n"
"s_cbranch_scc1 .Lde_113\n"
"s_sleep 1\n"
"s_branch .Ldl_112\n"
".Lde_113:\n"
"s_branch .Ltp_110\n"
".Lou_111:\n"
"s_waitcnt vmcnt(0)\n"
"s_mov_b32 s89, 8\n"
".Ldl_114:\n"
"s_sub_u32 s89, s89, 1\n"
"s_cbranch_scc1 .Lde_115\n"
"s_sleep 1\n"
"s_branch .Ldl_114\n"
".Lde_115:\n"
".Lnw_109:\n"
"s_mov_b32 s17, 0x100000\n"
".Ltp_116:\n"
"ds_read_b32 v90, v14 offset:256\n"
"s_sub_u32 s17, s17, 1\n"
"s_cbranch_scc1 .Lou_117\n"
"s_waitcnt lgkmcnt(0)\n"
"v_cmp_eq_u32 vcc, 0, v90\n"
"s_cbranch_vccnz .Ltp_116\n"
".Lou_117:\n"
"s_waitcnt lgkmcnt(0)\n"
"s_mov_b32 s54, 0\n"
"s_nop 1\n"
".Lag_118:\n"
"v_mov_b32 v92, 0\n"
"s_add_u32 s46, s44, 0xf000\n"
"s_add_u32 s47, s46, 0x1000\n"
"v_add_u32 v2, s29, v1\n"
"buffer_load_dwordx4 v[28:31], v1, s[4:7], s46 offen offset:0 sc1\n"
"buffer_load_dwordx4 v[32:35], v1, s[4:7], s46 offen offset:1024 sc1\n"
"buffer_load_dwordx4 v[36:39], v1, s[4:7], s46 offen offset:2048 sc1\n"
"buffer_load_dwordx4 v[40:43], v1, s[4:7], s46 offen offset:3072 sc1\n"
"buffer_load_dwordx4 v[44:47], v1, s[4:7], s47 offen offset:0 sc1\n"
"s_mov_b32 s17, 32768\n"
".Ltp_121:\n"
"s_waitcnt vmcnt(0)\n"
"s_sub_u32 s17, s17, 1\n"
"s_cbranch_scc1 .Lou_122\n"
"v_max_u32 v48, v28, v29\n"
"v_max3_u32 v48, v30, v31, v48\n"
"v_max3_u32 v48, v32, v33, v48\n"
"v_max3_u32 v48, v34, v35, v48\n"
"v_max3_u32 v48, v36, v37, v48\n"
"v_max3_u32 v48, v38, v39, v48\n"
"v_max3_u32 v48, v40, v41, v48\n"
"v_max3_u32 v48, v42, v43, v48\n"
"v_max3_u32 v48, v44, v45, v48\n"
"v_max3_u32 v48, v46, v47, v48\n"
"v_cmp_eq_u32 vcc, -1, v48\n"
"s_cbranch_vccz .Lou_122\n"
"s_sleep 1\n"
"v_max_u32 v48, v30, v31\n"
"v_max3_u32 v48, v28, v29, v48\n"
"v_cmp_eq_u32 vcc, -1, v48\n"
"s_cbranch_vccz .Lsk_123\n"
"s_mov_b64 exec, vcc\n"
"buffer_load_dwordx4 v[28:31], v1, s[4:7], s46 offen offset:0 sc1\n"
"s_mov_b64 exec, -1\n"
".Lsk_123:\n"
"v_max_u32 v48, v34, v35\n"
"v_max3_u32 v48, v32, v33, v48\n"
"v_cmp_eq_u32 vcc, -1, v48\n"
"s_cbranch_vccz .Lsk_124\n"
"s_mov_b64 exec, vcc\n"
"buffer_load_dwordx4 v[32:35], v1, s[4:7], s46 offen offset:1024 sc1\n"
"s_mov_b64 exec, -1\n"
".Lsk_124:\n"
"v_max_u32 v48, v38, v39\n"
"v_max3_u32 v48, v36, v37, v48\n"
"v_cmp_eq_u32 vcc, -1, v48\n"
"s_cbranch_vccz .Lsk_125\n"
"s_mov_b64 exec, vcc\n"
"buffer_load_dwordx4 v[36:39], v1, s[4:7], s46 offen offset:2048 sc1\n"
"s_mov_b64 exec, -1\n"
".Lsk_125:\n"
"v_max_u32 v48, v42, v43\n"
"v_max3_u32 v48, v40, v41, v48\n"
"v_cmp_eq_u32 vcc, -1, v48\n"
"s_cbranch_vccz .Lsk_126\n"
"s_mov_b64 exec, vcc\n"
"buffer_load_dwordx4 v[40:43], v1, s[4:7], s46 offen offset:3072 sc1\n"
"s_mov_b64 exec, -1\n"
".Lsk_126:\n"
"v_max_u32 v48, v46, v47\n"
"v_max3_u32 v48, v44, v45, v48\n"
"v_cmp_eq_u32 vcc, -1, v48\n"
"s_cbranch_vccz .Lsk_127\n"
"s_mov_b64 exec, vcc\n"
"buffer_load_dwordx4 v[44:47], v1, s[4:7], s47 offen offset:0 sc1\n"
"s_mov_b64 exec, -1\n"
".Lsk_127:\n"
"s_branch .Ltp_121\n"
".Lou_122:\n"
"s_waitcnt vmcnt(0)\n"
"s_add_u32 m0, s29, 0x5000\n"
"s_mov_b32 s31, s44\n"
"buffer_load_dwordx4 v1, s[4:7], s31 offen offset:0 sc1 lds\n"
"ds_write_b128 v2, v[28:31] offset:0\n"
"buffer_load_dwordx4 v1, s[4:7], s31 offen offset:1024 sc1 lds\n"
"ds_write_b128 v2, v[32:35] offset:1024\n"
"buffer_load_dwordx4 v1, s[4:7], s31 offen offset:2048 sc1 lds\n"
"ds_write_b128 v2, v[36:39] offset:2048\n"
"buffer_load_dwordx4 v1, s[4:7], s31 offen offset:3072 sc1 lds\n"
"ds_write_b128 v2, v[40:43] offset:3072\n"
"s_add_u32 m0, m0, 1024\n"
"s_add_u32 s31, s31, 1024\n"
"buffer_load_dwordx4 v1, s[4:7], s31 offen offset:3072 sc1 lds\n"
"ds_write_b128 v2, v[44:47] offset:4096\n"
"ds_write_b32 v13, v13 offset:272\n"
"s_mov_b32 s17, 0x100000\n"
".Ltp_128:\n"
"ds_read_b32 v90, v14 offset:272\n"
"s_sub_u32 s17, s17, 1\n"
"s_cbranch_scc1 .Lou_129\n"
"s_waitcnt lgkmcnt(0)\n"
"v_cmp_eq_u32 vcc, 0, v90\n"
"s_cbranch_vccnz .Ltp_128\n"
".Lou_129:\n"
"s_waitcnt lgkmcnt(0)\n"
"ds_read_b64 v[56:57], v3 offset:0\n"
"ds_read_b64 v[58:59], v3 offset:256\n"
"ds_read_b64 v[60:61], v3 offset:4096\n"
"ds_read_b64 v[62:63], v3 offset:4352\n"
"ds_read_b64 v[64:65], v3 offset:8192\n"
"ds_read_b64 v[66:67], v3 offset:8448\n"
"ds_read_b64 v[68:69], v3 offset:12288\n"
"ds_read_b64 v[70:71], v3 offset:12544\n"
"v_mov_b32 v80, 0x3f803f80\n"
"v_mov_b32 v81, 0x3f803f80\n"
"v_mov_b32 v82, 0x3f803f80\n"
"v_mov_b32 v83, 0x3f803f80\n"
"s_waitcnt lgkmcnt(4)\n"
"v_mfma_f32_16x16x32_bf16 v[28:31], v[96:99], v[56:59], 0\n"
"ds_read_b64 v[72:73], v3 offset:16384\n"
"s_add_u32 m0, s29, 0xa000\n"
"v_mfma_f32_16x16x32_bf16 v[32:35], v[100:103], v[56:59], 0\n"
"ds_read_b64 v[74:75], v3 offset:16640\n"
"s_add_u32 s31, s44, 0x5000\n"
"v_mfma_f32_16x16x32_bf16 v[52:55], v[56:59], v[56:59], 0\n"
"s_add_u32 s24, s84, 0x440000\n"
"s_add_u32 s25, s84, 0x420000\n"
"v_mfma_f32_16x16x32_bf16 v[76:79], v[80:83], v[56:59], 0\n"
"s_andn2_b32 s26, 0x40000000, s86\n"
"s_or_b32 s25, s25, s26\n"
"buffer_load_dwordx4 v1, s[4:7], s31 offen offset:0 sc1 lds\n"
"v_mfma_f32_16x16x32_bf16 v[28:31], v[128:131], v[60:63], v[28:31]\n"
"ds_read_b64 v[56:57], v3 offset:1024\n"
"s_and_b32 s26, s85, s86\n"
"v_mfma_f32_16x16x32_bf16 v[32:35], v[132:135], v[60:63], v[32:35]\n"
"ds_read_b64 v[58:59], v3 offset:1280\n"
"s_mov_b32 s27, s86\n"
"v_mfma_f32_16x16x32_bf16 v[52:55], v[60:63], v[60:63], v[52:55]\n"
"v_mfma_f32_16x16x32_bf16 v[76:79], v[80:83], v[60:63], v[76:79]\n"
"buffer_load_dwordx4 v1, s[4:7], s31 offen offset:1024 sc1 lds\n"
"s_waitcnt lgkmcnt(4)\n"
"v_mfma_f32_16x16x32_bf16 v[28:31], v[160:163], v[64:67], v[28:31]\n"
"ds_read_b64 v[60:61], v3 offset:2048\n"
"v_mfma_f32_16x16x32_bf16 v[32:35], v[164:167], v[64:67], v[32:35]\n"
"ds_read_b64 v[62:63], v3 offset:2304\n"
"v_mfma_f32_16x16x32_bf16 v[52:55], v[64:67], v[64:67], v[52:55]\n"
"v_mfma_f32_16x16x32_bf16 v[76:79], v[80:83], v[64:67], v[76:79]\n"
"buffer_load_dwordx4 v1, s[4:7], s31 offen offset:2048 sc1 lds\n"
"v_mfma_f32_16x16x32_bf16 v[28:31], v[192:195], v[68:71], v[28:31]\n"
"ds_read_b64 v[64:65], v3 offset:3072\n"
"v_mfma_f32_16x16x32_bf16 v[32:35], v[196:199], v[68:71], v[32:35]\n"
"ds_read_b64 v[66:67], v3 offset:3328\n"
"v_mfma_f32_16x16x32_bf16 v[52:55], v[68:71], v[68:71], v[52:55]\n"
"v_mfma_f32_16x16x32_bf16 v[76:79], v[80:83], v[68:71], v[76:79]\n"
"buffer_load_dwordx4 v1, s[4:7], s31 offen offset:3072 sc1 lds\n"
"s_waitcnt lgkmcnt(4)\n"
"v_mfma_f32_16x16x32_bf16 v[28:31], v[224:227], v[72:75], v[28:31]\n"
"ds_read_b64 v[68:69], v3 offset:5120\n"
"s_add_u32 m0, m0, 1024\n"
"v_mfma_f32_16x16x32_bf16 v[32:35], v[228:231], v[72:75], v[32:35]\n"
"ds_read_b64 v[70:71], v3 offset:5376\n"
"s_add_u32 s31, s31, 1024\n"
"v_mfma_f32_16x16x32_bf16 v[52:55], v[72:75], v[72:75], v[52:55]\n"
"v_mfma_f32_16x16x32_bf16 v[76:79], v[80:83], v[72:75], v[76:79]\n"
"buffer_load_dwordx4 v1, s[4:7], s31 offen offset:3072 sc1 lds\n"
"v_mfma_f32_16x16x32_bf16 v[28:31], v[104:107], v[56:59], v[28:31]\n"
"ds_read_b64 v[72:73], v3 offset:6144\n"
"s_add_u32 m0, s29, 0xf000\n"
"s_add_u32 s31, s44, 0xa000\n"
"v_mfma_f32_16x16x32_bf16 v[32:35], v[108:111], v[56:59], v[32:35]\n"
"ds_read_b64 v[74:75], v3 offset:6400\n"
"v_cndmask_b32 v94, v52, v53, s[34:35]\n"
"v_cndmask_b32 v54, v54, v55, s[34:35]\n"
"s_waitcnt lgkmcnt(4)\n"
"v_mfma_f32_16x16x32_bf16 v[28:31], v[112:115], v[60:63], v[28:31]\n"
"ds_read_b64 v[56:57], v3 offset:7168\n"
"v_cndmask_b32 v94, v94, v54, s[20:21]\n"
"ds_write_b32 v10, v76 offset:3136\n"
"v_mfma_f32_16x16x32_bf16 v[32:35], v[116:119], v[60:63], v[32:35]\n"
"ds_read_b64 v[58:59], v3 offset:7424\n"
"s_waitcnt vmcnt(5)\n"
"ds_write_b32 v9, v94 offset:1856\n"
"buffer_load_dwordx4 v1, s[4:7], s31 offen offset:0 sc1 lds\n"
"v_mfma_f32_16x16x32_bf16 v[28:31], v[120:123], v[64:67], v[28:31]\n"
"ds_read_b64 v[60:61], v3 offset:9216\n"
"v_mfma_f32_16x16x32_bf16 v[32:35], v[124:127], v[64:67], v[32:35]\n"
"ds_read_b64 v[62:63], v3 offset:9472\n"
"s_waitcnt lgkmcnt(6)\n"
"v_mfma_f32_16x16x32_bf16 v[28:31], v[136:139], v[68:71], v[28:31]\n"
"ds_read_b64 v[64:65], v3 offset:10240\n"
"v_mfma_f32_16x16x32_bf16 v[32:35], v[140:143], v[68:71], v[32:35]\n"
"ds_read_b64 v[66:67], v3 offset:10496\n"
"ds_read_b128 v[36:39], v11 offset:1856\n"
"ds_read_b128 v[40:43], v11 offset:3136\n"
"buffer_load_dwordx4 v1, s[4:7], s31 offen offset:1024 sc1 lds\n"
"v_mfma_f32_16x16x32_bf16 v[28:31], v[144:147], v[72:75], v[28:31]\n"
"ds_read_b64 v[68:69], v3 offset:11264\n"
"v_mfma_f32_16x16x32_bf16 v[32:35], v[148:151], v[72:75], v[32:35]\n"
"ds_read_b64 v[70:71], v3 offset:11520\n"
"s_waitcnt lgkmcnt(6)\n"
"v_mfma_f32_16x16x32_bf16 v[28:31], v[152:155], v[56:59], v[28:31]\n"
"ds_read_b64 v[72:73], v3 offset:13312\n"
"v_mfma_f32_16x16x32_bf16 v[32:35], v[156:159], v[56:59], v[32:35]\n"
"ds_read_b64 v[74:75], v3 offset:13568\n"
"buffer_load_dwordx4 v1, s[4:7], s31 offen offset:2048 sc1 lds\n"
"v_mfma_f32_16x16x32_bf16 v[28:31], v[168:171], v[60:63], v[28:31]\n"
"ds_read_b64 v[56:57], v3 offset:14336\n"
"v_mfma_f32_16x16x32_bf16 v[32:35], v[172:175], v[60:63], v[32:35]\n"
"ds_read_b64 v[58:59], v3 offset:14592\n"
"s_waitcnt lgkmcnt(4)\n"
"v_mfma_f32_16x16x32_bf16 v[28:31], v[176:179], v[64:67], v[28:31]\n"
"ds_read_b64 v[60:61], v3 offset:15360\n"
"v_add_f32 v36, v36, v37\n"
"v_add_f32 v38, v38, v39\n"
"v_add_f32 v40, v40, v41\n"
"v_mfma_f32_16x16x32_bf16 v[32:35], v[180:183], v[64:67], v[32:35]\n"
"ds_read_b64 v[62:63], v3 offset:15616\n"
"v_add_f32 v42, v42, v43\n"
"v_add_f32 v40, v40, v42\n"
"buffer_load_dwordx4 v1, s[4:7], s31 offen offset:3072 sc1 lds\n"
"v_mfma_f32_16x16x32_bf16 v[28:31], v[184:187], v[68:71], v[28:31]\n"
"ds_read_b64 v[64:65], v4 offset:0\n"
"v_add_f32 v36, v36, v38\n"
"v_mul_f32 v89, 0x3acccccd, v40\n"
"s_add_u32 m0, m0, 1024\n"
"v_mfma_f32_16x16x32_bf16 v[32:35], v[188:191], v[68:71], v[32:35]\n"
"ds_read_b64 v[66:67], v4 offset:256\n"
"v_fma_f32 v36, -v40, v89, v36\n"
"s_add_u32 s31, s31, 1024\n"
"s_waitcnt lgkmcnt(4)\n"
"v_mfma_f32_16x16x32_bf16 v[28:31], v[200:203], v[72:75], v[28:31]\n"
"ds_read_b64 v[68:69], v5 offset:0\n"
"v_fmamk_f32 v36, v36, 0x3acccccd, v27\n"
"v_cmp_u_f32 vcc, v36, v36\n"
"v_mfma_f32_16x16x32_bf16 v[32:35], v[204:207], v[72:75], v[32:35]\n"
"ds_read_b64 v[70:71], v5 offset:256\n"
"v_max_f32 v36, v36, v27\n"
"v_rsq_f32 v88, v36\n"
"buffer_load_dwordx4 v1, s[4:7], s31 offen offset:3072 sc1 lds\n"
"v_mfma_f32_16x16x32_bf16 v[28:31], v[208:211], v[56:59], v[28:31]\n"
"ds_read_b64 v[72:73], v6 offset:0\n"
"v_mul_f32 v89, -v89, v88\n"
"v_mfma_f32_16x16x32_bf16 v[32:35], v[212:215], v[56:59], v[32:35]\n"
"ds_read_b64 v[74:75], v6 offset:256\n"
"s_cbranch_vccnz .Lsl_134\n"
".Lbk_135:\n"
"s_waitcnt lgkmcnt(4)\n"
"v_mfma_f32_16x16x32_bf16 v[28:31], v[216:219], v[60:63], v[28:31]\n"
"ds_read_b64 v[56:57], v3 offset:20480\n"
"v_mfma_f32_16x16x32_bf16 v[32:35], v[220:223], v[60:63], v[32:35]\n"
"ds_read_b64 v[58:59], v3 offset:20736\n"
"v_mfma_f32_16x16x32_bf16 v[28:31], v[232:235], v[64:67], v[28:31]\n"
"ds_read_b64 v[60:61], v3 offset:21504\n"
"v_mfma_f32_16x16x32_bf16 v[32:35], v[236:239], v[64:67], v[32:35]\n"
"ds_read_b64 v[62:63], v3 offset:21760\n"
"s_waitcnt lgkmcnt(4)\n"
"v_mfma_f32_16x16x32_bf16 v[28:31], v[240:243], v[68:71], v[28:31]\n"
"ds_read_b64 v[64:65], v3 offset:22528\n"
"v_mfma_f32_16x16x32_bf16 v[32:35], v[244:247], v[68:71], v[32:35]\n"
"ds_read_b64 v[66:67], v3 offset:22784\n"
"v_mfma_f32_16x16x32_bf16 v[28:31], v[248:251], v[72:75], v[28:31]\n"
"ds_read_b64 v[68:69], v3 offset:23552\n"
"v_mfma_f32_16x16x32_bf16 v[32:35], v[252:255], v[72:75], v[32:35]\n"
"ds_read_b64 v[70:71], v3 offset:23808\n"
"s_waitcnt lgkmcnt(4)\n"
"v_mfma_f32_16x16x32_bf16 v[36:39], v[96:99], v[56:59], 0\n"
"ds_read_b64 v[72:73], v3 offset:24576\n"
"v_mfma_f32_16x16x32_bf16 v[40:43], v[100:103], v[56:59], 0\n"
"ds_read_b64 v[74:75], v3 offset:24832\n"
"v_mfma_f32_16x16x32_bf16 v[52:55], v[56:59], v[56:59], 0\n"
"v_mul_f32 v32, v88, v32\n"
"v_mul_f32 v33, v88, v33\n"
"v_mul_f32 v34, v88, v34\n"
"v_mfma_f32_16x16x32_bf16 v[36:39], v[104:107], v[60:63], v[36:39]\n"
"ds_read_b64 v[56:57], v3 offset:25600\n"
"v_mul_f32 v35, v88, v35\n"
"v_fmac_f32 v32, v89, v20\n"
"v_mfma_f32_16x16x32_bf16 v[40:43], v[108:111], v[60:63], v[40:43]\n"
"ds_read_b64 v[58:59], v3 offset:25856\n"
"v_fmac_f32 v33, v89, v21\n"
"s_waitcnt lgkmcnt(4)\n"
"v_mfma_f32_16x16x32_bf16 v[36:39], v[112:115], v[64:67], v[36:39]\n"
"ds_read_b64 v[60:61], v3 offset:26624\n"
"v_fmac_f32 v34, v89, v22\n"
"v_fmac_f32 v35, v89, v23\n"
"v_mfma_f32_16x16x32_bf16 v[40:43], v[116:119], v[64:67], v[40:43]\n"
"ds_read_b64 v[62:63], v3 offset:26880\n"
"v_cvt_pk_bf16_f32 v32, v32, v33\n"
"v_cvt_pk_bf16_f32 v34, v34, v35\n"
"v_mfma_f32_16x16x32_bf16 v[36:39], v[120:123], v[68:71], v[36:39]\n"
"ds_read_b64 v[64:65], v3 offset:27648\n"
"v_mov_b32 v33, s16\n"
"v_mov_b32 v35, s16\n"
"v_mfma_f32_16x16x32_bf16 v[40:43], v[124:127], v[68:71], v[40:43]\n"
"ds_read_b64 v[66:67], v3 offset:27904\n"
"buffer_store_dwordx4 v[32:35], v1, s[4:7], s24 offen offset:1024 sc1\n"
"s_waitcnt lgkmcnt(4)\n"
"v_mfma_f32_16x16x32_bf16 v[36:39], v[128:131], v[72:75], v[36:39]\n"
"ds_read_b64 v[68:69], v3 offset:28672\n"
"v_mul_f32 v28, v88, v28\n"
"v_mul_f32 v29, v88, v29\n"
"v_mfma_f32_16x16x32_bf16 v[40:43], v[132:135], v[72:75], v[40:43]\n"
"ds_read_b64 v[70:71], v3 offset:28928\n"
"v_mul_f32 v30, v88, v30\n"
"v_mul_f32 v31, v88, v31\n"
"v_mfma_f32_16x16x32_bf16 v[52:55], v[72:75], v[72:75], v[52:55]\n"
"v_fmac_f32 v28, v89, v16\n"
"v_fmac_f32 v29, v89, v17\n"
"v_mfma_f32_16x16x32_bf16 v[36:39], v[136:139], v[56:59], v[36:39]\n"
"ds_read_b64 v[72:73], v3 offset:29696\n"
"v_fmac_f32 v30, v89, v18\n"
"v_mfma_f32_16x16x32_bf16 v[40:43], v[140:143], v[56:59], v[40:43]\n"
"ds_read_b64 v[74:75], v3 offset:29952\n"
"s_waitcnt lgkmcnt(4)\n"
"v_mfma_f32_16x16x32_bf16 v[36:39], v[144:147], v[60:63], v[36:39]\n"
"ds_read_b64 v[56:57], v3 offset:30720\n"
"v_fmac_f32 v31, v89, v19\n"
"v_mfma_f32_16x16x32_bf16 v[40:43], v[148:151], v[60:63], v[40:43]\n"
"ds_read_b64 v[58:59], v3 offset:30976\n"
"v_cvt_pk_bf16_f32 v28, v28, v29\n"
"v_mfma_f32_16x16x32_bf16 v[36:39], v[152:155], v[64:67], v[36:39]\n"
"ds_read_b64 v[60:61], v3 offset:31744\n"
"v_cvt_pk_bf16_f32 v30, v30, v31\n"
"v_mfma_f32_16x16x32_bf16 v[40:43], v[156:159], v[64:67], v[40:43]\n"
"ds_read_b64 v[62:63], v3 offset:32000\n"
"s_waitcnt lgkmcnt(4)\n"
"v_mfma_f32_16x16x32_bf16 v[36:39], v[160:163], v[68:71], v[36:39]\n"
"ds_read_b64 v[64:65], v3 offset:32768\n"
"v_mov_b32 v29, s16\n"
"v_mfma_f32_16x16x32_bf16 v[40:43], v[164:167], v[68:71], v[40:43]\n"
"ds_read_b64 v[66:67], v3 offset:33024\n"
"v_mov_b32 v31, s16\n"
"v_mfma_f32_16x16x32_bf16 v[52:55], v[68:71], v[68:71], v[52:55]\n"
"s_waitcnt vmcnt(6)\n"
"ds_write_b32 v13, v13 offset:288\n"
"v_mfma_f32_16x16x32_bf16 v[36:39], v[168:171], v[72:75], v[36:39]\n"
"ds_read_b64 v[68:69], v3 offset:33792\n"
"buffer_store_dwordx4 v[28:31], v93, s[4:7], s24 offen offset:0 sc1\n"
"v_mfma_f32_16x16x32_bf16 v[40:43], v[172:175], v[72:75], v[40:43]\n"
"ds_read_b64 v[70:71], v3 offset:34048\n"
"s_waitcnt lgkmcnt(5)\n"
"v_mfma_f32_16x16x32_bf16 v[36:39], v[176:179], v[56:59], v[36:39]\n"
"ds_read_b64 v[72:73], v3 offset:34816\n"
"v_mfma_f32_16x16x32_bf16 v[40:43], v[180:183], v[56:59], v[40:43]\n"
"ds_read_b64 v[74:75], v3 offset:35072\n"
"v_mfma_f32_16x16x32_bf16 v[36:39], v[184:187], v[60:63], v[36:39]\n"
"ds_read_b64 v[56:57], v3 offset:35840\n"
"v_mfma_f32_16x16x32_bf16 v[40:43], v[188:191], v[60:63], v[40:43]\n"
"ds_read_b64 v[58:59], v3 offset:36096\n"
"s_waitcnt lgkmcnt(4)\n"
"v_mfma_f32_16x16x32_bf16 v[36:39], v[192:195], v[64:67], v[36:39]\n"
"ds_read_b64 v[60:61], v3 offset:36864\n"
"v_mfma_f32_16x16x32_bf16 v[40:43], v[196:199], v[64:67], v[40:43]\n"
"ds_read_b64 v[62:63], v3 offset:37120\n"
"v_mfma_f32_16x16x32_bf16 v[52:55], v[64:67], v[64:67], v[52:55]\n"
"ds_read_b32 v90, v14 offset:288\n"
"v_mfma_f32_16x16x32_bf16 v[36:39], v[200:203], v[68:71], v[36:39]\n"
"ds_read_b64 v[64:65], v4 offset:20480\n"
"v_mfma_f32_16x16x32_bf16 v[40:43], v[204:207], v[68:71], v[40:43]\n"
"ds_read_b64 v[66:67], v4 offset:20736\n"
"s_waitcnt lgkmcnt(5)\n"
"v_mfma_f32_16x16x32_bf16 v[36:39], v[208:211], v[72:75], v[36:39]\n"
"ds_read_b64 v[68:69], v5 offset:20480\n"
"v_mfma_f32_16x16x32_bf16 v[40:43], v[212:215], v[72:75], v[40:43]\n"
"ds_read_b64 v[70:71], v5 offset:20736\n"
"v_mfma_f32_16x16x32_bf16 v[36:39], v[216:219], v[56:59], v[36:39]\n"
"ds_read_b64 v[72:73], v6 offset:20480\n"
"s_waitcnt lgkmcnt(5)\n"
"v_cmp_eq_u32 vcc, 0, v90\n"
"v_mfma_f32_16x16x32_bf16 v[40:43], v[220:223], v[56:59], v[40:43]\n"
"ds_read_b64 v[74:75], v6 offset:20736\n"
"s_waitcnt lgkmcnt(4)\n"
"v_mfma_f32_16x16x32_bf16 v[36:39], v[224:227], v[60:63], v[36:39]\n"
"s_cbranch_vccnz .Lsl_130\n"
".Lbk_131:\n"
"ds_read_b64 v[56:57], v3 offset:40960\n"
"v_mfma_f32_16x16x32_bf16 v[40:43], v[228:231], v[60:63], v[40:43]\n"
"ds_read_b64 v[58:59], v3 offset:41216\n"
"v_mov_b32 v29, v30\n"
"v_mfma_f32_16x16x32_bf16 v[52:55], v[60:63], v[60:63], v[52:55]\n"
"v_mov_b32 v33, v34\n"
"v_mfma_f32_16x16x32_bf16 v[36:39], v[232:235], v[64:67], v[36:39]\n"
"ds_read_b64 v[60:61], v3 offset:41984\n"
"v_mfma_f32_16x16x32_bf16 v[40:43], v[236:239], v[64:67], v[40:43]\n"
"ds_read_b64 v[62:63], v3 offset:42240\n"
"s_waitcnt lgkmcnt(4)\n"
"v_mfma_f32_16x16x32_bf16 v[36:39], v[240:243], v[68:71], v[36:39]\n"
"ds_read_b64 v[64:65], v3 offset:43008\n"
"v_cndmask_b32 v94, v52, v53, s[34:35]\n"
"v_mfma_f32_16x16x32_bf16 v[40:43], v[244:247], v[68:71], v[40:43]\n"
"ds_read_b64 v[66:67], v3 offset:43264\n"
"v_cndmask_b32 v54, v54, v55, s[34:35]\n"
"v_mfma_f32_16x16x32_bf16 v[36:39], v[248:251], v[72:75], v[36:39]\n"
"ds_read_b64 v[68:69], v3 offset:44032\n"
"v_cndmask_b32 v94, v94, v54, s[20:21]\n"
"v_mfma_f32_16x16x32_bf16 v[40:43], v[252:255], v[72:75], v[40:43]\n"
"ds_read_b64 v[70:71], v3 offset:44288\n"
"s_waitcnt lgkmcnt(4)\n"
"v_mfma_f32_16x16x32_bf16 v[44:47], v[96:99], v[56:59], 0\n"
"ds_read_b64 v[72:73], v3 offset:45056\n"
"v_mfma_f32_16x16x32_bf16 v[48:51], v[100:103], v[56:59], 0\n"
"ds_read_b64 v[74:75], v3 offset:45312\n"
"v_cndmask_b32 v94, v36, v94, s[22:23]\n"
"s_waitcnt vmcnt(2)\n"
"v_mfma_f32_16x16x32_bf16 v[52:55], v[56:59], v[56:59], 0\n"
"ds_write_b32 v9, v94 offset:2176\n"
"v_mfma_f32_16x16x32_bf16 v[44:47], v[104:107], v[60:63], v[44:47]\n"
"ds_read_b64 v[56:57], v3 offset:46080\n"
"v_mfma_f32_16x16x32_bf16 v[48:51], v[108:111], v[60:63], v[48:51]\n"
"ds_read_b64 v[58:59], v3 offset:46336\n"
"s_waitcnt lgkmcnt(5)\n"
"v_mfma_f32_16x16x32_bf16 v[44:47], v[112:115], v[64:67], v[44:47]\n"
"ds_read_b64 v[60:61], v3 offset:47104\n"
"v_mfma_f32_16x16x32_bf16 v[48:51], v[116:119], v[64:67], v[48:51]\n"
"ds_read_b64 v[62:63], v3 offset:47360\n"
"v_mfma_f32_16x16x32_bf16 v[44:47], v[120:123], v[68:71], v[44:47]\n"
"ds_read_b64 v[64:65], v3 offset:48128\n"
"v_mfma_f32_16x16x32_bf16 v[48:51], v[124:127], v[68:71], v[48:51]\n"
"ds_read_b64 v[66:67], v3 offset:48384\n"
"s_waitcnt lgkmcnt(4)\n"
"v_mfma_f32_16x16x32_bf16 v[44:47], v[128:131], v[72:75], v[44:47]\n"
"ds_read_b64 v[68:69], v3 offset:49152\n"
"v_mfma_f32_16x16x32_bf16 v[48:51], v[132:135], v[72:75], v[48:51]\n"
"ds_read_b64 v[70:71], v3 offset:49408\n"
"v_mfma_f32_16x16x32_bf16 v[52:55], v[72:75], v[72:75], v[52:55]\n"
"v_mfma_f32_16x16x32_bf16 v[44:47], v[136:139], v[56:59], v[44:47]\n"
"ds_read_b64 v[72:73], v3 offset:50176\n"
"v_mfma_f32_16x16x32_bf16 v[48:51], v[140:143], v[56:59], v[48:51]\n"
"ds_read_b64 v[74:75], v3 offset:50432\n"
"s_waitcnt lgkmcnt(4)\n"
"v_mfma_f32_16x16x32_bf16 v[44:47], v[144:147], v[60:63], v[44:47]\n"
"ds_read_b64 v[56:57], v3 offset:51200\n"
"ds_read_b128 v[84:87], v11 offset:2176\n"
"ds_read_b32 v91, v12 offset:2176\n"
"v_mfma_f32_16x16x32_bf16 v[48:51], v[148:151], v[60:63], v[48:51]\n"
"ds_read_b64 v[58:59], v3 offset:51456\n"
"v_mfma_f32_16x16x32_bf16 v[44:47], v[152:155], v[64:67], v[44:47]\n"
"ds_read_b64 v[60:61], v3 offset:52224\n"
"v_mfma_f32_16x16x32_bf16 v[48:51], v[156:159], v[64:67], v[48:51]\n"
"ds_read_b64 v[62:63], v3 offset:52480\n"
"s_waitcnt lgkmcnt(6)\n"
"v_mfma_f32_16x16x32_bf16 v[44:47], v[160:163], v[68:71], v[44:47]\n"
"ds_read_b64 v[64:65], v3 offset:53248\n"
"v_mfma_f32_16x16x32_bf16 v[48:51], v[164:167], v[68:71], v[48:51]\n"
"ds_read_b64 v[66:67], v3 offset:53504\n"
"v_mfma_f32_16x16x32_bf16 v[52:55], v[68:71], v[68:71], v[52:55]\n"
"v_mfma_f32_16x16x32_bf16 v[44:47], v[168:171], v[72:75], v[44:47]\n"
"ds_read_b64 v[68:69], v3 offset:54272\n"
"s_waitcnt lgkmcnt(7)\n"
"v_add_f32 v84, v84, v85\n"
"v_add_f32 v86, v86, v87\n"
"v_mfma_f32_16x16x32_bf16 v[48:51], v[172:175], v[72:75], v[48:51]\n"
"ds_read_b64 v[70:71], v3 offset:54528\n"
"v_add_f32 v84, v84, v86\n"
"s_waitcnt lgkmcnt(4)\n"
"v_mfma_f32_16x16x32_bf16 v[44:47], v[176:179], v[56:59], v[44:47]\n"
"ds_read_b64 v[72:73], v3 offset:55296\n"
"v_mul_f32 v89, 0x3acccccd, v91\n"
"v_fma_f32 v84, -v91, v89, v84\n"
"v_mfma_f32_16x16x32_bf16 v[48:51], v[180:183], v[56:59], v[48:51]\n"
"ds_read_b64 v[74:75], v3 offset:55552\n"
"v_fmamk_f32 v84, v84, 0x3acccccd, v27\n"
"v_mfma_f32_16x16x32_bf16 v[44:47], v[184:187], v[60:63], v[44:47]\n"
"ds_read_b64 v[56:57], v3 offset:56320\n"
"v_cmp_u_f32 vcc, v84, v84\n"
"v_mfma_f32_16x16x32_bf16 v[48:51], v[188:191], v[60:63], v[48:51]\n"
"ds_read_b64 v[58:59], v3 offset:56576\n"
"s_waitcnt lgkmcnt(4)\n"
"v_mfma_f32_16x16x32_bf16 v[44:47], v[192:195], v[64:67], v[44:47]\n"
"ds_read_b64 v[60:61], v3 offset:57344\n"
"v_max_f32 v84, v84, v27\n"
"v_mfma_f32_16x16x32_bf16 v[48:51], v[196:199], v[64:67], v[48:51]\n"
"ds_read_b64 v[62:63], v3 offset:57600\n"
"v_rsq_f32 v88, v84\n"
"v_mfma_f32_16x16x32_bf16 v[52:55], v[64:67], v[64:67], v[52:55]\n"
"v_mul_f32 v89, -v89, v88\n"
"v_mfma_f32_16x16x32_bf16 v[44:47], v[200:203], v[68:71], v[44:47]\n"
"ds_read_b64 v[64:65], v4 offset:40960\n"
"s_cbranch_vccnz .Lsl_138\n"
".Lbk_139:\n"
"v_mfma_f32_16x16x32_bf16 v[48:51], v[204:207], v[68:71], v[48:51]\n"
"ds_read_b64 v[66:67], v4 offset:41216\n"
"ds_write_b64 v7, v[28:29] offset:15872\n"
"s_waitcnt lgkmcnt(5)\n"
"v_mfma_f32_16x16x32_bf16 v[44:47], v[208:211], v[72:75], v[44:47]\n"
"ds_read_b64 v[68:69], v5 offset:40960\n"
"v_mul_f32 v36, v88, v36\n"
"v_mfma_f32_16x16x32_bf16 v[48:51], v[212:215], v[72:75], v[48:51]\n"
"ds_read_b64 v[70:71], v5 offset:41216\n"
"v_mul_f32 v37, v88, v37\n"
"v_mfma_f32_16x16x32_bf16 v[44:47], v[216:219], v[56:59], v[44:47]\n"
"ds_read_b64 v[72:73], v6 offset:40960\n"
"v_mul_f32 v38, v88, v38\n"
"v_mfma_f32_16x16x32_bf16 v[48:51], v[220:223], v[56:59], v[48:51]\n"
"ds_read_b64 v[74:75], v6 offset:41216\n"
"s_waitcnt lgkmcnt(5)\n"
"v_mfma_f32_16x16x32_bf16 v[44:47], v[224:227], v[60:63], v[44:47]\n"
"v_add_u32 v3, 0x5000, v3\n"
"ds_read_b64 v[56:57], v3 offset:40960\n"
"v_mfma_f32_16x16x32_bf16 v[48:51], v[228:231], v[60:63], v[48:51]\n"
"ds_read_b64 v[58:59], v3 offset:41216\n"
"v_mul_f32 v39, v88, v39\n"
"v_mfma_f32_16x16x32_bf16 v[52:55], v[60:63], v[60:63], v[52:55]\n"
"ds_write_b64 v8, v[32:33] offset:15872\n"
"v_mfma_f32_16x16x32_bf16 v[44:47], v[232:235], v[64:67], v[44:47]\n"
"ds_read_b64 v[60:61], v3 offset:45056\n"
"v_fmac_f32 v36, v89, v16\n"
"v_mul_f32 v40, v88, v40\n"
"v_mfma_f32_16x16x32_bf16 v[48:51], v[236:239], v[64:67], v[48:51]\n"
"ds_read_b64 v[62:63], v3 offset:45312\n"
"v_mul_f32 v41, v88, v41\n"
"s_waitcnt lgkmcnt(5)\n"
"v_mfma_f32_16x16x32_bf16 v[44:47], v[240:243], v[68:71], v[44:47]\n"
"ds_read_b64 v[64:65], v3 offset:49152\n"
"v_cndmask_b32 v94, v52, v53, s[34:35]\n"
"v_mul_f32 v42, v88, v42\n"
"v_mfma_f32_16x16x32_bf16 v[48:51], v[244:247], v[68:71], v[48:51]\n"
"ds_read_b64 v[66:67], v3 offset:49408\n"
"v_cndmask_b32 v54, v54, v55, s[34:35]\n"
"v_mfma_f32_16x16x32_bf16 v[44:47], v[248:251], v[72:75], v[44:47]\n"
"ds_read_b64 v[68:69], v3 offset:53248\n"
"v_cndmask_b32 v94, v94, v54, s[20:21]\n"
"v_mfma_f32_16x16x32_bf16 v[48:51], v[252:255], v[72:75], v[48:51]\n"
"ds_read_b64 v[70:71], v3 offset:53504\n"
"s_waitcnt lgkmcnt(4)\n"
"v_mfma_f32_16x16x32_bf16 v[28:31], v[96:99], v[56:59], 0\n"
"ds_read_b64 v[72:73], v3 offset:57344\n"
"v_fmac_f32 v37, v89, v17\n"
"v_mfma_f32_16x16x32_bf16 v[32:35], v[100:103], v[56:59], 0\n"
"ds_read_b64 v[74:75], v3 offset:57600\n"
"v_cndmask_b32 v94, v44, v94, s[22:23]\n"
"v_mfma_f32_16x16x32_bf16 v[52:55], v[56:59], v[56:59], 0\n"
"ds_write_b32 v9, v94 offset:2496\n"
"v_mfma_f32_16x16x32_bf16 v[76:79], v[80:83], v[56:59], 0\n"
"v_fmac_f32 v38, v89, v18\n"
"v_fmac_f32 v39, v89, v19\n"
"v_mfma_f32_16x16x32_bf16 v[28:31], v[128:131], v[60:63], v[28:31]\n"
"ds_read_b64 v[56:57], v3 offset:41984\n"
"v_cvt_pk_bf16_f32 v36, v36, v37\n"
"v_mfma_f32_16x16x32_bf16 v[32:35], v[132:135], v[60:63], v[32:35]\n"
"ds_read_b64 v[58:59], v3 offset:42240\n"
"v_cvt_pk_bf16_f32 v37, v38, v39\n"
"v_mfma_f32_16x16x32_bf16 v[52:55], v[60:63], v[60:63], v[52:55]\n"
"ds_write_b64 v7, v[36:37] offset:3968\n"
"v_mfma_f32_16x16x32_bf16 v[76:79], v[80:83], v[60:63], v[76:79]\n"
"v_mul_f32 v43, v88, v43\n"
"s_waitcnt lgkmcnt(6)\n"
"v_mfma_f32_16x16x32_bf16 v[28:31], v[160:163], v[64:67], v[28:31]\n"
"ds_read_b64 v[60:61], v3 offset:43008\n"
"v_fmac_f32 v40, v89, v20\n"
"v_mfma_f32_16x16x32_bf16 v[32:35], v[164:167], v[64:67], v[32:35]\n"
"ds_read_b64 v[62:63], v3 offset:43264\n"
"v_fmac_f32 v41, v89, v21\n"
"v_mfma_f32_16x16x32_bf16 v[52:55], v[64:67], v[64:67], v[52:55]\n"
"ds_read_b128 v[84:87], v11 offset:2496\n"
"ds_read_b32 v91, v12 offset:2496\n"
"v_mfma_f32_16x16x32_bf16 v[76:79], v[80:83], v[64:67], v[76:79]\n"
"v_fmac_f32 v42, v89, v22\n"
"v_fmac_f32 v43, v89, v23\n"
"v_mfma_f32_16x16x32_bf16 v[28:31], v[192:195], v[68:71], v[28:31]\n"
"ds_read_b64 v[64:65], v3 offset:44032\n"
"v_cvt_pk_bf16_f32 v40, v40, v41\n"
"v_mfma_f32_16x16x32_bf16 v[32:35], v[196:199], v[68:71], v[32:35]\n"
"ds_read_b64 v[66:67], v3 offset:44288\n"
"v_cvt_pk_bf16_f32 v41, v42, v43\n"
"v_mfma_f32_16x16x32_bf16 v[52:55], v[68:71], v[68:71], v[52:55]\n"
"ds_write_b64 v8, v[40:41] offset:3968\n"
"v_mfma_f32_16x16x32_bf16 v[76:79], v[80:83], v[68:71], v[76:79]\n"
"s_waitcnt lgkmcnt(8)\n"
"v_mfma_f32_16x16x32_bf16 v[28:31], v[224:227], v[72:75], v[28:31]\n"
"ds_read_b64 v[68:69], v3 offset:46080\n"
"v_mfma_f32_16x16x32_bf16 v[32:35], v[228:231], v[72:75], v[32:35]\n"
"ds_read_b64 v[70:71], v3 offset:46336\n"
"s_waitcnt lgkmcnt(6)\n"
"v_add_f32 v84, v84, v85\n"
"v_add_f32 v86, v86, v87\n"
"v_mfma_f32_16x16x32_bf16 v[52:55], v[72:75], v[72:75], v[52:55]\n"
"v_add_f32 v84, v84, v86\n"
"s_waitcnt lgkmcnt(5)\n"
"v_mul_f32 v89, 0x3acccccd, v91\n"
"v_fma_f32 v84, -v91, v89, v84\n"
"v_mfma_f32_16x16x32_bf16 v[76:79], v[80:83], v[72:75], v[76:79]\n"
"v_fmamk_f32 v84, v84, 0x3acccccd, v27\n"
"v_cmp_u_f32 vcc, v84, v84\n"
"v_max_f32 v84, v84, v27\n"
"v_mfma_f32_16x16x32_bf16 v[28:31], v[104:107], v[56:59], v[28:31]\n"
"ds_read_b64 v[72:73], v3 offset:47104\n"
"v_rsq_f32 v88, v84\n"
"v_mfma_f32_16x16x32_bf16 v[32:35], v[108:111], v[56:59], v[32:35]\n"
"ds_read_b64 v[74:75], v3 offset:47360\n"
"v_cndmask_b32 v94, v52, v53, s[34:35]\n"
"v_cndmask_b32 v54, v54, v55, s[34:35]\n"
"s_waitcnt lgkmcnt(5)\n"
"v_mfma_f32_16x16x32_bf16 v[28:31], v[112:115], v[60:63], v[28:31]\n"
"ds_read_b64 v[56:57], v3 offset:48128\n"
"v_mul_f32 v89, -v89, v88\n"
"v_cndmask_b32 v94, v94, v54, s[20:21]\n"
"ds_write_b32 v10, v76 offset:3456\n"
"v_mfma_f32_16x16x32_bf16 v[32:35], v[116:119], v[60:63], v[32:35]\n"
"ds_read_b64 v[58:59], v3 offset:48384\n"
"s_cbranch_vccnz .Lsl_142\n"
".Lbk_143:\n"
"ds_write_b32 v9, v94 offset:2816\n"
"v_mfma_f32_16x16x32_bf16 v[28:31], v[120:123], v[64:67], v[28:31]\n"
"ds_read_b64 v[60:61], v3 offset:50176\n"
"v_mul_f32 v44, v88, v44\n"
"v_mul_f32 v45, v88, v45\n"
"v_mfma_f32_16x16x32_bf16 v[32:35], v[124:127], v[64:67], v[32:35]\n"
"ds_read_b64 v[62:63], v3 offset:50432\n"
"v_mul_f32 v46, v88, v46\n"
"s_waitcnt lgkmcnt(6)\n"
"v_mfma_f32_16x16x32_bf16 v[28:31], v[136:139], v[68:71], v[28:31]\n"
"ds_read_b64 v[64:65], v3 offset:51200\n"
"buffer_load_dwordx4 v[40:43], v1, s[4:7], s25 offen offset:1024 sc1\n"
"v_mul_f32 v47, v88, v47\n"
"v_mfma_f32_16x16x32_bf16 v[32:35], v[140:143], v[68:71], v[32:35]\n"
"ds_read_b64 v[66:67], v3 offset:51456\n"
"buffer_load_dwordx4 v[36:39], v93, s[4:7], s25 offen offset:0 sc1\n"
"v_fmac_f32 v44, v89, v16\n"
"v_mfma_f32_16x16x32_bf16 v[28:31], v[144:147], v[72:75], v[28:31]\n"
"ds_read_b64 v[68:69], v3 offset:52224\n"
"v_fmac_f32 v45, v89, v17\n"
"v_fmac_f32 v46, v89, v18\n"
"v_mfma_f32_16x16x32_bf16 v[32:35], v[148:151], v[72:75], v[32:35]\n"
"ds_read_b64 v[70:71], v3 offset:52480\n"
"v_fmac_f32 v47, v89, v19\n"
"s_waitcnt lgkmcnt(4)\n"
"v_mfma_f32_16x16x32_bf16 v[28:31], v[152:155], v[56:59], v[28:31]\n"
"ds_read_b64 v[72:73], v3 offset:54272\n"
"v_cvt_pk_bf16_f32 v44, v44, v45\n"
"v_cvt_pk_bf16_f32 v45, v46, v47\n"
"v_mfma_f32_16x16x32_bf16 v[32:35], v[156:159], v[56:59], v[32:35]\n"
"ds_read_b64 v[74:75], v3 offset:54528\n"
"ds_write_b64 v7, v[44:45] offset:7936\n"
"v_mfma_f32_16x16x32_bf16 v[28:31], v[168:171], v[60:63], v[28:31]\n"
"ds_read_b64 v[56:57], v3 offset:55296\n"
"ds_read_b128 v[84:87], v11 offset:2816\n"
"ds_read_b128 v[76:79], v11 offset:3456\n"
"v_mfma_f32_16x16x32_bf16 v[32:35], v[172:175], v[60:63], v[32:35]\n"
"ds_read_b64 v[58:59], v3 offset:55552\n"
"v_mul_f32 v48, v88, v48\n"
"s_waitcnt lgkmcnt(7)\n"
"v_mfma_f32_16x16x32_bf16 v[28:31], v[176:179], v[64:67], v[28:31]\n"
"ds_read_b64 v[60:61], v3 offset:56320\n"
"v_mul_f32 v49, v88, v49\n"
"v_mul_f32 v50, v88, v50\n"
"v_mfma_f32_16x16x32_bf16 v[32:35], v[180:183], v[64:67], v[32:35]\n"
"ds_read_b64 v[62:63], v3 offset:56576\n"
"v_mul_f32 v51, v88, v51\n"
"v_fmac_f32 v48, v89, v20\n"
"v_mfma_f32_16x16x32_bf16 v[28:31], v[184:187], v[68:71], v[28:31]\n"
"v_add_u32 v3, 0xffffb000, v3\n"
"ds_read_b64 v[64:65], v4 offset:61440\n"
"v_fmac_f32 v49, v89, v21\n"
"v_mfma_f32_16x16x32_bf16 v[32:35], v[188:191], v[68:71], v[32:35]\n"
"ds_read_b64 v[66:67], v4 offset:61696\n"
"v_fmac_f32 v50, v89, v22\n"
"s_waitcnt lgkmcnt(4)\n"
"v_mfma_f32_16x16x32_bf16 v[28:31], v[200:203], v[72:75], v[28:31]\n"
"ds_read_b64 v[68:69], v5 offset:61440\n"
"v_fmac_f32 v51, v89, v23\n"
"v_cvt_pk_bf16_f32 v48, v48, v49\n"
"v_mfma_f32_16x16x32_bf16 v[32:35], v[204:207], v[72:75], v[32:35]\n"
"ds_read_b64 v[70:71], v5 offset:61696\n"
"v_add_f32 v84, v84, v85\n"
"v_cvt_pk_bf16_f32 v49, v50, v51\n"
"v_mfma_f32_16x16x32_bf16 v[28:31], v[208:211], v[56:59], v[28:31]\n"
"ds_read_b64 v[72:73], v6 offset:61440\n"
"v_add_f32 v86, v86, v87\n"
"ds_write_b64 v8, v[48:49] offset:7936\n"
"v_mfma_f32_16x16x32_bf16 v[32:35], v[212:215], v[56:59], v[32:35]\n"
"ds_read_b64 v[74:75], v6 offset:61696\n"
"s_waitcnt lgkmcnt(5)\n"
"v_mfma_f32_16x16x32_bf16 v[28:31], v[216:219], v[60:63], v[28:31]\n"
"v_add_f32 v76, v76, v77\n"
"v_add_f32 v78, v78, v79\n"
"v_mfma_f32_16x16x32_bf16 v[32:35], v[220:223], v[60:63], v[32:35]\n"
"v_add_f32 v76, v76, v78\n"
"v_add_f32 v84, v84, v86\n"
"v_mfma_f32_16x16x32_bf16 v[28:31], v[232:235], v[64:67], v[28:31]\n"
"v_mul_f32 v53, 0x3acccccd, v76\n"
"v_fma_f32 v84, -v76, v53, v84\n"
"v_mfma_f32_16x16x32_bf16 v[32:35], v[236:239], v[64:67], v[32:35]\n"
"v_fmamk_f32 v84, v84, 0x3acccccd, v27\n"
"s_waitcnt lgkmcnt(0)\n"
"v_mfma_f32_16x16x32_bf16 v[28:31], v[240:243], v[68:71], v[28:31]\n"
"v_cmp_u_f32 vcc, v84, v84\n"
"v_max_f32 v84, v84, v27\n"
"v_mfma_f32_16x16x32_bf16 v[32:35], v[244:247], v[68:71], v[32:35]\n"
"v_rsq_f32 v52, v84\n"
"v_mfma_f32_16x16x32_bf16 v[28:31], v[248:251], v[72:75], v[28:31]\n"
"v_mul_f32 v53, -v53, v52\n"
"v_mfma_f32_16x16x32_bf16 v[32:35], v[252:255], v[72:75], v[32:35]\n"
"s_cbranch_vccnz .Lsl_146\n"
".Lbk_147:\n"
"v_cmp_u_f32 vcc, v92, v92\n"
"s_nop 2\n"
"v_mul_f32 v28, v52, v28\n"
"v_mul_f32 v29, v52, v29\n"
"v_mul_f32 v30, v52, v30\n"
"v_mul_f32 v31, v52, v31\n"
"v_fmac_f32 v28, v53, v16\n"
"v_fmac_f32 v29, v53, v17\n"
"v_fmac_f32 v30, v53, v18\n"
"v_fmac_f32 v31, v53, v19\n"
"v_cvt_pk_bf16_f32 v28, v28, v29\n"
"v_cvt_pk_bf16_f32 v29, v30, v31\n"
"ds_write_b64 v7, v[28:29] offset:11904\n"
"s_cbranch_vccnz .Lrd_120\n"
".Lgo_119:\n"
"v_mul_f32 v32, v52, v32\n"
"v_mul_f32 v33, v52, v33\n"
"v_mul_f32 v34, v52, v34\n"
"v_mul_f32 v35, v52, v35\n"
"v_fmac_f32 v32, v53, v20\n"
"v_fmac_f32 v33, v53, v21\n"
"s_waitcnt vmcnt(0)\n"
"v_bitop3_b32 v84, v37, v39, s16 bitop3:0x7e\n"
"v_bitop3_b32 v85, v41, v43, s16 bitop3:0x7e\n"
"v_and_b32 v84, s26, v84\n"
"v_and_or_b32 v84, v85, s27, v84\n"
"v_cmp_ne_u32 vcc, 0, v84\n"
"v_fmac_f32 v34, v53, v22\n"
"v_fmac_f32 v35, v53, v23\n"
"v_cvt_pk_bf16_f32 v32, v32, v33\n"
"v_cvt_pk_bf16_f32 v33, v34, v35\n"
"ds_write_b64 v8, v[32:33] offset:11904\n"
"s_cbranch_vccnz .Lsl_150\n"
".Lbk_151:\n"
"v_mov_b32 v37, v38\n"
"v_mov_b32 v41, v42\n"
"ds_write_b64 v8, v[40:41] offset:0\n"
"ds_write_b64 v7, v[36:37] offset:0\n"
"ds_write_b32 v13, v13 offset:304\n"
"v_mov_b32 v46, 0\n"
"v_mov_b32 v47, 0\n"
"v_mov_b32 v50, 0\n"
"v_mov_b32 v51, 0\n"
"v_mov_b32 v72, s42\n"
"v_mov_b32 v73, s42\n"
"v_mov_b32 v74, s42\n"
"v_mov_b32 v75, s42\n"
"ds_read_b32 v90, v14 offset:304\n"
"s_mov_b64 exec, 0xffff\n"
"ds_read_b64 v[46:47], v24 offset:4000\n"
"ds_read_b64 v[50:51], v24 offset:4040\n"
"s_mov_b64 exec, -1\n"
"ds_read_b64 v[44:45], v25 offset:3968\n"
"ds_read_b64 v[56:57], v25 offset:80\n"
"ds_read_b64 v[58:59], v24 offset:112\n"
"ds_read_b64 v[64:65], v25 offset:4048\n"
"ds_read_b64 v[66:67], v24 offset:4080\n"
"ds_read_b64 v[48:49], v25 offset:4008\n"
"ds_read_b64 v[60:61], v25 offset:120\n"
"ds_read_b64 v[62:63], v24 offset:152\n"
"ds_read_b64 v[68:69], v25 offset:4088\n"
"ds_read_b64 v[70:71], v24 offset:4120\n"
"s_mov_b32 s17, 0x100000\n"
"s_waitcnt lgkmcnt(12)\n"
"v_cmp_eq_u32 vcc, 0, v90\n"
"s_cbranch_vccz .Lok_189\n"
".Ltp_188:\n"
"ds_read_b32 v90, v14 offset:304\n"
"s_mov_b64 exec, 0xffff\n"
"ds_read_b64 v[46:47], v24 offset:4000\n"
"ds_read_b64 v[50:51], v24 offset:4040\n"
"s_mov_b64 exec, -1\n"
"ds_read_b64 v[44:45], v25 offset:3968\n"
"ds_read_b64 v[56:57], v25 offset:80\n"
"ds_read_b64 v[58:59], v24 offset:112\n"
"ds_read_b64 v[64:65], v25 offset:4048\n"
"ds_read_b64 v[66:67], v24 offset:4080\n"
"ds_read_b64 v[48:49], v25 offset:4008\n"
"ds_read_b64 v[60:61], v25 offset:120\n"
"ds_read_b64 v[62:63], v24 offset:152\n"
"ds_read_b64 v[68:69], v25 offset:4088\n"
"ds_read_b64 v[70:71], v24 offset:4120\n"
"s_sub_u32 s17, s17, 1\n"
"s_cbranch_scc1 .Lok_189\n"
"s_waitcnt lgkmcnt(12)\n"
"v_cmp_eq_u32 vcc, 0, v90\n"
"s_cbranch_vccnz .Ltp_188\n"
"s_waitcnt lgkmcnt(0)\n"
".Lok_189:\n"
"ds_read_b64_tr_b16 v[28:29], v26 offset:0\n"
"ds_read_b64_tr_b16 v[30:31], v26 offset:3968\n"
"ds_read_b64_tr_b16 v[32:33], v26 offset:40\n"
"v_mov_b32 v52, 0x0c0c0c0c\n"
"v_mov_b32 v53, 0xc0c0100\n"
"v_mov_b32 v54, 0x1000c0c\n"
"v_cndmask_b32 v53, v53, v54, s[34:35]\n"
"v_cndmask_b32 v80, v53, v52, s[36:37]\n"
"v_cndmask_b32 v82, v52, v53, s[36:37]\n"
"v_mov_b32 v53, 0xc0c0302\n"
"v_mov_b32 v54, 0x3020c0c\n"
"v_cndmask_b32 v53, v53, v54, s[34:35]\n"
"v_cndmask_b32 v81, v53, v52, s[38:39]\n"
"v_cndmask_b32 v83, v52, v53, s[38:39]\n"
"s_waitcnt lgkmcnt(10)\n"
"v_mfma_f32_16x16x32_bf16 v[56:59], v[56:59], v[44:47], v[72:75]\n"
"s_waitcnt lgkmcnt(8)\n"
"v_mfma_f32_16x16x32_bf16 v[64:67], v[64:67], v[44:47], 0\n"
"ds_read_b64_tr_b16 v[34:35], v26 offset:4008\n"
"ds_read_b64_tr_b16 v[36:37], v26 offset:32\n"
"ds_read_b64_tr_b16 v[38:39], v26 offset:4000\n"
"s_waitcnt lgkmcnt(8)\n"
"v_mfma_f32_16x16x32_bf16 v[60:63], v[60:63], v[48:51], v[72:75]\n"
"s_waitcnt lgkmcnt(6)\n"
"v_mfma_f32_16x16x32_bf16 v[68:71], v[68:71], v[48:51], 0\n"
"ds_read_b64_tr_b16 v[40:41], v26 offset:72\n"
"ds_read_b64_tr_b16 v[42:43], v26 offset:4040\n"
"v_mov_b32 v84, 0x3f803f80\n"
"v_mov_b32 v85, 0x3f803f80\n"
"v_mov_b32 v86, 0x3f803f80\n"
"v_mov_b32 v87, 0x3f803f80\n"
"v_cndmask_b32 v76, v56, v57, s[34:35]\n"
"v_cndmask_b32 v72, v64, v65, s[34:35]\n"
"v_cndmask_b32 v76, v76, v72, s[36:37]\n"
"v_cndmask_b32 v77, v58, v59, s[34:35]\n"
"v_cndmask_b32 v72, v66, v67, s[34:35]\n"
"v_cndmask_b32 v77, v77, v72, s[38:39]\n"
"v_max_f32 v52, v76, v77\n"
"v_max_f32 v54, v76, v77\n"
"v_cndmask_b32 v78, v60, v61, s[34:35]\n"
"v_cndmask_b32 v72, v68, v69, s[34:35]\n"
"v_cndmask_b32 v78, v78, v72, s[36:37]\n"
"v_cndmask_b32 v79, v62, v63, s[34:35]\n"
"v_cndmask_b32 v72, v70, v71, s[34:35]\n"
"v_cndmask_b32 v79, v79, v72, s[38:39]\n"
"v_max_f32 v53, v78, v79\n"
"v_max_f32 v55, v78, v79\n"
"v_permlane16_swap_b32 v52, v54\n"
"s_nop 0\n"
"v_permlane16_swap_b32 v53, v55\n"
"v_max_f32 v72, v52, v54\n"
"v_max_f32 v52, v52, v54\n"
"v_max_f32 v73, v53, v55\n"
"v_max_f32 v53, v53, v55\n"
"v_permlane32_swap_b32 v52, v72\n"
"s_nop 0\n"
"v_permlane32_swap_b32 v53, v73\n"
"v_max_f32 v52, v52, v72\n"
"v_max_f32 v53, v53, v73\n"
"v_mul_f32 v52, 0xbea52b5b, v52\n"
"v_mul_f32 v53, 0xbea52b5b, v53\n"
"v_fmamk_f32 v76, v76, 0x3ea52b5b, v52\n"
"v_fmamk_f32 v77, v77, 0x3ea52b5b, v52\n"
"v_fmamk_f32 v78, v78, 0x3ea52b5b, v53\n"
"v_fmamk_f32 v79, v79, 0x3ea52b5b, v53\n"
"v_exp_f32 v76, v76\n"
"v_exp_f32 v77, v77\n"
"v_exp_f32 v78, v78\n"
"v_exp_f32 v79, v79\n"
"v_cvt_pk_bf16_f32 v52, v76, v77\n"
"v_cvt_pk_bf16_f32 v53, v78, v79\n"
"v_perm_b32 v44, v52, v52, v80\n"
"v_perm_b32 v45, v52, v52, v81\n"
"v_perm_b32 v46, v52, v52, v82\n"
"v_perm_b32 v47, v52, v52, v83\n"
"v_perm_b32 v48, v53, v53, v80\n"
"v_perm_b32 v49, v53, v53, v81\n"
"v_perm_b32 v50, v53, v53, v82\n"
"v_perm_b32 v51, v53, v53, v83\n"
"v_mfma_f32_16x16x32_bf16 v[56:59], v[84:87], v[44:47], 0\n"
"s_nop 0\n"
"v_mfma_f32_16x16x32_bf16 v[60:63], v[84:87], v[48:51], 0\n"
"s_waitcnt lgkmcnt(6)\n"
"v_mfma_f32_16x16x32_bf16 v[28:31], v[28:31], v[44:47], 0\n"
"s_waitcnt lgkmcnt(4)\n"
"v_mfma_f32_16x16x32_bf16 v[32:35], v[32:35], v[48:51], 0\n"
"s_waitcnt lgkmcnt(2)\n"
"v_mfma_f32_16x16x32_bf16 v[36:39], v[36:39], v[44:47], 0\n"
"s_waitcnt lgkmcnt(0)\n"
"v_mfma_f32_16x16x32_bf16 v[40:43], v[40:43], v[48:51], 0\n"
"v_cndmask_b32 v88, 2.0, v1, s[40:41]\n"
"v_rcp_f32 v52, v56\n"
"v_rcp_f32 v53, v60\n"
"ds_read_b32 v90, v14 offset:368\n"
"ds_read_b64 v[56:57], v3 offset:0\n"
"ds_read_b64 v[58:59], v3 offset:256\n"
"s_add_u32 s31, s30, 0xa60000\n"
"v_mul_f32 v28, v52, v28\n"
"v_mul_f32 v29, v52, v29\n"
"v_mul_f32 v30, v52, v30\n"
"v_mul_f32 v31, v52, v31\n"
"v_mul_f32 v32, v53, v32\n"
"v_mul_f32 v33, v53, v33\n"
"ds_read_b64 v[60:61], v3 offset:4096\n"
"ds_read_b64 v[62:63], v3 offset:4352\n"
"v_mul_f32 v34, v53, v34\n"
"v_mul_f32 v35, v53, v35\n"
"v_cvt_pk_bf16_f32 v28, v28, v29\n"
"v_cvt_pk_bf16_f32 v29, v30, v31\n"
"v_cvt_pk_bf16_f32 v30, v32, v33\n"
"v_cvt_pk_bf16_f32 v31, v34, v35\n"
"v_min_u32 v28, -2, v28\n"
"v_min_u32 v29, -2, v29\n"
"ds_read_b64 v[64:65], v3 offset:8192\n"
"ds_read_b64 v[66:67], v3 offset:8448\n"
"v_min_u32 v30, -2, v30\n"
"v_min_u32 v31, -2, v31\n"
"buffer_store_dwordx4 v[28:31], v1, s[4:7], s31 offen offset:0 sc1\n"
"v_mul_f32 v36, v52, v36\n"
"v_mul_f32 v37, v52, v37\n"
"v_mul_f32 v38, v52, v38\n"
"v_mul_f32 v39, v52, v39\n"
"ds_read_b64 v[68:69], v3 offset:12288\n"
"ds_read_b64 v[70:71], v3 offset:12544\n"
"v_mul_f32 v40, v53, v40\n"
"v_mul_f32 v41, v53, v41\n"
"v_mul_f32 v42, v53, v42\n"
"v_mul_f32 v43, v53, v43\n"
"v_cvt_pk_bf16_f32 v36, v36, v37\n"
"v_cvt_pk_bf16_f32 v37, v38, v39\n"
"v_cvt_pk_bf16_f32 v38, v40, v41\n"
"v_cvt_pk_bf16_f32 v39, v42, v43\n"
"v_min_u32 v36, -2, v36\n"
"v_min_u32 v37, -2, v37\n"
"v_min_u32 v38, -2, v38\n"
"v_min_u32 v39, -2, v39\n"
"buffer_store_dwordx4 v[36:39], v88, s[4:7], s31 offen offset:1024 sc1\n"
"ds_write_b32 v13, v13 offset:384\n"
"s_cmp_lg_u32 s54, 0\n"
"s_cbranch_scc1 .Lfx_193\n"
".Lbk_194:\n"
"s_mov_b32 s54, 0\n"
".Lag_190:\n"
"v_mov_b32 v92, 0\n"
"s_waitcnt lgkmcnt(9)\n"
"v_cmp_eq_u32 vcc, 0, v90\n"
"s_cbranch_vccnz .Lsl_228\n"
".Lbk_229:\n"
"v_mov_b32 v80, 0x3f803f80\n"
"v_mov_b32 v81, 0x3f803f80\n"
"v_mov_b32 v82, 0x3f803f80\n"
"v_mov_b32 v83, 0x3f803f80\n"
"s_waitcnt lgkmcnt(5)\n"
"v_mfma_f32_16x16x32_bf16 v[28:31], v[96:99], v[56:59], 0\n"
"ds_read_b64 v[72:73], v3 offset:16384\n"
"v_mfma_f32_16x16x32_bf16 v[32:35], v[100:103], v[56:59], 0\n"
"ds_read_b64 v[74:75], v3 offset:16640\n"
"v_mfma_f32_16x16x32_bf16 v[52:55], v[56:59], v[56:59], 0\n"
"s_add_u32 s24, s84, 0x640000\n"
"s_add_u32 s25, s84, 0x620000\n"
"v_mfma_f32_16x16x32_bf16 v[76:79], v[80:83], v[56:59], 0\n"
"s_mov_b32 s26, s85\n"
"s_mov_b32 s27, -1\n"
"v_mfma_f32_16x16x32_bf16 v[28:31], v[128:131], v[60:63], v[28:31]\n"
"ds_read_b64 v[56:57], v3 offset:1024\n"
"v_mfma_f32_16x16x32_bf16 v[32:35], v[132:135], v[60:63], v[32:35]\n"
"ds_read_b64 v[58:59], v3 offset:1280\n"
"v_mfma_f32_16x16x32_bf16 v[52:55], v[60:63], v[60:63], v[52:55]\n"
"v_mfma_f32_16x16x32_bf16 v[76:79], v[80:83], v[60:63], v[76:79]\n"
"s_waitcnt lgkmcnt(5)\n"
"v_mfma_f32_16x16x32_bf16 v[28:31], v[160:163], v[64:67], v[28:31]\n"
"ds_read_b64 v[60:61], v3 offset:2048\n"
"v_mfma_f32_16x16x32_bf16 v[32:35], v[164:167], v[64:67], v[32:35]\n"
"ds_read_b64 v[62:63], v3 offset:2304\n"
"v_mfma_f32_16x16x32_bf16 v[52:55], v[64:67], v[64:67], v[52:55]\n"
"v_mfma_f32_16x16x32_bf16 v[76:79], v[80:83], v[64:67], v[76:79]\n"
"v_mfma_f32_16x16x32_bf16 v[28:31], v[192:195], v[68:71], v[28:31]\n"
"ds_read_b64 v[64:65], v3 offset:3072\n"
"v_mfma_f32_16x16x32_bf16 v[32:35], v[196:199], v[68:71], v[32:35]\n"
"ds_read_b64 v[66:67], v3 offset:3328\n"
"v_mfma_f32_16x16x32_bf16 v[52:55], v[68:71], v[68:71], v[52:55]\n"
"v_mfma_f32_16x16x32_bf16 v[76:79], v[80:83], v[68:71], v[76:79]\n"
"s_waitcnt lgkmcnt(4)\n"
"v_mfma_f32_16x16x32_bf16 v[28:31], v[224:227], v[72:75], v[28:31]\n"
"ds_read_b64 v[68:69], v3 offset:5120\n"
"v_mfma_f32_16x16x32_bf16 v[32:35], v[228:231], v[72:75], v[32:35]\n"
"ds_read_b64 v[70:71], v3 offset:5376\n"
"v_mfma_f32_16x16x32_bf16 v[52:55], v[72:75], v[72:75], v[52:55]\n"
"v_mfma_f32_16x16x32_bf16 v[76:79], v[80:83], v[72:75], v[76:79]\n"
"v_mfma_f32_16x16x32_bf16 v[28:31], v[104:107], v[56:59], v[28:31]\n"
"ds_read_b64 v[72:73], v3 offset:6144\n"
"v_mfma_f32_16x16x32_bf16 v[32:35], v[108:111], v[56:59], v[32:35]\n"
"ds_read_b64 v[74:75], v3 offset:6400\n"
"v_cndmask_b32 v94, v52, v53, s[34:35]\n"
"s_waitcnt lgkmcnt(4)\n"
"v_mfma_f32_16x16x32_bf16 v[28:31], v[112:115], v[60:63], v[28:31]\n"
"ds_read_b64 v[56:57], v3 offset:7168\n"
"v_cndmask_b32 v54, v54, v55, s[34:35]\n"
"v_cndmask_b32 v94, v94, v54, s[20:21]\n"
"v_mfma_f32_16x16x32_bf16 v[32:35], v[116:119], v[60:63], v[32:35]\n"
"ds_read_b64 v[58:59], v3 offset:7424\n"
"ds_write_b32 v10, v76 offset:4992\n"
"v_mfma_f32_16x16x32_bf16 v[28:31], v[120:123], v[64:67], v[28:31]\n"
"ds_read_b64 v[60:61], v3 offset:9216\n"
"ds_write_b32 v9, v94 offset:3712\n"
"ds_read_b128 v[36:39], v11 offset:3712\n"
"ds_read_b128 v[40:43], v11 offset:4992\n"
"v_mfma_f32_16x16x32_bf16 v[32:35], v[124:127], v[64:67], v[32:35]\n"
"ds_read_b64 v[62:63], v3 offset:9472\n"
"s_waitcnt lgkmcnt(8)\n"
"v_mfma_f32_16x16x32_bf16 v[28:31], v[136:139], v[68:71], v[28:31]\n"
"ds_read_b64 v[64:65], v3 offset:10240\n"
"v_mfma_f32_16x16x32_bf16 v[32:35], v[140:143], v[68:71], v[32:35]\n"
"ds_read_b64 v[66:67], v3 offset:10496\n"
"v_mfma_f32_16x16x32_bf16 v[28:31], v[144:147], v[72:75], v[28:31]\n"
"ds_read_b64 v[68:69], v3 offset:11264\n"
"v_mfma_f32_16x16x32_bf16 v[32:35], v[148:151], v[72:75], v[32:35]\n"
"ds_read_b64 v[70:71], v3 offset:11520\n"
"s_waitcnt lgkmcnt(4)\n"
"v_mfma_f32_16x16x32_bf16 v[28:31], v[152:155], v[56:59], v[28:31]\n"
"ds_read_b64 v[72:73], v3 offset:13312\n"
"v_mfma_f32_16x16x32_bf16 v[32:35], v[156:159], v[56:59], v[32:35]\n"
"ds_read_b64 v[74:75], v3 offset:13568\n"
"v_add_f32 v36, v36, v37\n"
"v_add_f32 v38, v38, v39\n"
"v_add_f32 v40, v40, v41\n"
"v_mfma_f32_16x16x32_bf16 v[28:31], v[168:171], v[60:63], v[28:31]\n"
"ds_read_b64 v[56:57], v3 offset:14336\n"
"v_add_f32 v42, v42, v43\n"
"v_add_f32 v40, v40, v42\n"
"v_add_f32 v36, v36, v38\n"
"v_mfma_f32_16x16x32_bf16 v[32:35], v[172:175], v[60:63], v[32:35]\n"
"ds_read_b64 v[58:59], v3 offset:14592\n"
"v_mul_f32 v89, 0x3acccccd, v40\n"
"v_fma_f32 v36, -v40, v89, v36\n"
"s_waitcnt lgkmcnt(4)\n"
"v_mfma_f32_16x16x32_bf16 v[28:31], v[176:179], v[64:67], v[28:31]\n"
"ds_read_b64 v[60:61], v3 offset:15360\n"
"v_fmamk_f32 v36, v36, 0x3acccccd, v27\n"
"v_cmp_u_f32 vcc, v36, v36\n"
"v_mfma_f32_16x16x32_bf16 v[32:35], v[180:183], v[64:67], v[32:35]\n"
"ds_read_b64 v[62:63], v3 offset:15616\n"
"ds_read_b32 v90, v14 offset:320\n"
"v_max_f32 v36, v36, v27\n"
"v_mfma_f32_16x16x32_bf16 v[28:31], v[184:187], v[68:71], v[28:31]\n"
"ds_read_b64 v[64:65], v4 offset:0\n"
"v_rsq_f32 v88, v36\n"
"v_mfma_f32_16x16x32_bf16 v[32:35], v[188:191], v[68:71], v[32:35]\n"
"ds_read_b64 v[66:67], v4 offset:256\n"
"v_mul_f32 v89, -v89, v88\n"
"s_waitcnt lgkmcnt(5)\n"
"v_mfma_f32_16x16x32_bf16 v[28:31], v[200:203], v[72:75], v[28:31]\n"
"ds_read_b64 v[68:69], v5 offset:0\n"
"s_cbranch_vccnz .Lsl_209\n"
".Lbk_210:\n"
"v_mfma_f32_16x16x32_bf16 v[32:35], v[204:207], v[72:75], v[32:35]\n"
"ds_read_b64 v[70:71], v5 offset:256\n"
"v_mfma_f32_16x16x32_bf16 v[28:31], v[208:211], v[56:59], v[28:31]\n"
"ds_read_b64 v[72:73], v6 offset:0\n"
"s_waitcnt lgkmcnt(5)\n"
"v_cmp_eq_u32 vcc, 0, v90\n"
"v_mfma_f32_16x16x32_bf16 v[32:35], v[212:215], v[56:59], v[32:35]\n"
"ds_read_b64 v[74:75], v6 offset:256\n"
"s_waitcnt lgkmcnt(4)\n"
"v_mfma_f32_16x16x32_bf16 v[28:31], v[216:219], v[60:63], v[28:31]\n"
"s_cbranch_vccnz .Lsl_197\n"
".Lbk_198:\n"
"ds_read_b64 v[56:57], v3 offset:40960\n"
"v_mfma_f32_16x16x32_bf16 v[32:35], v[220:223], v[60:63], v[32:35]\n"
"ds_read_b64 v[58:59], v3 offset:41216\n"
"v_mfma_f32_16x16x32_bf16 v[28:31], v[232:235], v[64:67], v[28:31]\n"
"ds_read_b64 v[60:61], v3 offset:41984\n"
"v_mfma_f32_16x16x32_bf16 v[32:35], v[236:239], v[64:67], v[32:35]\n"
"ds_read_b64 v[62:63], v3 offset:42240\n"
"s_waitcnt lgkmcnt(4)\n"
"v_mfma_f32_16x16x32_bf16 v[28:31], v[240:243], v[68:71], v[28:31]\n"
"ds_read_b64 v[64:65], v3 offset:43008\n"
"v_mfma_f32_16x16x32_bf16 v[32:35], v[244:247], v[68:71], v[32:35]\n"
"ds_read_b64 v[66:67], v3 offset:43264\n"
"v_mfma_f32_16x16x32_bf16 v[28:31], v[248:251], v[72:75], v[28:31]\n"
"ds_read_b64 v[68:69], v3 offset:44032\n"
"v_mfma_f32_16x16x32_bf16 v[32:35], v[252:255], v[72:75], v[32:35]\n"
"ds_read_b64 v[70:71], v3 offset:44288\n"
"s_waitcnt lgkmcnt(4)\n"
"v_mfma_f32_16x16x32_bf16 v[36:39], v[96:99], v[56:59], 0\n"
"ds_read_b64 v[72:73], v3 offset:45056\n"
"v_mfma_f32_16x16x32_bf16 v[40:43], v[100:103], v[56:59], 0\n"
"ds_read_b64 v[74:75], v3 offset:45312\n"
"v_mfma_f32_16x16x32_bf16 v[52:55], v[56:59], v[56:59], 0\n"
"v_mul_f32 v32, v88, v32\n"
"v_mul_f32 v33, v88, v33\n"
"v_mul_f32 v34, v88, v34\n"
"v_mfma_f32_16x16x32_bf16 v[36:39], v[104:107], v[60:63], v[36:39]\n"
"ds_read_b64 v[56:57], v3 offset:46080\n"
"v_mul_f32 v35, v88, v35\n"
"v_fmac_f32 v32, v89, v20\n"
"v_mfma_f32_16x16x32_bf16 v[40:43], v[108:111], v[60:63], v[40:43]\n"
"ds_read_b64 v[58:59], v3 offset:46336\n"
"v_fmac_f32 v33, v89, v21\n"
"s_waitcnt lgkmcnt(4)\n"
"v_mfma_f32_16x16x32_bf16 v[36:39], v[112:115], v[64:67], v[36:39]\n"
"ds_read_b64 v[60:61], v3 offset:47104\n"
"v_fmac_f32 v34, v89, v22\n"
"v_fmac_f32 v35, v89, v23\n"
"v_mfma_f32_16x16x32_bf16 v[40:43], v[116:119], v[64:67], v[40:43]\n"
"ds_read_b64 v[62:63], v3 offset:47360\n"
"v_cvt_pk_bf16_f32 v32, v32, v33\n"
"v_cvt_pk_bf16_f32 v34, v34, v35\n"
"v_mfma_f32_16x16x32_bf16 v[36:39], v[120:123], v[68:71], v[36:39]\n"
"ds_read_b64 v[64:65], v3 offset:48128\n"
"v_mov_b32 v33, s16\n"
"v_mov_b32 v35, s16\n"
"v_mfma_f32_16x16x32_bf16 v[40:43], v[124:127], v[68:71], v[40:43]\n"
"ds_read_b64 v[66:67], v3 offset:48384\n"
"buffer_store_dwordx4 v[32:35], v1, s[4:7], s24 offen offset:1024 sc1\n"
"s_waitcnt lgkmcnt(4)\n"
"v_mfma_f32_16x16x32_bf16 v[36:39], v[128:131], v[72:75], v[36:39]\n"
"ds_read_b64 v[68:69], v3 offset:49152\n"
"v_mul_f32 v28, v88, v28\n"
"v_mfma_f32_16x16x32_bf16 v[40:43], v[132:135], v[72:75], v[40:43]\n"
"ds_read_b64 v[70:71], v3 offset:49408\n"
"v_mul_f32 v29, v88, v29\n"
"v_mfma_f32_16x16x32_bf16 v[52:55], v[72:75], v[72:75], v[52:55]\n"
"v_mul_f32 v30, v88, v30\n"
"v_mul_f32 v31, v88, v31\n"
"v_mfma_f32_16x16x32_bf16 v[36:39], v[136:139], v[56:59], v[36:39]\n"
"ds_read_b64 v[72:73], v3 offset:50176\n"
"v_fmac_f32 v28, v89, v16\n"
"v_mfma_f32_16x16x32_bf16 v[40:43], v[140:143], v[56:59], v[40:43]\n"
"ds_read_b64 v[74:75], v3 offset:50432\n"
"s_waitcnt lgkmcnt(4)\n"
"v_mfma_f32_16x16x32_bf16 v[36:39], v[144:147], v[60:63], v[36:39]\n"
"ds_read_b64 v[56:57], v3 offset:51200\n"
"v_fmac_f32 v29, v89, v17\n"
"v_mfma_f32_16x16x32_bf16 v[40:43], v[148:151], v[60:63], v[40:43]\n"
"ds_read_b64 v[58:59], v3 offset:51456\n"
"v_fmac_f32 v30, v89, v18\n"
"v_mfma_f32_16x16x32_bf16 v[36:39], v[152:155], v[64:67], v[36:39]\n"
"ds_read_b64 v[60:61], v3 offset:52224\n"
"v_fmac_f32 v31, v89, v19\n"
"v_mfma_f32_16x16x32_bf16 v[40:43], v[156:159], v[64:67], v[40:43]\n"
"ds_read_b64 v[62:63], v3 offset:52480\n"
"s_waitcnt lgkmcnt(4)\n"
"v_mfma_f32_16x16x32_bf16 v[36:39], v[160:163], v[68:71], v[36:39]\n"
"ds_read_b64 v[64:65], v3 offset:53248\n"
"v_cvt_pk_bf16_f32 v28, v28, v29\n"
"v_mfma_f32_16x16x32_bf16 v[40:43], v[164:167], v[68:71], v[40:43]\n"
"ds_read_b64 v[66:67], v3 offset:53504\n"
"v_cvt_pk_bf16_f32 v30, v30, v31\n"
"v_mfma_f32_16x16x32_bf16 v[52:55], v[68:71], v[68:71], v[52:55]\n"
"v_mov_b32 v29, s16\n"
"v_mov_b32 v31, s16\n"
"v_mfma_f32_16x16x32_bf16 v[36:39], v[168:171], v[72:75], v[36:39]\n"
"ds_read_b64 v[68:69], v3 offset:54272\n"
"buffer_store_dwordx4 v[28:31], v93, s[4:7], s24 offen offset:0 sc1\n"
"v_mfma_f32_16x16x32_bf16 v[40:43], v[172:175], v[72:75], v[40:43]\n"
"ds_read_b64 v[70:71], v3 offset:54528\n"
"s_waitcnt lgkmcnt(4)\n"
"v_mfma_f32_16x16x32_bf16 v[36:39], v[176:179], v[56:59], v[36:39]\n"
"ds_read_b64 v[72:73], v3 offset:55296\n"
"v_mfma_f32_16x16x32_bf16 v[40:43], v[180:183], v[56:59], v[40:43]\n"
"ds_read_b64 v[74:75], v3 offset:55552\n"
"v_mfma_f32_16x16x32_bf16 v[36:39], v[184:187], v[60:63], v[36:39]\n"
"ds_read_b64 v[56:57], v3 offset:56320\n"
"v_mfma_f32_16x16x32_bf16 v[40:43], v[188:191], v[60:63], v[40:43]\n"
"ds_read_b64 v[58:59], v3 offset:56576\n"
"s_waitcnt lgkmcnt(4)\n"
"v_mfma_f32_16x16x32_bf16 v[36:39], v[192:195], v[64:67], v[36:39]\n"
"ds_read_b64 v[60:61], v3 offset:57344\n"
"v_mfma_f32_16x16x32_bf16 v[40:43], v[196:199], v[64:67], v[40:43]\n"
"ds_read_b64 v[62:63], v3 offset:57600\n"
"v_mfma_f32_16x16x32_bf16 v[52:55], v[64:67], v[64:67], v[52:55]\n"
"ds_read_b32 v90, v14 offset:416\n"
"v_mfma_f32_16x16x32_bf16 v[36:39], v[200:203], v[68:71], v[36:39]\n"
"ds_read_b64 v[64:65], v4 offset:40960\n"
"v_mfma_f32_16x16x32_bf16 v[40:43], v[204:207], v[68:71], v[40:43]\n"
"ds_read_b64 v[66:67], v4 offset:41216\n"
"s_waitcnt lgkmcnt(5)\n"
"v_mfma_f32_16x16x32_bf16 v[36:39], v[208:211], v[72:75], v[36:39]\n"
"ds_read_b64 v[68:69], v5 offset:40960\n"
"v_mfma_f32_16x16x32_bf16 v[40:43], v[212:215], v[72:75], v[40:43]\n"
"ds_read_b64 v[70:71], v5 offset:41216\n"
"v_mfma_f32_16x16x32_bf16 v[36:39], v[216:219], v[56:59], v[36:39]\n"
"ds_read_b64 v[72:73], v6 offset:40960\n"
"s_waitcnt lgkmcnt(5)\n"
"v_cmp_eq_u32 vcc, 0, v90\n"
"v_mfma_f32_16x16x32_bf16 v[40:43], v[220:223], v[56:59], v[40:43]\n"
"ds_read_b64 v[74:75], v6 offset:41216\n"
"s_waitcnt lgkmcnt(4)\n"
"v_mfma_f32_16x16x32_bf16 v[36:39], v[224:227], v[60:63], v[36:39]\n"
"s_cbranch_vccnz .Lsl_201\n"
".Lbk_202:\n"
"v_add_u32 v3, 0x5000, v3\n"
"ds_read_b64 v[56:57], v3 offset:40960\n"
"v_mfma_f32_16x16x32_bf16 v[40:43], v[228:231], v[60:63], v[40:43]\n"
"ds_read_b64 v[58:59], v3 offset:41216\n"
"v_mov_b32 v29, v30\n"
"v_mfma_f32_16x16x32_bf16 v[52:55], v[60:63], v[60:63], v[52:55]\n"
"v_mov_b32 v33, v34\n"
"v_mfma_f32_16x16x32_bf16 v[36:39], v[232:235], v[64:67], v[36:39]\n"
"ds_read_b64 v[60:61], v3 offset:41984\n"
"v_mfma_f32_16x16x32_bf16 v[40:43], v[236:239], v[64:67], v[40:43]\n"
"ds_read_b64 v[62:63], v3 offset:42240\n"
"s_waitcnt lgkmcnt(4)\n"
"v_mfma_f32_16x16x32_bf16 v[36:39], v[240:243], v[68:71], v[36:39]\n"
"ds_read_b64 v[64:65], v3 offset:43008\n"
"v_cndmask_b32 v94, v52, v53, s[34:35]\n"
"v_mfma_f32_16x16x32_bf16 v[40:43], v[244:247], v[68:71], v[40:43]\n"
"ds_read_b64 v[66:67], v3 offset:43264\n"
"v_cndmask_b32 v54, v54, v55, s[34:35]\n"
"v_mfma_f32_16x16x32_bf16 v[36:39], v[248:251], v[72:75], v[36:39]\n"
"ds_read_b64 v[68:69], v3 offset:44032\n"
"v_cndmask_b32 v94, v94, v54, s[20:21]\n"
"v_mfma_f32_16x16x32_bf16 v[40:43], v[252:255], v[72:75], v[40:43]\n"
"ds_read_b64 v[70:71], v3 offset:44288\n"
"s_waitcnt lgkmcnt(4)\n"
"v_mfma_f32_16x16x32_bf16 v[44:47], v[96:99], v[56:59], 0\n"
"ds_read_b64 v[72:73], v3 offset:45056\n"
"v_mfma_f32_16x16x32_bf16 v[48:51], v[100:103], v[56:59], 0\n"
"ds_read_b64 v[74:75], v3 offset:45312\n"
"v_cndmask_b32 v94, v36, v94, s[22:23]\n"
"v_mfma_f32_16x16x32_bf16 v[52:55], v[56:59], v[56:59], 0\n"
"ds_write_b32 v9, v94 offset:4032\n"
"v_mfma_f32_16x16x32_bf16 v[44:47], v[104:107], v[60:63], v[44:47]\n"
"ds_read_b64 v[56:57], v3 offset:46080\n"
"v_mfma_f32_16x16x32_bf16 v[48:51], v[108:111], v[60:63], v[48:51]\n"
"ds_read_b64 v[58:59], v3 offset:46336\n"
"s_waitcnt lgkmcnt(5)\n"
"v_mfma_f32_16x16x32_bf16 v[44:47], v[112:115], v[64:67], v[44:47]\n"
"ds_read_b64 v[60:61], v3 offset:47104\n"
"v_mfma_f32_16x16x32_bf16 v[48:51], v[116:119], v[64:67], v[48:51]\n"
"ds_read_b64 v[62:63], v3 offset:47360\n"
"v_mfma_f32_16x16x32_bf16 v[44:47], v[120:123], v[68:71], v[44:47]\n"
"ds_read_b64 v[64:65], v3 offset:48128\n"
"v_mfma_f32_16x16x32_bf16 v[48:51], v[124:127], v[68:71], v[48:51]\n"
"ds_read_b64 v[66:67], v3 offset:48384\n"
"s_waitcnt lgkmcnt(4)\n"
"v_mfma_f32_16x16x32_bf16 v[44:47], v[128:131], v[72:75], v[44:47]\n"
"ds_read_b64 v[68:69], v3 offset:49152\n"
"v_mfma_f32_16x16x32_bf16 v[48:51], v[132:135], v[72:75], v[48:51]\n"
"ds_read_b64 v[70:71], v3 offset:49408\n"
"v_mfma_f32_16x16x32_bf16 v[52:55], v[72:75], v[72:75], v[52:55]\n"
"v_mfma_f32_16x16x32_bf16 v[44:47], v[136:139], v[56:59], v[44:47]\n"
"ds_read_b64 v[72:73], v3 offset:50176\n"
"v_mfma_f32_16x16x32_bf16 v[48:51], v[140:143], v[56:59], v[48:51]\n"
"ds_read_b64 v[74:75], v3 offset:50432\n"
"s_waitcnt lgkmcnt(4)\n"
"v_mfma_f32_16x16x32_bf16 v[44:47], v[144:147], v[60:63], v[44:47]\n"
"ds_read_b64 v[56:57], v3 offset:51200\n"
"ds_read_b128 v[84:87], v11 offset:4032\n"
"ds_read_b32 v91, v12 offset:4032\n"
"v_mfma_f32_16x16x32_bf16 v[48:51], v[148:151], v[60:63], v[48:51]\n"
"ds_read_b64 v[58:59], v3 offset:51456\n"
"v_mfma_f32_16x16x32_bf16 v[44:47], v[152:155], v[64:67], v[44:47]\n"
"ds_read_b64 v[60:61], v3 offset:52224\n"
"v_mfma_f32_16x16x32_bf16 v[48:51], v[156:159], v[64:67], v[48:51]\n"
"ds_read_b64 v[62:63], v3 offset:52480\n"
"s_waitcnt lgkmcnt(6)\n"
"v_mfma_f32_16x16x32_bf16 v[44:47], v[160:163], v[68:71], v[44:47]\n"
"ds_read_b64 v[64:65], v3 offset:53248\n"
"v_mfma_f32_16x16x32_bf16 v[48:51], v[164:167], v[68:71], v[48:51]\n"
"ds_read_b64 v[66:67], v3 offset:53504\n"
"v_mfma_f32_16x16x32_bf16 v[52:55], v[68:71], v[68:71], v[52:55]\n"
"v_mfma_f32_16x16x32_bf16 v[44:47], v[168:171], v[72:75], v[44:47]\n"
"ds_read_b64 v[68:69], v3 offset:54272\n"
"s_waitcnt lgkmcnt(7)\n"
"v_add_f32 v84, v84, v85\n"
"v_add_f32 v86, v86, v87\n"
"v_mfma_f32_16x16x32_bf16 v[48:51], v[172:175], v[72:75], v[48:51]\n"
"ds_read_b64 v[70:71], v3 offset:54528\n"
"v_add_f32 v84, v84, v86\n"
"s_waitcnt lgkmcnt(4)\n"
"v_mfma_f32_16x16x32_bf16 v[44:47], v[176:179], v[56:59], v[44:47]\n"
"ds_read_b64 v[72:73], v3 offset:55296\n"
"v_mul_f32 v89, 0x3acccccd, v91\n"
"v_fma_f32 v84, -v91, v89, v84\n"
"v_mfma_f32_16x16x32_bf16 v[48:51], v[180:183], v[56:59], v[48:51]\n"
"ds_read_b64 v[74:75], v3 offset:55552\n"
"v_fmamk_f32 v84, v84, 0x3acccccd, v27\n"
"v_cmp_u_f32 vcc, v84, v84\n"
"v_mfma_f32_16x16x32_bf16 v[44:47], v[184:187], v[60:63], v[44:47]\n"
"ds_read_b64 v[56:57], v3 offset:56320\n"
"v_max_f32 v84, v84, v27\n"
"v_mfma_f32_16x16x32_bf16 v[48:51], v[188:191], v[60:63], v[48:51]\n"
"ds_read_b64 v[58:59], v3 offset:56576\n"
"s_waitcnt lgkmcnt(4)\n"
"v_mfma_f32_16x16x32_bf16 v[44:47], v[192:195], v[64:67], v[44:47]\n"
"ds_read_b64 v[60:61], v3 offset:57344\n"
"v_rsq_f32 v88, v84\n"
"v_mfma_f32_16x16x32_bf16 v[48:51], v[196:199], v[64:67], v[48:51]\n"
"ds_read_b64 v[62:63], v3 offset:57600\n"
"v_mul_f32 v89, -v89, v88\n"
"v_mfma_f32_16x16x32_bf16 v[52:55], v[64:67], v[64:67], v[52:55]\n"
"ds_read_b32 v90, v14 offset:432\n"
"s_cbranch_vccnz .Lsl_213\n"
".Lbk_214:\n"
"v_mfma_f32_16x16x32_bf16 v[44:47], v[200:203], v[68:71], v[44:47]\n"
"ds_read_b64 v[64:65], v4 offset:61440\n"
"ds_write_b64 v7, v[28:29] offset:36352\n"
"v_mfma_f32_16x16x32_bf16 v[48:51], v[204:207], v[68:71], v[48:51]\n"
"ds_read_b64 v[66:67], v4 offset:61696\n"
"v_mul_f32 v36, v88, v36\n"
"s_waitcnt lgkmcnt(6)\n"
"v_mfma_f32_16x16x32_bf16 v[44:47], v[208:211], v[72:75], v[44:47]\n"
"ds_read_b64 v[68:69], v5 offset:61440\n"
"v_mul_f32 v37, v88, v37\n"
"v_mfma_f32_16x16x32_bf16 v[48:51], v[212:215], v[72:75], v[48:51]\n"
"ds_read_b64 v[70:71], v5 offset:61696\n"
"v_mul_f32 v38, v88, v38\n"
"v_mfma_f32_16x16x32_bf16 v[44:47], v[216:219], v[56:59], v[44:47]\n"
"ds_read_b64 v[72:73], v6 offset:61440\n"
"s_waitcnt lgkmcnt(6)\n"
"v_cmp_eq_u32 vcc, 0, v90\n"
"v_mfma_f32_16x16x32_bf16 v[48:51], v[220:223], v[56:59], v[48:51]\n"
"ds_read_b64 v[74:75], v6 offset:61696\n"
"s_waitcnt lgkmcnt(4)\n"
"v_mfma_f32_16x16x32_bf16 v[44:47], v[224:227], v[60:63], v[44:47]\n"
"s_cbranch_vccnz .Lsl_205\n"
".Lbk_206:\n"
"ds_read_b64 v[56:57], v3 offset:0\n"
"v_mfma_f32_16x16x32_bf16 v[48:51], v[228:231], v[60:63], v[48:51]\n"
"ds_read_b64 v[58:59], v3 offset:256\n"
"v_mul_f32 v39, v88, v39\n"
"v_mfma_f32_16x16x32_bf16 v[52:55], v[60:63], v[60:63], v[52:55]\n"
"ds_write_b64 v8, v[32:33] offset:36352\n"
"v_mfma_f32_16x16x32_bf16 v[44:47], v[232:235], v[64:67], v[44:47]\n"
"ds_read_b64 v[60:61], v3 offset:4096\n"
"v_fmac_f32 v36, v89, v16\n"
"v_mul_f32 v40, v88, v40\n"
"v_mfma_f32_16x16x32_bf16 v[48:51], v[236:239], v[64:67], v[48:51]\n"
"ds_read_b64 v[62:63], v3 offset:4352\n"
"v_mul_f32 v41, v88, v41\n"
"s_waitcnt lgkmcnt(5)\n"
"v_mfma_f32_16x16x32_bf16 v[44:47], v[240:243], v[68:71], v[44:47]\n"
"ds_read_b64 v[64:65], v3 offset:8192\n"
"v_cndmask_b32 v94, v52, v53, s[34:35]\n"
"v_mul_f32 v42, v88, v42\n"
"v_mfma_f32_16x16x32_bf16 v[48:51], v[244:247], v[68:71], v[48:51]\n"
"ds_read_b64 v[66:67], v3 offset:8448\n"
"v_cndmask_b32 v54, v54, v55, s[34:35]\n"
"v_mfma_f32_16x16x32_bf16 v[44:47], v[248:251], v[72:75], v[44:47]\n"
"ds_read_b64 v[68:69], v3 offset:12288\n"
"v_cndmask_b32 v94, v94, v54, s[20:21]\n"
"v_mfma_f32_16x16x32_bf16 v[48:51], v[252:255], v[72:75], v[48:51]\n"
"ds_read_b64 v[70:71], v3 offset:12544\n"
"s_waitcnt lgkmcnt(4)\n"
"v_mfma_f32_16x16x32_bf16 v[28:31], v[96:99], v[56:59], 0\n"
"ds_read_b64 v[72:73], v3 offset:16384\n"
"v_fmac_f32 v37, v89, v17\n"
"v_mfma_f32_16x16x32_bf16 v[32:35], v[100:103], v[56:59], 0\n"
"ds_read_b64 v[74:75], v3 offset:16640\n"
"v_cndmask_b32 v94, v44, v94, s[22:23]\n"
"v_mfma_f32_16x16x32_bf16 v[52:55], v[56:59], v[56:59], 0\n"
"ds_write_b32 v9, v94 offset:4352\n"
"v_mfma_f32_16x16x32_bf16 v[76:79], v[80:83], v[56:59], 0\n"
"v_fmac_f32 v38, v89, v18\n"
"v_fmac_f32 v39, v89, v19\n"
"v_mfma_f32_16x16x32_bf16 v[28:31], v[128:131], v[60:63], v[28:31]\n"
"ds_read_b64 v[56:57], v3 offset:1024\n"
"v_cvt_pk_bf16_f32 v36, v36, v37\n"
"v_mfma_f32_16x16x32_bf16 v[32:35], v[132:135], v[60:63], v[32:35]\n"
"ds_read_b64 v[58:59], v3 offset:1280\n"
"v_cvt_pk_bf16_f32 v37, v38, v39\n"
"v_mfma_f32_16x16x32_bf16 v[52:55], v[60:63], v[60:63], v[52:55]\n"
"ds_write_b64 v7, v[36:37] offset:24448\n"
"v_mfma_f32_16x16x32_bf16 v[76:79], v[80:83], v[60:63], v[76:79]\n"
"v_mul_f32 v43, v88, v43\n"
"s_waitcnt lgkmcnt(6)\n"
"v_mfma_f32_16x16x32_bf16 v[28:31], v[160:163], v[64:67], v[28:31]\n"
"ds_read_b64 v[60:61], v3 offset:2048\n"
"v_fmac_f32 v40, v89, v20\n"
"v_mfma_f32_16x16x32_bf16 v[32:35], v[164:167], v[64:67], v[32:35]\n"
"ds_read_b64 v[62:63], v3 offset:2304\n"
"v_fmac_f32 v41, v89, v21\n"
"v_mfma_f32_16x16x32_bf16 v[52:55], v[64:67], v[64:67], v[52:55]\n"
"ds_read_b128 v[84:87], v11 offset:4352\n"
"ds_read_b32 v91, v12 offset:4352\n"
"v_mfma_f32_16x16x32_bf16 v[76:79], v[80:83], v[64:67], v[76:79]\n"
"v_fmac_f32 v42, v89, v22\n"
"v_fmac_f32 v43, v89, v23\n"
"v_mfma_f32_16x16x32_bf16 v[28:31], v[192:195], v[68:71], v[28:31]\n"
"ds_read_b64 v[64:65], v3 offset:3072\n"
"v_cvt_pk_bf16_f32 v40, v40, v41\n"
"v_mfma_f32_16x16x32_bf16 v[32:35], v[196:199], v[68:71], v[32:35]\n"
"ds_read_b64 v[66:67], v3 offset:3328\n"
"v_cvt_pk_bf16_f32 v41, v42, v43\n"
"v_mfma_f32_16x16x32_bf16 v[52:55], v[68:71], v[68:71], v[52:55]\n"
"ds_write_b64 v8, v[40:41] offset:24448\n"
"v_mfma_f32_16x16x32_bf16 v[76:79], v[80:83], v[68:71], v[76:79]\n"
"s_waitcnt lgkmcnt(8)\n"
"v_mfma_f32_16x16x32_bf16 v[28:31], v[224:227], v[72:75], v[28:31]\n"
"ds_read_b64 v[68:69], v3 offset:5120\n"
"v_mfma_f32_16x16x32_bf16 v[32:35], v[228:231], v[72:75], v[32:35]\n"
"ds_read_b64 v[70:71], v3 offset:5376\n"
"s_waitcnt lgkmcnt(6)\n"
"v_add_f32 v84, v84, v85\n"
"v_add_f32 v86, v86, v87\n"
"v_mfma_f32_16x16x32_bf16 v[52:55], v[72:75], v[72:75], v[52:55]\n"
"v_add_f32 v84, v84, v86\n"
"s_waitcnt lgkmcnt(5)\n"
"v_mul_f32 v89, 0x3acccccd, v91\n"
"v_fma_f32 v84, -v91, v89, v84\n"
"v_mfma_f32_16x16x32_bf16 v[76:79], v[80:83], v[72:75], v[76:79]\n"
"v_fmamk_f32 v84, v84, 0x3acccccd, v27\n"
"v_cmp_u_f32 vcc, v84, v84\n"
"v_max_f32 v84, v84, v27\n"
"v_mfma_f32_16x16x32_bf16 v[28:31], v[104:107], v[56:59], v[28:31]\n"
"ds_read_b64 v[72:73], v3 offset:6144\n"
"v_rsq_f32 v88, v84\n"
"v_mfma_f32_16x16x32_bf16 v[32:35], v[108:111], v[56:59], v[32:35]\n"
"ds_read_b64 v[74:75], v3 offset:6400\n"
"v_cndmask_b32 v94, v52, v53, s[34:35]\n"
"v_cndmask_b32 v54, v54, v55, s[34:35]\n"
"s_waitcnt lgkmcnt(5)\n"
"v_mfma_f32_16x16x32_bf16 v[28:31], v[112:115], v[60:63], v[28:31]\n"
"ds_read_b64 v[56:57], v3 offset:7168\n"
"v_mul_f32 v89, -v89, v88\n"
"v_cndmask_b32 v94, v94, v54, s[20:21]\n"
"ds_write_b32 v10, v76 offset:5312\n"
"v_mfma_f32_16x16x32_bf16 v[32:35], v[116:119], v[60:63], v[32:35]\n"
"ds_read_b64 v[58:59], v3 offset:7424\n"
"s_cbranch_vccnz .Lsl_217\n"
".Lbk_218:\n"
"ds_write_b32 v9, v94 offset:4672\n"
"v_mfma_f32_16x16x32_bf16 v[28:31], v[120:123], v[64:67], v[28:31]\n"
"ds_read_b64 v[60:61], v3 offset:9216\n"
"v_mul_f32 v44, v88, v44\n"
"v_mul_f32 v45, v88, v45\n"
"v_mfma_f32_16x16x32_bf16 v[32:35], v[124:127], v[64:67], v[32:35]\n"
"ds_read_b64 v[62:63], v3 offset:9472\n"
"v_mul_f32 v46, v88, v46\n"
"s_waitcnt lgkmcnt(6)\n"
"v_mfma_f32_16x16x32_bf16 v[28:31], v[136:139], v[68:71], v[28:31]\n"
"ds_read_b64 v[64:65], v3 offset:10240\n"
"buffer_load_dwordx4 v[40:43], v1, s[4:7], s25 offen offset:1024 sc1\n"
"v_mul_f32 v47, v88, v47\n"
"v_mfma_f32_16x16x32_bf16 v[32:35], v[140:143], v[68:71], v[32:35]\n"
"ds_read_b64 v[66:67], v3 offset:10496\n"
"buffer_load_dwordx4 v[36:39], v93, s[4:7], s25 offen offset:0 sc1\n"
"v_fmac_f32 v44, v89, v16\n"
"v_mfma_f32_16x16x32_bf16 v[28:31], v[144:147], v[72:75], v[28:31]\n"
"ds_read_b64 v[68:69], v3 offset:11264\n"
"v_fmac_f32 v45, v89, v17\n"
"v_fmac_f32 v46, v89, v18\n"
"v_mfma_f32_16x16x32_bf16 v[32:35], v[148:151], v[72:75], v[32:35]\n"
"ds_read_b64 v[70:71], v3 offset:11520\n"
"v_fmac_f32 v47, v89, v19\n"
"s_waitcnt lgkmcnt(4)\n"
"v_mfma_f32_16x16x32_bf16 v[28:31], v[152:155], v[56:59], v[28:31]\n"
"ds_read_b64 v[72:73], v3 offset:13312\n"
"v_cvt_pk_bf16_f32 v44, v44, v45\n"
"v_cvt_pk_bf16_f32 v45, v46, v47\n"
"v_mfma_f32_16x16x32_bf16 v[32:35], v[156:159], v[56:59], v[32:35]\n"
"ds_read_b64 v[74:75], v3 offset:13568\n"
"ds_write_b64 v7, v[44:45] offset:28416\n"
"v_mfma_f32_16x16x32_bf16 v[28:31], v[168:171], v[60:63], v[28:31]\n"
"ds_read_b64 v[56:57], v3 offset:14336\n"
"ds_read_b128 v[84:87], v11 offset:4672\n"
"ds_read_b128 v[76:79], v11 offset:5312\n"
"v_mfma_f32_16x16x32_bf16 v[32:35], v[172:175], v[60:63], v[32:35]\n"
"ds_read_b64 v[58:59], v3 offset:14592\n"
"v_mul_f32 v48, v88, v48\n"
"s_waitcnt lgkmcnt(7)\n"
"v_mfma_f32_16x16x32_bf16 v[28:31], v[176:179], v[64:67], v[28:31]\n"
"ds_read_b64 v[60:61], v3 offset:15360\n"
"v_mul_f32 v49, v88, v49\n"
"v_mul_f32 v50, v88, v50\n"
"v_mfma_f32_16x16x32_bf16 v[32:35], v[180:183], v[64:67], v[32:35]\n"
"ds_read_b64 v[62:63], v3 offset:15616\n"
"v_mul_f32 v51, v88, v51\n"
"v_fmac_f32 v48, v89, v20\n"
"v_mfma_f32_16x16x32_bf16 v[28:31], v[184:187], v[68:71], v[28:31]\n"
"v_add_u32 v3, 0xffffb000, v3\n"
"ds_read_b64 v[64:65], v4 offset:20480\n"
"v_fmac_f32 v49, v89, v21\n"
"v_mfma_f32_16x16x32_bf16 v[32:35], v[188:191], v[68:71], v[32:35]\n"
"ds_read_b64 v[66:67], v4 offset:20736\n"
"v_fmac_f32 v50, v89, v22\n"
"s_waitcnt lgkmcnt(4)\n"
"v_mfma_f32_16x16x32_bf16 v[28:31], v[200:203], v[72:75], v[28:31]\n"
"ds_read_b64 v[68:69], v5 offset:20480\n"
"v_fmac_f32 v51, v89, v23\n"
"v_cvt_pk_bf16_f32 v48, v48, v49\n"
"v_mfma_f32_16x16x32_bf16 v[32:35], v[204:207], v[72:75], v[32:35]\n"
"ds_read_b64 v[70:71], v5 offset:20736\n"
"v_add_f32 v84, v84, v85\n"
"v_cvt_pk_bf16_f32 v49, v50, v51\n"
"v_mfma_f32_16x16x32_bf16 v[28:31], v[208:211], v[56:59], v[28:31]\n"
"ds_read_b64 v[72:73], v6 offset:20480\n"
"v_add_f32 v86, v86, v87\n"
"ds_write_b64 v8, v[48:49] offset:28416\n"
"v_mfma_f32_16x16x32_bf16 v[32:35], v[212:215], v[56:59], v[32:35]\n"
"ds_read_b64 v[74:75], v6 offset:20736\n"
"s_waitcnt lgkmcnt(5)\n"
"v_mfma_f32_16x16x32_bf16 v[28:31], v[216:219], v[60:63], v[28:31]\n"
"v_add_f32 v76, v76, v77\n"
"v_add_f32 v78, v78, v79\n"
"v_mfma_f32_16x16x32_bf16 v[32:35], v[220:223], v[60:63], v[32:35]\n"
"v_add_f32 v76, v76, v78\n"
"v_add_f32 v84, v84, v86\n"
"v_mfma_f32_16x16x32_bf16 v[28:31], v[232:235], v[64:67], v[28:31]\n"
"v_mul_f32 v53, 0x3acccccd, v76\n"
"v_fma_f32 v84, -v76, v53, v84\n"
"v_mfma_f32_16x16x32_bf16 v[32:35], v[236:239], v[64:67], v[32:35]\n"
"v_fmamk_f32 v84, v84, 0x3acccccd, v27\n"
"s_waitcnt lgkmcnt(0)\n"
"v_mfma_f32_16x16x32_bf16 v[28:31], v[240:243], v[68:71], v[28:31]\n"
"v_cmp_u_f32 vcc, v84, v84\n"
"v_max_f32 v84, v84, v27\n"
"v_mfma_f32_16x16x32_bf16 v[32:35], v[244:247], v[68:71], v[32:35]\n"
"v_rsq_f32 v52, v84\n"
"v_mfma_f32_16x16x32_bf16 v[28:31], v[248:251], v[72:75], v[28:31]\n"
"v_mul_f32 v53, -v53, v52\n"
"v_mfma_f32_16x16x32_bf16 v[32:35], v[252:255], v[72:75], v[32:35]\n"
"s_cbranch_vccnz .Lsl_221\n"
".Lbk_222:\n"
"v_cmp_u_f32 vcc, v92, v92\n"
"s_nop 2\n"
"v_mul_f32 v28, v52, v28\n"
"v_mul_f32 v29, v52, v29\n"
"v_mul_f32 v30, v52, v30\n"
"v_mul_f32 v31, v52, v31\n"
"v_fmac_f32 v28, v53, v16\n"
"v_fmac_f32 v29, v53, v17\n"
"v_fmac_f32 v30, v53, v18\n"
"v_fmac_f32 v31, v53, v19\n"
"v_cvt_pk_bf16_f32 v28, v28, v29\n"
"v_cvt_pk_bf16_f32 v29, v30, v31\n"
"ds_write_b64 v7, v[28:29] offset:32384\n"
"s_cbranch_vccnz .Lrd_192\n"
".Lgo_191:\n"
"v_mul_f32 v32, v52, v32\n"
"v_mul_f32 v33, v52, v33\n"
"v_mul_f32 v34, v52, v34\n"
"v_mul_f32 v35, v52, v35\n"
"v_fmac_f32 v32, v53, v20\n"
"v_fmac_f32 v33, v53, v21\n"
"s_waitcnt vmcnt(0)\n"
"v_bitop3_b32 v84, v37, v39, s16 bitop3:0x7e\n"
"v_bitop3_b32 v85, v41, v43, s16 bitop3:0x7e\n"
"v_and_b32 v84, s26, v84\n"
"v_and_or_b32 v84, v85, s27, v84\n"
"v_cmp_ne_u32 vcc, 0, v84\n"
"v_fmac_f32 v34, v53, v22\n"
"v_fmac_f32 v35, v53, v23\n"
"v_cvt_pk_bf16_f32 v32, v32, v33\n"
"v_cvt_pk_bf16_f32 v33, v34, v35\n"
"ds_write_b64 v8, v[32:33] offset:32384\n"
"s_cbranch_vccnz .Lsl_225\n"
".Lbk_226:\n"
"v_mov_b32 v37, v38\n"
"v_mov_b32 v41, v42\n"
"ds_write_b64 v8, v[40:41] offset:20480\n"
"ds_write_b64 v7, v[36:37] offset:20480\n"
"ds_write_b32 v13, v13 offset:448\n"
"v_mov_b32 v46, 0\n"
"v_mov_b32 v47, 0\n"
"v_mov_b32 v50, 0\n"
"v_mov_b32 v51, 0\n"
"ds_read_b32 v90, v14 offset:448\n"
"s_mov_b64 exec, 0xffff\n"
"ds_read_b64 v[46:47], v24 offset:24480\n"
"ds_read_b64 v[50:51], v24 offset:24520\n"
"s_mov_b64 exec, -1\n"
"ds_read_b64 v[44:45], v25 offset:24448\n"
"ds_read_b64 v[56:57], v25 offset:20560\n"
"ds_read_b64 v[58:59], v24 offset:20592\n"
"ds_read_b64 v[64:65], v25 offset:24528\n"
"ds_read_b64 v[66:67], v24 offset:24560\n"
"ds_read_b64 v[48:49], v25 offset:24488\n"
"ds_read_b64 v[60:61], v25 offset:20600\n"
"ds_read_b64 v[62:63], v24 offset:20632\n"
"ds_read_b64 v[68:69], v25 offset:24568\n"
"ds_read_b64 v[70:71], v24 offset:24600\n"
"s_mov_b32 s17, 0x100000\n"
"s_waitcnt lgkmcnt(12)\n"
"v_cmp_eq_u32 vcc, 0, v90\n"
"s_cbranch_vccz .Lok_266\n"
".Ltp_265:\n"
"ds_read_b32 v90, v14 offset:448\n"
"s_mov_b64 exec, 0xffff\n"
"ds_read_b64 v[46:47], v24 offset:24480\n"
"ds_read_b64 v[50:51], v24 offset:24520\n"
"s_mov_b64 exec, -1\n"
"ds_read_b64 v[44:45], v25 offset:24448\n"
"ds_read_b64 v[56:57], v25 offset:20560\n"
"ds_read_b64 v[58:59], v24 offset:20592\n"
"ds_read_b64 v[64:65], v25 offset:24528\n"
"ds_read_b64 v[66:67], v24 offset:24560\n"
"ds_read_b64 v[48:49], v25 offset:24488\n"
"ds_read_b64 v[60:61], v25 offset:20600\n"
"ds_read_b64 v[62:63], v24 offset:20632\n"
"ds_read_b64 v[68:69], v25 offset:24568\n"
"ds_read_b64 v[70:71], v24 offset:24600\n"
"s_sub_u32 s17, s17, 1\n"
"s_cbranch_scc1 .Lok_266\n"
"s_waitcnt lgkmcnt(12)\n"
"v_cmp_eq_u32 vcc, 0, v90\n"
"s_cbranch_vccnz .Ltp_265\n"
"s_waitcnt lgkmcnt(0)\n"
".Lok_266:\n"
"ds_read_b64_tr_b16 v[28:29], v26 offset:20480\n"
"ds_read_b64_tr_b16 v[30:31], v26 offset:24448\n"
"ds_read_b64_tr_b16 v[32:33], v26 offset:20520\n"
"v_mov_b32 v52, 0x0c0c0c0c\n"
"v_mov_b32 v53, 0xc0c0100\n"
"v_mov_b32 v54, 0x1000c0c\n"
"v_cndmask_b32 v53, v53, v54, s[34:35]\n"
"v_cndmask_b32 v80, v53, v52, s[36:37]\n"
"v_cndmask_b32 v82, v52, v53, s[36:37]\n"
"v_mov_b32 v53, 0xc0c0302\n"
"v_mov_b32 v54, 0x3020c0c\n"
"v_cndmask_b32 v53, v53, v54, s[34:35]\n"
"v_cndmask_b32 v81, v53, v52, s[38:39]\n"
"v_cndmask_b32 v83, v52, v53, s[38:39]\n"
"s_waitcnt lgkmcnt(10)\n"
"v_mfma_f32_16x16x32_bf16 v[56:59], v[56:59], v[44:47], 0\n"
"s_waitcnt lgkmcnt(8)\n"
"v_mfma_f32_16x16x32_bf16 v[64:67], v[64:67], v[44:47], 0\n"
"ds_read_b64_tr_b16 v[34:35], v26 offset:24488\n"
"ds_read_b64_tr_b16 v[36:37], v26 offset:20512\n"
"ds_read_b64_tr_b16 v[38:39], v26 offset:24480\n"
"s_waitcnt lgkmcnt(8)\n"
"v_mfma_f32_16x16x32_bf16 v[60:63], v[60:63], v[48:51], 0\n"
"s_waitcnt lgkmcnt(6)\n"
"v_mfma_f32_16x16x32_bf16 v[68:71], v[68:71], v[48:51], 0\n"
"ds_read_b64_tr_b16 v[40:41], v26 offset:20552\n"
"ds_read_b64_tr_b16 v[42:43], v26 offset:24520\n"
"v_mov_b32 v84, 0x3f803f80\n"
"v_mov_b32 v85, 0x3f803f80\n"
"v_mov_b32 v86, 0x3f803f80\n"
"v_mov_b32 v87, 0x3f803f80\n"
"v_cndmask_b32 v76, v56, v57, s[34:35]\n"
"v_cndmask_b32 v72, v64, v65, s[34:35]\n"
"v_cndmask_b32 v76, v76, v72, s[36:37]\n"
"v_cndmask_b32 v77, v58, v59, s[34:35]\n"
"v_cndmask_b32 v72, v66, v67, s[34:35]\n"
"v_cndmask_b32 v77, v77, v72, s[38:39]\n"
"v_max_f32 v52, v76, v77\n"
"v_max_f32 v54, v76, v77\n"
"v_cndmask_b32 v78, v60, v61, s[34:35]\n"
"v_cndmask_b32 v72, v68, v69, s[34:35]\n"
"v_cndmask_b32 v78, v78, v72, s[36:37]\n"
"v_cndmask_b32 v79, v62, v63, s[34:35]\n"
"v_cndmask_b32 v72, v70, v71, s[34:35]\n"
"v_cndmask_b32 v79, v79, v72, s[38:39]\n"
"v_max_f32 v53, v78, v79\n"
"v_max_f32 v55, v78, v79\n"
"v_permlane16_swap_b32 v52, v54\n"
"s_nop 0\n"
"v_permlane16_swap_b32 v53, v55\n"
"v_max_f32 v72, v52, v54\n"
"v_max_f32 v52, v52, v54\n"
"v_max_f32 v73, v53, v55\n"
"v_max_f32 v53, v53, v55\n"
"v_permlane32_swap_b32 v52, v72\n"
"s_nop 0\n"
"v_permlane32_swap_b32 v53, v73\n"
"v_max_f32 v52, v52, v72\n"
"v_max_f32 v53, v53, v73\n"
"v_mul_f32 v52, 0xbea52b5b, v52\n"
"v_mul_f32 v53, 0xbea52b5b, v53\n"
"v_fmamk_f32 v76, v76, 0x3ea52b5b, v52\n"
"v_fmamk_f32 v77, v77, 0x3ea52b5b, v52\n"
"v_fmamk_f32 v78, v78, 0x3ea52b5b, v53\n"
"v_fmamk_f32 v79, v79, 0x3ea52b5b, v53\n"
"v_exp_f32 v76, v76\n"
"v_exp_f32 v77, v77\n"
"v_exp_f32 v78, v78\n"
"v_exp_f32 v79, v79\n"
"v_cvt_pk_bf16_f32 v52, v76, v77\n"
"v_cvt_pk_bf16_f32 v53, v78, v79\n"
"v_perm_b32 v44, v52, v52, v80\n"
"v_perm_b32 v45, v52, v52, v81\n"
"v_perm_b32 v46, v52, v52, v82\n"
"v_perm_b32 v47, v52, v52, v83\n"
"v_perm_b32 v48, v53, v53, v80\n"
"v_perm_b32 v49, v53, v53, v81\n"
"v_perm_b32 v50, v53, v53, v82\n"
"v_perm_b32 v51, v53, v53, v83\n"
"v_mfma_f32_16x16x32_bf16 v[56:59], v[84:87], v[44:47], 0\n"
"s_nop 0\n"
"v_mfma_f32_16x16x32_bf16 v[60:63], v[84:87], v[48:51], 0\n"
"s_waitcnt lgkmcnt(6)\n"
"v_mfma_f32_16x16x32_bf16 v[28:31], v[28:31], v[44:47], 0\n"
"s_waitcnt lgkmcnt(4)\n"
"v_mfma_f32_16x16x32_bf16 v[32:35], v[32:35], v[48:51], 0\n"
"s_waitcnt lgkmcnt(2)\n"
"v_mfma_f32_16x16x32_bf16 v[36:39], v[36:39], v[44:47], 0\n"
"s_waitcnt lgkmcnt(0)\n"
"v_mfma_f32_16x16x32_bf16 v[40:43], v[40:43], v[48:51], 0\n"
"v_cndmask_b32 v88, 2.0, v1, s[40:41]\n"
"v_rcp_f32 v52, v56\n"
"v_rcp_f32 v53, v60\n"
"s_add_u32 s31, s30, 0xba0000\n"
"v_mul_f32 v28, v52, v28\n"
"v_mul_f32 v29, v52, v29\n"
"v_mul_f32 v30, v52, v30\n"
"v_mul_f32 v31, v52, v31\n"
"v_mul_f32 v32, v53, v32\n"
"v_mul_f32 v33, v53, v33\n"
"v_mul_f32 v34, v53, v34\n"
"v_mul_f32 v35, v53, v35\n"
"v_cvt_pk_bf16_f32 v28, v28, v29\n"
"v_cvt_pk_bf16_f32 v29, v30, v31\n"
"v_cvt_pk_bf16_f32 v30, v32, v33\n"
"v_cvt_pk_bf16_f32 v31, v34, v35\n"
"v_min_u32 v28, -2, v28\n"
"v_min_u32 v29, -2, v29\n"
"v_min_u32 v30, -2, v30\n"
"v_min_u32 v31, -2, v31\n"
"buffer_store_dwordx4 v[28:31], v1, s[4:7], s31 offen offset:0 sc1\n"
"v_mul_f32 v36, v52, v36\n"
"v_mul_f32 v37, v52, v37\n"
"v_mul_f32 v38, v52, v38\n"
"v_mul_f32 v39, v52, v39\n"
"v_mul_f32 v40, v53, v40\n"
"v_mul_f32 v41, v53, v41\n"
"v_mul_f32 v42, v53, v42\n"
"v_mul_f32 v43, v53, v43\n"
"v_cvt_pk_bf16_f32 v36, v36, v37\n"
"v_cvt_pk_bf16_f32 v37, v38, v39\n"
"v_cvt_pk_bf16_f32 v38, v40, v41\n"
"v_cvt_pk_bf16_f32 v39, v42, v43\n"
"v_min_u32 v36, -2, v36\n"
"v_min_u32 v37, -2, v37\n"
"v_min_u32 v38, -2, v38\n"
"v_min_u32 v39, -2, v39\n"
"buffer_store_dwordx4 v[36:39], v88, s[4:7], s31 offen offset:1024 sc1\n"
"ds_write_b32 v13, v13 offset:496\n"
"s_nop 2\n"
"s_cmp_lt_i32 s45, 0\n"
"s_cbranch_scc1 .Lnc_267\n"
"s_setprio 3\n"
"s_lshl_b32 s79, s3, 10\n"
"v_lshl_add_u32 v34, v0, 4, s79\n"
"v_lshrrev_b32 v59, 7, v34\n"
"v_and_b32 v58, 0x70, v34\n"
"s_mov_b32 s79, 0x5000\n"
"v_mad_i32_i24 v33, v59, s79, v58\n"
"s_mov_b32 s17, 0x100000\n"
".Ltp_268:\n"
"s_sleep 1\n"
"ds_read_b32 v42, v14 offset:448\n"
"s_sub_u32 s17, s17, 1\n"
"s_cbranch_scc1 .Lou_269\n"
"s_waitcnt lgkmcnt(0)\n"
"v_cmp_eq_u32 vcc, 0, v42\n"
"s_cbranch_vccnz .Ltp_268\n"
".Lou_269:\n"
"s_waitcnt lgkmcnt(0)\n"
"s_mov_b32 s17, 0x100000\n"
".Ltp_270:\n"
"s_sleep 1\n"
"ds_read_b32 v42, v14 offset:512\n"
"s_sub_u32 s17, s17, 1\n"
"s_cbranch_scc1 .Lou_271\n"
"s_waitcnt lgkmcnt(0)\n"
"v_cmp_eq_u32 vcc, 0, v42\n"
"s_cbranch_vccnz .Ltp_270\n"
".Lou_271:\n"
"s_waitcnt lgkmcnt(0)\n"
"s_lshl_b32 s79, s45, 8\n"
"s_add_u32 s46, s79, 0x1128000\n"
"s_add_u32 s47, s46, 0x140000\n"
"s_and_b32 s79, s2, 7\n"
"s_lshl_b32 s79, s79, 14\n"
"s_add_u32 s48, s79, 0x1668000\n"
"s_add_u32 s49, s48, 0x1000\n"
"v_and_b32 v94, 0xffffff80, v33\n"
"v_add_u32 v94, v94, v33\n"
"buffer_load_dwordx4 v[78:81], v94, s[4:7], s46 offen offset:0 sc1\n"
"buffer_load_dwordx4 v[82:85], v94, s[4:7], s46 offen offset:128 sc1\n"
"buffer_load_dwordx4 v[86:89], v94, s[4:7], s47 offen offset:0 sc1\n"
"buffer_load_dwordx4 v[90:93], v94, s[4:7], s47 offen offset:128 sc1\n"
"v_lshrrev_b32 v44, 4, v0\n"
"v_mov_b32 v59, 0xffff0000\n"
"v_mov_b32 v58, 0xffff\n"
"s_mov_b32 vcc_lo, 0xaaaaaaaa\n"
"s_mov_b32 vcc_hi, 0xaaaaaaaa\n"
"v_cndmask_b32 v45, v58, v59, vcc\n"
"v_and_b32 v59, 15, v0\n"
"v_lshlrev_b32 v46, 5, v59\n"
"v_and_b32 v59, 15, v0\n"
"v_lshrrev_b32 v47, 2, v59\n"
"s_lshl_b32 s79, s3, 10\n"
"v_lshl_add_u32 v48, v0, 4, s79\n"
"v_lshrrev_b32 v49, 2, v48\n"
"v_lshrrev_b32 v50, 6, v48\n"
"v_lshlrev_b32 v59, 3, v44\n"
"v_and_b32 v58, 15, v0\n"
"v_sub_u32 v59, v59, v58\n"
"v_add_u32 v51, 0x3ef, v59\n"
"v_and_b32 v59, 24, v50\n"
"v_xor_b32 v36, v49, v59\n"
"s_lshl_b32 s79, s3, 8\n"
"v_and_b32 v59, 15, v0\n"
"v_xor_b32 v59, s3, v59\n"
"v_and_b32 v59, 1, v59\n"
"v_lshrrev_b32 v58, 1, v44\n"
"v_xor_b32 v58, v47, v58\n"
"v_and_b32 v58, 1, v58\n"
"v_lshl_add_u32 v59, v59, 1, v58\n"
"v_and_b32 v58, 15, v0\n"
"v_and_b32 v58, 2, v58\n"
"v_and_b32 v57, 15, v0\n"
"v_lshrrev_b32 v57, 3, v57\n"
"v_lshlrev_b32 v57, 7, v57\n"
"v_lshl_add_u32 v57, v47, 11, v57\n"
"v_lshl_add_u32 v57, v44, 6, v57\n"
"v_lshl_add_u32 v58, v58, 4, v57\n"
"v_lshl_add_u32 v59, v59, 3, v58\n"
"v_add_u32 v31, s79, v59\n"
"s_mul_i32 s79, s3, 0x2100\n"
"s_add_u32 s79, s79, 0x13840\n"
"v_lshrrev_b32 v59, 2, v51\n"
"v_and_b32 v58, 3, v51\n"
"v_mul_i32_i24 v58, 0x840, v58\n"
"v_lshl_add_u32 v59, v59, 3, v58\n"
"v_add_u32 v28, s79, v59\n"
"s_mul_i32 s79, s3, 0x1200\n"
"s_add_u32 s79, s79, 0x1c420\n"
"v_lshl_add_u32 v59, v44, 4, v46\n"
"v_add_u32 v29, s79, v59\n"
"s_lshl_b32 s79, s3, 11\n"
"s_lshr_b32 s78, s3, 1\n"
"s_lshl_b32 s78, s78, 7\n"
"s_add_u32 s79, s79, s78\n"
"s_and_b32 s78, s3, 1\n"
"v_xor_b32 v59, v44, v47\n"
"v_xor_b32 v59, s78, v59\n"
"v_and_b32 v59, 3, v59\n"
"v_lshl_add_u32 v59, v59, 3, v46\n"
"v_add_u32 v30, s79, v59\n"
"v_xor_b32 v32, 8, v31\n"
"v_add_u32 v35, 0x1c600, v49\n"
"v_xor_b32 v37, 8, v36\n"
"s_mov_b32 vcc_lo, 0x300003\n"
"s_mov_b32 vcc_hi, 0x30000300\n"
"v_cndmask_b32 v38, 0, v45, vcc\n"
"s_mov_b32 vcc_lo, 0xc0000c\n"
"s_mov_b32 vcc_hi, 0xc0000c00\n"
"v_cndmask_b32 v39, 0, v45, vcc\n"
"s_lshl_b32 s79, s3, 2\n"
"s_add_u32 s79, s79, 0x26800\n"
"v_mov_b32 v40, s79\n"
"v_and_b32 v59, 48, v48\n"
"v_mul_i32_i24 v59, 0x50, v59\n"
"s_mov_b32 s79, 0x2800\n"
"v_mad_i32_i24 v41, v50, s79, v59\n"
"ds_read_b32 v40, v40\n"
"s_waitcnt lgkmcnt(0)\n"
"v_sub_u32 v44, 0x7f000000, v40\n"
"v_lshrrev_b32 v45, 16, v44\n"
"v_or_b32 v44, v44, v45\n"
"v_and_b32 v38, v44, v38\n"
"v_and_b32 v39, v44, v39\n"
"ds_read_b64 v[96:97], v28 offset:1984\n"
"ds_read_b64 v[98:99], v28 offset:1992\n"
"ds_read_b64 v[100:101], v28 offset:1920\n"
"ds_read_b64 v[102:103], v28 offset:1928\n"
"ds_read_b64 v[104:105], v28 offset:1856\n"
"ds_read_b64 v[106:107], v28 offset:1864\n"
"ds_read_b64 v[108:109], v28 offset:1792\n"
"ds_read_b64 v[110:111], v28 offset:1800\n"
"ds_read_b64 v[112:113], v28 offset:1728\n"
"ds_read_b64 v[114:115], v28 offset:1736\n"
"ds_read_b64 v[116:117], v28 offset:1664\n"
"ds_read_b64 v[118:119], v28 offset:1672\n"
"ds_read_b64 v[120:121], v28 offset:1600\n"
"ds_read_b64 v[122:123], v28 offset:1608\n"
"ds_read_b64 v[124:125], v28 offset:1536\n"
"ds_read_b64 v[126:127], v28 offset:1544\n"
"ds_read_b64 v[128:129], v28 offset:1472\n"
"ds_read_b64 v[130:131], v28 offset:1480\n"
"ds_read_b64 v[132:133], v28 offset:1408\n"
"ds_read_b64 v[134:135], v28 offset:1416\n"
"ds_read_b64 v[136:137], v28 offset:1344\n"
"ds_read_b64 v[138:139], v28 offset:1352\n"
"ds_read_b64 v[140:141], v28 offset:1280\n"
"ds_read_b64 v[142:143], v28 offset:1288\n"
"ds_read_b64 v[144:145], v28 offset:1216\n"
"ds_read_b64 v[146:147], v28 offset:1224\n"
"ds_read_b64 v[148:149], v28 offset:1152\n"
"ds_read_b64 v[150:151], v28 offset:1160\n"
"ds_read_b64 v[152:153], v28 offset:1088\n"
"ds_read_b64 v[154:155], v28 offset:1096\n"
"ds_read_b64 v[156:157], v28 offset:1024\n"
"ds_read_b64 v[158:159], v28 offset:1032\n"
"ds_read_b64 v[160:161], v28 offset:960\n"
"ds_read_b64 v[162:163], v28 offset:968\n"
"ds_read_b64 v[164:165], v28 offset:896\n"
"ds_read_b64 v[166:167], v28 offset:904\n"
"ds_read_b64 v[168:169], v28 offset:832\n"
"ds_read_b64 v[170:171], v28 offset:840\n"
"ds_read_b64 v[172:173], v28 offset:768\n"
"ds_read_b64 v[174:175], v28 offset:776\n"
"ds_read_b64 v[176:177], v28 offset:704\n"
"ds_read_b64 v[178:179], v28 offset:712\n"
"ds_read_b64 v[180:181], v28 offset:640\n"
"ds_read_b64 v[182:183], v28 offset:648\n"
"ds_read_b64 v[184:185], v28 offset:576\n"
"ds_read_b64 v[186:187], v28 offset:584\n"
"ds_read_b64 v[188:189], v28 offset:512\n"
"ds_read_b64 v[190:191], v28 offset:520\n"
"ds_read_b64 v[192:193], v28 offset:448\n"
"ds_read_b64 v[194:195], v28 offset:456\n"
"ds_read_b64 v[196:197], v28 offset:384\n"
"ds_read_b64 v[198:199], v28 offset:392\n"
"ds_read_b64 v[200:201], v28 offset:320\n"
"ds_read_b64 v[202:203], v28 offset:328\n"
"ds_read_b64 v[204:205], v28 offset:256\n"
"ds_read_b64 v[206:207], v28 offset:264\n"
"ds_read_b64 v[208:209], v28 offset:192\n"
"ds_read_b64 v[210:211], v28 offset:200\n"
"ds_read_b64 v[212:213], v28 offset:128\n"
"ds_read_b64 v[214:215], v28 offset:136\n"
"ds_read_b64 v[216:217], v28 offset:64\n"
"ds_read_b64 v[218:219], v28 offset:72\n"
"ds_read_b64 v[220:221], v28 offset:0\n"
"ds_read_b64 v[222:223], v28 offset:8\n"
"s_mov_b32 s52, 0x05040100\n"
"s_mov_b32 s53, 0x07060302\n"
"s_mov_b32 s17, 32768\n"
".Ltp_272:\n"
"s_waitcnt vmcnt(0)\n"
"s_sub_u32 s17, s17, 1\n"
"s_cbranch_scc1 .Lou_273\n"
"v_bitop3_b32 v54, v79, v81, s16 bitop3:0x7e\n"
"v_bitop3_b32 v55, v83, v85, s16 bitop3:0x7e\n"
"v_bitop3_b32 v56, v87, v89, s16 bitop3:0x7e\n"
"v_bitop3_b32 v57, v91, v93, s16 bitop3:0x7e\n"
"v_or3_b32 v58, v54, v55, v56\n"
"v_or_b32 v58, v58, v57\n"
"v_cmp_ne_u32 vcc, 0, v58\n"
"s_cbranch_vccz .Lou_273\n"
"s_sleep 2\n"
"v_cmp_ne_u32 vcc, 0, v54\n"
"s_cbranch_vccz .Lsk_274\n"
"s_mov_b64 exec, vcc\n"
"buffer_load_dwordx4 v[78:81], v94, s[4:7], s46 offen offset:0 sc1\n"
"s_mov_b64 exec, -1\n"
".Lsk_274:\n"
"v_cmp_ne_u32 vcc, 0, v55\n"
"s_cbranch_vccz .Lsk_275\n"
"s_mov_b64 exec, vcc\n"
"buffer_load_dwordx4 v[82:85], v94, s[4:7], s46 offen offset:128 sc1\n"
"s_mov_b64 exec, -1\n"
".Lsk_275:\n"
"v_cmp_ne_u32 vcc, 0, v56\n"
"s_cbranch_vccz .Lsk_276\n"
"s_mov_b64 exec, vcc\n"
"buffer_load_dwordx4 v[86:89], v94, s[4:7], s47 offen offset:0 sc1\n"
"s_mov_b64 exec, -1\n"
".Lsk_276:\n"
"v_cmp_ne_u32 vcc, 0, v57\n"
"s_cbranch_vccz .Lsk_277\n"
"s_mov_b64 exec, vcc\n"
"buffer_load_dwordx4 v[90:93], v94, s[4:7], s47 offen offset:128 sc1\n"
"s_mov_b64 exec, -1\n"
".Lsk_277:\n"
"s_branch .Ltp_272\n"
".Lou_273:\n"
"s_waitcnt vmcnt(0)\n"
"buffer_load_dwordx4 v[70:73], v34, s[4:7], s48 offen sc1\n"
"buffer_load_dwordx4 v[74:77], v34, s[4:7], s49 offen sc1\n"
"v_perm_b32 v54, v82, v78, s52\n"
"v_perm_b32 v55, v82, v78, s53\n"
"v_perm_b32 v56, v84, v80, s52\n"
"v_perm_b32 v57, v84, v80, s53\n"
"ds_write_b32 v36, v54 offset:0\n"
"ds_write_b32 v37, v55 offset:2048\n"
"ds_write_b32 v36, v56 offset:4224\n"
"ds_write_b32 v37, v57 offset:6272\n"
"v_perm_b32 v54, v90, v86, s52\n"
"v_perm_b32 v55, v90, v86, s53\n"
"v_perm_b32 v56, v92, v88, s52\n"
"v_perm_b32 v57, v92, v88, s53\n"
"ds_write_b32 v36, v54 offset:1024\n"
"ds_write_b32 v37, v55 offset:3072\n"
"ds_write_b32 v36, v56 offset:5248\n"
"ds_write_b32 v37, v57 offset:7296\n"
"v_lshlrev_b32 v54, 16, v78\n"
"v_lshlrev_b32 v55, 16, v82\n"
"v_and_b32 v56, 0xffff0000, v78\n"
"v_and_b32 v57, 0xffff0000, v82\n"
"v_lshlrev_b32 v58, 16, v80\n"
"v_lshlrev_b32 v59, 16, v84\n"
"v_and_b32 v60, 0xffff0000, v80\n"
"v_and_b32 v61, 0xffff0000, v84\n"
"v_lshlrev_b32 v62, 16, v86\n"
"v_lshlrev_b32 v63, 16, v90\n"
"v_and_b32 v64, 0xffff0000, v86\n"
"v_and_b32 v65, 0xffff0000, v90\n"
"v_lshlrev_b32 v66, 16, v88\n"
"v_lshlrev_b32 v67, 16, v92\n"
"v_and_b32 v68, 0xffff0000, v88\n"
"v_and_b32 v69, 0xffff0000, v92\n"
"s_mov_b32 s17, 32768\n"
".Ltp_278:\n"
"s_waitcnt vmcnt(0)\n"
"s_sub_u32 s17, s17, 1\n"
"s_cbranch_scc1 .Lou_279\n"
"v_max_u32 v78, v70, v71\n"
"v_max3_u32 v78, v72, v73, v78\n"
"v_max3_u32 v78, v74, v75, v78\n"
"v_max3_u32 v78, v76, v77, v78\n"
"v_cmp_eq_u32 vcc, -1, v78\n"
"s_cbranch_vccz .Lou_279\n"
"v_max_u32 v78, v72, v73\n"
"v_max3_u32 v78, v70, v71, v78\n"
"v_cmp_eq_u32 vcc, -1, v78\n"
"s_cbranch_vccz .Lsk_280\n"
"s_mov_b64 exec, vcc\n"
"buffer_load_dwordx4 v[70:73], v34, s[4:7], s48 offen sc1\n"
"s_mov_b64 exec, -1\n"
".Lsk_280:\n"
"v_max_u32 v78, v76, v77\n"
"v_max3_u32 v78, v74, v75, v78\n"
"v_cmp_eq_u32 vcc, -1, v78\n"
"s_cbranch_vccz .Lsk_281\n"
"s_mov_b64 exec, vcc\n"
"buffer_load_dwordx4 v[74:77], v34, s[4:7], s49 offen sc1\n"
"s_mov_b64 exec, -1\n"
".Lsk_281:\n"
"s_branch .Ltp_278\n"
".Lou_279:\n"
"s_waitcnt vmcnt(0)\n"
"v_fma_f32 v54, v54, v71, v70\n"
"v_fma_f32 v55, v55, v73, v72\n"
"v_cvt_pk_f16_f32 v54, v54, v55\n"
"v_fma_f32 v56, v56, v71, v70\n"
"v_fma_f32 v57, v57, v73, v72\n"
"v_cvt_pk_f16_f32 v56, v56, v57\n"
"v_fma_f32 v58, v58, v71, v70\n"
"v_fma_f32 v59, v59, v73, v72\n"
"v_cvt_pk_f16_f32 v58, v58, v59\n"
"v_fma_f32 v60, v60, v71, v70\n"
"v_fma_f32 v61, v61, v73, v72\n"
"v_cvt_pk_f16_f32 v60, v60, v61\n"
"ds_write2st64_b32 v35, v54, v56 offset0:0 offset1:18\n"
"ds_write2st64_b32 v35, v58, v60 offset0:36 offset1:54\n"
"v_fma_f32 v62, v62, v75, v74\n"
"v_fma_f32 v63, v63, v77, v76\n"
"v_cvt_pk_f16_f32 v62, v62, v63\n"
"v_fma_f32 v64, v64, v75, v74\n"
"v_fma_f32 v65, v65, v77, v76\n"
"v_cvt_pk_f16_f32 v64, v64, v65\n"
"v_fma_f32 v66, v66, v75, v74\n"
"v_fma_f32 v67, v67, v77, v76\n"
"v_cvt_pk_f16_f32 v66, v66, v67\n"
"v_fma_f32 v68, v68, v75, v74\n"
"v_fma_f32 v69, v69, v77, v76\n"
"v_cvt_pk_f16_f32 v68, v68, v69\n"
"ds_write2st64_b32 v35, v62, v64 offset0:4 offset1:22\n"
"ds_write2st64_b32 v35, v66, v68 offset0:40 offset1:58\n"
"ds_write_b32 v13, v13 offset:528\n"
"s_mov_b32 s17, 0x100000\n"
".Ltp_282:\n"
"ds_read_b32 v42, v14 offset:528\n"
"s_sub_u32 s17, s17, 1\n"
"s_cbranch_scc1 .Lou_283\n"
"s_waitcnt lgkmcnt(0)\n"
"v_cmp_eq_u32 vcc, 0, v42\n"
"s_cbranch_vccnz .Ltp_282\n"
".Lou_283:\n"
"s_waitcnt lgkmcnt(0)\n"
"s_setprio 0\n"
"s_mov_b32 s50, 0xfffefffe\n"
"s_lshl_b32 s46, s45, 3\n"
"s_add_u32 s47, s46, 0x1400\n"
"s_add_u32 s48, s46, 0xa0000\n"
"s_add_u32 s49, s46, 0xa1400\n"
"ds_read_b64 v[60:61], v30 offset:0\n"
"ds_read_b64 v[64:65], v30 offset:512\n"
"ds_read_b64 v[68:69], v30 offset:1024\n"
"ds_read_b64 v[72:73], v30 offset:1536\n"
"ds_read_b128 v[76:79], v29 offset:448\n"
"s_waitcnt lgkmcnt(1)\n"
"v_mfma_f32_16x16x16_bf16 v[44:47], v[38:39], v[60:61], 0\n"
"ds_read_b128 v[80:83], v29 offset:960\n"
"ds_read_b128 v[84:87], v29 offset:384\n"
"v_mfma_f32_16x16x16_bf16 v[48:51], v[38:39], v[64:65], 0\n"
"ds_read_b128 v[88:91], v29 offset:896\n"
"ds_read_b128 v[92:95], v29 offset:320\n"
"v_mfma_f32_16x16x16_bf16 v[52:55], v[38:39], v[68:69], 0\n"
"ds_read_b128 v[2:5], v29 offset:832\n"
"v_mfma_f32_16x16x16_bf16 v[56:59], v[38:39], v[72:73], 0\n"
"ds_read_b128 v[34:37], v29 offset:256\n"
"s_waitcnt lgkmcnt(4)\n"
"v_mfma_f32_16x16x32_f16 v[44:47], v[96:99], v[76:79], v[44:47]\n"
"ds_read_b128 v[60:63], v29 offset:768\n"
"v_mfma_f32_16x16x32_f16 v[48:51], v[128:131], v[76:79], v[48:51]\n"
"ds_read_b128 v[64:67], v29 offset:192\n"
"v_mfma_f32_16x16x32_f16 v[48:51], v[96:99], v[80:83], v[48:51]\n"
"ds_read_b128 v[68:71], v29 offset:704\n"
"v_mfma_f32_16x16x32_f16 v[44:47], v[100:103], v[84:87], v[44:47]\n"
"ds_read_b128 v[72:75], v29 offset:128\n"
"s_waitcnt lgkmcnt(6)\n"
"v_mfma_f32_16x16x32_f16 v[48:51], v[132:135], v[84:87], v[48:51]\n"
"v_mfma_f32_16x16x32_f16 v[48:51], v[100:103], v[88:91], v[48:51]\n"
"ds_read_b128 v[76:79], v29 offset:640\n"
"v_mfma_f32_16x16x32_f16 v[44:47], v[104:107], v[92:95], v[44:47]\n"
"ds_read_b128 v[80:83], v29 offset:64\n"
"v_mfma_f32_16x16x32_f16 v[48:51], v[136:139], v[92:95], v[48:51]\n"
"s_waitcnt lgkmcnt(5)\n"
"v_mfma_f32_16x16x32_f16 v[48:51], v[104:107], v[2:5], v[48:51]\n"
"ds_read_b128 v[84:87], v29 offset:576\n"
"v_mfma_f32_16x16x32_f16 v[44:47], v[108:111], v[34:37], v[44:47]\n"
"ds_read_b128 v[88:91], v29 offset:0\n"
"v_mfma_f32_16x16x32_f16 v[48:51], v[140:143], v[34:37], v[48:51]\n"
"v_mfma_f32_16x16x32_f16 v[48:51], v[108:111], v[60:63], v[48:51]\n"
"ds_read_b128 v[92:95], v29 offset:512\n"
"s_waitcnt lgkmcnt(5)\n"
"v_mfma_f32_16x16x32_f16 v[44:47], v[112:115], v[64:67], v[44:47]\n"
"ds_read_b128 v[2:5], v29 offset:448\n"
"v_mfma_f32_16x16x32_f16 v[48:51], v[144:147], v[64:67], v[48:51]\n"
"v_mfma_f32_16x16x32_f16 v[48:51], v[112:115], v[68:71], v[48:51]\n"
"ds_read_b128 v[34:37], v29 offset:960\n"
"v_mfma_f32_16x16x32_f16 v[44:47], v[116:119], v[72:75], v[44:47]\n"
"s_waitcnt lgkmcnt(5)\n"
"v_mfma_f32_16x16x32_f16 v[48:51], v[148:151], v[72:75], v[48:51]\n"
"ds_read_b128 v[60:63], v29 offset:1472\n"
"v_mfma_f32_16x16x32_f16 v[48:51], v[116:119], v[76:79], v[48:51]\n"
"v_mfma_f32_16x16x32_f16 v[44:47], v[120:123], v[80:83], v[44:47]\n"
"ds_read_b128 v[64:67], v29 offset:1984\n"
"v_mfma_f32_16x16x32_f16 v[48:51], v[152:155], v[80:83], v[48:51]\n"
"ds_read_b128 v[68:71], v29 offset:384\n"
"s_waitcnt lgkmcnt(5)\n"
"v_mfma_f32_16x16x32_f16 v[48:51], v[120:123], v[84:87], v[48:51]\n"
"v_mfma_f32_16x16x32_f16 v[44:47], v[124:127], v[88:91], v[44:47]\n"
"ds_read_b128 v[72:75], v29 offset:896\n"
"v_mfma_f32_16x16x32_f16 v[48:51], v[156:159], v[88:91], v[48:51]\n"
"v_mfma_f32_16x16x32_f16 v[48:51], v[124:127], v[92:95], v[48:51]\n"
"ds_read_b128 v[76:79], v29 offset:1408\n"
"s_waitcnt lgkmcnt(5)\n"
"v_mfma_f32_16x16x32_f16 v[52:55], v[160:163], v[2:5], v[52:55]\n"
"v_mfma_f32_16x16x32_f16 v[56:59], v[192:195], v[2:5], v[56:59]\n"
"ds_read_b128 v[80:83], v29 offset:1920\n"
"v_mfma_f32_16x16x32_f16 v[52:55], v[128:131], v[34:37], v[52:55]\n"
"ds_read_b128 v[84:87], v29 offset:320\n"
"v_mul_f32 v44, v40, v44\n"
"v_mul_f32 v45, v40, v45\n"
"v_mfma_f32_16x16x32_f16 v[56:59], v[160:163], v[34:37], v[56:59]\n"
"v_mul_f32 v46, v40, v46\n"
"v_mul_f32 v47, v40, v47\n"
"s_waitcnt lgkmcnt(4)\n"
"v_mfma_f32_16x16x32_f16 v[52:55], v[96:99], v[60:63], v[52:55]\n"
"ds_read_b128 v[88:91], v29 offset:832\n"
"v_cvt_pk_bf16_f32 v44, v44, v45\n"
"v_cvt_pk_bf16_f32 v45, v46, v47\n"
"v_mfma_f32_16x16x32_f16 v[56:59], v[128:131], v[60:63], v[56:59]\n"
"v_pk_min_u16 v44, v44, s50\n"
"v_pk_min_u16 v45, v45, s50\n"
"ds_write_b64 v30, v[44:45] offset:0\n"
"v_mfma_f32_16x16x32_f16 v[56:59], v[96:99], v[64:67], v[56:59]\n"
"ds_read_b128 v[92:95], v29 offset:1344\n"
"v_mul_f32 v48, v40, v48\n"
"v_mul_f32 v49, v40, v49\n"
"v_mfma_f32_16x16x32_f16 v[52:55], v[164:167], v[68:71], v[52:55]\n"
"v_mul_f32 v50, v40, v50\n"
"v_mul_f32 v51, v40, v51\n"
"s_waitcnt lgkmcnt(5)\n"
"v_mfma_f32_16x16x32_f16 v[56:59], v[196:199], v[68:71], v[56:59]\n"
"ds_read_b128 v[2:5], v29 offset:1856\n"
"v_cvt_pk_bf16_f32 v48, v48, v49\n"
"v_cvt_pk_bf16_f32 v49, v50, v51\n"
"v_mfma_f32_16x16x32_f16 v[52:55], v[132:135], v[72:75], v[52:55]\n"
"ds_read_b128 v[34:37], v29 offset:256\n"
"v_pk_min_u16 v48, v48, s50\n"
"v_pk_min_u16 v49, v49, s50\n"
"v_mfma_f32_16x16x32_f16 v[56:59], v[164:167], v[72:75], v[56:59]\n"
"ds_write_b64 v30, v[48:49] offset:512\n"
"v_mfma_f32_16x16x32_f16 v[52:55], v[100:103], v[76:79], v[52:55]\n"
"ds_read_b128 v[60:63], v29 offset:768\n"
"s_waitcnt lgkmcnt(7)\n"
"v_mfma_f32_16x16x32_f16 v[56:59], v[132:135], v[76:79], v[56:59]\n"
"ds_write_b32 v13, v13 offset:544\n"
"v_mfma_f32_16x16x32_f16 v[56:59], v[100:103], v[80:83], v[56:59]\n"
"ds_read_b128 v[64:67], v29 offset:1280\n"
"v_mfma_f32_16x16x32_f16 v[52:55], v[168:171], v[84:87], v[52:55]\n"
"v_mfma_f32_16x16x32_f16 v[56:59], v[200:203], v[84:87], v[56:59]\n"
"ds_read_b128 v[68:71], v29 offset:1792\n"
"s_waitcnt lgkmcnt(7)\n"
"v_mfma_f32_16x16x32_f16 v[52:55], v[136:139], v[88:91], v[52:55]\n"
"ds_read_b128 v[72:75], v29 offset:192\n"
"v_mfma_f32_16x16x32_f16 v[56:59], v[168:171], v[88:91], v[56:59]\n"
"v_mfma_f32_16x16x32_f16 v[52:55], v[104:107], v[92:95], v[52:55]\n"
"ds_read_b128 v[76:79], v29 offset:704\n"
"v_mfma_f32_16x16x32_f16 v[56:59], v[136:139], v[92:95], v[56:59]\n"
"s_waitcnt lgkmcnt(5)\n"
"v_mfma_f32_16x16x32_f16 v[56:59], v[104:107], v[2:5], v[56:59]\n"
"ds_read_b128 v[80:83], v29 offset:1216\n"
"v_mfma_f32_16x16x32_f16 v[52:55], v[172:175], v[34:37], v[52:55]\n"
"v_mfma_f32_16x16x32_f16 v[56:59], v[204:207], v[34:37], v[56:59]\n"
"ds_read_b128 v[84:87], v29 offset:1728\n"
"v_mfma_f32_16x16x32_f16 v[52:55], v[140:143], v[60:63], v[52:55]\n"
"ds_read_b128 v[88:91], v29 offset:128\n"
"s_waitcnt lgkmcnt(5)\n"
"v_mfma_f32_16x16x32_f16 v[56:59], v[172:175], v[60:63], v[56:59]\n"
"v_mfma_f32_16x16x32_f16 v[52:55], v[108:111], v[64:67], v[52:55]\n"
"ds_read_b128 v[92:95], v29 offset:640\n"
"v_mfma_f32_16x16x32_f16 v[56:59], v[140:143], v[64:67], v[56:59]\n"
"v_mfma_f32_16x16x32_f16 v[56:59], v[108:111], v[68:71], v[56:59]\n"
"ds_read_b128 v[2:5], v29 offset:1152\n"
"s_waitcnt lgkmcnt(5)\n"
"v_mfma_f32_16x16x32_f16 v[52:55], v[176:179], v[72:75], v[52:55]\n"
"v_mfma_f32_16x16x32_f16 v[56:59], v[208:211], v[72:75], v[56:59]\n"
"ds_read_b128 v[34:37], v29 offset:1664\n"
"v_mfma_f32_16x16x32_f16 v[52:55], v[144:147], v[76:79], v[52:55]\n"
"ds_read_b128 v[60:63], v29 offset:64\n"
"v_mfma_f32_16x16x32_f16 v[56:59], v[176:179], v[76:79], v[56:59]\n"
"s_waitcnt lgkmcnt(4)\n"
"v_mfma_f32_16x16x32_f16 v[52:55], v[112:115], v[80:83], v[52:55]\n"
"ds_read_b128 v[64:67], v29 offset:576\n"
"ds_read_b32 v42, v14 offset:544\n"
"v_mfma_f32_16x16x32_f16 v[56:59], v[144:147], v[80:83], v[56:59]\n"
"ds_read_b64_tr_b16 v[224:225], v31 offset:0\n"
"ds_read_b64_tr_b16 v[226:227], v32 offset:0\n"
"v_mfma_f32_16x16x32_f16 v[56:59], v[112:115], v[84:87], v[56:59]\n"
"ds_read_b128 v[68:71], v29 offset:1088\n"
"v_mfma_f32_16x16x32_f16 v[52:55], v[180:183], v[88:91], v[52:55]\n"
"s_waitcnt lgkmcnt(7)\n"
"v_mfma_f32_16x16x32_f16 v[56:59], v[212:215], v[88:91], v[56:59]\n"
"ds_read_b128 v[72:75], v29 offset:1600\n"
"v_mfma_f32_16x16x32_f16 v[52:55], v[148:151], v[92:95], v[52:55]\n"
"ds_read_b128 v[76:79], v29 offset:0\n"
"v_mfma_f32_16x16x32_f16 v[56:59], v[180:183], v[92:95], v[56:59]\n"
"ds_read_b128 v[80:83], v29 offset:512\n"
"v_mfma_f32_16x16x32_f16 v[52:55], v[116:119], v[2:5], v[52:55]\n"
"ds_read_b128 v[84:87], v29 offset:1024\n"
"s_waitcnt lgkmcnt(7)\n"
"v_cmp_eq_u32 vcc, 0, v42\n"
"v_mfma_f32_16x16x32_f16 v[56:59], v[148:151], v[2:5], v[56:59]\n"
"v_mfma_f32_16x16x32_f16 v[56:59], v[116:119], v[34:37], v[56:59]\n"
"s_cbranch_vccnz .Lsl_284\n"
".Lbk_285:\n"
"v_mfma_f32_16x16x32_f16 v[52:55], v[184:187], v[60:63], v[52:55]\n"
"s_waitcnt lgkmcnt(6)\n"
"buffer_store_dwordx2 v[224:225], v41, s[12:15], s46 offen\n"
"v_mfma_f32_16x16x32_f16 v[56:59], v[216:219], v[60:63], v[56:59]\n"
"ds_read_b128 v[88:91], v29 offset:1536\n"
"s_waitcnt lgkmcnt(5)\n"
"v_mfma_f32_16x16x32_f16 v[52:55], v[152:155], v[64:67], v[52:55]\n"
"v_mfma_f32_16x16x32_f16 v[56:59], v[184:187], v[64:67], v[56:59]\n"
"v_mfma_f32_16x16x32_f16 v[52:55], v[120:123], v[68:71], v[52:55]\n"
"v_mfma_f32_16x16x32_f16 v[56:59], v[152:155], v[68:71], v[56:59]\n"
"s_waitcnt lgkmcnt(1)\n"
"v_mfma_f32_16x16x32_f16 v[56:59], v[120:123], v[72:75], v[56:59]\n"
"v_mfma_f32_16x16x32_f16 v[52:55], v[188:191], v[76:79], v[52:55]\n"
"v_mfma_f32_16x16x32_f16 v[52:55], v[156:159], v[80:83], v[52:55]\n"
"v_mfma_f32_16x16x32_f16 v[52:55], v[124:127], v[84:87], v[52:55]\n"
"s_waitcnt lgkmcnt(0)\n"
"v_mfma_f32_16x16x32_f16 v[56:59], v[220:223], v[76:79], v[56:59]\n"
"v_mfma_f32_16x16x32_f16 v[56:59], v[188:191], v[80:83], v[56:59]\n"
"v_mfma_f32_16x16x32_f16 v[56:59], v[156:159], v[84:87], v[56:59]\n"
"buffer_store_dwordx2 v[226:227], v41, s[12:15], s47 offen\n"
"v_mul_f32 v52, v40, v52\n"
"v_mul_f32 v53, v40, v53\n"
"v_mfma_f32_16x16x32_f16 v[56:59], v[124:127], v[88:91], v[56:59]\n"
"v_mul_f32 v54, v40, v54\n"
"v_mul_f32 v55, v40, v55\n"
"v_cvt_pk_bf16_f32 v52, v52, v53\n"
"v_cvt_pk_bf16_f32 v53, v54, v55\n"
"v_pk_min_u16 v52, v52, s50\n"
"v_pk_min_u16 v53, v53, s50\n"
"ds_write_b64 v30, v[52:53] offset:1024\n"
"s_nop 0\n"
"v_mul_f32 v56, v40, v56\n"
"v_mul_f32 v57, v40, v57\n"
"v_mul_f32 v58, v40, v58\n"
"v_mul_f32 v59, v40, v59\n"
"v_cvt_pk_bf16_f32 v56, v56, v57\n"
"v_cvt_pk_bf16_f32 v57, v58, v59\n"
"v_pk_min_u16 v56, v56, s50\n"
"v_pk_min_u16 v57, v57, s50\n"
"ds_write_b64 v30, v[56:57] offset:1536\n"
"ds_write_b32 v13, v13 offset:560\n"
"s_mov_b32 s17, 0x100000\n"
".Ltp_288:\n"
"ds_read_b32 v42, v14 offset:560\n"
"s_sub_u32 s17, s17, 1\n"
"s_cbranch_scc1 .Lou_289\n"
"s_waitcnt lgkmcnt(0)\n"
"v_cmp_eq_u32 vcc, 0, v42\n"
"s_cbranch_vccnz .Ltp_288\n"
".Lou_289:\n"
"s_waitcnt lgkmcnt(0)\n"
"ds_read_b64_tr_b16 v[228:229], v31 offset:1024\n"
"ds_read_b64_tr_b16 v[230:231], v32 offset:1024\n"
"s_waitcnt lgkmcnt(1)\n"
"buffer_store_dwordx2 v[228:229], v41, s[12:15], s48 offen\n"
"s_mov_b32 s89, 18\n"
".Ldl_290:\n"
"s_sub_u32 s89, s89, 1\n"
"s_cbranch_scc1 .Lde_291\n"
"s_sleep 1\n"
"s_branch .Ldl_290\n"
".Lde_291:\n"
"s_waitcnt lgkmcnt(0)\n"
"buffer_store_dwordx2 v[230:231], v41, s[12:15], s49 offen\n"
"v_add_u32 v31, 0x20c00, v31\n"
"v_add_u32 v32, 0x20c00, v32\n"
"s_add_u32 s46, s46, 0x140000\n"
"s_add_u32 s47, s47, 0x140000\n"
"s_add_u32 s48, s48, 0x140000\n"
"s_add_u32 s49, s49, 0x140000\n"
"s_lshr_b32 s79, s3, 1\n"
"s_mul_i32 s79, s79, -16\n"
"v_add_u32 v76, s79, v14\n"
"s_mov_b32 s17, 0x100000\n"
".Ltp_292:\n"
"s_sleep 1\n"
"ds_read_b32 v42, v14 offset:608\n"
"s_sub_u32 s17, s17, 1\n"
"s_cbranch_scc1 .Lou_293\n"
"s_waitcnt lgkmcnt(0)\n"
"v_cmp_eq_u32 vcc, 0, v42\n"
"s_cbranch_vccnz .Ltp_292\n"
".Lou_293:\n"
"s_waitcnt lgkmcnt(0)\n"
"ds_read_b64_tr_b16 v[68:69], v31 offset:0\n"
"ds_read_b64_tr_b16 v[70:71], v32 offset:0\n"
"s_waitcnt lgkmcnt(1)\n"
"buffer_store_dwordx2 v[68:69], v41, s[12:15], s46 offen\n"
"s_waitcnt lgkmcnt(0)\n"
"buffer_store_dwordx2 v[70:71], v41, s[12:15], s47 offen\n"
"s_mov_b32 s17, 0x100000\n"
".Ltp_294:\n"
"s_sleep 1\n"
"ds_read_b32 v42, v76 offset:592\n"
"s_sub_u32 s17, s17, 1\n"
"s_cbranch_scc1 .Lou_295\n"
"s_waitcnt lgkmcnt(0)\n"
"v_cmp_eq_u32 vcc, 0, v42\n"
"s_cbranch_vccnz .Ltp_294\n"
".Lou_295:\n"
"s_waitcnt lgkmcnt(0)\n"
"ds_read_b64_tr_b16 v[72:73], v31 offset:1024\n"
"ds_read_b64_tr_b16 v[74:75], v32 offset:1024\n"
"s_waitcnt lgkmcnt(1)\n"
"buffer_store_dwordx2 v[72:73], v41, s[12:15], s48 offen\n"
"s_waitcnt lgkmcnt(0)\n"
"buffer_store_dwordx2 v[74:75], v41, s[12:15], s49 offen\n"
"s_setprio 1\n"
"s_nop 0\n"
".Lnc_267:\n"
"s_add_u32 s31, s2, s3\n"
"s_cmp_lg_u32 s31, 0\n"
"s_cbranch_scc1 .Lfin_296\n"
"v_mov_b32 v32, s16\n"
"s_mov_b64 exec, 1\n"
"buffer_store_dword v32, off, s[4:7], 0 sc1\n"
"s_mov_b64 exec, -1\n"
".Lfin_296:\n"
"s_endpgm\n"
"s_branch .L_xend\n"
".Lsl_7:\n"
"s_nop 7\n"
"s_mov_b32 s17, 0x100000\n"
".Ltp_9:\n"
"ds_read_b32 v90, v14 offset:0\n"
"s_sub_u32 s17, s17, 1\n"
"s_cbranch_scc1 .Lou_10\n"
"s_waitcnt lgkmcnt(0)\n"
"v_cmp_eq_u32 vcc, 0, v90\n"
"s_cbranch_vccnz .Ltp_9\n"
".Lou_10:\n"
"s_waitcnt lgkmcnt(0)\n"
"s_branch .Lbk_8\n"
".Lsl_18:\n"
"s_nop 7\n"
"s_mov_b32 s17, 0x100000\n"
".Ltp_20:\n"
"ds_read_b32 v90, v14 offset:48\n"
"s_sub_u32 s17, s17, 1\n"
"s_cbranch_scc1 .Lou_21\n"
"s_waitcnt lgkmcnt(0)\n"
"v_cmp_eq_u32 vcc, 0, v90\n"
"s_cbranch_vccnz .Ltp_20\n"
".Lou_21:\n"
"s_waitcnt lgkmcnt(0)\n"
"s_branch .Lbk_19\n"
".Lsl_22:\n"
"s_nop 7\n"
"s_mov_b32 s17, 0x100000\n"
".Ltp_24:\n"
"ds_read_b32 v90, v14 offset:64\n"
"s_sub_u32 s17, s17, 1\n"
"s_cbranch_scc1 .Lou_25\n"
"s_waitcnt lgkmcnt(0)\n"
"v_cmp_eq_u32 vcc, 0, v90\n"
"s_cbranch_vccnz .Ltp_24\n"
".Lou_25:\n"
"s_waitcnt lgkmcnt(0)\n"
"s_branch .Lbk_23\n"
".Lsl_26:\n"
"s_nop 7\n"
"s_mov_b32 s17, 0x100000\n"
".Ltp_28:\n"
"ds_read_b32 v90, v14 offset:16\n"
"s_sub_u32 s17, s17, 1\n"
"s_cbranch_scc1 .Lou_29\n"
"s_waitcnt lgkmcnt(0)\n"
"v_cmp_eq_u32 vcc, 0, v90\n"
"s_cbranch_vccnz .Ltp_28\n"
".Lou_29:\n"
"s_waitcnt lgkmcnt(0)\n"
"s_branch .Lbk_27\n"
".Lsl_30:\n"
"s_nop 7\n"
"s_mov_b32 s17, 0x100000\n"
".Ltp_32:\n"
"ds_read_b32 v90, v14 offset:80\n"
"s_sub_u32 s17, s17, 1\n"
"s_cbranch_scc1 .Lou_33\n"
"s_waitcnt lgkmcnt(0)\n"
"v_cmp_eq_u32 vcc, 0, v90\n"
"s_cbranch_vccnz .Ltp_32\n"
".Lou_33:\n"
"s_waitcnt lgkmcnt(0)\n"
"s_branch .Lbk_31\n"
".Lsl_38:\n"
"s_nop 7\n"
"s_mov_b32 s17, 0x100000\n"
".Ltp_40:\n"
"ds_read_b128 v[84:87], v11 offset:0\n"
"ds_read_b32 v91, v12 offset:0\n"
"s_sub_u32 s17, s17, 1\n"
"s_cbranch_scc1 .Lou_41\n"
"s_waitcnt lgkmcnt(0)\n"
"v_max3_u32 v88, v84, v85, v91\n"
"v_max3_u32 v88, v86, v87, v88\n"
"v_cmp_eq_u32 vcc, -1, v88\n"
"s_cbranch_vccnz .Ltp_40\n"
".Lou_41:\n"
"s_waitcnt lgkmcnt(0)\n"
"v_add_f32 v84, v84, v85\n"
"v_add_f32 v86, v86, v87\n"
"v_add_f32 v84, v84, v86\n"
"v_mul_f32 v89, 0x3acccccd, v91\n"
"v_fma_f32 v84, -v91, v89, v84\n"
"v_fmamk_f32 v84, v84, 0x3acccccd, v27\n"
"v_max_f32 v84, v84, v27\n"
"v_rsq_f32 v88, v84\n"
"s_nop 0\n"
"v_mul_f32 v89, -v89, v88\n"
"s_nop 1\n"
"s_branch .Lbk_39\n"
".Lsl_42:\n"
"s_nop 7\n"
"s_mov_b32 s17, 0x100000\n"
".Ltp_44:\n"
"ds_read_b128 v[84:87], v11 offset:320\n"
"ds_read_b32 v91, v12 offset:320\n"
"s_sub_u32 s17, s17, 1\n"
"s_cbranch_scc1 .Lou_45\n"
"s_waitcnt lgkmcnt(0)\n"
"v_max3_u32 v88, v84, v85, v91\n"
"v_max3_u32 v88, v86, v87, v88\n"
"v_cmp_eq_u32 vcc, -1, v88\n"
"s_cbranch_vccnz .Ltp_44\n"
".Lou_45:\n"
"s_waitcnt lgkmcnt(0)\n"
"v_add_f32 v84, v84, v85\n"
"v_add_f32 v86, v86, v87\n"
"v_add_f32 v84, v84, v86\n"
"v_mul_f32 v89, 0x3acccccd, v91\n"
"v_fma_f32 v84, -v91, v89, v84\n"
"v_fmamk_f32 v84, v84, 0x3acccccd, v27\n"
"v_max_f32 v84, v84, v27\n"
"v_rsq_f32 v88, v84\n"
"s_nop 0\n"
"v_mul_f32 v89, -v89, v88\n"
"s_nop 1\n"
"s_branch .Lbk_43\n"
".Lsl_46:\n"
"s_nop 7\n"
"s_mov_b32 s17, 0x100000\n"
".Ltp_48:\n"
"ds_read_b128 v[84:87], v11 offset:640\n"
"ds_read_b32 v91, v12 offset:640\n"
"s_sub_u32 s17, s17, 1\n"
"s_cbranch_scc1 .Lou_49\n"
"s_waitcnt lgkmcnt(0)\n"
"v_max3_u32 v88, v84, v85, v91\n"
"v_max3_u32 v88, v86, v87, v88\n"
"v_cmp_eq_u32 vcc, -1, v88\n"
"s_cbranch_vccnz .Ltp_48\n"
".Lou_49:\n"
"s_waitcnt lgkmcnt(0)\n"
"v_add_f32 v84, v84, v85\n"
"v_add_f32 v86, v86, v87\n"
"v_add_f32 v84, v84, v86\n"
"v_mul_f32 v89, 0x3acccccd, v91\n"
"v_fma_f32 v84, -v91, v89, v84\n"
"v_fmamk_f32 v84, v84, 0x3acccccd, v27\n"
"v_max_f32 v84, v84, v27\n"
"v_rsq_f32 v88, v84\n"
"s_nop 0\n"
"v_mul_f32 v89, -v89, v88\n"
"s_nop 1\n"
"s_branch .Lbk_47\n"
".Lsl_50:\n"
"s_nop 7\n"
"s_mov_b32 s17, 0x100000\n"
".Ltp_52:\n"
"ds_read_b128 v[84:87], v11 offset:960\n"
"ds_read_b32 v91, v12 offset:960\n"
"s_sub_u32 s17, s17, 1\n"
"s_cbranch_scc1 .Lou_53\n"
"s_waitcnt lgkmcnt(0)\n"
"v_max3_u32 v88, v84, v85, v91\n"
"v_max3_u32 v88, v86, v87, v88\n"
"v_cmp_eq_u32 vcc, -1, v88\n"
"s_cbranch_vccnz .Ltp_52\n"
".Lou_53:\n"
"s_waitcnt lgkmcnt(0)\n"
"v_add_f32 v84, v84, v85\n"
"v_add_f32 v86, v86, v87\n"
"v_add_f32 v84, v84, v86\n"
"v_mul_f32 v89, 0x3acccccd, v91\n"
"v_fma_f32 v84, -v91, v89, v84\n"
"v_fmamk_f32 v84, v84, 0x3acccccd, v27\n"
"v_max_f32 v84, v84, v27\n"
"v_rsq_f32 v88, v84\n"
"s_nop 0\n"
"v_mul_f32 v89, -v89, v88\n"
"s_nop 1\n"
"s_branch .Lbk_51\n"
".Lsl_54:\n"
"s_nop 7\n"
"s_mov_b32 s17, 0x100000\n"
".Ltp_56:\n"
"ds_read_b128 v[44:47], v11 offset:1280\n"
"ds_read_b128 v[48:51], v11 offset:1600\n"
"s_sub_u32 s17, s17, 1\n"
"s_cbranch_scc1 .Lou_57\n"
"s_waitcnt lgkmcnt(0)\n"
"v_max3_u32 v52, v44, v45, v51\n"
"v_max3_u32 v52, v46, v47, v52\n"
"v_max3_u32 v52, v48, v49, v52\n"
"v_max3_u32 v52, v50, v51, v52\n"
"v_cmp_eq_u32 vcc, -1, v52\n"
"s_cbranch_vccnz .Ltp_56\n"
".Lou_57:\n"
"s_waitcnt lgkmcnt(0)\n"
"v_add_f32 v44, v44, v45\n"
"v_add_f32 v46, v46, v47\n"
"v_add_f32 v48, v48, v49\n"
"v_add_f32 v50, v50, v51\n"
"v_add_f32 v48, v48, v50\n"
"v_add_f32 v44, v44, v46\n"
"v_mul_f32 v53, 0x3acccccd, v48\n"
"v_fma_f32 v44, -v48, v53, v44\n"
"v_fmamk_f32 v44, v44, 0x3acccccd, v27\n"
"v_max_f32 v44, v44, v27\n"
"v_rsq_f32 v52, v44\n"
"s_nop 0\n"
"v_mul_f32 v53, -v53, v52\n"
"s_nop 1\n"
"s_branch .Lbk_55\n"
".Lsl_50c:\n"
"s_nop 7\n"
"s_mov_b32 s17, 0x100000\n"
".Ltp_52c:\n"
"ds_read_b128 v[84:87], v11 offset:960\n"
"ds_read_b32 v91, v12 offset:960\n"
"s_sub_u32 s17, s17, 1\n"
"s_cbranch_scc1 .Lou_53c\n"
"s_waitcnt lgkmcnt(0)\n"
"v_max3_u32 v88, v84, v85, v91\n"
"v_max3_u32 v88, v86, v87, v88\n"
"v_cmp_eq_u32 vcc, -1, v88\n"
"s_cbranch_vccnz .Ltp_52c\n"
".Lou_53c:\n"
"s_waitcnt lgkmcnt(0)\n"
"v_add_f32 v84, v84, v85\n"
"v_add_f32 v86, v86, v87\n"
"v_add_f32 v84, v84, v86\n"
"v_mul_f32 v89, 0x3acccccd, v91\n"
"v_fma_f32 v84, -v91, v89, v84\n"
"v_fmamk_f32 v84, v84, 0x3acccccd, v27\n"
"v_max_f32 v84, v84, v27\n"
"v_rsq_f32 v88, v84\n"
"s_nop 0\n"
"v_mul_f32 v89, -v89, v88\n"
"s_nop 1\n"
"s_branch .Lbk_51c\n"
".Lsl_54c:\n"
"s_nop 7\n"
"s_mov_b32 s17, 0x100000\n"
".Ltp_56c:\n"
"ds_read_b128 v[44:47], v11 offset:1280\n"
"ds_read_b128 v[48:51], v11 offset:1600\n"
"s_sub_u32 s17, s17, 1\n"
"s_cbranch_scc1 .Lou_57c\n"
"s_waitcnt lgkmcnt(0)\n"
"v_max3_u32 v52, v44, v45, v51\n"
"v_max3_u32 v52, v46, v47, v52\n"
"v_max3_u32 v52, v48, v49, v52\n"
"v_max3_u32 v52, v50, v51, v52\n"
"v_cmp_eq_u32 vcc, -1, v52\n"
"s_cbranch_vccnz .Ltp_56c\n"
".Lou_57c:\n"
"s_waitcnt lgkmcnt(0)\n"
"v_add_f32 v44, v44, v45\n"
"v_add_f32 v46, v46, v47\n"
"v_add_f32 v48, v48, v49\n"
"v_add_f32 v50, v50, v51\n"
"v_add_f32 v48, v48, v50\n"
"v_add_f32 v44, v44, v46\n"
"v_mul_f32 v53, 0x3acccccd, v48\n"
"v_fma_f32 v44, -v48, v53, v44\n"
"v_fmamk_f32 v44, v44, 0x3acccccd, v27\n"
"v_max_f32 v44, v44, v27\n"
"v_rsq_f32 v52, v44\n"
"s_nop 0\n"
"v_mul_f32 v53, -v53, v52\n"
"s_nop 1\n"
"s_branch .Lbk_55c\n"
".Lsl_130:\n"
"s_nop 7\n"
"s_mov_b32 s17, 0x100000\n"
".Ltp_132:\n"
"ds_read_b32 v90, v14 offset:288\n"
"s_sub_u32 s17, s17, 1\n"
"s_cbranch_scc1 .Lou_133\n"
"s_waitcnt lgkmcnt(0)\n"
"v_cmp_eq_u32 vcc, 0, v90\n"
"s_cbranch_vccnz .Ltp_132\n"
".Lou_133:\n"
"s_waitcnt lgkmcnt(0)\n"
"s_branch .Lbk_131\n"
".Lsl_134:\n"
"s_nop 7\n"
"s_mov_b32 s17, 0x100000\n"
".Ltp_136:\n"
"ds_read_b128 v[36:39], v11 offset:1856\n"
"ds_read_b128 v[40:43], v11 offset:3136\n"
"s_sub_u32 s17, s17, 1\n"
"s_cbranch_scc1 .Lou_137\n"
"s_waitcnt lgkmcnt(0)\n"
"v_max3_u32 v88, v36, v37, v43\n"
"v_max3_u32 v88, v38, v39, v88\n"
"v_max3_u32 v88, v40, v41, v88\n"
"v_max3_u32 v88, v42, v43, v88\n"
"v_cmp_eq_u32 vcc, -1, v88\n"
"s_cbranch_vccnz .Ltp_136\n"
".Lou_137:\n"
"s_waitcnt lgkmcnt(0)\n"
"v_add_f32 v36, v36, v37\n"
"v_add_f32 v38, v38, v39\n"
"v_add_f32 v40, v40, v41\n"
"v_add_f32 v42, v42, v43\n"
"v_add_f32 v40, v40, v42\n"
"v_add_f32 v36, v36, v38\n"
"v_mul_f32 v89, 0x3acccccd, v40\n"
"v_fma_f32 v36, -v40, v89, v36\n"
"v_fmamk_f32 v36, v36, 0x3acccccd, v27\n"
"v_add_f32 v92, v92, v36\n"
"v_max_f32 v36, v36, v27\n"
"v_rsq_f32 v88, v36\n"
"s_nop 0\n"
"v_mul_f32 v89, -v89, v88\n"
"s_nop 1\n"
"s_branch .Lbk_135\n"
".Lsl_138:\n"
"s_nop 7\n"
"s_mov_b32 s17, 0x100000\n"
".Ltp_140:\n"
"ds_read_b128 v[84:87], v11 offset:2176\n"
"ds_read_b32 v91, v12 offset:2176\n"
"s_sub_u32 s17, s17, 1\n"
"s_cbranch_scc1 .Lou_141\n"
"s_waitcnt lgkmcnt(0)\n"
"v_max3_u32 v88, v84, v85, v91\n"
"v_max3_u32 v88, v86, v87, v88\n"
"v_cmp_eq_u32 vcc, -1, v88\n"
"s_cbranch_vccnz .Ltp_140\n"
".Lou_141:\n"
"s_waitcnt lgkmcnt(0)\n"
"v_add_f32 v84, v84, v85\n"
"v_add_f32 v86, v86, v87\n"
"v_add_f32 v84, v84, v86\n"
"v_mul_f32 v89, 0x3acccccd, v91\n"
"v_fma_f32 v84, -v91, v89, v84\n"
"v_fmamk_f32 v84, v84, 0x3acccccd, v27\n"
"v_add_f32 v92, v92, v84\n"
"v_max_f32 v84, v84, v27\n"
"v_rsq_f32 v88, v84\n"
"s_nop 0\n"
"v_mul_f32 v89, -v89, v88\n"
"s_nop 1\n"
"s_branch .Lbk_139\n"
".Lsl_142:\n"
"s_nop 7\n"
"s_mov_b32 s17, 0x100000\n"
".Ltp_144:\n"
"ds_read_b128 v[84:87], v11 offset:2496\n"
"ds_read_b32 v91, v12 offset:2496\n"
"s_sub_u32 s17, s17, 1\n"
"s_cbranch_scc1 .Lou_145\n"
"s_waitcnt lgkmcnt(0)\n"
"v_max3_u32 v88, v84, v85, v91\n"
"v_max3_u32 v88, v86, v87, v88\n"
"v_cmp_eq_u32 vcc, -1, v88\n"
"s_cbranch_vccnz .Ltp_144\n"
".Lou_145:\n"
"s_waitcnt lgkmcnt(0)\n"
"v_add_f32 v84, v84, v85\n"
"v_add_f32 v86, v86, v87\n"
"v_add_f32 v84, v84, v86\n"
"v_mul_f32 v89, 0x3acccccd, v91\n"
"v_fma_f32 v84, -v91, v89, v84\n"
"v_fmamk_f32 v84, v84, 0x3acccccd, v27\n"
"v_add_f32 v92, v92, v84\n"
"v_max_f32 v84, v84, v27\n"
"v_rsq_f32 v88, v84\n"
"s_nop 0\n"
"v_mul_f32 v89, -v89, v88\n"
"s_nop 1\n"
"s_branch .Lbk_143\n"
".Lsl_146:\n"
"s_nop 7\n"
"s_mov_b32 s17, 0x100000\n"
".Ltp_148:\n"
"ds_read_b128 v[84:87], v11 offset:2816\n"
"ds_read_b128 v[76:79], v11 offset:3456\n"
"s_sub_u32 s17, s17, 1\n"
"s_cbranch_scc1 .Lou_149\n"
"s_waitcnt lgkmcnt(0)\n"
"v_max3_u32 v52, v84, v85, v79\n"
"v_max3_u32 v52, v86, v87, v52\n"
"v_max3_u32 v52, v76, v77, v52\n"
"v_max3_u32 v52, v78, v79, v52\n"
"v_cmp_eq_u32 vcc, -1, v52\n"
"s_cbranch_vccnz .Ltp_148\n"
".Lou_149:\n"
"s_waitcnt lgkmcnt(0)\n"
"v_add_f32 v84, v84, v85\n"
"v_add_f32 v86, v86, v87\n"
"v_add_f32 v76, v76, v77\n"
"v_add_f32 v78, v78, v79\n"
"v_add_f32 v76, v76, v78\n"
"v_add_f32 v84, v84, v86\n"
"v_mul_f32 v53, 0x3acccccd, v76\n"
"v_fma_f32 v84, -v76, v53, v84\n"
"v_fmamk_f32 v84, v84, 0x3acccccd, v27\n"
"v_add_f32 v92, v92, v84\n"
"v_max_f32 v84, v84, v27\n"
"v_rsq_f32 v52, v84\n"
"s_nop 0\n"
"v_mul_f32 v53, -v53, v52\n"
"s_nop 1\n"
"s_branch .Lbk_147\n"
".Lsl_150:\n"
"s_nop 7\n"
"s_mov_b32 s17, 32768\n"
".Ltp_152:\n"
"buffer_load_dwordx4 v[40:43], v1, s[4:7], s25 offen offset:1024 sc1\n"
"buffer_load_dwordx4 v[36:39], v93, s[4:7], s25 offen offset:0 sc1\n"
"s_waitcnt vmcnt(0)\n"
"s_sub_u32 s17, s17, 1\n"
"s_cbranch_scc1 .Lbk_151\n"
"v_bitop3_b32 v84, v37, v39, s16 bitop3:0x7e\n"
"v_bitop3_b32 v85, v41, v43, s16 bitop3:0x7e\n"
"v_and_b32 v84, s26, v84\n"
"v_and_or_b32 v84, v85, s27, v84\n"
"v_cmp_ne_u32 vcc, 0, v84\n"
"s_cbranch_vccnz .Ltp_152\n"
"s_branch .Lbk_151\n"
".Lrd_120:\n"
"s_nop 7\n"
"s_cmp_eq_u32 s54, 0\n"
"s_cbranch_scc1 .Lfi_153\n"
"s_branch .Lgo_119\n"
".Lfi_153:\n"
"s_mov_b32 s54, 1\n"
"s_mov_b32 s46, s44\n"
"s_add_u32 s47, s46, 0x1000\n"
"buffer_load_dwordx4 v[28:31], v1, s[4:7], s46 offen offset:0 sc1\n"
"buffer_load_dwordx4 v[32:35], v1, s[4:7], s46 offen offset:1024 sc1\n"
"buffer_load_dwordx4 v[36:39], v1, s[4:7], s46 offen offset:2048 sc1\n"
"buffer_load_dwordx4 v[40:43], v1, s[4:7], s46 offen offset:3072 sc1\n"
"buffer_load_dwordx4 v[44:47], v1, s[4:7], s47 offen offset:0 sc1\n"
"s_mov_b32 s17, 32768\n"
".Ltp_154:\n"
"s_waitcnt vmcnt(0)\n"
"s_sub_u32 s17, s17, 1\n"
"s_cbranch_scc1 .Lou_155\n"
"v_max_u32 v48, v28, v29\n"
"v_max3_u32 v48, v30, v31, v48\n"
"v_max3_u32 v48, v32, v33, v48\n"
"v_max3_u32 v48, v34, v35, v48\n"
"v_max3_u32 v48, v36, v37, v48\n"
"v_max3_u32 v48, v38, v39, v48\n"
"v_max3_u32 v48, v40, v41, v48\n"
"v_max3_u32 v48, v42, v43, v48\n"
"v_max3_u32 v48, v44, v45, v48\n"
"v_max3_u32 v48, v46, v47, v48\n"
"v_cmp_eq_u32 vcc, -1, v48\n"
"s_cbranch_vccz .Lou_155\n"
"v_max_u32 v48, v30, v31\n"
"v_max3_u32 v48, v28, v29, v48\n"
"v_cmp_eq_u32 vcc, -1, v48\n"
"s_cbranch_vccz .Lsk_156\n"
"s_mov_b64 exec, vcc\n"
"buffer_load_dwordx4 v[28:31], v1, s[4:7], s46 offen offset:0 sc1\n"
"s_mov_b64 exec, -1\n"
".Lsk_156:\n"
"v_max_u32 v48, v34, v35\n"
"v_max3_u32 v48, v32, v33, v48\n"
"v_cmp_eq_u32 vcc, -1, v48\n"
"s_cbranch_vccz .Lsk_157\n"
"s_mov_b64 exec, vcc\n"
"buffer_load_dwordx4 v[32:35], v1, s[4:7], s46 offen offset:1024 sc1\n"
"s_mov_b64 exec, -1\n"
".Lsk_157:\n"
"v_max_u32 v48, v38, v39\n"
"v_max3_u32 v48, v36, v37, v48\n"
"v_cmp_eq_u32 vcc, -1, v48\n"
"s_cbranch_vccz .Lsk_158\n"
"s_mov_b64 exec, vcc\n"
"buffer_load_dwordx4 v[36:39], v1, s[4:7], s46 offen offset:2048 sc1\n"
"s_mov_b64 exec, -1\n"
".Lsk_158:\n"
"v_max_u32 v48, v42, v43\n"
"v_max3_u32 v48, v40, v41, v48\n"
"v_cmp_eq_u32 vcc, -1, v48\n"
"s_cbranch_vccz .Lsk_159\n"
"s_mov_b64 exec, vcc\n"
"buffer_load_dwordx4 v[40:43], v1, s[4:7], s46 offen offset:3072 sc1\n"
"s_mov_b64 exec, -1\n"
".Lsk_159:\n"
"v_max_u32 v48, v46, v47\n"
"v_max3_u32 v48, v44, v45, v48\n"
"v_cmp_eq_u32 vcc, -1, v48\n"
"s_cbranch_vccz .Lsk_160\n"
"s_mov_b64 exec, vcc\n"
"buffer_load_dwordx4 v[44:47], v1, s[4:7], s47 offen offset:0 sc1\n"
"s_mov_b64 exec, -1\n"
".Lsk_160:\n"
"s_branch .Ltp_154\n"
".Lou_155:\n"
"s_waitcnt vmcnt(0)\n"
"s_add_u32 s46, s44, 0x5000\n"
"s_add_u32 s47, s46, 0x1000\n"
"buffer_load_dwordx4 v[28:31], v1, s[4:7], s46 offen offset:0 sc1\n"
"buffer_load_dwordx4 v[32:35], v1, s[4:7], s46 offen offset:1024 sc1\n"
"buffer_load_dwordx4 v[36:39], v1, s[4:7], s46 offen offset:2048 sc1\n"
"buffer_load_dwordx4 v[40:43], v1, s[4:7], s46 offen offset:3072 sc1\n"
"buffer_load_dwordx4 v[44:47], v1, s[4:7], s47 offen offset:0 sc1\n"
"s_mov_b32 s17, 32768\n"
".Ltp_161:\n"
"s_waitcnt vmcnt(0)\n"
"s_sub_u32 s17, s17, 1\n"
"s_cbranch_scc1 .Lou_162\n"
"v_max_u32 v48, v28, v29\n"
"v_max3_u32 v48, v30, v31, v48\n"
"v_max3_u32 v48, v32, v33, v48\n"
"v_max3_u32 v48, v34, v35, v48\n"
"v_max3_u32 v48, v36, v37, v48\n"
"v_max3_u32 v48, v38, v39, v48\n"
"v_max3_u32 v48, v40, v41, v48\n"
"v_max3_u32 v48, v42, v43, v48\n"
"v_max3_u32 v48, v44, v45, v48\n"
"v_max3_u32 v48, v46, v47, v48\n"
"v_cmp_eq_u32 vcc, -1, v48\n"
"s_cbranch_vccz .Lou_162\n"
"v_max_u32 v48, v30, v31\n"
"v_max3_u32 v48, v28, v29, v48\n"
"v_cmp_eq_u32 vcc, -1, v48\n"
"s_cbranch_vccz .Lsk_163\n"
"s_mov_b64 exec, vcc\n"
"buffer_load_dwordx4 v[28:31], v1, s[4:7], s46 offen offset:0 sc1\n"
"s_mov_b64 exec, -1\n"
".Lsk_163:\n"
"v_max_u32 v48, v34, v35\n"
"v_max3_u32 v48, v32, v33, v48\n"
"v_cmp_eq_u32 vcc, -1, v48\n"
"s_cbranch_vccz .Lsk_164\n"
"s_mov_b64 exec, vcc\n"
"buffer_load_dwordx4 v[32:35], v1, s[4:7], s46 offen offset:1024 sc1\n"
"s_mov_b64 exec, -1\n"
".Lsk_164:\n"
"v_max_u32 v48, v38, v39\n"
"v_max3_u32 v48, v36, v37, v48\n"
"v_cmp_eq_u32 vcc, -1, v48\n"
"s_cbranch_vccz .Lsk_165\n"
"s_mov_b64 exec, vcc\n"
"buffer_load_dwordx4 v[36:39], v1, s[4:7], s46 offen offset:2048 sc1\n"
"s_mov_b64 exec, -1\n"
".Lsk_165:\n"
"v_max_u32 v48, v42, v43\n"
"v_max3_u32 v48, v40, v41, v48\n"
"v_cmp_eq_u32 vcc, -1, v48\n"
"s_cbranch_vccz .Lsk_166\n"
"s_mov_b64 exec, vcc\n"
"buffer_load_dwordx4 v[40:43], v1, s[4:7], s46 offen offset:3072 sc1\n"
"s_mov_b64 exec, -1\n"
".Lsk_166:\n"
"v_max_u32 v48, v46, v47\n"
"v_max3_u32 v48, v44, v45, v48\n"
"v_cmp_eq_u32 vcc, -1, v48\n"
"s_cbranch_vccz .Lsk_167\n"
"s_mov_b64 exec, vcc\n"
"buffer_load_dwordx4 v[44:47], v1, s[4:7], s47 offen offset:0 sc1\n"
"s_mov_b64 exec, -1\n"
".Lsk_167:\n"
"s_branch .Ltp_161\n"
".Lou_162:\n"
"s_waitcnt vmcnt(0)\n"
"s_add_u32 s46, s44, 0xa000\n"
"s_add_u32 s47, s46, 0x1000\n"
"buffer_load_dwordx4 v[28:31], v1, s[4:7], s46 offen offset:0 sc1\n"
"buffer_load_dwordx4 v[32:35], v1, s[4:7], s46 offen offset:1024 sc1\n"
"buffer_load_dwordx4 v[36:39], v1, s[4:7], s46 offen offset:2048 sc1\n"
"buffer_load_dwordx4 v[40:43], v1, s[4:7], s46 offen offset:3072 sc1\n"
"buffer_load_dwordx4 v[44:47], v1, s[4:7], s47 offen offset:0 sc1\n"
"s_mov_b32 s17, 32768\n"
".Ltp_168:\n"
"s_waitcnt vmcnt(0)\n"
"s_sub_u32 s17, s17, 1\n"
"s_cbranch_scc1 .Lou_169\n"
"v_max_u32 v48, v28, v29\n"
"v_max3_u32 v48, v30, v31, v48\n"
"v_max3_u32 v48, v32, v33, v48\n"
"v_max3_u32 v48, v34, v35, v48\n"
"v_max3_u32 v48, v36, v37, v48\n"
"v_max3_u32 v48, v38, v39, v48\n"
"v_max3_u32 v48, v40, v41, v48\n"
"v_max3_u32 v48, v42, v43, v48\n"
"v_max3_u32 v48, v44, v45, v48\n"
"v_max3_u32 v48, v46, v47, v48\n"
"v_cmp_eq_u32 vcc, -1, v48\n"
"s_cbranch_vccz .Lou_169\n"
"v_max_u32 v48, v30, v31\n"
"v_max3_u32 v48, v28, v29, v48\n"
"v_cmp_eq_u32 vcc, -1, v48\n"
"s_cbranch_vccz .Lsk_170\n"
"s_mov_b64 exec, vcc\n"
"buffer_load_dwordx4 v[28:31], v1, s[4:7], s46 offen offset:0 sc1\n"
"s_mov_b64 exec, -1\n"
".Lsk_170:\n"
"v_max_u32 v48, v34, v35\n"
"v_max3_u32 v48, v32, v33, v48\n"
"v_cmp_eq_u32 vcc, -1, v48\n"
"s_cbranch_vccz .Lsk_171\n"
"s_mov_b64 exec, vcc\n"
"buffer_load_dwordx4 v[32:35], v1, s[4:7], s46 offen offset:1024 sc1\n"
"s_mov_b64 exec, -1\n"
".Lsk_171:\n"
"v_max_u32 v48, v38, v39\n"
"v_max3_u32 v48, v36, v37, v48\n"
"v_cmp_eq_u32 vcc, -1, v48\n"
"s_cbranch_vccz .Lsk_172\n"
"s_mov_b64 exec, vcc\n"
"buffer_load_dwordx4 v[36:39], v1, s[4:7], s46 offen offset:2048 sc1\n"
"s_mov_b64 exec, -1\n"
".Lsk_172:\n"
"v_max_u32 v48, v42, v43\n"
"v_max3_u32 v48, v40, v41, v48\n"
"v_cmp_eq_u32 vcc, -1, v48\n"
"s_cbranch_vccz .Lsk_173\n"
"s_mov_b64 exec, vcc\n"
"buffer_load_dwordx4 v[40:43], v1, s[4:7], s46 offen offset:3072 sc1\n"
"s_mov_b64 exec, -1\n"
".Lsk_173:\n"
"v_max_u32 v48, v46, v47\n"
"v_max3_u32 v48, v44, v45, v48\n"
"v_cmp_eq_u32 vcc, -1, v48\n"
"s_cbranch_vccz .Lsk_174\n"
"s_mov_b64 exec, vcc\n"
"buffer_load_dwordx4 v[44:47], v1, s[4:7], s47 offen offset:0 sc1\n"
"s_mov_b64 exec, -1\n"
".Lsk_174:\n"
"s_branch .Ltp_168\n"
".Lou_169:\n"
"s_waitcnt vmcnt(0)\n"
"s_add_u32 s46, s44, 0xf000\n"
"s_add_u32 s47, s46, 0x1000\n"
"buffer_load_dwordx4 v[28:31], v1, s[4:7], s46 offen offset:0 sc1\n"
"buffer_load_dwordx4 v[32:35], v1, s[4:7], s46 offen offset:1024 sc1\n"
"buffer_load_dwordx4 v[36:39], v1, s[4:7], s46 offen offset:2048 sc1\n"
"buffer_load_dwordx4 v[40:43], v1, s[4:7], s46 offen offset:3072 sc1\n"
"buffer_load_dwordx4 v[44:47], v1, s[4:7], s47 offen offset:0 sc1\n"
"s_mov_b32 s17, 32768\n"
".Ltp_175:\n"
"s_waitcnt vmcnt(0)\n"
"s_sub_u32 s17, s17, 1\n"
"s_cbranch_scc1 .Lou_176\n"
"v_max_u32 v48, v28, v29\n"
"v_max3_u32 v48, v30, v31, v48\n"
"v_max3_u32 v48, v32, v33, v48\n"
"v_max3_u32 v48, v34, v35, v48\n"
"v_max3_u32 v48, v36, v37, v48\n"
"v_max3_u32 v48, v38, v39, v48\n"
"v_max3_u32 v48, v40, v41, v48\n"
"v_max3_u32 v48, v42, v43, v48\n"
"v_max3_u32 v48, v44, v45, v48\n"
"v_max3_u32 v48, v46, v47, v48\n"
"v_cmp_eq_u32 vcc, -1, v48\n"
"s_cbranch_vccz .Lou_176\n"
"v_max_u32 v48, v30, v31\n"
"v_max3_u32 v48, v28, v29, v48\n"
"v_cmp_eq_u32 vcc, -1, v48\n"
"s_cbranch_vccz .Lsk_177\n"
"s_mov_b64 exec, vcc\n"
"buffer_load_dwordx4 v[28:31], v1, s[4:7], s46 offen offset:0 sc1\n"
"s_mov_b64 exec, -1\n"
".Lsk_177:\n"
"v_max_u32 v48, v34, v35\n"
"v_max3_u32 v48, v32, v33, v48\n"
"v_cmp_eq_u32 vcc, -1, v48\n"
"s_cbranch_vccz .Lsk_178\n"
"s_mov_b64 exec, vcc\n"
"buffer_load_dwordx4 v[32:35], v1, s[4:7], s46 offen offset:1024 sc1\n"
"s_mov_b64 exec, -1\n"
".Lsk_178:\n"
"v_max_u32 v48, v38, v39\n"
"v_max3_u32 v48, v36, v37, v48\n"
"v_cmp_eq_u32 vcc, -1, v48\n"
"s_cbranch_vccz .Lsk_179\n"
"s_mov_b64 exec, vcc\n"
"buffer_load_dwordx4 v[36:39], v1, s[4:7], s46 offen offset:2048 sc1\n"
"s_mov_b64 exec, -1\n"
".Lsk_179:\n"
"v_max_u32 v48, v42, v43\n"
"v_max3_u32 v48, v40, v41, v48\n"
"v_cmp_eq_u32 vcc, -1, v48\n"
"s_cbranch_vccz .Lsk_180\n"
"s_mov_b64 exec, vcc\n"
"buffer_load_dwordx4 v[40:43], v1, s[4:7], s46 offen offset:3072 sc1\n"
"s_mov_b64 exec, -1\n"
".Lsk_180:\n"
"v_max_u32 v48, v46, v47\n"
"v_max3_u32 v48, v44, v45, v48\n"
"v_cmp_eq_u32 vcc, -1, v48\n"
"s_cbranch_vccz .Lsk_181\n"
"s_mov_b64 exec, vcc\n"
"buffer_load_dwordx4 v[44:47], v1, s[4:7], s47 offen offset:0 sc1\n"
"s_mov_b64 exec, -1\n"
".Lsk_181:\n"
"s_branch .Ltp_175\n"
".Lou_176:\n"
"s_waitcnt vmcnt(0)\n"
"s_mov_b32 s17, 0x100000\n"
".Ltp_182:\n"
"ds_read_b32 v90, v14 offset:320\n"
"s_sub_u32 s17, s17, 1\n"
"s_cbranch_scc1 .Lou_183\n"
"s_waitcnt lgkmcnt(0)\n"
"v_cmp_eq_u32 vcc, 0, v90\n"
"s_cbranch_vccnz .Ltp_182\n"
".Lou_183:\n"
"s_waitcnt lgkmcnt(0)\n"
"ds_write_b32 v13, v13 offset:336\n"
"s_mov_b32 s17, 0x100000\n"
".Ltp_184:\n"
"ds_read_b32 v90, v14 offset:336\n"
"s_sub_u32 s17, s17, 1\n"
"s_cbranch_scc1 .Lou_185\n"
"s_waitcnt lgkmcnt(0)\n"
"v_cmp_eq_u32 vcc, 0, v90\n"
"s_cbranch_vccnz .Ltp_184\n"
".Lou_185:\n"
"s_waitcnt lgkmcnt(0)\n"
"v_mov_b32 v90, 0\n"
"ds_write_b32 v13, v90 offset:272\n"
"ds_write_b32 v13, v90 offset:288\n"
"v_mov_b32 v90, -1\n"
"ds_write_b32 v9, v90 offset:1856\n"
"ds_write_b32 v9, v90 offset:2176\n"
"ds_write_b32 v9, v90 offset:2496\n"
"ds_write_b32 v9, v90 offset:2816\n"
"ds_write_b32 v9, v90 offset:3136\n"
"ds_write_b32 v10, v90 offset:3456\n"
"s_waitcnt vmcnt(0)\n"
"ds_write_b32 v13, v13 offset:352\n"
"s_mov_b32 s17, 0x100000\n"
".Ltp_186:\n"
"ds_read_b32 v90, v14 offset:352\n"
"s_sub_u32 s17, s17, 1\n"
"s_cbranch_scc1 .Lou_187\n"
"s_waitcnt lgkmcnt(0)\n"
"v_cmp_eq_u32 vcc, 0, v90\n"
"s_cbranch_vccnz .Ltp_186\n"
".Lou_187:\n"
"s_waitcnt lgkmcnt(0)\n"
"s_branch .Lag_118\n"
".Lfx_193:\n"
"s_nop 7\n"
"s_add_u32 m0, s29, 0\n"
"s_add_u32 s31, s44, 0x14f000\n"
"buffer_load_dwordx4 v1, s[4:7], s31 offen offset:0 sc1 lds\n"
"buffer_load_dwordx4 v1, s[4:7], s31 offen offset:1024 sc1 lds\n"
"buffer_load_dwordx4 v1, s[4:7], s31 offen offset:2048 sc1 lds\n"
"buffer_load_dwordx4 v1, s[4:7], s31 offen offset:3072 sc1 lds\n"
"s_add_u32 m0, m0, 1024\n"
"s_add_u32 s31, s31, 1024\n"
"buffer_load_dwordx4 v1, s[4:7], s31 offen offset:3072 sc1 lds\n"
"s_add_u32 m0, s29, 0xa000\n"
"s_add_u32 s31, s44, 0x140000\n"
"buffer_load_dwordx4 v1, s[4:7], s31 offen offset:0 sc1 lds\n"
"buffer_load_dwordx4 v1, s[4:7], s31 offen offset:1024 sc1 lds\n"
"buffer_load_dwordx4 v1, s[4:7], s31 offen offset:2048 sc1 lds\n"
"buffer_load_dwordx4 v1, s[4:7], s31 offen offset:3072 sc1 lds\n"
"s_add_u32 m0, m0, 1024\n"
"s_add_u32 s31, s31, 1024\n"
"buffer_load_dwordx4 v1, s[4:7], s31 offen offset:3072 sc1 lds\n"
"s_waitcnt vmcnt(0)\n"
"ds_write_b32 v13, v13 offset:400\n"
"s_mov_b32 s17, 0x100000\n"
".Ltp_195:\n"
"ds_read_b32 v90, v14 offset:400\n"
"s_sub_u32 s17, s17, 1\n"
"s_cbranch_scc1 .Lou_196\n"
"s_waitcnt lgkmcnt(0)\n"
"v_cmp_eq_u32 vcc, 0, v90\n"
"s_cbranch_vccnz .Ltp_195\n"
".Lou_196:\n"
"s_waitcnt lgkmcnt(0)\n"
"v_mov_b32 v90, 0\n"
"s_branch .Lbk_194\n"
".Lsl_197:\n"
"s_nop 7\n"
"s_mov_b32 s17, 0x100000\n"
".Ltp_199:\n"
"ds_read_b32 v90, v14 offset:320\n"
"s_sub_u32 s17, s17, 1\n"
"s_cbranch_scc1 .Lou_200\n"
"s_waitcnt lgkmcnt(0)\n"
"v_cmp_eq_u32 vcc, 0, v90\n"
"s_cbranch_vccnz .Ltp_199\n"
".Lou_200:\n"
"s_waitcnt lgkmcnt(0)\n"
"s_branch .Lbk_198\n"
".Lsl_201:\n"
"s_nop 7\n"
"s_mov_b32 s17, 0x100000\n"
".Ltp_203:\n"
"ds_read_b32 v90, v14 offset:416\n"
"s_sub_u32 s17, s17, 1\n"
"s_cbranch_scc1 .Lou_204\n"
"s_waitcnt lgkmcnt(0)\n"
"v_cmp_eq_u32 vcc, 0, v90\n"
"s_cbranch_vccnz .Ltp_203\n"
".Lou_204:\n"
"s_waitcnt lgkmcnt(0)\n"
"s_branch .Lbk_202\n"
".Lsl_205:\n"
"s_nop 7\n"
"s_mov_b32 s17, 0x100000\n"
".Ltp_207:\n"
"ds_read_b32 v90, v14 offset:432\n"
"s_sub_u32 s17, s17, 1\n"
"s_cbranch_scc1 .Lou_208\n"
"s_waitcnt lgkmcnt(0)\n"
"v_cmp_eq_u32 vcc, 0, v90\n"
"s_cbranch_vccnz .Ltp_207\n"
".Lou_208:\n"
"s_waitcnt lgkmcnt(0)\n"
"s_branch .Lbk_206\n"
".Lsl_209:\n"
"s_nop 7\n"
"s_mov_b32 s17, 0x100000\n"
".Ltp_211:\n"
"ds_read_b128 v[36:39], v11 offset:3712\n"
"ds_read_b128 v[40:43], v11 offset:4992\n"
"s_sub_u32 s17, s17, 1\n"
"s_cbranch_scc1 .Lou_212\n"
"s_waitcnt lgkmcnt(0)\n"
"v_max3_u32 v88, v36, v37, v43\n"
"v_max3_u32 v88, v38, v39, v88\n"
"v_max3_u32 v88, v40, v41, v88\n"
"v_max3_u32 v88, v42, v43, v88\n"
"v_cmp_eq_u32 vcc, -1, v88\n"
"s_cbranch_vccnz .Ltp_211\n"
".Lou_212:\n"
"s_waitcnt lgkmcnt(0)\n"
"v_add_f32 v36, v36, v37\n"
"v_add_f32 v38, v38, v39\n"
"v_add_f32 v40, v40, v41\n"
"v_add_f32 v42, v42, v43\n"
"v_add_f32 v40, v40, v42\n"
"v_add_f32 v36, v36, v38\n"
"v_mul_f32 v89, 0x3acccccd, v40\n"
"v_fma_f32 v36, -v40, v89, v36\n"
"v_fmamk_f32 v36, v36, 0x3acccccd, v27\n"
"v_add_f32 v92, v92, v36\n"
"v_max_f32 v36, v36, v27\n"
"v_rsq_f32 v88, v36\n"
"s_nop 0\n"
"v_mul_f32 v89, -v89, v88\n"
"s_nop 1\n"
"s_branch .Lbk_210\n"
".Lsl_213:\n"
"s_nop 7\n"
"s_mov_b32 s17, 0x100000\n"
".Ltp_215:\n"
"ds_read_b128 v[84:87], v11 offset:4032\n"
"ds_read_b32 v91, v12 offset:4032\n"
"s_sub_u32 s17, s17, 1\n"
"s_cbranch_scc1 .Lou_216\n"
"s_waitcnt lgkmcnt(0)\n"
"v_max3_u32 v88, v84, v85, v91\n"
"v_max3_u32 v88, v86, v87, v88\n"
"v_cmp_eq_u32 vcc, -1, v88\n"
"s_cbranch_vccnz .Ltp_215\n"
".Lou_216:\n"
"s_waitcnt lgkmcnt(0)\n"
"v_add_f32 v84, v84, v85\n"
"v_add_f32 v86, v86, v87\n"
"v_add_f32 v84, v84, v86\n"
"v_mul_f32 v89, 0x3acccccd, v91\n"
"v_fma_f32 v84, -v91, v89, v84\n"
"v_fmamk_f32 v84, v84, 0x3acccccd, v27\n"
"v_add_f32 v92, v92, v84\n"
"v_max_f32 v84, v84, v27\n"
"v_rsq_f32 v88, v84\n"
"s_nop 0\n"
"v_mul_f32 v89, -v89, v88\n"
"s_nop 1\n"
"s_branch .Lbk_214\n"
".Lsl_217:\n"
"s_nop 7\n"
"s_mov_b32 s17, 0x100000\n"
".Ltp_219:\n"
"ds_read_b128 v[84:87], v11 offset:4352\n"
"ds_read_b32 v91, v12 offset:4352\n"
"s_sub_u32 s17, s17, 1\n"
"s_cbranch_scc1 .Lou_220\n"
"s_waitcnt lgkmcnt(0)\n"
"v_max3_u32 v88, v84, v85, v91\n"
"v_max3_u32 v88, v86, v87, v88\n"
"v_cmp_eq_u32 vcc, -1, v88\n"
"s_cbranch_vccnz .Ltp_219\n"
".Lou_220:\n"
"s_waitcnt lgkmcnt(0)\n"
"v_add_f32 v84, v84, v85\n"
"v_add_f32 v86, v86, v87\n"
"v_add_f32 v84, v84, v86\n"
"v_mul_f32 v89, 0x3acccccd, v91\n"
"v_fma_f32 v84, -v91, v89, v84\n"
"v_fmamk_f32 v84, v84, 0x3acccccd, v27\n"
"v_add_f32 v92, v92, v84\n"
"v_max_f32 v84, v84, v27\n"
"v_rsq_f32 v88, v84\n"
"s_nop 0\n"
"v_mul_f32 v89, -v89, v88\n"
"s_nop 1\n"
"s_branch .Lbk_218\n"
".Lsl_221:\n"
"s_nop 7\n"
"s_mov_b32 s17, 0x100000\n"
".Ltp_223:\n"
"ds_read_b128 v[84:87], v11 offset:4672\n"
"ds_read_b128 v[76:79], v11 offset:5312\n"
"s_sub_u32 s17, s17, 1\n"
"s_cbranch_scc1 .Lou_224\n"
"s_waitcnt lgkmcnt(0)\n"
"v_max3_u32 v52, v84, v85, v79\n"
"v_max3_u32 v52, v86, v87, v52\n"
"v_max3_u32 v52, v76, v77, v52\n"
"v_max3_u32 v52, v78, v79, v52\n"
"v_cmp_eq_u32 vcc, -1, v52\n"
"s_cbranch_vccnz .Ltp_223\n"
".Lou_224:\n"
"s_waitcnt lgkmcnt(0)\n"
"v_add_f32 v84, v84, v85\n"
"v_add_f32 v86, v86, v87\n"
"v_add_f32 v76, v76, v77\n"
"v_add_f32 v78, v78, v79\n"
"v_add_f32 v76, v76, v78\n"
"v_add_f32 v84, v84, v86\n"
"v_mul_f32 v53, 0x3acccccd, v76\n"
"v_fma_f32 v84, -v76, v53, v84\n"
"v_fmamk_f32 v84, v84, 0x3acccccd, v27\n"
"v_add_f32 v92, v92, v84\n"
"v_max_f32 v84, v84, v27\n"
"v_rsq_f32 v52, v84\n"
"s_nop 0\n"
"v_mul_f32 v53, -v53, v52\n"
"s_nop 1\n"
"s_branch .Lbk_222\n"
".Lsl_225:\n"
"s_nop 7\n"
"s_mov_b32 s17, 32768\n"
".Ltp_227:\n"
"buffer_load_dwordx4 v[40:43], v1, s[4:7], s25 offen offset:1024 sc1\n"
"buffer_load_dwordx4 v[36:39], v93, s[4:7], s25 offen offset:0 sc1\n"
"s_waitcnt vmcnt(0)\n"
"s_sub_u32 s17, s17, 1\n"
"s_cbranch_scc1 .Lbk_226\n"
"v_bitop3_b32 v84, v37, v39, s16 bitop3:0x7e\n"
"v_bitop3_b32 v85, v41, v43, s16 bitop3:0x7e\n"
"v_and_b32 v84, s26, v84\n"
"v_and_or_b32 v84, v85, s27, v84\n"
"v_cmp_ne_u32 vcc, 0, v84\n"
"s_cbranch_vccnz .Ltp_227\n"
"s_branch .Lbk_226\n"
".Lsl_228:\n"
"s_nop 7\n"
"s_mov_b32 s17, 0x100000\n"
".Ltp_230:\n"
"ds_read_b32 v90, v14 offset:368\n"
"s_sub_u32 s17, s17, 1\n"
"s_cbranch_scc1 .Lou_231\n"
"s_waitcnt lgkmcnt(0)\n"
"v_cmp_eq_u32 vcc, 0, v90\n"
"s_cbranch_vccnz .Ltp_230\n"
".Lou_231:\n"
"ds_read_b64 v[56:57], v3 offset:0\n"
"ds_read_b64 v[58:59], v3 offset:256\n"
"ds_read_b64 v[60:61], v3 offset:4096\n"
"ds_read_b64 v[62:63], v3 offset:4352\n"
"ds_read_b64 v[64:65], v3 offset:8192\n"
"ds_read_b64 v[66:67], v3 offset:8448\n"
"ds_read_b64 v[68:69], v3 offset:12288\n"
"ds_read_b64 v[70:71], v3 offset:12544\n"
"s_waitcnt lgkmcnt(0)\n"
"s_branch .Lbk_229\n"
".Lrd_192:\n"
"s_nop 7\n"
"s_cmp_eq_u32 s54, 0\n"
"s_cbranch_scc1 .Lfi_232\n"
"s_branch .Lgo_191\n"
".Lfi_232:\n"
"s_mov_b32 s54, 1\n"
"s_add_u32 s46, s44, 0x140000\n"
"s_add_u32 s47, s46, 0x1000\n"
"buffer_load_dwordx4 v[28:31], v1, s[4:7], s46 offen offset:0 sc1\n"
"buffer_load_dwordx4 v[32:35], v1, s[4:7], s46 offen offset:1024 sc1\n"
"buffer_load_dwordx4 v[36:39], v1, s[4:7], s46 offen offset:2048 sc1\n"
"buffer_load_dwordx4 v[40:43], v1, s[4:7], s46 offen offset:3072 sc1\n"
"buffer_load_dwordx4 v[44:47], v1, s[4:7], s47 offen offset:0 sc1\n"
"s_mov_b32 s17, 32768\n"
".Ltp_233:\n"
"s_waitcnt vmcnt(0)\n"
"s_sub_u32 s17, s17, 1\n"
"s_cbranch_scc1 .Lou_234\n"
"v_max_u32 v48, v28, v29\n"
"v_max3_u32 v48, v30, v31, v48\n"
"v_max3_u32 v48, v32, v33, v48\n"
"v_max3_u32 v48, v34, v35, v48\n"
"v_max3_u32 v48, v36, v37, v48\n"
"v_max3_u32 v48, v38, v39, v48\n"
"v_max3_u32 v48, v40, v41, v48\n"
"v_max3_u32 v48, v42, v43, v48\n"
"v_max3_u32 v48, v44, v45, v48\n"
"v_max3_u32 v48, v46, v47, v48\n"
"v_cmp_eq_u32 vcc, -1, v48\n"
"s_cbranch_vccz .Lou_234\n"
"v_max_u32 v48, v30, v31\n"
"v_max3_u32 v48, v28, v29, v48\n"
"v_cmp_eq_u32 vcc, -1, v48\n"
"s_cbranch_vccz .Lsk_235\n"
"s_mov_b64 exec, vcc\n"
"buffer_load_dwordx4 v[28:31], v1, s[4:7], s46 offen offset:0 sc1\n"
"s_mov_b64 exec, -1\n"
".Lsk_235:\n"
"v_max_u32 v48, v34, v35\n"
"v_max3_u32 v48, v32, v33, v48\n"
"v_cmp_eq_u32 vcc, -1, v48\n"
"s_cbranch_vccz .Lsk_236\n"
"s_mov_b64 exec, vcc\n"
"buffer_load_dwordx4 v[32:35], v1, s[4:7], s46 offen offset:1024 sc1\n"
"s_mov_b64 exec, -1\n"
".Lsk_236:\n"
"v_max_u32 v48, v38, v39\n"
"v_max3_u32 v48, v36, v37, v48\n"
"v_cmp_eq_u32 vcc, -1, v48\n"
"s_cbranch_vccz .Lsk_237\n"
"s_mov_b64 exec, vcc\n"
"buffer_load_dwordx4 v[36:39], v1, s[4:7], s46 offen offset:2048 sc1\n"
"s_mov_b64 exec, -1\n"
".Lsk_237:\n"
"v_max_u32 v48, v42, v43\n"
"v_max3_u32 v48, v40, v41, v48\n"
"v_cmp_eq_u32 vcc, -1, v48\n"
"s_cbranch_vccz .Lsk_238\n"
"s_mov_b64 exec, vcc\n"
"buffer_load_dwordx4 v[40:43], v1, s[4:7], s46 offen offset:3072 sc1\n"
"s_mov_b64 exec, -1\n"
".Lsk_238:\n"
"v_max_u32 v48, v46, v47\n"
"v_max3_u32 v48, v44, v45, v48\n"
"v_cmp_eq_u32 vcc, -1, v48\n"
"s_cbranch_vccz .Lsk_239\n"
"s_mov_b64 exec, vcc\n"
"buffer_load_dwordx4 v[44:47], v1, s[4:7], s47 offen offset:0 sc1\n"
"s_mov_b64 exec, -1\n"
".Lsk_239:\n"
"s_branch .Ltp_233\n"
".Lou_234:\n"
"s_waitcnt vmcnt(0)\n"
"s_add_u32 s46, s44, 0x145000\n"
"s_add_u32 s47, s46, 0x1000\n"
"buffer_load_dwordx4 v[28:31], v1, s[4:7], s46 offen offset:0 sc1\n"
"buffer_load_dwordx4 v[32:35], v1, s[4:7], s46 offen offset:1024 sc1\n"
"buffer_load_dwordx4 v[36:39], v1, s[4:7], s46 offen offset:2048 sc1\n"
"buffer_load_dwordx4 v[40:43], v1, s[4:7], s46 offen offset:3072 sc1\n"
"buffer_load_dwordx4 v[44:47], v1, s[4:7], s47 offen offset:0 sc1\n"
"s_mov_b32 s17, 32768\n"
".Ltp_240:\n"
"s_waitcnt vmcnt(0)\n"
"s_sub_u32 s17, s17, 1\n"
"s_cbranch_scc1 .Lou_241\n"
"v_max_u32 v48, v28, v29\n"
"v_max3_u32 v48, v30, v31, v48\n"
"v_max3_u32 v48, v32, v33, v48\n"
"v_max3_u32 v48, v34, v35, v48\n"
"v_max3_u32 v48, v36, v37, v48\n"
"v_max3_u32 v48, v38, v39, v48\n"
"v_max3_u32 v48, v40, v41, v48\n"
"v_max3_u32 v48, v42, v43, v48\n"
"v_max3_u32 v48, v44, v45, v48\n"
"v_max3_u32 v48, v46, v47, v48\n"
"v_cmp_eq_u32 vcc, -1, v48\n"
"s_cbranch_vccz .Lou_241\n"
"v_max_u32 v48, v30, v31\n"
"v_max3_u32 v48, v28, v29, v48\n"
"v_cmp_eq_u32 vcc, -1, v48\n"
"s_cbranch_vccz .Lsk_242\n"
"s_mov_b64 exec, vcc\n"
"buffer_load_dwordx4 v[28:31], v1, s[4:7], s46 offen offset:0 sc1\n"
"s_mov_b64 exec, -1\n"
".Lsk_242:\n"
"v_max_u32 v48, v34, v35\n"
"v_max3_u32 v48, v32, v33, v48\n"
"v_cmp_eq_u32 vcc, -1, v48\n"
"s_cbranch_vccz .Lsk_243\n"
"s_mov_b64 exec, vcc\n"
"buffer_load_dwordx4 v[32:35], v1, s[4:7], s46 offen offset:1024 sc1\n"
"s_mov_b64 exec, -1\n"
".Lsk_243:\n"
"v_max_u32 v48, v38, v39\n"
"v_max3_u32 v48, v36, v37, v48\n"
"v_cmp_eq_u32 vcc, -1, v48\n"
"s_cbranch_vccz .Lsk_244\n"
"s_mov_b64 exec, vcc\n"
"buffer_load_dwordx4 v[36:39], v1, s[4:7], s46 offen offset:2048 sc1\n"
"s_mov_b64 exec, -1\n"
".Lsk_244:\n"
"v_max_u32 v48, v42, v43\n"
"v_max3_u32 v48, v40, v41, v48\n"
"v_cmp_eq_u32 vcc, -1, v48\n"
"s_cbranch_vccz .Lsk_245\n"
"s_mov_b64 exec, vcc\n"
"buffer_load_dwordx4 v[40:43], v1, s[4:7], s46 offen offset:3072 sc1\n"
"s_mov_b64 exec, -1\n"
".Lsk_245:\n"
"v_max_u32 v48, v46, v47\n"
"v_max3_u32 v48, v44, v45, v48\n"
"v_cmp_eq_u32 vcc, -1, v48\n"
"s_cbranch_vccz .Lsk_246\n"
"s_mov_b64 exec, vcc\n"
"buffer_load_dwordx4 v[44:47], v1, s[4:7], s47 offen offset:0 sc1\n"
"s_mov_b64 exec, -1\n"
".Lsk_246:\n"
"s_branch .Ltp_240\n"
".Lou_241:\n"
"s_waitcnt vmcnt(0)\n"
"s_add_u32 s46, s44, 0x14a000\n"
"s_add_u32 s47, s46, 0x1000\n"
"buffer_load_dwordx4 v[28:31], v1, s[4:7], s46 offen offset:0 sc1\n"
"buffer_load_dwordx4 v[32:35], v1, s[4:7], s46 offen offset:1024 sc1\n"
"buffer_load_dwordx4 v[36:39], v1, s[4:7], s46 offen offset:2048 sc1\n"
"buffer_load_dwordx4 v[40:43], v1, s[4:7], s46 offen offset:3072 sc1\n"
"buffer_load_dwordx4 v[44:47], v1, s[4:7], s47 offen offset:0 sc1\n"
"s_mov_b32 s17, 32768\n"
".Ltp_247:\n"
"s_waitcnt vmcnt(0)\n"
"s_sub_u32 s17, s17, 1\n"
"s_cbranch_scc1 .Lou_248\n"
"v_max_u32 v48, v28, v29\n"
"v_max3_u32 v48, v30, v31, v48\n"
"v_max3_u32 v48, v32, v33, v48\n"
"v_max3_u32 v48, v34, v35, v48\n"
"v_max3_u32 v48, v36, v37, v48\n"
"v_max3_u32 v48, v38, v39, v48\n"
"v_max3_u32 v48, v40, v41, v48\n"
"v_max3_u32 v48, v42, v43, v48\n"
"v_max3_u32 v48, v44, v45, v48\n"
"v_max3_u32 v48, v46, v47, v48\n"
"v_cmp_eq_u32 vcc, -1, v48\n"
"s_cbranch_vccz .Lou_248\n"
"v_max_u32 v48, v30, v31\n"
"v_max3_u32 v48, v28, v29, v48\n"
"v_cmp_eq_u32 vcc, -1, v48\n"
"s_cbranch_vccz .Lsk_249\n"
"s_mov_b64 exec, vcc\n"
"buffer_load_dwordx4 v[28:31], v1, s[4:7], s46 offen offset:0 sc1\n"
"s_mov_b64 exec, -1\n"
".Lsk_249:\n"
"v_max_u32 v48, v34, v35\n"
"v_max3_u32 v48, v32, v33, v48\n"
"v_cmp_eq_u32 vcc, -1, v48\n"
"s_cbranch_vccz .Lsk_250\n"
"s_mov_b64 exec, vcc\n"
"buffer_load_dwordx4 v[32:35], v1, s[4:7], s46 offen offset:1024 sc1\n"
"s_mov_b64 exec, -1\n"
".Lsk_250:\n"
"v_max_u32 v48, v38, v39\n"
"v_max3_u32 v48, v36, v37, v48\n"
"v_cmp_eq_u32 vcc, -1, v48\n"
"s_cbranch_vccz .Lsk_251\n"
"s_mov_b64 exec, vcc\n"
"buffer_load_dwordx4 v[36:39], v1, s[4:7], s46 offen offset:2048 sc1\n"
"s_mov_b64 exec, -1\n"
".Lsk_251:\n"
"v_max_u32 v48, v42, v43\n"
"v_max3_u32 v48, v40, v41, v48\n"
"v_cmp_eq_u32 vcc, -1, v48\n"
"s_cbranch_vccz .Lsk_252\n"
"s_mov_b64 exec, vcc\n"
"buffer_load_dwordx4 v[40:43], v1, s[4:7], s46 offen offset:3072 sc1\n"
"s_mov_b64 exec, -1\n"
".Lsk_252:\n"
"v_max_u32 v48, v46, v47\n"
"v_max3_u32 v48, v44, v45, v48\n"
"v_cmp_eq_u32 vcc, -1, v48\n"
"s_cbranch_vccz .Lsk_253\n"
"s_mov_b64 exec, vcc\n"
"buffer_load_dwordx4 v[44:47], v1, s[4:7], s47 offen offset:0 sc1\n"
"s_mov_b64 exec, -1\n"
".Lsk_253:\n"
"s_branch .Ltp_247\n"
".Lou_248:\n"
"s_waitcnt vmcnt(0)\n"
"s_add_u32 s46, s44, 0x14f000\n"
"s_add_u32 s47, s46, 0x1000\n"
"buffer_load_dwordx4 v[28:31], v1, s[4:7], s46 offen offset:0 sc1\n"
"buffer_load_dwordx4 v[32:35], v1, s[4:7], s46 offen offset:1024 sc1\n"
"buffer_load_dwordx4 v[36:39], v1, s[4:7], s46 offen offset:2048 sc1\n"
"buffer_load_dwordx4 v[40:43], v1, s[4:7], s46 offen offset:3072 sc1\n"
"buffer_load_dwordx4 v[44:47], v1, s[4:7], s47 offen offset:0 sc1\n"
"s_mov_b32 s17, 32768\n"
".Ltp_254:\n"
"s_waitcnt vmcnt(0)\n"
"s_sub_u32 s17, s17, 1\n"
"s_cbranch_scc1 .Lou_255\n"
"v_max_u32 v48, v28, v29\n"
"v_max3_u32 v48, v30, v31, v48\n"
"v_max3_u32 v48, v32, v33, v48\n"
"v_max3_u32 v48, v34, v35, v48\n"
"v_max3_u32 v48, v36, v37, v48\n"
"v_max3_u32 v48, v38, v39, v48\n"
"v_max3_u32 v48, v40, v41, v48\n"
"v_max3_u32 v48, v42, v43, v48\n"
"v_max3_u32 v48, v44, v45, v48\n"
"v_max3_u32 v48, v46, v47, v48\n"
"v_cmp_eq_u32 vcc, -1, v48\n"
"s_cbranch_vccz .Lou_255\n"
"v_max_u32 v48, v30, v31\n"
"v_max3_u32 v48, v28, v29, v48\n"
"v_cmp_eq_u32 vcc, -1, v48\n"
"s_cbranch_vccz .Lsk_256\n"
"s_mov_b64 exec, vcc\n"
"buffer_load_dwordx4 v[28:31], v1, s[4:7], s46 offen offset:0 sc1\n"
"s_mov_b64 exec, -1\n"
".Lsk_256:\n"
"v_max_u32 v48, v34, v35\n"
"v_max3_u32 v48, v32, v33, v48\n"
"v_cmp_eq_u32 vcc, -1, v48\n"
"s_cbranch_vccz .Lsk_257\n"
"s_mov_b64 exec, vcc\n"
"buffer_load_dwordx4 v[32:35], v1, s[4:7], s46 offen offset:1024 sc1\n"
"s_mov_b64 exec, -1\n"
".Lsk_257:\n"
"v_max_u32 v48, v38, v39\n"
"v_max3_u32 v48, v36, v37, v48\n"
"v_cmp_eq_u32 vcc, -1, v48\n"
"s_cbranch_vccz .Lsk_258\n"
"s_mov_b64 exec, vcc\n"
"buffer_load_dwordx4 v[36:39], v1, s[4:7], s46 offen offset:2048 sc1\n"
"s_mov_b64 exec, -1\n"
".Lsk_258:\n"
"v_max_u32 v48, v42, v43\n"
"v_max3_u32 v48, v40, v41, v48\n"
"v_cmp_eq_u32 vcc, -1, v48\n"
"s_cbranch_vccz .Lsk_259\n"
"s_mov_b64 exec, vcc\n"
"buffer_load_dwordx4 v[40:43], v1, s[4:7], s46 offen offset:3072 sc1\n"
"s_mov_b64 exec, -1\n"
".Lsk_259:\n"
"v_max_u32 v48, v46, v47\n"
"v_max3_u32 v48, v44, v45, v48\n"
"v_cmp_eq_u32 vcc, -1, v48\n"
"s_cbranch_vccz .Lsk_260\n"
"s_mov_b64 exec, vcc\n"
"buffer_load_dwordx4 v[44:47], v1, s[4:7], s47 offen offset:0 sc1\n"
"s_mov_b64 exec, -1\n"
".Lsk_260:\n"
"s_branch .Ltp_254\n"
".Lou_255:\n"
"s_waitcnt vmcnt(0)\n"
"ds_write_b32 v13, v13 offset:464\n"
"s_mov_b32 s17, 0x100000\n"
".Ltp_261:\n"
"ds_read_b32 v90, v14 offset:464\n"
"s_sub_u32 s17, s17, 1\n"
"s_cbranch_scc1 .Lou_262\n"
"s_waitcnt lgkmcnt(0)\n"
"v_cmp_eq_u32 vcc, 0, v90\n"
"s_cbranch_vccnz .Ltp_261\n"
".Lou_262:\n"
"s_waitcnt lgkmcnt(0)\n"
"v_mov_b32 v90, 0\n"
"v_mov_b32 v90, -1\n"
"ds_write_b32 v9, v90 offset:3712\n"
"ds_write_b32 v9, v90 offset:4032\n"
"ds_write_b32 v9, v90 offset:4352\n"
"ds_write_b32 v9, v90 offset:4672\n"
"ds_write_b32 v9, v90 offset:4992\n"
"ds_write_b32 v10, v90 offset:5312\n"
"s_waitcnt vmcnt(0)\n"
"s_add_u32 m0, s29, 0\n"
"s_add_u32 s31, s44, 0x14f000\n"
"buffer_load_dwordx4 v1, s[4:7], s31 offen offset:0 sc1 lds\n"
"buffer_load_dwordx4 v1, s[4:7], s31 offen offset:1024 sc1 lds\n"
"buffer_load_dwordx4 v1, s[4:7], s31 offen offset:2048 sc1 lds\n"
"buffer_load_dwordx4 v1, s[4:7], s31 offen offset:3072 sc1 lds\n"
"s_add_u32 m0, m0, 1024\n"
"s_add_u32 s31, s31, 1024\n"
"buffer_load_dwordx4 v1, s[4:7], s31 offen offset:3072 sc1 lds\n"
"s_add_u32 m0, s29, 0xa000\n"
"s_add_u32 s31, s44, 0x140000\n"
"buffer_load_dwordx4 v1, s[4:7], s31 offen offset:0 sc1 lds\n"
"buffer_load_dwordx4 v1, s[4:7], s31 offen offset:1024 sc1 lds\n"
"buffer_load_dwordx4 v1, s[4:7], s31 offen offset:2048 sc1 lds\n"
"buffer_load_dwordx4 v1, s[4:7], s31 offen offset:3072 sc1 lds\n"
"s_add_u32 m0, m0, 1024\n"
"s_add_u32 s31, s31, 1024\n"
"buffer_load_dwordx4 v1, s[4:7], s31 offen offset:3072 sc1 lds\n"
"s_add_u32 m0, s29, 0xf000\n"
"s_add_u32 s31, s44, 0x145000\n"
"buffer_load_dwordx4 v1, s[4:7], s31 offen offset:0 sc1 lds\n"
"buffer_load_dwordx4 v1, s[4:7], s31 offen offset:1024 sc1 lds\n"
"buffer_load_dwordx4 v1, s[4:7], s31 offen offset:2048 sc1 lds\n"
"buffer_load_dwordx4 v1, s[4:7], s31 offen offset:3072 sc1 lds\n"
"s_add_u32 m0, m0, 1024\n"
"s_add_u32 s31, s31, 1024\n"
"buffer_load_dwordx4 v1, s[4:7], s31 offen offset:3072 sc1 lds\n"
"s_add_u32 m0, s29, 0x5000\n"
"s_add_u32 s31, s44, 0x14a000\n"
"buffer_load_dwordx4 v1, s[4:7], s31 offen offset:0 sc1 lds\n"
"buffer_load_dwordx4 v1, s[4:7], s31 offen offset:1024 sc1 lds\n"
"buffer_load_dwordx4 v1, s[4:7], s31 offen offset:2048 sc1 lds\n"
"buffer_load_dwordx4 v1, s[4:7], s31 offen offset:3072 sc1 lds\n"
"s_add_u32 m0, m0, 1024\n"
"s_add_u32 s31, s31, 1024\n"
"buffer_load_dwordx4 v1, s[4:7], s31 offen offset:3072 sc1 lds\n"
"s_waitcnt vmcnt(0)\n"
"ds_write_b32 v13, v13 offset:480\n"
"s_mov_b32 s17, 0x100000\n"
".Ltp_263:\n"
"ds_read_b32 v90, v14 offset:480\n"
"s_sub_u32 s17, s17, 1\n"
"s_cbranch_scc1 .Lou_264\n"
"s_waitcnt lgkmcnt(0)\n"
"v_cmp_eq_u32 vcc, 0, v90\n"
"s_cbranch_vccnz .Ltp_263\n"
".Lou_264:\n"
"s_waitcnt lgkmcnt(0)\n"
"v_mov_b32 v90, 0\n"
"s_branch .Lag_190\n"
".Lsl_284:\n"
"s_nop 7\n"
"s_mov_b32 s17, 0x100000\n"
".Ltp_286:\n"
"ds_read_b32 v42, v14 offset:544\n"
"s_sub_u32 s17, s17, 1\n"
"s_cbranch_scc1 .Lou_287\n"
"s_waitcnt lgkmcnt(0)\n"
"v_cmp_eq_u32 vcc, 0, v42\n"
"s_cbranch_vccnz .Ltp_286\n"
".Lou_287:\n"
"ds_read_b64_tr_b16 v[224:225], v31 offset:0\n"
"ds_read_b64_tr_b16 v[226:227], v32 offset:0\n"
"s_waitcnt lgkmcnt(0)\n"
"s_branch .Lbk_285\n"
".L_xend:\n"
"v_and_b32 v0, 63, v0\n"
"s_and_b32 s3, s3, 3\n"
"s_and_b32 s79, s2, 7\n"
"s_lshl_b32 s79, s79, 4\n"
"s_lshr_b32 s78, s2, 3\n"
"s_add_u32 s79, s79, s78\n"
"s_and_b32 s78, s2, 3\n"
"s_lshl_b32 s78, s78, 3\n"
"s_and_b32 s77, s2, 4\n"
"s_cmp_eq_i32 s77, 4\n"
"s_cselect_b32 s78, 32, s78\n"
"s_add_u32 s79, s79, s78\n"
"s_and_b32 s78, s2, 4\n"
"s_lshl_b32 s78, s78, 4\n"
"s_add_u32 s78, s2, s78\n"
"s_cmp_lt_i32 s78, 0xc0\n"
"s_cselect_b32 s18, s79, -1\n"
"s_and_b32 s79, s2, 7\n"
"s_lshl_b32 s79, s79, 4\n"
"s_lshr_b32 s78, s2, 3\n"
"s_add_u32 s79, s79, s78\n"
"s_and_b32 s78, s2, 3\n"
"s_lshl_b32 s78, s78, 3\n"
"s_add_u32 s78, s78, 24\n"
"s_and_b32 s77, s2, 4\n"
"s_cmp_eq_i32 s77, 4\n"
"s_cselect_b32 s78, 48, s78\n"
"s_sub_u32 s79, s79, s78\n"
"s_and_b32 s78, s2, 4\n"
"s_lshl_b32 s78, s78, 4\n"
"s_add_u32 s78, s2, s78\n"
"s_cmp_lt_i32 s78, 0xc0\n"
"s_cselect_b32 s19, 0x3e8, s79\n"
"v_lshlrev_b32 v2, 4, v0\n"
"s_mov_b32 s4, s26\n"
"s_mov_b32 s5, s27\n"
"s_mov_b32 s6, 0x1ec8000\n"
"s_mov_b32 s7, 0x20000\n"
"s_mul_i32 s79, s3, 0x2100\n"
"s_add_u32 s74, s79, 0x14000\n"
"v_lshl_add_u32 v152, v0, 2, s74\n"
"v_add_u32 v153, 0x440, v152\n"
"v_add_u32 v154, 0x880, v152\n"
"v_add_u32 v155, 0xcc0, v152\n"
"v_and_b32 v99, 15, v0\n"
"v_and_b32 v98, 48, v0\n"
"s_mov_b32 s79, 0x110\n"
"v_mad_i32_i24 v99, v99, s79, v98\n"
"v_add_u32 v141, s74, v99\n"
"s_lshl_b32 s79, s3, 2\n"
"s_add_u32 s79, s79, 0x26400\n"
"v_mov_b32 v99, s79\n"
"s_mov_b32 vcc_lo, 1\n"
"s_mov_b32 vcc_hi, 0\n"
"v_cndmask_b32 v4, 2.0, v99, vcc\n"
"v_and_b32 v99, 3, v0\n"
"v_lshlrev_b32 v99, 2, v99\n"
"v_add_u32 v5, 0x26400, v99\n"
"v_min_i32 v99, 49, v0\n"
"v_lshlrev_b32 v38, 5, v99\n"
"v_mov_b32 v128, 0\n"
"v_mov_b32 v129, 0\n"
"v_mov_b32 v130, 0\n"
"v_mov_b32 v131, 0\n"
"v_mov_b32 v132, 0\n"
"v_mov_b32 v133, 0\n"
"v_mov_b32 v134, 0\n"
"v_mov_b32 v135, 0\n"
"v_mov_b32 v136, 0\n"
"v_mov_b32 v137, 0\n"
"v_mov_b32 v138, 0\n"
"v_mov_b32 v139, 0\n"
"s_waitcnt lgkmcnt(0)\n"
"s_and_b32 s79, s3, 1\n"
"s_lshl_b32 s60, s79, 4\n"
"s_lshr_b32 s40, s40, s60\n"
"s_lshr_b32 s41, s41, s60\n"
"v_mov_b32 v142, s40\n"
"v_mov_b32 v140, s41\n"
"s_mul_i32 s16, s16, 0x2c9277b5\n"
"s_add_u32 s16, s16, 0xac564b05\n"
"s_cmp_lt_i32 s18, 0\n"
"s_cbranch_scc1 .Lnk_297\n"
"s_setprio 0\n"
"s_mov_b32 s55, 2\n"
"s_mov_b32 s52, 0xc2f00000\n"
"s_mov_b32 s53, 0x3fb8aa3b\n"
"s_mov_b32 s54, 0x3f317218\n"
"v_mov_b32 v31, 0x42f00000\n"
"v_lshlrev_b32 v142, 16, v142\n"
"v_med3_f32 v142, v142, s52, v31\n"
"v_mul_f32 v28, s53, v142\n"
"v_exp_f32 v29, v28\n"
"v_fma_f32 v28, v142, s53, -v28\n"
"v_fmac_f32 v28, 0x32a57060, v142\n"
"v_fma_f32 v28, v28, s54, 1.0\n"
"v_mul_f32 v142, v29, v28\n"
"v_cvt_pk_bf16_f32 v142, v142, v142\n"
"v_lshlrev_b32 v142, 16, v142\n"
"s_waitcnt vmcnt(8)\n"
"s_nop 3\n"
".Lkg_304:\n"
"v_lshlrev_b32 v32, 16, v32\n"
"v_lshlrev_b32 v33, 16, v33\n"
"v_lshlrev_b32 v34, 16, v34\n"
"v_lshlrev_b32 v35, 16, v35\n"
"v_mul_f32 v26, v142, v34\n"
"v_mul_f32 v27, v142, v35\n"
"v_max3_f32 v30, |v26|, |v27|, 0\n"
"s_nop 1\n"
"v_max_u32_dpp v30, v30, v30 row_ror:8 row_mask:0xf bank_mask:0xf\n"
"v_med3_f32 v32, v32, s52, v31\n"
"s_nop 0\n"
"v_max_u32_dpp v30, v30, v30 row_ror:4 row_mask:0xf bank_mask:0xf\n"
"v_mul_f32 v28, s53, v32\n"
"s_nop 0\n"
"v_max_u32_dpp v30, v30, v30 row_ror:2 row_mask:0xf bank_mask:0xf\n"
"v_exp_f32 v29, v28\n"
"s_nop 0\n"
"v_max_u32_dpp v30, v30, v30 row_ror:1 row_mask:0xf bank_mask:0xf\n"
"v_fma_f32 v28, v32, s53, -v28\n"
"v_fmac_f32 v28, 0x32a57060, v32\n"
"v_mov_b32 v34, v30\n"
"v_fma_f32 v28, v28, s54, 1.0\n"
"s_nop 0\n"
"v_permlane16_swap_b32 v30, v34\n"
"v_mul_f32 v32, v29, v28\n"
"v_max_u32 v30, v30, v34\n"
"v_cvt_pk_bf16_f32 v32, v32, v32\n"
"v_lshlrev_b32 v32, 16, v32\n"
"v_mov_b32 v34, v30\n"
"v_mul_f32 v25, v142, v33\n"
"s_nop 0\n"
"v_permlane32_swap_b32 v30, v34\n"
"v_mul_f32 v24, v142, -v32\n"
"v_max_u32 v30, v30, v34\n"
"v_mul_f32 v25, 0.15915494, v25\n"
"v_mul_f32 v24, s53, v24\n"
"v_readfirstlane_b32 s51, v30\n"
"s_nop 2\n"
"s_bfe_u32 s50, s51, 0x80017\n"
"s_max_i32 s50, s50, 37\n"
"s_min_i32 s50, s50, 217\n"
"s_cmp_gt_i32 s51, 0\n"
"s_cselect_b32 s50, s50, 127\n"
"s_sub_u32 s51, 267, s50\n"
"s_lshl_b32 s51, s51, 23\n"
"s_sub_u32 s50, s50, 27\n"
"s_lshl_b32 s50, s50, 23\n"
"v_mul_f32 v26, s51, v26\n"
"v_mul_f32 v27, s51, v27\n"
"v_mul_f32 v111, 0x41800000, v25\n"
"v_mul_f32 v100, 0x41800000, v24\n"
"v_fract_f32 v111, v111\n"
"v_exp_f32 v100, v100\n"
"v_cos_f32 v110, v111\n"
"v_sin_f32 v111, -v111\n"
"v_mul_f32 v110, v110, v100\n"
"v_mul_f32 v111, v111, v100\n"
"v_fract_f32 v113, v25\n"
"v_exp_f32 v100, v24\n"
"v_cos_f32 v112, v113\n"
"v_sin_f32 v113, v113\n"
"v_mul_f32 v112, v112, v100\n"
"v_mul_f32 v113, v113, v100\n"
"v_mul_f32 v115, 0x43800000, v25\n"
"v_mul_f32 v100, 0x43800000, v24\n"
"v_fract_f32 v115, v115\n"
"v_exp_f32 v100, v100\n"
"v_cos_f32 v114, v115\n"
"v_sin_f32 v115, v115\n"
"v_mul_f32 v114, v114, v100\n"
"v_mul_f32 v115, v115, v100\n"
"v_mul_f32 v116, v111, v111\n"
"v_add_f32 v117, v110, v110\n"
"v_fma_f32 v116, v110, v110, -v116\n"
"v_mul_f32 v117, v117, v111\n"
"v_mul_f32 v120, v117, v117\n"
"v_add_f32 v121, v116, v116\n"
"v_fma_f32 v120, v116, v116, -v120\n"
"v_mul_f32 v121, v121, v117\n"
"v_mul_f32 v118, v117, v111\n"
"v_mul_f32 v119, v116, v111\n"
"v_fma_f32 v118, v116, v110, -v118\n"
"v_fmac_f32 v119, v117, v110\n"
"v_mul_f32 v122, v121, v121\n"
"v_add_f32 v123, v120, v120\n"
"v_fma_f32 v122, v120, v120, -v122\n"
"v_mul_f32 v123, v123, v121\n"
"v_mov_b32 v84, 0x7400\n"
"v_mul_f32 v124, 0x46800000, v110\n"
"v_mul_f32 v125, 0x46800000, v111\n"
"v_cvt_pk_f16_f32 v100, v124, v125\n"
"v_mul_f32 v124, 0x46800000, v116\n"
"v_mul_f32 v125, 0x46800000, v117\n"
"v_cvt_pk_f16_f32 v85, v124, v125\n"
"v_mul_f32 v124, 0x46800000, v118\n"
"v_mul_f32 v125, 0x46800000, v119\n"
"v_cvt_pk_f16_f32 v101, v124, v125\n"
"v_cvt_pk_f16_f32 v93, v120, v121\n"
"v_cvt_pk_f16_f32 v94, v122, v123\n"
"v_mul_f32 v124, v123, v121\n"
"v_mul_f32 v125, v122, v121\n"
"v_fma_f32 v124, v122, v120, -v124\n"
"v_fmac_f32 v125, v123, v120\n"
"v_cvt_pk_f16_f32 v95, v124, v125\n"
"ds_write2_b32 v152, v84, v100 offset0:0 offset1:68\n"
"v_pk_mul_f16 v86, v84, v93 op_sel:[0,0] op_sel_hi:[1,0]\n"
"v_pk_mul_f16 v102, v100, v93 op_sel:[0,0] op_sel_hi:[1,0]\n"
"v_pk_fma_f16 v86, v84, v93, v86 op_sel:[1,1,0] op_sel_hi:[0,1,1] neg_lo:[1,0,0]\n"
"v_pk_fma_f16 v102, v100, v93, v102 op_sel:[1,1,0] op_sel_hi:[0,1,1] neg_lo:[1,0,0]\n"
"ds_write2_b32 v152, v85, v101 offset0:136 offset1:204\n"
"v_pk_mul_f16 v87, v85, v93 op_sel:[0,0] op_sel_hi:[1,0]\n"
"v_pk_mul_f16 v103, v101, v93 op_sel:[0,0] op_sel_hi:[1,0]\n"
"v_pk_fma_f16 v87, v85, v93, v87 op_sel:[1,1,0] op_sel_hi:[0,1,1] neg_lo:[1,0,0]\n"
"v_pk_fma_f16 v103, v101, v93, v103 op_sel:[1,1,0] op_sel_hi:[0,1,1] neg_lo:[1,0,0]\n"
"ds_write2_b32 v153, v86, v102 offset0:0 offset1:68\n"
"v_pk_mul_f16 v88, v84, v94 op_sel:[0,0] op_sel_hi:[1,0]\n"
"v_pk_mul_f16 v102, v100, v94 op_sel:[0,0] op_sel_hi:[1,0]\n"
"v_pk_fma_f16 v88, v84, v94, v88 op_sel:[1,1,0] op_sel_hi:[0,1,1] neg_lo:[1,0,0]\n"
"v_pk_fma_f16 v102, v100, v94, v102 op_sel:[1,1,0] op_sel_hi:[0,1,1] neg_lo:[1,0,0]\n"
"ds_write2_b32 v153, v87, v103 offset0:136 offset1:204\n"
"v_pk_mul_f16 v89, v85, v94 op_sel:[0,0] op_sel_hi:[1,0]\n"
"v_pk_mul_f16 v103, v101, v94 op_sel:[0,0] op_sel_hi:[1,0]\n"
"v_pk_fma_f16 v89, v85, v94, v89 op_sel:[1,1,0] op_sel_hi:[0,1,1] neg_lo:[1,0,0]\n"
"v_pk_fma_f16 v103, v101, v94, v103 op_sel:[1,1,0] op_sel_hi:[0,1,1] neg_lo:[1,0,0]\n"
"ds_write2_b32 v154, v88, v102 offset0:0 offset1:68\n"
"v_pk_mul_f16 v90, v84, v95 op_sel:[0,0] op_sel_hi:[1,0]\n"
"v_pk_mul_f16 v102, v100, v95 op_sel:[0,0] op_sel_hi:[1,0]\n"
"v_pk_fma_f16 v90, v84, v95, v90 op_sel:[1,1,0] op_sel_hi:[0,1,1] neg_lo:[1,0,0]\n"
"v_pk_fma_f16 v102, v100, v95, v102 op_sel:[1,1,0] op_sel_hi:[0,1,1] neg_lo:[1,0,0]\n"
"ds_write2_b32 v154, v89, v103 offset0:136 offset1:204\n"
"v_pk_mul_f16 v91, v85, v95 op_sel:[0,0] op_sel_hi:[1,0]\n"
"v_pk_mul_f16 v103, v101, v95 op_sel:[0,0] op_sel_hi:[1,0]\n"
"v_pk_fma_f16 v91, v85, v95, v91 op_sel:[1,1,0] op_sel_hi:[0,1,1] neg_lo:[1,0,0]\n"
"v_pk_fma_f16 v103, v101, v95, v103 op_sel:[1,1,0] op_sel_hi:[0,1,1] neg_lo:[1,0,0]\n"
"ds_write2_b32 v155, v90, v102 offset0:0 offset1:68\n"
"ds_write2_b32 v155, v91, v103 offset0:136 offset1:204\n"
"ds_read_b128 v[52:55], v141 offset:0\n"
"ds_read_b128 v[56:59], v141 offset:64\n"
"ds_read_b128 v[60:63], v141 offset:128\n"
"ds_read_b128 v[64:67], v141 offset:192\n"
"v_mul_f32 v116, v113, v113\n"
"v_add_f32 v117, v112, v112\n"
"v_fma_f32 v116, v112, v112, -v116\n"
"v_mul_f32 v117, v117, v113\n"
"v_mul_f32 v120, v117, v117\n"
"v_add_f32 v121, v116, v116\n"
"v_fma_f32 v120, v116, v116, -v120\n"
"v_mul_f32 v121, v121, v117\n"
"v_mul_f32 v118, v117, v113\n"
"v_mul_f32 v119, v116, v113\n"
"v_fma_f32 v118, v116, v112, -v118\n"
"v_fmac_f32 v119, v117, v112\n"
"v_mul_f32 v122, v121, v121\n"
"v_add_f32 v123, v120, v120\n"
"v_fma_f32 v122, v120, v120, -v122\n"
"v_mul_f32 v123, v123, v121\n"
"v_cvt_pk_f16_f32 v84, v26, v27\n"
"v_mul_f32 v124, v27, v113\n"
"v_mul_f32 v125, v26, v113\n"
"v_fma_f32 v124, v26, v112, -v124\n"
"v_fmac_f32 v125, v27, v112\n"
"v_cvt_pk_f16_f32 v100, v124, v125\n"
"v_mul_f32 v124, v27, v117\n"
"v_mul_f32 v125, v26, v117\n"
"v_fma_f32 v124, v26, v116, -v124\n"
"v_fmac_f32 v125, v27, v116\n"
"v_cvt_pk_f16_f32 v85, v124, v125\n"
"v_mul_f32 v124, v27, v119\n"
"v_mul_f32 v125, v26, v119\n"
"v_fma_f32 v124, v26, v118, -v124\n"
"v_fmac_f32 v125, v27, v118\n"
"v_cvt_pk_f16_f32 v101, v124, v125\n"
"v_cvt_pk_f16_f32 v93, v120, v121\n"
"v_cvt_pk_f16_f32 v94, v122, v123\n"
"v_mul_f32 v124, v123, v121\n"
"v_mul_f32 v125, v122, v121\n"
"v_fma_f32 v124, v122, v120, -v124\n"
"v_fmac_f32 v125, v123, v120\n"
"v_cvt_pk_f16_f32 v95, v124, v125\n"
"ds_write2_b32 v152, v84, v100 offset0:0 offset1:68\n"
"v_pk_mul_f16 v86, v84, v93 op_sel:[0,0] op_sel_hi:[1,0]\n"
"v_pk_mul_f16 v102, v100, v93 op_sel:[0,0] op_sel_hi:[1,0]\n"
"v_pk_fma_f16 v86, v84, v93, v86 op_sel:[1,1,0] op_sel_hi:[0,1,1] neg_lo:[1,0,0]\n"
"v_pk_fma_f16 v102, v100, v93, v102 op_sel:[1,1,0] op_sel_hi:[0,1,1] neg_lo:[1,0,0]\n"
"ds_write2_b32 v152, v85, v101 offset0:136 offset1:204\n"
"v_pk_mul_f16 v87, v85, v93 op_sel:[0,0] op_sel_hi:[1,0]\n"
"v_pk_mul_f16 v103, v101, v93 op_sel:[0,0] op_sel_hi:[1,0]\n"
"v_pk_fma_f16 v87, v85, v93, v87 op_sel:[1,1,0] op_sel_hi:[0,1,1] neg_lo:[1,0,0]\n"
"v_pk_fma_f16 v103, v101, v93, v103 op_sel:[1,1,0] op_sel_hi:[0,1,1] neg_lo:[1,0,0]\n"
"ds_write2_b32 v153, v86, v102 offset0:0 offset1:68\n"
"v_pk_mul_f16 v88, v84, v94 op_sel:[0,0] op_sel_hi:[1,0]\n"
"v_pk_mul_f16 v102, v100, v94 op_sel:[0,0] op_sel_hi:[1,0]\n"
"v_pk_fma_f16 v88, v84, v94, v88 op_sel:[1,1,0] op_sel_hi:[0,1,1] neg_lo:[1,0,0]\n"
"v_pk_fma_f16 v102, v100, v94, v102 op_sel:[1,1,0] op_sel_hi:[0,1,1] neg_lo:[1,0,0]\n"
"ds_write2_b32 v153, v87, v103 offset0:136 offset1:204\n"
"v_pk_mul_f16 v89, v85, v94 op_sel:[0,0] op_sel_hi:[1,0]\n"
"v_pk_mul_f16 v103, v101, v94 op_sel:[0,0] op_sel_hi:[1,0]\n"
"v_pk_fma_f16 v89, v85, v94, v89 op_sel:[1,1,0] op_sel_hi:[0,1,1] neg_lo:[1,0,0]\n"
"v_pk_fma_f16 v103, v101, v94, v103 op_sel:[1,1,0] op_sel_hi:[0,1,1] neg_lo:[1,0,0]\n"
"ds_write2_b32 v154, v88, v102 offset0:0 offset1:68\n"
"v_pk_mul_f16 v90, v84, v95 op_sel:[0,0] op_sel_hi:[1,0]\n"
"v_pk_mul_f16 v102, v100, v95 op_sel:[0,0] op_sel_hi:[1,0]\n"
"v_pk_fma_f16 v90, v84, v95, v90 op_sel:[1,1,0] op_sel_hi:[0,1,1] neg_lo:[1,0,0]\n"
"v_pk_fma_f16 v102, v100, v95, v102 op_sel:[1,1,0] op_sel_hi:[0,1,1] neg_lo:[1,0,0]\n"
"ds_write2_b32 v154, v89, v103 offset0:136 offset1:204\n"
"v_pk_mul_f16 v91, v85, v95 op_sel:[0,0] op_sel_hi:[1,0]\n"
"v_pk_mul_f16 v103, v101, v95 op_sel:[0,0] op_sel_hi:[1,0]\n"
"v_pk_fma_f16 v91, v85, v95, v91 op_sel:[1,1,0] op_sel_hi:[0,1,1] neg_lo:[1,0,0]\n"
"v_pk_fma_f16 v103, v101, v95, v103 op_sel:[1,1,0] op_sel_hi:[0,1,1] neg_lo:[1,0,0]\n"
"ds_write2_b32 v155, v90, v102 offset0:0 offset1:68\n"
"ds_write2_b32 v155, v91, v103 offset0:136 offset1:204\n"
"ds_read_b128 v[68:71], v141 offset:0\n"
"ds_read_b128 v[72:75], v141 offset:64\n"
"ds_read_b128 v[76:79], v141 offset:128\n"
"ds_read_b128 v[80:83], v141 offset:192\n"
"v_mul_f32 v116, v115, v115\n"
"v_add_f32 v117, v114, v114\n"
"v_fma_f32 v116, v114, v114, -v116\n"
"v_mul_f32 v117, v117, v115\n"
"v_mul_f32 v120, v117, v117\n"
"v_add_f32 v121, v116, v116\n"
"v_fma_f32 v120, v116, v116, -v120\n"
"v_mul_f32 v121, v121, v117\n"
"v_mul_f32 v118, v117, v115\n"
"v_mul_f32 v119, v116, v115\n"
"v_fma_f32 v118, v116, v114, -v118\n"
"v_fmac_f32 v119, v117, v114\n"
"v_cvt_pk_f16_f32 v92, v114, v115\n"
"v_cvt_pk_f16_f32 v93, v116, v117\n"
"v_cvt_pk_f16_f32 v95, v120, v121\n"
"v_cvt_pk_f16_f32 v94, v118, v119\n"
"v_mul_f32 v124, v121, v115\n"
"v_mul_f32 v125, v120, v115\n"
"v_fma_f32 v124, v120, v114, -v124\n"
"v_fmac_f32 v125, v121, v114\n"
"v_cvt_pk_f16_f32 v96, v124, v125\n"
"v_mul_f32 v126, v121, v117\n"
"v_mul_f32 v127, v120, v117\n"
"v_fma_f32 v126, v120, v116, -v126\n"
"v_fmac_f32 v127, v121, v116\n"
"v_cvt_pk_f16_f32 v97, v126, v127\n"
"v_mul_f32 v124, v121, v119\n"
"v_mul_f32 v125, v120, v119\n"
"v_fma_f32 v124, v120, v118, -v124\n"
"v_fmac_f32 v125, v121, v118\n"
"v_cvt_pk_f16_f32 v98, v124, v125\n"
"v_mul_f32 v126, v121, v121\n"
"v_mul_f32 v127, v120, v121\n"
"v_fma_f32 v126, v120, v120, -v126\n"
"v_fmac_f32 v127, v121, v120\n"
"v_cvt_pk_f16_f32 v99, v126, v127\n"
"s_setprio 3\n"
"s_waitcnt lgkmcnt(3)\n"
"v_mfma_f32_16x16x32_f16 v[144:147], v[68:71], v[52:55], 0\n"
"v_pk_mul_f16 v100, v84, v92 op_sel:[0,0] op_sel_hi:[1,0]\n"
"v_pk_mul_f16 v101, v85, v92 op_sel:[0,0] op_sel_hi:[1,0]\n"
"s_waitcnt lgkmcnt(2)\n"
"v_mfma_f32_16x16x32_f16 v[144:147], v[72:75], v[56:59], v[144:147]\n"
"v_pk_fma_f16 v100, v84, v92, v100 op_sel:[1,1,0] op_sel_hi:[0,1,1] neg_lo:[1,0,0]\n"
"v_pk_fma_f16 v101, v85, v92, v101 op_sel:[1,1,0] op_sel_hi:[0,1,1] neg_lo:[1,0,0]\n"
"s_waitcnt lgkmcnt(1)\n"
"v_mfma_f32_16x16x32_f16 v[144:147], v[76:79], v[60:63], v[144:147]\n"
"v_pk_mul_f16 v102, v86, v92 op_sel:[0,0] op_sel_hi:[1,0]\n"
"v_pk_mul_f16 v103, v87, v92 op_sel:[0,0] op_sel_hi:[1,0]\n"
"s_waitcnt lgkmcnt(0)\n"
"v_mfma_f32_16x16x32_f16 v[144:147], v[80:83], v[64:67], v[144:147]\n"
"s_setprio 0\n"
"v_pk_fma_f16 v102, v86, v92, v102 op_sel:[1,1,0] op_sel_hi:[0,1,1] neg_lo:[1,0,0]\n"
"v_pk_fma_f16 v103, v87, v92, v103 op_sel:[1,1,0] op_sel_hi:[0,1,1] neg_lo:[1,0,0]\n"
"ds_write2_b32 v152, v100, v101 offset0:0 offset1:68\n"
"v_pk_mul_f16 v100, v88, v92 op_sel:[0,0] op_sel_hi:[1,0]\n"
"v_pk_mul_f16 v101, v89, v92 op_sel:[0,0] op_sel_hi:[1,0]\n"
"v_pk_fma_f16 v100, v88, v92, v100 op_sel:[1,1,0] op_sel_hi:[0,1,1] neg_lo:[1,0,0]\n"
"v_pk_fma_f16 v101, v89, v92, v101 op_sel:[1,1,0] op_sel_hi:[0,1,1] neg_lo:[1,0,0]\n"
"ds_write2_b32 v152, v102, v103 offset0:136 offset1:204\n"
"v_pk_mul_f16 v102, v90, v92 op_sel:[0,0] op_sel_hi:[1,0]\n"
"v_pk_mul_f16 v103, v91, v92 op_sel:[0,0] op_sel_hi:[1,0]\n"
"v_pk_fma_f16 v102, v90, v92, v102 op_sel:[1,1,0] op_sel_hi:[0,1,1] neg_lo:[1,0,0]\n"
"v_pk_fma_f16 v103, v91, v92, v103 op_sel:[1,1,0] op_sel_hi:[0,1,1] neg_lo:[1,0,0]\n"
"ds_write2_b32 v153, v100, v101 offset0:0 offset1:68\n"
"v_pk_mul_f16 v100, v84, v93 op_sel:[0,0] op_sel_hi:[1,0]\n"
"v_pk_mul_f16 v101, v86, v93 op_sel:[0,0] op_sel_hi:[1,0]\n"
"v_pk_fma_f16 v100, v84, v93, v100 op_sel:[1,1,0] op_sel_hi:[0,1,1] neg_lo:[1,0,0]\n"
"v_pk_fma_f16 v101, v86, v93, v101 op_sel:[1,1,0] op_sel_hi:[0,1,1] neg_lo:[1,0,0]\n"
"ds_write2_b32 v153, v102, v103 offset0:136 offset1:204\n"
"v_pk_mul_f16 v102, v88, v93 op_sel:[0,0] op_sel_hi:[1,0]\n"
"v_pk_mul_f16 v103, v90, v93 op_sel:[0,0] op_sel_hi:[1,0]\n"
"v_pk_fma_f16 v102, v88, v93, v102 op_sel:[1,1,0] op_sel_hi:[0,1,1] neg_lo:[1,0,0]\n"
"v_pk_fma_f16 v103, v90, v93, v103 op_sel:[1,1,0] op_sel_hi:[0,1,1] neg_lo:[1,0,0]\n"
"ds_write2_b32 v154, v100, v101 offset0:0 offset1:68\n"
"v_pk_mul_f16 v100, v84, v94 op_sel:[0,0] op_sel_hi:[1,0]\n"
"v_pk_mul_f16 v101, v86, v94 op_sel:[0,0] op_sel_hi:[1,0]\n"
"v_pk_fma_f16 v100, v84, v94, v100 op_sel:[1,1,0] op_sel_hi:[0,1,1] neg_lo:[1,0,0]\n"
"v_pk_fma_f16 v101, v86, v94, v101 op_sel:[1,1,0] op_sel_hi:[0,1,1] neg_lo:[1,0,0]\n"
"ds_write2_b32 v154, v102, v103 offset0:136 offset1:204\n"
"v_pk_mul_f16 v102, v88, v94 op_sel:[0,0] op_sel_hi:[1,0]\n"
"v_pk_mul_f16 v103, v90, v94 op_sel:[0,0] op_sel_hi:[1,0]\n"
"v_pk_fma_f16 v102, v88, v94, v102 op_sel:[1,1,0] op_sel_hi:[0,1,1] neg_lo:[1,0,0]\n"
"v_pk_fma_f16 v103, v90, v94, v103 op_sel:[1,1,0] op_sel_hi:[0,1,1] neg_lo:[1,0,0]\n"
"ds_write2_b32 v155, v100, v101 offset0:0 offset1:68\n"
"ds_write2_b32 v155, v102, v103 offset0:136 offset1:204\n"
"ds_read_b128 v[68:71], v141 offset:0\n"
"ds_read_b128 v[72:75], v141 offset:64\n"
"ds_read_b128 v[76:79], v141 offset:128\n"
"ds_read_b128 v[80:83], v141 offset:192\n"
"v_pk_mul_f16 v100, v84, v95 op_sel:[0,0] op_sel_hi:[1,0]\n"
"v_pk_mul_f16 v101, v88, v95 op_sel:[0,0] op_sel_hi:[1,0]\n"
"v_pk_fma_f16 v100, v84, v95, v100 op_sel:[1,1,0] op_sel_hi:[0,1,1] neg_lo:[1,0,0]\n"
"v_pk_fma_f16 v101, v88, v95, v101 op_sel:[1,1,0] op_sel_hi:[0,1,1] neg_lo:[1,0,0]\n"
"v_pk_mul_f16 v102, v84, v96 op_sel:[0,0] op_sel_hi:[1,0]\n"
"v_pk_mul_f16 v103, v88, v96 op_sel:[0,0] op_sel_hi:[1,0]\n"
"v_pk_fma_f16 v102, v84, v96, v102 op_sel:[1,1,0] op_sel_hi:[0,1,1] neg_lo:[1,0,0]\n"
"v_pk_fma_f16 v103, v88, v96, v103 op_sel:[1,1,0] op_sel_hi:[0,1,1] neg_lo:[1,0,0]\n"
"v_pk_mul_f16 v104, v84, v97 op_sel:[0,0] op_sel_hi:[1,0]\n"
"v_pk_mul_f16 v105, v88, v97 op_sel:[0,0] op_sel_hi:[1,0]\n"
"v_pk_fma_f16 v104, v84, v97, v104 op_sel:[1,1,0] op_sel_hi:[0,1,1] neg_lo:[1,0,0]\n"
"v_pk_fma_f16 v105, v88, v97, v105 op_sel:[1,1,0] op_sel_hi:[0,1,1] neg_lo:[1,0,0]\n"
"v_pk_mul_f16 v106, v84, v98 op_sel:[0,0] op_sel_hi:[1,0]\n"
"v_pk_mul_f16 v107, v88, v98 op_sel:[0,0] op_sel_hi:[1,0]\n"
"v_pk_fma_f16 v106, v84, v98, v106 op_sel:[1,1,0] op_sel_hi:[0,1,1] neg_lo:[1,0,0]\n"
"v_pk_fma_f16 v107, v88, v98, v107 op_sel:[1,1,0] op_sel_hi:[0,1,1] neg_lo:[1,0,0]\n"
"v_pk_mul_f16 v108, v84, v99 op_sel:[0,0] op_sel_hi:[1,0]\n"
"v_pk_mul_f16 v109, v88, v99 op_sel:[0,0] op_sel_hi:[1,0]\n"
"v_pk_fma_f16 v108, v84, v99, v108 op_sel:[1,1,0] op_sel_hi:[0,1,1] neg_lo:[1,0,0]\n"
"v_pk_fma_f16 v109, v88, v99, v109 op_sel:[1,1,0] op_sel_hi:[0,1,1] neg_lo:[1,0,0]\n"
"s_setprio 3\n"
"s_waitcnt lgkmcnt(3)\n"
"v_mfma_f32_16x16x32_f16 v[148:151], v[68:71], v[52:55], 0\n"
"ds_write2_b32 v152, v100, v101 offset0:0 offset1:68\n"
"s_waitcnt lgkmcnt(3)\n"
"v_mfma_f32_16x16x32_f16 v[148:151], v[72:75], v[56:59], v[148:151]\n"
"ds_write2_b32 v152, v102, v103 offset0:136 offset1:204\n"
"s_waitcnt lgkmcnt(3)\n"
"v_mfma_f32_16x16x32_f16 v[148:151], v[76:79], v[60:63], v[148:151]\n"
"ds_write2_b32 v153, v104, v105 offset0:0 offset1:68\n"
"s_waitcnt lgkmcnt(3)\n"
"v_mfma_f32_16x16x32_f16 v[148:151], v[80:83], v[64:67], v[148:151]\n"
"s_setprio 0\n"
"ds_write2_b32 v153, v106, v107 offset0:136 offset1:204\n"
"ds_write2_b32 v154, v108, v109 offset0:0 offset1:68\n"
"ds_read_b128 v[68:71], v141 offset:0\n"
"ds_read_b128 v[72:75], v141 offset:64\n"
"ds_read_b128 v[76:79], v141 offset:128\n"
"ds_read_b128 v[80:83], v141 offset:192\n"
"v_fmac_f32 v128, s50, v144\n"
"v_fmac_f32 v129, s50, v145\n"
"v_fmac_f32 v130, s50, v146\n"
"v_fmac_f32 v131, s50, v147\n"
"v_fmac_f32 v132, s50, v148\n"
"v_fmac_f32 v133, s50, v149\n"
"v_fmac_f32 v134, s50, v150\n"
"v_fmac_f32 v135, s50, v151\n"
"s_setprio 3\n"
"s_waitcnt lgkmcnt(3)\n"
"v_mfma_f32_16x16x32_f16 v[48:51], v[68:71], v[52:55], 0\n"
"s_waitcnt lgkmcnt(2)\n"
"v_mfma_f32_16x16x32_f16 v[48:51], v[72:75], v[56:59], v[48:51]\n"
"s_waitcnt lgkmcnt(1)\n"
"v_mfma_f32_16x16x32_f16 v[48:51], v[76:79], v[60:63], v[48:51]\n"
"s_waitcnt lgkmcnt(0)\n"
"v_mfma_f32_16x16x32_f16 v[48:51], v[80:83], v[64:67], v[48:51]\n"
"s_setprio 0\n"
"s_sub_u32 s55, s55, 1\n"
"s_nop 5\n"
"v_fmac_f32 v136, s50, v48\n"
"v_fmac_f32 v137, s50, v49\n"
"v_fmac_f32 v138, s50, v50\n"
"v_fmac_f32 v139, s50, v51\n"
"s_cbranch_scc0 .Lgo_305\n"
"s_setpc_b64 s[60:61]\n"
".Lgo_305:\n"
"s_waitcnt vmcnt(0)\n"
"v_mov_b32 v32, v40\n"
"v_mov_b32 v40, v44\n"
"v_mov_b32 v33, v41\n"
"v_mov_b32 v41, v45\n"
"v_mov_b32 v34, v42\n"
"v_mov_b32 v42, v46\n"
"v_mov_b32 v35, v43\n"
"v_mov_b32 v43, v47\n"
"s_cmp_lg_u32 s55, 0\n"
"s_cbranch_scc1 .Lkg_304\n"
"s_and_b32 s79, s2, 1\n"
"s_lshl_b32 s79, s79, 2\n"
"s_lshr_b32 s78, s2, 3\n"
"s_and_b32 s78, s78, 3\n"
"s_add_u32 s28, s79, s78\n"
"s_lshr_b32 s79, s2, 1\n"
"s_and_b32 s79, s79, 3\n"
"s_lshl_b32 s79, s79, 3\n"
"s_lshr_b32 s78, s2, 3\n"
"s_lshr_b32 s78, s78, 2\n"
"s_add_u32 s29, s79, s78\n"
"s_and_b32 s79, s2, 7\n"
"s_lshl_b32 s79, s79, 14\n"
"s_add_u32 s48, s79, 0x4000\n"
"s_mov_b32 s8, s22\n"
"s_mov_b32 s9, s23\n"
"s_mov_b32 s10, 0x280000\n"
"s_mov_b32 s11, 0x20000\n"
"s_mov_b32 s12, s24\n"
"s_mov_b32 s13, s25\n"
"s_mov_b32 s14, 0x280000\n"
"s_mov_b32 s15, 0x20000\n"
"s_mov_b32 s44, 0xcccccccc\n"
"s_mov_b32 s45, 0xcccccccc\n"
"s_mov_b32 s56, 0\n"
"s_mov_b32 s57, -2\n"
"s_mov_b32 s58, 0\n"
"s_mov_b32 s59, -1\n"
"s_lshl_b32 s79, s29, 1\n"
"s_lshr_b32 s78, s3, 1\n"
"s_add_u32 s79, s79, s78\n"
"s_mul_i32 s30, s79, 0x5000\n"
"s_and_b32 s79, s2, 7\n"
"s_lshl_b32 s79, s79, 14\n"
"s_add_u32 s81, s79, 0x3800\n"
"s_lshl_b32 s79, s28, 2\n"
"s_add_u32 s79, s79, s3\n"
"s_mul_i32 s79, s79, 0x6400\n"
"s_add_u32 s80, s79, 0x380000\n"
"s_lshr_b32 s79, s2, 1\n"
"s_and_b32 s79, s79, 3\n"
"s_lshl_b32 s79, s79, 2\n"
"s_lshr_b32 s78, s2, 3\n"
"s_lshr_b32 s78, s78, 3\n"
"s_add_u32 s79, s79, s78\n"
"s_lshl_b32 s79, s79, 2\n"
"s_add_u32 s79, s79, s3\n"
"s_mul_i32 s79, s79, 0x5000\n"
"s_and_b32 s78, s2, 1\n"
"s_lshl_b32 s78, s78, 3\n"
"s_lshr_b32 s77, s2, 3\n"
"s_and_b32 s77, s77, 7\n"
"s_add_u32 s78, s78, s77\n"
"s_mul_i32 s78, s78, 0x500\n"
"s_add_u32 s79, s79, s78\n"
"s_add_u32 s31, s79, 0x448000\n"
"s_mov_b32 s40, 0\n"
"s_mov_b32 s40, 1\n"
"s_setprio 0\n"
"v_lshlrev_b32 v32, 16, v32\n"
"v_lshlrev_b32 v33, 16, v33\n"
"v_lshlrev_b32 v34, 16, v34\n"
"v_lshlrev_b32 v35, 16, v35\n"
"v_mul_f32 v26, v142, v34\n"
"v_mul_f32 v27, v142, v35\n"
"v_max3_f32 v30, |v26|, |v27|, 0\n"
"s_nop 1\n"
"v_max_u32_dpp v30, v30, v30 row_ror:8 row_mask:0xf bank_mask:0xf\n"
"v_med3_f32 v32, v32, s52, v31\n"
"s_nop 0\n"
"v_max_u32_dpp v30, v30, v30 row_ror:4 row_mask:0xf bank_mask:0xf\n"
"v_mul_f32 v28, s53, v32\n"
"s_nop 0\n"
"v_max_u32_dpp v30, v30, v30 row_ror:2 row_mask:0xf bank_mask:0xf\n"
"v_exp_f32 v29, v28\n"
"s_nop 0\n"
"v_max_u32_dpp v30, v30, v30 row_ror:1 row_mask:0xf bank_mask:0xf\n"
"v_fma_f32 v28, v32, s53, -v28\n"
"v_fmac_f32 v28, 0x32a57060, v32\n"
"v_mov_b32 v34, v30\n"
"v_fma_f32 v28, v28, s54, 1.0\n"
"s_nop 0\n"
"v_permlane16_swap_b32 v30, v34\n"
"v_mul_f32 v32, v29, v28\n"
"v_max_u32 v30, v30, v34\n"
"v_cvt_pk_bf16_f32 v32, v32, v32\n"
"v_lshlrev_b32 v32, 16, v32\n"
"v_mov_b32 v34, v30\n"
"v_mul_f32 v25, v142, v33\n"
"s_nop 0\n"
"v_permlane32_swap_b32 v30, v34\n"
"v_mul_f32 v24, v142, -v32\n"
"v_max_u32 v30, v30, v34\n"
"v_mul_f32 v25, 0.15915494, v25\n"
"v_mul_f32 v24, s53, v24\n"
"v_readfirstlane_b32 s51, v30\n"
"s_nop 2\n"
"s_bfe_u32 s50, s51, 0x80017\n"
"s_max_i32 s50, s50, 37\n"
"s_min_i32 s50, s50, 217\n"
"s_cmp_gt_i32 s51, 0\n"
"s_cselect_b32 s50, s50, 127\n"
"s_sub_u32 s51, 267, s50\n"
"s_lshl_b32 s51, s51, 23\n"
"s_sub_u32 s50, s50, 27\n"
"s_lshl_b32 s50, s50, 23\n"
"v_mul_f32 v26, s51, v26\n"
"v_mul_f32 v27, s51, v27\n"
"v_mul_f32 v111, 0x41800000, v25\n"
"v_mul_f32 v100, 0x41800000, v24\n"
"v_fract_f32 v111, v111\n"
"v_exp_f32 v100, v100\n"
"v_cos_f32 v110, v111\n"
"v_sin_f32 v111, -v111\n"
"v_mul_f32 v110, v110, v100\n"
"v_mul_f32 v111, v111, v100\n"
"v_fract_f32 v113, v25\n"
"v_exp_f32 v100, v24\n"
"v_cos_f32 v112, v113\n"
"v_sin_f32 v113, v113\n"
"v_mul_f32 v112, v112, v100\n"
"v_mul_f32 v113, v113, v100\n"
"v_mul_f32 v115, 0x43800000, v25\n"
"v_mul_f32 v100, 0x43800000, v24\n"
"v_fract_f32 v115, v115\n"
"v_exp_f32 v100, v100\n"
"v_cos_f32 v114, v115\n"
"v_sin_f32 v115, v115\n"
"v_mul_f32 v114, v114, v100\n"
"v_mul_f32 v115, v115, v100\n"
"v_mul_f32 v116, v111, v111\n"
"v_add_f32 v117, v110, v110\n"
"v_fma_f32 v116, v110, v110, -v116\n"
"v_mul_f32 v117, v117, v111\n"
"v_mul_f32 v120, v117, v117\n"
"v_add_f32 v121, v116, v116\n"
"v_fma_f32 v120, v116, v116, -v120\n"
"v_mul_f32 v121, v121, v117\n"
"v_mul_f32 v118, v117, v111\n"
"v_mul_f32 v119, v116, v111\n"
"v_fma_f32 v118, v116, v110, -v118\n"
"v_fmac_f32 v119, v117, v110\n"
"v_mul_f32 v122, v121, v121\n"
"v_add_f32 v123, v120, v120\n"
"v_fma_f32 v122, v120, v120, -v122\n"
"v_mul_f32 v123, v123, v121\n"
"v_mov_b32 v84, 0x7400\n"
"v_mul_f32 v124, 0x46800000, v110\n"
"v_mul_f32 v125, 0x46800000, v111\n"
"v_cvt_pk_f16_f32 v100, v124, v125\n"
"v_mul_f32 v124, 0x46800000, v116\n"
"v_mul_f32 v125, 0x46800000, v117\n"
"v_cvt_pk_f16_f32 v85, v124, v125\n"
"v_mul_f32 v124, 0x46800000, v118\n"
"v_mul_f32 v125, 0x46800000, v119\n"
"v_cvt_pk_f16_f32 v101, v124, v125\n"
"v_cvt_pk_f16_f32 v93, v120, v121\n"
"v_cvt_pk_f16_f32 v94, v122, v123\n"
"v_mul_f32 v124, v123, v121\n"
"v_mul_f32 v125, v122, v121\n"
"v_fma_f32 v124, v122, v120, -v124\n"
"v_fmac_f32 v125, v123, v120\n"
"v_cvt_pk_f16_f32 v95, v124, v125\n"
"ds_write2_b32 v152, v84, v100 offset0:0 offset1:68\n"
"v_pk_mul_f16 v86, v84, v93 op_sel:[0,0] op_sel_hi:[1,0]\n"
"v_pk_mul_f16 v102, v100, v93 op_sel:[0,0] op_sel_hi:[1,0]\n"
"v_pk_fma_f16 v86, v84, v93, v86 op_sel:[1,1,0] op_sel_hi:[0,1,1] neg_lo:[1,0,0]\n"
"v_pk_fma_f16 v102, v100, v93, v102 op_sel:[1,1,0] op_sel_hi:[0,1,1] neg_lo:[1,0,0]\n"
"ds_write2_b32 v152, v85, v101 offset0:136 offset1:204\n"
"v_pk_mul_f16 v87, v85, v93 op_sel:[0,0] op_sel_hi:[1,0]\n"
"v_pk_mul_f16 v103, v101, v93 op_sel:[0,0] op_sel_hi:[1,0]\n"
"v_pk_fma_f16 v87, v85, v93, v87 op_sel:[1,1,0] op_sel_hi:[0,1,1] neg_lo:[1,0,0]\n"
"v_pk_fma_f16 v103, v101, v93, v103 op_sel:[1,1,0] op_sel_hi:[0,1,1] neg_lo:[1,0,0]\n"
"ds_write2_b32 v153, v86, v102 offset0:0 offset1:68\n"
"v_pk_mul_f16 v88, v84, v94 op_sel:[0,0] op_sel_hi:[1,0]\n"
"v_pk_mul_f16 v102, v100, v94 op_sel:[0,0] op_sel_hi:[1,0]\n"
"v_pk_fma_f16 v88, v84, v94, v88 op_sel:[1,1,0] op_sel_hi:[0,1,1] neg_lo:[1,0,0]\n"
"v_pk_fma_f16 v102, v100, v94, v102 op_sel:[1,1,0] op_sel_hi:[0,1,1] neg_lo:[1,0,0]\n"
"ds_write2_b32 v153, v87, v103 offset0:136 offset1:204\n"
"v_pk_mul_f16 v89, v85, v94 op_sel:[0,0] op_sel_hi:[1,0]\n"
"v_pk_mul_f16 v103, v101, v94 op_sel:[0,0] op_sel_hi:[1,0]\n"
"v_pk_fma_f16 v89, v85, v94, v89 op_sel:[1,1,0] op_sel_hi:[0,1,1] neg_lo:[1,0,0]\n"
"v_pk_fma_f16 v103, v101, v94, v103 op_sel:[1,1,0] op_sel_hi:[0,1,1] neg_lo:[1,0,0]\n"
"ds_write2_b32 v154, v88, v102 offset0:0 offset1:68\n"
"v_pk_mul_f16 v90, v84, v95 op_sel:[0,0] op_sel_hi:[1,0]\n"
"v_pk_mul_f16 v102, v100, v95 op_sel:[0,0] op_sel_hi:[1,0]\n"
"v_pk_fma_f16 v90, v84, v95, v90 op_sel:[1,1,0] op_sel_hi:[0,1,1] neg_lo:[1,0,0]\n"
"v_pk_fma_f16 v102, v100, v95, v102 op_sel:[1,1,0] op_sel_hi:[0,1,1] neg_lo:[1,0,0]\n"
"ds_write2_b32 v154, v89, v103 offset0:136 offset1:204\n"
"v_pk_mul_f16 v91, v85, v95 op_sel:[0,0] op_sel_hi:[1,0]\n"
"v_pk_mul_f16 v103, v101, v95 op_sel:[0,0] op_sel_hi:[1,0]\n"
"v_pk_fma_f16 v91, v85, v95, v91 op_sel:[1,1,0] op_sel_hi:[0,1,1] neg_lo:[1,0,0]\n"
"v_pk_fma_f16 v103, v101, v95, v103 op_sel:[1,1,0] op_sel_hi:[0,1,1] neg_lo:[1,0,0]\n"
"ds_write2_b32 v155, v90, v102 offset0:0 offset1:68\n"
"ds_write2_b32 v155, v91, v103 offset0:136 offset1:204\n"
"ds_read_b128 v[52:55], v141 offset:0\n"
"ds_read_b128 v[56:59], v141 offset:64\n"
"ds_read_b128 v[60:63], v141 offset:128\n"
"ds_read_b128 v[64:67], v141 offset:192\n"
"v_mul_f32 v116, v113, v113\n"
"v_add_f32 v117, v112, v112\n"
"v_fma_f32 v116, v112, v112, -v116\n"
"v_mul_f32 v117, v117, v113\n"
"v_mul_f32 v120, v117, v117\n"
"v_add_f32 v121, v116, v116\n"
"v_fma_f32 v120, v116, v116, -v120\n"
"v_mul_f32 v121, v121, v117\n"
"v_mul_f32 v118, v117, v113\n"
"s_setprio 3\n"
"buffer_load_dwordx4 v[44:47], v38, s[4:7], s81 offen sc1\n"
"s_setprio 0\n"
"v_mul_f32 v119, v116, v113\n"
"v_fma_f32 v118, v116, v112, -v118\n"
"v_fmac_f32 v119, v117, v112\n"
"v_mul_f32 v122, v121, v121\n"
"v_add_f32 v123, v120, v120\n"
"v_fma_f32 v122, v120, v120, -v122\n"
"v_mul_f32 v123, v123, v121\n"
"v_cvt_pk_f16_f32 v84, v26, v27\n"
"v_mul_f32 v124, v27, v113\n"
"v_mul_f32 v125, v26, v113\n"
"v_fma_f32 v124, v26, v112, -v124\n"
"v_fmac_f32 v125, v27, v112\n"
"v_cvt_pk_f16_f32 v100, v124, v125\n"
"v_mul_f32 v124, v27, v117\n"
"v_mul_f32 v125, v26, v117\n"
"v_fma_f32 v124, v26, v116, -v124\n"
"v_fmac_f32 v125, v27, v116\n"
"v_cvt_pk_f16_f32 v85, v124, v125\n"
"v_mul_f32 v124, v27, v119\n"
"v_mul_f32 v125, v26, v119\n"
"v_fma_f32 v124, v26, v118, -v124\n"
"v_fmac_f32 v125, v27, v118\n"
"v_cvt_pk_f16_f32 v101, v124, v125\n"
"v_cvt_pk_f16_f32 v93, v120, v121\n"
"v_cvt_pk_f16_f32 v94, v122, v123\n"
"v_mul_f32 v124, v123, v121\n"
"v_mul_f32 v125, v122, v121\n"
"v_fma_f32 v124, v122, v120, -v124\n"
"v_fmac_f32 v125, v123, v120\n"
"v_cvt_pk_f16_f32 v95, v124, v125\n"
"ds_write2_b32 v152, v84, v100 offset0:0 offset1:68\n"
"v_pk_mul_f16 v86, v84, v93 op_sel:[0,0] op_sel_hi:[1,0]\n"
"v_pk_mul_f16 v102, v100, v93 op_sel:[0,0] op_sel_hi:[1,0]\n"
"v_pk_fma_f16 v86, v84, v93, v86 op_sel:[1,1,0] op_sel_hi:[0,1,1] neg_lo:[1,0,0]\n"
"v_pk_fma_f16 v102, v100, v93, v102 op_sel:[1,1,0] op_sel_hi:[0,1,1] neg_lo:[1,0,0]\n"
"ds_write2_b32 v152, v85, v101 offset0:136 offset1:204\n"
"v_pk_mul_f16 v87, v85, v93 op_sel:[0,0] op_sel_hi:[1,0]\n"
"v_pk_mul_f16 v103, v101, v93 op_sel:[0,0] op_sel_hi:[1,0]\n"
"v_pk_fma_f16 v87, v85, v93, v87 op_sel:[1,1,0] op_sel_hi:[0,1,1] neg_lo:[1,0,0]\n"
"v_pk_fma_f16 v103, v101, v93, v103 op_sel:[1,1,0] op_sel_hi:[0,1,1] neg_lo:[1,0,0]\n"
"ds_write2_b32 v153, v86, v102 offset0:0 offset1:68\n"
"v_pk_mul_f16 v88, v84, v94 op_sel:[0,0] op_sel_hi:[1,0]\n"
"v_pk_mul_f16 v102, v100, v94 op_sel:[0,0] op_sel_hi:[1,0]\n"
"v_pk_fma_f16 v88, v84, v94, v88 op_sel:[1,1,0] op_sel_hi:[0,1,1] neg_lo:[1,0,0]\n"
"v_pk_fma_f16 v102, v100, v94, v102 op_sel:[1,1,0] op_sel_hi:[0,1,1] neg_lo:[1,0,0]\n"
"ds_write2_b32 v153, v87, v103 offset0:136 offset1:204\n"
"v_pk_mul_f16 v89, v85, v94 op_sel:[0,0] op_sel_hi:[1,0]\n"
"v_pk_mul_f16 v103, v101, v94 op_sel:[0,0] op_sel_hi:[1,0]\n"
"v_pk_fma_f16 v89, v85, v94, v89 op_sel:[1,1,0] op_sel_hi:[0,1,1] neg_lo:[1,0,0]\n"
"v_pk_fma_f16 v103, v101, v94, v103 op_sel:[1,1,0] op_sel_hi:[0,1,1] neg_lo:[1,0,0]\n"
"ds_write2_b32 v154, v88, v102 offset0:0 offset1:68\n"
"v_pk_mul_f16 v90, v84, v95 op_sel:[0,0] op_sel_hi:[1,0]\n"
"v_pk_mul_f16 v102, v100, v95 op_sel:[0,0] op_sel_hi:[1,0]\n"
"v_pk_fma_f16 v90, v84, v95, v90 op_sel:[1,1,0] op_sel_hi:[0,1,1] neg_lo:[1,0,0]\n"
"s_waitcnt vmcnt(0)\n"
"v_xor_b32 v44, s16, v44\n"
"v_xor_b32 v45, s16, v45\n"
"v_xor_b32 v46, s16, v46\n"
"v_xor_b32 v47, s16, v47\n"
"v_or_b32 v44, v44, v45\n"
"v_or3_b32 v44, v44, v46, v47\n"
"v_cmp_ne_u32 vcc, 0, v44\n"
"s_cbranch_vccnz .Lsl_300\n"
".Lbk_301:\n"
"v_pk_fma_f16 v102, v100, v95, v102 op_sel:[1,1,0] op_sel_hi:[0,1,1] neg_lo:[1,0,0]\n"
"ds_write2_b32 v154, v89, v103 offset0:136 offset1:204\n"
"v_pk_mul_f16 v91, v85, v95 op_sel:[0,0] op_sel_hi:[1,0]\n"
"v_pk_mul_f16 v103, v101, v95 op_sel:[0,0] op_sel_hi:[1,0]\n"
"v_pk_fma_f16 v91, v85, v95, v91 op_sel:[1,1,0] op_sel_hi:[0,1,1] neg_lo:[1,0,0]\n"
"v_pk_fma_f16 v103, v101, v95, v103 op_sel:[1,1,0] op_sel_hi:[0,1,1] neg_lo:[1,0,0]\n"
"buffer_load_dwordx4 v[156:159], v2, s[4:7], s80 offen offset:0 sc1\n"
"ds_write2_b32 v155, v90, v102 offset0:0 offset1:68\n"
"ds_write2_b32 v155, v91, v103 offset0:136 offset1:204\n"
"ds_read_b128 v[68:71], v141 offset:0\n"
"ds_read_b128 v[72:75], v141 offset:64\n"
"ds_read_b128 v[76:79], v141 offset:128\n"
"ds_read_b128 v[80:83], v141 offset:192\n"
"buffer_load_dwordx4 v[160:163], v2, s[4:7], s80 offen offset:1024 sc1\n"
"v_mul_f32 v116, v115, v115\n"
"v_add_f32 v117, v114, v114\n"
"v_fma_f32 v116, v114, v114, -v116\n"
"v_mul_f32 v117, v117, v115\n"
"v_mul_f32 v120, v117, v117\n"
"v_add_f32 v121, v116, v116\n"
"buffer_load_dwordx4 v[164:167], v2, s[4:7], s80 offen offset:2048 sc1\n"
"v_fma_f32 v120, v116, v116, -v120\n"
"v_mul_f32 v121, v121, v117\n"
"v_mul_f32 v118, v117, v115\n"
"v_mul_f32 v119, v116, v115\n"
"v_fma_f32 v118, v116, v114, -v118\n"
"v_fmac_f32 v119, v117, v114\n"
"buffer_load_dwordx4 v[168:171], v2, s[4:7], s80 offen offset:3072 sc1\n"
"v_cvt_pk_f16_f32 v92, v114, v115\n"
"v_cvt_pk_f16_f32 v93, v116, v117\n"
"v_cvt_pk_f16_f32 v95, v120, v121\n"
"v_cvt_pk_f16_f32 v94, v118, v119\n"
"v_mul_f32 v124, v121, v115\n"
"v_mul_f32 v125, v120, v115\n"
"s_add_u32 s80, s80, 4096\n"
"buffer_load_dwordx4 v[172:175], v2, s[4:7], s80 offen offset:0 sc1\n"
"v_fma_f32 v124, v120, v114, -v124\n"
"v_fmac_f32 v125, v121, v114\n"
"v_cvt_pk_f16_f32 v96, v124, v125\n"
"v_mul_f32 v126, v121, v117\n"
"v_mul_f32 v127, v120, v117\n"
"v_fma_f32 v126, v120, v116, -v126\n"
"buffer_load_dwordx4 v[176:179], v2, s[4:7], s80 offen offset:1024 sc1\n"
"v_fmac_f32 v127, v121, v116\n"
"v_cvt_pk_f16_f32 v97, v126, v127\n"
"v_mul_f32 v124, v121, v119\n"
"v_mul_f32 v125, v120, v119\n"
"v_fma_f32 v124, v120, v118, -v124\n"
"v_fmac_f32 v125, v121, v118\n"
"s_waitcnt vmcnt(5)\n"
"buffer_load_dwordx4 v[180:183], v2, s[4:7], s80 offen offset:2048 sc1\n"
"v_cvt_pk_f16_f32 v98, v124, v125\n"
"v_mul_f32 v126, v121, v121\n"
"v_mul_f32 v127, v120, v121\n"
"v_fma_f32 v126, v120, v120, -v126\n"
"v_fmac_f32 v127, v121, v120\n"
"v_cvt_pk_f16_f32 v99, v126, v127\n"
"s_waitcnt vmcnt(5)\n"
"buffer_load_dwordx4 v[184:187], v2, s[4:7], s80 offen offset:3072 sc1\n"
"s_setprio 3\n"
"s_waitcnt lgkmcnt(3)\n"
"v_mfma_f32_16x16x32_f16 v[144:147], v[68:71], v[52:55], 0\n"
"v_pk_mul_f16 v100, v84, v92 op_sel:[0,0] op_sel_hi:[1,0]\n"
"v_pk_mul_f16 v101, v85, v92 op_sel:[0,0] op_sel_hi:[1,0]\n"
"s_waitcnt lgkmcnt(2)\n"
"v_mfma_f32_16x16x32_f16 v[144:147], v[72:75], v[56:59], v[144:147]\n"
"v_pk_fma_f16 v100, v84, v92, v100 op_sel:[1,1,0] op_sel_hi:[0,1,1] neg_lo:[1,0,0]\n"
"s_add_u32 s80, s80, 4096\n"
"s_waitcnt vmcnt(5)\n"
"buffer_load_dwordx4 v[188:191], v2, s[4:7], s80 offen offset:0 sc1\n"
"v_pk_fma_f16 v101, v85, v92, v101 op_sel:[1,1,0] op_sel_hi:[0,1,1] neg_lo:[1,0,0]\n"
"s_waitcnt lgkmcnt(1)\n"
"v_mfma_f32_16x16x32_f16 v[144:147], v[76:79], v[60:63], v[144:147]\n"
"v_pk_mul_f16 v102, v86, v92 op_sel:[0,0] op_sel_hi:[1,0]\n"
"v_pk_mul_f16 v103, v87, v92 op_sel:[0,0] op_sel_hi:[1,0]\n"
"s_waitcnt lgkmcnt(0)\n"
"v_mfma_f32_16x16x32_f16 v[144:147], v[80:83], v[64:67], v[144:147]\n"
"s_setprio 0\n"
"s_waitcnt vmcnt(5)\n"
"buffer_load_dwordx4 v[192:195], v2, s[4:7], s80 offen offset:1024 sc1\n"
"v_pk_fma_f16 v102, v86, v92, v102 op_sel:[1,1,0] op_sel_hi:[0,1,1] neg_lo:[1,0,0]\n"
"v_pk_fma_f16 v103, v87, v92, v103 op_sel:[1,1,0] op_sel_hi:[0,1,1] neg_lo:[1,0,0]\n"
"ds_write2_b32 v152, v100, v101 offset0:0 offset1:68\n"
"v_pk_mul_f16 v100, v88, v92 op_sel:[0,0] op_sel_hi:[1,0]\n"
"v_pk_mul_f16 v101, v89, v92 op_sel:[0,0] op_sel_hi:[1,0]\n"
"v_pk_fma_f16 v100, v88, v92, v100 op_sel:[1,1,0] op_sel_hi:[0,1,1] neg_lo:[1,0,0]\n"
"s_waitcnt vmcnt(5)\n"
"buffer_load_dwordx4 v[196:199], v2, s[4:7], s80 offen offset:2048 sc1\n"
"v_pk_fma_f16 v101, v89, v92, v101 op_sel:[1,1,0] op_sel_hi:[0,1,1] neg_lo:[1,0,0]\n"
"ds_write2_b32 v152, v102, v103 offset0:136 offset1:204\n"
"v_pk_mul_f16 v102, v90, v92 op_sel:[0,0] op_sel_hi:[1,0]\n"
"v_pk_mul_f16 v103, v91, v92 op_sel:[0,0] op_sel_hi:[1,0]\n"
"v_pk_fma_f16 v102, v90, v92, v102 op_sel:[1,1,0] op_sel_hi:[0,1,1] neg_lo:[1,0,0]\n"
"v_pk_fma_f16 v103, v91, v92, v103 op_sel:[1,1,0] op_sel_hi:[0,1,1] neg_lo:[1,0,0]\n"
"s_waitcnt vmcnt(5)\n"
"buffer_load_dwordx4 v[200:203], v2, s[4:7], s80 offen offset:3072 sc1\n"
"ds_write2_b32 v153, v100, v101 offset0:0 offset1:68\n"
"v_pk_mul_f16 v100, v84, v93 op_sel:[0,0] op_sel_hi:[1,0]\n"
"v_pk_mul_f16 v101, v86, v93 op_sel:[0,0] op_sel_hi:[1,0]\n"
"v_pk_fma_f16 v100, v84, v93, v100 op_sel:[1,1,0] op_sel_hi:[0,1,1] neg_lo:[1,0,0]\n"
"v_pk_fma_f16 v101, v86, v93, v101 op_sel:[1,1,0] op_sel_hi:[0,1,1] neg_lo:[1,0,0]\n"
"ds_write2_b32 v153, v102, v103 offset0:136 offset1:204\n"
"s_add_u32 s80, s80, 4096\n"
"s_waitcnt vmcnt(5)\n"
"buffer_load_dwordx4 v[204:207], v2, s[4:7], s80 offen offset:0 sc1\n"
"v_pk_mul_f16 v102, v88, v93 op_sel:[0,0] op_sel_hi:[1,0]\n"
"v_pk_mul_f16 v103, v90, v93 op_sel:[0,0] op_sel_hi:[1,0]\n"
"v_pk_fma_f16 v102, v88, v93, v102 op_sel:[1,1,0] op_sel_hi:[0,1,1] neg_lo:[1,0,0]\n"
"v_pk_fma_f16 v103, v90, v93, v103 op_sel:[1,1,0] op_sel_hi:[0,1,1] neg_lo:[1,0,0]\n"
"ds_write2_b32 v154, v100, v101 offset0:0 offset1:68\n"
"v_pk_mul_f16 v100, v84, v94 op_sel:[0,0] op_sel_hi:[1,0]\n"
"s_waitcnt vmcnt(5)\n"
"buffer_load_dwordx4 v[208:211], v2, s[4:7], s80 offen offset:1024 sc1\n"
"v_pk_mul_f16 v101, v86, v94 op_sel:[0,0] op_sel_hi:[1,0]\n"
"v_pk_fma_f16 v100, v84, v94, v100 op_sel:[1,1,0] op_sel_hi:[0,1,1] neg_lo:[1,0,0]\n"
"v_pk_fma_f16 v101, v86, v94, v101 op_sel:[1,1,0] op_sel_hi:[0,1,1] neg_lo:[1,0,0]\n"
"ds_write2_b32 v154, v102, v103 offset0:136 offset1:204\n"
"v_pk_mul_f16 v102, v88, v94 op_sel:[0,0] op_sel_hi:[1,0]\n"
"v_pk_mul_f16 v103, v90, v94 op_sel:[0,0] op_sel_hi:[1,0]\n"
"s_waitcnt vmcnt(5)\n"
"buffer_load_dwordx4 v[212:215], v2, s[4:7], s80 offen offset:2048 sc1\n"
"v_pk_fma_f16 v102, v88, v94, v102 op_sel:[1,1,0] op_sel_hi:[0,1,1] neg_lo:[1,0,0]\n"
"v_pk_fma_f16 v103, v90, v94, v103 op_sel:[1,1,0] op_sel_hi:[0,1,1] neg_lo:[1,0,0]\n"
"ds_write2_b32 v155, v100, v101 offset0:0 offset1:68\n"
"ds_write2_b32 v155, v102, v103 offset0:136 offset1:204\n"
"ds_read_b128 v[68:71], v141 offset:0\n"
"ds_read_b128 v[72:75], v141 offset:64\n"
"s_waitcnt vmcnt(5)\n"
"buffer_load_dwordx4 v[216:219], v2, s[4:7], s80 offen offset:3072 sc1\n"
"ds_read_b128 v[76:79], v141 offset:128\n"
"ds_read_b128 v[80:83], v141 offset:192\n"
"v_pk_mul_f16 v100, v84, v95 op_sel:[0,0] op_sel_hi:[1,0]\n"
"v_pk_mul_f16 v101, v88, v95 op_sel:[0,0] op_sel_hi:[1,0]\n"
"v_pk_fma_f16 v100, v84, v95, v100 op_sel:[1,1,0] op_sel_hi:[0,1,1] neg_lo:[1,0,0]\n"
"v_pk_fma_f16 v101, v88, v95, v101 op_sel:[1,1,0] op_sel_hi:[0,1,1] neg_lo:[1,0,0]\n"
"s_add_u32 s80, s80, 4096\n"
"s_waitcnt vmcnt(5)\n"
"buffer_load_dwordx4 v[220:223], v2, s[4:7], s80 offen offset:0 sc1\n"
"v_pk_mul_f16 v102, v84, v96 op_sel:[0,0] op_sel_hi:[1,0]\n"
"v_pk_mul_f16 v103, v88, v96 op_sel:[0,0] op_sel_hi:[1,0]\n"
"v_pk_fma_f16 v102, v84, v96, v102 op_sel:[1,1,0] op_sel_hi:[0,1,1] neg_lo:[1,0,0]\n"
"v_pk_fma_f16 v103, v88, v96, v103 op_sel:[1,1,0] op_sel_hi:[0,1,1] neg_lo:[1,0,0]\n"
"v_pk_mul_f16 v104, v84, v97 op_sel:[0,0] op_sel_hi:[1,0]\n"
"v_pk_mul_f16 v105, v88, v97 op_sel:[0,0] op_sel_hi:[1,0]\n"
"s_waitcnt vmcnt(5)\n"
"buffer_load_dwordx4 v[224:227], v2, s[4:7], s80 offen offset:1024 sc1\n"
"v_pk_fma_f16 v104, v84, v97, v104 op_sel:[1,1,0] op_sel_hi:[0,1,1] neg_lo:[1,0,0]\n"
"v_pk_fma_f16 v105, v88, v97, v105 op_sel:[1,1,0] op_sel_hi:[0,1,1] neg_lo:[1,0,0]\n"
"v_pk_mul_f16 v106, v84, v98 op_sel:[0,0] op_sel_hi:[1,0]\n"
"v_pk_mul_f16 v107, v88, v98 op_sel:[0,0] op_sel_hi:[1,0]\n"
"v_pk_fma_f16 v106, v84, v98, v106 op_sel:[1,1,0] op_sel_hi:[0,1,1] neg_lo:[1,0,0]\n"
"v_pk_fma_f16 v107, v88, v98, v107 op_sel:[1,1,0] op_sel_hi:[0,1,1] neg_lo:[1,0,0]\n"
"s_waitcnt vmcnt(5)\n"
"buffer_load_dwordx4 v[228:231], v2, s[4:7], s80 offen offset:2048 sc1\n"
"v_pk_mul_f16 v108, v84, v99 op_sel:[0,0] op_sel_hi:[1,0]\n"
"v_pk_mul_f16 v109, v88, v99 op_sel:[0,0] op_sel_hi:[1,0]\n"
"v_pk_fma_f16 v108, v84, v99, v108 op_sel:[1,1,0] op_sel_hi:[0,1,1] neg_lo:[1,0,0]\n"
"v_pk_fma_f16 v109, v88, v99, v109 op_sel:[1,1,0] op_sel_hi:[0,1,1] neg_lo:[1,0,0]\n"
"s_setprio 3\n"
"s_waitcnt lgkmcnt(3)\n"
"v_mfma_f32_16x16x32_f16 v[148:151], v[68:71], v[52:55], 0\n"
"s_waitcnt vmcnt(5)\n"
"buffer_load_dwordx4 v[232:235], v2, s[4:7], s80 offen offset:3072 sc1\n"
"ds_write2_b32 v152, v100, v101 offset0:0 offset1:68\n"
"s_waitcnt lgkmcnt(3)\n"
"v_mfma_f32_16x16x32_f16 v[148:151], v[72:75], v[56:59], v[148:151]\n"
"ds_write2_b32 v152, v102, v103 offset0:136 offset1:204\n"
"s_waitcnt lgkmcnt(3)\n"
"v_mfma_f32_16x16x32_f16 v[148:151], v[76:79], v[60:63], v[148:151]\n"
"ds_write2_b32 v153, v104, v105 offset0:0 offset1:68\n"
"s_waitcnt lgkmcnt(3)\n"
"v_mfma_f32_16x16x32_f16 v[148:151], v[80:83], v[64:67], v[148:151]\n"
"s_add_u32 s80, s80, 4096\n"
"s_waitcnt vmcnt(5)\n"
"buffer_load_dwordx4 v[236:239], v2, s[4:7], s80 offen offset:0 sc1\n"
"s_setprio 0\n"
"ds_write2_b32 v153, v106, v107 offset0:136 offset1:204\n"
"ds_write2_b32 v154, v108, v109 offset0:0 offset1:68\n"
"ds_read_b128 v[68:71], v141 offset:0\n"
"ds_read_b128 v[72:75], v141 offset:64\n"
"ds_read_b128 v[76:79], v141 offset:128\n"
"s_waitcnt vmcnt(5)\n"
"buffer_load_dwordx4 v[240:243], v2, s[4:7], s80 offen offset:1024 sc1\n"
"ds_read_b128 v[80:83], v141 offset:192\n"
"v_fmac_f32 v128, s50, v144\n"
"v_fmac_f32 v129, s50, v145\n"
"v_fmac_f32 v130, s50, v146\n"
"v_fmac_f32 v131, s50, v147\n"
"v_fmac_f32 v132, s50, v148\n"
"s_waitcnt vmcnt(5)\n"
"buffer_load_dwordx4 v[244:247], v2, s[4:7], s80 offen offset:2048 sc1\n"
"v_fmac_f32 v133, s50, v149\n"
"v_fmac_f32 v134, s50, v150\n"
"v_fmac_f32 v135, s50, v151\n"
"s_setprio 3\n"
"s_waitcnt lgkmcnt(3)\n"
"v_mfma_f32_16x16x32_f16 v[48:51], v[68:71], v[52:55], 0\n"
"s_waitcnt lgkmcnt(2)\n"
"v_mfma_f32_16x16x32_f16 v[48:51], v[72:75], v[56:59], v[48:51]\n"
"s_waitcnt vmcnt(5)\n"
"buffer_load_dwordx4 v[248:251], v2, s[4:7], s80 offen offset:3072 sc1\n"
"s_waitcnt lgkmcnt(1)\n"
"v_mfma_f32_16x16x32_f16 v[48:51], v[76:79], v[60:63], v[48:51]\n"
"s_waitcnt lgkmcnt(0)\n"
"v_mfma_f32_16x16x32_f16 v[48:51], v[80:83], v[64:67], v[48:51]\n"
"s_setprio 0\n"
"s_nop 6\n"
"v_fmac_f32 v136, s50, v48\n"
"v_fmac_f32 v137, s50, v49\n"
"v_fmac_f32 v138, s50, v50\n"
"s_add_u32 s80, s80, 4096\n"
"s_waitcnt vmcnt(5)\n"
"buffer_load_dwordx4 v[252:255], v2, s[4:7], s80 offen offset:0 sc1\n"
"v_fmac_f32 v139, s50, v51\n"
"s_and_b32 s73, s3, 1\n"
"v_and_b32 v78, 15, v0\n"
"v_lshrrev_b32 v79, 4, v0\n"
"s_lshl_b32 s74, s73, 30\n"
"v_mul_i32_i24 v99, 0x500, v78\n"
"v_lshl_add_u32 v14, v79, 3, v99\n"
"v_lshrrev_b32 v99, 1, v78\n"
"v_and_b32 v98, 1, v78\n"
"v_lshlrev_b32 v98, 7, v98\n"
"v_lshl_add_u32 v98, v79, 8, v98\n"
"v_lshl_add_u32 v17, v99, 4, v98\n"
"s_mov_b32 s79, 0x25400\n"
"s_cmp_eq_i32 s3, 0\n"
"s_cselect_b32 s79, s79, 0x21000\n"
"s_mov_b32 s78, 0x25c00\n"
"s_cmp_eq_i32 s3, 2\n"
"s_cselect_b32 s78, s78, 0x23400\n"
"s_cmp_lt_i32 s3, 2\n"
"s_cselect_b32 s79, s79, s78\n"
"v_add_u32 v6, s79, v2\n"
"s_mov_b32 s79, 0x40000000\n"
"s_cmp_eq_i32 s3, 0\n"
"s_cselect_b32 s79, s79, 0x20c00\n"
"s_mov_b32 s78, 0x40000000\n"
"s_cmp_eq_i32 s3, 2\n"
"s_cselect_b32 s78, s78, 0x23000\n"
"s_cmp_lt_i32 s3, 2\n"
"s_cselect_b32 s79, s79, s78\n"
"v_add_u32 v7, s79, v2\n"
"s_mov_b32 s79, 0x23c00\n"
"s_cmp_eq_i32 s3, 0\n"
"s_cselect_b32 s79, s79, 0x1f200\n"
"s_mov_b32 s78, 0x21800\n"
"s_cmp_eq_i32 s3, 2\n"
"s_cselect_b32 s78, s78, 0x1ce00\n"
"s_cmp_lt_i32 s3, 2\n"
"s_cselect_b32 s79, s79, s78\n"
"v_add_u32 v8, s79, v2\n"
"s_mov_b32 s79, 0x20400\n"
"s_cmp_eq_i32 s3, 0\n"
"s_cselect_b32 s79, s79, 0x24c00\n"
"s_mov_b32 s78, 0x1e000\n"
"s_cmp_eq_i32 s3, 2\n"
"s_cselect_b32 s78, s78, 0x22800\n"
"s_cmp_lt_i32 s3, 2\n"
"s_cselect_b32 s79, s79, s78\n"
"v_add_u32 v9, s79, v2\n"
"s_mov_b32 s79, 0x24400\n"
"s_cmp_eq_i32 s3, 0\n"
"s_cselect_b32 s79, s79, 0x24800\n"
"s_mov_b32 s78, 0x22000\n"
"s_cmp_eq_i32 s3, 2\n"
"s_cselect_b32 s78, s78, 0x22400\n"
"s_cmp_lt_i32 s3, 2\n"
"s_cselect_b32 s79, s79, s78\n"
"v_add_u32 v10, s79, v2\n"
"v_add_u32 v15, s74, v14\n"
"v_lshlrev_b32 v99, 3, v78\n"
"s_mov_b32 vcc_lo, 0x500005\n"
"s_mov_b32 vcc_hi, 0x50000500\n"
"v_cndmask_b32 v16, 2.0, v99, vcc\n"
"v_add_u32 v18, s74, v17\n"
"s_sub_u32 s79, s73, 1\n"
"v_mov_b32 v19, s79\n"
"v_lshrrev_b32 v99, 1, v79\n"
"v_and_b32 v98, 1, v79\n"
"v_and_b32 v97, 4, v78\n"
"v_and_b32 v96, 3, v78\n"
"v_lshrrev_b32 v95, 3, v78\n"
"v_lshlrev_b32 v95, 6, v95\n"
"v_lshl_add_u32 v96, v96, 4, v95\n"
"v_lshl_add_u32 v97, v97, 1, v96\n"
"v_lshl_add_u32 v98, v98, 8, v97\n"
"v_lshl_add_u32 v99, v99, 7, v98\n"
"v_sub_u32 v39, v99, v14\n"
"s_mov_b32 s79, 0x20c00\n"
"s_cmp_eq_i32 s3, 0\n"
"s_cselect_b32 s79, s79, 0x25400\n"
"s_mov_b32 s78, 0x23000\n"
"s_cmp_eq_i32 s3, 2\n"
"s_cselect_b32 s78, s78, 0x25c00\n"
"s_cmp_lt_i32 s3, 2\n"
"s_cselect_b32 s79, s79, s78\n"
"v_add_u32 v11, s79, v2\n"
"s_mov_b32 s79, 0x21800\n"
"s_cmp_eq_i32 s3, 0\n"
"s_cselect_b32 s79, s79, 0x1ce00\n"
"s_mov_b32 s78, 0x23c00\n"
"s_cmp_eq_i32 s3, 2\n"
"s_cselect_b32 s78, s78, 0x1f200\n"
"s_cmp_lt_i32 s3, 2\n"
"s_cselect_b32 s79, s79, s78\n"
"v_add_u32 v12, s79, v2\n"
"s_mov_b32 s79, 0x22400\n"
"s_cmp_eq_i32 s3, 0\n"
"s_cselect_b32 s79, s79, 0x1e000\n"
"s_mov_b32 s78, 0x24800\n"
"s_cmp_eq_i32 s3, 2\n"
"s_cselect_b32 s78, s78, 0x20400\n"
"s_cmp_lt_i32 s3, 2\n"
"s_cselect_b32 s79, s79, s78\n"
"v_add_u32 v13, s79, v2\n"
"v_mov_b32 v20, 0x3f803f80\n"
"v_mov_b32 v21, 0x3f803f80\n"
"v_mov_b32 v22, 0x3f803f80\n"
"v_mov_b32 v23, 0x3f803f80\n"
"s_branch .Ljk_298\n"
".Lnk_297:\n"
"s_and_b32 s79, s2, 1\n"
"s_lshl_b32 s79, s79, 2\n"
"s_lshr_b32 s78, s2, 3\n"
"s_and_b32 s78, s78, 3\n"
"s_add_u32 s28, s79, s78\n"
"s_lshr_b32 s79, s2, 1\n"
"s_and_b32 s79, s79, 3\n"
"s_lshl_b32 s79, s79, 3\n"
"s_lshr_b32 s78, s2, 3\n"
"s_lshr_b32 s78, s78, 2\n"
"s_add_u32 s29, s79, s78\n"
"s_and_b32 s79, s2, 7\n"
"s_lshl_b32 s79, s79, 14\n"
"s_add_u32 s48, s79, 0x4000\n"
"s_mov_b32 s8, s22\n"
"s_mov_b32 s9, s23\n"
"s_mov_b32 s10, 0x280000\n"
"s_mov_b32 s11, 0x20000\n"
"s_mov_b32 s12, s24\n"
"s_mov_b32 s13, s25\n"
"s_mov_b32 s14, 0x280000\n"
"s_mov_b32 s15, 0x20000\n"
"s_mov_b32 s44, 0xcccccccc\n"
"s_mov_b32 s45, 0xcccccccc\n"
"s_mov_b32 s56, 0\n"
"s_mov_b32 s57, -2\n"
"s_mov_b32 s58, 0\n"
"s_mov_b32 s59, -1\n"
"s_lshl_b32 s79, s29, 1\n"
"s_lshr_b32 s78, s3, 1\n"
"s_add_u32 s79, s79, s78\n"
"s_mul_i32 s30, s79, 0x5000\n"
"s_and_b32 s79, s2, 7\n"
"s_lshl_b32 s79, s79, 14\n"
"s_add_u32 s81, s79, 0x3800\n"
"s_lshl_b32 s79, s28, 2\n"
"s_add_u32 s79, s79, s3\n"
"s_mul_i32 s79, s79, 0x6400\n"
"s_add_u32 s80, s79, 0x380000\n"
"s_lshr_b32 s79, s2, 1\n"
"s_and_b32 s79, s79, 3\n"
"s_lshl_b32 s79, s79, 2\n"
"s_lshr_b32 s78, s2, 3\n"
"s_lshr_b32 s78, s78, 3\n"
"s_add_u32 s79, s79, s78\n"
"s_lshl_b32 s79, s79, 2\n"
"s_add_u32 s79, s79, s3\n"
"s_mul_i32 s79, s79, 0x5000\n"
"s_and_b32 s78, s2, 1\n"
"s_lshl_b32 s78, s78, 3\n"
"s_lshr_b32 s77, s2, 3\n"
"s_and_b32 s77, s77, 7\n"
"s_add_u32 s78, s78, s77\n"
"s_mul_i32 s78, s78, 0x500\n"
"s_add_u32 s79, s79, s78\n"
"s_add_u32 s31, s79, 0x448000\n"
"s_and_b32 s73, s3, 1\n"
"v_and_b32 v78, 15, v0\n"
"v_lshrrev_b32 v79, 4, v0\n"
"s_lshl_b32 s74, s73, 30\n"
"v_mul_i32_i24 v99, 0x500, v78\n"
"v_lshl_add_u32 v14, v79, 3, v99\n"
"v_lshrrev_b32 v99, 1, v78\n"
"v_and_b32 v98, 1, v78\n"
"v_lshlrev_b32 v98, 7, v98\n"
"v_lshl_add_u32 v98, v79, 8, v98\n"
"v_lshl_add_u32 v17, v99, 4, v98\n"
"s_mov_b32 s79, 0x25400\n"
"s_cmp_eq_i32 s3, 0\n"
"s_cselect_b32 s79, s79, 0x21000\n"
"s_mov_b32 s78, 0x25c00\n"
"s_cmp_eq_i32 s3, 2\n"
"s_cselect_b32 s78, s78, 0x23400\n"
"s_cmp_lt_i32 s3, 2\n"
"s_cselect_b32 s79, s79, s78\n"
"v_add_u32 v6, s79, v2\n"
"s_mov_b32 s79, 0x40000000\n"
"s_cmp_eq_i32 s3, 0\n"
"s_cselect_b32 s79, s79, 0x20c00\n"
"s_mov_b32 s78, 0x40000000\n"
"s_cmp_eq_i32 s3, 2\n"
"s_cselect_b32 s78, s78, 0x23000\n"
"s_cmp_lt_i32 s3, 2\n"
"s_cselect_b32 s79, s79, s78\n"
"v_add_u32 v7, s79, v2\n"
"s_mov_b32 s79, 0x23c00\n"
"s_cmp_eq_i32 s3, 0\n"
"s_cselect_b32 s79, s79, 0x1f200\n"
"s_mov_b32 s78, 0x21800\n"
"s_cmp_eq_i32 s3, 2\n"
"s_cselect_b32 s78, s78, 0x1ce00\n"
"s_cmp_lt_i32 s3, 2\n"
"s_cselect_b32 s79, s79, s78\n"
"v_add_u32 v8, s79, v2\n"
"s_mov_b32 s79, 0x20400\n"
"s_cmp_eq_i32 s3, 0\n"
"s_cselect_b32 s79, s79, 0x24c00\n"
"s_mov_b32 s78, 0x1e000\n"
"s_cmp_eq_i32 s3, 2\n"
"s_cselect_b32 s78, s78, 0x22800\n"
"s_cmp_lt_i32 s3, 2\n"
"s_cselect_b32 s79, s79, s78\n"
"v_add_u32 v9, s79, v2\n"
"s_mov_b32 s79, 0x24400\n"
"s_cmp_eq_i32 s3, 0\n"
"s_cselect_b32 s79, s79, 0x24800\n"
"s_mov_b32 s78, 0x22000\n"
"s_cmp_eq_i32 s3, 2\n"
"s_cselect_b32 s78, s78, 0x22400\n"
"s_cmp_lt_i32 s3, 2\n"
"s_cselect_b32 s79, s79, s78\n"
"v_add_u32 v10, s79, v2\n"
"v_add_u32 v15, s74, v14\n"
"v_lshlrev_b32 v99, 3, v78\n"
"s_mov_b32 vcc_lo, 0x500005\n"
"s_mov_b32 vcc_hi, 0x50000500\n"
"v_cndmask_b32 v16, 2.0, v99, vcc\n"
"v_add_u32 v18, s74, v17\n"
"s_sub_u32 s79, s73, 1\n"
"v_mov_b32 v19, s79\n"
"v_lshrrev_b32 v99, 1, v79\n"
"v_and_b32 v98, 1, v79\n"
"v_and_b32 v97, 4, v78\n"
"v_and_b32 v96, 3, v78\n"
"v_lshrrev_b32 v95, 3, v78\n"
"v_lshlrev_b32 v95, 6, v95\n"
"v_lshl_add_u32 v96, v96, 4, v95\n"
"v_lshl_add_u32 v97, v97, 1, v96\n"
"v_lshl_add_u32 v98, v98, 8, v97\n"
"v_lshl_add_u32 v99, v99, 7, v98\n"
"v_sub_u32 v39, v99, v14\n"
"s_mov_b32 s79, 0x20c00\n"
"s_cmp_eq_i32 s3, 0\n"
"s_cselect_b32 s79, s79, 0x25400\n"
"s_mov_b32 s78, 0x23000\n"
"s_cmp_eq_i32 s3, 2\n"
"s_cselect_b32 s78, s78, 0x25c00\n"
"s_cmp_lt_i32 s3, 2\n"
"s_cselect_b32 s79, s79, s78\n"
"v_add_u32 v11, s79, v2\n"
"s_mov_b32 s79, 0x21800\n"
"s_cmp_eq_i32 s3, 0\n"
"s_cselect_b32 s79, s79, 0x1ce00\n"
"s_mov_b32 s78, 0x23c00\n"
"s_cmp_eq_i32 s3, 2\n"
"s_cselect_b32 s78, s78, 0x1f200\n"
"s_cmp_lt_i32 s3, 2\n"
"s_cselect_b32 s79, s79, s78\n"
"v_add_u32 v12, s79, v2\n"
"s_mov_b32 s79, 0x22400\n"
"s_cmp_eq_i32 s3, 0\n"
"s_cselect_b32 s79, s79, 0x1e000\n"
"s_mov_b32 s78, 0x24800\n"
"s_cmp_eq_i32 s3, 2\n"
"s_cselect_b32 s78, s78, 0x20400\n"
"s_cmp_lt_i32 s3, 2\n"
"s_cselect_b32 s79, s79, s78\n"
"v_add_u32 v13, s79, v2\n"
"v_mov_b32 v20, 0x3f803f80\n"
"v_mov_b32 v21, 0x3f803f80\n"
"v_mov_b32 v22, 0x3f803f80\n"
"v_mov_b32 v23, 0x3f803f80\n"
"s_mov_b32 s17, 0x100000\n"
".Ltp_306:\n"
"s_sleep 2\n"
"ds_read_b32 v3, v5 offset:16\n"
"s_sub_u32 s17, s17, 1\n"
"s_cbranch_scc1 .Lou_307\n"
"s_waitcnt lgkmcnt(0)\n"
"v_cmp_eq_u32 vcc, 0, v3\n"
"s_cbranch_vccnz .Ltp_306\n"
".Lou_307:\n"
"s_waitcnt lgkmcnt(0)\n"
"s_nop 2\n"
"s_mov_b32 s17, 32768\n"
".Ltp_308:\n"
"buffer_load_dwordx4 v[40:43], v38, s[4:7], s81 offen sc1\n"
"s_sub_u32 s17, s17, 1\n"
"s_cbranch_scc1 .Lou_309\n"
"s_waitcnt vmcnt(0)\n"
"v_xor_b32 v40, s16, v40\n"
"v_xor_b32 v41, s16, v41\n"
"v_xor_b32 v42, s16, v42\n"
"v_xor_b32 v43, s16, v43\n"
"v_or_b32 v40, v40, v41\n"
"v_or3_b32 v40, v40, v42, v43\n"
"v_cmp_ne_u32 vcc, 0, v40\n"
"s_cbranch_vccz .Lou_309\n"
"s_sleep 1\n"
"s_branch .Ltp_308\n"
".Lou_309:\n"
"s_waitcnt vmcnt(0)\n"
".Lpl_299:\n"
"s_mov_b32 s17, 0x100000\n"
".Ltp_310:\n"
"s_sleep 2\n"
"ds_read_b32 v3, v5 offset:32\n"
"s_sub_u32 s17, s17, 1\n"
"s_cbranch_scc1 .Lou_311\n"
"s_waitcnt lgkmcnt(0)\n"
"v_cmp_eq_u32 vcc, 0, v3\n"
"s_cbranch_vccnz .Ltp_310\n"
".Lou_311:\n"
"s_waitcnt lgkmcnt(0)\n"
"buffer_load_dwordx4 v[156:159], v2, s[4:7], s80 offen offset:0 sc1\n"
"buffer_load_dwordx4 v[160:163], v2, s[4:7], s80 offen offset:1024 sc1\n"
"buffer_load_dwordx4 v[164:167], v2, s[4:7], s80 offen offset:2048 sc1\n"
"buffer_load_dwordx4 v[168:171], v2, s[4:7], s80 offen offset:3072 sc1\n"
"s_add_u32 s80, s80, 4096\n"
"buffer_load_dwordx4 v[172:175], v2, s[4:7], s80 offen offset:0 sc1\n"
"buffer_load_dwordx4 v[176:179], v2, s[4:7], s80 offen offset:1024 sc1\n"
"s_waitcnt vmcnt(5)\n"
"buffer_load_dwordx4 v[180:183], v2, s[4:7], s80 offen offset:2048 sc1\n"
"s_waitcnt vmcnt(5)\n"
"buffer_load_dwordx4 v[184:187], v2, s[4:7], s80 offen offset:3072 sc1\n"
"s_add_u32 s80, s80, 4096\n"
"s_waitcnt vmcnt(5)\n"
"buffer_load_dwordx4 v[188:191], v2, s[4:7], s80 offen offset:0 sc1\n"
"s_waitcnt vmcnt(5)\n"
"buffer_load_dwordx4 v[192:195], v2, s[4:7], s80 offen offset:1024 sc1\n"
"s_waitcnt vmcnt(5)\n"
"buffer_load_dwordx4 v[196:199], v2, s[4:7], s80 offen offset:2048 sc1\n"
"s_waitcnt vmcnt(5)\n"
"buffer_load_dwordx4 v[200:203], v2, s[4:7], s80 offen offset:3072 sc1\n"
"s_add_u32 s80, s80, 4096\n"
"s_waitcnt vmcnt(5)\n"
"buffer_load_dwordx4 v[204:207], v2, s[4:7], s80 offen offset:0 sc1\n"
"s_waitcnt vmcnt(5)\n"
"buffer_load_dwordx4 v[208:211], v2, s[4:7], s80 offen offset:1024 sc1\n"
"s_waitcnt vmcnt(5)\n"
"buffer_load_dwordx4 v[212:215], v2, s[4:7], s80 offen offset:2048 sc1\n"
"s_waitcnt vmcnt(5)\n"
"buffer_load_dwordx4 v[216:219], v2, s[4:7], s80 offen offset:3072 sc1\n"
"s_add_u32 s80, s80, 4096\n"
"s_waitcnt vmcnt(5)\n"
"buffer_load_dwordx4 v[220:223], v2, s[4:7], s80 offen offset:0 sc1\n"
"s_waitcnt vmcnt(5)\n"
"buffer_load_dwordx4 v[224:227], v2, s[4:7], s80 offen offset:1024 sc1\n"
"s_waitcnt vmcnt(5)\n"
"buffer_load_dwordx4 v[228:231], v2, s[4:7], s80 offen offset:2048 sc1\n"
"s_waitcnt vmcnt(5)\n"
"buffer_load_dwordx4 v[232:235], v2, s[4:7], s80 offen offset:3072 sc1\n"
"s_add_u32 s80, s80, 4096\n"
"s_waitcnt vmcnt(5)\n"
"buffer_load_dwordx4 v[236:239], v2, s[4:7], s80 offen offset:0 sc1\n"
"s_waitcnt vmcnt(5)\n"
"buffer_load_dwordx4 v[240:243], v2, s[4:7], s80 offen offset:1024 sc1\n"
"s_waitcnt vmcnt(5)\n"
"buffer_load_dwordx4 v[244:247], v2, s[4:7], s80 offen offset:2048 sc1\n"
"s_waitcnt vmcnt(5)\n"
"buffer_load_dwordx4 v[248:251], v2, s[4:7], s80 offen offset:3072 sc1\n"
"s_add_u32 s80, s80, 4096\n"
"s_waitcnt vmcnt(5)\n"
"buffer_load_dwordx4 v[252:255], v2, s[4:7], s80 offen offset:0 sc1\n"
".Ljk_298:\n"
"s_cmp_lt_i32 s18, 0\n"
"s_cselect_b32 s43, 3, 0\n"
"s_setprio 2\n"
"v_mov_b32 v40, -1\n"
"v_mov_b32 v41, -1\n"
"v_mov_b32 v42, -1\n"
"v_mov_b32 v43, -1\n"
"s_lshl_b32 s79, s2, 2\n"
"s_add_u32 s79, s79, s3\n"
"s_lshl_b32 s79, s79, 10\n"
"s_add_u32 s49, s79, 0xc28000\n"
"buffer_store_dwordx4 v[40:43], v2, s[4:7], s49 offen sc1\n"
"s_add_u32 s49, s49, 0x100000\n"
"buffer_store_dwordx4 v[40:43], v2, s[4:7], s49 offen sc1\n"
"s_add_u32 s49, s49, 0x100000\n"
"s_sub_u32 s79, 0xea7fff, s49\n"
"s_and_b32 s79, s79, 0x80000000\n"
"s_or_b32 s49, s49, s79\n"
"buffer_store_dwordx4 v[40:43], v2, s[4:7], s49 offen sc1\n"
"s_nop 1\n"
"s_setprio 0\n"
"s_cmp_lg_u32 s43, 0\n"
"s_cbranch_scc1 .Lsk_330\n"
"ds_read_b32 v3, v5 offset:0\n"
"s_waitcnt lgkmcnt(0)\n"
"v_cmp_eq_u32 vcc, 0, v3\n"
"s_cbranch_vccz .Len_329\n"
"s_call_b64 s[46:47], .Lst_312\n"
".Lsk_330:\n"
"s_cmp_lg_u32 s43, 1\n"
"s_cbranch_scc1 .Lsk_331\n"
"ds_read_b32 v3, v5 offset:128\n"
"s_waitcnt lgkmcnt(0)\n"
"v_cmp_eq_u32 vcc, 0, v3\n"
"s_cbranch_vccz .Len_329\n"
"s_call_b64 s[46:47], .Lst_313\n"
".Lsk_331:\n"
"s_cmp_lg_u32 s43, 2\n"
"s_cbranch_scc1 .Lsk_332\n"
"ds_read_b32 v3, v5 offset:128\n"
"s_waitcnt lgkmcnt(0)\n"
"v_cmp_eq_u32 vcc, 0, v3\n"
"s_cbranch_vccz .Len_329\n"
"s_call_b64 s[46:47], .Lst_314\n"
".Lsk_332:\n"
".Len_329:\n"
"s_setprio 2\n"
"s_mul_i32 s79, s28, 0xa0\n"
"s_add_u32 s79, s30, s79\n"
"s_and_b32 s78, s3, 1\n"
"s_mul_i32 s78, s78, 0x60\n"
"s_add_u32 s36, s79, s78\n"
"s_mul_i32 s79, s3, 0x1400\n"
"s_add_u32 s79, s30, s79\n"
"s_add_u32 s32, s79, 0x448000\n"
"s_add_u32 s33, s32, 0x1000\n"
"s_lshl_b32 s79, s29, 1\n"
"s_lshr_b32 s78, s3, 1\n"
"s_xor_b32 s78, s78, 1\n"
"s_add_u32 s79, s79, s78\n"
"s_mul_i32 s79, s79, 0x5000\n"
"s_mul_i32 s78, s3, 0x1400\n"
"s_add_u32 s79, s79, s78\n"
"s_add_u32 s34, s79, 0x448000\n"
"s_add_u32 s35, s34, 0x1000\n"
"s_lshl_b32 s79, s30, 1\n"
"s_mul_i32 s78, s28, 0x1400\n"
"s_add_u32 s79, s79, s78\n"
"s_and_b32 s78, s3, 1\n"
"s_mul_i32 s78, s78, 0xc00\n"
"s_add_u32 s79, s79, s78\n"
"s_add_u32 s37, s79, 0x6c8000\n"
"s_lshl_b32 s79, s29, 1\n"
"s_lshr_b32 s78, s3, 1\n"
"s_add_u32 s79, s79, s78\n"
"s_lshl_b32 s79, s79, 11\n"
"s_lshl_b32 s78, s28, 8\n"
"s_add_u32 s79, s79, s78\n"
"s_and_b32 s78, s3, 1\n"
"s_lshl_b32 s78, s78, 7\n"
"s_add_u32 s79, s79, s78\n"
"s_add_u32 s38, s79, 0xbc8000\n"
"buffer_load_dwordx2 v[40:41], v14, s[8:11], s36 offen offset:0\n"
"buffer_load_dwordx2 v[42:43], v14, s[8:11], s36 offen offset:32\n"
"buffer_load_dwordx2 v[44:45], v15, s[8:11], s36 offen offset:64\n"
"s_mov_b32 s17, 0x100000\n"
".Ltp_333:\n"
"s_sleep 2\n"
"ds_read_b32 v3, v5 offset:96\n"
"s_sub_u32 s17, s17, 1\n"
"s_cbranch_scc1 .Lou_334\n"
"s_waitcnt lgkmcnt(0)\n"
"v_cmp_eq_u32 vcc, 0, v3\n"
"s_cbranch_vccnz .Ltp_333\n"
".Lou_334:\n"
"s_waitcnt lgkmcnt(0)\n"
"s_cmp_eq_u32 s17, 0xfffff\n"
"s_cbranch_scc1 .Lsk_335\n"
".Lsk_335:\n"
"v_lshlrev_b32 v38, 5, v0\n"
"s_waitcnt vmcnt(0)\n"
"s_nop 3\n"
"s_mov_b32 s17, 32768\n"
".Ltp_337:\n"
"buffer_load_dwordx4 v[88:91], v38, s[4:7], s48 offen sc1\n"
"s_sub_u32 s17, s17, 1\n"
"s_cbranch_scc1 .Lou_338\n"
"s_waitcnt vmcnt(0)\n"
"v_xor_b32 v88, s16, v88\n"
"v_xor_b32 v89, s16, v89\n"
"v_xor_b32 v90, s16, v90\n"
"v_xor_b32 v91, s16, v91\n"
"v_or_b32 v88, v88, v89\n"
"v_or3_b32 v88, v88, v90, v91\n"
"v_cmp_ne_u32 vcc, 0, v88\n"
"s_cbranch_vccz .Lou_338\n"
"s_mov_b32 s89, 6\n"
".Ldl_339:\n"
"s_sub_u32 s89, s89, 1\n"
"s_cbranch_scc1 .Lde_340\n"
"s_sleep 1\n"
"s_branch .Ldl_339\n"
".Lde_340:\n"
"s_branch .Ltp_337\n"
".Lou_338:\n"
"s_waitcnt vmcnt(0)\n"
"v_lshlrev_b32 v48, 16, v40\n"
"v_and_b32 v49, 0xffff0000, v40\n"
"v_lshlrev_b32 v50, 16, v41\n"
"v_and_b32 v51, 0xffff0000, v41\n"
"v_lshlrev_b32 v52, 16, v42\n"
"v_and_b32 v53, 0xffff0000, v42\n"
"v_lshlrev_b32 v54, 16, v43\n"
"v_and_b32 v55, 0xffff0000, v43\n"
"v_lshlrev_b32 v56, 16, v44\n"
"v_and_b32 v57, 0xffff0000, v44\n"
"v_lshlrev_b32 v58, 16, v45\n"
"v_and_b32 v59, 0xffff0000, v45\n"
"v_mov_b32 v46, 0\n"
"v_mov_b32 v47, 0\n"
"s_mov_b32 s17, 0x100000\n"
".Ltp_341:\n"
"s_sleep 1\n"
"ds_read_b32 v3, v5 offset:128\n"
"s_sub_u32 s17, s17, 1\n"
"s_cbranch_scc1 .Lou_342\n"
"s_waitcnt lgkmcnt(0)\n"
"v_cmp_eq_u32 vcc, 0, v3\n"
"s_cbranch_vccnz .Ltp_341\n"
".Lou_342:\n"
"s_waitcnt lgkmcnt(0)\n"
"s_cmp_eq_u32 s17, 0xfffff\n"
"s_cbranch_scc1 .Lsk_343\n"
"s_mov_b32 s89, 18\n"
".Ldl_345:\n"
"s_sub_u32 s89, s89, 1\n"
"s_cbranch_scc1 .Lde_346\n"
"s_sleep 1\n"
"s_branch .Ldl_345\n"
".Lde_346:\n"
"s_branch .Ljn_344\n"
".Lsk_343:\n"
".Ljn_344:\n"
"buffer_load_dwordx4 v[108:111], v2, s[4:7], s34 offen offset:0 sc1\n"
"s_nop 9\n"
"buffer_load_dwordx4 v[112:115], v2, s[4:7], s34 offen offset:1024 sc1\n"
"s_nop 9\n"
"buffer_load_dwordx4 v[116:119], v2, s[4:7], s34 offen offset:2048 sc1\n"
"s_nop 9\n"
"buffer_load_dwordx4 v[120:123], v2, s[4:7], s34 offen offset:3072 sc1\n"
"s_nop 9\n"
"buffer_load_dwordx4 v[124:127], v2, s[4:7], s35 offen offset:0 sc1\n"
"s_nop 9\n"
"buffer_load_dwordx4 v[88:91], v2, s[4:7], s32 offen offset:0 sc1\n"
"s_nop 9\n"
"buffer_load_dwordx4 v[92:95], v2, s[4:7], s32 offen offset:1024 sc1\n"
"s_nop 9\n"
"buffer_load_dwordx4 v[96:99], v2, s[4:7], s32 offen offset:2048 sc1\n"
"s_nop 9\n"
"buffer_load_dwordx4 v[100:103], v2, s[4:7], s32 offen offset:3072 sc1\n"
"s_nop 9\n"
"buffer_load_dwordx4 v[104:107], v2, s[4:7], s33 offen offset:0 sc1\n"
"s_waitcnt vmcnt(9)\n"
"v_max_u32 v38, v108, v109\n"
"v_max3_u32 v38, v110, v111, v38\n"
"v_cmp_eq_u32 vcc, -1, v38\n"
"s_cbranch_vccnz .Lsl_347\n"
".Lbk_348:\n"
"v_mfma_f32_16x16x32_bf16 v[68:71], v[156:159], v[108:111], 0\n"
"v_mfma_f32_16x16x32_bf16 v[72:75], v[160:163], v[108:111], 0\n"
"v_mfma_f32_16x16x32_bf16 v[76:79], v[164:167], v[108:111], 0\n"
"s_waitcnt vmcnt(8)\n"
"v_max_u32 v38, v112, v113\n"
"v_max3_u32 v38, v114, v115, v38\n"
"v_mfma_f32_16x16x32_bf16 v[80:83], v[168:171], v[108:111], 0\n"
"v_cmp_eq_u32 vcc, -1, v38\n"
"v_mfma_f32_16x16x32_bf16 v[84:87], v[172:175], v[108:111], 0\n"
"s_cbranch_vccnz .Lsl_349\n"
".Lbk_350:\n"
"v_mfma_f32_16x16x32_bf16 v[68:71], v[176:179], v[112:115], v[68:71]\n"
"v_mfma_f32_16x16x32_bf16 v[72:75], v[180:183], v[112:115], v[72:75]\n"
"v_mfma_f32_16x16x32_bf16 v[76:79], v[184:187], v[112:115], v[76:79]\n"
"s_waitcnt vmcnt(7)\n"
"v_max_u32 v38, v116, v117\n"
"v_max3_u32 v38, v118, v119, v38\n"
"v_mfma_f32_16x16x32_bf16 v[80:83], v[188:191], v[112:115], v[80:83]\n"
"v_cmp_eq_u32 vcc, -1, v38\n"
"v_mfma_f32_16x16x32_bf16 v[84:87], v[192:195], v[112:115], v[84:87]\n"
"s_cbranch_vccnz .Lsl_351\n"
".Lbk_352:\n"
"v_mfma_f32_16x16x32_bf16 v[68:71], v[196:199], v[116:119], v[68:71]\n"
"v_mfma_f32_16x16x32_bf16 v[72:75], v[200:203], v[116:119], v[72:75]\n"
"v_mfma_f32_16x16x32_bf16 v[76:79], v[204:207], v[116:119], v[76:79]\n"
"s_waitcnt vmcnt(6)\n"
"v_max_u32 v38, v120, v121\n"
"v_max3_u32 v38, v122, v123, v38\n"
"v_mfma_f32_16x16x32_bf16 v[80:83], v[208:211], v[116:119], v[80:83]\n"
"v_cmp_eq_u32 vcc, -1, v38\n"
"v_mfma_f32_16x16x32_bf16 v[84:87], v[212:215], v[116:119], v[84:87]\n"
"s_cbranch_vccnz .Lsl_353\n"
".Lbk_354:\n"
"v_mfma_f32_16x16x32_bf16 v[68:71], v[216:219], v[120:123], v[68:71]\n"
"v_mfma_f32_16x16x32_bf16 v[72:75], v[220:223], v[120:123], v[72:75]\n"
"v_mfma_f32_16x16x32_bf16 v[76:79], v[224:227], v[120:123], v[76:79]\n"
"s_waitcnt vmcnt(5)\n"
"v_max_u32 v38, v124, v125\n"
"v_max3_u32 v38, v126, v127, v38\n"
"v_mfma_f32_16x16x32_bf16 v[80:83], v[228:231], v[120:123], v[80:83]\n"
"v_cmp_eq_u32 vcc, -1, v38\n"
"v_mfma_f32_16x16x32_bf16 v[84:87], v[232:235], v[120:123], v[84:87]\n"
"s_cbranch_vccnz .Lsl_355\n"
".Lbk_356:\n"
"v_mfma_f32_16x16x32_bf16 v[68:71], v[236:239], v[124:127], v[68:71]\n"
"v_mfma_f32_16x16x32_bf16 v[72:75], v[240:243], v[124:127], v[72:75]\n"
"v_mfma_f32_16x16x32_bf16 v[76:79], v[244:247], v[124:127], v[76:79]\n"
"s_waitcnt vmcnt(4)\n"
"v_max_u32 v38, v88, v89\n"
"v_max3_u32 v38, v90, v91, v38\n"
"v_mfma_f32_16x16x32_bf16 v[80:83], v[248:251], v[124:127], v[80:83]\n"
"v_cmp_eq_u32 vcc, -1, v38\n"
"v_mfma_f32_16x16x32_bf16 v[84:87], v[252:255], v[124:127], v[84:87]\n"
"ds_write_b128 v8, v[68:71] offset:0\n"
"s_cbranch_vccnz .Lsl_357\n"
".Lbk_358:\n"
"v_mfma_f32_16x16x32_bf16 v[56:59], v[164:167], v[88:91], v[56:59]\n"
"s_waitcnt vmcnt(3)\n"
"v_max_u32 v38, v92, v93\n"
"v_max3_u32 v38, v94, v95, v38\n"
"ds_write_b128 v8, v[72:75] offset:1024\n"
"v_mfma_f32_16x16x32_bf16 v[60:63], v[168:171], v[88:91], 0\n"
"v_cmp_eq_u32 vcc, -1, v38\n"
"ds_write_b128 v10, v[76:79] offset:0\n"
"v_mfma_f32_16x16x32_bf16 v[64:67], v[172:175], v[88:91], 0\n"
"v_mov_b32 v117, s16\n"
"s_cbranch_vccnz .Lsl_359\n"
".Lbk_360:\n"
"v_mfma_f32_16x16x32_bf16 v[56:59], v[184:187], v[92:95], v[56:59]\n"
"s_waitcnt vmcnt(2)\n"
"v_max_u32 v38, v96, v97\n"
"v_max3_u32 v38, v98, v99, v38\n"
"v_mov_b32 v119, s16\n"
"v_mfma_f32_16x16x32_bf16 v[60:63], v[188:191], v[92:95], v[60:63]\n"
"v_cmp_eq_u32 vcc, -1, v38\n"
"ds_write_b128 v9, v[80:83] offset:0\n"
"v_mfma_f32_16x16x32_bf16 v[64:67], v[192:195], v[92:95], v[64:67]\n"
"ds_write_b128 v9, v[84:87] offset:1024\n"
"s_cbranch_vccnz .Lsl_361\n"
".Lbk_362:\n"
"v_mfma_f32_16x16x32_bf16 v[56:59], v[204:207], v[96:99], v[56:59]\n"
"s_waitcnt vmcnt(1)\n"
"v_max_u32 v38, v100, v101\n"
"v_max3_u32 v38, v102, v103, v38\n"
"ds_write_b32 v4, v4 offset:624\n"
"v_mfma_f32_16x16x32_bf16 v[60:63], v[208:211], v[96:99], v[60:63]\n"
"v_cmp_eq_u32 vcc, -1, v38\n"
"v_mov_b32 v121, s16\n"
"v_mov_b32 v123, s16\n"
"v_mfma_f32_16x16x32_bf16 v[64:67], v[212:215], v[96:99], v[64:67]\n"
"v_mov_b32 v125, s16\n"
"s_cbranch_vccnz .Lsl_363\n"
".Lbk_364:\n"
"v_mfma_f32_16x16x32_bf16 v[56:59], v[224:227], v[100:103], v[56:59]\n"
"s_waitcnt vmcnt(0)\n"
"v_max_u32 v38, v104, v105\n"
"v_max3_u32 v38, v106, v107, v38\n"
"v_mov_b32 v127, s16\n"
"v_mfma_f32_16x16x32_bf16 v[60:63], v[228:231], v[100:103], v[60:63]\n"
"v_cmp_eq_u32 vcc, -1, v38\n"
"ds_read_b32 v3, v5 offset:624\n"
"v_mfma_f32_16x16x32_bf16 v[64:67], v[232:235], v[100:103], v[64:67]\n"
"s_cbranch_vccnz .Lsl_365\n"
".Lbk_366:\n"
"v_mfma_f32_16x16x32_bf16 v[56:59], v[244:247], v[104:107], v[56:59]\n"
"ds_read_b128 v[76:79], v12 offset:2048\n"
"ds_read_b128 v[108:111], v13 offset:2048\n"
"v_mfma_f32_16x16x32_bf16 v[60:63], v[248:251], v[104:107], v[60:63]\n"
"ds_read_b128 v[68:71], v12 offset:0\n"
"ds_read_b128 v[80:83], v13 offset:0\n"
"v_mfma_f32_16x16x32_bf16 v[64:67], v[252:255], v[104:107], v[64:67]\n"
"ds_read_b128 v[72:75], v12 offset:1024\n"
"ds_read_b128 v[84:87], v13 offset:1024\n"
"v_mfma_f32_16x16x32_bf16 v[48:51], v[156:159], v[88:91], v[48:51]\n"
"ds_write_b128 v7, v[56:59] offset:0\n"
"v_mfma_f32_16x16x32_bf16 v[52:55], v[160:163], v[88:91], v[52:55]\n"
"ds_write_b128 v6, v[60:63] offset:0\n"
"v_mfma_f32_16x16x32_bf16 v[48:51], v[176:179], v[92:95], v[48:51]\n"
"ds_write_b128 v6, v[64:67] offset:1024\n"
"v_mfma_f32_16x16x32_bf16 v[52:55], v[180:183], v[92:95], v[52:55]\n"
"ds_write_b32 v4, v4 offset:640\n"
"v_mfma_f32_16x16x32_bf16 v[48:51], v[196:199], v[96:99], v[48:51]\n"
"s_mov_b32 s17, 0x100000\n"
"s_waitcnt lgkmcnt(10)\n"
"v_cmp_eq_u32 vcc, 0, v3\n"
"s_cbranch_vccz .Lok_368\n"
".Ltp_367:\n"
"ds_read_b32 v3, v5 offset:624\n"
"ds_read_b128 v[76:79], v12 offset:2048\n"
"ds_read_b128 v[108:111], v13 offset:2048\n"
"ds_read_b128 v[68:71], v12 offset:0\n"
"ds_read_b128 v[80:83], v13 offset:0\n"
"ds_read_b128 v[72:75], v12 offset:1024\n"
"ds_read_b128 v[84:87], v13 offset:1024\n"
"s_sub_u32 s17, s17, 1\n"
"s_cbranch_scc1 .Lok_368\n"
"s_waitcnt lgkmcnt(6)\n"
"v_cmp_eq_u32 vcc, 0, v3\n"
"s_cbranch_vccnz .Ltp_367\n"
"s_waitcnt lgkmcnt(0)\n"
".Lok_368:\n"
"v_mfma_f32_16x16x32_bf16 v[52:55], v[200:203], v[96:99], v[52:55]\n"
"s_waitcnt lgkmcnt(8)\n"
"v_add_f32 v76, v76, v108\n"
"v_add_f32 v77, v77, v109\n"
"v_add_f32 v78, v78, v110\n"
"v_mfma_f32_16x16x32_bf16 v[48:51], v[216:219], v[100:103], v[48:51]\n"
"v_add_f32 v79, v79, v111\n"
"s_waitcnt lgkmcnt(6)\n"
"v_add_f32 v68, v68, v80\n"
"v_add_f32 v69, v69, v81\n"
"v_mfma_f32_16x16x32_bf16 v[52:55], v[220:223], v[100:103], v[52:55]\n"
"v_add_f32 v70, v70, v82\n"
"v_add_f32 v71, v71, v83\n"
"s_waitcnt lgkmcnt(4)\n"
"v_add_f32 v72, v72, v84\n"
"v_mfma_f32_16x16x32_bf16 v[48:51], v[236:239], v[104:107], v[48:51]\n"
"v_add_f32 v73, v73, v85\n"
"v_add_f32 v74, v74, v86\n"
"v_add_f32 v75, v75, v87\n"
"v_mfma_f32_16x16x32_bf16 v[52:55], v[240:243], v[104:107], v[52:55]\n"
"ds_read_b32 v3, v5 offset:640\n"
"ds_read_b128 v[112:115], v11 offset:2048\n"
"ds_read_b128 v[60:63], v11 offset:0\n"
"ds_read_b128 v[64:67], v11 offset:1024\n"
"v_add_f32 v56, v56, v76\n"
"v_add_f32 v57, v57, v77\n"
"v_add_f32 v58, v58, v78\n"
"v_add_f32 v59, v59, v79\n"
"v_add_f32 v48, v48, v68\n"
"v_add_f32 v49, v49, v69\n"
"v_add_f32 v50, v50, v70\n"
"v_add_f32 v51, v51, v71\n"
"v_add_f32 v52, v52, v72\n"
"v_add_f32 v53, v53, v73\n"
"v_add_f32 v54, v54, v74\n"
"v_add_f32 v55, v55, v75\n"
"s_mov_b32 s17, 0x100000\n"
"s_waitcnt lgkmcnt(3)\n"
"v_cmp_eq_u32 vcc, 0, v3\n"
"s_cbranch_vccz .Lok_370\n"
".Ltp_369:\n"
"ds_read_b32 v3, v5 offset:640\n"
"ds_read_b128 v[112:115], v11 offset:2048\n"
"ds_read_b128 v[60:63], v11 offset:0\n"
"ds_read_b128 v[64:67], v11 offset:1024\n"
"s_sub_u32 s17, s17, 1\n"
"s_cbranch_scc1 .Lok_370\n"
"s_waitcnt lgkmcnt(3)\n"
"v_cmp_eq_u32 vcc, 0, v3\n"
"s_cbranch_vccnz .Ltp_369\n"
"s_waitcnt lgkmcnt(0)\n"
".Lok_370:\n"
"s_waitcnt lgkmcnt(2)\n"
"v_add_f32 v56, v56, v112\n"
"v_add_f32 v57, v57, v113\n"
"v_add_f32 v58, v58, v114\n"
"v_add_f32 v59, v59, v115\n"
"v_cvt_pk_bf16_f32 v44, v56, v57\n"
"v_cvt_pk_bf16_f32 v45, v58, v59\n"
"v_and_b32 v44, v19, v44\n"
"v_and_b32 v45, v19, v45\n"
"s_waitcnt lgkmcnt(1)\n"
"v_add_f32 v48, v48, v60\n"
"v_add_f32 v49, v49, v61\n"
"v_add_f32 v50, v50, v62\n"
"v_add_f32 v51, v51, v63\n"
"v_cvt_pk_bf16_f32 v40, v48, v49\n"
"v_cvt_pk_bf16_f32 v41, v50, v51\n"
"v_mfma_f32_16x16x32_bf16 v[88:91], v[44:47], v[44:47], 0\n"
"v_mfma_f32_16x16x32_bf16 v[92:95], v[20:23], v[44:47], 0\n"
"s_waitcnt lgkmcnt(0)\n"
"v_add_f32 v52, v52, v64\n"
"v_add_f32 v53, v53, v65\n"
"v_add_f32 v54, v54, v66\n"
"v_add_f32 v55, v55, v67\n"
"v_cvt_pk_bf16_f32 v42, v52, v53\n"
"v_cvt_pk_bf16_f32 v43, v54, v55\n"
"v_cvt_pk_bf16_f32 v116, v48, v49\n"
"v_cvt_pk_bf16_f32 v118, v50, v51\n"
"v_mfma_f32_16x16x32_bf16 v[88:91], v[40:43], v[40:43], v[88:91]\n"
"v_mfma_f32_16x16x32_bf16 v[92:95], v[20:23], v[40:43], v[92:95]\n"
"v_cvt_pk_bf16_f32 v120, v52, v53\n"
"v_cvt_pk_bf16_f32 v122, v54, v55\n"
"v_cvt_pk_bf16_f32 v124, v56, v57\n"
"v_cvt_pk_bf16_f32 v126, v58, v59\n"
"s_nop 0\n"
"v_cndmask_b32 v101, v88, v90, s[44:45]\n"
"v_cndmask_b32 v88, v89, v91, s[44:45]\n"
"s_nop 0\n"
"v_min_u32 v100, -2, v92\n"
"v_min_u32 v88, -2, v88\n"
"v_min_u32 v101, -2, v101\n"
"v_mov_b32_dpp v102, v100 quad_perm:[1,0,3,2] row_mask:0xf bank_mask:0xf\n"
"v_mov_b32_dpp v103, v88 quad_perm:[1,0,3,2] row_mask:0xf bank_mask:0xf\n"
"buffer_store_dwordx4 v[100:103], v16, s[4:7], s38 offen sc1\n"
"buffer_store_dwordx4 v[116:119], v17, s[4:7], s37 offen offset:0 sc1\n"
"buffer_store_dwordx4 v[120:123], v17, s[4:7], s37 offen offset:1024 sc1\n"
"buffer_store_dwordx4 v[124:127], v18, s[4:7], s37 offen offset:2048 sc1\n"
"ds_write_b32 v4, v4 offset:176\n"
"s_xor_b32 s79, s3, 1\n"
"s_lshl_b32 s79, s79, 8\n"
"s_add_u32 s32, s19, s79\n"
"s_cmp_ge_u32 s32, 64\n"
"s_cbranch_scc1 .Lnr_371\n"
"s_lshl_b32 s79, s19, 11\n"
"s_add_u32 s32, s79, 0xbc8000\n"
"s_lshl_b32 s79, s19, 7\n"
"s_add_u32 s33, s79, 0xc08000\n"
"v_lshrrev_b32 v99, 3, v0\n"
"v_and_b32 v98, 7, v0\n"
"v_lshlrev_b32 v98, 4, v98\n"
"v_lshl_add_u32 v40, v99, 14, v98\n"
"v_mov_b32 v41, 0x3727c5ac\n"
"s_mov_b32 s89, 15\n"
".Ldl_372:\n"
"s_sub_u32 s89, s89, 1\n"
"s_cbranch_scc1 .Lde_373\n"
"s_sleep 1\n"
"s_branch .Ldl_372\n"
".Lde_373:\n"
"buffer_load_dwordx4 v[48:51], v2, s[4:7], s32 offen offset:0 sc1\n"
"buffer_load_dwordx4 v[52:55], v2, s[4:7], s32 offen offset:1024 sc1\n"
"s_mov_b32 s17, 32768\n"
".Ltp_374:\n"
"s_waitcnt vmcnt(0)\n"
"s_sub_u32 s17, s17, 1\n"
"s_cbranch_scc1 .Lou_375\n"
"v_max_u32 v38, v48, v49\n"
"v_max3_u32 v38, v50, v51, v38\n"
"v_max3_u32 v38, v52, v53, v38\n"
"v_max3_u32 v38, v54, v55, v38\n"
"v_cmp_eq_u32 vcc, -1, v38\n"
"s_cbranch_vccz .Lou_375\n"
"v_max_u32 v38, v50, v51\n"
"v_max3_u32 v38, v48, v49, v38\n"
"v_cmp_eq_u32 vcc, -1, v38\n"
"s_cbranch_vccz .Lsk_376\n"
"s_mov_b64 exec, vcc\n"
"buffer_load_dwordx4 v[48:51], v2, s[4:7], s32 offen offset:0 sc1\n"
"s_mov_b64 exec, -1\n"
".Lsk_376:\n"
"v_max_u32 v38, v54, v55\n"
"v_max3_u32 v38, v52, v53, v38\n"
"v_cmp_eq_u32 vcc, -1, v38\n"
"s_cbranch_vccz .Lsk_377\n"
"s_mov_b64 exec, vcc\n"
"buffer_load_dwordx4 v[52:55], v2, s[4:7], s32 offen offset:1024 sc1\n"
"s_mov_b64 exec, -1\n"
".Lsk_377:\n"
"s_branch .Ltp_374\n"
".Lou_375:\n"
"s_waitcnt vmcnt(0)\n"
"v_add_f32 v48, v48, v52\n"
"v_add_f32 v49, v49, v53\n"
"v_add_f32 v50, v50, v54\n"
"v_add_f32 v51, v51, v55\n"
"v_add_f32_dpp v52, v48, v48 row_ror:8 row_mask:0xf bank_mask:0xf\n"
"v_add_f32_dpp v53, v49, v49 row_ror:8 row_mask:0xf bank_mask:0xf\n"
"v_add_f32_dpp v54, v50, v50 row_ror:8 row_mask:0xf bank_mask:0xf\n"
"v_add_f32_dpp v55, v51, v51 row_ror:8 row_mask:0xf bank_mask:0xf\n"
"v_mov_b32 v48, v52\n"
"v_mov_b32 v49, v53\n"
"v_mov_b32 v50, v54\n"
"v_mov_b32 v51, v55\n"
"v_permlane16_swap_b32 v48, v52\n"
"v_permlane16_swap_b32 v49, v53\n"
"v_permlane16_swap_b32 v50, v54\n"
"v_permlane16_swap_b32 v51, v55\n"
"v_add_f32 v52, v48, v52\n"
"v_add_f32 v53, v49, v53\n"
"v_add_f32 v54, v50, v54\n"
"v_add_f32 v55, v51, v55\n"
"v_mov_b32 v48, v52\n"
"v_mov_b32 v49, v53\n"
"v_mov_b32 v50, v54\n"
"v_mov_b32 v51, v55\n"
"v_permlane32_swap_b32 v48, v52\n"
"v_permlane32_swap_b32 v49, v53\n"
"v_permlane32_swap_b32 v50, v54\n"
"v_permlane32_swap_b32 v51, v55\n"
"v_add_f32 v52, v48, v52\n"
"v_add_f32 v53, v49, v53\n"
"v_add_f32 v54, v50, v54\n"
"v_add_f32 v55, v51, v55\n"
"v_mul_f32 v48, 0x3acccccd, v52\n"
"v_mul_f32 v50, 0x3acccccd, v54\n"
"v_fma_f32 v53, -v52, v48, v53\n"
"v_fma_f32 v55, -v54, v50, v55\n"
"v_fmamk_f32 v53, v53, 0x3acccccd, v41\n"
"v_fmamk_f32 v55, v55, 0x3acccccd, v41\n"
"v_max_f32 v53, v53, v41\n"
"v_max_f32 v55, v55, v41\n"
"v_rsq_f32 v49, v53\n"
"v_rsq_f32 v51, v55\n"
"v_mul_f32 v48, -v48, v49\n"
"v_mul_f32 v50, -v50, v51\n"
"v_min_u32 v49, -2, v49\n"
"v_min_u32 v51, -2, v51\n"
"v_min_u32 v48, -2, v48\n"
"v_min_u32 v50, -2, v50\n"
"buffer_store_dwordx4 v[48:51], v40, s[4:7], s33 offen sc1\n"
"s_nop 3\n"
".Lnr_371:\n"
"v_lshlrev_b32 v99, 4, v0\n"
"v_mov_b32 v98, 0x40000000\n"
"v_cmp_gt_i32 vcc, 16, v0\n"
"s_nop 1\n"
"v_cndmask_b32 v43, v98, v99, vcc\n"
"v_mov_b32 v44, -1\n"
"v_mov_b32 v45, -1\n"
"v_mov_b32 v46, -1\n"
"v_mov_b32 v47, -1\n"
"s_add_u32 s49, s31, 0xa60000\n"
"buffer_store_dwordx4 v[44:47], v2, s[4:7], s49 offen offset:0 sc1\n"
"buffer_store_dwordx4 v[44:47], v43, s[4:7], s49 offen offset:1024 sc1\n"
"s_add_u32 s49, s31, 0xba0000\n"
"buffer_store_dwordx4 v[44:47], v2, s[4:7], s49 offen offset:0 sc1\n"
"buffer_store_dwordx4 v[44:47], v43, s[4:7], s49 offen offset:1024 sc1\n"
"s_lshl_b32 s79, s2, 2\n"
"s_add_u32 s79, s79, s3\n"
"s_lshl_b32 s79, s79, 10\n"
"s_add_u32 s49, s79, 0x1628000\n"
"s_sub_u32 s79, 0x1687fff, s49\n"
"s_and_b32 s79, s79, 0x80000000\n"
"s_or_b32 s49, s49, s79\n"
"buffer_store_dwordx4 v[44:47], v2, s[4:7], s49 offen sc1\n"
"s_nop 1\n"
"s_setprio 0\n"
"s_cmp_lg_u32 s43, 0\n"
"s_cbranch_scc1 .Lsk_379\n"
"s_call_b64 s[46:47], .Lst_312\n"
".Lsk_379:\n"
"s_cmp_lg_u32 s43, 1\n"
"s_cbranch_scc1 .Lsk_380\n"
"s_call_b64 s[46:47], .Lst_313\n"
".Lsk_380:\n"
"s_cmp_lg_u32 s43, 2\n"
"s_cbranch_scc1 .Lsk_381\n"
"ds_read_b32 v3, v5 offset:160\n"
"s_waitcnt lgkmcnt(0)\n"
"v_cmp_eq_u32 vcc, 0, v3\n"
"s_cbranch_vccz .Len_378\n"
"s_call_b64 s[46:47], .Lst_314\n"
".Lsk_381:\n"
".Len_378:\n"
"s_setprio 2\n"
"s_mul_i32 s79, s28, 0xa0\n"
"s_add_u32 s79, s30, s79\n"
"s_and_b32 s78, s3, 1\n"
"s_mul_i32 s78, s78, 0x60\n"
"s_add_u32 s79, s79, s78\n"
"s_add_u32 s36, s79, 0x140000\n"
"s_mul_i32 s79, s3, 0x1400\n"
"s_add_u32 s79, s30, s79\n"
"s_add_u32 s32, s79, 0x588000\n"
"s_add_u32 s33, s32, 0x1000\n"
"s_lshl_b32 s79, s29, 1\n"
"s_lshr_b32 s78, s3, 1\n"
"s_xor_b32 s78, s78, 1\n"
"s_add_u32 s79, s79, s78\n"
"s_mul_i32 s79, s79, 0x5000\n"
"s_mul_i32 s78, s3, 0x1400\n"
"s_add_u32 s79, s79, s78\n"
"s_add_u32 s34, s79, 0x588000\n"
"s_add_u32 s35, s34, 0x1000\n"
"s_lshl_b32 s79, s30, 1\n"
"s_mul_i32 s78, s28, 0x1400\n"
"s_add_u32 s79, s79, s78\n"
"s_and_b32 s78, s3, 1\n"
"s_mul_i32 s78, s78, 0xc00\n"
"s_add_u32 s79, s79, s78\n"
"s_add_u32 s37, s79, 0x948000\n"
"s_lshl_b32 s79, s29, 1\n"
"s_lshr_b32 s78, s3, 1\n"
"s_add_u32 s79, s79, s78\n"
"s_lshl_b32 s79, s79, 11\n"
"s_lshl_b32 s78, s28, 8\n"
"s_add_u32 s79, s79, s78\n"
"s_and_b32 s78, s3, 1\n"
"s_lshl_b32 s78, s78, 7\n"
"s_add_u32 s79, s79, s78\n"
"s_add_u32 s38, s79, 0xbe8000\n"
"buffer_load_dwordx2 v[40:41], v14, s[8:11], s36 offen offset:0\n"
"buffer_load_dwordx2 v[42:43], v14, s[8:11], s36 offen offset:32\n"
"buffer_load_dwordx2 v[44:45], v15, s[8:11], s36 offen offset:64\n"
"s_mov_b32 s17, 0x100000\n"
".Ltp_382:\n"
"ds_read_b32 v3, v5 offset:176\n"
"s_sub_u32 s17, s17, 1\n"
"s_cbranch_scc1 .Lou_383\n"
"s_waitcnt lgkmcnt(0)\n"
"v_cmp_eq_u32 vcc, 0, v3\n"
"s_cbranch_vccnz .Ltp_382\n"
".Lou_383:\n"
"s_waitcnt lgkmcnt(0)\n"
"s_waitcnt vmcnt(2)\n"
"v_lshlrev_b32 v48, 16, v40\n"
"v_and_b32 v49, 0xffff0000, v40\n"
"v_lshlrev_b32 v50, 16, v41\n"
"v_and_b32 v51, 0xffff0000, v41\n"
"s_waitcnt vmcnt(1)\n"
"v_lshlrev_b32 v52, 16, v42\n"
"v_and_b32 v53, 0xffff0000, v42\n"
"v_lshlrev_b32 v54, 16, v43\n"
"v_and_b32 v55, 0xffff0000, v43\n"
"s_waitcnt vmcnt(0)\n"
"v_lshlrev_b32 v56, 16, v44\n"
"v_and_b32 v57, 0xffff0000, v44\n"
"v_lshlrev_b32 v58, 16, v45\n"
"v_and_b32 v59, 0xffff0000, v45\n"
"v_mov_b32 v46, 0\n"
"v_mov_b32 v47, 0\n"
"s_mov_b32 s17, 0x100000\n"
".Ltp_384:\n"
"s_sleep 1\n"
"ds_read_b32 v3, v5 offset:160\n"
"s_sub_u32 s17, s17, 1\n"
"s_cbranch_scc1 .Lou_385\n"
"s_waitcnt lgkmcnt(0)\n"
"v_cmp_eq_u32 vcc, 0, v3\n"
"s_cbranch_vccnz .Ltp_384\n"
".Lou_385:\n"
"s_waitcnt lgkmcnt(0)\n"
"s_cmp_eq_u32 s17, 0xfffff\n"
"s_cbranch_scc1 .Lsk_386\n"
"s_mov_b32 s89, 24\n"
".Ldl_388:\n"
"s_sub_u32 s89, s89, 1\n"
"s_cbranch_scc1 .Lde_389\n"
"s_sleep 1\n"
"s_branch .Ldl_388\n"
".Lde_389:\n"
"s_branch .Ljn_387\n"
".Lsk_386:\n"
".Ljn_387:\n"
"buffer_load_dwordx4 v[108:111], v2, s[4:7], s34 offen offset:0 sc1\n"
"s_nop 9\n"
"buffer_load_dwordx4 v[112:115], v2, s[4:7], s34 offen offset:1024 sc1\n"
"s_nop 9\n"
"buffer_load_dwordx4 v[116:119], v2, s[4:7], s34 offen offset:2048 sc1\n"
"s_nop 9\n"
"buffer_load_dwordx4 v[120:123], v2, s[4:7], s34 offen offset:3072 sc1\n"
"s_nop 9\n"
"buffer_load_dwordx4 v[124:127], v2, s[4:7], s35 offen offset:0 sc1\n"
"s_nop 9\n"
"buffer_load_dwordx4 v[88:91], v2, s[4:7], s32 offen offset:0 sc1\n"
"s_nop 9\n"
"buffer_load_dwordx4 v[92:95], v2, s[4:7], s32 offen offset:1024 sc1\n"
"s_nop 9\n"
"buffer_load_dwordx4 v[96:99], v2, s[4:7], s32 offen offset:2048 sc1\n"
"s_nop 9\n"
"buffer_load_dwordx4 v[100:103], v2, s[4:7], s32 offen offset:3072 sc1\n"
"s_nop 9\n"
"buffer_load_dwordx4 v[104:107], v2, s[4:7], s33 offen offset:0 sc1\n"
"s_waitcnt vmcnt(9)\n"
"v_max_u32 v38, v108, v109\n"
"v_max3_u32 v38, v110, v111, v38\n"
"v_cmp_eq_u32 vcc, -1, v38\n"
"s_cbranch_vccnz .Lsl_390\n"
".Lbk_391:\n"
"v_mfma_f32_16x16x32_bf16 v[68:71], v[156:159], v[108:111], 0\n"
"v_mfma_f32_16x16x32_bf16 v[72:75], v[160:163], v[108:111], 0\n"
"v_mfma_f32_16x16x32_bf16 v[76:79], v[164:167], v[108:111], 0\n"
"s_waitcnt vmcnt(8)\n"
"v_max_u32 v38, v112, v113\n"
"v_max3_u32 v38, v114, v115, v38\n"
"v_mfma_f32_16x16x32_bf16 v[80:83], v[168:171], v[108:111], 0\n"
"v_cmp_eq_u32 vcc, -1, v38\n"
"v_mfma_f32_16x16x32_bf16 v[84:87], v[172:175], v[108:111], 0\n"
"s_cbranch_vccnz .Lsl_392\n"
".Lbk_393:\n"
"v_mfma_f32_16x16x32_bf16 v[68:71], v[176:179], v[112:115], v[68:71]\n"
"v_mfma_f32_16x16x32_bf16 v[72:75], v[180:183], v[112:115], v[72:75]\n"
"v_mfma_f32_16x16x32_bf16 v[76:79], v[184:187], v[112:115], v[76:79]\n"
"s_waitcnt vmcnt(7)\n"
"v_max_u32 v38, v116, v117\n"
"v_max3_u32 v38, v118, v119, v38\n"
"v_mfma_f32_16x16x32_bf16 v[80:83], v[188:191], v[112:115], v[80:83]\n"
"v_cmp_eq_u32 vcc, -1, v38\n"
"v_mfma_f32_16x16x32_bf16 v[84:87], v[192:195], v[112:115], v[84:87]\n"
"s_cbranch_vccnz .Lsl_394\n"
".Lbk_395:\n"
"v_mfma_f32_16x16x32_bf16 v[68:71], v[196:199], v[116:119], v[68:71]\n"
"v_mfma_f32_16x16x32_bf16 v[72:75], v[200:203], v[116:119], v[72:75]\n"
"v_mfma_f32_16x16x32_bf16 v[76:79], v[204:207], v[116:119], v[76:79]\n"
"s_waitcnt vmcnt(6)\n"
"v_max_u32 v38, v120, v121\n"
"v_max3_u32 v38, v122, v123, v38\n"
"v_mfma_f32_16x16x32_bf16 v[80:83], v[208:211], v[116:119], v[80:83]\n"
"v_cmp_eq_u32 vcc, -1, v38\n"
"v_mfma_f32_16x16x32_bf16 v[84:87], v[212:215], v[116:119], v[84:87]\n"
"s_cbranch_vccnz .Lsl_396\n"
".Lbk_397:\n"
"v_mfma_f32_16x16x32_bf16 v[68:71], v[216:219], v[120:123], v[68:71]\n"
"v_mfma_f32_16x16x32_bf16 v[72:75], v[220:223], v[120:123], v[72:75]\n"
"v_mfma_f32_16x16x32_bf16 v[76:79], v[224:227], v[120:123], v[76:79]\n"
"s_waitcnt vmcnt(5)\n"
"v_max_u32 v38, v124, v125\n"
"v_max3_u32 v38, v126, v127, v38\n"
"v_mfma_f32_16x16x32_bf16 v[80:83], v[228:231], v[120:123], v[80:83]\n"
"v_cmp_eq_u32 vcc, -1, v38\n"
"v_mfma_f32_16x16x32_bf16 v[84:87], v[232:235], v[120:123], v[84:87]\n"
"s_cbranch_vccnz .Lsl_398\n"
".Lbk_399:\n"
"v_mfma_f32_16x16x32_bf16 v[68:71], v[236:239], v[124:127], v[68:71]\n"
"v_mfma_f32_16x16x32_bf16 v[72:75], v[240:243], v[124:127], v[72:75]\n"
"v_mfma_f32_16x16x32_bf16 v[76:79], v[244:247], v[124:127], v[76:79]\n"
"s_waitcnt vmcnt(4)\n"
"v_max_u32 v38, v88, v89\n"
"v_max3_u32 v38, v90, v91, v38\n"
"v_mfma_f32_16x16x32_bf16 v[80:83], v[248:251], v[124:127], v[80:83]\n"
"v_cmp_eq_u32 vcc, -1, v38\n"
"v_mfma_f32_16x16x32_bf16 v[84:87], v[252:255], v[124:127], v[84:87]\n"
"ds_write_b128 v8, v[68:71] offset:0\n"
"s_cbranch_vccnz .Lsl_400\n"
".Lbk_401:\n"
"v_mfma_f32_16x16x32_bf16 v[56:59], v[164:167], v[88:91], v[56:59]\n"
"s_waitcnt vmcnt(3)\n"
"v_max_u32 v38, v92, v93\n"
"v_max3_u32 v38, v94, v95, v38\n"
"ds_write_b128 v8, v[72:75] offset:1024\n"
"v_mfma_f32_16x16x32_bf16 v[60:63], v[168:171], v[88:91], 0\n"
"v_cmp_eq_u32 vcc, -1, v38\n"
"ds_write_b128 v10, v[76:79] offset:0\n"
"v_mfma_f32_16x16x32_bf16 v[64:67], v[172:175], v[88:91], 0\n"
"v_mov_b32 v117, s16\n"
"s_cbranch_vccnz .Lsl_402\n"
".Lbk_403:\n"
"v_mfma_f32_16x16x32_bf16 v[56:59], v[184:187], v[92:95], v[56:59]\n"
"s_waitcnt vmcnt(2)\n"
"v_max_u32 v38, v96, v97\n"
"v_max3_u32 v38, v98, v99, v38\n"
"v_mov_b32 v119, s16\n"
"v_mfma_f32_16x16x32_bf16 v[60:63], v[188:191], v[92:95], v[60:63]\n"
"v_cmp_eq_u32 vcc, -1, v38\n"
"ds_write_b128 v9, v[80:83] offset:0\n"
"v_mfma_f32_16x16x32_bf16 v[64:67], v[192:195], v[92:95], v[64:67]\n"
"ds_write_b128 v9, v[84:87] offset:1024\n"
"s_cbranch_vccnz .Lsl_404\n"
".Lbk_405:\n"
"v_mfma_f32_16x16x32_bf16 v[56:59], v[204:207], v[96:99], v[56:59]\n"
"s_waitcnt vmcnt(1)\n"
"v_max_u32 v38, v100, v101\n"
"v_max3_u32 v38, v102, v103, v38\n"
"ds_write_b32 v4, v4 offset:656\n"
"v_mfma_f32_16x16x32_bf16 v[60:63], v[208:211], v[96:99], v[60:63]\n"
"v_cmp_eq_u32 vcc, -1, v38\n"
"v_mov_b32 v121, s16\n"
"v_mov_b32 v123, s16\n"
"v_mfma_f32_16x16x32_bf16 v[64:67], v[212:215], v[96:99], v[64:67]\n"
"v_mov_b32 v125, s16\n"
"s_cbranch_vccnz .Lsl_406\n"
".Lbk_407:\n"
"v_mfma_f32_16x16x32_bf16 v[56:59], v[224:227], v[100:103], v[56:59]\n"
"s_waitcnt vmcnt(0)\n"
"v_max_u32 v38, v104, v105\n"
"v_max3_u32 v38, v106, v107, v38\n"
"v_mov_b32 v127, s16\n"
"v_mfma_f32_16x16x32_bf16 v[60:63], v[228:231], v[100:103], v[60:63]\n"
"v_cmp_eq_u32 vcc, -1, v38\n"
"ds_read_b32 v3, v5 offset:656\n"
"v_mfma_f32_16x16x32_bf16 v[64:67], v[232:235], v[100:103], v[64:67]\n"
"s_cbranch_vccnz .Lsl_408\n"
".Lbk_409:\n"
"v_mfma_f32_16x16x32_bf16 v[56:59], v[244:247], v[104:107], v[56:59]\n"
"ds_read_b128 v[76:79], v12 offset:2048\n"
"ds_read_b128 v[108:111], v13 offset:2048\n"
"v_mfma_f32_16x16x32_bf16 v[60:63], v[248:251], v[104:107], v[60:63]\n"
"ds_read_b128 v[68:71], v12 offset:0\n"
"ds_read_b128 v[80:83], v13 offset:0\n"
"v_mfma_f32_16x16x32_bf16 v[64:67], v[252:255], v[104:107], v[64:67]\n"
"ds_read_b128 v[72:75], v12 offset:1024\n"
"ds_read_b128 v[84:87], v13 offset:1024\n"
"v_mfma_f32_16x16x32_bf16 v[48:51], v[156:159], v[88:91], v[48:51]\n"
"ds_write_b128 v7, v[56:59] offset:0\n"
"v_mfma_f32_16x16x32_bf16 v[52:55], v[160:163], v[88:91], v[52:55]\n"
"ds_write_b128 v6, v[60:63] offset:0\n"
"v_mfma_f32_16x16x32_bf16 v[48:51], v[176:179], v[92:95], v[48:51]\n"
"ds_write_b128 v6, v[64:67] offset:1024\n"
"v_mfma_f32_16x16x32_bf16 v[52:55], v[180:183], v[92:95], v[52:55]\n"
"ds_write_b32 v4, v4 offset:672\n"
"v_mfma_f32_16x16x32_bf16 v[48:51], v[196:199], v[96:99], v[48:51]\n"
"s_mov_b32 s17, 0x100000\n"
"s_waitcnt lgkmcnt(10)\n"
"v_cmp_eq_u32 vcc, 0, v3\n"
"s_cbranch_vccz .Lok_411\n"
".Ltp_410:\n"
"ds_read_b32 v3, v5 offset:656\n"
"ds_read_b128 v[76:79], v12 offset:2048\n"
"ds_read_b128 v[108:111], v13 offset:2048\n"
"ds_read_b128 v[68:71], v12 offset:0\n"
"ds_read_b128 v[80:83], v13 offset:0\n"
"ds_read_b128 v[72:75], v12 offset:1024\n"
"ds_read_b128 v[84:87], v13 offset:1024\n"
"s_sub_u32 s17, s17, 1\n"
"s_cbranch_scc1 .Lok_411\n"
"s_waitcnt lgkmcnt(6)\n"
"v_cmp_eq_u32 vcc, 0, v3\n"
"s_cbranch_vccnz .Ltp_410\n"
"s_waitcnt lgkmcnt(0)\n"
".Lok_411:\n"
"v_mfma_f32_16x16x32_bf16 v[52:55], v[200:203], v[96:99], v[52:55]\n"
"s_waitcnt lgkmcnt(8)\n"
"v_add_f32 v76, v76, v108\n"
"v_add_f32 v77, v77, v109\n"
"v_add_f32 v78, v78, v110\n"
"v_mfma_f32_16x16x32_bf16 v[48:51], v[216:219], v[100:103], v[48:51]\n"
"v_add_f32 v79, v79, v111\n"
"s_waitcnt lgkmcnt(6)\n"
"v_add_f32 v68, v68, v80\n"
"v_add_f32 v69, v69, v81\n"
"v_mfma_f32_16x16x32_bf16 v[52:55], v[220:223], v[100:103], v[52:55]\n"
"v_add_f32 v70, v70, v82\n"
"v_add_f32 v71, v71, v83\n"
"s_waitcnt lgkmcnt(4)\n"
"v_add_f32 v72, v72, v84\n"
"v_mfma_f32_16x16x32_bf16 v[48:51], v[236:239], v[104:107], v[48:51]\n"
"v_add_f32 v73, v73, v85\n"
"v_add_f32 v74, v74, v86\n"
"v_add_f32 v75, v75, v87\n"
"v_mfma_f32_16x16x32_bf16 v[52:55], v[240:243], v[104:107], v[52:55]\n"
"ds_read_b32 v3, v5 offset:672\n"
"ds_read_b128 v[112:115], v11 offset:2048\n"
"ds_read_b128 v[60:63], v11 offset:0\n"
"ds_read_b128 v[64:67], v11 offset:1024\n"
"v_add_f32 v56, v56, v76\n"
"v_add_f32 v57, v57, v77\n"
"v_add_f32 v58, v58, v78\n"
"v_add_f32 v59, v59, v79\n"
"v_add_f32 v48, v48, v68\n"
"v_add_f32 v49, v49, v69\n"
"v_add_f32 v50, v50, v70\n"
"v_add_f32 v51, v51, v71\n"
"v_add_f32 v52, v52, v72\n"
"v_add_f32 v53, v53, v73\n"
"v_add_f32 v54, v54, v74\n"
"v_add_f32 v55, v55, v75\n"
"s_mov_b32 s17, 0x100000\n"
"s_waitcnt lgkmcnt(3)\n"
"v_cmp_eq_u32 vcc, 0, v3\n"
"s_cbranch_vccz .Lok_413\n"
".Ltp_412:\n"
"ds_read_b32 v3, v5 offset:672\n"
"ds_read_b128 v[112:115], v11 offset:2048\n"
"ds_read_b128 v[60:63], v11 offset:0\n"
"ds_read_b128 v[64:67], v11 offset:1024\n"
"s_sub_u32 s17, s17, 1\n"
"s_cbranch_scc1 .Lok_413\n"
"s_waitcnt lgkmcnt(3)\n"
"v_cmp_eq_u32 vcc, 0, v3\n"
"s_cbranch_vccnz .Ltp_412\n"
"s_waitcnt lgkmcnt(0)\n"
".Lok_413:\n"
"s_waitcnt lgkmcnt(2)\n"
"v_add_f32 v56, v56, v112\n"
"v_add_f32 v57, v57, v113\n"
"v_add_f32 v58, v58, v114\n"
"v_add_f32 v59, v59, v115\n"
"v_cvt_pk_bf16_f32 v44, v56, v57\n"
"v_cvt_pk_bf16_f32 v45, v58, v59\n"
"v_and_b32 v44, v19, v44\n"
"v_and_b32 v45, v19, v45\n"
"s_waitcnt lgkmcnt(1)\n"
"v_add_f32 v48, v48, v60\n"
"v_add_f32 v49, v49, v61\n"
"v_add_f32 v50, v50, v62\n"
"v_add_f32 v51, v51, v63\n"
"v_cvt_pk_bf16_f32 v40, v48, v49\n"
"v_cvt_pk_bf16_f32 v41, v50, v51\n"
"v_mfma_f32_16x16x32_bf16 v[88:91], v[44:47], v[44:47], 0\n"
"v_mfma_f32_16x16x32_bf16 v[92:95], v[20:23], v[44:47], 0\n"
"s_waitcnt lgkmcnt(0)\n"
"v_add_f32 v52, v52, v64\n"
"v_add_f32 v53, v53, v65\n"
"v_add_f32 v54, v54, v66\n"
"v_add_f32 v55, v55, v67\n"
"v_cvt_pk_bf16_f32 v42, v52, v53\n"
"v_cvt_pk_bf16_f32 v43, v54, v55\n"
"v_cvt_pk_bf16_f32 v116, v48, v49\n"
"v_cvt_pk_bf16_f32 v118, v50, v51\n"
"v_mfma_f32_16x16x32_bf16 v[88:91], v[40:43], v[40:43], v[88:91]\n"
"v_mfma_f32_16x16x32_bf16 v[92:95], v[20:23], v[40:43], v[92:95]\n"
"v_cvt_pk_bf16_f32 v120, v52, v53\n"
"v_cvt_pk_bf16_f32 v122, v54, v55\n"
"v_cvt_pk_bf16_f32 v124, v56, v57\n"
"v_cvt_pk_bf16_f32 v126, v58, v59\n"
"s_nop 0\n"
"v_cndmask_b32 v101, v88, v90, s[44:45]\n"
"v_cndmask_b32 v88, v89, v91, s[44:45]\n"
"s_nop 0\n"
"v_min_u32 v100, -2, v92\n"
"v_min_u32 v88, -2, v88\n"
"v_min_u32 v101, -2, v101\n"
"v_mov_b32_dpp v102, v100 quad_perm:[1,0,3,2] row_mask:0xf bank_mask:0xf\n"
"v_mov_b32_dpp v103, v88 quad_perm:[1,0,3,2] row_mask:0xf bank_mask:0xf\n"
"buffer_store_dwordx4 v[100:103], v16, s[4:7], s38 offen sc1\n"
"buffer_store_dwordx4 v[116:119], v17, s[4:7], s37 offen offset:0 sc1\n"
"buffer_store_dwordx4 v[120:123], v17, s[4:7], s37 offen offset:1024 sc1\n"
"buffer_store_dwordx4 v[124:127], v18, s[4:7], s37 offen offset:2048 sc1\n"
"ds_write_b32 v4, v4 offset:256\n"
"s_xor_b32 s79, s3, 1\n"
"s_lshl_b32 s79, s79, 8\n"
"s_add_u32 s32, s19, s79\n"
"s_cmp_ge_u32 s32, 64\n"
"s_cbranch_scc1 .Lnr_414\n"
"s_lshl_b32 s79, s19, 11\n"
"s_add_u32 s32, s79, 0xbe8000\n"
"s_lshl_b32 s79, s19, 7\n"
"s_add_u32 s33, s79, 0xc0a000\n"
"v_lshrrev_b32 v99, 3, v0\n"
"v_and_b32 v98, 7, v0\n"
"v_lshlrev_b32 v98, 4, v98\n"
"v_lshl_add_u32 v40, v99, 14, v98\n"
"v_mov_b32 v41, 0x3727c5ac\n"
"s_mov_b32 s89, 12\n"
".Ldl_415:\n"
"s_sub_u32 s89, s89, 1\n"
"s_cbranch_scc1 .Lde_416\n"
"s_sleep 1\n"
"s_branch .Ldl_415\n"
".Lde_416:\n"
"buffer_load_dwordx4 v[48:51], v2, s[4:7], s32 offen offset:0 sc1\n"
"buffer_load_dwordx4 v[52:55], v2, s[4:7], s32 offen offset:1024 sc1\n"
"s_mov_b32 s17, 32768\n"
".Ltp_417:\n"
"s_waitcnt vmcnt(0)\n"
"s_sub_u32 s17, s17, 1\n"
"s_cbranch_scc1 .Lou_418\n"
"v_max_u32 v38, v48, v49\n"
"v_max3_u32 v38, v50, v51, v38\n"
"v_max3_u32 v38, v52, v53, v38\n"
"v_max3_u32 v38, v54, v55, v38\n"
"v_cmp_eq_u32 vcc, -1, v38\n"
"s_cbranch_vccz .Lou_418\n"
"v_max_u32 v38, v50, v51\n"
"v_max3_u32 v38, v48, v49, v38\n"
"v_cmp_eq_u32 vcc, -1, v38\n"
"s_cbranch_vccz .Lsk_419\n"
"s_mov_b64 exec, vcc\n"
"buffer_load_dwordx4 v[48:51], v2, s[4:7], s32 offen offset:0 sc1\n"
"s_mov_b64 exec, -1\n"
".Lsk_419:\n"
"v_max_u32 v38, v54, v55\n"
"v_max3_u32 v38, v52, v53, v38\n"
"v_cmp_eq_u32 vcc, -1, v38\n"
"s_cbranch_vccz .Lsk_420\n"
"s_mov_b64 exec, vcc\n"
"buffer_load_dwordx4 v[52:55], v2, s[4:7], s32 offen offset:1024 sc1\n"
"s_mov_b64 exec, -1\n"
".Lsk_420:\n"
"s_branch .Ltp_417\n"
".Lou_418:\n"
"s_waitcnt vmcnt(0)\n"
"v_add_f32 v48, v48, v52\n"
"v_add_f32 v49, v49, v53\n"
"v_add_f32 v50, v50, v54\n"
"v_add_f32 v51, v51, v55\n"
"v_add_f32_dpp v52, v48, v48 row_ror:8 row_mask:0xf bank_mask:0xf\n"
"v_add_f32_dpp v53, v49, v49 row_ror:8 row_mask:0xf bank_mask:0xf\n"
"v_add_f32_dpp v54, v50, v50 row_ror:8 row_mask:0xf bank_mask:0xf\n"
"v_add_f32_dpp v55, v51, v51 row_ror:8 row_mask:0xf bank_mask:0xf\n"
"v_mov_b32 v48, v52\n"
"v_mov_b32 v49, v53\n"
"v_mov_b32 v50, v54\n"
"v_mov_b32 v51, v55\n"
"v_permlane16_swap_b32 v48, v52\n"
"v_permlane16_swap_b32 v49, v53\n"
"v_permlane16_swap_b32 v50, v54\n"
"v_permlane16_swap_b32 v51, v55\n"
"v_add_f32 v52, v48, v52\n"
"v_add_f32 v53, v49, v53\n"
"v_add_f32 v54, v50, v54\n"
"v_add_f32 v55, v51, v55\n"
"v_mov_b32 v48, v52\n"
"v_mov_b32 v49, v53\n"
"v_mov_b32 v50, v54\n"
"v_mov_b32 v51, v55\n"
"v_permlane32_swap_b32 v48, v52\n"
"v_permlane32_swap_b32 v49, v53\n"
"v_permlane32_swap_b32 v50, v54\n"
"v_permlane32_swap_b32 v51, v55\n"
"v_add_f32 v52, v48, v52\n"
"v_add_f32 v53, v49, v53\n"
"v_add_f32 v54, v50, v54\n"
"v_add_f32 v55, v51, v55\n"
"v_mul_f32 v48, 0x3acccccd, v52\n"
"v_mul_f32 v50, 0x3acccccd, v54\n"
"v_fma_f32 v53, -v52, v48, v53\n"
"v_fma_f32 v55, -v54, v50, v55\n"
"v_fmamk_f32 v53, v53, 0x3acccccd, v41\n"
"v_fmamk_f32 v55, v55, 0x3acccccd, v41\n"
"v_max_f32 v53, v53, v41\n"
"v_max_f32 v55, v55, v41\n"
"v_rsq_f32 v49, v53\n"
"v_rsq_f32 v51, v55\n"
"v_mul_f32 v48, -v48, v49\n"
"v_mul_f32 v50, -v50, v51\n"
"v_min_u32 v49, -2, v49\n"
"v_min_u32 v51, -2, v51\n"
"v_min_u32 v48, -2, v48\n"
"v_min_u32 v50, -2, v50\n"
"buffer_store_dwordx4 v[48:51], v40, s[4:7], s33 offen sc1\n"
"s_nop 3\n"
".Lnr_414:\n"
"s_cmp_lt_i32 s18, 0\n"
"s_cbranch_scc1 .Lnc_421\n"
"s_nop 0\n"
"s_setprio 0\n"
"s_cmp_lg_u32 s43, 0\n"
"s_cbranch_scc1 .Lsk_423\n"
"s_call_b64 s[46:47], .Lst_312\n"
".Lsk_423:\n"
"s_cmp_lg_u32 s43, 1\n"
"s_cbranch_scc1 .Lsk_424\n"
"s_call_b64 s[46:47], .Lst_313\n"
".Lsk_424:\n"
"s_cmp_lg_u32 s43, 2\n"
"s_cbranch_scc1 .Lsk_425\n"
"s_call_b64 s[46:47], .Lst_314\n"
".Lsk_425:\n"
".Len_422:\n"
"s_setprio 2\n"
"s_mov_b32 s17, 0x100000\n"
".Ltp_426:\n"
"s_sleep 1\n"
"ds_read_b32 v3, v5 offset:208\n"
"s_sub_u32 s17, s17, 1\n"
"s_cbranch_scc1 .Lou_427\n"
"s_waitcnt lgkmcnt(0)\n"
"v_cmp_eq_u32 vcc, 0, v3\n"
"s_cbranch_vccnz .Ltp_426\n"
".Lou_427:\n"
"s_waitcnt lgkmcnt(0)\n"
"s_setprio 0\n"
"s_lshl_b32 s79, s18, 8\n"
"s_add_u32 s32, s79, 0x948000\n"
"s_add_u32 s33, s32, 0x140000\n"
"s_and_b32 s79, s2, 7\n"
"s_lshl_b32 s79, s79, 14\n"
"s_add_u32 s34, s79, 0xc0a000\n"
"s_add_u32 s35, s34, 0x1000\n"
"v_and_b32 v126, 0xffffff80, v29\n"
"v_add_u32 v126, v126, v29\n"
"ds_read_b128 v[88:91], v25 offset:448\n"
"ds_read_b128 v[92:95], v25 offset:432\n"
"ds_read_b64 v[96:97], v24 offset:448\n"
"ds_read_b64 v[98:99], v24 offset:456\n"
"ds_read_b128 v[100:103], v25 offset:960\n"
"ds_read_b128 v[104:107], v25 offset:1472\n"
"s_waitcnt lgkmcnt(1)\n"
"v_mfma_f32_16x16x32_f16 v[64:67], v[88:91], v[144:147], 0\n"
"ds_read_b128 v[40:43], v25 offset:1984\n"
"ds_read_b64 v[44:45], v24 offset:960\n"
"v_mfma_f32_16x16x32_f16 v[68:71], v[92:95], v[148:151], 0\n"
"ds_read_b64 v[46:47], v24 offset:968\n"
"ds_read_b64 v[72:73], v24 offset:1472\n"
"v_mfma_f32_16x16x32_f16 v[48:51], v[152:155], v[88:91], 0\n"
"ds_read_b64 v[74:75], v24 offset:1480\n"
"ds_read_b64 v[76:77], v24 offset:384\n"
"v_mfma_f32_16x16x32_f16 v[48:51], v[96:99], v[100:103], v[48:51]\n"
"ds_read_b64 v[78:79], v24 offset:392\n"
"s_waitcnt lgkmcnt(4)\n"
"v_mfma_f32_16x16x32_f16 v[52:55], v[96:99], v[104:107], 0\n"
"ds_read_b128 v[84:87], v25 offset:896\n"
"ds_read_b128 v[92:95], v25 offset:1408\n"
"v_mfma_f32_16x16x32_f16 v[56:59], v[96:99], v[40:43], 0\n"
"ds_read_b128 v[88:91], v25 offset:1920\n"
"ds_read_b64 v[100:101], v24 offset:896\n"
"v_mfma_f32_16x16x32_f16 v[52:55], v[44:47], v[40:43], v[52:55]\n"
"ds_read_b64 v[102:103], v24 offset:904\n"
"ds_read_b64 v[96:97], v24 offset:1408\n"
"v_cvt_pk_f16_f32 v80, v64, v65\n"
"v_mfma_f32_16x16x32_f16 v[48:51], v[44:47], v[104:107], v[48:51]\n"
"ds_read_b64 v[98:99], v24 offset:1416\n"
"s_waitcnt lgkmcnt(4)\n"
"v_mfma_f32_16x16x32_f16 v[48:51], v[72:75], v[40:43], v[48:51]\n"
"ds_read_b128 v[104:107], v25 offset:960\n"
"v_cvt_pk_f16_f32 v81, v66, v67\n"
"v_mfma_f32_16x16x32_f16 v[48:51], v[76:79], v[84:87], v[48:51]\n"
"ds_read_b128 v[44:47], v25 offset:944\n"
"v_cvt_pk_f16_f32 v82, v68, v69\n"
"v_mfma_f32_16x16x32_f16 v[52:55], v[76:79], v[92:95], v[52:55]\n"
"ds_read_b64 v[40:41], v24 offset:320\n"
"ds_read_b64 v[42:43], v24 offset:328\n"
"v_mfma_f32_16x16x32_f16 v[56:59], v[76:79], v[88:91], v[56:59]\n"
"ds_read_b128 v[72:75], v25 offset:832\n"
"s_waitcnt lgkmcnt(5)\n"
"v_mfma_f32_16x16x32_f16 v[52:55], v[100:103], v[88:91], v[52:55]\n"
"ds_read_b128 v[84:87], v25 offset:1344\n"
"v_cvt_pk_f16_f32 v83, v70, v71\n"
"v_mfma_f32_16x16x32_f16 v[48:51], v[100:103], v[92:95], v[48:51]\n"
"ds_read_b128 v[76:79], v25 offset:1856\n"
"ds_read_b64 v[92:93], v24 offset:832\n"
"v_mfma_f32_16x16x32_f16 v[48:51], v[96:99], v[88:91], v[48:51]\n"
"ds_read_b64 v[94:95], v24 offset:840\n"
"v_mfma_f32_16x16x32_f16 v[48:51], v[80:83], v[128:131], v[48:51]\n"
"ds_read_b64 v[100:101], v24 offset:1344\n"
"s_waitcnt lgkmcnt(9)\n"
"v_mfma_f32_16x16x32_f16 v[52:55], v[80:83], v[132:135], v[52:55]\n"
"ds_read_b64 v[102:103], v24 offset:1352\n"
"ds_read_b64 v[88:89], v24 offset:256\n"
"v_mfma_f32_16x16x32_f16 v[56:59], v[80:83], v[136:139], v[56:59]\n"
"ds_read_b64 v[90:91], v24 offset:264\n"
"v_mfma_f32_16x16x32_f16 v[60:63], v[80:83], v[140:143], 0\n"
"ds_read_b128 v[96:99], v25 offset:768\n"
"v_mfma_f32_16x16x32_f16 v[64:67], v[104:107], v[144:147], 0\n"
"s_waitcnt lgkmcnt(8)\n"
"v_mfma_f32_16x16x32_f16 v[68:71], v[44:47], v[148:151], 0\n"
"ds_read_b128 v[44:47], v25 offset:1280\n"
"v_mfma_f32_16x16x32_f16 v[52:55], v[152:155], v[104:107], v[52:55]\n"
"ds_read_b128 v[104:107], v25 offset:1792\n"
"v_mfma_f32_16x16x32_f16 v[48:51], v[40:43], v[72:75], v[48:51]\n"
"ds_read_b64 v[72:73], v24 offset:768\n"
"v_mfma_f32_16x16x32_f16 v[52:55], v[40:43], v[84:87], v[52:55]\n"
"ds_read_b64 v[74:75], v24 offset:776\n"
"s_waitcnt lgkmcnt(7)\n"
"v_mfma_f32_16x16x32_f16 v[56:59], v[40:43], v[76:79], v[56:59]\n"
"ds_read_b64 v[40:41], v24 offset:1280\n"
"v_mfma_f32_16x16x32_f16 v[52:55], v[92:95], v[76:79], v[52:55]\n"
"ds_read_b64 v[42:43], v24 offset:1288\n"
"v_cvt_pk_f16_f32 v80, v64, v65\n"
"v_mfma_f32_16x16x32_f16 v[48:51], v[92:95], v[84:87], v[48:51]\n"
"ds_read_b128 v[84:87], v25 offset:1472\n"
"v_cvt_pk_f16_f32 v81, v66, v67\n"
"v_mfma_f32_16x16x32_f16 v[48:51], v[100:103], v[76:79], v[48:51]\n"
"ds_read_b128 v[92:95], v25 offset:1456\n"
"s_waitcnt lgkmcnt(4)\n"
"v_mfma_f32_16x16x32_f16 v[48:51], v[88:91], v[96:99], v[48:51]\n"
"ds_read_b64 v[76:77], v24 offset:192\n"
"ds_read_b64 v[78:79], v24 offset:200\n"
"v_mfma_f32_16x16x32_f16 v[52:55], v[88:91], v[44:47], v[52:55]\n"
"ds_read_b128 v[100:103], v25 offset:704\n"
"v_cvt_pk_f16_f32 v82, v68, v69\n"
"v_mfma_f32_16x16x32_f16 v[56:59], v[88:91], v[104:107], v[56:59]\n"
"ds_read_b128 v[96:99], v25 offset:1216\n"
"v_cvt_pk_f16_f32 v83, v70, v71\n"
"v_mfma_f32_16x16x32_f16 v[52:55], v[72:75], v[104:107], v[52:55]\n"
"ds_read_b128 v[88:91], v25 offset:1728\n"
"s_waitcnt lgkmcnt(7)\n"
"v_mfma_f32_16x16x32_f16 v[48:51], v[72:75], v[44:47], v[48:51]\n"
"ds_read_b64 v[44:45], v24 offset:704\n"
"v_mfma_f32_16x16x32_f16 v[48:51], v[40:43], v[104:107], v[48:51]\n"
"ds_read_b64 v[46:47], v24 offset:712\n"
"ds_read_b64 v[72:73], v24 offset:1216\n"
"v_mfma_f32_16x16x32_f16 v[52:55], v[80:83], v[128:131], v[52:55]\n"
"ds_read_b64 v[74:75], v24 offset:1224\n"
"ds_read_b64 v[104:105], v24 offset:128\n"
"v_mfma_f32_16x16x32_f16 v[56:59], v[80:83], v[132:135], v[56:59]\n"
"ds_read_b64 v[106:107], v24 offset:136\n"
"s_waitcnt lgkmcnt(11)\n"
"v_mfma_f32_16x16x32_f16 v[60:63], v[80:83], v[136:139], v[60:63]\n"
"ds_read_b128 v[40:43], v25 offset:640\n"
"v_mfma_f32_16x16x32_f16 v[64:67], v[84:87], v[144:147], 0\n"
"v_mfma_f32_16x16x32_f16 v[68:71], v[92:95], v[148:151], 0\n"
"ds_read_b128 v[92:95], v25 offset:1152\n"
"v_mfma_f32_16x16x32_f16 v[56:59], v[152:155], v[84:87], v[56:59]\n"
"ds_read_b128 v[84:87], v25 offset:1664\n"
"s_waitcnt lgkmcnt(7)\n"
"v_mfma_f32_16x16x32_f16 v[48:51], v[76:79], v[100:103], v[48:51]\n"
"ds_read_b64 v[100:101], v24 offset:640\n"
"v_mfma_f32_16x16x32_f16 v[52:55], v[76:79], v[96:99], v[52:55]\n"
"ds_read_b64 v[102:103], v24 offset:648\n"
"v_mfma_f32_16x16x32_f16 v[56:59], v[76:79], v[88:91], v[56:59]\n"
"ds_read_b64 v[76:77], v24 offset:1152\n"
"v_mfma_f32_16x16x32_f16 v[52:55], v[44:47], v[88:91], v[52:55]\n"
"ds_read_b64 v[78:79], v24 offset:1160\n"
"s_waitcnt lgkmcnt(5)\n"
"v_mfma_f32_16x16x32_f16 v[48:51], v[44:47], v[96:99], v[48:51]\n"
"ds_read_b128 v[96:99], v25 offset:1984\n"
"v_cvt_pk_f16_f32 v80, v64, v65\n"
"v_mfma_f32_16x16x32_f16 v[48:51], v[72:75], v[88:91], v[48:51]\n"
"ds_read_b128 v[44:47], v25 offset:1968\n"
"ds_read_b64 v[88:89], v24 offset:64\n"
"v_mfma_f32_16x16x32_f16 v[48:51], v[104:107], v[40:43], v[48:51]\n"
"ds_read_b64 v[90:91], v24 offset:72\n"
"ds_read_b128 v[72:75], v25 offset:576\n"
"v_mfma_f32_16x16x32_f16 v[52:55], v[104:107], v[92:95], v[52:55]\n"
"ds_read_b128 v[40:43], v25 offset:1088\n"
"s_waitcnt lgkmcnt(6)\n"
"v_mfma_f32_16x16x32_f16 v[56:59], v[104:107], v[84:87], v[56:59]\n"
"ds_read_b128 v[104:107], v25 offset:1600\n"
"v_cvt_pk_f16_f32 v81, v66, v67\n"
"v_mfma_f32_16x16x32_f16 v[52:55], v[100:103], v[84:87], v[52:55]\n"
"v_cvt_pk_f16_f32 v82, v68, v69\n"
"v_cvt_pk_f16_f32 v83, v70, v71\n"
"v_mfma_f32_16x16x32_f16 v[48:51], v[100:103], v[92:95], v[48:51]\n"
"ds_read_b64 v[92:93], v24 offset:576\n"
"ds_read_b64 v[94:95], v24 offset:584\n"
"v_mfma_f32_16x16x32_f16 v[48:51], v[76:79], v[84:87], v[48:51]\n"
"ds_read_b64 v[100:101], v24 offset:1088\n"
"s_waitcnt lgkmcnt(8)\n"
"v_mfma_f32_16x16x32_f16 v[56:59], v[80:83], v[128:131], v[56:59]\n"
"ds_read_b64 v[102:103], v24 offset:1096\n"
"ds_read_b64 v[84:85], v24 offset:0\n"
"v_mfma_f32_16x16x32_f16 v[60:63], v[80:83], v[132:135], v[60:63]\n"
"ds_read_b64 v[86:87], v24 offset:8\n"
"v_mfma_f32_16x16x32_f16 v[64:67], v[96:99], v[144:147], 0\n"
"ds_read_b128 v[76:79], v25 offset:512\n"
"v_mfma_f32_16x16x32_f16 v[68:71], v[44:47], v[148:151], 0\n"
"ds_read_b128 v[44:47], v25 offset:1024\n"
"s_waitcnt lgkmcnt(8)\n"
"v_mfma_f32_16x16x32_f16 v[60:63], v[152:155], v[96:99], v[60:63]\n"
"ds_read_b128 v[96:99], v25 offset:1536\n"
"v_mfma_f32_16x16x32_f16 v[48:51], v[88:91], v[72:75], v[48:51]\n"
"ds_read_b64 v[72:73], v24 offset:512\n"
"v_mfma_f32_16x16x32_f16 v[52:55], v[88:91], v[40:43], v[52:55]\n"
"ds_read_b64 v[74:75], v24 offset:520\n"
"v_mfma_f32_16x16x32_f16 v[56:59], v[88:91], v[104:107], v[56:59]\n"
"ds_read_b64 v[88:89], v24 offset:1024\n"
"s_waitcnt lgkmcnt(5)\n"
"v_mfma_f32_16x16x32_f16 v[52:55], v[92:95], v[104:107], v[52:55]\n"
"ds_read_b64 v[90:91], v24 offset:1032\n"
"v_cvt_pk_f16_f32 v80, v64, v65\n"
"v_mfma_f32_16x16x32_f16 v[48:51], v[92:95], v[40:43], v[48:51]\n"
"v_cvt_pk_f16_f32 v81, v66, v67\n"
"v_cvt_pk_f16_f32 v82, v68, v69\n"
"v_mfma_f32_16x16x32_f16 v[48:51], v[100:103], v[104:107], v[48:51]\n"
"v_cvt_pk_f16_f32 v83, v70, v71\n"
"v_mfma_f32_16x16x32_f16 v[48:51], v[84:87], v[76:79], v[48:51]\n"
"s_waitcnt lgkmcnt(2)\n"
"v_mfma_f32_16x16x32_f16 v[52:55], v[84:87], v[44:47], v[52:55]\n"
"v_mfma_f32_16x16x32_f16 v[56:59], v[84:87], v[96:99], v[56:59]\n"
"v_mfma_f32_16x16x32_f16 v[52:55], v[72:75], v[96:99], v[52:55]\n"
"v_mfma_f32_16x16x32_f16 v[48:51], v[72:75], v[44:47], v[48:51]\n"
"s_waitcnt lgkmcnt(0)\n"
"v_mfma_f32_16x16x32_f16 v[48:51], v[88:91], v[96:99], v[48:51]\n"
"v_mfma_f32_16x16x32_f16 v[60:63], v[80:83], v[128:131], v[60:63]\n"
"s_setprio 2\n"
"s_mov_b32 s17, 0x100000\n"
".Ltp_428:\n"
"ds_read_b32 v3, v5 offset:256\n"
"s_sub_u32 s17, s17, 1\n"
"s_cbranch_scc1 .Lou_429\n"
"s_waitcnt lgkmcnt(0)\n"
"v_cmp_eq_u32 vcc, 0, v3\n"
"s_cbranch_vccnz .Ltp_428\n"
".Lou_429:\n"
"s_waitcnt lgkmcnt(0)\n"
"buffer_load_dwordx4 v[110:113], v126, s[4:7], s32 offen offset:0 sc1\n"
"buffer_load_dwordx4 v[114:117], v126, s[4:7], s32 offen offset:128 sc1\n"
"buffer_load_dwordx4 v[118:121], v126, s[4:7], s33 offen offset:0 sc1\n"
"buffer_load_dwordx4 v[122:125], v126, s[4:7], s33 offen offset:128 sc1\n"
"s_mov_b32 s38, 0x05040100\n"
"s_mov_b32 s39, 0x07060302\n"
"s_mov_b32 s17, 32768\n"
".Ltp_430:\n"
"s_waitcnt vmcnt(0)\n"
"s_sub_u32 s17, s17, 1\n"
"s_cbranch_scc1 .Lou_431\n"
"v_bitop3_b32 v64, v111, v113, s16 bitop3:0x7e\n"
"v_bitop3_b32 v65, v115, v117, s16 bitop3:0x7e\n"
"v_bitop3_b32 v66, v119, v121, s16 bitop3:0x7e\n"
"v_bitop3_b32 v67, v123, v125, s16 bitop3:0x7e\n"
"v_or3_b32 v68, v64, v65, v66\n"
"v_or_b32 v68, v68, v67\n"
"v_cmp_ne_u32 vcc, 0, v68\n"
"s_cbranch_vccz .Lou_431\n"
"s_sleep 2\n"
"v_cmp_ne_u32 vcc, 0, v64\n"
"s_cbranch_vccz .Lsk_432\n"
"s_mov_b64 exec, vcc\n"
"buffer_load_dwordx4 v[110:113], v126, s[4:7], s32 offen offset:0 sc1\n"
"s_mov_b64 exec, -1\n"
".Lsk_432:\n"
"v_cmp_ne_u32 vcc, 0, v65\n"
"s_cbranch_vccz .Lsk_433\n"
"s_mov_b64 exec, vcc\n"
"buffer_load_dwordx4 v[114:117], v126, s[4:7], s32 offen offset:128 sc1\n"
"s_mov_b64 exec, -1\n"
".Lsk_433:\n"
"v_cmp_ne_u32 vcc, 0, v66\n"
"s_cbranch_vccz .Lsk_434\n"
"s_mov_b64 exec, vcc\n"
"buffer_load_dwordx4 v[118:121], v126, s[4:7], s33 offen offset:0 sc1\n"
"s_mov_b64 exec, -1\n"
".Lsk_434:\n"
"v_cmp_ne_u32 vcc, 0, v67\n"
"s_cbranch_vccz .Lsk_435\n"
"s_mov_b64 exec, vcc\n"
"buffer_load_dwordx4 v[122:125], v126, s[4:7], s33 offen offset:128 sc1\n"
"s_mov_b64 exec, -1\n"
".Lsk_435:\n"
"s_branch .Ltp_430\n"
".Lou_431:\n"
"s_waitcnt vmcnt(0)\n"
"s_mov_b32 s89, 2\n"
".Ldl_436:\n"
"s_sub_u32 s89, s89, 1\n"
"s_cbranch_scc1 .Lde_437\n"
"s_sleep 1\n"
"s_branch .Ldl_436\n"
".Lde_437:\n"
"buffer_load_dwordx4 v[88:91], v30, s[4:7], s34 offen sc1\n"
"buffer_load_dwordx4 v[92:95], v30, s[4:7], s35 offen sc1\n"
"v_perm_b32 v64, v114, v110, s38\n"
"v_perm_b32 v65, v114, v110, s39\n"
"v_perm_b32 v66, v116, v112, s38\n"
"v_perm_b32 v67, v116, v112, s39\n"
"ds_write_b32 v32, v64 offset:0\n"
"ds_write_b32 v33, v65 offset:2048\n"
"ds_write_b32 v32, v66 offset:4224\n"
"ds_write_b32 v33, v67 offset:6272\n"
"v_perm_b32 v64, v122, v118, s38\n"
"v_perm_b32 v65, v122, v118, s39\n"
"v_perm_b32 v66, v124, v120, s38\n"
"v_perm_b32 v67, v124, v120, s39\n"
"ds_write_b32 v32, v64 offset:1024\n"
"ds_write_b32 v33, v65 offset:3072\n"
"ds_write_b32 v32, v66 offset:5248\n"
"ds_write_b32 v33, v67 offset:7296\n"
"v_lshlrev_b32 v64, 16, v110\n"
"v_lshlrev_b32 v65, 16, v114\n"
"v_and_b32 v66, 0xffff0000, v110\n"
"v_and_b32 v67, 0xffff0000, v114\n"
"v_lshlrev_b32 v68, 16, v112\n"
"v_lshlrev_b32 v69, 16, v116\n"
"v_and_b32 v70, 0xffff0000, v112\n"
"v_and_b32 v71, 0xffff0000, v116\n"
"v_lshlrev_b32 v72, 16, v118\n"
"v_lshlrev_b32 v73, 16, v122\n"
"v_and_b32 v74, 0xffff0000, v118\n"
"v_and_b32 v75, 0xffff0000, v122\n"
"v_lshlrev_b32 v76, 16, v120\n"
"v_lshlrev_b32 v77, 16, v124\n"
"v_and_b32 v78, 0xffff0000, v120\n"
"v_and_b32 v79, 0xffff0000, v124\n"
"s_mov_b32 s17, 32768\n"
".Ltp_438:\n"
"s_waitcnt vmcnt(0)\n"
"s_sub_u32 s17, s17, 1\n"
"s_cbranch_scc1 .Lou_439\n"
"v_max_u32 v110, v88, v89\n"
"v_max3_u32 v110, v90, v91, v110\n"
"v_max3_u32 v110, v92, v93, v110\n"
"v_max3_u32 v110, v94, v95, v110\n"
"v_cmp_eq_u32 vcc, -1, v110\n"
"s_cbranch_vccz .Lou_439\n"
"v_max_u32 v110, v90, v91\n"
"v_max3_u32 v110, v88, v89, v110\n"
"v_cmp_eq_u32 vcc, -1, v110\n"
"s_cbranch_vccz .Lsk_440\n"
"s_mov_b64 exec, vcc\n"
"buffer_load_dwordx4 v[88:91], v30, s[4:7], s34 offen sc1\n"
"s_mov_b64 exec, -1\n"
".Lsk_440:\n"
"v_max_u32 v110, v94, v95\n"
"v_max3_u32 v110, v92, v93, v110\n"
"v_cmp_eq_u32 vcc, -1, v110\n"
"s_cbranch_vccz .Lsk_441\n"
"s_mov_b64 exec, vcc\n"
"buffer_load_dwordx4 v[92:95], v30, s[4:7], s35 offen sc1\n"
"s_mov_b64 exec, -1\n"
".Lsk_441:\n"
"s_branch .Ltp_438\n"
".Lou_439:\n"
"s_waitcnt vmcnt(0)\n"
"v_fma_f32 v64, v64, v89, v88\n"
"v_fma_f32 v65, v65, v91, v90\n"
"v_cvt_pk_f16_f32 v64, v64, v65\n"
"v_fma_f32 v66, v66, v89, v88\n"
"v_fma_f32 v67, v67, v91, v90\n"
"v_cvt_pk_f16_f32 v66, v66, v67\n"
"v_fma_f32 v68, v68, v89, v88\n"
"v_fma_f32 v69, v69, v91, v90\n"
"v_cvt_pk_f16_f32 v68, v68, v69\n"
"v_fma_f32 v70, v70, v89, v88\n"
"v_fma_f32 v71, v71, v91, v90\n"
"v_cvt_pk_f16_f32 v70, v70, v71\n"
"ds_write2st64_b32 v31, v64, v66 offset0:0 offset1:18\n"
"ds_write2st64_b32 v31, v68, v70 offset0:36 offset1:54\n"
"v_fma_f32 v72, v72, v93, v92\n"
"v_fma_f32 v73, v73, v95, v94\n"
"v_cvt_pk_f16_f32 v72, v72, v73\n"
"v_fma_f32 v74, v74, v93, v92\n"
"v_fma_f32 v75, v75, v95, v94\n"
"v_cvt_pk_f16_f32 v74, v74, v75\n"
"v_fma_f32 v76, v76, v93, v92\n"
"v_fma_f32 v77, v77, v95, v94\n"
"v_cvt_pk_f16_f32 v76, v76, v77\n"
"v_fma_f32 v78, v78, v93, v92\n"
"v_fma_f32 v79, v79, v95, v94\n"
"v_cvt_pk_f16_f32 v78, v78, v79\n"
"ds_write2st64_b32 v31, v72, v74 offset0:4 offset1:22\n"
"ds_write2st64_b32 v31, v76, v78 offset0:40 offset1:58\n"
"ds_write_b32 v4, v4 offset:688\n"
"s_mov_b32 s17, 0x100000\n"
".Ltp_442:\n"
"ds_read_b32 v3, v5 offset:688\n"
"s_sub_u32 s17, s17, 1\n"
"s_cbranch_scc1 .Lou_443\n"
"s_waitcnt lgkmcnt(0)\n"
"v_cmp_eq_u32 vcc, 0, v3\n"
"s_cbranch_vccnz .Ltp_442\n"
".Lou_443:\n"
"s_waitcnt lgkmcnt(0)\n"
"s_mov_b32 s36, 0xfffefffe\n"
"ds_read_b64 v[88:89], v24 offset:1984\n"
"ds_read_b64 v[90:91], v24 offset:1992\n"
"ds_read_b128 v[92:95], v25 offset:4032\n"
"ds_read_b128 v[96:99], v25 offset:3520\n"
"ds_read_b64 v[100:101], v24 offset:1472\n"
"ds_read_b64 v[102:103], v24 offset:1480\n"
"ds_read_b128 v[104:107], v25 offset:3008\n"
"s_waitcnt lgkmcnt(0)\n"
"v_mfma_f32_16x16x32_f16 v[60:63], v[88:91], v[92:95], v[60:63]\n"
"ds_read_b64 v[108:109], v24 offset:960\n"
"ds_read_b64 v[110:111], v24 offset:968\n"
"v_mfma_f32_16x16x32_f16 v[56:59], v[88:91], v[96:99], v[56:59]\n"
"ds_read_b128 v[112:115], v25 offset:2496\n"
"ds_read_b64 v[116:117], v24 offset:448\n"
"v_mfma_f32_16x16x32_f16 v[60:63], v[100:103], v[96:99], v[60:63]\n"
"ds_read_b64 v[118:119], v24 offset:456\n"
"ds_read_b64 v[120:121], v24 offset:1920\n"
"v_mfma_f32_16x16x32_f16 v[52:55], v[88:91], v[104:107], v[52:55]\n"
"ds_read_b64 v[122:123], v24 offset:1928\n"
"s_waitcnt lgkmcnt(4)\n"
"v_mfma_f32_16x16x32_f16 v[56:59], v[100:103], v[104:107], v[56:59]\n"
"ds_read_b128 v[124:127], v25 offset:3968\n"
"ds_read_b128 v[40:43], v25 offset:3456\n"
"v_mfma_f32_16x16x32_f16 v[60:63], v[108:111], v[104:107], v[60:63]\n"
"ds_read_b64 v[44:45], v24 offset:1408\n"
"ds_read_b64 v[46:47], v24 offset:1416\n"
"v_mfma_f32_16x16x32_f16 v[48:51], v[88:91], v[112:115], v[48:51]\n"
"ds_read_b128 v[92:95], v25 offset:2944\n"
"ds_read_b64 v[96:97], v24 offset:896\n"
"v_mfma_f32_16x16x32_f16 v[52:55], v[100:103], v[112:115], v[52:55]\n"
"ds_read_b64 v[98:99], v24 offset:904\n"
"s_waitcnt lgkmcnt(5)\n"
"v_mfma_f32_16x16x32_f16 v[56:59], v[108:111], v[112:115], v[56:59]\n"
"ds_read_b128 v[104:107], v25 offset:2432\n"
"ds_read_b64 v[88:89], v24 offset:384\n"
"v_mfma_f32_16x16x32_f16 v[60:63], v[116:119], v[112:115], v[60:63]\n"
"ds_read_b64 v[90:91], v24 offset:392\n"
"ds_read_b64 v[100:101], v24 offset:1856\n"
"v_mfma_f32_16x16x32_f16 v[60:63], v[120:123], v[124:127], v[60:63]\n"
"ds_read_b64 v[102:103], v24 offset:1864\n"
"ds_read_b128 v[108:111], v25 offset:3904\n"
"v_mfma_f32_16x16x32_f16 v[56:59], v[120:123], v[40:43], v[56:59]\n"
"ds_read_b128 v[112:115], v25 offset:3392\n"
"s_waitcnt lgkmcnt(7)\n"
"v_mfma_f32_16x16x32_f16 v[60:63], v[44:47], v[40:43], v[60:63]\n"
"ds_read_b64 v[116:117], v26 offset:0\n"
"ds_read_b64 v[124:125], v24 offset:1344\n"
"v_mfma_f32_16x16x32_f16 v[52:55], v[120:123], v[92:95], v[52:55]\n"
"ds_read_b64 v[126:127], v24 offset:1352\n"
"v_mfma_f32_16x16x32_f16 v[56:59], v[44:47], v[92:95], v[56:59]\n"
"ds_read_b128 v[40:43], v25 offset:2880\n"
"v_mfma_f32_16x16x32_f16 v[60:63], v[96:99], v[92:95], v[60:63]\n"
"ds_read_b64 v[92:93], v24 offset:832\n"
"s_waitcnt lgkmcnt(9)\n"
"v_mfma_f32_16x16x32_f16 v[48:51], v[120:123], v[104:107], v[48:51]\n"
"ds_read_b64 v[94:95], v24 offset:840\n"
"v_mfma_f32_16x16x32_f16 v[52:55], v[44:47], v[104:107], v[52:55]\n"
"ds_read_b128 v[120:123], v25 offset:2368\n"
"v_mfma_f32_16x16x32_f16 v[56:59], v[96:99], v[104:107], v[56:59]\n"
"ds_read_b64 v[44:45], v24 offset:320\n"
"v_mfma_f32_16x16x32_f16 v[60:63], v[88:91], v[104:107], v[60:63]\n"
"ds_read_b64 v[46:47], v24 offset:328\n"
"s_waitcnt lgkmcnt(6)\n"
"v_mfma_f32_16x16x32_f16 v[60:63], v[100:103], v[108:111], v[60:63]\n"
"ds_read_b64 v[96:97], v26 offset:512\n"
"ds_read_b64 v[104:105], v24 offset:1792\n"
"v_mfma_f32_16x16x32_f16 v[56:59], v[100:103], v[112:115], v[56:59]\n"
"ds_read_b64 v[106:107], v24 offset:1800\n"
"ds_read_b128 v[88:91], v25 offset:3840\n"
"v_mfma_f32_16x16x16_bf16 v[48:51], v[34:35], v[116:117], v[48:51]\n"
"ds_read_b128 v[108:111], v25 offset:3328\n"
"ds_read_b64 v[116:117], v24 offset:1280\n"
"v_mfma_f32_16x16x32_f16 v[60:63], v[124:127], v[112:115], v[60:63]\n"
"ds_read_b64 v[118:119], v24 offset:1288\n"
"s_waitcnt lgkmcnt(9)\n"
"v_mfma_f32_16x16x32_f16 v[52:55], v[100:103], v[40:43], v[52:55]\n"
"ds_read_b128 v[112:115], v25 offset:2816\n"
"s_and_b32 s79, s18, 0xfc\n"
"v_mfma_f32_16x16x32_f16 v[56:59], v[124:127], v[40:43], v[56:59]\n"
"s_and_b32 s78, s18, 1\n"
"s_lshl_b32 s78, s78, 1\n"
"v_mfma_f32_16x16x32_f16 v[60:63], v[92:95], v[40:43], v[60:63]\n"
"ds_read_b64 v[40:41], v24 offset:768\n"
"s_add_u32 s79, s79, s78\n"
"v_mfma_f32_16x16x32_f16 v[48:51], v[100:103], v[120:123], v[48:51]\n"
"ds_read_b64 v[42:43], v24 offset:776\n"
"s_waitcnt lgkmcnt(9)\n"
"v_mfma_f32_16x16x32_f16 v[52:55], v[124:127], v[120:123], v[52:55]\n"
"ds_read_b128 v[100:103], v25 offset:2304\n"
"s_lshr_b32 s78, s18, 1\n"
"v_mfma_f32_16x16x32_f16 v[56:59], v[92:95], v[120:123], v[56:59]\n"
"ds_read_b64 v[124:125], v24 offset:256\n"
"s_and_b32 s78, s78, 1\n"
"v_mfma_f32_16x16x32_f16 v[60:63], v[44:47], v[120:123], v[60:63]\n"
"ds_read_b64 v[126:127], v24 offset:264\n"
"ds_read_b64 v[92:93], v24 offset:1728\n"
"v_mfma_f32_16x16x16_bf16 v[52:55], v[34:35], v[96:97], v[52:55]\n"
"ds_read_b64 v[94:95], v24 offset:1736\n"
"s_waitcnt lgkmcnt(7)\n"
"v_mfma_f32_16x16x32_f16 v[60:63], v[104:107], v[88:91], v[60:63]\n"
"ds_read_b128 v[120:123], v25 offset:3776\n"
"ds_read_b128 v[44:47], v25 offset:3264\n"
"v_mfma_f32_16x16x32_f16 v[56:59], v[104:107], v[108:111], v[56:59]\n"
"ds_read_b64 v[96:97], v24 offset:1216\n"
"ds_read_b64 v[98:99], v24 offset:1224\n"
"v_mfma_f32_16x16x32_f16 v[60:63], v[116:119], v[108:111], v[60:63]\n"
"ds_read_b128 v[88:91], v25 offset:2752\n"
"s_add_u32 s79, s79, s78\n"
"v_mfma_f32_16x16x32_f16 v[52:55], v[104:107], v[112:115], v[52:55]\n"
"ds_read_b64 v[108:109], v26 offset:1024\n"
"s_waitcnt lgkmcnt(10)\n"
"v_mfma_f32_16x16x32_f16 v[56:59], v[116:119], v[112:115], v[56:59]\n"
"s_lshl_b32 s79, s79, 7\n"
"s_add_u32 s32, s79, 0xd68000\n"
"v_mfma_f32_16x16x32_f16 v[60:63], v[40:43], v[112:115], v[60:63]\n"
"ds_read_b64 v[112:113], v24 offset:704\n"
"v_mfma_f32_16x16x32_f16 v[48:51], v[104:107], v[100:103], v[48:51]\n"
"ds_read_b64 v[114:115], v24 offset:712\n"
"v_mfma_f32_16x16x32_f16 v[52:55], v[116:119], v[100:103], v[52:55]\n"
"ds_read_b128 v[104:107], v25 offset:2240\n"
"s_waitcnt lgkmcnt(7)\n"
"v_mfma_f32_16x16x32_f16 v[56:59], v[40:43], v[100:103], v[56:59]\n"
"ds_read_b64 v[116:117], v24 offset:192\n"
"v_mfma_f32_16x16x32_f16 v[60:63], v[124:127], v[100:103], v[60:63]\n"
"ds_read_b64 v[118:119], v24 offset:200\n"
"ds_read_b64 v[40:41], v24 offset:1664\n"
"v_mfma_f32_16x16x32_f16 v[60:63], v[92:95], v[120:123], v[60:63]\n"
"ds_read_b64 v[42:43], v24 offset:1672\n"
"ds_read_b128 v[100:103], v25 offset:3712\n"
"v_mfma_f32_16x16x32_f16 v[56:59], v[92:95], v[44:47], v[56:59]\n"
"ds_read_b128 v[124:127], v25 offset:3200\n"
"s_waitcnt lgkmcnt(9)\n"
"v_mfma_f32_16x16x32_f16 v[60:63], v[96:99], v[44:47], v[60:63]\n"
"ds_read_b64 v[120:121], v24 offset:1152\n"
"ds_read_b64 v[122:123], v24 offset:1160\n"
"v_mfma_f32_16x16x32_f16 v[52:55], v[92:95], v[88:91], v[52:55]\n"
"ds_read_b128 v[44:47], v25 offset:2688\n"
"v_mfma_f32_16x16x16_bf16 v[56:59], v[34:35], v[108:109], v[56:59]\n"
"ds_read_b64 v[108:109], v24 offset:640\n"
"s_nop 4\n"
"v_mfma_f32_16x16x32_f16 v[56:59], v[96:99], v[88:91], v[56:59]\n"
"ds_read_b64 v[110:111], v24 offset:648\n"
"s_waitcnt lgkmcnt(11)\n"
"v_mfma_f32_16x16x32_f16 v[60:63], v[112:115], v[88:91], v[60:63]\n"
"ds_read_b128 v[88:91], v25 offset:2176\n"
"v_mfma_f32_16x16x32_f16 v[48:51], v[92:95], v[104:107], v[48:51]\n"
"ds_read_b64 v[92:93], v26 offset:1536\n"
"v_mfma_f32_16x16x32_f16 v[52:55], v[96:99], v[104:107], v[52:55]\n"
"v_mfma_f32_16x16x32_f16 v[56:59], v[112:115], v[104:107], v[56:59]\n"
"ds_read_b64 v[96:97], v24 offset:128\n"
"s_waitcnt lgkmcnt(6)\n"
"v_mfma_f32_16x16x32_f16 v[60:63], v[116:119], v[104:107], v[60:63]\n"
"ds_read_b64 v[98:99], v24 offset:136\n"
"ds_read_b64 v[112:113], v24 offset:1600\n"
"v_mfma_f32_16x16x32_f16 v[60:63], v[40:43], v[100:103], v[60:63]\n"
"ds_read_b64 v[114:115], v24 offset:1608\n"
"ds_read_b128 v[104:107], v25 offset:3648\n"
"v_mfma_f32_16x16x32_f16 v[56:59], v[40:43], v[124:127], v[56:59]\n"
"ds_read_b128 v[116:119], v25 offset:3136\n"
"ds_read_b64 v[100:101], v24 offset:1088\n"
"v_mfma_f32_16x16x32_f16 v[60:63], v[120:123], v[124:127], v[60:63]\n"
"ds_read_b64 v[102:103], v24 offset:1096\n"
"s_waitcnt lgkmcnt(9)\n"
"v_mfma_f32_16x16x32_f16 v[52:55], v[40:43], v[44:47], v[52:55]\n"
"ds_read_b128 v[124:127], v25 offset:2624\n"
"v_mfma_f32_16x16x32_f16 v[56:59], v[120:123], v[44:47], v[56:59]\n"
"v_mfma_f32_16x16x32_f16 v[60:63], v[108:111], v[44:47], v[60:63]\n"
"ds_read_b64 v[44:45], v24 offset:576\n"
"v_mfma_f32_16x16x32_f16 v[48:51], v[40:43], v[88:91], v[48:51]\n"
"ds_read_b64 v[46:47], v24 offset:584\n"
"s_waitcnt lgkmcnt(9)\n"
"s_nop 0\n"
"v_mfma_f32_16x16x16_bf16 v[60:63], v[34:35], v[92:93], v[60:63]\n"
"ds_read_b128 v[40:43], v25 offset:2112\n"
"v_mfma_f32_16x16x32_f16 v[52:55], v[120:123], v[88:91], v[52:55]\n"
"ds_read_b64 v[92:93], v24 offset:64\n"
"v_mfma_f32_16x16x32_f16 v[56:59], v[108:111], v[88:91], v[56:59]\n"
"ds_read_b64 v[94:95], v24 offset:72\n"
"ds_read_b64 v[120:121], v24 offset:1536\n"
"v_mfma_f32_16x16x32_f16 v[60:63], v[96:99], v[88:91], v[60:63]\n"
"ds_read_b64 v[122:123], v24 offset:1544\n"
"s_waitcnt lgkmcnt(7)\n"
"v_mfma_f32_16x16x32_f16 v[60:63], v[112:115], v[104:107], v[60:63]\n"
"ds_read_b128 v[108:111], v25 offset:2048\n"
"ds_read_b128 v[88:91], v25 offset:2560\n"
"v_mfma_f32_16x16x32_f16 v[56:59], v[112:115], v[116:119], v[56:59]\n"
"ds_read_b64 v[96:97], v24 offset:1024\n"
"ds_read_b64 v[98:99], v24 offset:1032\n"
"v_mfma_f32_16x16x32_f16 v[60:63], v[100:103], v[116:119], v[60:63]\n"
"ds_read_b128 v[104:107], v25 offset:3072\n"
"v_mfma_f32_16x16x32_f16 v[52:55], v[112:115], v[124:127], v[52:55]\n"
"ds_read_b64 v[116:117], v24 offset:512\n"
"s_waitcnt lgkmcnt(10)\n"
"v_mfma_f32_16x16x32_f16 v[56:59], v[100:103], v[124:127], v[56:59]\n"
"ds_read_b64 v[118:119], v24 offset:520\n"
"v_mfma_f32_16x16x32_f16 v[60:63], v[44:47], v[124:127], v[60:63]\n"
"ds_read_b128 v[124:127], v25 offset:3584\n"
"v_mfma_f32_16x16x32_f16 v[48:51], v[112:115], v[40:43], v[48:51]\n"
"v_mfma_f32_16x16x32_f16 v[52:55], v[100:103], v[40:43], v[52:55]\n"
"ds_read_b64 v[112:113], v24 offset:0\n"
"s_waitcnt lgkmcnt(7)\n"
"v_mfma_f32_16x16x32_f16 v[56:59], v[44:47], v[40:43], v[56:59]\n"
"ds_read_b64 v[114:115], v24 offset:8\n"
"v_mfma_f32_16x16x32_f16 v[60:63], v[92:95], v[40:43], v[60:63]\n"
"v_mfma_f32_16x16x32_f16 v[48:51], v[120:123], v[108:111], v[48:51]\n"
"v_mfma_f32_16x16x32_f16 v[52:55], v[120:123], v[88:91], v[52:55]\n"
"s_waitcnt lgkmcnt(3)\n"
"v_mfma_f32_16x16x32_f16 v[52:55], v[96:99], v[108:111], v[52:55]\n"
"v_mfma_f32_16x16x32_f16 v[56:59], v[120:123], v[104:107], v[56:59]\n"
"v_mul_f32 v48, v36, v48\n"
"v_mul_f32 v49, v36, v49\n"
"v_mul_f32 v50, v36, v50\n"
"v_mfma_f32_16x16x32_f16 v[56:59], v[96:99], v[88:91], v[56:59]\n"
"v_mul_f32 v51, v36, v51\n"
"v_cvt_pk_bf16_f32 v48, v48, v49\n"
"v_cvt_pk_bf16_f32 v49, v50, v51\n"
"v_mfma_f32_16x16x32_f16 v[56:59], v[116:119], v[108:111], v[56:59]\n"
"v_pk_min_u16 v48, v48, s36\n"
"v_pk_min_u16 v49, v49, s36\n"
"s_waitcnt lgkmcnt(0)\n"
"v_mfma_f32_16x16x32_f16 v[60:63], v[120:123], v[124:127], v[60:63]\n"
"ds_write_b64 v26, v[48:49] offset:0\n"
"v_mfma_f32_16x16x32_f16 v[60:63], v[96:99], v[104:107], v[60:63]\n"
"v_mul_f32 v52, v36, v52\n"
"v_mul_f32 v53, v36, v53\n"
"v_mfma_f32_16x16x32_f16 v[60:63], v[116:119], v[88:91], v[60:63]\n"
"v_mul_f32 v54, v36, v54\n"
"v_mul_f32 v55, v36, v55\n"
"v_mfma_f32_16x16x32_f16 v[60:63], v[112:115], v[108:111], v[60:63]\n"
"v_cvt_pk_bf16_f32 v52, v52, v53\n"
"v_cvt_pk_bf16_f32 v53, v54, v55\n"
"v_pk_min_u16 v52, v52, s36\n"
"v_pk_min_u16 v53, v53, s36\n"
"ds_write_b64 v26, v[52:53] offset:512\n"
"v_mul_f32 v56, v36, v56\n"
"v_mul_f32 v57, v36, v57\n"
"v_mul_f32 v58, v36, v58\n"
"v_mul_f32 v59, v36, v59\n"
"v_cvt_pk_bf16_f32 v56, v56, v57\n"
"v_cvt_pk_bf16_f32 v57, v58, v59\n"
"v_pk_min_u16 v56, v56, s36\n"
"v_pk_min_u16 v57, v57, s36\n"
"ds_write_b64 v26, v[56:57] offset:1024\n"
"v_mul_f32 v60, v36, v60\n"
"v_mul_f32 v61, v36, v61\n"
"v_mul_f32 v62, v36, v62\n"
"v_mul_f32 v63, v36, v63\n"
"v_cvt_pk_bf16_f32 v60, v60, v61\n"
"v_cvt_pk_bf16_f32 v61, v62, v63\n"
"v_pk_min_u16 v60, v60, s36\n"
"v_pk_min_u16 v61, v61, s36\n"
"ds_write_b64 v26, v[60:61] offset:1536\n"
"ds_write_b32 v4, v4 offset:704\n"
"s_mov_b32 s17, 0x100000\n"
".Ltp_444:\n"
"ds_read_b32 v3, v5 offset:704\n"
"s_sub_u32 s17, s17, 1\n"
"s_cbranch_scc1 .Lou_445\n"
"s_waitcnt lgkmcnt(0)\n"
"v_cmp_eq_u32 vcc, 0, v3\n"
"s_cbranch_vccnz .Ltp_444\n"
".Lou_445:\n"
"s_waitcnt lgkmcnt(0)\n"
"ds_read_b64_tr_b16 v[64:65], v27 offset:0\n"
"ds_read_b64_tr_b16 v[66:67], v28 offset:0\n"
"ds_read_b64_tr_b16 v[68:69], v27 offset:1024\n"
"ds_read_b64_tr_b16 v[70:71], v28 offset:1024\n"
"s_add_u32 s33, s32, 0xa0000\n"
"s_waitcnt lgkmcnt(2)\n"
"buffer_store_dwordx4 v[64:67], v29, s[4:7], s32 offen sc1\n"
"s_waitcnt lgkmcnt(0)\n"
"buffer_store_dwordx4 v[68:71], v29, s[4:7], s33 offen sc1\n"
"ds_write_b32 v4, v4 offset:720\n"
"s_nop 0\n"
"s_branch .Ljc_446\n"
".Lnc_421:\n"
"s_mov_b32 s17, 0x100000\n"
".Ltp_447:\n"
"s_sleep 2\n"
"ds_read_b32 v3, v5 offset:272\n"
"s_sub_u32 s17, s17, 1\n"
"s_cbranch_scc1 .Lou_448\n"
"s_waitcnt lgkmcnt(0)\n"
"v_cmp_eq_u32 vcc, 0, v3\n"
"s_cbranch_vccnz .Ltp_447\n"
".Lou_448:\n"
"s_waitcnt lgkmcnt(0)\n"
"s_and_b32 s79, s2, 7\n"
"s_lshl_b32 s79, s79, 14\n"
"s_add_u32 s32, s79, 0xc0a000\n"
"s_add_u32 s33, s32, 0x1000\n"
"buffer_load_dwordx4 v[48:51], v2, s[4:7], s32 offen offset:0 sc1\n"
"buffer_load_dwordx4 v[52:55], v2, s[4:7], s32 offen offset:1024 sc1\n"
"buffer_load_dwordx4 v[56:59], v2, s[4:7], s32 offen offset:2048 sc1\n"
"buffer_load_dwordx4 v[60:63], v2, s[4:7], s32 offen offset:3072 sc1\n"
"buffer_load_dwordx4 v[64:67], v2, s[4:7], s33 offen offset:0 sc1\n"
"buffer_load_dwordx4 v[68:71], v2, s[4:7], s33 offen offset:1024 sc1\n"
"buffer_load_dwordx4 v[72:75], v2, s[4:7], s33 offen offset:2048 sc1\n"
"buffer_load_dwordx4 v[76:79], v2, s[4:7], s33 offen offset:3072 sc1\n"
"s_mov_b32 s17, 32768\n"
".Ltp_449:\n"
"s_waitcnt vmcnt(0)\n"
"s_sub_u32 s17, s17, 1\n"
"s_cbranch_scc1 .Lou_450\n"
"v_max_u32 v80, v48, v49\n"
"v_max3_u32 v80, v50, v51, v80\n"
"v_max3_u32 v80, v52, v53, v80\n"
"v_max3_u32 v80, v54, v55, v80\n"
"v_max3_u32 v80, v56, v57, v80\n"
"v_max3_u32 v80, v58, v59, v80\n"
"v_max3_u32 v80, v60, v61, v80\n"
"v_max3_u32 v80, v62, v63, v80\n"
"v_max3_u32 v80, v64, v65, v80\n"
"v_max3_u32 v80, v66, v67, v80\n"
"v_max3_u32 v80, v68, v69, v80\n"
"v_max3_u32 v80, v70, v71, v80\n"
"v_max3_u32 v80, v72, v73, v80\n"
"v_max3_u32 v80, v74, v75, v80\n"
"v_max3_u32 v80, v76, v77, v80\n"
"v_max3_u32 v80, v78, v79, v80\n"
"v_cmp_eq_u32 vcc, -1, v80\n"
"s_cbranch_vccz .Lou_450\n"
"s_sleep 4\n"
"v_max_u32 v80, v50, v51\n"
"v_max3_u32 v80, v48, v49, v80\n"
"v_cmp_eq_u32 vcc, -1, v80\n"
"s_cbranch_vccz .Lsk_451\n"
"s_mov_b64 exec, vcc\n"
"buffer_load_dwordx4 v[48:51], v2, s[4:7], s32 offen offset:0 sc1\n"
"s_mov_b64 exec, -1\n"
".Lsk_451:\n"
"v_max_u32 v80, v54, v55\n"
"v_max3_u32 v80, v52, v53, v80\n"
"v_cmp_eq_u32 vcc, -1, v80\n"
"s_cbranch_vccz .Lsk_452\n"
"s_mov_b64 exec, vcc\n"
"buffer_load_dwordx4 v[52:55], v2, s[4:7], s32 offen offset:1024 sc1\n"
"s_mov_b64 exec, -1\n"
".Lsk_452:\n"
"v_max_u32 v80, v58, v59\n"
"v_max3_u32 v80, v56, v57, v80\n"
"v_cmp_eq_u32 vcc, -1, v80\n"
"s_cbranch_vccz .Lsk_453\n"
"s_mov_b64 exec, vcc\n"
"buffer_load_dwordx4 v[56:59], v2, s[4:7], s32 offen offset:2048 sc1\n"
"s_mov_b64 exec, -1\n"
".Lsk_453:\n"
"v_max_u32 v80, v62, v63\n"
"v_max3_u32 v80, v60, v61, v80\n"
"v_cmp_eq_u32 vcc, -1, v80\n"
"s_cbranch_vccz .Lsk_454\n"
"s_mov_b64 exec, vcc\n"
"buffer_load_dwordx4 v[60:63], v2, s[4:7], s32 offen offset:3072 sc1\n"
"s_mov_b64 exec, -1\n"
".Lsk_454:\n"
"v_max_u32 v80, v66, v67\n"
"v_max3_u32 v80, v64, v65, v80\n"
"v_cmp_eq_u32 vcc, -1, v80\n"
"s_cbranch_vccz .Lsk_455\n"
"s_mov_b64 exec, vcc\n"
"buffer_load_dwordx4 v[64:67], v2, s[4:7], s33 offen offset:0 sc1\n"
"s_mov_b64 exec, -1\n"
".Lsk_455:\n"
"v_max_u32 v80, v70, v71\n"
"v_max3_u32 v80, v68, v69, v80\n"
"v_cmp_eq_u32 vcc, -1, v80\n"
"s_cbranch_vccz .Lsk_456\n"
"s_mov_b64 exec, vcc\n"
"buffer_load_dwordx4 v[68:71], v2, s[4:7], s33 offen offset:1024 sc1\n"
"s_mov_b64 exec, -1\n"
".Lsk_456:\n"
"v_max_u32 v80, v74, v75\n"
"v_max3_u32 v80, v72, v73, v80\n"
"v_cmp_eq_u32 vcc, -1, v80\n"
"s_cbranch_vccz .Lsk_457\n"
"s_mov_b64 exec, vcc\n"
"buffer_load_dwordx4 v[72:75], v2, s[4:7], s33 offen offset:2048 sc1\n"
"s_mov_b64 exec, -1\n"
".Lsk_457:\n"
"v_max_u32 v80, v78, v79\n"
"v_max3_u32 v80, v76, v77, v80\n"
"v_cmp_eq_u32 vcc, -1, v80\n"
"s_cbranch_vccz .Lsk_458\n"
"s_mov_b64 exec, vcc\n"
"buffer_load_dwordx4 v[76:79], v2, s[4:7], s33 offen offset:3072 sc1\n"
"s_mov_b64 exec, -1\n"
".Lsk_458:\n"
"s_branch .Ltp_449\n"
".Lou_450:\n"
"s_waitcnt vmcnt(0)\n"
".Ljc_446:\n"
"s_setprio 0\n"
"s_lshr_b32 s79, s2, 1\n"
"s_and_b32 s79, s79, 3\n"
"s_lshl_b32 s79, s79, 2\n"
"s_lshr_b32 s78, s2, 3\n"
"s_lshr_b32 s78, s78, 3\n"
"s_add_u32 s79, s79, s78\n"
"s_lshl_b32 s79, s79, 2\n"
"s_add_u32 s79, s79, 63\n"
"s_add_u32 s79, s79, 4\n"
"s_mul_i32 s79, s79, 0x5000\n"
"s_mul_i32 s78, s3, 0x1400\n"
"s_add_u32 s79, s79, s78\n"
"s_add_u32 s50, s79, 0xc28000\n"
"s_add_u32 s51, s50, 0x1000\n"
"v_min_i32 v99, 3, v0\n"
"v_mul_i32_i24 v48, 0x500, v99\n"
"s_mul_i32 s79, s3, 0x1400\n"
"v_add_u32 v50, s79, v2\n"
"s_and_b32 s79, s2, 7\n"
"s_lshl_b32 s79, s79, 3\n"
"s_add_u32 s79, s79, 0x47\n"
"s_mul_i32 s79, s79, 0x5000\n"
"s_and_b32 s78, s2, 7\n"
"s_lshr_b32 s78, s78, 1\n"
"s_lshl_b32 s78, s78, 7\n"
"s_add_u32 s79, s79, s78\n"
"s_mul_i32 s78, s3, 0x1400\n"
"s_add_u32 s79, s79, s78\n"
"s_add_u32 s49, s79, 0xc28000\n"
"s_mov_b32 s17, 0x100000\n"
".Ltp_459:\n"
"s_sleep 2\n"
"ds_read_b32 v3, v5 offset:2944\n"
"s_sub_u32 s17, s17, 1\n"
"s_cbranch_scc1 .Lou_460\n"
"s_waitcnt lgkmcnt(0)\n"
"v_cmp_eq_u32 vcc, -1, v3\n"
"s_cbranch_vccnz .Ltp_459\n"
".Lou_460:\n"
"s_waitcnt lgkmcnt(0)\n"
"s_mov_b32 s89, 8\n"
".Ldl_461:\n"
"s_sub_u32 s89, s89, 1\n"
"s_cbranch_scc1 .Lde_462\n"
"s_sleep 1\n"
"s_branch .Ldl_461\n"
".Lde_462:\n"
"s_mov_b32 s17, 32768\n"
".Ltp_464:\n"
"buffer_load_dword v49, v48, s[4:7], s49 offen sc1\n"
"s_sub_u32 s17, s17, 1\n"
"s_cbranch_scc1 .Lou_465\n"
"s_waitcnt vmcnt(0)\n"
"v_cmp_eq_u32 vcc, -1, v49\n"
"s_cbranch_vccz .Lou_465\n"
"s_mov_b32 s89, 5\n"
".Ldl_466:\n"
"s_sub_u32 s89, s89, 1\n"
"s_cbranch_scc1 .Lde_467\n"
"s_sleep 1\n"
"s_branch .Ldl_466\n"
".Lde_467:\n"
"s_branch .Ltp_464\n"
".Lou_465:\n"
"s_waitcnt vmcnt(0)\n"
".Lnw_463:\n"
"s_setprio 0\n"
"buffer_load_dwordx4 v[52:55], v2, s[4:7], s50 offen offset:0 sc1\n"
"buffer_load_dwordx4 v[56:59], v2, s[4:7], s50 offen offset:1024 sc1\n"
"buffer_load_dwordx4 v[60:63], v2, s[4:7], s50 offen offset:2048 sc1\n"
"buffer_load_dwordx4 v[64:67], v2, s[4:7], s50 offen offset:3072 sc1\n"
"buffer_load_dwordx4 v[68:71], v2, s[4:7], s51 offen offset:0 sc1\n"
"s_mov_b32 s17, 32768\n"
".Ltp_468:\n"
"s_waitcnt vmcnt(0)\n"
"s_sub_u32 s17, s17, 1\n"
"s_cbranch_scc1 .Lou_469\n"
"v_max_u32 v38, v52, v53\n"
"v_max3_u32 v38, v54, v55, v38\n"
"v_max3_u32 v38, v56, v57, v38\n"
"v_max3_u32 v38, v58, v59, v38\n"
"v_max3_u32 v38, v60, v61, v38\n"
"v_max3_u32 v38, v62, v63, v38\n"
"v_max3_u32 v38, v64, v65, v38\n"
"v_max3_u32 v38, v66, v67, v38\n"
"v_max3_u32 v38, v68, v69, v38\n"
"v_max3_u32 v38, v70, v71, v38\n"
"v_cmp_eq_u32 vcc, -1, v38\n"
"s_cbranch_vccz .Lou_469\n"
"s_sleep 1\n"
"v_max_u32 v38, v54, v55\n"
"v_max3_u32 v38, v52, v53, v38\n"
"v_cmp_eq_u32 vcc, -1, v38\n"
"s_cbranch_vccz .Lsk_470\n"
"s_mov_b64 exec, vcc\n"
"buffer_load_dwordx4 v[52:55], v2, s[4:7], s50 offen offset:0 sc1\n"
"s_mov_b64 exec, -1\n"
".Lsk_470:\n"
"v_max_u32 v38, v58, v59\n"
"v_max3_u32 v38, v56, v57, v38\n"
"v_cmp_eq_u32 vcc, -1, v38\n"
"s_cbranch_vccz .Lsk_471\n"
"s_mov_b64 exec, vcc\n"
"buffer_load_dwordx4 v[56:59], v2, s[4:7], s50 offen offset:1024 sc1\n"
"s_mov_b64 exec, -1\n"
".Lsk_471:\n"
"v_max_u32 v38, v62, v63\n"
"v_max3_u32 v38, v60, v61, v38\n"
"v_cmp_eq_u32 vcc, -1, v38\n"
"s_cbranch_vccz .Lsk_472\n"
"s_mov_b64 exec, vcc\n"
"buffer_load_dwordx4 v[60:63], v2, s[4:7], s50 offen offset:2048 sc1\n"
"s_mov_b64 exec, -1\n"
".Lsk_472:\n"
"v_max_u32 v38, v66, v67\n"
"v_max3_u32 v38, v64, v65, v38\n"
"v_cmp_eq_u32 vcc, -1, v38\n"
"s_cbranch_vccz .Lsk_473\n"
"s_mov_b64 exec, vcc\n"
"buffer_load_dwordx4 v[64:67], v2, s[4:7], s50 offen offset:3072 sc1\n"
"s_mov_b64 exec, -1\n"
".Lsk_473:\n"
"v_max_u32 v38, v70, v71\n"
"v_max3_u32 v38, v68, v69, v38\n"
"v_cmp_eq_u32 vcc, -1, v38\n"
"s_cbranch_vccz .Lsk_474\n"
"s_mov_b64 exec, vcc\n"
"buffer_load_dwordx4 v[68:71], v2, s[4:7], s51 offen offset:0 sc1\n"
"s_mov_b64 exec, -1\n"
".Lsk_474:\n"
"s_branch .Ltp_468\n"
".Lou_469:\n"
"s_waitcnt vmcnt(0)\n"
"s_mov_b32 s17, 0x100000\n"
".Ltp_475:\n"
"s_sleep 1\n"
"ds_read_b32 v3, v5 offset:3584\n"
"s_sub_u32 s17, s17, 1\n"
"s_cbranch_scc1 .Lou_476\n"
"s_waitcnt lgkmcnt(0)\n"
"v_cmp_eq_u32 vcc, -1, v3\n"
"s_cbranch_vccnz .Ltp_475\n"
".Lou_476:\n"
"s_waitcnt lgkmcnt(0)\n"
"s_mul_i32 s79, s3, 0x1400\n"
"s_add_u32 m0, s79, 0xa000\n"
"s_add_u32 s49, s50, 0xffff1000\n"
"buffer_load_dwordx4 v2, s[4:7], s49 offen offset:0 sc1 lds\n"
"buffer_load_dwordx4 v2, s[4:7], s49 offen offset:1024 sc1 lds\n"
"buffer_load_dwordx4 v2, s[4:7], s49 offen offset:2048 sc1 lds\n"
"buffer_load_dwordx4 v2, s[4:7], s49 offen offset:3072 sc1 lds\n"
"s_add_u32 m0, m0, 1024\n"
"s_add_u32 s49, s49, 1024\n"
"buffer_load_dwordx4 v2, s[4:7], s49 offen offset:3072 sc1 lds\n"
"s_mov_b32 s17, 0x100000\n"
".Ltp_477:\n"
"s_sleep 2\n"
"ds_read_b32 v3, v5 offset:3264\n"
"s_sub_u32 s17, s17, 1\n"
"s_cbranch_scc1 .Lou_478\n"
"s_waitcnt lgkmcnt(0)\n"
"v_cmp_eq_u32 vcc, -1, v3\n"
"s_cbranch_vccnz .Ltp_477\n"
".Lou_478:\n"
"s_waitcnt lgkmcnt(0)\n"
"ds_write_b128 v50, v[52:55] offset:0\n"
"ds_write_b128 v50, v[56:59] offset:1024\n"
"ds_write_b128 v50, v[60:63] offset:2048\n"
"ds_write_b128 v50, v[64:67] offset:3072\n"
"ds_write_b128 v50, v[68:71] offset:4096\n"
"ds_write_b32 v4, v4 offset:368\n"
"s_setprio 2\n"
"v_add_u32 v14, v39, v14\n"
"v_add_u32 v15, v39, v15\n"
"s_mul_i32 s79, s28, 0xa00\n"
"s_add_u32 s79, s30, s79\n"
"s_and_b32 s78, s3, 1\n"
"s_mul_i32 s78, s78, 0x600\n"
"s_add_u32 s79, s79, s78\n"
"s_add_u32 s36, s79, 0xc28000\n"
"s_mul_i32 s79, s3, 0x1400\n"
"s_add_u32 s79, s30, s79\n"
"s_add_u32 s32, s79, 0xea8000\n"
"s_add_u32 s33, s32, 0x1000\n"
"s_lshl_b32 s79, s29, 1\n"
"s_lshr_b32 s78, s3, 1\n"
"s_xor_b32 s78, s78, 1\n"
"s_add_u32 s79, s79, s78\n"
"s_mul_i32 s79, s79, 0x5000\n"
"s_mul_i32 s78, s3, 0x1400\n"
"s_add_u32 s79, s79, s78\n"
"s_add_u32 s34, s79, 0xea8000\n"
"s_add_u32 s35, s34, 0x1000\n"
"s_lshl_b32 s79, s30, 1\n"
"s_mul_i32 s78, s28, 0x1400\n"
"s_add_u32 s79, s79, s78\n"
"s_and_b32 s78, s3, 1\n"
"s_mul_i32 s78, s78, 0xc00\n"
"s_add_u32 s79, s79, s78\n"
"s_add_u32 s37, s79, 0x1128000\n"
"s_lshl_b32 s79, s29, 1\n"
"s_lshr_b32 s78, s3, 1\n"
"s_add_u32 s79, s79, s78\n"
"s_lshl_b32 s79, s79, 11\n"
"s_lshl_b32 s78, s28, 8\n"
"s_add_u32 s79, s79, s78\n"
"s_and_b32 s78, s3, 1\n"
"s_lshl_b32 s78, s78, 7\n"
"s_add_u32 s79, s79, s78\n"
"s_add_u32 s38, s79, 0x1628000\n"
"s_mov_b32 s17, 0x100000\n"
".Ltp_479:\n"
"ds_read_b32 v3, v5 offset:256\n"
"s_sub_u32 s17, s17, 1\n"
"s_cbranch_scc1 .Lou_480\n"
"s_waitcnt lgkmcnt(0)\n"
"v_cmp_eq_u32 vcc, 0, v3\n"
"s_cbranch_vccnz .Ltp_479\n"
".Lou_480:\n"
"s_waitcnt lgkmcnt(0)\n"
"s_cmp_lt_i32 s18, 0\n"
"s_cbranch_scc1 .Lcw_481\n"
"s_mov_b32 s17, 0x100000\n"
".Ltp_482:\n"
"ds_read_b32 v3, v5 offset:720\n"
"s_sub_u32 s17, s17, 1\n"
"s_cbranch_scc1 .Lou_483\n"
"s_waitcnt lgkmcnt(0)\n"
"v_cmp_eq_u32 vcc, 0, v3\n"
"s_cbranch_vccnz .Ltp_482\n"
".Lou_483:\n"
"s_waitcnt lgkmcnt(0)\n"
".Lcw_481:\n"
"buffer_load_dwordx2 v[40:41], v14, s[4:7], s36 offen offset:0 sc1\n"
"buffer_load_dwordx2 v[42:43], v14, s[4:7], s36 offen offset:512 sc1\n"
"buffer_load_dwordx2 v[44:45], v15, s[4:7], s36 offen offset:1024 sc1\n"
"s_waitcnt vmcnt(3)\n"
"ds_write_b32 v4, v4 offset:320\n"
"s_mov_b32 s17, 0x100000\n"
".Ltp_487:\n"
"s_sleep 1\n"
"ds_read_b32 v3, v5 offset:304\n"
"s_sub_u32 s17, s17, 1\n"
"s_cbranch_scc1 .Lou_488\n"
"s_waitcnt lgkmcnt(0)\n"
"v_cmp_eq_u32 vcc, 0, v3\n"
"s_cbranch_vccnz .Ltp_487\n"
".Lou_488:\n"
"s_waitcnt lgkmcnt(0)\n"
"s_mul_i32 s79, s3, 0x1400\n"
"s_add_u32 m0, s79, 0xf000\n"
"s_add_u32 s49, s50, 0xffff6000\n"
"buffer_load_dwordx4 v2, s[4:7], s49 offen offset:0 sc1 lds\n"
"buffer_load_dwordx4 v2, s[4:7], s49 offen offset:1024 sc1 lds\n"
"buffer_load_dwordx4 v2, s[4:7], s49 offen offset:2048 sc1 lds\n"
"buffer_load_dwordx4 v2, s[4:7], s49 offen offset:3072 sc1 lds\n"
"s_add_u32 m0, m0, 1024\n"
"s_add_u32 s49, s49, 1024\n"
"buffer_load_dwordx4 v2, s[4:7], s49 offen offset:3072 sc1 lds\n"
"s_waitcnt vmcnt(5)\n"
"s_mov_b32 s17, 32768\n"
"s_branch .Lmd_486\n"
".Ltp_484:\n"
"buffer_load_dwordx2 v[40:41], v14, s[4:7], s36 offen offset:0 sc1\n"
"buffer_load_dwordx2 v[42:43], v14, s[4:7], s36 offen offset:512 sc1\n"
"buffer_load_dwordx2 v[44:45], v15, s[4:7], s36 offen offset:1024 sc1\n"
"s_waitcnt vmcnt(0)\n"
".Lmd_486:\n"
"s_sub_u32 s17, s17, 1\n"
"s_cbranch_scc1 .Lou_485\n"
"v_max3_u32 v38, v40, v41, v42\n"
"v_max3_u32 v38, v43, v44, v38\n"
"v_max_u32 v38, v45, v38\n"
"v_cmp_eq_u32 vcc, -1, v38\n"
"s_cbranch_vccnz .Ltp_484\n"
".Lou_485:\n"
"v_lshlrev_b32 v48, 16, v40\n"
"v_and_b32 v49, 0xffff0000, v40\n"
"v_lshlrev_b32 v50, 16, v41\n"
"v_and_b32 v51, 0xffff0000, v41\n"
"v_lshlrev_b32 v52, 16, v42\n"
"v_and_b32 v53, 0xffff0000, v42\n"
"v_lshlrev_b32 v54, 16, v43\n"
"v_and_b32 v55, 0xffff0000, v43\n"
"v_lshlrev_b32 v56, 16, v44\n"
"v_and_b32 v57, 0xffff0000, v44\n"
"v_lshlrev_b32 v58, 16, v45\n"
"v_and_b32 v59, 0xffff0000, v45\n"
"v_mov_b32 v46, 0\n"
"v_mov_b32 v47, 0\n"
"s_mov_b32 s17, 0x100000\n"
".Ltp_489:\n"
"s_sleep 1\n"
"ds_read_b32 v3, v5 offset:384\n"
"s_sub_u32 s17, s17, 1\n"
"s_cbranch_scc1 .Lou_490\n"
"s_waitcnt lgkmcnt(0)\n"
"v_cmp_eq_u32 vcc, 0, v3\n"
"s_cbranch_vccnz .Ltp_489\n"
".Lou_490:\n"
"s_waitcnt lgkmcnt(0)\n"
"s_mul_i32 s79, s3, 0x1400\n"
"s_add_u32 m0, s79, 0x5000\n"
"s_add_u32 s49, s50, 0xffffb000\n"
"buffer_load_dwordx4 v2, s[4:7], s49 offen offset:0 sc1 lds\n"
"buffer_load_dwordx4 v2, s[4:7], s49 offen offset:1024 sc1 lds\n"
"buffer_load_dwordx4 v2, s[4:7], s49 offen offset:2048 sc1 lds\n"
"buffer_load_dwordx4 v2, s[4:7], s49 offen offset:3072 sc1 lds\n"
"s_add_u32 m0, m0, 1024\n"
"s_add_u32 s49, s49, 1024\n"
"buffer_load_dwordx4 v2, s[4:7], s49 offen offset:3072 sc1 lds\n"
"s_waitcnt vmcnt(5)\n"
"ds_write_b32 v4, v4 offset:416\n"
"s_waitcnt vmcnt(0)\n"
"ds_write_b32 v4, v4 offset:432\n"
"buffer_load_dwordx4 v[108:111], v2, s[4:7], s34 offen offset:0 sc1\n"
"s_nop 9\n"
"buffer_load_dwordx4 v[112:115], v2, s[4:7], s34 offen offset:1024 sc1\n"
"s_nop 9\n"
"buffer_load_dwordx4 v[116:119], v2, s[4:7], s34 offen offset:2048 sc1\n"
"s_nop 9\n"
"buffer_load_dwordx4 v[120:123], v2, s[4:7], s34 offen offset:3072 sc1\n"
"s_nop 9\n"
"buffer_load_dwordx4 v[124:127], v2, s[4:7], s35 offen offset:0 sc1\n"
"s_nop 9\n"
"buffer_load_dwordx4 v[88:91], v2, s[4:7], s32 offen offset:0 sc1\n"
"s_nop 9\n"
"buffer_load_dwordx4 v[92:95], v2, s[4:7], s32 offen offset:1024 sc1\n"
"s_nop 9\n"
"buffer_load_dwordx4 v[96:99], v2, s[4:7], s32 offen offset:2048 sc1\n"
"s_nop 9\n"
"buffer_load_dwordx4 v[100:103], v2, s[4:7], s32 offen offset:3072 sc1\n"
"s_nop 9\n"
"buffer_load_dwordx4 v[104:107], v2, s[4:7], s33 offen offset:0 sc1\n"
"s_waitcnt vmcnt(9)\n"
"v_max_u32 v38, v108, v109\n"
"v_max3_u32 v38, v110, v111, v38\n"
"v_cmp_eq_u32 vcc, -1, v38\n"
"s_cbranch_vccnz .Lsl_491\n"
".Lbk_492:\n"
"v_mfma_f32_16x16x32_bf16 v[68:71], v[156:159], v[108:111], 0\n"
"v_mfma_f32_16x16x32_bf16 v[72:75], v[160:163], v[108:111], 0\n"
"v_mfma_f32_16x16x32_bf16 v[76:79], v[164:167], v[108:111], 0\n"
"s_waitcnt vmcnt(8)\n"
"v_max_u32 v38, v112, v113\n"
"v_max3_u32 v38, v114, v115, v38\n"
"v_mfma_f32_16x16x32_bf16 v[80:83], v[168:171], v[108:111], 0\n"
"v_cmp_eq_u32 vcc, -1, v38\n"
"v_mfma_f32_16x16x32_bf16 v[84:87], v[172:175], v[108:111], 0\n"
"s_cbranch_vccnz .Lsl_493\n"
".Lbk_494:\n"
"v_mfma_f32_16x16x32_bf16 v[68:71], v[176:179], v[112:115], v[68:71]\n"
"v_mfma_f32_16x16x32_bf16 v[72:75], v[180:183], v[112:115], v[72:75]\n"
"v_mfma_f32_16x16x32_bf16 v[76:79], v[184:187], v[112:115], v[76:79]\n"
"s_waitcnt vmcnt(7)\n"
"v_max_u32 v38, v116, v117\n"
"v_max3_u32 v38, v118, v119, v38\n"
"v_mfma_f32_16x16x32_bf16 v[80:83], v[188:191], v[112:115], v[80:83]\n"
"v_cmp_eq_u32 vcc, -1, v38\n"
"v_mfma_f32_16x16x32_bf16 v[84:87], v[192:195], v[112:115], v[84:87]\n"
"s_cbranch_vccnz .Lsl_495\n"
".Lbk_496:\n"
"v_mfma_f32_16x16x32_bf16 v[68:71], v[196:199], v[116:119], v[68:71]\n"
"v_mfma_f32_16x16x32_bf16 v[72:75], v[200:203], v[116:119], v[72:75]\n"
"v_mfma_f32_16x16x32_bf16 v[76:79], v[204:207], v[116:119], v[76:79]\n"
"s_waitcnt vmcnt(6)\n"
"v_max_u32 v38, v120, v121\n"
"v_max3_u32 v38, v122, v123, v38\n"
"v_mfma_f32_16x16x32_bf16 v[80:83], v[208:211], v[116:119], v[80:83]\n"
"v_cmp_eq_u32 vcc, -1, v38\n"
"v_mfma_f32_16x16x32_bf16 v[84:87], v[212:215], v[116:119], v[84:87]\n"
"s_cbranch_vccnz .Lsl_497\n"
".Lbk_498:\n"
"v_mfma_f32_16x16x32_bf16 v[68:71], v[216:219], v[120:123], v[68:71]\n"
"v_mfma_f32_16x16x32_bf16 v[72:75], v[220:223], v[120:123], v[72:75]\n"
"v_mfma_f32_16x16x32_bf16 v[76:79], v[224:227], v[120:123], v[76:79]\n"
"s_waitcnt vmcnt(5)\n"
"v_max_u32 v38, v124, v125\n"
"v_max3_u32 v38, v126, v127, v38\n"
"v_mfma_f32_16x16x32_bf16 v[80:83], v[228:231], v[120:123], v[80:83]\n"
"v_cmp_eq_u32 vcc, -1, v38\n"
"v_mfma_f32_16x16x32_bf16 v[84:87], v[232:235], v[120:123], v[84:87]\n"
"s_cbranch_vccnz .Lsl_499\n"
".Lbk_500:\n"
"v_mfma_f32_16x16x32_bf16 v[68:71], v[236:239], v[124:127], v[68:71]\n"
"v_mfma_f32_16x16x32_bf16 v[72:75], v[240:243], v[124:127], v[72:75]\n"
"v_mfma_f32_16x16x32_bf16 v[76:79], v[244:247], v[124:127], v[76:79]\n"
"s_waitcnt vmcnt(4)\n"
"v_max_u32 v38, v88, v89\n"
"v_max3_u32 v38, v90, v91, v38\n"
"v_mfma_f32_16x16x32_bf16 v[80:83], v[248:251], v[124:127], v[80:83]\n"
"v_cmp_eq_u32 vcc, -1, v38\n"
"v_mfma_f32_16x16x32_bf16 v[84:87], v[252:255], v[124:127], v[84:87]\n"
"ds_write_b128 v8, v[68:71] offset:0\n"
"s_cbranch_vccnz .Lsl_501\n"
".Lbk_502:\n"
"v_mfma_f32_16x16x32_bf16 v[56:59], v[164:167], v[88:91], v[56:59]\n"
"s_waitcnt vmcnt(3)\n"
"v_max_u32 v38, v92, v93\n"
"v_max3_u32 v38, v94, v95, v38\n"
"ds_write_b128 v8, v[72:75] offset:1024\n"
"v_mfma_f32_16x16x32_bf16 v[60:63], v[168:171], v[88:91], 0\n"
"v_cmp_eq_u32 vcc, -1, v38\n"
"ds_write_b128 v10, v[76:79] offset:0\n"
"v_mfma_f32_16x16x32_bf16 v[64:67], v[172:175], v[88:91], 0\n"
"v_mov_b32 v117, s16\n"
"s_cbranch_vccnz .Lsl_503\n"
".Lbk_504:\n"
"v_mfma_f32_16x16x32_bf16 v[56:59], v[184:187], v[92:95], v[56:59]\n"
"s_waitcnt vmcnt(2)\n"
"v_max_u32 v38, v96, v97\n"
"v_max3_u32 v38, v98, v99, v38\n"
"v_mov_b32 v119, s16\n"
"v_mfma_f32_16x16x32_bf16 v[60:63], v[188:191], v[92:95], v[60:63]\n"
"v_cmp_eq_u32 vcc, -1, v38\n"
"ds_write_b128 v9, v[80:83] offset:0\n"
"v_mfma_f32_16x16x32_bf16 v[64:67], v[192:195], v[92:95], v[64:67]\n"
"ds_write_b128 v9, v[84:87] offset:1024\n"
"s_cbranch_vccnz .Lsl_505\n"
".Lbk_506:\n"
"v_mfma_f32_16x16x32_bf16 v[56:59], v[204:207], v[96:99], v[56:59]\n"
"s_waitcnt vmcnt(1)\n"
"v_max_u32 v38, v100, v101\n"
"v_max3_u32 v38, v102, v103, v38\n"
"ds_write_b32 v4, v4 offset:736\n"
"v_mfma_f32_16x16x32_bf16 v[60:63], v[208:211], v[96:99], v[60:63]\n"
"v_cmp_eq_u32 vcc, -1, v38\n"
"v_mov_b32 v121, s16\n"
"v_mov_b32 v123, s16\n"
"v_mfma_f32_16x16x32_bf16 v[64:67], v[212:215], v[96:99], v[64:67]\n"
"v_mov_b32 v125, s16\n"
"s_cbranch_vccnz .Lsl_507\n"
".Lbk_508:\n"
"v_mfma_f32_16x16x32_bf16 v[56:59], v[224:227], v[100:103], v[56:59]\n"
"s_waitcnt vmcnt(0)\n"
"v_max_u32 v38, v104, v105\n"
"v_max3_u32 v38, v106, v107, v38\n"
"v_mov_b32 v127, s16\n"
"v_mfma_f32_16x16x32_bf16 v[60:63], v[228:231], v[100:103], v[60:63]\n"
"v_cmp_eq_u32 vcc, -1, v38\n"
"ds_read_b32 v3, v5 offset:736\n"
"v_mfma_f32_16x16x32_bf16 v[64:67], v[232:235], v[100:103], v[64:67]\n"
"s_cbranch_vccnz .Lsl_509\n"
".Lbk_510:\n"
"v_mfma_f32_16x16x32_bf16 v[56:59], v[244:247], v[104:107], v[56:59]\n"
"ds_read_b128 v[76:79], v12 offset:2048\n"
"ds_read_b128 v[108:111], v13 offset:2048\n"
"v_mfma_f32_16x16x32_bf16 v[60:63], v[248:251], v[104:107], v[60:63]\n"
"ds_read_b128 v[68:71], v12 offset:0\n"
"ds_read_b128 v[80:83], v13 offset:0\n"
"v_mfma_f32_16x16x32_bf16 v[64:67], v[252:255], v[104:107], v[64:67]\n"
"ds_read_b128 v[72:75], v12 offset:1024\n"
"ds_read_b128 v[84:87], v13 offset:1024\n"
"v_mfma_f32_16x16x32_bf16 v[48:51], v[156:159], v[88:91], v[48:51]\n"
"ds_write_b128 v7, v[56:59] offset:0\n"
"v_mfma_f32_16x16x32_bf16 v[52:55], v[160:163], v[88:91], v[52:55]\n"
"ds_write_b128 v6, v[60:63] offset:0\n"
"v_mfma_f32_16x16x32_bf16 v[48:51], v[176:179], v[92:95], v[48:51]\n"
"ds_write_b128 v6, v[64:67] offset:1024\n"
"v_mfma_f32_16x16x32_bf16 v[52:55], v[180:183], v[92:95], v[52:55]\n"
"ds_write_b32 v4, v4 offset:752\n"
"v_mfma_f32_16x16x32_bf16 v[48:51], v[196:199], v[96:99], v[48:51]\n"
"s_mov_b32 s17, 0x100000\n"
"s_waitcnt lgkmcnt(10)\n"
"v_cmp_eq_u32 vcc, 0, v3\n"
"s_cbranch_vccz .Lok_512\n"
".Ltp_511:\n"
"ds_read_b32 v3, v5 offset:736\n"
"ds_read_b128 v[76:79], v12 offset:2048\n"
"ds_read_b128 v[108:111], v13 offset:2048\n"
"ds_read_b128 v[68:71], v12 offset:0\n"
"ds_read_b128 v[80:83], v13 offset:0\n"
"ds_read_b128 v[72:75], v12 offset:1024\n"
"ds_read_b128 v[84:87], v13 offset:1024\n"
"s_sub_u32 s17, s17, 1\n"
"s_cbranch_scc1 .Lok_512\n"
"s_waitcnt lgkmcnt(6)\n"
"v_cmp_eq_u32 vcc, 0, v3\n"
"s_cbranch_vccnz .Ltp_511\n"
"s_waitcnt lgkmcnt(0)\n"
".Lok_512:\n"
"v_mfma_f32_16x16x32_bf16 v[52:55], v[200:203], v[96:99], v[52:55]\n"
"s_waitcnt lgkmcnt(8)\n"
"v_add_f32 v76, v76, v108\n"
"v_add_f32 v77, v77, v109\n"
"v_add_f32 v78, v78, v110\n"
"v_mfma_f32_16x16x32_bf16 v[48:51], v[216:219], v[100:103], v[48:51]\n"
"v_add_f32 v79, v79, v111\n"
"s_waitcnt lgkmcnt(6)\n"
"v_add_f32 v68, v68, v80\n"
"v_add_f32 v69, v69, v81\n"
"v_mfma_f32_16x16x32_bf16 v[52:55], v[220:223], v[100:103], v[52:55]\n"
"v_add_f32 v70, v70, v82\n"
"v_add_f32 v71, v71, v83\n"
"s_waitcnt lgkmcnt(4)\n"
"v_add_f32 v72, v72, v84\n"
"v_mfma_f32_16x16x32_bf16 v[48:51], v[236:239], v[104:107], v[48:51]\n"
"v_add_f32 v73, v73, v85\n"
"v_add_f32 v74, v74, v86\n"
"v_add_f32 v75, v75, v87\n"
"v_mfma_f32_16x16x32_bf16 v[52:55], v[240:243], v[104:107], v[52:55]\n"
"ds_read_b32 v3, v5 offset:752\n"
"ds_read_b128 v[112:115], v11 offset:2048\n"
"ds_read_b128 v[60:63], v11 offset:0\n"
"ds_read_b128 v[64:67], v11 offset:1024\n"
"v_add_f32 v56, v56, v76\n"
"v_add_f32 v57, v57, v77\n"
"v_add_f32 v58, v58, v78\n"
"v_add_f32 v59, v59, v79\n"
"v_add_f32 v48, v48, v68\n"
"v_add_f32 v49, v49, v69\n"
"v_add_f32 v50, v50, v70\n"
"v_add_f32 v51, v51, v71\n"
"v_add_f32 v52, v52, v72\n"
"v_add_f32 v53, v53, v73\n"
"v_add_f32 v54, v54, v74\n"
"v_add_f32 v55, v55, v75\n"
"s_mov_b32 s17, 0x100000\n"
"s_waitcnt lgkmcnt(3)\n"
"v_cmp_eq_u32 vcc, 0, v3\n"
"s_cbranch_vccz .Lok_514\n"
".Ltp_513:\n"
"ds_read_b32 v3, v5 offset:752\n"
"ds_read_b128 v[112:115], v11 offset:2048\n"
"ds_read_b128 v[60:63], v11 offset:0\n"
"ds_read_b128 v[64:67], v11 offset:1024\n"
"s_sub_u32 s17, s17, 1\n"
"s_cbranch_scc1 .Lok_514\n"
"s_waitcnt lgkmcnt(3)\n"
"v_cmp_eq_u32 vcc, 0, v3\n"
"s_cbranch_vccnz .Ltp_513\n"
"s_waitcnt lgkmcnt(0)\n"
".Lok_514:\n"
"s_waitcnt lgkmcnt(2)\n"
"v_add_f32 v56, v56, v112\n"
"v_add_f32 v57, v57, v113\n"
"v_add_f32 v58, v58, v114\n"
"v_add_f32 v59, v59, v115\n"
"v_cvt_pk_bf16_f32 v44, v56, v57\n"
"v_cvt_pk_bf16_f32 v45, v58, v59\n"
"v_and_b32 v44, v19, v44\n"
"v_and_b32 v45, v19, v45\n"
"s_waitcnt lgkmcnt(1)\n"
"v_add_f32 v48, v48, v60\n"
"v_add_f32 v49, v49, v61\n"
"v_add_f32 v50, v50, v62\n"
"v_add_f32 v51, v51, v63\n"
"v_cvt_pk_bf16_f32 v40, v48, v49\n"
"v_cvt_pk_bf16_f32 v41, v50, v51\n"
"v_mfma_f32_16x16x32_bf16 v[88:91], v[44:47], v[44:47], 0\n"
"v_mfma_f32_16x16x32_bf16 v[92:95], v[20:23], v[44:47], 0\n"
"s_waitcnt lgkmcnt(0)\n"
"v_add_f32 v52, v52, v64\n"
"v_add_f32 v53, v53, v65\n"
"v_add_f32 v54, v54, v66\n"
"v_add_f32 v55, v55, v67\n"
"v_cvt_pk_bf16_f32 v42, v52, v53\n"
"v_cvt_pk_bf16_f32 v43, v54, v55\n"
"v_cvt_pk_bf16_f32 v116, v48, v49\n"
"v_cvt_pk_bf16_f32 v118, v50, v51\n"
"v_mfma_f32_16x16x32_bf16 v[88:91], v[40:43], v[40:43], v[88:91]\n"
"v_mfma_f32_16x16x32_bf16 v[92:95], v[20:23], v[40:43], v[92:95]\n"
"v_cvt_pk_bf16_f32 v120, v52, v53\n"
"v_cvt_pk_bf16_f32 v122, v54, v55\n"
"v_cvt_pk_bf16_f32 v124, v56, v57\n"
"v_cvt_pk_bf16_f32 v126, v58, v59\n"
"s_nop 0\n"
"v_cndmask_b32 v101, v88, v90, s[44:45]\n"
"v_cndmask_b32 v88, v89, v91, s[44:45]\n"
"s_nop 0\n"
"v_min_u32 v100, -2, v92\n"
"v_min_u32 v88, -2, v88\n"
"v_min_u32 v101, -2, v101\n"
"v_mov_b32_dpp v102, v100 quad_perm:[1,0,3,2] row_mask:0xf bank_mask:0xf\n"
"v_mov_b32_dpp v103, v88 quad_perm:[1,0,3,2] row_mask:0xf bank_mask:0xf\n"
"buffer_store_dwordx4 v[100:103], v16, s[4:7], s38 offen sc1\n"
"buffer_store_dwordx4 v[116:119], v17, s[4:7], s37 offen offset:0 sc1\n"
"buffer_store_dwordx4 v[120:123], v17, s[4:7], s37 offen offset:1024 sc1\n"
"buffer_store_dwordx4 v[124:127], v18, s[4:7], s37 offen offset:2048 sc1\n"
"ds_write_b32 v4, v4 offset:512\n"
"s_xor_b32 s79, s3, 1\n"
"s_lshl_b32 s79, s79, 8\n"
"s_add_u32 s32, s19, s79\n"
"s_cmp_ge_u32 s32, 64\n"
"s_cbranch_scc1 .Lnr_515\n"
"s_lshl_b32 s79, s19, 11\n"
"s_add_u32 s32, s79, 0x1628000\n"
"s_lshl_b32 s79, s19, 7\n"
"s_add_u32 s33, s79, 0x1668000\n"
"v_lshrrev_b32 v99, 3, v0\n"
"v_and_b32 v98, 7, v0\n"
"v_lshlrev_b32 v98, 4, v98\n"
"v_lshl_add_u32 v40, v99, 14, v98\n"
"v_mov_b32 v41, 0x3727c5ac\n"
"s_mov_b32 s89, 14\n"
".Ldl_516:\n"
"s_sub_u32 s89, s89, 1\n"
"s_cbranch_scc1 .Lde_517\n"
"s_sleep 1\n"
"s_branch .Ldl_516\n"
".Lde_517:\n"
"buffer_load_dwordx4 v[48:51], v2, s[4:7], s32 offen offset:0 sc1\n"
"buffer_load_dwordx4 v[52:55], v2, s[4:7], s32 offen offset:1024 sc1\n"
"s_mov_b32 s17, 32768\n"
".Ltp_518:\n"
"s_waitcnt vmcnt(0)\n"
"s_sub_u32 s17, s17, 1\n"
"s_cbranch_scc1 .Lou_519\n"
"v_max_u32 v38, v48, v49\n"
"v_max3_u32 v38, v50, v51, v38\n"
"v_max3_u32 v38, v52, v53, v38\n"
"v_max3_u32 v38, v54, v55, v38\n"
"v_cmp_eq_u32 vcc, -1, v38\n"
"s_cbranch_vccz .Lou_519\n"
"v_max_u32 v38, v50, v51\n"
"v_max3_u32 v38, v48, v49, v38\n"
"v_cmp_eq_u32 vcc, -1, v38\n"
"s_cbranch_vccz .Lsk_520\n"
"s_mov_b64 exec, vcc\n"
"buffer_load_dwordx4 v[48:51], v2, s[4:7], s32 offen offset:0 sc1\n"
"s_mov_b64 exec, -1\n"
".Lsk_520:\n"
"v_max_u32 v38, v54, v55\n"
"v_max3_u32 v38, v52, v53, v38\n"
"v_cmp_eq_u32 vcc, -1, v38\n"
"s_cbranch_vccz .Lsk_521\n"
"s_mov_b64 exec, vcc\n"
"buffer_load_dwordx4 v[52:55], v2, s[4:7], s32 offen offset:1024 sc1\n"
"s_mov_b64 exec, -1\n"
".Lsk_521:\n"
"s_branch .Ltp_518\n"
".Lou_519:\n"
"s_waitcnt vmcnt(0)\n"
"v_add_f32 v48, v48, v52\n"
"v_add_f32 v49, v49, v53\n"
"v_add_f32 v50, v50, v54\n"
"v_add_f32 v51, v51, v55\n"
"v_add_f32_dpp v52, v48, v48 row_ror:8 row_mask:0xf bank_mask:0xf\n"
"v_add_f32_dpp v53, v49, v49 row_ror:8 row_mask:0xf bank_mask:0xf\n"
"v_add_f32_dpp v54, v50, v50 row_ror:8 row_mask:0xf bank_mask:0xf\n"
"v_add_f32_dpp v55, v51, v51 row_ror:8 row_mask:0xf bank_mask:0xf\n"
"v_mov_b32 v48, v52\n"
"v_mov_b32 v49, v53\n"
"v_mov_b32 v50, v54\n"
"v_mov_b32 v51, v55\n"
"v_permlane16_swap_b32 v48, v52\n"
"v_permlane16_swap_b32 v49, v53\n"
"v_permlane16_swap_b32 v50, v54\n"
"v_permlane16_swap_b32 v51, v55\n"
"v_add_f32 v52, v48, v52\n"
"v_add_f32 v53, v49, v53\n"
"v_add_f32 v54, v50, v54\n"
"v_add_f32 v55, v51, v55\n"
"v_mov_b32 v48, v52\n"
"v_mov_b32 v49, v53\n"
"v_mov_b32 v50, v54\n"
"v_mov_b32 v51, v55\n"
"v_permlane32_swap_b32 v48, v52\n"
"v_permlane32_swap_b32 v49, v53\n"
"v_permlane32_swap_b32 v50, v54\n"
"v_permlane32_swap_b32 v51, v55\n"
"v_add_f32 v52, v48, v52\n"
"v_add_f32 v53, v49, v53\n"
"v_add_f32 v54, v50, v54\n"
"v_add_f32 v55, v51, v55\n"
"v_mul_f32 v48, 0x3acccccd, v52\n"
"v_mul_f32 v50, 0x3acccccd, v54\n"
"v_fma_f32 v53, -v52, v48, v53\n"
"v_fma_f32 v55, -v54, v50, v55\n"
"v_fmamk_f32 v53, v53, 0x3acccccd, v41\n"
"v_fmamk_f32 v55, v55, 0x3acccccd, v41\n"
"v_max_f32 v53, v53, v41\n"
"v_max_f32 v55, v55, v41\n"
"v_rsq_f32 v49, v53\n"
"v_rsq_f32 v51, v55\n"
"v_mul_f32 v48, -v48, v49\n"
"v_mul_f32 v50, -v50, v51\n"
"v_min_u32 v49, -2, v49\n"
"v_min_u32 v51, -2, v51\n"
"v_min_u32 v48, -2, v48\n"
"v_min_u32 v50, -2, v50\n"
"buffer_store_dwordx4 v[48:51], v40, s[4:7], s33 offen sc1\n"
"s_nop 3\n"
".Lnr_515:\n"
"s_mul_i32 s79, s28, 0xa00\n"
"s_add_u32 s79, s30, s79\n"
"s_and_b32 s78, s3, 1\n"
"s_mul_i32 s78, s78, 0x600\n"
"s_add_u32 s79, s79, s78\n"
"s_add_u32 s36, s79, 0xd68000\n"
"s_mul_i32 s79, s3, 0x1400\n"
"s_add_u32 s79, s30, s79\n"
"s_add_u32 s32, s79, 0xfe8000\n"
"s_add_u32 s33, s32, 0x1000\n"
"s_lshl_b32 s79, s29, 1\n"
"s_lshr_b32 s78, s3, 1\n"
"s_xor_b32 s78, s78, 1\n"
"s_add_u32 s79, s79, s78\n"
"s_mul_i32 s79, s79, 0x5000\n"
"s_mul_i32 s78, s3, 0x1400\n"
"s_add_u32 s79, s79, s78\n"
"s_add_u32 s34, s79, 0xfe8000\n"
"s_add_u32 s35, s34, 0x1000\n"
"s_lshl_b32 s79, s30, 1\n"
"s_mul_i32 s78, s28, 0x1400\n"
"s_add_u32 s79, s79, s78\n"
"s_and_b32 s78, s3, 1\n"
"s_mul_i32 s78, s78, 0xc00\n"
"s_add_u32 s79, s79, s78\n"
"s_add_u32 s37, s79, 0x13a8000\n"
"s_lshl_b32 s79, s29, 1\n"
"s_lshr_b32 s78, s3, 1\n"
"s_add_u32 s79, s79, s78\n"
"s_lshl_b32 s79, s79, 11\n"
"s_lshl_b32 s78, s28, 8\n"
"s_add_u32 s79, s79, s78\n"
"s_and_b32 s78, s3, 1\n"
"s_lshl_b32 s78, s78, 7\n"
"s_add_u32 s79, s79, s78\n"
"s_add_u32 s38, s79, 0x1648000\n"
"s_mov_b32 s17, 0x100000\n"
".Ltp_522:\n"
"ds_read_b32 v3, v5 offset:512\n"
"s_sub_u32 s17, s17, 1\n"
"s_cbranch_scc1 .Lou_523\n"
"s_waitcnt lgkmcnt(0)\n"
"v_cmp_eq_u32 vcc, 0, v3\n"
"s_cbranch_vccnz .Ltp_522\n"
".Lou_523:\n"
"s_waitcnt lgkmcnt(0)\n"
"s_mov_b32 s17, 0x100000\n"
".Ltp_527:\n"
"s_sleep 2\n"
"ds_read_b32 v3, v5 offset:4800\n"
"s_sub_u32 s17, s17, 1\n"
"s_cbranch_scc1 .Lou_528\n"
"s_waitcnt lgkmcnt(0)\n"
"v_cmp_eq_u32 vcc, -1, v3\n"
"s_cbranch_vccnz .Ltp_527\n"
".Lou_528:\n"
"s_waitcnt lgkmcnt(0)\n"
"s_mov_b32 s17, 32768\n"
".Ltp_524:\n"
"buffer_load_dwordx2 v[40:41], v14, s[4:7], s36 offen offset:0 sc1\n"
"buffer_load_dwordx2 v[42:43], v14, s[4:7], s36 offen offset:512 sc1\n"
"buffer_load_dwordx2 v[44:45], v15, s[4:7], s36 offen offset:1024 sc1\n"
"s_waitcnt vmcnt(0)\n"
".Lmd_526:\n"
"s_sub_u32 s17, s17, 1\n"
"s_cbranch_scc1 .Lou_525\n"
"v_max3_u32 v38, v40, v41, v42\n"
"v_max3_u32 v38, v43, v44, v38\n"
"v_max_u32 v38, v45, v38\n"
"v_cmp_eq_u32 vcc, -1, v38\n"
"s_cbranch_vccnz .Ltp_524\n"
".Lou_525:\n"
"v_lshlrev_b32 v48, 16, v40\n"
"v_and_b32 v49, 0xffff0000, v40\n"
"v_lshlrev_b32 v50, 16, v41\n"
"v_and_b32 v51, 0xffff0000, v41\n"
"v_lshlrev_b32 v52, 16, v42\n"
"v_and_b32 v53, 0xffff0000, v42\n"
"v_lshlrev_b32 v54, 16, v43\n"
"v_and_b32 v55, 0xffff0000, v43\n"
"v_lshlrev_b32 v56, 16, v44\n"
"v_and_b32 v57, 0xffff0000, v44\n"
"v_lshlrev_b32 v58, 16, v45\n"
"v_and_b32 v59, 0xffff0000, v45\n"
"v_mov_b32 v46, 0\n"
"v_mov_b32 v47, 0\n"
"s_mov_b32 s17, 0x100000\n"
".Ltp_529:\n"
"s_sleep 1\n"
"ds_read_b32 v3, v5 offset:496\n"
"s_sub_u32 s17, s17, 1\n"
"s_cbranch_scc1 .Lou_530\n"
"s_waitcnt lgkmcnt(0)\n"
"v_cmp_eq_u32 vcc, 0, v3\n"
"s_cbranch_vccnz .Ltp_529\n"
".Lou_530:\n"
"s_waitcnt lgkmcnt(0)\n"
"s_cmp_eq_u32 s17, 0xfffff\n"
"s_cbranch_scc1 .Lsk_531\n"
"s_mov_b32 s89, 19\n"
".Ldl_533:\n"
"s_sub_u32 s89, s89, 1\n"
"s_cbranch_scc1 .Lde_534\n"
"s_sleep 1\n"
"s_branch .Ldl_533\n"
".Lde_534:\n"
"s_branch .Ljn_532\n"
".Lsk_531:\n"
"s_mov_b32 s89, 12\n"
".Ldl_535:\n"
"s_sub_u32 s89, s89, 1\n"
"s_cbranch_scc1 .Lde_536\n"
"s_sleep 1\n"
"s_branch .Ldl_535\n"
".Lde_536:\n"
".Ljn_532:\n"
"buffer_load_dwordx4 v[108:111], v2, s[4:7], s34 offen offset:0 sc1\n"
"s_nop 9\n"
"buffer_load_dwordx4 v[112:115], v2, s[4:7], s34 offen offset:1024 sc1\n"
"s_nop 9\n"
"buffer_load_dwordx4 v[116:119], v2, s[4:7], s34 offen offset:2048 sc1\n"
"s_nop 9\n"
"buffer_load_dwordx4 v[120:123], v2, s[4:7], s34 offen offset:3072 sc1\n"
"s_nop 9\n"
"buffer_load_dwordx4 v[124:127], v2, s[4:7], s35 offen offset:0 sc1\n"
"s_nop 9\n"
"buffer_load_dwordx4 v[88:91], v2, s[4:7], s32 offen offset:0 sc1\n"
"s_nop 9\n"
"buffer_load_dwordx4 v[92:95], v2, s[4:7], s32 offen offset:1024 sc1\n"
"s_nop 9\n"
"buffer_load_dwordx4 v[96:99], v2, s[4:7], s32 offen offset:2048 sc1\n"
"s_nop 9\n"
"buffer_load_dwordx4 v[100:103], v2, s[4:7], s32 offen offset:3072 sc1\n"
"s_nop 9\n"
"buffer_load_dwordx4 v[104:107], v2, s[4:7], s33 offen offset:0 sc1\n"
"s_waitcnt vmcnt(9)\n"
"v_max_u32 v38, v108, v109\n"
"v_max3_u32 v38, v110, v111, v38\n"
"v_cmp_eq_u32 vcc, -1, v38\n"
"s_cbranch_vccnz .Lsl_537\n"
".Lbk_538:\n"
"v_mfma_f32_16x16x32_bf16 v[68:71], v[156:159], v[108:111], 0\n"
"v_mfma_f32_16x16x32_bf16 v[72:75], v[160:163], v[108:111], 0\n"
"v_mfma_f32_16x16x32_bf16 v[76:79], v[164:167], v[108:111], 0\n"
"s_waitcnt vmcnt(8)\n"
"v_max_u32 v38, v112, v113\n"
"v_max3_u32 v38, v114, v115, v38\n"
"v_mfma_f32_16x16x32_bf16 v[80:83], v[168:171], v[108:111], 0\n"
"v_cmp_eq_u32 vcc, -1, v38\n"
"v_mfma_f32_16x16x32_bf16 v[84:87], v[172:175], v[108:111], 0\n"
"s_cbranch_vccnz .Lsl_539\n"
".Lbk_540:\n"
"v_mfma_f32_16x16x32_bf16 v[68:71], v[176:179], v[112:115], v[68:71]\n"
"v_mfma_f32_16x16x32_bf16 v[72:75], v[180:183], v[112:115], v[72:75]\n"
"v_mfma_f32_16x16x32_bf16 v[76:79], v[184:187], v[112:115], v[76:79]\n"
"s_waitcnt vmcnt(7)\n"
"v_max_u32 v38, v116, v117\n"
"v_max3_u32 v38, v118, v119, v38\n"
"v_mfma_f32_16x16x32_bf16 v[80:83], v[188:191], v[112:115], v[80:83]\n"
"v_cmp_eq_u32 vcc, -1, v38\n"
"v_mfma_f32_16x16x32_bf16 v[84:87], v[192:195], v[112:115], v[84:87]\n"
"s_cbranch_vccnz .Lsl_541\n"
".Lbk_542:\n"
"v_mfma_f32_16x16x32_bf16 v[68:71], v[196:199], v[116:119], v[68:71]\n"
"v_mfma_f32_16x16x32_bf16 v[72:75], v[200:203], v[116:119], v[72:75]\n"
"v_mfma_f32_16x16x32_bf16 v[76:79], v[204:207], v[116:119], v[76:79]\n"
"s_waitcnt vmcnt(6)\n"
"v_max_u32 v38, v120, v121\n"
"v_max3_u32 v38, v122, v123, v38\n"
"v_mfma_f32_16x16x32_bf16 v[80:83], v[208:211], v[116:119], v[80:83]\n"
"v_cmp_eq_u32 vcc, -1, v38\n"
"v_mfma_f32_16x16x32_bf16 v[84:87], v[212:215], v[116:119], v[84:87]\n"
"s_cbranch_vccnz .Lsl_543\n"
".Lbk_544:\n"
"v_mfma_f32_16x16x32_bf16 v[68:71], v[216:219], v[120:123], v[68:71]\n"
"v_mfma_f32_16x16x32_bf16 v[72:75], v[220:223], v[120:123], v[72:75]\n"
"v_mfma_f32_16x16x32_bf16 v[76:79], v[224:227], v[120:123], v[76:79]\n"
"s_waitcnt vmcnt(5)\n"
"v_max_u32 v38, v124, v125\n"
"v_max3_u32 v38, v126, v127, v38\n"
"v_mfma_f32_16x16x32_bf16 v[80:83], v[228:231], v[120:123], v[80:83]\n"
"v_cmp_eq_u32 vcc, -1, v38\n"
"v_mfma_f32_16x16x32_bf16 v[84:87], v[232:235], v[120:123], v[84:87]\n"
"s_cbranch_vccnz .Lsl_545\n"
".Lbk_546:\n"
"v_mfma_f32_16x16x32_bf16 v[68:71], v[236:239], v[124:127], v[68:71]\n"
"v_mfma_f32_16x16x32_bf16 v[72:75], v[240:243], v[124:127], v[72:75]\n"
"v_mfma_f32_16x16x32_bf16 v[76:79], v[244:247], v[124:127], v[76:79]\n"
"s_waitcnt vmcnt(4)\n"
"v_max_u32 v38, v88, v89\n"
"v_max3_u32 v38, v90, v91, v38\n"
"v_mfma_f32_16x16x32_bf16 v[80:83], v[248:251], v[124:127], v[80:83]\n"
"v_cmp_eq_u32 vcc, -1, v38\n"
"v_mfma_f32_16x16x32_bf16 v[84:87], v[252:255], v[124:127], v[84:87]\n"
"ds_write_b128 v8, v[68:71] offset:0\n"
"s_cbranch_vccnz .Lsl_547\n"
".Lbk_548:\n"
"v_mfma_f32_16x16x32_bf16 v[56:59], v[164:167], v[88:91], v[56:59]\n"
"s_waitcnt vmcnt(3)\n"
"v_max_u32 v38, v92, v93\n"
"v_max3_u32 v38, v94, v95, v38\n"
"ds_write_b128 v8, v[72:75] offset:1024\n"
"v_mfma_f32_16x16x32_bf16 v[60:63], v[168:171], v[88:91], 0\n"
"v_cmp_eq_u32 vcc, -1, v38\n"
"ds_write_b128 v10, v[76:79] offset:0\n"
"v_mfma_f32_16x16x32_bf16 v[64:67], v[172:175], v[88:91], 0\n"
"v_mov_b32 v117, s16\n"
"s_cbranch_vccnz .Lsl_549\n"
".Lbk_550:\n"
"v_mfma_f32_16x16x32_bf16 v[56:59], v[184:187], v[92:95], v[56:59]\n"
"s_waitcnt vmcnt(2)\n"
"v_max_u32 v38, v96, v97\n"
"v_max3_u32 v38, v98, v99, v38\n"
"v_mov_b32 v119, s16\n"
"v_mfma_f32_16x16x32_bf16 v[60:63], v[188:191], v[92:95], v[60:63]\n"
"v_cmp_eq_u32 vcc, -1, v38\n"
"ds_write_b128 v9, v[80:83] offset:0\n"
"v_mfma_f32_16x16x32_bf16 v[64:67], v[192:195], v[92:95], v[64:67]\n"
"ds_write_b128 v9, v[84:87] offset:1024\n"
"s_cbranch_vccnz .Lsl_551\n"
".Lbk_552:\n"
"v_mfma_f32_16x16x32_bf16 v[56:59], v[204:207], v[96:99], v[56:59]\n"
"s_waitcnt vmcnt(1)\n"
"v_max_u32 v38, v100, v101\n"
"v_max3_u32 v38, v102, v103, v38\n"
"ds_write_b32 v4, v4 offset:768\n"
"v_mfma_f32_16x16x32_bf16 v[60:63], v[208:211], v[96:99], v[60:63]\n"
"v_cmp_eq_u32 vcc, -1, v38\n"
"v_mov_b32 v121, s16\n"
"v_mov_b32 v123, s16\n"
"v_mfma_f32_16x16x32_bf16 v[64:67], v[212:215], v[96:99], v[64:67]\n"
"v_mov_b32 v125, s16\n"
"s_cbranch_vccnz .Lsl_553\n"
".Lbk_554:\n"
"v_mfma_f32_16x16x32_bf16 v[56:59], v[224:227], v[100:103], v[56:59]\n"
"s_waitcnt vmcnt(0)\n"
"v_max_u32 v38, v104, v105\n"
"v_max3_u32 v38, v106, v107, v38\n"
"v_mov_b32 v127, s16\n"
"v_mfma_f32_16x16x32_bf16 v[60:63], v[228:231], v[100:103], v[60:63]\n"
"v_cmp_eq_u32 vcc, -1, v38\n"
"ds_read_b32 v3, v5 offset:768\n"
"v_mfma_f32_16x16x32_bf16 v[64:67], v[232:235], v[100:103], v[64:67]\n"
"s_cbranch_vccnz .Lsl_555\n"
".Lbk_556:\n"
"v_mfma_f32_16x16x32_bf16 v[56:59], v[244:247], v[104:107], v[56:59]\n"
"ds_read_b128 v[76:79], v12 offset:2048\n"
"ds_read_b128 v[108:111], v13 offset:2048\n"
"v_mfma_f32_16x16x32_bf16 v[60:63], v[248:251], v[104:107], v[60:63]\n"
"ds_read_b128 v[68:71], v12 offset:0\n"
"ds_read_b128 v[80:83], v13 offset:0\n"
"v_mfma_f32_16x16x32_bf16 v[64:67], v[252:255], v[104:107], v[64:67]\n"
"ds_read_b128 v[72:75], v12 offset:1024\n"
"ds_read_b128 v[84:87], v13 offset:1024\n"
"v_mfma_f32_16x16x32_bf16 v[48:51], v[156:159], v[88:91], v[48:51]\n"
"ds_write_b128 v7, v[56:59] offset:0\n"
"v_mfma_f32_16x16x32_bf16 v[52:55], v[160:163], v[88:91], v[52:55]\n"
"ds_write_b128 v6, v[60:63] offset:0\n"
"v_mfma_f32_16x16x32_bf16 v[48:51], v[176:179], v[92:95], v[48:51]\n"
"ds_write_b128 v6, v[64:67] offset:1024\n"
"v_mfma_f32_16x16x32_bf16 v[52:55], v[180:183], v[92:95], v[52:55]\n"
"ds_write_b32 v4, v4 offset:784\n"
"v_mfma_f32_16x16x32_bf16 v[48:51], v[196:199], v[96:99], v[48:51]\n"
"s_mov_b32 s17, 0x100000\n"
"s_waitcnt lgkmcnt(10)\n"
"v_cmp_eq_u32 vcc, 0, v3\n"
"s_cbranch_vccz .Lok_558\n"
".Ltp_557:\n"
"ds_read_b32 v3, v5 offset:768\n"
"ds_read_b128 v[76:79], v12 offset:2048\n"
"ds_read_b128 v[108:111], v13 offset:2048\n"
"ds_read_b128 v[68:71], v12 offset:0\n"
"ds_read_b128 v[80:83], v13 offset:0\n"
"ds_read_b128 v[72:75], v12 offset:1024\n"
"ds_read_b128 v[84:87], v13 offset:1024\n"
"s_sub_u32 s17, s17, 1\n"
"s_cbranch_scc1 .Lok_558\n"
"s_waitcnt lgkmcnt(6)\n"
"v_cmp_eq_u32 vcc, 0, v3\n"
"s_cbranch_vccnz .Ltp_557\n"
"s_waitcnt lgkmcnt(0)\n"
".Lok_558:\n"
"v_mfma_f32_16x16x32_bf16 v[52:55], v[200:203], v[96:99], v[52:55]\n"
"s_waitcnt lgkmcnt(8)\n"
"v_add_f32 v76, v76, v108\n"
"v_add_f32 v77, v77, v109\n"
"v_add_f32 v78, v78, v110\n"
"v_mfma_f32_16x16x32_bf16 v[48:51], v[216:219], v[100:103], v[48:51]\n"
"v_add_f32 v79, v79, v111\n"
"s_waitcnt lgkmcnt(6)\n"
"v_add_f32 v68, v68, v80\n"
"v_add_f32 v69, v69, v81\n"
"v_mfma_f32_16x16x32_bf16 v[52:55], v[220:223], v[100:103], v[52:55]\n"
"v_add_f32 v70, v70, v82\n"
"v_add_f32 v71, v71, v83\n"
"s_waitcnt lgkmcnt(4)\n"
"v_add_f32 v72, v72, v84\n"
"v_mfma_f32_16x16x32_bf16 v[48:51], v[236:239], v[104:107], v[48:51]\n"
"v_add_f32 v73, v73, v85\n"
"v_add_f32 v74, v74, v86\n"
"v_add_f32 v75, v75, v87\n"
"v_mfma_f32_16x16x32_bf16 v[52:55], v[240:243], v[104:107], v[52:55]\n"
"ds_read_b32 v3, v5 offset:784\n"
"ds_read_b128 v[112:115], v11 offset:2048\n"
"ds_read_b128 v[60:63], v11 offset:0\n"
"ds_read_b128 v[64:67], v11 offset:1024\n"
"v_add_f32 v56, v56, v76\n"
"v_add_f32 v57, v57, v77\n"
"v_add_f32 v58, v58, v78\n"
"v_add_f32 v59, v59, v79\n"
"v_add_f32 v48, v48, v68\n"
"v_add_f32 v49, v49, v69\n"
"v_add_f32 v50, v50, v70\n"
"v_add_f32 v51, v51, v71\n"
"v_add_f32 v52, v52, v72\n"
"v_add_f32 v53, v53, v73\n"
"v_add_f32 v54, v54, v74\n"
"v_add_f32 v55, v55, v75\n"
"s_mov_b32 s17, 0x100000\n"
"s_waitcnt lgkmcnt(3)\n"
"v_cmp_eq_u32 vcc, 0, v3\n"
"s_cbranch_vccz .Lok_560\n"
".Ltp_559:\n"
"ds_read_b32 v3, v5 offset:784\n"
"ds_read_b128 v[112:115], v11 offset:2048\n"
"ds_read_b128 v[60:63], v11 offset:0\n"
"ds_read_b128 v[64:67], v11 offset:1024\n"
"s_sub_u32 s17, s17, 1\n"
"s_cbranch_scc1 .Lok_560\n"
"s_waitcnt lgkmcnt(3)\n"
"v_cmp_eq_u32 vcc, 0, v3\n"
"s_cbranch_vccnz .Ltp_559\n"
"s_waitcnt lgkmcnt(0)\n"
".Lok_560:\n"
"s_waitcnt lgkmcnt(2)\n"
"v_add_f32 v56, v56, v112\n"
"v_add_f32 v57, v57, v113\n"
"v_add_f32 v58, v58, v114\n"
"v_add_f32 v59, v59, v115\n"
"v_cvt_pk_bf16_f32 v44, v56, v57\n"
"v_cvt_pk_bf16_f32 v45, v58, v59\n"
"v_and_b32 v44, v19, v44\n"
"v_and_b32 v45, v19, v45\n"
"s_waitcnt lgkmcnt(1)\n"
"v_add_f32 v48, v48, v60\n"
"v_add_f32 v49, v49, v61\n"
"v_add_f32 v50, v50, v62\n"
"v_add_f32 v51, v51, v63\n"
"v_cvt_pk_bf16_f32 v40, v48, v49\n"
"v_cvt_pk_bf16_f32 v41, v50, v51\n"
"v_mfma_f32_16x16x32_bf16 v[88:91], v[44:47], v[44:47], 0\n"
"v_mfma_f32_16x16x32_bf16 v[92:95], v[20:23], v[44:47], 0\n"
"s_waitcnt lgkmcnt(0)\n"
"v_add_f32 v52, v52, v64\n"
"v_add_f32 v53, v53, v65\n"
"v_add_f32 v54, v54, v66\n"
"v_add_f32 v55, v55, v67\n"
"v_cvt_pk_bf16_f32 v42, v52, v53\n"
"v_cvt_pk_bf16_f32 v43, v54, v55\n"
"v_cvt_pk_bf16_f32 v116, v48, v49\n"
"v_cvt_pk_bf16_f32 v118, v50, v51\n"
"v_mfma_f32_16x16x32_bf16 v[88:91], v[40:43], v[40:43], v[88:91]\n"
"v_mfma_f32_16x16x32_bf16 v[92:95], v[20:23], v[40:43], v[92:95]\n"
"v_cvt_pk_bf16_f32 v120, v52, v53\n"
"v_cvt_pk_bf16_f32 v122, v54, v55\n"
"v_cvt_pk_bf16_f32 v124, v56, v57\n"
"v_cvt_pk_bf16_f32 v126, v58, v59\n"
"s_nop 0\n"
"v_cndmask_b32 v101, v88, v90, s[44:45]\n"
"v_cndmask_b32 v88, v89, v91, s[44:45]\n"
"s_nop 0\n"
"v_min_u32 v100, -2, v92\n"
"v_min_u32 v88, -2, v88\n"
"v_min_u32 v101, -2, v101\n"
"v_mov_b32_dpp v102, v100 quad_perm:[1,0,3,2] row_mask:0xf bank_mask:0xf\n"
"v_mov_b32_dpp v103, v88 quad_perm:[1,0,3,2] row_mask:0xf bank_mask:0xf\n"
"buffer_store_dwordx4 v[100:103], v16, s[4:7], s38 offen sc1\n"
"buffer_store_dwordx4 v[116:119], v17, s[4:7], s37 offen offset:0 sc1\n"
"buffer_store_dwordx4 v[120:123], v17, s[4:7], s37 offen offset:1024 sc1\n"
"buffer_store_dwordx4 v[124:127], v18, s[4:7], s37 offen offset:2048 sc1\n"
"ds_write_b32 v4, v4 offset:800\n"
"s_xor_b32 s79, s3, 1\n"
"s_lshl_b32 s79, s79, 8\n"
"s_add_u32 s32, s19, s79\n"
"s_cmp_ge_u32 s32, 64\n"
"s_cbranch_scc1 .Lnr_561\n"
"s_lshl_b32 s79, s19, 11\n"
"s_add_u32 s32, s79, 0x1648000\n"
"s_lshl_b32 s79, s19, 7\n"
"s_add_u32 s33, s79, 0x166a000\n"
"v_lshrrev_b32 v99, 3, v0\n"
"v_and_b32 v98, 7, v0\n"
"v_lshlrev_b32 v98, 4, v98\n"
"v_lshl_add_u32 v40, v99, 14, v98\n"
"v_mov_b32 v41, 0x3727c5ac\n"
"s_mov_b32 s89, 16\n"
".Ldl_562:\n"
"s_sub_u32 s89, s89, 1\n"
"s_cbranch_scc1 .Lde_563\n"
"s_sleep 1\n"
"s_branch .Ldl_562\n"
".Lde_563:\n"
"buffer_load_dwordx4 v[48:51], v2, s[4:7], s32 offen offset:0 sc1\n"
"buffer_load_dwordx4 v[52:55], v2, s[4:7], s32 offen offset:1024 sc1\n"
"s_mov_b32 s17, 32768\n"
".Ltp_564:\n"
"s_waitcnt vmcnt(0)\n"
"s_sub_u32 s17, s17, 1\n"
"s_cbranch_scc1 .Lou_565\n"
"v_max_u32 v38, v48, v49\n"
"v_max3_u32 v38, v50, v51, v38\n"
"v_max3_u32 v38, v52, v53, v38\n"
"v_max3_u32 v38, v54, v55, v38\n"
"v_cmp_eq_u32 vcc, -1, v38\n"
"s_cbranch_vccz .Lou_565\n"
"v_max_u32 v38, v50, v51\n"
"v_max3_u32 v38, v48, v49, v38\n"
"v_cmp_eq_u32 vcc, -1, v38\n"
"s_cbranch_vccz .Lsk_566\n"
"s_mov_b64 exec, vcc\n"
"buffer_load_dwordx4 v[48:51], v2, s[4:7], s32 offen offset:0 sc1\n"
"s_mov_b64 exec, -1\n"
".Lsk_566:\n"
"v_max_u32 v38, v54, v55\n"
"v_max3_u32 v38, v52, v53, v38\n"
"v_cmp_eq_u32 vcc, -1, v38\n"
"s_cbranch_vccz .Lsk_567\n"
"s_mov_b64 exec, vcc\n"
"buffer_load_dwordx4 v[52:55], v2, s[4:7], s32 offen offset:1024 sc1\n"
"s_mov_b64 exec, -1\n"
".Lsk_567:\n"
"s_branch .Ltp_564\n"
".Lou_565:\n"
"s_waitcnt vmcnt(0)\n"
"v_add_f32 v48, v48, v52\n"
"v_add_f32 v49, v49, v53\n"
"v_add_f32 v50, v50, v54\n"
"v_add_f32 v51, v51, v55\n"
"v_add_f32_dpp v52, v48, v48 row_ror:8 row_mask:0xf bank_mask:0xf\n"
"v_add_f32_dpp v53, v49, v49 row_ror:8 row_mask:0xf bank_mask:0xf\n"
"v_add_f32_dpp v54, v50, v50 row_ror:8 row_mask:0xf bank_mask:0xf\n"
"v_add_f32_dpp v55, v51, v51 row_ror:8 row_mask:0xf bank_mask:0xf\n"
"v_mov_b32 v48, v52\n"
"v_mov_b32 v49, v53\n"
"v_mov_b32 v50, v54\n"
"v_mov_b32 v51, v55\n"
"v_permlane16_swap_b32 v48, v52\n"
"v_permlane16_swap_b32 v49, v53\n"
"v_permlane16_swap_b32 v50, v54\n"
"v_permlane16_swap_b32 v51, v55\n"
"v_add_f32 v52, v48, v52\n"
"v_add_f32 v53, v49, v53\n"
"v_add_f32 v54, v50, v54\n"
"v_add_f32 v55, v51, v55\n"
"v_mov_b32 v48, v52\n"
"v_mov_b32 v49, v53\n"
"v_mov_b32 v50, v54\n"
"v_mov_b32 v51, v55\n"
"v_permlane32_swap_b32 v48, v52\n"
"v_permlane32_swap_b32 v49, v53\n"
"v_permlane32_swap_b32 v50, v54\n"
"v_permlane32_swap_b32 v51, v55\n"
"v_add_f32 v52, v48, v52\n"
"v_add_f32 v53, v49, v53\n"
"v_add_f32 v54, v50, v54\n"
"v_add_f32 v55, v51, v55\n"
"v_mul_f32 v48, 0x3acccccd, v52\n"
"v_mul_f32 v50, 0x3acccccd, v54\n"
"v_fma_f32 v53, -v52, v48, v53\n"
"v_fma_f32 v55, -v54, v50, v55\n"
"v_fmamk_f32 v53, v53, 0x3acccccd, v41\n"
"v_fmamk_f32 v55, v55, 0x3acccccd, v41\n"
"v_max_f32 v53, v53, v41\n"
"v_max_f32 v55, v55, v41\n"
"v_rsq_f32 v49, v53\n"
"v_rsq_f32 v51, v55\n"
"v_mul_f32 v48, -v48, v49\n"
"v_mul_f32 v50, -v50, v51\n"
"v_min_u32 v49, -2, v49\n"
"v_min_u32 v51, -2, v51\n"
"v_min_u32 v48, -2, v48\n"
"v_min_u32 v50, -2, v50\n"
"buffer_store_dwordx4 v[48:51], v40, s[4:7], s33 offen sc1\n"
"s_nop 3\n"
".Lnr_561:\n"
"s_cmp_lt_i32 s18, 0\n"
"s_cbranch_scc1 .Lnc_568\n"
"s_mov_b32 s17, 0x100000\n"
".Ltp_569:\n"
"s_sleep 1\n"
"ds_read_b32 v3, v5 offset:528\n"
"s_sub_u32 s17, s17, 1\n"
"s_cbranch_scc1 .Lou_570\n"
"s_waitcnt lgkmcnt(0)\n"
"v_cmp_eq_u32 vcc, 0, v3\n"
"s_cbranch_vccnz .Ltp_569\n"
".Lou_570:\n"
"s_waitcnt lgkmcnt(0)\n"
"s_setprio 0\n"
"s_lshl_b32 s79, s18, 8\n"
"s_add_u32 s32, s79, 0x13a8000\n"
"s_add_u32 s33, s32, 0x140000\n"
"s_and_b32 s79, s2, 7\n"
"s_lshl_b32 s79, s79, 14\n"
"s_add_u32 s34, s79, 0x166a000\n"
"s_add_u32 s35, s34, 0x1000\n"
"v_and_b32 v126, 0xffffff80, v29\n"
"v_add_u32 v126, v126, v29\n"
"ds_read_b128 v[88:91], v25 offset:448\n"
"ds_read_b128 v[92:95], v25 offset:432\n"
"ds_read_b64 v[96:97], v24 offset:448\n"
"ds_read_b64 v[98:99], v24 offset:456\n"
"ds_read_b128 v[100:103], v25 offset:960\n"
"ds_read_b128 v[104:107], v25 offset:1472\n"
"s_waitcnt lgkmcnt(1)\n"
"v_mfma_f32_16x16x32_f16 v[64:67], v[88:91], v[144:147], 0\n"
"ds_read_b128 v[40:43], v25 offset:1984\n"
"ds_read_b64 v[44:45], v24 offset:960\n"
"v_mfma_f32_16x16x32_f16 v[68:71], v[92:95], v[148:151], 0\n"
"ds_read_b64 v[46:47], v24 offset:968\n"
"ds_read_b64 v[72:73], v24 offset:1472\n"
"v_mfma_f32_16x16x32_f16 v[48:51], v[152:155], v[88:91], 0\n"
"ds_read_b64 v[74:75], v24 offset:1480\n"
"ds_read_b64 v[76:77], v24 offset:384\n"
"v_mfma_f32_16x16x32_f16 v[48:51], v[96:99], v[100:103], v[48:51]\n"
"ds_read_b64 v[78:79], v24 offset:392\n"
"s_waitcnt lgkmcnt(4)\n"
"v_mfma_f32_16x16x32_f16 v[52:55], v[96:99], v[104:107], 0\n"
"ds_read_b128 v[84:87], v25 offset:896\n"
"ds_read_b128 v[92:95], v25 offset:1408\n"
"v_mfma_f32_16x16x32_f16 v[56:59], v[96:99], v[40:43], 0\n"
"ds_read_b128 v[88:91], v25 offset:1920\n"
"ds_read_b64 v[100:101], v24 offset:896\n"
"v_mfma_f32_16x16x32_f16 v[52:55], v[44:47], v[40:43], v[52:55]\n"
"ds_read_b64 v[102:103], v24 offset:904\n"
"ds_read_b64 v[96:97], v24 offset:1408\n"
"v_cvt_pk_f16_f32 v80, v64, v65\n"
"v_mfma_f32_16x16x32_f16 v[48:51], v[44:47], v[104:107], v[48:51]\n"
"ds_read_b64 v[98:99], v24 offset:1416\n"
"s_waitcnt lgkmcnt(4)\n"
"v_mfma_f32_16x16x32_f16 v[48:51], v[72:75], v[40:43], v[48:51]\n"
"ds_read_b128 v[104:107], v25 offset:960\n"
"v_cvt_pk_f16_f32 v81, v66, v67\n"
"v_mfma_f32_16x16x32_f16 v[48:51], v[76:79], v[84:87], v[48:51]\n"
"ds_read_b128 v[44:47], v25 offset:944\n"
"v_cvt_pk_f16_f32 v82, v68, v69\n"
"v_mfma_f32_16x16x32_f16 v[52:55], v[76:79], v[92:95], v[52:55]\n"
"ds_read_b64 v[40:41], v24 offset:320\n"
"ds_read_b64 v[42:43], v24 offset:328\n"
"v_mfma_f32_16x16x32_f16 v[56:59], v[76:79], v[88:91], v[56:59]\n"
"ds_read_b128 v[72:75], v25 offset:832\n"
"s_waitcnt lgkmcnt(5)\n"
"v_mfma_f32_16x16x32_f16 v[52:55], v[100:103], v[88:91], v[52:55]\n"
"ds_read_b128 v[84:87], v25 offset:1344\n"
"v_cvt_pk_f16_f32 v83, v70, v71\n"
"v_mfma_f32_16x16x32_f16 v[48:51], v[100:103], v[92:95], v[48:51]\n"
"ds_read_b128 v[76:79], v25 offset:1856\n"
"ds_read_b64 v[92:93], v24 offset:832\n"
"v_mfma_f32_16x16x32_f16 v[48:51], v[96:99], v[88:91], v[48:51]\n"
"ds_read_b64 v[94:95], v24 offset:840\n"
"v_mfma_f32_16x16x32_f16 v[48:51], v[80:83], v[128:131], v[48:51]\n"
"ds_read_b64 v[100:101], v24 offset:1344\n"
"s_waitcnt lgkmcnt(9)\n"
"v_mfma_f32_16x16x32_f16 v[52:55], v[80:83], v[132:135], v[52:55]\n"
"ds_read_b64 v[102:103], v24 offset:1352\n"
"ds_read_b64 v[88:89], v24 offset:256\n"
"v_mfma_f32_16x16x32_f16 v[56:59], v[80:83], v[136:139], v[56:59]\n"
"ds_read_b64 v[90:91], v24 offset:264\n"
"v_mfma_f32_16x16x32_f16 v[60:63], v[80:83], v[140:143], 0\n"
"ds_read_b128 v[96:99], v25 offset:768\n"
"v_mfma_f32_16x16x32_f16 v[64:67], v[104:107], v[144:147], 0\n"
"s_waitcnt lgkmcnt(8)\n"
"v_mfma_f32_16x16x32_f16 v[68:71], v[44:47], v[148:151], 0\n"
"ds_read_b128 v[44:47], v25 offset:1280\n"
"v_mfma_f32_16x16x32_f16 v[52:55], v[152:155], v[104:107], v[52:55]\n"
"ds_read_b128 v[104:107], v25 offset:1792\n"
"v_mfma_f32_16x16x32_f16 v[48:51], v[40:43], v[72:75], v[48:51]\n"
"ds_read_b64 v[72:73], v24 offset:768\n"
"v_mfma_f32_16x16x32_f16 v[52:55], v[40:43], v[84:87], v[52:55]\n"
"ds_read_b64 v[74:75], v24 offset:776\n"
"s_waitcnt lgkmcnt(7)\n"
"v_mfma_f32_16x16x32_f16 v[56:59], v[40:43], v[76:79], v[56:59]\n"
"ds_read_b64 v[40:41], v24 offset:1280\n"
"v_mfma_f32_16x16x32_f16 v[52:55], v[92:95], v[76:79], v[52:55]\n"
"ds_read_b64 v[42:43], v24 offset:1288\n"
"v_cvt_pk_f16_f32 v80, v64, v65\n"
"v_mfma_f32_16x16x32_f16 v[48:51], v[92:95], v[84:87], v[48:51]\n"
"ds_read_b128 v[84:87], v25 offset:1472\n"
"v_cvt_pk_f16_f32 v81, v66, v67\n"
"v_mfma_f32_16x16x32_f16 v[48:51], v[100:103], v[76:79], v[48:51]\n"
"ds_read_b128 v[92:95], v25 offset:1456\n"
"s_waitcnt lgkmcnt(4)\n"
"v_mfma_f32_16x16x32_f16 v[48:51], v[88:91], v[96:99], v[48:51]\n"
"ds_read_b64 v[76:77], v24 offset:192\n"
"ds_read_b64 v[78:79], v24 offset:200\n"
"v_mfma_f32_16x16x32_f16 v[52:55], v[88:91], v[44:47], v[52:55]\n"
"ds_read_b128 v[100:103], v25 offset:704\n"
"v_cvt_pk_f16_f32 v82, v68, v69\n"
"v_mfma_f32_16x16x32_f16 v[56:59], v[88:91], v[104:107], v[56:59]\n"
"ds_read_b128 v[96:99], v25 offset:1216\n"
"v_cvt_pk_f16_f32 v83, v70, v71\n"
"v_mfma_f32_16x16x32_f16 v[52:55], v[72:75], v[104:107], v[52:55]\n"
"ds_read_b128 v[88:91], v25 offset:1728\n"
"s_waitcnt lgkmcnt(7)\n"
"v_mfma_f32_16x16x32_f16 v[48:51], v[72:75], v[44:47], v[48:51]\n"
"ds_read_b64 v[44:45], v24 offset:704\n"
"v_mfma_f32_16x16x32_f16 v[48:51], v[40:43], v[104:107], v[48:51]\n"
"ds_read_b64 v[46:47], v24 offset:712\n"
"ds_read_b64 v[72:73], v24 offset:1216\n"
"v_mfma_f32_16x16x32_f16 v[52:55], v[80:83], v[128:131], v[52:55]\n"
"ds_read_b64 v[74:75], v24 offset:1224\n"
"ds_read_b64 v[104:105], v24 offset:128\n"
"v_mfma_f32_16x16x32_f16 v[56:59], v[80:83], v[132:135], v[56:59]\n"
"ds_read_b64 v[106:107], v24 offset:136\n"
"s_waitcnt lgkmcnt(11)\n"
"v_mfma_f32_16x16x32_f16 v[60:63], v[80:83], v[136:139], v[60:63]\n"
"ds_read_b128 v[40:43], v25 offset:640\n"
"v_mfma_f32_16x16x32_f16 v[64:67], v[84:87], v[144:147], 0\n"
"v_mfma_f32_16x16x32_f16 v[68:71], v[92:95], v[148:151], 0\n"
"ds_read_b128 v[92:95], v25 offset:1152\n"
"v_mfma_f32_16x16x32_f16 v[56:59], v[152:155], v[84:87], v[56:59]\n"
"ds_read_b128 v[84:87], v25 offset:1664\n"
"s_waitcnt lgkmcnt(7)\n"
"v_mfma_f32_16x16x32_f16 v[48:51], v[76:79], v[100:103], v[48:51]\n"
"ds_read_b64 v[100:101], v24 offset:640\n"
"v_mfma_f32_16x16x32_f16 v[52:55], v[76:79], v[96:99], v[52:55]\n"
"ds_read_b64 v[102:103], v24 offset:648\n"
"v_mfma_f32_16x16x32_f16 v[56:59], v[76:79], v[88:91], v[56:59]\n"
"ds_read_b64 v[76:77], v24 offset:1152\n"
"v_mfma_f32_16x16x32_f16 v[52:55], v[44:47], v[88:91], v[52:55]\n"
"ds_read_b64 v[78:79], v24 offset:1160\n"
"s_waitcnt lgkmcnt(5)\n"
"v_mfma_f32_16x16x32_f16 v[48:51], v[44:47], v[96:99], v[48:51]\n"
"ds_read_b128 v[96:99], v25 offset:1984\n"
"v_cvt_pk_f16_f32 v80, v64, v65\n"
"v_mfma_f32_16x16x32_f16 v[48:51], v[72:75], v[88:91], v[48:51]\n"
"ds_read_b128 v[44:47], v25 offset:1968\n"
"ds_read_b64 v[88:89], v24 offset:64\n"
"v_mfma_f32_16x16x32_f16 v[48:51], v[104:107], v[40:43], v[48:51]\n"
"ds_read_b64 v[90:91], v24 offset:72\n"
"ds_read_b128 v[72:75], v25 offset:576\n"
"v_mfma_f32_16x16x32_f16 v[52:55], v[104:107], v[92:95], v[52:55]\n"
"ds_read_b128 v[40:43], v25 offset:1088\n"
"s_waitcnt lgkmcnt(6)\n"
"v_mfma_f32_16x16x32_f16 v[56:59], v[104:107], v[84:87], v[56:59]\n"
"ds_read_b128 v[104:107], v25 offset:1600\n"
"v_cvt_pk_f16_f32 v81, v66, v67\n"
"v_mfma_f32_16x16x32_f16 v[52:55], v[100:103], v[84:87], v[52:55]\n"
"v_cvt_pk_f16_f32 v82, v68, v69\n"
"v_cvt_pk_f16_f32 v83, v70, v71\n"
"v_mfma_f32_16x16x32_f16 v[48:51], v[100:103], v[92:95], v[48:51]\n"
"ds_read_b64 v[92:93], v24 offset:576\n"
"ds_read_b64 v[94:95], v24 offset:584\n"
"v_mfma_f32_16x16x32_f16 v[48:51], v[76:79], v[84:87], v[48:51]\n"
"ds_read_b64 v[100:101], v24 offset:1088\n"
"s_waitcnt lgkmcnt(8)\n"
"v_mfma_f32_16x16x32_f16 v[56:59], v[80:83], v[128:131], v[56:59]\n"
"ds_read_b64 v[102:103], v24 offset:1096\n"
"ds_read_b64 v[84:85], v24 offset:0\n"
"v_mfma_f32_16x16x32_f16 v[60:63], v[80:83], v[132:135], v[60:63]\n"
"ds_read_b64 v[86:87], v24 offset:8\n"
"v_mfma_f32_16x16x32_f16 v[64:67], v[96:99], v[144:147], 0\n"
"ds_read_b128 v[76:79], v25 offset:512\n"
"v_mfma_f32_16x16x32_f16 v[68:71], v[44:47], v[148:151], 0\n"
"ds_read_b128 v[44:47], v25 offset:1024\n"
"s_waitcnt lgkmcnt(8)\n"
"v_mfma_f32_16x16x32_f16 v[60:63], v[152:155], v[96:99], v[60:63]\n"
"ds_read_b128 v[96:99], v25 offset:1536\n"
"v_mfma_f32_16x16x32_f16 v[48:51], v[88:91], v[72:75], v[48:51]\n"
"ds_read_b64 v[72:73], v24 offset:512\n"
"v_mfma_f32_16x16x32_f16 v[52:55], v[88:91], v[40:43], v[52:55]\n"
"ds_read_b64 v[74:75], v24 offset:520\n"
"v_mfma_f32_16x16x32_f16 v[56:59], v[88:91], v[104:107], v[56:59]\n"
"ds_read_b64 v[88:89], v24 offset:1024\n"
"s_waitcnt lgkmcnt(5)\n"
"v_mfma_f32_16x16x32_f16 v[52:55], v[92:95], v[104:107], v[52:55]\n"
"ds_read_b64 v[90:91], v24 offset:1032\n"
"v_cvt_pk_f16_f32 v80, v64, v65\n"
"v_mfma_f32_16x16x32_f16 v[48:51], v[92:95], v[40:43], v[48:51]\n"
"v_cvt_pk_f16_f32 v81, v66, v67\n"
"v_cvt_pk_f16_f32 v82, v68, v69\n"
"v_mfma_f32_16x16x32_f16 v[48:51], v[100:103], v[104:107], v[48:51]\n"
"v_cvt_pk_f16_f32 v83, v70, v71\n"
"v_mfma_f32_16x16x32_f16 v[48:51], v[84:87], v[76:79], v[48:51]\n"
"s_waitcnt lgkmcnt(2)\n"
"v_mfma_f32_16x16x32_f16 v[52:55], v[84:87], v[44:47], v[52:55]\n"
"v_mfma_f32_16x16x32_f16 v[56:59], v[84:87], v[96:99], v[56:59]\n"
"v_mfma_f32_16x16x32_f16 v[52:55], v[72:75], v[96:99], v[52:55]\n"
"v_mfma_f32_16x16x32_f16 v[48:51], v[72:75], v[44:47], v[48:51]\n"
"s_waitcnt lgkmcnt(0)\n"
"v_mfma_f32_16x16x32_f16 v[48:51], v[88:91], v[96:99], v[48:51]\n"
"v_mfma_f32_16x16x32_f16 v[60:63], v[80:83], v[128:131], v[60:63]\n"
"s_setprio 2\n"
"s_mov_b32 s17, 0x100000\n"
".Ltp_571:\n"
"ds_read_b32 v3, v5 offset:800\n"
"s_sub_u32 s17, s17, 1\n"
"s_cbranch_scc1 .Lou_572\n"
"s_waitcnt lgkmcnt(0)\n"
"v_cmp_eq_u32 vcc, 0, v3\n"
"s_cbranch_vccnz .Ltp_571\n"
".Lou_572:\n"
"s_waitcnt lgkmcnt(0)\n"
"buffer_load_dwordx4 v[110:113], v126, s[4:7], s32 offen offset:0 sc1\n"
"buffer_load_dwordx4 v[114:117], v126, s[4:7], s32 offen offset:128 sc1\n"
"buffer_load_dwordx4 v[118:121], v126, s[4:7], s33 offen offset:0 sc1\n"
"buffer_load_dwordx4 v[122:125], v126, s[4:7], s33 offen offset:128 sc1\n"
"ds_read_b64 v[128:129], v24 offset:1984\n"
"ds_read_b64 v[130:131], v24 offset:1992\n"
"ds_read_b64 v[132:133], v24 offset:1920\n"
"ds_read_b64 v[134:135], v24 offset:1928\n"
"ds_read_b64 v[136:137], v24 offset:1856\n"
"ds_read_b64 v[138:139], v24 offset:1864\n"
"ds_read_b64 v[140:141], v24 offset:1792\n"
"ds_read_b64 v[142:143], v24 offset:1800\n"
"ds_read_b64 v[144:145], v24 offset:1728\n"
"ds_read_b64 v[146:147], v24 offset:1736\n"
"ds_read_b64 v[148:149], v24 offset:1664\n"
"ds_read_b64 v[150:151], v24 offset:1672\n"
"ds_read_b64 v[152:153], v24 offset:1600\n"
"ds_read_b64 v[154:155], v24 offset:1608\n"
"ds_read_b64 v[156:157], v24 offset:1536\n"
"ds_read_b64 v[158:159], v24 offset:1544\n"
"ds_read_b64 v[160:161], v24 offset:1472\n"
"ds_read_b64 v[162:163], v24 offset:1480\n"
"ds_read_b64 v[164:165], v24 offset:1408\n"
"ds_read_b64 v[166:167], v24 offset:1416\n"
"ds_read_b64 v[168:169], v24 offset:1344\n"
"ds_read_b64 v[170:171], v24 offset:1352\n"
"ds_read_b64 v[172:173], v24 offset:1280\n"
"ds_read_b64 v[174:175], v24 offset:1288\n"
"ds_read_b64 v[176:177], v24 offset:1216\n"
"ds_read_b64 v[178:179], v24 offset:1224\n"
"ds_read_b64 v[180:181], v24 offset:1152\n"
"ds_read_b64 v[182:183], v24 offset:1160\n"
"ds_read_b64 v[184:185], v24 offset:1088\n"
"ds_read_b64 v[186:187], v24 offset:1096\n"
"ds_read_b64 v[188:189], v24 offset:1024\n"
"ds_read_b64 v[190:191], v24 offset:1032\n"
"ds_read_b64 v[192:193], v24 offset:960\n"
"ds_read_b64 v[194:195], v24 offset:968\n"
"ds_read_b64 v[196:197], v24 offset:896\n"
"ds_read_b64 v[198:199], v24 offset:904\n"
"ds_read_b64 v[200:201], v24 offset:832\n"
"ds_read_b64 v[202:203], v24 offset:840\n"
"ds_read_b64 v[204:205], v24 offset:768\n"
"ds_read_b64 v[206:207], v24 offset:776\n"
"ds_read_b64 v[208:209], v24 offset:704\n"
"ds_read_b64 v[210:211], v24 offset:712\n"
"ds_read_b64 v[212:213], v24 offset:640\n"
"ds_read_b64 v[214:215], v24 offset:648\n"
"ds_read_b64 v[216:217], v24 offset:576\n"
"ds_read_b64 v[218:219], v24 offset:584\n"
"ds_read_b64 v[220:221], v24 offset:512\n"
"ds_read_b64 v[222:223], v24 offset:520\n"
"ds_read_b64 v[224:225], v24 offset:448\n"
"ds_read_b64 v[226:227], v24 offset:456\n"
"ds_read_b64 v[228:229], v24 offset:384\n"
"ds_read_b64 v[230:231], v24 offset:392\n"
"ds_read_b64 v[232:233], v24 offset:320\n"
"ds_read_b64 v[234:235], v24 offset:328\n"
"ds_read_b64 v[236:237], v24 offset:256\n"
"ds_read_b64 v[238:239], v24 offset:264\n"
"ds_read_b64 v[240:241], v24 offset:192\n"
"ds_read_b64 v[242:243], v24 offset:200\n"
"ds_read_b64 v[244:245], v24 offset:128\n"
"ds_read_b64 v[246:247], v24 offset:136\n"
"ds_read_b64 v[248:249], v24 offset:64\n"
"ds_read_b64 v[250:251], v24 offset:72\n"
"ds_read_b64 v[252:253], v24 offset:0\n"
"ds_read_b64 v[254:255], v24 offset:8\n"
"s_mov_b32 s38, 0x05040100\n"
"s_mov_b32 s39, 0x07060302\n"
"s_mov_b32 s17, 32768\n"
".Ltp_573:\n"
"s_waitcnt vmcnt(0)\n"
"s_sub_u32 s17, s17, 1\n"
"s_cbranch_scc1 .Lou_574\n"
"v_bitop3_b32 v64, v111, v113, s16 bitop3:0x7e\n"
"v_bitop3_b32 v65, v115, v117, s16 bitop3:0x7e\n"
"v_bitop3_b32 v66, v119, v121, s16 bitop3:0x7e\n"
"v_bitop3_b32 v67, v123, v125, s16 bitop3:0x7e\n"
"v_or3_b32 v68, v64, v65, v66\n"
"v_or_b32 v68, v68, v67\n"
"v_cmp_ne_u32 vcc, 0, v68\n"
"s_cbranch_vccz .Lou_574\n"
"s_sleep 2\n"
"v_cmp_ne_u32 vcc, 0, v64\n"
"s_cbranch_vccz .Lsk_575\n"
"s_mov_b64 exec, vcc\n"
"buffer_load_dwordx4 v[110:113], v126, s[4:7], s32 offen offset:0 sc1\n"
"s_mov_b64 exec, -1\n"
".Lsk_575:\n"
"v_cmp_ne_u32 vcc, 0, v65\n"
"s_cbranch_vccz .Lsk_576\n"
"s_mov_b64 exec, vcc\n"
"buffer_load_dwordx4 v[114:117], v126, s[4:7], s32 offen offset:128 sc1\n"
"s_mov_b64 exec, -1\n"
".Lsk_576:\n"
"v_cmp_ne_u32 vcc, 0, v66\n"
"s_cbranch_vccz .Lsk_577\n"
"s_mov_b64 exec, vcc\n"
"buffer_load_dwordx4 v[118:121], v126, s[4:7], s33 offen offset:0 sc1\n"
"s_mov_b64 exec, -1\n"
".Lsk_577:\n"
"v_cmp_ne_u32 vcc, 0, v67\n"
"s_cbranch_vccz .Lsk_578\n"
"s_mov_b64 exec, vcc\n"
"buffer_load_dwordx4 v[122:125], v126, s[4:7], s33 offen offset:128 sc1\n"
"s_mov_b64 exec, -1\n"
".Lsk_578:\n"
"s_branch .Ltp_573\n"
".Lou_574:\n"
"s_waitcnt vmcnt(0)\n"
"buffer_load_dwordx4 v[88:91], v30, s[4:7], s34 offen sc1\n"
"buffer_load_dwordx4 v[92:95], v30, s[4:7], s35 offen sc1\n"
"v_perm_b32 v64, v114, v110, s38\n"
"v_perm_b32 v65, v114, v110, s39\n"
"v_perm_b32 v66, v116, v112, s38\n"
"v_perm_b32 v67, v116, v112, s39\n"
"ds_write_b32 v32, v64 offset:0\n"
"ds_write_b32 v33, v65 offset:2048\n"
"ds_write_b32 v32, v66 offset:4224\n"
"ds_write_b32 v33, v67 offset:6272\n"
"v_perm_b32 v64, v122, v118, s38\n"
"v_perm_b32 v65, v122, v118, s39\n"
"v_perm_b32 v66, v124, v120, s38\n"
"v_perm_b32 v67, v124, v120, s39\n"
"ds_write_b32 v32, v64 offset:1024\n"
"ds_write_b32 v33, v65 offset:3072\n"
"ds_write_b32 v32, v66 offset:5248\n"
"ds_write_b32 v33, v67 offset:7296\n"
"v_lshlrev_b32 v64, 16, v110\n"
"v_lshlrev_b32 v65, 16, v114\n"
"v_and_b32 v66, 0xffff0000, v110\n"
"v_and_b32 v67, 0xffff0000, v114\n"
"v_lshlrev_b32 v68, 16, v112\n"
"v_lshlrev_b32 v69, 16, v116\n"
"v_and_b32 v70, 0xffff0000, v112\n"
"v_and_b32 v71, 0xffff0000, v116\n"
"v_lshlrev_b32 v72, 16, v118\n"
"v_lshlrev_b32 v73, 16, v122\n"
"v_and_b32 v74, 0xffff0000, v118\n"
"v_and_b32 v75, 0xffff0000, v122\n"
"v_lshlrev_b32 v76, 16, v120\n"
"v_lshlrev_b32 v77, 16, v124\n"
"v_and_b32 v78, 0xffff0000, v120\n"
"v_and_b32 v79, 0xffff0000, v124\n"
"s_mov_b32 s17, 32768\n"
".Ltp_579:\n"
"s_waitcnt vmcnt(0)\n"
"s_sub_u32 s17, s17, 1\n"
"s_cbranch_scc1 .Lou_580\n"
"v_max_u32 v110, v88, v89\n"
"v_max3_u32 v110, v90, v91, v110\n"
"v_max3_u32 v110, v92, v93, v110\n"
"v_max3_u32 v110, v94, v95, v110\n"
"v_cmp_eq_u32 vcc, -1, v110\n"
"s_cbranch_vccz .Lou_580\n"
"v_max_u32 v110, v90, v91\n"
"v_max3_u32 v110, v88, v89, v110\n"
"v_cmp_eq_u32 vcc, -1, v110\n"
"s_cbranch_vccz .Lsk_581\n"
"s_mov_b64 exec, vcc\n"
"buffer_load_dwordx4 v[88:91], v30, s[4:7], s34 offen sc1\n"
"s_mov_b64 exec, -1\n"
".Lsk_581:\n"
"v_max_u32 v110, v94, v95\n"
"v_max3_u32 v110, v92, v93, v110\n"
"v_cmp_eq_u32 vcc, -1, v110\n"
"s_cbranch_vccz .Lsk_582\n"
"s_mov_b64 exec, vcc\n"
"buffer_load_dwordx4 v[92:95], v30, s[4:7], s35 offen sc1\n"
"s_mov_b64 exec, -1\n"
".Lsk_582:\n"
"s_branch .Ltp_579\n"
".Lou_580:\n"
"s_waitcnt vmcnt(0)\n"
"v_fma_f32 v64, v64, v89, v88\n"
"v_fma_f32 v65, v65, v91, v90\n"
"v_cvt_pk_f16_f32 v64, v64, v65\n"
"v_fma_f32 v66, v66, v89, v88\n"
"v_fma_f32 v67, v67, v91, v90\n"
"v_cvt_pk_f16_f32 v66, v66, v67\n"
"v_fma_f32 v68, v68, v89, v88\n"
"v_fma_f32 v69, v69, v91, v90\n"
"v_cvt_pk_f16_f32 v68, v68, v69\n"
"v_fma_f32 v70, v70, v89, v88\n"
"v_fma_f32 v71, v71, v91, v90\n"
"v_cvt_pk_f16_f32 v70, v70, v71\n"
"ds_write2st64_b32 v31, v64, v66 offset0:0 offset1:18\n"
"ds_write2st64_b32 v31, v68, v70 offset0:36 offset1:54\n"
"v_fma_f32 v72, v72, v93, v92\n"
"v_fma_f32 v73, v73, v95, v94\n"
"v_cvt_pk_f16_f32 v72, v72, v73\n"
"v_fma_f32 v74, v74, v93, v92\n"
"v_fma_f32 v75, v75, v95, v94\n"
"v_cvt_pk_f16_f32 v74, v74, v75\n"
"v_fma_f32 v76, v76, v93, v92\n"
"v_fma_f32 v77, v77, v95, v94\n"
"v_cvt_pk_f16_f32 v76, v76, v77\n"
"v_fma_f32 v78, v78, v93, v92\n"
"v_fma_f32 v79, v79, v95, v94\n"
"v_cvt_pk_f16_f32 v78, v78, v79\n"
"ds_write2st64_b32 v31, v72, v74 offset0:4 offset1:22\n"
"ds_write2st64_b32 v31, v76, v78 offset0:40 offset1:58\n"
"ds_write_b32 v4, v4 offset:816\n"
"s_mov_b32 s17, 0x100000\n"
".Ltp_583:\n"
"ds_read_b32 v3, v5 offset:816\n"
"s_sub_u32 s17, s17, 1\n"
"s_cbranch_scc1 .Lou_584\n"
"s_waitcnt lgkmcnt(0)\n"
"v_cmp_eq_u32 vcc, 0, v3\n"
"s_cbranch_vccnz .Ltp_583\n"
".Lou_584:\n"
"s_waitcnt lgkmcnt(0)\n"
"s_mov_b32 s36, 0xfffefffe\n"
"ds_read_b64 v[88:89], v26 offset:0\n"
"ds_read_b64 v[92:93], v26 offset:512\n"
"ds_read_b64 v[96:97], v26 offset:1024\n"
"ds_read_b64 v[100:101], v26 offset:1536\n"
"ds_read_b128 v[104:107], v25 offset:2496\n"
"s_waitcnt lgkmcnt(1)\n"
"v_mfma_f32_16x16x16_bf16 v[48:51], v[34:35], v[88:89], v[48:51]\n"
"ds_read_b128 v[108:111], v25 offset:3008\n"
"ds_read_b128 v[112:115], v25 offset:2432\n"
"v_mfma_f32_16x16x16_bf16 v[52:55], v[34:35], v[92:93], v[52:55]\n"
"ds_read_b128 v[116:119], v25 offset:2944\n"
"ds_read_b128 v[120:123], v25 offset:2368\n"
"v_mfma_f32_16x16x16_bf16 v[56:59], v[34:35], v[96:97], v[56:59]\n"
"ds_read_b128 v[124:127], v25 offset:2880\n"
"v_mfma_f32_16x16x16_bf16 v[60:63], v[34:35], v[100:101], v[60:63]\n"
"ds_read_b128 v[40:43], v25 offset:2304\n"
"s_waitcnt lgkmcnt(4)\n"
"v_mfma_f32_16x16x32_f16 v[48:51], v[128:131], v[104:107], v[48:51]\n"
"ds_read_b128 v[44:47], v25 offset:2816\n"
"v_mfma_f32_16x16x32_f16 v[52:55], v[160:163], v[104:107], v[52:55]\n"
"ds_read_b128 v[88:91], v25 offset:2240\n"
"v_mfma_f32_16x16x32_f16 v[52:55], v[128:131], v[108:111], v[52:55]\n"
"ds_read_b128 v[92:95], v25 offset:2752\n"
"v_mfma_f32_16x16x32_f16 v[48:51], v[132:135], v[112:115], v[48:51]\n"
"ds_read_b128 v[96:99], v25 offset:2176\n"
"s_waitcnt lgkmcnt(6)\n"
"v_mfma_f32_16x16x32_f16 v[52:55], v[164:167], v[112:115], v[52:55]\n"
"v_mfma_f32_16x16x32_f16 v[52:55], v[132:135], v[116:119], v[52:55]\n"
"ds_read_b128 v[100:103], v25 offset:2688\n"
"v_mfma_f32_16x16x32_f16 v[48:51], v[136:139], v[120:123], v[48:51]\n"
"ds_read_b128 v[104:107], v25 offset:2112\n"
"v_mfma_f32_16x16x32_f16 v[52:55], v[168:171], v[120:123], v[52:55]\n"
"s_waitcnt lgkmcnt(5)\n"
"v_mfma_f32_16x16x32_f16 v[52:55], v[136:139], v[124:127], v[52:55]\n"
"ds_read_b128 v[108:111], v25 offset:2624\n"
"v_mfma_f32_16x16x32_f16 v[48:51], v[140:143], v[40:43], v[48:51]\n"
"ds_read_b128 v[112:115], v25 offset:2048\n"
"v_mfma_f32_16x16x32_f16 v[52:55], v[172:175], v[40:43], v[52:55]\n"
"v_mfma_f32_16x16x32_f16 v[52:55], v[140:143], v[44:47], v[52:55]\n"
"ds_read_b128 v[116:119], v25 offset:2560\n"
"s_waitcnt lgkmcnt(5)\n"
"v_mfma_f32_16x16x32_f16 v[48:51], v[144:147], v[88:91], v[48:51]\n"
"ds_read_b128 v[120:123], v25 offset:2496\n"
"v_mfma_f32_16x16x32_f16 v[52:55], v[176:179], v[88:91], v[52:55]\n"
"ds_read_b128 v[124:127], v25 offset:3008\n"
"v_mfma_f32_16x16x32_f16 v[52:55], v[144:147], v[92:95], v[52:55]\n"
"ds_read_b128 v[40:43], v25 offset:3520\n"
"v_mfma_f32_16x16x32_f16 v[48:51], v[148:151], v[96:99], v[48:51]\n"
"ds_read_b128 v[44:47], v25 offset:2432\n"
"s_waitcnt lgkmcnt(7)\n"
"v_mfma_f32_16x16x32_f16 v[52:55], v[180:183], v[96:99], v[52:55]\n"
"ds_read_b128 v[88:91], v25 offset:2944\n"
"v_mfma_f32_16x16x32_f16 v[52:55], v[148:151], v[100:103], v[52:55]\n"
"ds_read_b128 v[92:95], v25 offset:3456\n"
"v_mfma_f32_16x16x32_f16 v[48:51], v[152:155], v[104:107], v[48:51]\n"
"ds_read_b128 v[96:99], v25 offset:2368\n"
"v_mfma_f32_16x16x32_f16 v[52:55], v[184:187], v[104:107], v[52:55]\n"
"ds_read_b128 v[100:103], v25 offset:2880\n"
"s_waitcnt lgkmcnt(8)\n"
"v_mfma_f32_16x16x32_f16 v[52:55], v[152:155], v[108:111], v[52:55]\n"
"ds_read_b128 v[104:107], v25 offset:3392\n"
"v_mfma_f32_16x16x32_f16 v[48:51], v[156:159], v[112:115], v[48:51]\n"
"ds_read_b128 v[108:111], v25 offset:2304\n"
"v_mfma_f32_16x16x32_f16 v[52:55], v[188:191], v[112:115], v[52:55]\n"
"ds_read_b128 v[112:115], v25 offset:2816\n"
"v_mfma_f32_16x16x32_f16 v[52:55], v[156:159], v[116:119], v[52:55]\n"
"ds_read_b128 v[116:119], v25 offset:3328\n"
"s_waitcnt lgkmcnt(8)\n"
"v_mfma_f32_16x16x32_f16 v[56:59], v[192:195], v[120:123], v[56:59]\n"
"ds_read_b128 v[120:123], v25 offset:2240\n"
"v_mfma_f32_16x16x32_f16 v[56:59], v[160:163], v[124:127], v[56:59]\n"
"ds_read_b128 v[124:127], v25 offset:2752\n"
"v_mfma_f32_16x16x32_f16 v[56:59], v[128:131], v[40:43], v[56:59]\n"
"ds_read_b128 v[40:43], v25 offset:3264\n"
"v_mul_f32 v48, v36, v48\n"
"v_mul_f32 v49, v36, v49\n"
"v_mul_f32 v50, v36, v50\n"
"v_mfma_f32_16x16x32_f16 v[56:59], v[196:199], v[44:47], v[56:59]\n"
"ds_read_b128 v[44:47], v25 offset:2176\n"
"v_mul_f32 v51, v36, v51\n"
"v_cvt_pk_bf16_f32 v48, v48, v49\n"
"s_waitcnt lgkmcnt(8)\n"
"v_mfma_f32_16x16x32_f16 v[56:59], v[164:167], v[88:91], v[56:59]\n"
"ds_read_b128 v[88:91], v25 offset:2688\n"
"v_cvt_pk_bf16_f32 v49, v50, v51\n"
"v_pk_min_u16 v48, v48, s36\n"
"v_pk_min_u16 v49, v49, s36\n"
"v_mfma_f32_16x16x32_f16 v[56:59], v[132:135], v[92:95], v[56:59]\n"
"ds_read_b128 v[92:95], v25 offset:3200\n"
"ds_write_b64 v26, v[48:49] offset:0\n"
"v_mfma_f32_16x16x32_f16 v[56:59], v[200:203], v[96:99], v[56:59]\n"
"ds_read_b128 v[96:99], v25 offset:2112\n"
"v_mul_f32 v52, v36, v52\n"
"v_mul_f32 v53, v36, v53\n"
"v_mul_f32 v54, v36, v54\n"
"v_mfma_f32_16x16x32_f16 v[56:59], v[168:171], v[100:103], v[56:59]\n"
"ds_read_b128 v[100:103], v25 offset:2624\n"
"v_mul_f32 v55, v36, v55\n"
"v_cvt_pk_bf16_f32 v52, v52, v53\n"
"s_waitcnt lgkmcnt(9)\n"
"v_mfma_f32_16x16x32_f16 v[56:59], v[136:139], v[104:107], v[56:59]\n"
"ds_read_b128 v[104:107], v25 offset:3136\n"
"v_cvt_pk_bf16_f32 v53, v54, v55\n"
"v_pk_min_u16 v52, v52, s36\n"
"v_pk_min_u16 v53, v53, s36\n"
"v_mfma_f32_16x16x32_f16 v[56:59], v[204:207], v[108:111], v[56:59]\n"
"ds_read_b128 v[108:111], v25 offset:2048\n"
"ds_write_b64 v26, v[52:53] offset:512\n"
"v_mfma_f32_16x16x32_f16 v[56:59], v[172:175], v[112:115], v[56:59]\n"
"ds_read_b128 v[112:115], v25 offset:2560\n"
"ds_write_b32 v4, v4 offset:608\n"
"v_mfma_f32_16x16x32_f16 v[56:59], v[140:143], v[116:119], v[56:59]\n"
"ds_read_b128 v[116:119], v25 offset:3072\n"
"s_waitcnt lgkmcnt(11)\n"
"v_mfma_f32_16x16x32_f16 v[56:59], v[208:211], v[120:123], v[56:59]\n"
"ds_read_b128 v[120:123], v25 offset:2496\n"
"v_mfma_f32_16x16x32_f16 v[56:59], v[176:179], v[124:127], v[56:59]\n"
"ds_read_b128 v[124:127], v25 offset:3008\n"
"v_mfma_f32_16x16x32_f16 v[56:59], v[144:147], v[40:43], v[56:59]\n"
"ds_read_b128 v[40:43], v25 offset:3520\n"
"v_mfma_f32_16x16x32_f16 v[56:59], v[212:215], v[44:47], v[56:59]\n"
"ds_read_b128 v[44:47], v25 offset:4032\n"
"s_waitcnt lgkmcnt(10)\n"
"v_mfma_f32_16x16x32_f16 v[56:59], v[180:183], v[88:91], v[56:59]\n"
"ds_read_b128 v[88:91], v25 offset:2432\n"
"v_mfma_f32_16x16x32_f16 v[56:59], v[148:151], v[92:95], v[56:59]\n"
"ds_read_b128 v[92:95], v25 offset:2944\n"
"v_mfma_f32_16x16x32_f16 v[56:59], v[216:219], v[96:99], v[56:59]\n"
"ds_read_b128 v[96:99], v25 offset:3456\n"
"v_mfma_f32_16x16x32_f16 v[56:59], v[184:187], v[100:103], v[56:59]\n"
"ds_read_b128 v[100:103], v25 offset:3968\n"
"s_waitcnt lgkmcnt(8)\n"
"v_mfma_f32_16x16x32_f16 v[56:59], v[152:155], v[104:107], v[56:59]\n"
"ds_read_b128 v[104:107], v25 offset:2368\n"
"v_mfma_f32_16x16x32_f16 v[56:59], v[220:223], v[108:111], v[56:59]\n"
"ds_read_b128 v[108:111], v25 offset:2880\n"
"v_mfma_f32_16x16x32_f16 v[56:59], v[188:191], v[112:115], v[56:59]\n"
"ds_read_b128 v[112:115], v25 offset:3392\n"
"v_mfma_f32_16x16x32_f16 v[56:59], v[156:159], v[116:119], v[56:59]\n"
"ds_read_b128 v[116:119], v25 offset:3904\n"
"s_waitcnt lgkmcnt(8)\n"
"v_mfma_f32_16x16x32_f16 v[60:63], v[224:227], v[120:123], v[60:63]\n"
"ds_read_b128 v[120:123], v25 offset:2304\n"
"v_mfma_f32_16x16x32_f16 v[60:63], v[192:195], v[124:127], v[60:63]\n"
"ds_read_b128 v[124:127], v25 offset:2816\n"
"v_mfma_f32_16x16x32_f16 v[60:63], v[160:163], v[40:43], v[60:63]\n"
"ds_read_b128 v[40:43], v25 offset:3328\n"
"v_mul_f32 v56, v36, v56\n"
"v_mul_f32 v57, v36, v57\n"
"v_mul_f32 v58, v36, v58\n"
"v_mul_f32 v59, v36, v59\n"
"v_mfma_f32_16x16x32_f16 v[60:63], v[128:131], v[44:47], v[60:63]\n"
"ds_read_b128 v[44:47], v25 offset:3840\n"
"v_cvt_pk_bf16_f32 v56, v56, v57\n"
"v_cvt_pk_bf16_f32 v57, v58, v59\n"
"v_pk_min_u16 v56, v56, s36\n"
"s_waitcnt lgkmcnt(8)\n"
"v_mfma_f32_16x16x32_f16 v[60:63], v[228:231], v[88:91], v[60:63]\n"
"ds_read_b128 v[88:91], v25 offset:2240\n"
"v_pk_min_u16 v57, v57, s36\n"
"ds_write_b64 v26, v[56:57] offset:1024\n"
"v_mfma_f32_16x16x32_f16 v[60:63], v[196:199], v[92:95], v[60:63]\n"
"ds_read_b128 v[92:95], v25 offset:2752\n"
"ds_write_b32 v4, v4 offset:592\n"
"v_mfma_f32_16x16x32_f16 v[60:63], v[164:167], v[96:99], v[60:63]\n"
"ds_read_b128 v[96:99], v25 offset:3264\n"
"v_mfma_f32_16x16x32_f16 v[60:63], v[132:135], v[100:103], v[60:63]\n"
"ds_read_b128 v[100:103], v25 offset:3776\n"
"s_waitcnt lgkmcnt(10)\n"
"v_mfma_f32_16x16x32_f16 v[60:63], v[232:235], v[104:107], v[60:63]\n"
"ds_read_b128 v[104:107], v25 offset:2176\n"
"v_mfma_f32_16x16x32_f16 v[60:63], v[200:203], v[108:111], v[60:63]\n"
"ds_read_b128 v[108:111], v25 offset:2688\n"
"v_mfma_f32_16x16x32_f16 v[60:63], v[168:171], v[112:115], v[60:63]\n"
"ds_read_b128 v[112:115], v25 offset:3200\n"
"v_mfma_f32_16x16x32_f16 v[60:63], v[136:139], v[116:119], v[60:63]\n"
"ds_read_b128 v[116:119], v25 offset:3712\n"
"s_waitcnt lgkmcnt(10)\n"
"v_mfma_f32_16x16x32_f16 v[60:63], v[236:239], v[120:123], v[60:63]\n"
"ds_read_b128 v[120:123], v25 offset:2112\n"
"v_mfma_f32_16x16x32_f16 v[60:63], v[204:207], v[124:127], v[60:63]\n"
"ds_read_b128 v[124:127], v25 offset:2624\n"
"v_mfma_f32_16x16x32_f16 v[60:63], v[172:175], v[40:43], v[60:63]\n"
"ds_read_b128 v[40:43], v25 offset:3136\n"
"v_mfma_f32_16x16x32_f16 v[60:63], v[140:143], v[44:47], v[60:63]\n"
"ds_read_b128 v[44:47], v25 offset:3648\n"
"s_waitcnt lgkmcnt(8)\n"
"v_mfma_f32_16x16x32_f16 v[60:63], v[240:243], v[88:91], v[60:63]\n"
"ds_read_b128 v[88:91], v25 offset:2048\n"
"v_mfma_f32_16x16x32_f16 v[60:63], v[208:211], v[92:95], v[60:63]\n"
"ds_read_b128 v[92:95], v25 offset:2560\n"
"v_mfma_f32_16x16x32_f16 v[60:63], v[176:179], v[96:99], v[60:63]\n"
"ds_read_b128 v[96:99], v25 offset:3072\n"
"v_mfma_f32_16x16x32_f16 v[60:63], v[144:147], v[100:103], v[60:63]\n"
"ds_read_b128 v[100:103], v25 offset:3584\n"
"s_waitcnt lgkmcnt(8)\n"
"v_mfma_f32_16x16x32_f16 v[60:63], v[244:247], v[104:107], v[60:63]\n"
"v_mfma_f32_16x16x32_f16 v[60:63], v[212:215], v[108:111], v[60:63]\n"
"v_mfma_f32_16x16x32_f16 v[60:63], v[180:183], v[112:115], v[60:63]\n"
"v_mfma_f32_16x16x32_f16 v[60:63], v[148:151], v[116:119], v[60:63]\n"
"s_waitcnt lgkmcnt(4)\n"
"v_mfma_f32_16x16x32_f16 v[60:63], v[248:251], v[120:123], v[60:63]\n"
"v_mfma_f32_16x16x32_f16 v[60:63], v[216:219], v[124:127], v[60:63]\n"
"v_mfma_f32_16x16x32_f16 v[60:63], v[184:187], v[40:43], v[60:63]\n"
"v_mfma_f32_16x16x32_f16 v[60:63], v[152:155], v[44:47], v[60:63]\n"
"s_waitcnt lgkmcnt(0)\n"
"v_mfma_f32_16x16x32_f16 v[60:63], v[252:255], v[88:91], v[60:63]\n"
"v_mfma_f32_16x16x32_f16 v[60:63], v[220:223], v[92:95], v[60:63]\n"
"v_mfma_f32_16x16x32_f16 v[60:63], v[188:191], v[96:99], v[60:63]\n"
"v_mfma_f32_16x16x32_f16 v[60:63], v[156:159], v[100:103], v[60:63]\n"
"s_nop 7\n"
"v_mul_f32 v60, v36, v60\n"
"v_mul_f32 v61, v36, v61\n"
"v_mul_f32 v62, v36, v62\n"
"v_mul_f32 v63, v36, v63\n"
"v_cvt_pk_bf16_f32 v60, v60, v61\n"
"v_cvt_pk_bf16_f32 v61, v62, v63\n"
"v_pk_min_u16 v60, v60, s36\n"
"v_pk_min_u16 v61, v61, s36\n"
"ds_write_b64 v26, v[60:61] offset:1536\n"
"ds_write_b32 v4, v4 offset:576\n"
"s_nop 2\n"
".Lnc_568:\n"
"s_branch .L_yend\n"
".Lsl_300:\n"
"s_nop 7\n"
"s_mov_b32 s17, 32768\n"
".Ltp_302:\n"
"buffer_load_dwordx4 v[44:47], v38, s[4:7], s81 offen sc1\n"
"s_sub_u32 s17, s17, 1\n"
"s_cbranch_scc1 .Lou_303\n"
"s_waitcnt vmcnt(0)\n"
"v_xor_b32 v44, s16, v44\n"
"v_xor_b32 v45, s16, v45\n"
"v_xor_b32 v46, s16, v46\n"
"v_xor_b32 v47, s16, v47\n"
"v_or_b32 v44, v44, v45\n"
"v_or3_b32 v44, v44, v46, v47\n"
"v_cmp_ne_u32 vcc, 0, v44\n"
"s_cbranch_vccz .Lou_303\n"
"s_sleep 16\n"
"s_branch .Ltp_302\n"
".Lou_303:\n"
"s_waitcnt vmcnt(0)\n"
"s_branch .Lbk_301\n"
".Lst_312:\n"
"s_nop 7\n"
"s_lshl_b32 s79, s3, 2\n"
"s_add_u32 s79, s79, 0x26800\n"
"v_mov_b32 v36, s79\n"
"s_cmp_lg_u32 s40, 0\n"
"s_cbranch_scc1 .Lhv_315\n"
"s_call_b64 s[60:61], .Lkg_304\n"
"s_setprio 0\n"
".Lhv_315:\n"
"v_and_b32 v73, 15, v0\n"
"v_lshlrev_b32 v74, 2, v0\n"
"v_lshrrev_b32 v75, 4, v0\n"
"s_mul_i32 s79, s3, 0x2100\n"
"s_add_u32 s72, s79, 0x15100\n"
"v_lshlrev_b32 v76, 3, v73\n"
"v_lshlrev_b32 v77, 4, v75\n"
"v_lshlrev_b32 v78, 8, v75\n"
"s_add_u32 s73, s72, 0x400\n"
"s_sub_u32 s74, s72, 0x1100\n"
"s_add_u32 s79, s72, 0x800\n"
"v_add3_u32 v79, v78, v76, s79\n"
"v_lshl_add_u32 v99, v73, 6, v77\n"
"v_add_u32 v80, s72, v99\n"
"v_lshl_add_u32 v99, v73, 5, v77\n"
"v_lshl_add_u32 v98, v73, 4, v78\n"
"s_mov_b32 vcc_lo, -1\n"
"s_mov_b32 vcc_hi, 0\n"
"v_cndmask_b32 v99, v98, v99, vcc\n"
"v_add_u32 v81, s73, v99\n"
"s_mov_b32 vcc_lo, -1\n"
"s_mov_b32 vcc_hi, 1\n"
"v_cndmask_b32 v82, 2.0, v79, vcc\n"
"s_mov_b32 vcc_lo, -1\n"
"s_mov_b32 vcc_hi, 0\n"
"v_cndmask_b32 v83, 2.0, v79, vcc\n"
"s_add_u32 s79, s72, 0x7ec\n"
"s_add_u32 s79, s79, 0x2c0\n"
"v_lshlrev_b32 v99, 5, v75\n"
"v_sub_u32 v99, v76, v99\n"
"v_add_u32 v84, s79, v99\n"
"s_add_u32 s79, s72, 0xa40\n"
"v_add_u32 v99, s79, v74\n"
"s_mov_b32 vcc_lo, -1\n"
"s_mov_b32 vcc_hi, 1\n"
"v_cndmask_b32 v100, 2.0, v99, vcc\n"
"v_sub_u32 v99, 0x420, v0\n"
"v_sub_u32 v98, 0x41f, v0\n"
"v_bfe_u32 v98, v98, 2, 1\n"
"v_add_u32 v99, v99, v98\n"
"v_and_b32 v99, 0xffc, v99\n"
"v_min_i32 v99, 0x400, v99\n"
"v_mov_b32 v98, 0x640\n"
"s_mov_b32 vcc_lo, 0xf8000000\n"
"s_mov_b32 vcc_hi, -1\n"
"v_cndmask_b32 v99, v98, v99, vcc\n"
"v_add_u32 v101, s73, v99\n"
"v_add_u32 v102, s74, v74\n"
"v_lshlrev_b32 v99, 3, v75\n"
"v_sub_u32 v99, v99, v73\n"
"s_add_u32 s79, s74, 60\n"
"v_lshl_add_u32 v103, v99, 2, s79\n"
"v_mov_b32 v99, 0x3f800000\n"
"s_mov_b32 vcc_lo, 0x7f00000\n"
"s_mov_b32 vcc_hi, 0\n"
"v_cndmask_b32 v104, 0, v99, vcc\n"
"v_mov_b32 v99, 0x3f800000\n"
"s_mov_b32 vcc_lo, 0xff80000\n"
"s_mov_b32 vcc_hi, 0\n"
"v_cndmask_b32 v105, 0, v99, vcc\n"
"v_mov_b32 v106, 0\n"
"v_cmp_eq_u32 vcc, 0, v0\n"
"v_lshlrev_b32 v140, 16, v140\n"
"v_mul_f32 v140, 0.5, v140\n"
"v_cndmask_b32 v140, 0, v140, vcc\n"
"v_add_f32 v128, v128, v140\n"
"v_cndmask_b32 v136, v136, 0, s[56:57]\n"
"v_cndmask_b32 v137, v137, 0, s[58:59]\n"
"v_cndmask_b32 v138, v138, 0, s[58:59]\n"
"v_cndmask_b32 v139, v139, 0, s[58:59]\n"
"v_max3_f32 v85, |v128|, |v129|, 0\n"
"v_max3_f32 v85, |v130|, |v131|, v85\n"
"v_max3_f32 v85, |v132|, |v133|, v85\n"
"v_max3_f32 v85, |v134|, |v135|, v85\n"
"v_max3_f32 v85, |v136|, |v137|, v85\n"
"v_max3_f32 v85, |v138|, |v139|, v85\n"
"s_nop 1\n"
"v_max_u32_dpp v85, v85, v85 row_ror:8 row_mask:0xf bank_mask:0xf\n"
"s_nop 1\n"
"v_max_u32_dpp v85, v85, v85 row_ror:4 row_mask:0xf bank_mask:0xf\n"
"s_nop 1\n"
"v_max_u32_dpp v85, v85, v85 row_ror:2 row_mask:0xf bank_mask:0xf\n"
"s_nop 1\n"
"v_max_u32_dpp v85, v85, v85 row_ror:1 row_mask:0xf bank_mask:0xf\n"
"v_mov_b32 v86, v85\n"
"s_nop 1\n"
"v_permlane16_swap_b32 v85, v86\n"
"v_max_u32 v85, v85, v86\n"
"v_mov_b32 v86, v85\n"
"s_nop 1\n"
"v_permlane32_swap_b32 v85, v86\n"
"v_max_u32 v85, v85, v86\n"
"s_nop 0\n"
"v_readfirstlane_b32 s51, v85\n"
"s_nop 2\n"
"s_bfe_u32 s50, s51, 0x80017\n"
"s_max_i32 s50, s50, 77\n"
"s_min_i32 s50, s50, 227\n"
"s_cmp_gt_i32 s51, 0\n"
"s_cselect_b32 s50, s50, 127\n"
"s_sub_u32 s51, 268, s50\n"
"s_lshl_b32 s51, s51, 23\n"
"s_sub_u32 s50, s50, 13\n"
"s_lshl_b32 s50, s50, 23\n"
"v_mul_f32 v128, s51, v128\n"
"v_mul_f32 v129, s51, v129\n"
"v_mul_f32 v130, s51, v130\n"
"v_mul_f32 v131, s51, v131\n"
"v_mul_f32 v132, s51, v132\n"
"v_mul_f32 v133, s51, v133\n"
"v_mul_f32 v134, s51, v134\n"
"v_mul_f32 v135, s51, v135\n"
"v_mul_f32 v136, s51, v136\n"
"v_mul_f32 v137, s51, v137\n"
"v_mul_f32 v138, s51, v138\n"
"v_mul_f32 v139, s51, v139\n"
"ds_write_b128 v80, v[128:131]\n"
"ds_write_b128 v81, v[132:135]\n"
"ds_write_b64 v82, v[136:137]\n"
"ds_write_b64 v83, v[138:139] offset:128\n"
"v_mov_b32 v87, s50\n"
"ds_write_b32 v36, v87\n"
"ds_write_b64 v82, v[136:137] offset:704\n"
"ds_write_b64 v83, v[138:139] offset:832\n"
"ds_write_b32 v100, v106\n"
"ds_read_b32 v101, v101\n"
"ds_write2st64_b32 v102, v104, v105 offset0:1 offset1:2\n"
"ds_read2_b32 v[48:49], v84 offset0:6 offset1:4\n"
"ds_read2_b32 v[50:51], v84 offset0:2 offset1:0\n"
"ds_read2_b32 v[52:53], v84 offset0:7 offset1:5\n"
"ds_read2_b32 v[54:55], v84 offset0:3 offset1:1\n"
"ds_read2_b32 v[56:57], v84 offset0:38 offset1:36\n"
"ds_read2_b32 v[58:59], v84 offset0:34 offset1:32\n"
"ds_read2_b32 v[60:61], v84 offset0:39 offset1:37\n"
"ds_read2_b32 v[62:63], v84 offset0:35 offset1:33\n"
"s_waitcnt lgkmcnt(9)\n"
"ds_write_b32 v102, v101\n"
"ds_read2_b32 v[64:65], v84 offset0:70 offset1:68\n"
"ds_read2_b32 v[66:67], v84 offset0:66 offset1:64\n"
"ds_read2_b32 v[68:69], v84 offset0:71 offset1:69\n"
"ds_read2_b32 v[70:71], v84 offset0:67 offset1:65\n"
"ds_read2_b32 v[72:73], v84 offset0:102 offset1:100\n"
"ds_read2_b32 v[74:75], v84 offset0:98 offset1:96\n"
"ds_read2_b32 v[76:77], v84 offset0:103 offset1:101\n"
"ds_read2_b32 v[78:79], v84 offset0:99 offset1:97\n"
"ds_read2_b32 v[88:89], v103 offset0:0 offset1:1\n"
"s_waitcnt lgkmcnt(15)\n"
"v_cvt_pk_f16_f32 v128, v48, v49\n"
"ds_read2_b32 v[90:91], v103 offset0:2 offset1:3\n"
"v_cvt_pk_f16_f32 v129, v50, v51\n"
"ds_read2_b32 v[92:93], v103 offset0:4 offset1:5\n"
"v_cvt_pk_f16_f32 v130, v52, v53\n"
"ds_read2_b32 v[94:95], v103 offset0:6 offset1:7\n"
"s_waitcnt lgkmcnt(15)\n"
"v_cvt_pk_f16_f32 v131, v54, v55\n"
"ds_read2_b32 v[96:97], v103 offset0:64 offset1:65\n"
"v_cvt_pk_f16_f32 v132, v56, v57\n"
"ds_read2_b32 v[98:99], v103 offset0:66 offset1:67\n"
"v_cvt_pk_f16_f32 v133, v58, v59\n"
"ds_read2_b32 v[108:109], v103 offset0:68 offset1:69\n"
"s_waitcnt lgkmcnt(15)\n"
"v_cvt_pk_f16_f32 v134, v60, v61\n"
"ds_read2_b32 v[110:111], v103 offset0:70 offset1:71\n"
"v_cvt_pk_f16_f32 v135, v62, v63\n"
"ds_read2_b32 v[112:113], v103 offset0:128 offset1:129\n"
"s_waitcnt lgkmcnt(15)\n"
"v_cvt_pk_f16_f32 v136, v64, v65\n"
"ds_read2_b32 v[114:115], v103 offset0:130 offset1:131\n"
"v_cvt_pk_f16_f32 v137, v66, v67\n"
"ds_read2_b32 v[116:117], v103 offset0:132 offset1:133\n"
"s_waitcnt lgkmcnt(15)\n"
"v_cvt_pk_f16_f32 v138, v68, v69\n"
"ds_read2_b32 v[118:119], v103 offset0:134 offset1:135\n"
"v_cvt_pk_f16_f32 v139, v70, v71\n"
"s_waitcnt lgkmcnt(15)\n"
"v_cvt_pk_f16_f32 v140, v72, v73\n"
"s_waitcnt lgkmcnt(14)\n"
"v_cvt_pk_f16_f32 v141, v74, v75\n"
"s_waitcnt lgkmcnt(13)\n"
"v_cvt_pk_f16_f32 v142, v76, v77\n"
"s_waitcnt lgkmcnt(12)\n"
"v_cvt_pk_f16_f32 v143, v78, v79\n"
"s_waitcnt lgkmcnt(11)\n"
"v_cvt_pk_f16_f32 v152, v88, v89\n"
"s_waitcnt lgkmcnt(10)\n"
"v_cvt_pk_f16_f32 v153, v90, v91\n"
"s_waitcnt lgkmcnt(9)\n"
"v_cvt_pk_f16_f32 v154, v92, v93\n"
"s_waitcnt lgkmcnt(8)\n"
"v_cvt_pk_f16_f32 v155, v94, v95\n"
"s_waitcnt lgkmcnt(7)\n"
"v_cvt_pk_f16_f32 v144, v96, v97\n"
"s_waitcnt lgkmcnt(6)\n"
"v_cvt_pk_f16_f32 v145, v98, v99\n"
"s_waitcnt lgkmcnt(5)\n"
"v_cvt_pk_f16_f32 v146, v108, v109\n"
"s_waitcnt lgkmcnt(4)\n"
"v_cvt_pk_f16_f32 v147, v110, v111\n"
"s_waitcnt lgkmcnt(3)\n"
"v_cvt_pk_f16_f32 v148, v112, v113\n"
"s_waitcnt lgkmcnt(2)\n"
"v_cvt_pk_f16_f32 v149, v114, v115\n"
"s_waitcnt lgkmcnt(1)\n"
"v_cvt_pk_f16_f32 v150, v116, v117\n"
"s_waitcnt lgkmcnt(0)\n"
"v_cvt_pk_f16_f32 v151, v118, v119\n"
"ds_write_b32 v4, v4 offset:192\n"
"s_mov_b32 s43, 1\n"
"s_nop 2\n"
"s_setpc_b64 s[46:47]\n"
".Lst_313:\n"
"s_nop 7\n"
"s_mov_b32 s43, 2\n"
"s_nop 0\n"
"s_setpc_b64 s[46:47]\n"
".Lst_314:\n"
"s_nop 7\n"
"v_and_b32 v72, 15, v0\n"
"v_lshrrev_b32 v73, 4, v0\n"
"v_mov_b32 v99, 0xffff0000\n"
"v_mov_b32 v98, 0xffff\n"
"s_mov_b32 vcc_lo, 0xaaaaaaaa\n"
"s_mov_b32 vcc_hi, 0xaaaaaaaa\n"
"v_cndmask_b32 v74, v98, v99, vcc\n"
"v_lshlrev_b32 v75, 5, v72\n"
"v_lshrrev_b32 v76, 2, v72\n"
"s_lshl_b32 s79, s3, 10\n"
"v_lshl_add_u32 v30, v0, 4, s79\n"
"v_lshrrev_b32 v77, 2, v30\n"
"v_lshrrev_b32 v78, 6, v30\n"
"v_lshlrev_b32 v99, 3, v73\n"
"v_sub_u32 v99, v99, v72\n"
"v_add_u32 v79, 0x3ef, v99\n"
"v_and_b32 v99, 24, v78\n"
"v_xor_b32 v99, v77, v99\n"
"v_add_u32 v32, 0x20c00, v99\n"
"s_lshl_b32 s79, s3, 8\n"
"s_add_u32 s79, s79, 0x20c00\n"
"v_xor_b32 v99, s3, v72\n"
"v_and_b32 v99, 1, v99\n"
"v_lshrrev_b32 v98, 1, v73\n"
"v_xor_b32 v98, v76, v98\n"
"v_and_b32 v98, 1, v98\n"
"v_lshl_add_u32 v99, v99, 1, v98\n"
"v_and_b32 v98, 2, v72\n"
"v_lshrrev_b32 v97, 3, v72\n"
"v_lshlrev_b32 v97, 7, v97\n"
"v_lshl_add_u32 v97, v76, 11, v97\n"
"v_lshl_add_u32 v97, v73, 6, v97\n"
"v_lshl_add_u32 v98, v98, 4, v97\n"
"v_lshl_add_u32 v99, v99, 3, v98\n"
"v_add_u32 v27, s79, v99\n"
"s_mul_i32 s79, s3, 0x2100\n"
"s_add_u32 s79, s79, 0x13840\n"
"v_lshrrev_b32 v99, 2, v79\n"
"v_and_b32 v98, 3, v79\n"
"v_mul_i32_i24 v98, 0x840, v98\n"
"v_lshl_add_u32 v99, v99, 3, v98\n"
"v_add_u32 v24, s79, v99\n"
"s_mul_i32 s79, s3, 0x1200\n"
"s_add_u32 s79, s79, 0x1c420\n"
"v_lshl_add_u32 v99, v73, 4, v75\n"
"v_add_u32 v25, s79, v99\n"
"s_lshl_b32 s79, s3, 11\n"
"s_lshr_b32 s78, s3, 1\n"
"s_lshl_b32 s78, s78, 7\n"
"s_add_u32 s79, s79, s78\n"
"s_add_u32 s79, s79, 0x20c00\n"
"s_and_b32 s78, s3, 1\n"
"v_xor_b32 v99, v73, v76\n"
"v_xor_b32 v99, s78, v99\n"
"v_and_b32 v99, 3, v99\n"
"v_lshl_add_u32 v99, v99, 3, v75\n"
"v_add_u32 v26, s79, v99\n"
"v_xor_b32 v28, 8, v27\n"
"v_lshrrev_b32 v99, 7, v30\n"
"v_and_b32 v98, 0x70, v30\n"
"s_mov_b32 s79, 0x5000\n"
"v_mad_i32_i24 v29, v99, s79, v98\n"
"v_add_u32 v31, 0x1ce00, v77\n"
"v_xor_b32 v33, 8, v32\n"
"s_mov_b32 vcc_lo, 0x300003\n"
"s_mov_b32 vcc_hi, 0x30000300\n"
"v_cndmask_b32 v34, 0, v74, vcc\n"
"s_mov_b32 vcc_lo, 0xc0000c\n"
"s_mov_b32 vcc_hi, 0xc0000c00\n"
"v_cndmask_b32 v35, 0, v74, vcc\n"
"s_lshl_b32 s79, s3, 2\n"
"s_add_u32 s79, s79, 0x26800\n"
"v_mov_b32 v36, s79\n"
"v_and_b32 v99, 48, v30\n"
"v_mul_i32_i24 v99, 0x50, v99\n"
"s_mov_b32 s79, 0x2800\n"
"v_mad_i32_i24 v37, v78, s79, v99\n"
"ds_read_b32 v36, v36\n"
"s_waitcnt lgkmcnt(0)\n"
"v_sub_u32 v76, 0x7f000000, v36\n"
"v_lshrrev_b32 v77, 16, v76\n"
"v_or_b32 v76, v76, v77\n"
"v_and_b32 v34, v76, v34\n"
"v_and_b32 v35, v76, v35\n"
"s_mov_b32 s43, 3\n"
"s_nop 3\n"
"s_setpc_b64 s[46:47]\n"
".Loa_316:\n"
"s_nop 7\n"
"s_mov_b32 s17, 32768\n"
".Ltp_317:\n"
"s_waitcnt vmcnt(0)\n"
"s_sub_u32 s17, s17, 1\n"
"s_cbranch_scc1 .Lou_318\n"
"v_max_u32 v38, v88, v89\n"
"v_max3_u32 v38, v90, v91, v38\n"
"v_max3_u32 v38, v92, v93, v38\n"
"v_max3_u32 v38, v94, v95, v38\n"
"v_max3_u32 v38, v96, v97, v38\n"
"v_max3_u32 v38, v98, v99, v38\n"
"v_max3_u32 v38, v100, v101, v38\n"
"v_max3_u32 v38, v102, v103, v38\n"
"v_max3_u32 v38, v104, v105, v38\n"
"v_max3_u32 v38, v106, v107, v38\n"
"v_max3_u32 v38, v108, v109, v38\n"
"v_max3_u32 v38, v110, v111, v38\n"
"v_max3_u32 v38, v112, v113, v38\n"
"v_max3_u32 v38, v114, v115, v38\n"
"v_max3_u32 v38, v116, v117, v38\n"
"v_max3_u32 v38, v118, v119, v38\n"
"v_max3_u32 v38, v120, v121, v38\n"
"v_max3_u32 v38, v122, v123, v38\n"
"v_max3_u32 v38, v124, v125, v38\n"
"v_max3_u32 v38, v126, v127, v38\n"
"v_cmp_eq_u32 vcc, -1, v38\n"
"s_cbranch_vccz .Lou_318\n"
"s_sleep 1\n"
"v_max_u32 v38, v90, v91\n"
"v_max3_u32 v38, v88, v89, v38\n"
"v_cmp_eq_u32 vcc, -1, v38\n"
"s_cbranch_vccz .Lsk_319\n"
"s_mov_b64 exec, vcc\n"
"buffer_load_dwordx4 v[88:91], v2, s[4:7], s32 offen offset:0 sc1\n"
"s_mov_b64 exec, -1\n"
".Lsk_319:\n"
"v_max_u32 v38, v94, v95\n"
"v_max3_u32 v38, v92, v93, v38\n"
"v_cmp_eq_u32 vcc, -1, v38\n"
"s_cbranch_vccz .Lsk_320\n"
"s_mov_b64 exec, vcc\n"
"buffer_load_dwordx4 v[92:95], v2, s[4:7], s32 offen offset:1024 sc1\n"
"s_mov_b64 exec, -1\n"
".Lsk_320:\n"
"v_max_u32 v38, v98, v99\n"
"v_max3_u32 v38, v96, v97, v38\n"
"v_cmp_eq_u32 vcc, -1, v38\n"
"s_cbranch_vccz .Lsk_321\n"
"s_mov_b64 exec, vcc\n"
"buffer_load_dwordx4 v[96:99], v2, s[4:7], s32 offen offset:2048 sc1\n"
"s_mov_b64 exec, -1\n"
".Lsk_321:\n"
"v_max_u32 v38, v102, v103\n"
"v_max3_u32 v38, v100, v101, v38\n"
"v_cmp_eq_u32 vcc, -1, v38\n"
"s_cbranch_vccz .Lsk_322\n"
"s_mov_b64 exec, vcc\n"
"buffer_load_dwordx4 v[100:103], v2, s[4:7], s32 offen offset:3072 sc1\n"
"s_mov_b64 exec, -1\n"
".Lsk_322:\n"
"v_max_u32 v38, v106, v107\n"
"v_max3_u32 v38, v104, v105, v38\n"
"v_cmp_eq_u32 vcc, -1, v38\n"
"s_cbranch_vccz .Lsk_323\n"
"s_mov_b64 exec, vcc\n"
"buffer_load_dwordx4 v[104:107], v2, s[4:7], s33 offen offset:0 sc1\n"
"s_mov_b64 exec, -1\n"
".Lsk_323:\n"
"v_max_u32 v38, v110, v111\n"
"v_max3_u32 v38, v108, v109, v38\n"
"v_cmp_eq_u32 vcc, -1, v38\n"
"s_cbranch_vccz .Lsk_324\n"
"s_mov_b64 exec, vcc\n"
"buffer_load_dwordx4 v[108:111], v2, s[4:7], s34 offen offset:0 sc1\n"
"s_mov_b64 exec, -1\n"
".Lsk_324:\n"
"v_max_u32 v38, v114, v115\n"
"v_max3_u32 v38, v112, v113, v38\n"
"v_cmp_eq_u32 vcc, -1, v38\n"
"s_cbranch_vccz .Lsk_325\n"
"s_mov_b64 exec, vcc\n"
"buffer_load_dwordx4 v[112:115], v2, s[4:7], s34 offen offset:1024 sc1\n"
"s_mov_b64 exec, -1\n"
".Lsk_325:\n"
"v_max_u32 v38, v118, v119\n"
"v_max3_u32 v38, v116, v117, v38\n"
"v_cmp_eq_u32 vcc, -1, v38\n"
"s_cbranch_vccz .Lsk_326\n"
"s_mov_b64 exec, vcc\n"
"buffer_load_dwordx4 v[116:119], v2, s[4:7], s34 offen offset:2048 sc1\n"
"s_mov_b64 exec, -1\n"
".Lsk_326:\n"
"v_max_u32 v38, v122, v123\n"
"v_max3_u32 v38, v120, v121, v38\n"
"v_cmp_eq_u32 vcc, -1, v38\n"
"s_cbranch_vccz .Lsk_327\n"
"s_mov_b64 exec, vcc\n"
"buffer_load_dwordx4 v[120:123], v2, s[4:7], s34 offen offset:3072 sc1\n"
"s_mov_b64 exec, -1\n"
".Lsk_327:\n"
"v_max_u32 v38, v126, v127\n"
"v_max3_u32 v38, v124, v125, v38\n"
"v_cmp_eq_u32 vcc, -1, v38\n"
"s_cbranch_vccz .Lsk_328\n"
"s_mov_b64 exec, vcc\n"
"buffer_load_dwordx4 v[124:127], v2, s[4:7], s35 offen offset:0 sc1\n"
"s_mov_b64 exec, -1\n"
".Lsk_328:\n"
"s_branch .Ltp_317\n"
".Lou_318:\n"
"s_waitcnt vmcnt(0)\n"
"s_setpc_b64 s[46:47]\n"
".Lsl_347:\n"
"s_nop 7\n"
"s_call_b64 s[46:47], .Loa_316\n"
"s_branch .Lbk_348\n"
".Lsl_349:\n"
"s_nop 7\n"
"s_call_b64 s[46:47], .Loa_316\n"
"s_branch .Lbk_350\n"
".Lsl_351:\n"
"s_nop 7\n"
"s_call_b64 s[46:47], .Loa_316\n"
"s_branch .Lbk_352\n"
".Lsl_353:\n"
"s_nop 7\n"
"s_call_b64 s[46:47], .Loa_316\n"
"s_branch .Lbk_354\n"
".Lsl_355:\n"
"s_nop 7\n"
"s_call_b64 s[46:47], .Loa_316\n"
"s_branch .Lbk_356\n"
".Lsl_357:\n"
"s_nop 7\n"
"s_call_b64 s[46:47], .Loa_316\n"
"s_branch .Lbk_358\n"
".Lsl_359:\n"
"s_nop 7\n"
"s_call_b64 s[46:47], .Loa_316\n"
"s_branch .Lbk_360\n"
".Lsl_361:\n"
"s_nop 7\n"
"s_call_b64 s[46:47], .Loa_316\n"
"s_branch .Lbk_362\n"
".Lsl_363:\n"
"s_nop 7\n"
"s_call_b64 s[46:47], .Loa_316\n"
"s_branch .Lbk_364\n"
".Lsl_365:\n"
"s_nop 7\n"
"s_call_b64 s[46:47], .Loa_316\n"
"s_branch .Lbk_366\n"
".Lsl_390:\n"
"s_nop 7\n"
"s_call_b64 s[46:47], .Loa_316\n"
"s_branch .Lbk_391\n"
".Lsl_392:\n"
"s_nop 7\n"
"s_call_b64 s[46:47], .Loa_316\n"
"s_branch .Lbk_393\n"
".Lsl_394:\n"
"s_nop 7\n"
"s_call_b64 s[46:47], .Loa_316\n"
"s_branch .Lbk_395\n"
".Lsl_396:\n"
"s_nop 7\n"
"s_call_b64 s[46:47], .Loa_316\n"
"s_branch .Lbk_397\n"
".Lsl_398:\n"
"s_nop 7\n"
"s_call_b64 s[46:47], .Loa_316\n"
"s_branch .Lbk_399\n"
".Lsl_400:\n"
"s_nop 7\n"
"s_call_b64 s[46:47], .Loa_316\n"
"s_branch .Lbk_401\n"
".Lsl_402:\n"
"s_nop 7\n"
"s_call_b64 s[46:47], .Loa_316\n"
"s_branch .Lbk_403\n"
".Lsl_404:\n"
"s_nop 7\n"
"s_call_b64 s[46:47], .Loa_316\n"
"s_branch .Lbk_405\n"
".Lsl_406:\n"
"s_nop 7\n"
"s_call_b64 s[46:47], .Loa_316\n"
"s_branch .Lbk_407\n"
".Lsl_408:\n"
"s_nop 7\n"
"s_call_b64 s[46:47], .Loa_316\n"
"s_branch .Lbk_409\n"
".Lsl_491:\n"
"s_nop 7\n"
"s_call_b64 s[46:47], .Loa_316\n"
"s_branch .Lbk_492\n"
".Lsl_493:\n"
"s_nop 7\n"
"s_call_b64 s[46:47], .Loa_316\n"
"s_branch .Lbk_494\n"
".Lsl_495:\n"
"s_nop 7\n"
"s_call_b64 s[46:47], .Loa_316\n"
"s_branch .Lbk_496\n"
".Lsl_497:\n"
"s_nop 7\n"
"s_call_b64 s[46:47], .Loa_316\n"
"s_branch .Lbk_498\n"
".Lsl_499:\n"
"s_nop 7\n"
"s_call_b64 s[46:47], .Loa_316\n"
"s_branch .Lbk_500\n"
".Lsl_501:\n"
"s_nop 7\n"
"s_call_b64 s[46:47], .Loa_316\n"
"s_branch .Lbk_502\n"
".Lsl_503:\n"
"s_nop 7\n"
"s_call_b64 s[46:47], .Loa_316\n"
"s_branch .Lbk_504\n"
".Lsl_505:\n"
"s_nop 7\n"
"s_call_b64 s[46:47], .Loa_316\n"
"s_branch .Lbk_506\n"
".Lsl_507:\n"
"s_nop 7\n"
"s_call_b64 s[46:47], .Loa_316\n"
"s_branch .Lbk_508\n"
".Lsl_509:\n"
"s_nop 7\n"
"s_call_b64 s[46:47], .Loa_316\n"
"s_branch .Lbk_510\n"
".Lsl_537:\n"
"s_nop 7\n"
"s_call_b64 s[46:47], .Loa_316\n"
"s_branch .Lbk_538\n"
".Lsl_539:\n"
"s_nop 7\n"
"s_call_b64 s[46:47], .Loa_316\n"
"s_branch .Lbk_540\n"
".Lsl_541:\n"
"s_nop 7\n"
"s_call_b64 s[46:47], .Loa_316\n"
"s_branch .Lbk_542\n"
".Lsl_543:\n"
"s_nop 7\n"
"s_call_b64 s[46:47], .Loa_316\n"
"s_branch .Lbk_544\n"
".Lsl_545:\n"
"s_nop 7\n"
"s_call_b64 s[46:47], .Loa_316\n"
"s_branch .Lbk_546\n"
".Lsl_547:\n"
"s_nop 7\n"
"s_call_b64 s[46:47], .Loa_316\n"
"s_branch .Lbk_548\n"
".Lsl_549:\n"
"s_nop 7\n"
"s_call_b64 s[46:47], .Loa_316\n"
"s_branch .Lbk_550\n"
".Lsl_551:\n"
"s_nop 7\n"
"s_call_b64 s[46:47], .Loa_316\n"
"s_branch .Lbk_552\n"
".Lsl_553:\n"
"s_nop 7\n"
"s_call_b64 s[46:47], .Loa_316\n"
"s_branch .Lbk_554\n"
".Lsl_555:\n"
"s_nop 7\n"
"s_call_b64 s[46:47], .Loa_316\n"
"s_branch .Lbk_556\n"
".L_yend:\n"
      :: "{s[0:1]}"(__builtin_amdgcn_kernarg_segment_ptr()), "{s2}"((int)__builtin_amdgcn_workgroup_id_x()),
         "{s3}"(__builtin_amdgcn_readfirstlane(tid >> 6)), "{v0}"(tid),
         "{v1}"((unsigned)(size_t)(__attribute__((address_space(3))) unsigned char*)smem)
      : "memory", "vcc", "scc", "m0", "v255", "s95");
}

extern "C" void kernel_launch(void* const* d_in, const int* in_sizes, int n_in,
                              void* d_out, int out_size, void* d_ws, size_t ws_size,
                              hipStream_t stream) {
  mega_kernel<<<256, 512, 0, stream>>>((const u16*)d_in[0], (const u16*)d_in[1], (const u16*)d_in[2],
                                       (const u16*)d_in[4], (const u16*)d_in[5], (const u16*)d_in[6],
                                       (const u16*)d_in[7], (const u16*)d_in[8], (const u16*)d_in[9],
                                       (u16*)d_out, (unsigned char*)d_ws);
}
```
